```python
import torch

def _softmax(x, axis=-1):
    return torch.softmax(x, dim=axis)
def _gelu(x):
    return torch.nn.functional.gelu(x, approximate="tanh")
def _silu(x):
    return torch.nn.functional.silu(x)
def _sigmoid(x):
    return torch.sigmoid(x)
def _layernorm(x, axis=-1, eps=1e-5):
    m = x.mean(dim=axis, keepdim=True)
    v = x.var(dim=axis, keepdim=True, unbiased=False)
    return (x - m) / torch.sqrt(v + eps)
def _scatter_add(tgt, idx, src, axis=0):
    return tgt.clone().index_add_(axis, idx, src)
def _segment_sum(src, seg, n):
    out = torch.zeros((n,) + src.shape[1:], dtype=src.dtype)
    return out.index_add_(0, seg, src)
def _pad(x, pw):
    flat = tuple(v for pair in reversed(pw) for v in pair)
    return torch.nn.functional.pad(x, flat)

kernel_name = 'standalone__kv_cache_quant_int4_int2_kv_with_per_channel_or_per_token_scales__prompt'

def setup_inputs():

    _w = (lambda a: (lambda t: t.to(torch.bfloat16) if t.is_floating_point() else t)(torch.as_tensor(a)))
    B = 1
    Sq = 2048
    Skv = 2048
    Hq = 32
    Hkv = 32
    Dh = 192
    bits = 8
    G = 64
    if G <= 0 or G >= Skv:
        G = max(1, Skv // 2)
    Skv = (Skv // G) * G
    ngk = Skv // G
    qmax = (1 << bits) - 1
    return {
        "q": _w((lambda *s: torch.randn(*s, dtype=torch.bfloat16) * 0.2)(B, Hq, Sq, Dh)),
        "qk": _w(torch.randint(0, qmax + 1, (B, Hkv, Skv, Dh)).to(torch.int32)),
        "qv": _w(torch.randint(0, qmax + 1, (B, Hkv, Skv, Dh)).to(torch.int32)),
        "k_scales": _w(torch.abs((lambda *s: torch.randn(*s, dtype=torch.bfloat16) * 0.2)(B, Hkv, ngk, Dh)) * 0.05 + 0.02),
        "k_zeros": _w((lambda *s: torch.randn(*s, dtype=torch.bfloat16) * 0.2)(B, Hkv, ngk, Dh) + float(qmax) / 2.0),
        "v_scales": _w(torch.abs((lambda *s: torch.randn(*s, dtype=torch.bfloat16) * 0.05)(B, Hkv, Skv, 1)) * 0.05 + 0.02),
        "v_zeros": _w((lambda *s: torch.randn(*s, dtype=torch.bfloat16) * 0.05)(B, Hkv, Skv, 1) + float(qmax) / 2.0),
    }

def reference(q, qk, qv, k_scales, k_zeros, v_scales, v_zeros):
    B, Hq, Sq, Dh = q.shape
    Hkv, Skv = qk.shape[1], qk.shape[2]
    ngk = k_scales.shape[2]
    G = Skv // ngk
    ks = (lambda x, n, axis: torch.repeat_interleave(x, n, dim=axis))(k_scales, G, axis=2)
    kz = (lambda x, n, axis: torch.repeat_interleave(x, n, dim=axis))(k_zeros, G, axis=2)
    k = ((lambda x, dt: x.to(dt))(qk, q.dtype) - kz) * ks
    v = ((lambda x, dt: x.to(dt))(qv, q.dtype) - v_zeros) * v_scales
    rep = Hq // Hkv
    if rep > 1:
        k = (lambda x, n, axis: torch.repeat_interleave(x, n, dim=axis))(k, rep, axis=1)
        v = (lambda x, n, axis: torch.repeat_interleave(x, n, dim=axis))(v, rep, axis=1)
    scale = float(Dh) ** -0.5
    logits = torch.einsum("bhqd,bhkd->bhqk", q, k) * scale
    p = _softmax(logits, axis=-1)
    o = torch.einsum("bhqk,bhkd->bhqd", p, v)
    return o
```

```cpp
#include <hip/hip_runtime.h>

__global__ __launch_bounds__(256, 1) void attn_fused(const void* q, const void* qk, const void* qv, const void* ks, const void* kz,
                                                     const void* vs, const void* vz, void* ws, void* out) {
  const int wid = __builtin_amdgcn_readfirstlane(threadIdx.x >> 6);
  asm volatile(
    "s_load_dwordx16 s[8:23], %0, 0x0\n"
    "s_mov_b32 s58, %2\n"
    "s_and_b32 s76, %1, 7\n"
    "s_lshr_b32 s77, %1, 3\n"
    "s_and_b32 s78, s77, 3\n"
    "s_lshl_b32 s56, s76, 2\n"
    "s_add_u32 s56, s56, s78\n"
    "s_lshr_b32 s57, s77, 2\n"
    "v_mbcnt_lo_u32_b32 v231, -1, 0\n"
    "v_mbcnt_hi_u32_b32 v231, -1, v231\n"
    "s_getpc_b64 s[44:45]\n"
    "LPC:\n"
    "s_mov_b32 s46, LSLOW-LPC\n"
    "s_mov_b32 s47, 0x20000\n"
    "s_lshr_b32 s76, %1, 3\n"
    "s_lshl_b32 s76, s76, 2\n"
    "s_add_u32 s76, s76, s58\n"
    "s_lshl_b32 s76, s76, 9\n"
    "v_lshlrev_b32 v196, 3, v231\n"
    "buffer_load_dword v251, v196, s[44:47], s76 offen\n"
    "s_mul_i32 s72, s58, 0xc00\n"
    "s_lshr_b32 s86, s57, 2\n"
    "s_mul_i32 s64, s86, 0xc000\n"
    "s_lshl_b32 s86, s86, 3\n"
    "s_and_b32 s79, s57, 3\n"
    "s_add_u32 s86, s86, s79\n"
    "s_lshl_b32 s86, s86, 2\n"
    "s_xor_b32 s76, s57, 4\n"
    "s_and_b32 s77, s76, 6\n"
    "s_mul_i32 s77, s77, 15\n"
    "s_add_u32 s76, s76, s77\n"
    "s_and_b32 s77, s58, 1\n"
    "s_mul_i32 s78, s77, 12\n"
    "s_add_u32 s78, s78, s86\n"
    "s_lshl_b32 s78, s78, 1\n"
    "s_mul_i32 s77, s77, 0xc0\n"
    "s_movk_i32 s90, 0x556\n"
    "s_lshl_b32 s60, s56, 12\n"
    "s_cmp_lt_u32 s58, 2\n"
    "s_cselect_b32 s76, s76, s78\n"
    "s_cmovk_i32 s90, 0xaab\n"
    "s_cselect_b32 s91, 24, 0\n"
    "s_cselect_b32 s92, 1, 10\n"
    "s_cselect_b32 s89, 7, 0\n"
    "s_cselect_b32 s64, s64, 0\n"
    "s_add_u32 s60, s60, s76\n"
    "s_mul_i32 s60, s60, 0x180\n"
    "s_mul_i32 s76, s76, 0x180\n"
    "s_sub_u32 s34, 0x180000, s76\n"
    "s_mov_b32 s35, 0x20000\n"
    "v_add_u32 v144, s77, v231\n"
    "v_add_u32 v145, 64, v144\n"
    "v_add_u32 v146, 64, v145\n"
    "v_mul_u32_u24 v197, s90, v144\n"
    "v_lshrrev_b32 v197, 16, v197\n"
    "v_mul_u32_u24 v198, s92, v197\n"
    "v_lshrrev_b32 v198, 1, v198\n"
    "v_and_b32 v198, s89, v198\n"
    "v_xor_b32 v198, v198, v144\n"
    "v_mul_u32_u24 v197, s91, v197\n"
    "v_add_lshl_u32 v202, v197, v198, 4\n"
    "v_mul_u32_u24 v197, s90, v145\n"
    "v_lshrrev_b32 v197, 16, v197\n"
    "v_mul_u32_u24 v198, s92, v197\n"
    "v_lshrrev_b32 v198, 1, v198\n"
    "v_and_b32 v198, s89, v198\n"
    "v_xor_b32 v198, v198, v144\n"
    "v_mul_u32_u24 v197, s91, v197\n"
    "v_add_lshl_u32 v203, v197, v198, 4\n"
    "v_mul_u32_u24 v197, s90, v146\n"
    "v_lshrrev_b32 v197, 16, v197\n"
    "v_mul_u32_u24 v198, s92, v197\n"
    "v_lshrrev_b32 v198, 1, v198\n"
    "v_and_b32 v198, s89, v198\n"
    "v_xor_b32 v198, v198, v144\n"
    "v_mul_u32_u24 v197, s91, v197\n"
    "v_add_lshl_u32 v204, v197, v198, 4\n"
    "s_waitcnt lgkmcnt(0)\n"
    "s_cmp_lt_u32 s58, 2\n"
    "s_cselect_b64 s[80:81], s[10:11], s[12:13]\n"
    "s_cselect_b64 s[82:83], s[16:17], s[14:15]\n"
    "s_add_u32 s32, s80, s60\n"
    "s_addc_u32 s33, s81, 0\n"
    "s_cmp_eq_u32 s64, 0\n"
    "s_cbranch_scc1 LNOR\n"
    "s_add_u32 m0, s72, 0x12000\n"
    "s_nop 0\n"
    "buffer_load_dwordx4 v202, s[32:35], 0 offen nt lds\n"
    "buffer_load_dwordx4 v203, s[32:35], 0 offen offset:1024 nt lds\n"
    "buffer_load_dwordx4 v204, s[32:35], 0 offen offset:2048 nt lds\n"
    "LNOR:\n"
    "s_add_u32 m0, s72, 0x15000\n"
    "s_nop 0\n"
    "buffer_load_dwordx4 v202, s[32:35], s64 offen nt lds\n"
    "buffer_load_dwordx4 v203, s[32:35], s64 offen offset:1024 nt lds\n"
    "buffer_load_dwordx4 v204, s[32:35], s64 offen offset:2048 nt lds\n"
    "LR0:\n"
    "s_load_dwordx2 s[24:25], %0, 0x40\n"
    "s_mul_i32 s71, s58, 0x1800\n"
    "s_lshr_b32 s87, s57, 2\n"
    "s_mul_i32 s87, s87, 0x6000\n"
    "s_mul_i32 s61, s56, 0x3000\n"
    "s_lshl_b32 s62, s56, 12\n"
    "s_lshl_b32 s59, s56, 12\n"
    "s_mov_b32 s50, 0x3000\n"
    "s_mov_b32 s51, 0x20000\n"
    "s_and_b32 s77, s57, 1\n"
    "s_mul_i32 s77, s77, 0xc0\n"
    "v_mul_u32_u24 v197, 0x1556, v144\n"
    "v_lshrrev_b32 v197, 16, v197\n"
    "v_mul_u32_u24 v196, 12, v197\n"
    "v_sub_u32 v196, v144, v196\n"
    "v_mul_u32_u24 v248, 0x180, v197\n"
    "v_lshl_add_u32 v248, v196, 4, v248\n"
    "v_add_u32 v248, s77, v248\n"
    "s_add_u32 s48, s82, s61\n"
    "s_addc_u32 s49, s83, 0\n"
    "s_add_u32 m0, s72, 0x21000\n"
    "s_nop 0\n"
    "buffer_load_dwordx4 v248, s[48:51], 0 offen lds\n"
    "s_add_u32 s40, s20, s62\n"
    "s_addc_u32 s41, s21, 0\n"
    "s_add_u32 s52, s18, s62\n"
    "s_addc_u32 s53, s19, 0\n"
    "s_mov_b32 s42, 0x1000\n"
    "s_mov_b32 s43, 0x20000\n"
    "s_mov_b32 s54, 0x1000\n"
    "s_mov_b32 s55, 0x20000\n"
    "v_lshlrev_b32 v196, 1, v231\n"
    "buffer_load_ushort v44, v196, s[40:43], 0 offen\n"
    "buffer_load_ushort v45, v196, s[52:55], 0 offen\n"
    "s_lshl_b32 s77, s58, 6\n"
    "v_add_u32 v41, s77, v231\n"
    "v_bfe_u32 v197, v231, 2, 1\n"
    "v_mul_u32_u24 v197, 12, v197\n"
    "v_and_b32 v198, 7, v231\n"
    "v_add3_u32 v197, v197, v198, s86\n"
    "v_lshrrev_b32 v40, 3, v41\n"
    "v_lshlrev_b32 v40, 7, v40\n"
    "v_lshl_add_u32 v40, v197, 1, v40\n"
    "buffer_load_ushort v42, v40, s[40:43], 0 offen\n"
    "buffer_load_ushort v43, v40, s[52:55], 0 offen\n"
    "s_add_u32 s44, s22, s59\n"
    "s_addc_u32 s45, s23, 0\n"
    "s_mov_b32 s46, 0x900\n"
    "s_mov_b32 s47, 0x20000\n"
    "v_mov_b32 v196, 0x40000000\n"
    "s_cmp_eq_u32 s58, 0\n"
    "s_cselect_b32 s77, 64, 0\n"
    "s_cselect_b32 s74, 1, 0\n"
    "v_and_b32 v213, 7, v231\n"
    "v_mul_u32_u24 v213, 0x100, v213\n"
    "v_cmp_gt_u32 vcc, s77, v231\n"
    "v_mov_b32 v197, 0x800\n"
    "s_mul_i32 s77, s57, 0x100\n"
    "v_cndmask_b32 v213, v196, v213, vcc\n"
    "v_cndmask_b32 v197, v196, v197, vcc\n"
    "buffer_load_dword v221, v197, s[44:47], 0 offen sc1\n"
    "v_cmp_gt_u32 vcc, s74, v231\n"
    "v_mov_b32 v214, s77\n"
    "s_mul_i32 s66, s74, 3\n"
    "v_cndmask_b32 v214, v196, v214, vcc\n"
    "s_add_u32 s78, s64, 0xc000\n"
    "s_add_u32 m0, s72, 0x1e000\n"
    "s_nop 0\n"
    "buffer_load_dwordx4 v202, s[32:35], s78 offen nt lds\n"
    "buffer_load_dwordx4 v203, s[32:35], s78 offen offset:1024 nt lds\n"
    "buffer_load_dwordx4 v204, s[32:35], s78 offen offset:2048 nt lds\n"
    "s_lshl_b32 s76, s56, 3\n"
    "s_add_u32 s76, s76, s57\n"
    "s_mul_i32 s76, s76, 0x18000\n"
    "s_add_u32 s28, s8, s76\n"
    "s_addc_u32 s29, s9, 0\n"
    "s_mov_b32 s30, 0x18000\n"
    "s_mov_b32 s31, 0x20000\n"
    "s_mul_i32 s88, s58, 0x6000\n"
    "v_mul_u32_u24 v197, 0xaab, v231\n"
    "v_lshrrev_b32 v197, 16, v197\n"
    "v_mul_u32_u24 v196, 24, v197\n"
    "v_sub_u32 v196, v231, v196\n"
    "v_mul_u32_u24 v152, 0x180, v197\n"
    "v_lshrrev_b32 v197, 1, v197\n"
    "v_xor_b32 v196, v196, v197\n"
    "v_lshl_add_u32 v152, v196, 4, v152\n"
    "v_add_u32 v198, 64, v231\n"
    "v_mul_u32_u24 v197, 0xaab, v198\n"
    "v_lshrrev_b32 v197, 16, v197\n"
    "v_mul_u32_u24 v196, 24, v197\n"
    "v_sub_u32 v196, v198, v196\n"
    "v_mul_u32_u24 v153, 0x180, v197\n"
    "v_lshrrev_b32 v197, 1, v197\n"
    "v_xor_b32 v196, v196, v197\n"
    "v_lshl_add_u32 v153, v196, 4, v153\n"
    "v_add_u32 v198, 0x80, v231\n"
    "v_mul_u32_u24 v197, 0xaab, v198\n"
    "v_lshrrev_b32 v197, 16, v197\n"
    "v_mul_u32_u24 v196, 24, v197\n"
    "v_sub_u32 v196, v198, v196\n"
    "v_mul_u32_u24 v154, 0x180, v197\n"
    "v_lshrrev_b32 v197, 1, v197\n"
    "v_xor_b32 v196, v196, v197\n"
    "v_lshl_add_u32 v154, v196, 4, v154\n"
    "s_mov_b32 s76, 0xc00\n"
    "v_xad_u32 v155, v152, 64, s76\n"
    "v_xad_u32 v156, v153, 64, s76\n"
    "v_xad_u32 v157, v154, 64, s76\n"
    "s_add_u32 s78, s88, 0\n"
    "s_add_u32 m0, s71, 0\n"
    "s_nop 0\n"
    "buffer_load_dwordx4 v152, s[28:31], s78 offen nt lds\n"
    "s_add_u32 s78, s88, 0\n"
    "s_add_u32 m0, s71, 0x400\n"
    "s_nop 0\n"
    "buffer_load_dwordx4 v153, s[28:31], s78 offen nt lds\n"
    "s_add_u32 s78, s88, 0\n"
    "s_add_u32 m0, s71, 0x800\n"
    "s_nop 0\n"
    "buffer_load_dwordx4 v154, s[28:31], s78 offen nt lds\n"
    "v_mov_b32 v196, 0x40000000\n"
    "v_and_b32 v46, 15, v231\n"
    "v_lshrrev_b32 v47, 4, v231\n"
    "s_mov_b32 s67, 0x3dd53b95\n"
    "v_min_u32 v199, 2, v47\n"
    "v_lshlrev_b32 v197, 3, v199\n"
    "s_lshl_b32 s76, s58, 1\n"
    "v_add_u32 v197, s76, v197\n"
    "v_lshrrev_b32 v198, 1, v46\n"
    "v_xor_b32 v197, v197, v198\n"
    "v_mul_u32_u24 v205, 0x180, v46\n"
    "v_lshl_add_u32 v205, v197, 4, v205\n"
    "v_add_u32 v205, 0x12000, v205\n"
    "v_xor_b32 v206, 16, v205\n"
    "v_and_b32 v197, 7, v231\n"
    "v_lshl_add_u32 v209, v199, 5, v197\n"
    "s_lshl_b32 s76, s58, 3\n"
    "v_add_lshl_u32 v209, v209, s76, 1\n"
    "v_add_u32 v217, 0x21000, v209\n"
    "s_lshr_b32 s76, s57, 2\n"
    "s_mul_i32 s76, s76, 0xc0\n"
    "s_add_u32 s76, s76, 0x20f40\n"
    "v_add_u32 v209, s76, v209\n"
    "v_lshrrev_b32 v198, 3, v231\n"
    "s_mul_i32 s76, s58, 12\n"
    "v_lshl_add_u32 v199, v198, 1, s76\n"
    "v_mul_u32_u24 v207, 5, v197\n"
    "v_and_b32 v207, 9, v207\n"
    "v_xor_b32 v199, v199, v207\n"
    "v_mul_u32_u24 v207, 0x300, v197\n"
    "v_lshl_add_u32 v207, v199, 4, v207\n"
    "v_add_u32 v207, 0x13800, v207\n"
    "v_xor_b32 v208, 16, v207\n"
    "s_mul_i32 s76, s58, 0xc0\n"
    "s_add_u32 s76, s76, 0x13800\n"
    "v_lshl_add_u32 v207, v231, 2, s76\n"
    "v_lshlrev_b32 v210, 1, v197\n"
    "v_add_u32 v210, 0x24000, v210\n"
    "s_mul_i32 s76, s58, 0x480\n"
    "s_add_u32 s76, s76, 0x24400\n"
    "v_mul_u32_u24 v215, 0x90, v197\n"
    "v_lshl_add_u32 v215, v198, 4, v215\n"
    "v_add_u32 v215, s76, v215\n"
    "v_and_b32 v197, 1, v197\n"
    "v_lshlrev_b32 v197, 3, v197\n"
    "v_sub_u32 v215, v215, v197\n"
    "v_bfe_u32 v197, v231, 2, 2\n"
    "v_mul_u32_u24 v216, 0x90, v197\n"
    "v_lshl_add_u32 v216, v47, 5, v216\n"
    "v_and_b32 v197, 1, v197\n"
    "v_lshlrev_b32 v197, 3, v197\n"
    "v_sub_u32 v216, v216, v197\n"
    "v_and_b32 v197, 3, v231\n"
    "v_lshl_add_u32 v216, v197, 3, v216\n"
    "v_add_u32 v216, s76, v216\n"
    "s_lshr_b32 s76, s57, 1\n"
    "s_xor_b32 s76, s76, 2\n"
    "s_mul_i32 s76, s76, 0x1800\n"
    "s_and_b32 s77, s57, 1\n"
    "s_mul_i32 s77, s77, 0xc00\n"
    "s_add_u32 s76, s76, s77\n"
    "s_lshl_b32 s77, s58, 8\n"
    "s_add_u32 s76, s76, s77\n"
    "v_lshlrev_b32 v197, 4, v46\n"
    "v_lshl_add_u32 v211, v47, 10, v197\n"
    "v_add_u32 v211, s76, v211\n"
    "s_lshr_b32 s76, s57, 2\n"
    "s_lshl_b32 s76, s76, 10\n"
    "s_lshl_b32 s77, s79, 8\n"
    "s_add_u32 s76, s76, s77\n"
    "s_mul_i32 s77, s58, 48\n"
    "v_add_u32 v198, s77, v231\n"
    "v_lshrrev_b32 v197, 5, v198\n"
    "v_lshl_add_u32 v212, v197, 12, s76\n"
    "v_bfe_u32 v197, v198, 2, 1\n"
    "v_lshl_add_u32 v212, v197, 11, v212\n"
    "v_bfe_u32 v197, v198, 3, 2\n"
    "v_lshl_add_u32 v212, v197, 6, v212\n"
    "v_cmp_gt_u32 vcc, 48, v231\n"
    "v_and_b32 v197, 3, v198\n"
    "v_lshl_add_u32 v212, v197, 4, v212\n"
    "v_cndmask_b32 v211, v196, v211, vcc\n"
    "v_cndmask_b32 v212, v196, v212, vcc\n"
    "v_mov_b32 v222, 0\n"
    "v_mov_b32 v223, 0\n"
    "v_mov_b32 v224, 0\n"
    "v_mov_b32 v225, 0\n"
    "v_mov_b32 v160, 0\n"
    "v_mov_b32 v161, 0\n"
    "v_mov_b32 v162, 0\n"
    "v_mov_b32 v163, 0\n"
    "v_mov_b32 v164, 0\n"
    "v_mov_b32 v165, 0\n"
    "v_mov_b32 v172, 0\n"
    "v_mov_b32 v173, 0\n"
    "v_mov_b32 v174, 0\n"
    "v_mov_b32 v175, 0\n"
    "v_lshlrev_b32 v41, 1, v41\n"
    "v_add_u32 v41, 0x24000, v41\n"
    "s_mul_i32 s76, s56, 0xc0000\n"
    "s_add_u32 s77, s76, 0x40000\n"
    "s_add_u32 s36, s22, s77\n"
    "s_addc_u32 s37, s23, 0\n"
    "s_mov_b32 s38, 0xc0000\n"
    "s_mov_b32 s39, 0x20000\n"
    "s_cmp_eq_u32 s87, 0\n"
    "s_cselect_b32 s62, 0x40000000, 0\n"
    "s_waitcnt vmcnt(10)\n"
    "v_lshlrev_b32 v44, 16, v44\n"
    "s_waitcnt vmcnt(9)\n"
    "v_lshlrev_b32 v45, 16, v45\n"
    "v_mul_f32 v197, v44, v45\n"
    "s_waitcnt vmcnt(8)\n"
    "ds_write_b16 v41, v42\n"
    "s_waitcnt vmcnt(7)\n"
    "ds_write_b16 v41, v43 offset:512\n"
    "v_min_f32_dpp v197, v197, v197 row_ror:8 row_mask:0xf bank_mask:0xf\n"
    "s_mul_i32 s76, s56, 0xc0000\n"
    "s_add_u32 s77, s76, 0x1840000\n"
    "v_min_f32_dpp v197, v197, v197 row_ror:4 row_mask:0xf bank_mask:0xf\n"
    "s_add_u32 s40, s22, s77\n"
    "s_addc_u32 s41, s23, 0\n"
    "v_min_f32_dpp v197, v197, v197 row_ror:2 row_mask:0xf bank_mask:0xf\n"
    "s_mov_b32 s42, 0xc0000\n"
    "s_mov_b32 s43, 0x20000\n"
    "v_min_f32_dpp v197, v197, v197 row_ror:1 row_mask:0xf bank_mask:0xf\n"
    "v_mov_b32 v198, v197\n"
    "s_nop 1\n"
    "v_permlane32_swap_b32 v197, v198\n"
    "v_min_f32 v197, v197, v198\n"
    "v_mov_b32 v198, v197\n"
    "s_nop 1\n"
    "v_permlane16_swap_b32 v197, v198\n"
    "v_min_f32 v197, v197, v198\n"
    "v_rcp_f32 v198, v197\n"
    "v_subrev_u32 v199, 0x37800000, v197\n"
    "v_mul_f32 v197, 0x3fcccccd, v197\n"
    "v_cmp_gt_u32 vcc, 0x10000000, v199\n"
    "v_mul_f32 v198, 0x3f3425ed, v198\n"
    "v_mov_b32 v199, 0x3f800000\n"
    "v_cndmask_b32 v197, 0, v197, vcc\n"
    "v_cndmask_b32 v198, v199, v198, vcc\n"
    "v_readfirstlane_b32 s70, v197\n"
    "v_mul_f32 v197, v197, v198\n"
    "v_readfirstlane_b32 s68, v198\n"
    "v_readfirstlane_b32 s69, v197\n"
    "s_waitcnt lgkmcnt(0)\n"
    "s_barrier\n"
    "ds_read_u16_d16_hi v172, v217 offset:6144\n"
    "ds_read_u16_d16_hi v173, v217\n"
    "ds_read_u16_d16_hi v174, v217 offset:6336\n"
    "ds_read_u16_d16_hi v175, v217 offset:192\n"
    "ds_read_u16_d16_hi v223, v209 offset:6144\n"
    "ds_read_u16_d16_hi v222, v209\n"
    "ds_read_b128 v[232:235], v205\n"
    "ds_read_b128 v[236:239], v206\n"
    "ds_read_u16_d16_hi v161, v209 offset:6336\n"
    "ds_read_u16_d16_hi v160, v209 offset:192\n"
    "ds_read_b128 v[64:67], v205 offset:12288\n"
    "ds_read_b128 v[68:71], v206 offset:12288\n"
    "s_waitcnt lgkmcnt(10)\n"
    "v_mul_f32 v197, v172, v173\n"
    "s_waitcnt lgkmcnt(8)\n"
    "v_mul_f32 v198, v174, v175\n"
    "v_min_f32 v197, v197, v198\n"
    "v_max_f32 v197, 0, v197\n"
    "v_mul_f32 v217, 0x3e15434e, v197\n"
    "ds_read_u16_d16_hi v225, v210 offset:512\n"
    "ds_read_u16_d16_hi v224, v210\n"
    "ds_read2st64_b32 v[240:241], v207 offset0:48 offset1:51\n"
    "ds_read2st64_b32 v[242:243], v207 offset0:54 offset1:57\n"
    "ds_read2st64_b32 v[244:245], v207 offset0:60 offset1:63\n"
    "ds_read2st64_b32 v[246:247], v207 offset0:66 offset1:69\n"
    "s_waitcnt lgkmcnt(13)\n"
    "v_mul_f32 v226, s67, v223\n"
    "s_waitcnt lgkmcnt(12)\n"
    "v_fma_f32 v227, -v222, v226, v217\n"
    "s_waitcnt lgkmcnt(11)\n"
    "v_cvt_f32_i32 v232, v232\n"
    "v_cvt_f32_i32 v233, v233\n"
    "v_cvt_f32_i32 v234, v234\n"
    "v_cvt_f32_i32 v235, v235\n"
    "s_waitcnt lgkmcnt(10)\n"
    "v_cvt_f32_i32 v236, v236\n"
    "v_cvt_f32_i32 v237, v237\n"
    "v_cvt_f32_i32 v238, v238\n"
    "v_cvt_f32_i32 v239, v239\n"
    "v_mul_f32_dpp v232, v226, v232 row_newbcast:0 row_mask:0xf bank_mask:0xf\n"
    "v_mul_f32_dpp v233, v226, v233 row_newbcast:1 row_mask:0xf bank_mask:0xf\n"
    "v_mul_f32_dpp v234, v226, v234 row_newbcast:2 row_mask:0xf bank_mask:0xf\n"
    "v_mul_f32_dpp v235, v226, v235 row_newbcast:3 row_mask:0xf bank_mask:0xf\n"
    "v_mul_f32_dpp v236, v226, v236 row_newbcast:4 row_mask:0xf bank_mask:0xf\n"
    "v_mul_f32_dpp v237, v226, v237 row_newbcast:5 row_mask:0xf bank_mask:0xf\n"
    "v_mul_f32_dpp v238, v226, v238 row_newbcast:6 row_mask:0xf bank_mask:0xf\n"
    "v_mul_f32_dpp v239, v226, v239 row_newbcast:7 row_mask:0xf bank_mask:0xf\n"
    "v_add_f32_dpp v232, v227, v232 row_newbcast:0 row_mask:0xf bank_mask:0xf\n"
    "v_add_f32_dpp v233, v227, v233 row_newbcast:1 row_mask:0xf bank_mask:0xf\n"
    "v_add_f32_dpp v234, v227, v234 row_newbcast:2 row_mask:0xf bank_mask:0xf\n"
    "v_add_f32_dpp v235, v227, v235 row_newbcast:3 row_mask:0xf bank_mask:0xf\n"
    "v_add_f32_dpp v236, v227, v236 row_newbcast:4 row_mask:0xf bank_mask:0xf\n"
    "v_add_f32_dpp v237, v227, v237 row_newbcast:5 row_mask:0xf bank_mask:0xf\n"
    "v_add_f32_dpp v238, v227, v238 row_newbcast:6 row_mask:0xf bank_mask:0xf\n"
    "v_add_f32_dpp v239, v227, v239 row_newbcast:7 row_mask:0xf bank_mask:0xf\n"
    "v_cvt_pk_bf16_f32 v232, v232, v233\n"
    "v_cvt_pk_bf16_f32 v233, v234, v235\n"
    "v_cvt_pk_bf16_f32 v234, v236, v237\n"
    "v_cvt_pk_bf16_f32 v235, v238, v239\n"
    "buffer_store_dwordx4 v[232:235], v211, s[36:39], s62 offen sc1\n"
    "s_waitcnt lgkmcnt(9)\n"
    "v_mul_f32 v166, s67, v161\n"
    "s_waitcnt lgkmcnt(8)\n"
    "v_fma_f32 v167, -v160, v166, v217\n"
    "s_waitcnt lgkmcnt(7)\n"
    "v_cvt_f32_i32 v64, v64\n"
    "v_cvt_f32_i32 v65, v65\n"
    "v_cvt_f32_i32 v66, v66\n"
    "v_cvt_f32_i32 v67, v67\n"
    "s_waitcnt lgkmcnt(6)\n"
    "v_cvt_f32_i32 v68, v68\n"
    "v_cvt_f32_i32 v69, v69\n"
    "v_cvt_f32_i32 v70, v70\n"
    "v_cvt_f32_i32 v71, v71\n"
    "v_mul_f32_dpp v64, v166, v64 row_newbcast:0 row_mask:0xf bank_mask:0xf\n"
    "v_mul_f32_dpp v65, v166, v65 row_newbcast:1 row_mask:0xf bank_mask:0xf\n"
    "v_mul_f32_dpp v66, v166, v66 row_newbcast:2 row_mask:0xf bank_mask:0xf\n"
    "v_mul_f32_dpp v67, v166, v67 row_newbcast:3 row_mask:0xf bank_mask:0xf\n"
    "v_mul_f32_dpp v68, v166, v68 row_newbcast:4 row_mask:0xf bank_mask:0xf\n"
    "v_mul_f32_dpp v69, v166, v69 row_newbcast:5 row_mask:0xf bank_mask:0xf\n"
    "v_mul_f32_dpp v70, v166, v70 row_newbcast:6 row_mask:0xf bank_mask:0xf\n"
    "v_mul_f32_dpp v71, v166, v71 row_newbcast:7 row_mask:0xf bank_mask:0xf\n"
    "v_add_f32_dpp v64, v167, v64 row_newbcast:0 row_mask:0xf bank_mask:0xf\n"
    "v_add_f32_dpp v65, v167, v65 row_newbcast:1 row_mask:0xf bank_mask:0xf\n"
    "v_add_f32_dpp v66, v167, v66 row_newbcast:2 row_mask:0xf bank_mask:0xf\n"
    "v_add_f32_dpp v67, v167, v67 row_newbcast:3 row_mask:0xf bank_mask:0xf\n"
    "v_add_f32_dpp v68, v167, v68 row_newbcast:4 row_mask:0xf bank_mask:0xf\n"
    "v_add_f32_dpp v69, v167, v69 row_newbcast:5 row_mask:0xf bank_mask:0xf\n"
    "v_add_f32_dpp v70, v167, v70 row_newbcast:6 row_mask:0xf bank_mask:0xf\n"
    "v_add_f32_dpp v71, v167, v71 row_newbcast:7 row_mask:0xf bank_mask:0xf\n"
    "v_cvt_pk_bf16_f32 v64, v64, v65\n"
    "v_cvt_pk_bf16_f32 v65, v66, v67\n"
    "v_cvt_pk_bf16_f32 v66, v68, v69\n"
    "v_cvt_pk_bf16_f32 v67, v70, v71\n"
    "s_add_u32 s78, s87, 0\n"
    "buffer_store_dwordx4 v[64:67], v211, s[36:39], s78 offen sc1\n"
    "s_waitcnt lgkmcnt(5)\n"
    "v_mul_f32 v228, s68, v225\n"
    "s_waitcnt lgkmcnt(4)\n"
    "v_fma_f32 v229, -v224, v228, s69\n"
    "s_waitcnt lgkmcnt(3)\n"
    "v_cvt_f32_i32 v240, v240\n"
    "v_cvt_f32_i32 v241, v241\n"
    "s_waitcnt lgkmcnt(2)\n"
    "v_cvt_f32_i32 v242, v242\n"
    "v_cvt_f32_i32 v243, v243\n"
    "s_waitcnt lgkmcnt(1)\n"
    "v_cvt_f32_i32 v244, v244\n"
    "v_cvt_f32_i32 v245, v245\n"
    "s_waitcnt lgkmcnt(0)\n"
    "v_cvt_f32_i32 v246, v246\n"
    "v_cvt_f32_i32 v247, v247\n"
    "v_mul_f32_dpp v240, v228, v240 row_newbcast:0 row_mask:0xf bank_mask:0xf\n"
    "v_mul_f32_dpp v241, v228, v241 row_newbcast:1 row_mask:0xf bank_mask:0xf\n"
    "v_mul_f32_dpp v242, v228, v242 row_newbcast:2 row_mask:0xf bank_mask:0xf\n"
    "v_mul_f32_dpp v243, v228, v243 row_newbcast:3 row_mask:0xf bank_mask:0xf\n"
    "v_mul_f32_dpp v244, v228, v244 row_newbcast:4 row_mask:0xf bank_mask:0xf\n"
    "v_mul_f32_dpp v245, v228, v245 row_newbcast:5 row_mask:0xf bank_mask:0xf\n"
    "v_mul_f32_dpp v246, v228, v246 row_newbcast:6 row_mask:0xf bank_mask:0xf\n"
    "v_mul_f32_dpp v247, v228, v247 row_newbcast:7 row_mask:0xf bank_mask:0xf\n"
    "v_add_f32_dpp v240, v229, v240 row_newbcast:0 row_mask:0xf bank_mask:0xf\n"
    "v_add_f32_dpp v241, v229, v241 row_newbcast:1 row_mask:0xf bank_mask:0xf\n"
    "v_add_f32_dpp v242, v229, v242 row_newbcast:2 row_mask:0xf bank_mask:0xf\n"
    "v_add_f32_dpp v243, v229, v243 row_newbcast:3 row_mask:0xf bank_mask:0xf\n"
    "v_add_f32_dpp v244, v229, v244 row_newbcast:4 row_mask:0xf bank_mask:0xf\n"
    "v_add_f32_dpp v245, v229, v245 row_newbcast:5 row_mask:0xf bank_mask:0xf\n"
    "v_add_f32_dpp v246, v229, v246 row_newbcast:6 row_mask:0xf bank_mask:0xf\n"
    "v_add_f32_dpp v247, v229, v247 row_newbcast:7 row_mask:0xf bank_mask:0xf\n"
    "v_cvt_pk_bf16_f32 v240, v240, v241\n"
    "v_cvt_pk_bf16_f32 v241, v242, v243\n"
    "v_cvt_pk_bf16_f32 v242, v244, v245\n"
    "v_cvt_pk_bf16_f32 v243, v246, v247\n"
    "s_waitcnt vmcnt(8)\n"
    "v_add_u32 v221, s74, v221\n"
    "s_mov_b32 s73, 0\n"
    "v_readfirstlane_b32 s75, v221\n"
    "s_mov_b32 s78, 0\n"
    "buffer_store_dwordx4 v[240:243], v212, s[40:43], s78 offen sc1\n"
    "s_add_u32 s78, s88, 0\n"
    "s_add_u32 m0, s71, 0xc00\n"
    "s_nop 0\n"
    "buffer_load_dwordx4 v155, s[28:31], s78 offen nt lds\n"
    "s_add_u32 s78, s88, 0\n"
    "s_add_u32 m0, s71, 0x1000\n"
    "s_nop 0\n"
    "buffer_load_dwordx4 v156, s[28:31], s78 offen nt lds\n"
    "s_add_u32 s78, s88, 0\n"
    "s_add_u32 m0, s71, 0x1400\n"
    "s_nop 0\n"
    "buffer_load_dwordx4 v157, s[28:31], s78 offen nt lds\n"
    "s_add_u32 s78, s88, 0x1800\n"
    "s_add_u32 m0, s71, 0x6000\n"
    "s_nop 0\n"
    "buffer_load_dwordx4 v152, s[28:31], s78 offen nt lds\n"
    "v_lshlrev_b32 v200, 4, v231\n"
    "v_add_u32 v201, 0xc000, v200\n"
    "v_mul_u32_u24 v198, 0x180, v46\n"
    "v_add_u32 v198, s71, v198\n"
    "v_lshrrev_b32 v197, 1, v46\n"
    "v_xor_b32 v197, v197, v47\n"
    "v_lshl_add_u32 v158, v197, 4, v198\n"
    "v_xor_b32 v159, 64, v158\n"
    "v_and_b32 v197, 3, v231\n"
    "v_cmp_eq_u32 vcc, 0, v197\n"
    "v_mov_b32 v198, 0x3f803f80\n"
    "s_nop 0\n"
    "v_cndmask_b32 v192, 0, v198, vcc\n"
    "v_cndmask_b32 v193, 0, v198, vcc\n"
    "v_cndmask_b32 v194, 0, v198, vcc\n"
    "v_cndmask_b32 v195, 0, v198, vcc\n"
    "s_and_b32 s77, s57, 1\n"
    "s_mul_i32 s77, s77, 0xc0\n"
    "s_sub_u32 s77, s77, 0x400\n"
    "v_mul_u32_u24 v197, 0x1556, v145\n"
    "v_lshrrev_b32 v197, 16, v197\n"
    "v_mul_u32_u24 v196, 12, v197\n"
    "v_sub_u32 v196, v145, v196\n"
    "v_mul_u32_u24 v249, 0x180, v197\n"
    "v_lshl_add_u32 v249, v196, 4, v249\n"
    "v_add_u32 v249, s77, v249\n"
    "s_and_b32 s77, s57, 1\n"
    "s_mul_i32 s77, s77, 0xc0\n"
    "s_sub_u32 s77, s77, 0x800\n"
    "v_mul_u32_u24 v197, 0x1556, v146\n"
    "v_lshrrev_b32 v197, 16, v197\n"
    "v_mul_u32_u24 v196, 12, v197\n"
    "v_sub_u32 v196, v146, v196\n"
    "v_mul_u32_u24 v250, 0x180, v197\n"
    "v_lshl_add_u32 v250, v196, 4, v250\n"
    "v_add_u32 v250, s77, v250\n"
    "s_mul_i32 s66, s74, 2\n"
    "s_waitcnt vmcnt(4)\n"
    "s_barrier\n"
    "v_mov_b32 v220, 2\n"
    "buffer_store_dwordx2 v[220:221], v214, s[44:47], 0 offen sc1\n"
    "s_add_u32 s78, s64, 0x18000\n"
    "s_add_u32 m0, s72, 0x1b000\n"
    "s_nop 0\n"
    "buffer_load_dwordx4 v202, s[32:35], s78 offen nt lds\n"
    "buffer_load_dwordx4 v203, s[32:35], s78 offen offset:1024 nt lds\n"
    "buffer_load_dwordx4 v204, s[32:35], s78 offen offset:2048 nt lds\n"
    "ds_read_u16_d16_hi v163, v209 offset:6528\n"
    "ds_read_u16_d16_hi v162, v209 offset:384\n"
    "ds_read_b128 v[72:75], v205 offset:49152\n"
    "ds_read_b128 v[76:79], v206 offset:49152\n"
    "ds_read_u16_d16_hi v165, v210 offset:528\n"
    "ds_read_u16_d16_hi v164, v210 offset:16\n"
    "ds_read2st64_b32 v[80:81], v207 offset0:192 offset1:195\n"
    "ds_read2st64_b32 v[82:83], v207 offset0:198 offset1:201\n"
    "ds_read2st64_b32 v[84:85], v207 offset0:204 offset1:207\n"
    "ds_read2st64_b32 v[86:87], v207 offset0:210 offset1:213\n"
    "s_waitcnt lgkmcnt(9)\n"
    "v_mul_f32 v168, s67, v163\n"
    "s_waitcnt lgkmcnt(8)\n"
    "v_fma_f32 v169, -v162, v168, v217\n"
    "s_waitcnt lgkmcnt(7)\n"
    "v_cvt_f32_i32 v72, v72\n"
    "v_cvt_f32_i32 v73, v73\n"
    "v_cvt_f32_i32 v74, v74\n"
    "v_cvt_f32_i32 v75, v75\n"
    "s_waitcnt lgkmcnt(6)\n"
    "v_cvt_f32_i32 v76, v76\n"
    "v_cvt_f32_i32 v77, v77\n"
    "v_cvt_f32_i32 v78, v78\n"
    "v_cvt_f32_i32 v79, v79\n"
    "v_mul_f32_dpp v72, v168, v72 row_newbcast:0 row_mask:0xf bank_mask:0xf\n"
    "v_mul_f32_dpp v73, v168, v73 row_newbcast:1 row_mask:0xf bank_mask:0xf\n"
    "v_mul_f32_dpp v74, v168, v74 row_newbcast:2 row_mask:0xf bank_mask:0xf\n"
    "v_mul_f32_dpp v75, v168, v75 row_newbcast:3 row_mask:0xf bank_mask:0xf\n"
    "v_mul_f32_dpp v76, v168, v76 row_newbcast:4 row_mask:0xf bank_mask:0xf\n"
    "v_mul_f32_dpp v77, v168, v77 row_newbcast:5 row_mask:0xf bank_mask:0xf\n"
    "v_mul_f32_dpp v78, v168, v78 row_newbcast:6 row_mask:0xf bank_mask:0xf\n"
    "v_mul_f32_dpp v79, v168, v79 row_newbcast:7 row_mask:0xf bank_mask:0xf\n"
    "v_add_f32_dpp v72, v169, v72 row_newbcast:0 row_mask:0xf bank_mask:0xf\n"
    "v_add_f32_dpp v73, v169, v73 row_newbcast:1 row_mask:0xf bank_mask:0xf\n"
    "v_add_f32_dpp v74, v169, v74 row_newbcast:2 row_mask:0xf bank_mask:0xf\n"
    "v_add_f32_dpp v75, v169, v75 row_newbcast:3 row_mask:0xf bank_mask:0xf\n"
    "v_add_f32_dpp v76, v169, v76 row_newbcast:4 row_mask:0xf bank_mask:0xf\n"
    "v_add_f32_dpp v77, v169, v77 row_newbcast:5 row_mask:0xf bank_mask:0xf\n"
    "v_add_f32_dpp v78, v169, v78 row_newbcast:6 row_mask:0xf bank_mask:0xf\n"
    "v_add_f32_dpp v79, v169, v79 row_newbcast:7 row_mask:0xf bank_mask:0xf\n"
    "v_cvt_pk_bf16_f32 v72, v72, v73\n"
    "v_cvt_pk_bf16_f32 v73, v74, v75\n"
    "v_cvt_pk_bf16_f32 v74, v76, v77\n"
    "v_cvt_pk_bf16_f32 v75, v78, v79\n"
    "s_waitcnt lgkmcnt(5)\n"
    "v_mul_f32 v170, s68, v165\n"
    "s_waitcnt lgkmcnt(4)\n"
    "v_fma_f32 v171, -v164, v170, s69\n"
    "s_waitcnt lgkmcnt(3)\n"
    "v_cvt_f32_i32 v80, v80\n"
    "v_cvt_f32_i32 v81, v81\n"
    "s_waitcnt lgkmcnt(2)\n"
    "v_cvt_f32_i32 v82, v82\n"
    "v_cvt_f32_i32 v83, v83\n"
    "s_waitcnt lgkmcnt(1)\n"
    "v_cvt_f32_i32 v84, v84\n"
    "v_cvt_f32_i32 v85, v85\n"
    "s_waitcnt lgkmcnt(0)\n"
    "v_cvt_f32_i32 v86, v86\n"
    "v_cvt_f32_i32 v87, v87\n"
    "v_mul_f32_dpp v80, v170, v80 row_newbcast:0 row_mask:0xf bank_mask:0xf\n"
    "v_mul_f32_dpp v81, v170, v81 row_newbcast:1 row_mask:0xf bank_mask:0xf\n"
    "v_mul_f32_dpp v82, v170, v82 row_newbcast:2 row_mask:0xf bank_mask:0xf\n"
    "v_mul_f32_dpp v83, v170, v83 row_newbcast:3 row_mask:0xf bank_mask:0xf\n"
    "v_mul_f32_dpp v84, v170, v84 row_newbcast:4 row_mask:0xf bank_mask:0xf\n"
    "v_mul_f32_dpp v85, v170, v85 row_newbcast:5 row_mask:0xf bank_mask:0xf\n"
    "v_mul_f32_dpp v86, v170, v86 row_newbcast:6 row_mask:0xf bank_mask:0xf\n"
    "v_mul_f32_dpp v87, v170, v87 row_newbcast:7 row_mask:0xf bank_mask:0xf\n"
    "v_add_f32_dpp v80, v171, v80 row_newbcast:0 row_mask:0xf bank_mask:0xf\n"
    "v_add_f32_dpp v81, v171, v81 row_newbcast:1 row_mask:0xf bank_mask:0xf\n"
    "v_add_f32_dpp v82, v171, v82 row_newbcast:2 row_mask:0xf bank_mask:0xf\n"
    "v_add_f32_dpp v83, v171, v83 row_newbcast:3 row_mask:0xf bank_mask:0xf\n"
    "v_add_f32_dpp v84, v171, v84 row_newbcast:4 row_mask:0xf bank_mask:0xf\n"
    "v_add_f32_dpp v85, v171, v85 row_newbcast:5 row_mask:0xf bank_mask:0xf\n"
    "v_add_f32_dpp v86, v171, v86 row_newbcast:6 row_mask:0xf bank_mask:0xf\n"
    "v_add_f32_dpp v87, v171, v87 row_newbcast:7 row_mask:0xf bank_mask:0xf\n"
    "v_cvt_pk_bf16_f32 v80, v80, v81\n"
    "v_cvt_pk_bf16_f32 v81, v82, v83\n"
    "v_cvt_pk_bf16_f32 v82, v84, v85\n"
    "v_cvt_pk_bf16_f32 v83, v86, v87\n"
    "buffer_load_dwordx2 v[218:219], v213, s[44:47], 0 offen sc1\n"
    "s_add_u32 s78, s87, 0x6000\n"
    "buffer_store_dwordx4 v[72:75], v211, s[36:39], s78 offen sc1\n"
    "s_mov_b32 s78, 0x6000\n"
    "buffer_store_dwordx4 v[80:83], v212, s[40:43], s78 offen sc1\n"
    "s_add_u32 s78, s88, 0x1800\n"
    "s_add_u32 m0, s71, 0x6400\n"
    "s_nop 0\n"
    "buffer_load_dwordx4 v153, s[28:31], s78 offen nt lds\n"
    "s_add_u32 s78, s88, 0x1800\n"
    "s_add_u32 m0, s71, 0x6800\n"
    "s_nop 0\n"
    "buffer_load_dwordx4 v154, s[28:31], s78 offen nt lds\n"
    "s_add_u32 s78, s88, 0x1800\n"
    "s_add_u32 m0, s71, 0x6c00\n"
    "s_nop 0\n"
    "buffer_load_dwordx4 v155, s[28:31], s78 offen nt lds\n"
    "s_add_u32 s78, s88, 0x1800\n"
    "s_add_u32 m0, s71, 0x7000\n"
    "s_nop 0\n"
    "buffer_load_dwordx4 v156, s[28:31], s78 offen nt lds\n"
    "s_add_u32 s78, s88, 0x1800\n"
    "s_add_u32 m0, s71, 0x7400\n"
    "s_nop 0\n"
    "buffer_load_dwordx4 v157, s[28:31], s78 offen nt lds\n"
    "s_waitcnt vmcnt(13)\n"
    "ds_read_b128 a[192:195], v158\n"
    "ds_read_b128 a[196:199], v159\n"
    "ds_read_b128 a[200:203], v158 offset:128\n"
    "ds_read_b128 a[204:207], v159 offset:128\n"
    "ds_read_b128 a[208:211], v158 offset:256\n"
    "ds_read_b128 a[212:215], v159 offset:256\n"
    "s_waitcnt vmcnt(7)\n"
    "v_cmp_ne_u32 s[80:81], s75, v219\n"
    "v_cmp_gt_u32 vcc, s66, v218\n"
    "s_or_b64 vcc, vcc, s[80:81]\n"
    "s_cbranch_vccnz LSPRE0\n"
    "LSPB0:\n"
    "s_waitcnt vmcnt(5)\n"
    "s_waitcnt lgkmcnt(0)\n"
    "s_barrier\n"
    "v_mov_b32 v220, 3\n"
    "buffer_store_dwordx2 v[220:221], v214, s[44:47], 0 offen sc1\n"
    "s_add_u32 m0, s72, 0x1e000\n"
    "s_nop 0\n"
    "buffer_load_dwordx4 v200, s[36:39], s72 offen sc1 lds\n"
    "s_add_u32 m0, s72, 0x1e000\n"
    "s_nop 0\n"
    "buffer_load_dwordx4 v200, s[36:39], s72 offen offset:1024 sc1 lds\n"
    "s_add_u32 m0, s72, 0x1e000\n"
    "s_nop 0\n"
    "buffer_load_dwordx4 v200, s[36:39], s72 offen offset:2048 sc1 lds\n"
    "s_add_u32 s65, s71, 0x3000\n"
    "s_add_u32 m0, s71, 0\n"
    "s_nop 0\n"
    "buffer_load_dwordx4 v200, s[36:39], s65 offen sc1 lds\n"
    "s_add_u32 s65, s71, 0x3400\n"
    "s_add_u32 m0, s71, 0x400\n"
    "s_nop 0\n"
    "buffer_load_dwordx4 v200, s[36:39], s65 offen sc1 lds\n"
    "s_add_u32 s65, s71, 0x3800\n"
    "s_add_u32 m0, s71, 0x800\n"
    "s_nop 0\n"
    "buffer_load_dwordx4 v200, s[36:39], s65 offen sc1 lds\n"
    "s_add_u32 s65, s71, 0x3c00\n"
    "s_add_u32 m0, s71, 0xc00\n"
    "s_nop 0\n"
    "buffer_load_dwordx4 v200, s[36:39], s65 offen sc1 lds\n"
    "s_add_u32 s65, s71, 0x4000\n"
    "s_add_u32 m0, s71, 0x1000\n"
    "s_nop 0\n"
    "buffer_load_dwordx4 v200, s[36:39], s65 offen sc1 lds\n"
    "s_add_u32 s65, s71, 0x4400\n"
    "s_add_u32 m0, s71, 0x1400\n"
    "s_nop 0\n"
    "buffer_load_dwordx4 v200, s[36:39], s65 offen sc1 lds\n"
    "s_add_u32 s65, s71, 0\n"
    "s_add_u32 m0, s71, 0x12000\n"
    "s_nop 0\n"
    "buffer_load_dwordx4 v200, s[40:43], s65 offen sc1 lds\n"
    "s_add_u32 s65, s71, 0x400\n"
    "s_add_u32 m0, s71, 0x12400\n"
    "s_nop 0\n"
    "buffer_load_dwordx4 v200, s[40:43], s65 offen sc1 lds\n"
    "s_add_u32 s65, s71, 0x800\n"
    "s_add_u32 m0, s71, 0x12800\n"
    "s_nop 0\n"
    "buffer_load_dwordx4 v200, s[40:43], s65 offen sc1 lds\n"
    "s_add_u32 s65, s71, 0xc00\n"
    "s_add_u32 m0, s71, 0x12c00\n"
    "s_nop 0\n"
    "buffer_load_dwordx4 v200, s[40:43], s65 offen sc1 lds\n"
    "s_add_u32 s65, s71, 0x1000\n"
    "s_add_u32 m0, s71, 0x13000\n"
    "s_nop 0\n"
    "buffer_load_dwordx4 v200, s[40:43], s65 offen sc1 lds\n"
    "s_add_u32 s65, s71, 0x1400\n"
    "s_add_u32 m0, s71, 0x13400\n"
    "s_nop 0\n"
    "buffer_load_dwordx4 v200, s[40:43], s65 offen sc1 lds\n"
    "s_add_u32 s78, s64, 0x24000\n"
    "s_add_u32 m0, s72, 0x18000\n"
    "s_nop 0\n"
    "buffer_load_dwordx4 v202, s[32:35], s78 offen nt lds\n"
    "buffer_load_dwordx4 v203, s[32:35], s78 offen offset:1024 nt lds\n"
    "buffer_load_dwordx4 v204, s[32:35], s78 offen offset:2048 nt lds\n"
    "s_add_u32 s78, s88, 0x3000\n"
    "s_add_u32 m0, s71, 0xc000\n"
    "s_nop 0\n"
    "buffer_load_dwordx4 v152, s[28:31], s78 offen nt lds\n"
    "s_add_u32 s78, s88, 0x3000\n"
    "s_add_u32 m0, s71, 0xc400\n"
    "s_nop 0\n"
    "buffer_load_dwordx4 v153, s[28:31], s78 offen nt lds\n"
    "s_add_u32 s78, s88, 0x3000\n"
    "s_add_u32 m0, s71, 0xc800\n"
    "s_nop 0\n"
    "buffer_load_dwordx4 v154, s[28:31], s78 offen nt lds\n"
    "s_mul_i32 s66, s74, 3\n"
    "buffer_load_dwordx2 v[218:219], v213, s[44:47], 0 offen sc1\n"
    "s_waitcnt vmcnt(23)\n"
    "ds_read_b128 a[216:219], v158 offset:24576\n"
    "ds_read_b128 a[220:223], v159 offset:24576\n"
    "ds_read_b128 a[224:227], v158 offset:24704\n"
    "ds_read_b128 a[228:231], v159 offset:24704\n"
    "ds_read_b128 a[232:235], v158 offset:24832\n"
    "ds_read_b128 a[236:239], v159 offset:24832\n"
    "s_add_u32 m0, s72, 0x21000\n"
    "s_nop 0\n"
    "buffer_load_dwordx4 v249, s[48:51], 0 offen offset:1024 lds\n"
    "buffer_load_dwordx4 v250, s[48:51], 0 offen offset:2048 lds\n"
    "v_add_u32 v255, 0x1e000, v200\n"
    "s_waitcnt vmcnt(15) lgkmcnt(0)\n"
    "s_add_u32 s60, s71, 0x9000\n"
    "s_add_u32 s61, s71, 0x6000\n"
    "s_add_u32 s62, s87, 0x12000\n"
    "s_mov_b32 s63, 0x12000\n"
    "s_mov_b32 s59, 0\n"
    "s_mul_i32 s66, s74, 3\n"
    "s_mul_i32 s90, s58, 0x1c00\n"
    "s_sub_u32 s90, s90, 0x1000\n"
    "s_sub_u32 s92, s90, s71\n"
    "s_barrier\n"
    "ds_read_b128 v[160:163], v255\n"
    "ds_read_b128 v[164:167], v255 offset:1024\n"
    "ds_read_b128 v[168:171], v255 offset:2048\n"
    "ds_read_b128 v[172:175], v255 offset:3072\n"
    "ds_read_b128 v[176:179], v255 offset:4096\n"
    "ds_read_b128 v[180:183], v255 offset:5120\n"
    "s_add_u32 s76, s87, 0xc000\n"
    "s_mov_b32 s77, 0xc000\n"
    "s_waitcnt lgkmcnt(4)\n"
    "v_mfma_f32_16x16x32_bf16 v[40:43], v[160:163], a[192:195], 0\n"
    "ds_read_b128 v[184:187], v255 offset:6144\n"
    "ds_read_u16_d16_hi v225, v210 offset:544\n"
    "v_mfma_f32_16x16x32_bf16 v[40:43], v[164:167], a[196:199], v[40:43]\n"
    "s_add_u32 s78, s88, 0x3000\n"
    "ds_read_u16_d16_hi v224, v210 offset:32\n"
    "ds_read2st64_b32 v[240:241], v207 offset0:144 offset1:147\n"
    "v_mfma_f32_16x16x32_bf16 v[44:47], v[160:163], a[216:219], 0\n"
    "s_add_u32 m0, s71, 0xcc00\n"
    "ds_read_b128 v[188:191], v255 offset:7168\n"
    "ds_read2st64_b32 v[242:243], v207 offset0:150 offset1:153\n"
    "v_mfma_f32_16x16x32_bf16 v[44:47], v[164:167], a[220:223], v[44:47]\n"
    "s_waitcnt lgkmcnt(8)\n"
    "buffer_load_dwordx4 v155, s[28:31], s78 offen nt lds\n"
    "v_mfma_f32_16x16x32_bf16 v[40:43], v[168:171], a[200:203], v[40:43]\n"
    "ds_read_b128 v[160:163], v255 offset:8192\n"
    "ds_read2st64_b32 v[244:245], v207 offset0:156 offset1:159\n"
    "v_mfma_f32_16x16x32_bf16 v[40:43], v[172:175], a[204:207], v[40:43]\n"
    "ds_read2st64_b32 v[246:247], v207 offset0:162 offset1:165\n"
    "ds_read_u16_d16_hi v223, v209 offset:6720\n"
    "v_mfma_f32_16x16x32_bf16 v[44:47], v[168:171], a[224:227], v[44:47]\n"
    "ds_read_b128 v[164:167], v255 offset:9216\n"
    "ds_read_u16_d16_hi v222, v209 offset:576\n"
    "v_mfma_f32_16x16x32_bf16 v[44:47], v[172:175], a[228:231], v[44:47]\n"
    "s_waitcnt lgkmcnt(12)\n"
    "ds_read_b128 v[232:235], v205 offset:36864\n"
    "ds_read_b128 v[236:239], v206 offset:36864\n"
    "v_mfma_f32_16x16x32_bf16 v[40:43], v[176:179], a[208:211], v[40:43]\n"
    "ds_read_b128 v[168:171], v255 offset:10240\n"
    "v_mfma_f32_16x16x32_bf16 v[40:43], v[180:183], a[212:215], v[40:43]\n"
    "v_mfma_f32_16x16x32_bf16 v[44:47], v[176:179], a[232:235], v[44:47]\n"
    "s_waitcnt lgkmcnt(11)\n"
    "ds_read_b128 v[172:175], v255 offset:11264\n"
    "v_mfma_f32_16x16x32_bf16 v[44:47], v[180:183], a[236:239], v[44:47]\n"
    "s_waitcnt lgkmcnt(11)\n"
    "v_mfma_f32_16x16x32_bf16 v[56:59], v[184:187], a[192:195], 0\n"
    "ds_read_b128 v[176:179], v200\n"
    "v_mul_f32 v228, s68, v225\n"
    "v_mfma_f32_16x16x32_bf16 v[56:59], v[188:191], a[196:199], v[56:59]\n"
    "s_add_u32 s78, s88, 0x3000\n"
    "v_fma_f32 v229, -v224, v228, s69\n"
    "v_cvt_f32_i32 v240, v240\n"
    "v_mfma_f32_16x16x32_bf16 v[60:63], v[184:187], a[216:219], 0\n"
    "s_add_u32 m0, s71, 0xd000\n"
    "ds_read_b128 v[180:183], v200 offset:1024\n"
    "v_cvt_f32_i32 v241, v241\n"
    "v_mfma_f32_16x16x32_bf16 v[60:63], v[188:191], a[220:223], v[60:63]\n"
    "s_waitcnt lgkmcnt(7)\n"
    "buffer_load_dwordx4 v156, s[28:31], s78 offen nt lds\n"
    "v_mfma_f32_16x16x32_bf16 v[56:59], v[160:163], a[200:203], v[56:59]\n"
    "ds_read_b128 v[184:187], v200 offset:2048\n"
    "v_cvt_f32_i32 v242, v242\n"
    "v_mfma_f32_16x16x32_bf16 v[56:59], v[164:167], a[204:207], v[56:59]\n"
    "v_cvt_f32_i32 v243, v243\n"
    "v_cvt_f32_i32 v244, v244\n"
    "v_mfma_f32_16x16x32_bf16 v[60:63], v[160:163], a[224:227], v[60:63]\n"
    "ds_read_b128 v[188:191], v200 offset:3072\n"
    "v_cvt_f32_i32 v245, v245\n"
    "v_mfma_f32_16x16x32_bf16 v[60:63], v[164:167], a[228:231], v[60:63]\n"
    "s_waitcnt lgkmcnt(4)\n"
    "v_cvt_f32_i32 v246, v246\n"
    "v_mul_f32 v226, s67, v223\n"
    "v_mfma_f32_16x16x32_bf16 v[56:59], v[168:171], a[208:211], v[56:59]\n"
    "ds_read_b128 v[160:163], v200 offset:4096\n"
    "v_cvt_f32_i32 v247, v247\n"
    "v_mfma_f32_16x16x32_bf16 v[56:59], v[172:175], a[212:215], v[56:59]\n"
    "v_fma_f32 v227, -v222, v226, v217\n"
    "v_mul_f32_dpp v240, v228, v240 row_newbcast:0 row_mask:0xf bank_mask:0xf\n"
    "v_mfma_f32_16x16x32_bf16 v[60:63], v[168:171], a[232:235], v[60:63]\n"
    "ds_read_b128 v[164:167], v200 offset:5120\n"
    "v_cvt_f32_i32 v232, v232\n"
    "v_mfma_f32_16x16x32_bf16 v[60:63], v[172:175], a[236:239], v[60:63]\n"
    "s_waitcnt lgkmcnt(4)\n"
    "v_mul_f32_dpp v241, v228, v241 row_newbcast:1 row_mask:0xf bank_mask:0xf\n"
    "v_cvt_f32_i32 v233, v233\n"
    "v_mfma_f32_16x16x32_bf16 v[64:67], v[176:179], a[192:195], 0\n"
    "ds_read_b128 v[168:171], v200 offset:6144\n"
    "v_mul_f32_dpp v242, v228, v242 row_newbcast:2 row_mask:0xf bank_mask:0xf\n"
    "v_cvt_f32_i32 v234, v234\n"
    "v_mul_f32_dpp v243, v228, v243 row_newbcast:3 row_mask:0xf bank_mask:0xf\n"
    "v_mfma_f32_16x16x32_bf16 v[64:67], v[180:183], a[196:199], v[64:67]\n"
    "s_add_u32 s78, s88, 0x3000\n"
    "v_cvt_f32_i32 v235, v235\n"
    "v_mul_f32_dpp v244, v228, v244 row_newbcast:4 row_mask:0xf bank_mask:0xf\n"
    "v_cvt_f32_i32 v236, v236\n"
    "v_mul_f32_dpp v245, v228, v245 row_newbcast:5 row_mask:0xf bank_mask:0xf\n"
    "v_mfma_f32_16x16x32_bf16 v[68:71], v[176:179], a[216:219], 0\n"
    "s_add_u32 m0, s71, 0xd400\n"
    "ds_read_b128 v[172:175], v200 offset:7168\n"
    "v_cvt_f32_i32 v237, v237\n"
    "v_mul_f32_dpp v246, v228, v246 row_newbcast:6 row_mask:0xf bank_mask:0xf\n"
    "v_cvt_f32_i32 v238, v238\n"
    "v_mfma_f32_16x16x32_bf16 v[68:71], v[180:183], a[220:223], v[68:71]\n"
    "s_waitcnt lgkmcnt(4)\n"
    "buffer_load_dwordx4 v157, s[28:31], s78 offen nt lds\n"
    "v_mul_f32_dpp v247, v228, v247 row_newbcast:7 row_mask:0xf bank_mask:0xf\n"
    "v_cvt_f32_i32 v239, v239\n"
    "v_mfma_f32_16x16x32_bf16 v[64:67], v[184:187], a[200:203], v[64:67]\n"
    "ds_read_b128 v[176:179], v200 offset:8192\n"
    "v_add_f32_dpp v240, v229, v240 row_newbcast:0 row_mask:0xf bank_mask:0xf\n"
    "v_mul_f32_dpp v232, v226, v232 row_newbcast:0 row_mask:0xf bank_mask:0xf\n"
    "v_add_f32_dpp v241, v229, v241 row_newbcast:1 row_mask:0xf bank_mask:0xf\n"
    "v_mfma_f32_16x16x32_bf16 v[64:67], v[188:191], a[204:207], v[64:67]\n"
    "v_mul_f32_dpp v233, v226, v233 row_newbcast:1 row_mask:0xf bank_mask:0xf\n"
    "v_add_f32_dpp v242, v229, v242 row_newbcast:2 row_mask:0xf bank_mask:0xf\n"
    "v_mul_f32_dpp v234, v226, v234 row_newbcast:2 row_mask:0xf bank_mask:0xf\n"
    "v_add_f32_dpp v243, v229, v243 row_newbcast:3 row_mask:0xf bank_mask:0xf\n"
    "v_mfma_f32_16x16x32_bf16 v[68:71], v[184:187], a[224:227], v[68:71]\n"
    "ds_read_b128 v[180:183], v200 offset:9216\n"
    "v_mul_f32_dpp v235, v226, v235 row_newbcast:3 row_mask:0xf bank_mask:0xf\n"
    "v_add_f32_dpp v244, v229, v244 row_newbcast:4 row_mask:0xf bank_mask:0xf\n"
    "v_mul_f32_dpp v236, v226, v236 row_newbcast:4 row_mask:0xf bank_mask:0xf\n"
    "v_add_f32_dpp v245, v229, v245 row_newbcast:5 row_mask:0xf bank_mask:0xf\n"
    "v_mul_f32_dpp v237, v226, v237 row_newbcast:5 row_mask:0xf bank_mask:0xf\n"
    "v_add_f32_dpp v246, v229, v246 row_newbcast:6 row_mask:0xf bank_mask:0xf\n"
    "v_mul_f32_dpp v238, v226, v238 row_newbcast:6 row_mask:0xf bank_mask:0xf\n"
    "v_add_f32_dpp v247, v229, v247 row_newbcast:7 row_mask:0xf bank_mask:0xf\n"
    "v_mul_f32_dpp v239, v226, v239 row_newbcast:7 row_mask:0xf bank_mask:0xf\n"
    "v_cvt_pk_bf16_f32 v240, v240, v241\n"
    "v_add_f32_dpp v232, v227, v232 row_newbcast:0 row_mask:0xf bank_mask:0xf\n"
    "v_cvt_pk_bf16_f32 v241, v242, v243\n"
    "v_add_f32_dpp v233, v227, v233 row_newbcast:1 row_mask:0xf bank_mask:0xf\n"
    "v_cvt_pk_bf16_f32 v242, v244, v245\n"
    "v_add_f32_dpp v234, v227, v234 row_newbcast:2 row_mask:0xf bank_mask:0xf\n"
    "v_cvt_pk_bf16_f32 v243, v246, v247\n"
    "v_add_f32_dpp v235, v227, v235 row_newbcast:3 row_mask:0xf bank_mask:0xf\n"
    "v_add_f32_dpp v236, v227, v236 row_newbcast:4 row_mask:0xf bank_mask:0xf\n"
    "v_add_f32_dpp v237, v227, v237 row_newbcast:5 row_mask:0xf bank_mask:0xf\n"
    "v_add_f32_dpp v238, v227, v238 row_newbcast:6 row_mask:0xf bank_mask:0xf\n"
    "v_add_f32_dpp v239, v227, v239 row_newbcast:7 row_mask:0xf bank_mask:0xf\n"
    "v_cvt_pk_bf16_f32 v232, v232, v233\n"
    "v_cvt_pk_bf16_f32 v233, v234, v235\n"
    "v_cvt_pk_bf16_f32 v234, v236, v237\n"
    "v_cvt_pk_bf16_f32 v235, v238, v239\n"
    "v_mfma_f32_16x16x32_bf16 v[68:71], v[188:191], a[228:231], v[68:71]\n"
    "s_waitcnt lgkmcnt(4)\n"
    "v_max3_f32 v196, v40, v41, v42\n"
    "v_max3_f32 v196, v196, v43, v56\n"
    "v_max3_f32 v196, v196, v57, v58\n"
    "v_max_f32 v196, v196, v59\n"
    "buffer_store_dwordx4 v[232:235], v211, s[36:39], s76 offen sc1\n"
    "v_mfma_f32_16x16x32_bf16 v[64:67], v[160:163], a[208:211], v[64:67]\n"
    "ds_read_b128 v[184:187], v200 offset:10240\n"
    "v_max3_f32 v198, v44, v45, v46\n"
    "v_mov_b32 v197, v196\n"
    "v_max3_f32 v198, v198, v47, v60\n"
    "v_mfma_f32_16x16x32_bf16 v[64:67], v[164:167], a[212:215], v[64:67]\n"
    "v_permlane32_swap_b32 v196, v197\n"
    "v_max3_f32 v198, v198, v61, v62\n"
    "v_max_f32 v196, v196, v197\n"
    "v_max_f32 v198, v198, v63\n"
    "v_mfma_f32_16x16x32_bf16 v[68:71], v[160:163], a[232:235], v[68:71]\n"
    "ds_read_b128 v[188:191], v200 offset:11264\n"
    "v_mov_b32 v197, v196\n"
    "v_mov_b32 v199, v198\n"
    "s_nop 0\n"
    "v_permlane16_swap_b32 v196, v197\n"
    "v_mfma_f32_16x16x32_bf16 v[68:71], v[164:167], a[236:239], v[68:71]\n"
    "s_waitcnt lgkmcnt(4)\n"
    "v_permlane32_swap_b32 v198, v199\n"
    "v_max_f32 v196, v196, v197\n"
    "v_max_f32 v198, v198, v199\n"
    "v_add_f32 v196, 0x426c0000, v196\n"
    "v_mfma_f32_16x16x32_bf16 v[80:83], v[168:171], a[192:195], 0\n"
    "ds_read_b128 v[160:163], v200 offset:12288\n"
    "v_mov_b32 v199, v198\n"
    "v_sub_f32 v96, 0, v196\n"
    "s_nop 0\n"
    "v_permlane16_swap_b32 v198, v199\n"
    "v_mfma_f32_16x16x32_bf16 v[80:83], v[172:175], a[196:199], v[80:83]\n"
    "s_add_u32 s78, s88, 0x4800\n"
    "buffer_store_dwordx4 v[240:243], v212, s[40:43], s77 offen sc1\n"
    "v_sub_f32 v97, 0, v196\n"
    "v_max_f32 v198, v198, v199\n"
    "v_sub_f32 v98, 0, v196\n"
    "v_add_f32 v198, 0x426c0000, v198\n"
    "v_sub_f32 v99, 0, v196\n"
    "v_mfma_f32_16x16x32_bf16 v[84:87], v[168:171], a[216:219], 0\n"
    "s_add_u32 m0, s71, 0x6000\n"
    "ds_read_b128 v[164:167], v200 offset:13312\n"
    "v_sub_f32 v100, 0, v198\n"
    "v_add_f32 v40, v40, v96\n"
    "v_sub_f32 v101, 0, v198\n"
    "v_mfma_f32_16x16x32_bf16 v[84:87], v[172:175], a[220:223], v[84:87]\n"
    "s_waitcnt lgkmcnt(4)\n"
    "buffer_load_dwordx4 v152, s[28:31], s78 offen nt lds\n"
    "v_add_f32 v41, v41, v96\n"
    "v_sub_f32 v102, 0, v198\n"
    "v_mfma_f32_16x16x32_bf16 v[80:83], v[176:179], a[200:203], v[80:83]\n"
    "ds_read_b128 v[168:171], v200 offset:14336\n"
    "v_add_f32 v42, v42, v96\n"
    "v_sub_f32 v103, 0, v198\n"
    "v_add_f32 v43, v43, v96\n"
    "v_mfma_f32_16x16x32_bf16 v[80:83], v[180:183], a[204:207], v[80:83]\n"
    "v_add_f32 v44, v44, v100\n"
    "v_add_f32 v56, v56, v96\n"
    "v_add_f32 v45, v45, v100\n"
    "v_add_f32 v57, v57, v96\n"
    "v_mfma_f32_16x16x32_bf16 v[84:87], v[176:179], a[224:227], v[84:87]\n"
    "ds_read_b128 v[172:175], v200 offset:15360\n"
    "v_add_f32 v46, v46, v100\n"
    "v_add_f32 v58, v58, v96\n"
    "v_add_f32 v47, v47, v100\n"
    "v_mfma_f32_16x16x32_bf16 v[84:87], v[180:183], a[228:231], v[84:87]\n"
    "s_waitcnt lgkmcnt(4)\n"
    "v_add_f32 v59, v59, v96\n"
    "v_add_f32 v60, v60, v100\n"
    "v_exp_f32 v40, v40\n"
    "v_mfma_f32_16x16x32_bf16 v[80:83], v[184:187], a[208:211], v[80:83]\n"
    "ds_read_b128 v[176:179], v200 offset:16384\n"
    "v_add_f32 v61, v61, v100\n"
    "v_add_f32 v62, v62, v100\n"
    "v_add_f32 v63, v63, v100\n"
    "v_mfma_f32_16x16x32_bf16 v[80:83], v[188:191], a[212:215], v[80:83]\n"
    "v_exp_f32 v41, v41\n"
    "v_exp_f32 v44, v44\n"
    "v_mfma_f32_16x16x32_bf16 v[84:87], v[184:187], a[232:235], v[84:87]\n"
    "ds_read_b128 v[180:183], v200 offset:17408\n"
    "v_exp_f32 v42, v42\n"
    "v_mfma_f32_16x16x32_bf16 v[84:87], v[188:191], a[236:239], v[84:87]\n"
    "s_waitcnt lgkmcnt(4)\n"
    "v_exp_f32 v45, v45\n"
    "v_exp_f32 v43, v43\n"
    "v_mfma_f32_16x16x32_bf16 v[32:35], v[160:163], a[192:195], v[96:99]\n"
    "ds_read_b128 v[184:187], v200 offset:18432\n"
    "v_exp_f32 v46, v46\n"
    "v_mfma_f32_16x16x32_bf16 v[32:35], v[164:167], a[196:199], v[32:35]\n"
    "s_add_u32 s78, s88, 0x4800\n"
    "v_exp_f32 v56, v56\n"
    "v_exp_f32 v47, v47\n"
    "v_mfma_f32_16x16x32_bf16 v[36:39], v[160:163], a[216:219], v[100:103]\n"
    "s_add_u32 m0, s71, 0x6400\n"
    "ds_read_b128 v[188:191], v200 offset:19456\n"
    "v_exp_f32 v57, v57\n"
    "v_mfma_f32_16x16x32_bf16 v[36:39], v[164:167], a[220:223], v[36:39]\n"
    "s_waitcnt lgkmcnt(4)\n"
    "buffer_load_dwordx4 v153, s[28:31], s78 offen nt lds\n"
    "v_exp_f32 v60, v60\n"
    "v_mfma_f32_16x16x32_bf16 v[32:35], v[168:171], a[200:203], v[32:35]\n"
    "ds_read_b128 v[160:163], v200 offset:20480\n"
    "v_exp_f32 v58, v58\n"
    "v_mfma_f32_16x16x32_bf16 v[32:35], v[172:175], a[204:207], v[32:35]\n"
    "v_exp_f32 v61, v61\n"
    "v_exp_f32 v59, v59\n"
    "v_mfma_f32_16x16x32_bf16 v[36:39], v[168:171], a[224:227], v[36:39]\n"
    "ds_read_b128 v[164:167], v200 offset:21504\n"
    "v_exp_f32 v62, v62\n"
    "v_cvt_pk_bf16_f32 v128, v40, v41\n"
    "v_mfma_f32_16x16x32_bf16 v[36:39], v[172:175], a[228:231], v[36:39]\n"
    "s_waitcnt lgkmcnt(4)\n"
    "v_cvt_pk_bf16_f32 v129, v42, v43\n"
    "v_exp_f32 v63, v63\n"
    "v_cvt_pk_bf16_f32 v130, v56, v57\n"
    "v_mfma_f32_16x16x32_bf16 v[32:35], v[176:179], a[208:211], v[32:35]\n"
    "ds_read_b128 v[168:171], v200 offset:22528\n"
    "v_cvt_pk_bf16_f32 v131, v58, v59\n"
    "v_cvt_pk_bf16_f32 v132, v44, v45\n"
    "v_add_f32 v64, v64, v96\n"
    "v_mfma_f32_16x16x32_bf16 v[32:35], v[180:183], a[212:215], v[32:35]\n"
    "v_cvt_pk_bf16_f32 v133, v46, v47\n"
    "v_add_f32 v65, v65, v96\n"
    "v_cvt_pk_bf16_f32 v134, v60, v61\n"
    "v_add_f32 v66, v66, v96\n"
    "v_mfma_f32_16x16x32_bf16 v[36:39], v[176:179], a[232:235], v[36:39]\n"
    "ds_read_b128 v[172:175], v200 offset:23552\n"
    "v_cvt_pk_bf16_f32 v135, v62, v63\n"
    "v_add_f32 v67, v67, v96\n"
    "v_add_f32 v68, v68, v100\n"
    "v_mfma_f32_16x16x32_bf16 v[36:39], v[180:183], a[236:239], v[36:39]\n"
    "s_waitcnt lgkmcnt(4)\n"
    "v_add_f32 v80, v80, v96\n"
    "v_add_f32 v69, v69, v100\n"
    "s_waitcnt vmcnt(13)\n"
    "s_barrier\n"
    "v_mfma_f32_16x16x32_bf16 v[48:51], v[184:187], a[192:195], v[96:99]\n"
    "ds_read_b128 v[176:179], v201 offset:24576\n"
    "v_add_f32 v81, v81, v96\n"
    "v_add_f32 v70, v70, v100\n"
    "v_add_f32 v82, v82, v96\n"
    "v_mfma_f32_16x16x32_bf16 v[48:51], v[188:191], a[196:199], v[48:51]\n"
    "s_add_u32 s78, s88, 0x4800\n"
    "v_add_f32 v71, v71, v100\n"
    "v_add_f32 v83, v83, v96\n"
    "v_add_f32 v84, v84, v100\n"
    "v_add_f32 v85, v85, v100\n"
    "v_mfma_f32_16x16x32_bf16 v[52:55], v[184:187], a[216:219], v[100:103]\n"
    "s_add_u32 m0, s71, 0x6800\n"
    "ds_read_b128 v[180:183], v201 offset:25600\n"
    "v_exp_f32 v64, v64\n"
    "v_add_f32 v86, v86, v100\n"
    "v_mfma_f32_16x16x32_bf16 v[52:55], v[188:191], a[220:223], v[52:55]\n"
    "s_waitcnt lgkmcnt(4)\n"
    "buffer_load_dwordx4 v154, s[28:31], s78 offen nt lds\n"
    "v_exp_f32 v65, v65\n"
    "v_mfma_f32_16x16x32_bf16 v[48:51], v[160:163], a[200:203], v[48:51]\n"
    "ds_read_b128 v[184:187], v201 offset:26624\n"
    "v_add_f32 v87, v87, v100\n"
    "v_exp_f32 v68, v68\n"
    "v_mfma_f32_16x16x32_bf16 v[48:51], v[164:167], a[204:207], v[48:51]\n"
    "v_exp_f32 v66, v66\n"
    "v_exp_f32 v69, v69\n"
    "v_mfma_f32_16x16x32_bf16 v[52:55], v[160:163], a[224:227], v[52:55]\n"
    "ds_read_b128 v[188:191], v201 offset:27648\n"
    "v_exp_f32 v67, v67\n"
    "ds_read_u16_d16_hi v225, v210 offset:560\n"
    "v_mfma_f32_16x16x32_bf16 v[52:55], v[164:167], a[228:231], v[52:55]\n"
    "s_waitcnt lgkmcnt(5)\n"
    "v_exp_f32 v70, v70\n"
    "v_exp_f32 v80, v80\n"
    "v_mfma_f32_16x16x32_bf16 v[48:51], v[168:171], a[208:211], v[48:51]\n"
    "ds_read_b128 v[160:163], v201 offset:28672\n"
    "v_exp_f32 v71, v71\n"
    "ds_read_u16_d16_hi v224, v210 offset:48\n"
    "v_mfma_f32_16x16x32_bf16 v[48:51], v[172:175], a[212:215], v[48:51]\n"
    "v_exp_f32 v81, v81\n"
    "v_exp_f32 v84, v84\n"
    "v_mfma_f32_16x16x32_bf16 v[52:55], v[168:171], a[232:235], v[52:55]\n"
    "ds_read_b128 v[164:167], v201 offset:29696\n"
    "v_exp_f32 v82, v82\n"
    "ds_read2st64_b32 v[240:241], v207 offset0:96 offset1:99\n"
    "v_exp_f32 v83, v83\n"
    "v_cvt_pk_bf16_f32 v144, v64, v65\n"
    "v_cvt_pk_bf16_f32 v145, v66, v67\n"
    "v_cvt_pk_bf16_f32 v146, v80, v81\n"
    "v_cvt_pk_bf16_f32 v147, v82, v83\n"
    "v_mfma_f32_16x16x32_bf16 v[52:55], v[172:175], a[236:239], v[52:55]\n"
    "s_waitcnt lgkmcnt(7)\n"
    "v_exp_f32 v85, v85\n"
    "v_exp_f32 v86, v86\n"
    "v_mfma_f32_16x16x32_bf16 a[0:3], v[176:179], v[128:131], 0\n"
    "ds_read_b128 v[168:171], v201 offset:30720\n"
    "ds_read2st64_b32 v[242:243], v207 offset0:102 offset1:105\n"
    "v_exp_f32 v87, v87\n"
    "v_cvt_pk_bf16_f32 v148, v68, v69\n"
    "v_cvt_pk_bf16_f32 v149, v70, v71\n"
    "v_cvt_pk_bf16_f32 v150, v84, v85\n"
    "v_cvt_pk_bf16_f32 v151, v86, v87\n"
    "v_mfma_f32_16x16x32_bf16 a[0:3], v[180:183], v[144:147], a[0:3]\n"
    "s_add_u32 s78, s88, 0x4800\n"
    "ds_read2st64_b32 v[244:245], v207 offset0:108 offset1:111\n"
    "ds_read2st64_b32 v[246:247], v207 offset0:114 offset1:117\n"
    "v_mfma_f32_16x16x32_bf16 a[4:7], v[176:179], v[132:135], 0\n"
    "s_add_u32 m0, s71, 0x6c00\n"
    "ds_read_b128 v[172:175], v201 offset:31744\n"
    "ds_read_u16_d16_hi v223, v209 offset:6912\n"
    "v_mfma_f32_16x16x32_bf16 a[4:7], v[180:183], v[148:151], a[4:7]\n"
    "s_waitcnt lgkmcnt(11)\n"
    "buffer_load_dwordx4 v155, s[28:31], s78 offen nt lds\n"
    "v_mfma_f32_16x16x32_bf16 a[16:19], v[184:187], v[128:131], 0\n"
    "ds_read_b128 v[176:179], v201 offset:32768\n"
    "ds_read_u16_d16_hi v222, v209 offset:768\n"
    "v_mfma_f32_16x16x32_bf16 a[16:19], v[188:191], v[144:147], a[16:19]\n"
    "v_exp_f32 v32, v32\n"
    "v_mfma_f32_16x16x32_bf16 a[20:23], v[184:187], v[132:135], 0\n"
    "ds_read_b128 v[180:183], v201 offset:33792\n"
    "ds_read_b128 v[232:235], v205 offset:24576\n"
    "v_mfma_f32_16x16x32_bf16 a[20:23], v[188:191], v[148:151], a[20:23]\n"
    "s_waitcnt lgkmcnt(11)\n"
    "ds_read_b128 v[236:239], v206 offset:24576\n"
    "v_mfma_f32_16x16x32_bf16 a[32:35], v[160:163], v[128:131], 0\n"
    "ds_read_b128 v[184:187], v201 offset:34816\n"
    "v_mul_f32 v228, s68, v225\n"
    "v_mfma_f32_16x16x32_bf16 a[32:35], v[164:167], v[144:147], a[32:35]\n"
    "v_exp_f32 v33, v33\n"
    "v_mfma_f32_16x16x32_bf16 a[36:39], v[160:163], v[132:135], 0\n"
    "ds_read_b128 v[188:191], v201 offset:35840\n"
    "v_fma_f32 v229, -v224, v228, s69\n"
    "v_mfma_f32_16x16x32_bf16 a[36:39], v[164:167], v[148:151], a[36:39]\n"
    "v_exp_f32 v34, v34\n"
    "v_mfma_f32_16x16x32_bf16 v[112:115], v[192:195], v[128:131], 0\n"
    "s_waitcnt lgkmcnt(13)\n"
    "v_cvt_f32_i32 v240, v240\n"
    "v_cvt_f32_i32 v241, v241\n"
    "v_mfma_f32_16x16x32_bf16 v[112:115], v[192:195], v[144:147], v[112:115]\n"
    "s_waitcnt lgkmcnt(8)\n"
    "s_add_u32 s78, s88, 0x4800\n"
    "v_cvt_f32_i32 v242, v242\n"
    "v_mfma_f32_16x16x32_bf16 a[48:51], v[168:171], v[128:131], 0\n"
    "s_add_u32 m0, s71, 0x7000\n"
    "ds_read_b128 v[160:163], v201 offset:36864\n"
    "v_cvt_f32_i32 v243, v243\n"
    "v_mfma_f32_16x16x32_bf16 a[48:51], v[172:175], v[144:147], a[48:51]\n"
    "buffer_load_dwordx4 v156, s[28:31], s78 offen nt lds\n"
    "v_mfma_f32_16x16x32_bf16 a[52:55], v[168:171], v[132:135], 0\n"
    "ds_read_b128 v[164:167], v201 offset:37888\n"
    "v_cvt_f32_i32 v244, v244\n"
    "v_mfma_f32_16x16x32_bf16 a[52:55], v[172:175], v[148:151], a[52:55]\n"
    "v_exp_f32 v35, v35\n"
    "v_mfma_f32_16x16x32_bf16 v[116:119], v[192:195], v[132:135], 0\n"
    "v_exp_f32 v36, v36\n"
    "v_mfma_f32_16x16x32_bf16 v[116:119], v[192:195], v[148:151], v[116:119]\n"
    "s_waitcnt lgkmcnt(6)\n"
    "v_exp_f32 v37, v37\n"
    "v_mfma_f32_16x16x32_bf16 a[64:67], v[176:179], v[128:131], 0\n"
    "ds_read_b128 v[168:171], v201 offset:38912\n"
    "v_cvt_f32_i32 v245, v245\n"
    "v_mfma_f32_16x16x32_bf16 a[64:67], v[180:183], v[144:147], a[64:67]\n"
    "v_cvt_f32_i32 v246, v246\n"
    "v_mul_f32 v226, s67, v223\n"
    "v_mfma_f32_16x16x32_bf16 a[68:71], v[176:179], v[132:135], 0\n"
    "ds_read_b128 v[172:175], v201 offset:39936\n"
    "v_cvt_f32_i32 v247, v247\n"
    "v_mfma_f32_16x16x32_bf16 a[68:71], v[180:183], v[148:151], a[68:71]\n"
    "s_waitcnt lgkmcnt(4)\n"
    "v_exp_f32 v38, v38\n"
    "v_mfma_f32_16x16x32_bf16 a[80:83], v[184:187], v[128:131], 0\n"
    "ds_read_b128 v[176:179], v201 offset:40960\n"
    "v_fma_f32 v227, -v222, v226, v217\n"
    "v_mfma_f32_16x16x32_bf16 a[80:83], v[188:191], v[144:147], a[80:83]\n"
    "s_add_u32 s78, s88, 0x4800\n"
    "v_mul_f32_dpp v240, v228, v240 row_newbcast:0 row_mask:0xf bank_mask:0xf\n"
    "v_cvt_f32_i32 v232, v232\n"
    "v_mfma_f32_16x16x32_bf16 a[84:87], v[184:187], v[132:135], 0\n"
    "s_add_u32 m0, s71, 0x7400\n"
    "ds_read_b128 v[180:183], v201 offset:41984\n"
    "v_mul_f32_dpp v241, v228, v241 row_newbcast:1 row_mask:0xf bank_mask:0xf\n"
    "v_mfma_f32_16x16x32_bf16 a[84:87], v[188:191], v[148:151], a[84:87]\n"
    "s_waitcnt lgkmcnt(4)\n"
    "buffer_load_dwordx4 v157, s[28:31], s78 offen nt lds\n"
    "v_mfma_f32_16x16x32_bf16 a[96:99], v[160:163], v[128:131], 0\n"
    "ds_read_b128 v[184:187], v201 offset:43008\n"
    "v_cvt_f32_i32 v233, v233\n"
    "v_mfma_f32_16x16x32_bf16 a[96:99], v[164:167], v[144:147], a[96:99]\n"
    "v_exp_f32 v39, v39\n"
    "v_mfma_f32_16x16x32_bf16 a[100:103], v[160:163], v[132:135], 0\n"
    "ds_read_b128 v[188:191], v201 offset:44032\n"
    "v_mul_f32_dpp v242, v228, v242 row_newbcast:2 row_mask:0xf bank_mask:0xf\n"
    "v_mfma_f32_16x16x32_bf16 a[100:103], v[164:167], v[148:151], a[100:103]\n"
    "s_waitcnt lgkmcnt(4)\n"
    "v_exp_f32 v48, v48\n"
    "v_mfma_f32_16x16x32_bf16 a[112:115], v[168:171], v[128:131], 0\n"
    "ds_read_b128 v[160:163], v201 offset:45056\n"
    "v_cvt_f32_i32 v234, v234\n"
    "v_mfma_f32_16x16x32_bf16 a[112:115], v[172:175], v[144:147], a[112:115]\n"
    "v_mul_f32_dpp v243, v228, v243 row_newbcast:3 row_mask:0xf bank_mask:0xf\n"
    "v_cvt_f32_i32 v235, v235\n"
    "v_mfma_f32_16x16x32_bf16 a[116:119], v[168:171], v[132:135], 0\n"
    "ds_read_b128 v[164:167], v201 offset:46080\n"
    "v_mul_f32_dpp v244, v228, v244 row_newbcast:4 row_mask:0xf bank_mask:0xf\n"
    "v_mfma_f32_16x16x32_bf16 a[116:119], v[172:175], v[148:151], a[116:119]\n"
    "s_waitcnt lgkmcnt(4)\n"
    "v_exp_f32 v49, v49\n"
    "v_mfma_f32_16x16x32_bf16 a[128:131], v[176:179], v[128:131], 0\n"
    "ds_read_b128 v[168:171], v201 offset:47104\n"
    "v_cvt_f32_i32 v236, v236\n"
    "v_mfma_f32_16x16x32_bf16 a[128:131], v[180:183], v[144:147], a[128:131]\n"
    "v_mul_f32_dpp v245, v228, v245 row_newbcast:5 row_mask:0xf bank_mask:0xf\n"
    "v_cvt_f32_i32 v237, v237\n"
    "v_mfma_f32_16x16x32_bf16 a[132:135], v[176:179], v[132:135], 0\n"
    "ds_read_b128 v[172:175], v201 offset:48128\n"
    "v_mul_f32_dpp v246, v228, v246 row_newbcast:6 row_mask:0xf bank_mask:0xf\n"
    "v_mfma_f32_16x16x32_bf16 a[132:135], v[180:183], v[148:151], a[132:135]\n"
    "s_waitcnt lgkmcnt(4)\n"
    "v_exp_f32 v50, v50\n"
    "v_mfma_f32_16x16x32_bf16 a[144:147], v[184:187], v[128:131], 0\n"
    "v_cvt_f32_i32 v238, v238\n"
    "v_mul_f32_dpp v247, v228, v247 row_newbcast:7 row_mask:0xf bank_mask:0xf\n"
    "v_mfma_f32_16x16x32_bf16 a[144:147], v[188:191], v[144:147], a[144:147]\n"
    "v_cvt_f32_i32 v239, v239\n"
    "v_add_f32_dpp v240, v229, v240 row_newbcast:0 row_mask:0xf bank_mask:0xf\n"
    "v_mfma_f32_16x16x32_bf16 a[148:151], v[184:187], v[132:135], 0\n"
    "v_exp_f32 v51, v51\n"
    "v_mfma_f32_16x16x32_bf16 a[148:151], v[188:191], v[148:151], a[148:151]\n"
    "s_waitcnt lgkmcnt(2)\n"
    "v_mul_f32_dpp v232, v226, v232 row_newbcast:0 row_mask:0xf bank_mask:0xf\n"
    "v_add_f32_dpp v241, v229, v241 row_newbcast:1 row_mask:0xf bank_mask:0xf\n"
    "v_mfma_f32_16x16x32_bf16 a[160:163], v[160:163], v[128:131], 0\n"
    "v_mul_f32_dpp v233, v226, v233 row_newbcast:1 row_mask:0xf bank_mask:0xf\n"
    "v_add_f32_dpp v242, v229, v242 row_newbcast:2 row_mask:0xf bank_mask:0xf\n"
    "v_mfma_f32_16x16x32_bf16 a[160:163], v[164:167], v[144:147], a[160:163]\n"
    "v_exp_f32 v52, v52\n"
    "v_mfma_f32_16x16x32_bf16 a[164:167], v[160:163], v[132:135], 0\n"
    "v_mul_f32_dpp v234, v226, v234 row_newbcast:2 row_mask:0xf bank_mask:0xf\n"
    "v_add_f32_dpp v243, v229, v243 row_newbcast:3 row_mask:0xf bank_mask:0xf\n"
    "v_mfma_f32_16x16x32_bf16 a[164:167], v[164:167], v[148:151], a[164:167]\n"
    "s_waitcnt lgkmcnt(0)\n"
    "v_mul_f32_dpp v235, v226, v235 row_newbcast:3 row_mask:0xf bank_mask:0xf\n"
    "v_add_f32_dpp v244, v229, v244 row_newbcast:4 row_mask:0xf bank_mask:0xf\n"
    "v_mfma_f32_16x16x32_bf16 a[176:179], v[168:171], v[128:131], 0\n"
    "v_exp_f32 v53, v53\n"
    "v_mfma_f32_16x16x32_bf16 a[176:179], v[172:175], v[144:147], a[176:179]\n"
    "v_mul_f32_dpp v236, v226, v236 row_newbcast:4 row_mask:0xf bank_mask:0xf\n"
    "v_add_f32_dpp v245, v229, v245 row_newbcast:5 row_mask:0xf bank_mask:0xf\n"
    "v_mfma_f32_16x16x32_bf16 a[180:183], v[168:171], v[132:135], 0\n"
    "v_exp_f32 v54, v54\n"
    "v_mul_f32_dpp v237, v226, v237 row_newbcast:5 row_mask:0xf bank_mask:0xf\n"
    "v_add_f32_dpp v246, v229, v246 row_newbcast:6 row_mask:0xf bank_mask:0xf\n"
    "v_mul_f32_dpp v238, v226, v238 row_newbcast:6 row_mask:0xf bank_mask:0xf\n"
    "v_add_f32_dpp v247, v229, v247 row_newbcast:7 row_mask:0xf bank_mask:0xf\n"
    "v_exp_f32 v55, v55\n"
    "v_mul_f32_dpp v239, v226, v239 row_newbcast:7 row_mask:0xf bank_mask:0xf\n"
    "v_cvt_pk_bf16_f32 v240, v240, v241\n"
    "v_add_f32_dpp v232, v227, v232 row_newbcast:0 row_mask:0xf bank_mask:0xf\n"
    "v_cvt_pk_bf16_f32 v241, v242, v243\n"
    "v_add_f32_dpp v233, v227, v233 row_newbcast:1 row_mask:0xf bank_mask:0xf\n"
    "v_cvt_pk_bf16_f32 v242, v244, v245\n"
    "v_add_f32_dpp v234, v227, v234 row_newbcast:2 row_mask:0xf bank_mask:0xf\n"
    "v_cvt_pk_bf16_f32 v243, v246, v247\n"
    "v_add_f32_dpp v235, v227, v235 row_newbcast:3 row_mask:0xf bank_mask:0xf\n"
    "v_add_f32_dpp v236, v227, v236 row_newbcast:4 row_mask:0xf bank_mask:0xf\n"
    "v_add_f32_dpp v237, v227, v237 row_newbcast:5 row_mask:0xf bank_mask:0xf\n"
    "v_add_f32_dpp v238, v227, v238 row_newbcast:6 row_mask:0xf bank_mask:0xf\n"
    "v_add_f32_dpp v239, v227, v239 row_newbcast:7 row_mask:0xf bank_mask:0xf\n"
    "v_cvt_pk_bf16_f32 v232, v232, v233\n"
    "v_cvt_pk_bf16_f32 v233, v234, v235\n"
    "v_cvt_pk_bf16_f32 v234, v236, v237\n"
    "v_cvt_pk_bf16_f32 v235, v238, v239\n"
    "v_mfma_f32_16x16x32_bf16 a[180:183], v[172:175], v[148:151], a[180:183]\n"
    "v_add_f32 v113, v113, v32\n"
    "v_add_f32 v114, v114, v33\n"
    "v_add_f32 v113, v113, v34\n"
    "v_add_f32 v114, v114, v35\n"
    "v_add_f32 v117, v117, v36\n"
    "v_add_f32 v118, v118, v37\n"
    "v_add_f32 v117, v117, v38\n"
    "v_add_f32 v118, v118, v39\n"
    "v_add_f32 v113, v113, v48\n"
    "v_add_f32 v114, v114, v49\n"
    "v_add_f32 v113, v113, v50\n"
    "v_add_f32 v114, v114, v51\n"
    "v_add_f32 v117, v117, v52\n"
    "buffer_store_dwordx4 v[232:235], v211, s[36:39], s62 offen sc1\n"
    "buffer_store_dwordx4 v[240:243], v212, s[40:43], s63 offen sc1\n"
    "v_add_f32 v118, v118, v53\n"
    "v_add_f32 v117, v117, v54\n"
    "v_add_f32 v118, v118, v55\n"
    "s_waitcnt vmcnt(10)\n"
    "ds_read_b128 a[240:243], v158 offset:49152\n"
    "ds_read_b128 a[244:247], v159 offset:49152\n"
    "ds_read_b128 a[248:251], v158 offset:49280\n"
    "ds_read_b128 a[252:255], v159 offset:49280\n"
    "ds_read_b128 v[0:3], v158 offset:49408\n"
    "ds_read_b128 v[4:7], v159 offset:49408\n"
    "s_waitcnt vmcnt(8)\n"
    "v_cmp_ne_u32 s[80:81], s75, v219\n"
    "v_cmp_gt_u32 vcc, s66, v218\n"
    "s_or_b64 vcc, vcc, s[80:81]\n"
    "s_cbranch_vccnz LSPRE1\n"
    "LSPB1:\n"
    "v_mov_b32 v220, 4\n"
    "s_add_u32 s66, s66, s74\n"
    "s_waitcnt lgkmcnt(0)\n"
    "s_barrier\n"
    "buffer_store_dwordx2 v[220:221], v214, s[44:47], 0 offen sc1\n"
    "ds_read_b128 v[160:163], v255\n"
    "ds_read_b128 v[164:167], v255 offset:1024\n"
    "ds_read_b128 v[168:171], v255 offset:2048\n"
    "ds_read_b128 v[172:175], v255 offset:3072\n"
    "ds_read_b128 v[176:179], v255 offset:4096\n"
    "ds_read_b128 v[180:183], v255 offset:5120\n"
    "s_waitcnt lgkmcnt(4)\n"
    "v_mfma_f32_16x16x32_bf16 v[44:47], v[160:163], a[240:243], 0\n"
    "ds_read_b128 v[184:187], v255 offset:6144\n"
    "v_mfma_f32_16x16x32_bf16 v[44:47], v[164:167], a[244:247], v[44:47]\n"
    "s_waitcnt lgkmcnt(3)\n"
    "ds_read_b128 v[188:191], v255 offset:7168\n"
    "v_mfma_f32_16x16x32_bf16 v[44:47], v[168:171], a[248:251], v[44:47]\n"
    "s_add_u32 m0, s71, 0xc000\n"
    "ds_read_b128 v[160:163], v255 offset:8192\n"
    "v_mfma_f32_16x16x32_bf16 v[44:47], v[172:175], a[252:255], v[44:47]\n"
    "s_waitcnt lgkmcnt(3)\n"
    "ds_read_b128 v[164:167], v255 offset:9216\n"
    "buffer_load_dwordx4 v200, s[40:43], s61 offen sc1 lds\n"
    "v_mfma_f32_16x16x32_bf16 v[44:47], v[176:179], v[0:3], v[44:47]\n"
    "ds_read_b128 v[168:171], v255 offset:10240\n"
    "v_mfma_f32_16x16x32_bf16 v[44:47], v[180:183], v[4:7], v[44:47]\n"
    "s_waitcnt lgkmcnt(3)\n"
    "ds_read_b128 v[172:175], v255 offset:11264\n"
    "v_mfma_f32_16x16x32_bf16 v[60:63], v[184:187], a[240:243], 0\n"
    "ds_read_b128 v[176:179], v200\n"
    "v_mfma_f32_16x16x32_bf16 v[60:63], v[188:191], a[244:247], v[60:63]\n"
    "s_waitcnt lgkmcnt(3)\n"
    "ds_read_b128 v[180:183], v200 offset:1024\n"
    "v_mfma_f32_16x16x32_bf16 v[60:63], v[160:163], a[248:251], v[60:63]\n"
    "ds_read_b128 v[184:187], v200 offset:2048\n"
    "buffer_load_dwordx4 v200, s[40:43], s61 offen offset:1024 sc1 lds\n"
    "v_mfma_f32_16x16x32_bf16 v[60:63], v[164:167], a[252:255], v[60:63]\n"
    "s_waitcnt lgkmcnt(3)\n"
    "ds_read_b128 v[188:191], v200 offset:3072\n"
    "v_mfma_f32_16x16x32_bf16 v[60:63], v[168:171], v[0:3], v[60:63]\n"
    "ds_read_b128 v[160:163], v200 offset:4096\n"
    "v_mfma_f32_16x16x32_bf16 v[60:63], v[172:175], v[4:7], v[60:63]\n"
    "s_waitcnt lgkmcnt(3)\n"
    "ds_read_b128 v[164:167], v200 offset:5120\n"
    "v_mfma_f32_16x16x32_bf16 v[72:75], v[176:179], a[240:243], 0\n"
    "ds_read_b128 v[168:171], v200 offset:6144\n"
    "v_mfma_f32_16x16x32_bf16 v[72:75], v[180:183], a[244:247], v[72:75]\n"
    "s_waitcnt lgkmcnt(3)\n"
    "ds_read_b128 v[172:175], v200 offset:7168\n"
    "buffer_load_dwordx4 v200, s[40:43], s61 offen offset:2048 sc1 lds\n"
    "v_mfma_f32_16x16x32_bf16 v[72:75], v[184:187], a[248:251], v[72:75]\n"
    "ds_read_b128 v[176:179], v200 offset:8192\n"
    "v_mfma_f32_16x16x32_bf16 v[72:75], v[188:191], a[252:255], v[72:75]\n"
    "s_waitcnt lgkmcnt(3)\n"
    "ds_read_b128 v[180:183], v200 offset:9216\n"
    "v_mfma_f32_16x16x32_bf16 v[72:75], v[160:163], v[0:3], v[72:75]\n"
    "ds_read_b128 v[184:187], v200 offset:10240\n"
    "v_mfma_f32_16x16x32_bf16 v[72:75], v[164:167], v[4:7], v[72:75]\n"
    "s_waitcnt lgkmcnt(3)\n"
    "ds_read_b128 v[188:191], v200 offset:11264\n"
    "v_max3_f32 v196, v44, v45, v46\n"
    "v_max3_f32 v196, v196, v47, v60\n"
    "v_max3_f32 v196, v196, v61, v62\n"
    "v_mfma_f32_16x16x32_bf16 v[88:91], v[168:171], a[240:243], 0\n"
    "ds_read_b128 v[160:163], v200 offset:12288\n"
    "buffer_load_dwordx4 v200, s[40:43], s61 offen offset:3072 sc1 lds\n"
    "v_max_f32 v196, v196, v63\n"
    "v_mfma_f32_16x16x32_bf16 v[88:91], v[172:175], a[244:247], v[88:91]\n"
    "s_waitcnt lgkmcnt(3)\n"
    "ds_read_b128 v[164:167], v200 offset:13312\n"
    "v_mov_b32 v197, v196\n"
    "s_nop 1\n"
    "v_permlane32_swap_b32 v196, v197\n"
    "v_max_f32 v196, v196, v197\n"
    "v_mfma_f32_16x16x32_bf16 v[88:91], v[176:179], a[248:251], v[88:91]\n"
    "ds_read_b128 v[168:171], v200 offset:14336\n"
    "v_mov_b32 v197, v196\n"
    "s_nop 1\n"
    "v_permlane16_swap_b32 v196, v197\n"
    "v_max_f32 v196, v196, v197\n"
    "v_mfma_f32_16x16x32_bf16 v[88:91], v[180:183], a[252:255], v[88:91]\n"
    "s_waitcnt lgkmcnt(3)\n"
    "s_add_u32 s65, s61, 0x1000\n"
    "ds_read_b128 v[172:175], v200 offset:15360\n"
    "v_add_f32 v196, 0x426c0000, v196\n"
    "v_sub_f32 v104, 0, v196\n"
    "v_mfma_f32_16x16x32_bf16 v[88:91], v[184:187], v[0:3], v[88:91]\n"
    "s_add_u32 m0, s71, 0xd000\n"
    "ds_read_b128 v[176:179], v200 offset:16384\n"
    "v_sub_f32 v105, 0, v196\n"
    "v_sub_f32 v106, 0, v196\n"
    "v_sub_f32 v107, 0, v196\n"
    "v_mfma_f32_16x16x32_bf16 v[88:91], v[188:191], v[4:7], v[88:91]\n"
    "s_waitcnt lgkmcnt(3)\n"
    "ds_read_b128 v[180:183], v200 offset:17408\n"
    "buffer_load_dwordx4 v200, s[40:43], s65 offen sc1 lds\n"
    "v_add_f32 v44, v44, v104\n"
    "v_mfma_f32_16x16x32_bf16 v[40:43], v[160:163], a[240:243], v[104:107]\n"
    "ds_read_b128 v[184:187], v200 offset:18432\n"
    "v_add_f32 v45, v45, v104\n"
    "v_add_f32 v46, v46, v104\n"
    "v_add_f32 v47, v47, v104\n"
    "v_mfma_f32_16x16x32_bf16 v[40:43], v[164:167], a[244:247], v[40:43]\n"
    "s_waitcnt lgkmcnt(3)\n"
    "ds_read_b128 v[188:191], v200 offset:19456\n"
    "v_add_f32 v60, v60, v104\n"
    "v_add_f32 v61, v61, v104\n"
    "v_add_f32 v62, v62, v104\n"
    "v_mfma_f32_16x16x32_bf16 v[40:43], v[168:171], a[248:251], v[40:43]\n"
    "ds_read_b128 v[160:163], v200 offset:20480\n"
    "v_add_f32 v63, v63, v104\n"
    "v_exp_f32 v44, v44\n"
    "v_mfma_f32_16x16x32_bf16 v[40:43], v[172:175], a[252:255], v[40:43]\n"
    "s_waitcnt lgkmcnt(3)\n"
    "ds_read_b128 v[164:167], v200 offset:21504\n"
    "v_exp_f32 v45, v45\n"
    "v_mfma_f32_16x16x32_bf16 v[40:43], v[176:179], v[0:3], v[40:43]\n"
    "ds_read_b128 v[168:171], v200 offset:22528\n"
    "buffer_load_dwordx4 v200, s[40:43], s65 offen offset:1024 sc1 lds\n"
    "v_mfma_f32_16x16x32_bf16 v[40:43], v[180:183], v[4:7], v[40:43]\n"
    "s_waitcnt lgkmcnt(3)\n"
    "ds_read_b128 v[172:175], v200 offset:23552\n"
    "v_exp_f32 v46, v46\n"
    "v_mfma_f32_16x16x32_bf16 v[56:59], v[184:187], a[240:243], v[104:107]\n"
    "ds_read_b128 v[176:179], v201 offset:24576\n"
    "v_exp_f32 v47, v47\n"
    "v_mfma_f32_16x16x32_bf16 v[56:59], v[188:191], a[244:247], v[56:59]\n"
    "s_waitcnt lgkmcnt(3)\n"
    "s_add_u32 s78, s64, 0x30000\n"
    "ds_read_b128 v[180:183], v201 offset:25600\n"
    "v_exp_f32 v60, v60\n"
    "v_mfma_f32_16x16x32_bf16 v[56:59], v[160:163], a[248:251], v[56:59]\n"
    "s_add_u32 m0, s72, 0x1b000\n"
    "ds_read_b128 v[184:187], v201 offset:26624\n"
    "v_exp_f32 v61, v61\n"
    "v_mfma_f32_16x16x32_bf16 v[56:59], v[164:167], a[252:255], v[56:59]\n"
    "s_waitcnt lgkmcnt(3)\n"
    "ds_read_b128 v[188:191], v201 offset:27648\n"
    "buffer_load_dwordx4 v202, s[32:35], s78 offen nt lds\n"
    "v_mfma_f32_16x16x32_bf16 v[56:59], v[168:171], v[0:3], v[56:59]\n"
    "ds_read_b128 v[160:163], v201 offset:28672\n"
    "v_exp_f32 v62, v62\n"
    "v_exp_f32 v63, v63\n"
    "v_cvt_pk_bf16_f32 v136, v44, v45\n"
    "v_cvt_pk_bf16_f32 v137, v46, v47\n"
    "v_cvt_pk_bf16_f32 v138, v60, v61\n"
    "v_cvt_pk_bf16_f32 v139, v62, v63\n"
    "v_add_f32 v72, v72, v104\n"
    "v_add_f32 v73, v73, v104\n"
    "v_add_f32 v74, v74, v104\n"
    "v_add_f32 v75, v75, v104\n"
    "v_add_f32 v88, v88, v104\n"
    "v_add_f32 v89, v89, v104\n"
    "v_add_f32 v90, v90, v104\n"
    "v_add_f32 v91, v91, v104\n"
    "v_exp_f32 v72, v72\n"
    "v_exp_f32 v73, v73\n"
    "v_exp_f32 v74, v74\n"
    "v_exp_f32 v75, v75\n"
    "v_exp_f32 v88, v88\n"
    "v_exp_f32 v89, v89\n"
    "v_exp_f32 v90, v90\n"
    "v_exp_f32 v91, v91\n"
    "v_cvt_pk_bf16_f32 v152, v72, v73\n"
    "v_cvt_pk_bf16_f32 v153, v74, v75\n"
    "v_cvt_pk_bf16_f32 v154, v88, v89\n"
    "v_cvt_pk_bf16_f32 v155, v90, v91\n"
    "v_mfma_f32_16x16x32_bf16 v[56:59], v[172:175], v[4:7], v[56:59]\n"
    "s_waitcnt lgkmcnt(3)\n"
    "ds_read_b128 v[164:167], v201 offset:29696\n"
    "v_exp_f32 v40, v40\n"
    "v_mfma_f32_16x16x32_bf16 a[8:11], v[176:179], v[136:139], 0\n"
    "ds_read_b128 v[168:171], v201 offset:30720\n"
    "v_mfma_f32_16x16x32_bf16 a[8:11], v[180:183], v[152:155], a[8:11]\n"
    "s_waitcnt lgkmcnt(3)\n"
    "ds_read_b128 v[172:175], v201 offset:31744\n"
    "v_mfma_f32_16x16x32_bf16 a[24:27], v[184:187], v[136:139], 0\n"
    "ds_read_b128 v[176:179], v201 offset:32768\n"
    "buffer_load_dwordx4 v203, s[32:35], s78 offen offset:1024 nt lds\n"
    "v_mfma_f32_16x16x32_bf16 a[24:27], v[188:191], v[152:155], a[24:27]\n"
    "s_waitcnt lgkmcnt(3)\n"
    "ds_read_b128 v[180:183], v201 offset:33792\n"
    "v_mfma_f32_16x16x32_bf16 a[40:43], v[160:163], v[136:139], 0\n"
    "ds_read_b128 v[184:187], v201 offset:34816\n"
    "v_mfma_f32_16x16x32_bf16 a[40:43], v[164:167], v[152:155], a[40:43]\n"
    "ds_read_b128 v[188:191], v201 offset:35840\n"
    "v_mfma_f32_16x16x32_bf16 v[120:123], v[192:195], v[136:139], 0\n"
    "v_exp_f32 v41, v41\n"
    "v_mfma_f32_16x16x32_bf16 v[120:123], v[192:195], v[152:155], v[120:123]\n"
    "s_waitcnt lgkmcnt(4)\n"
    "buffer_load_dwordx4 v204, s[32:35], s78 offen offset:2048 nt lds\n"
    "v_mfma_f32_16x16x32_bf16 a[56:59], v[168:171], v[136:139], 0\n"
    "ds_read_b128 v[160:163], v201 offset:36864\n"
    "v_mfma_f32_16x16x32_bf16 a[56:59], v[172:175], v[152:155], a[56:59]\n"
    "s_waitcnt lgkmcnt(3)\n"
    "ds_read_b128 v[164:167], v201 offset:37888\n"
    "v_mfma_f32_16x16x32_bf16 a[72:75], v[176:179], v[136:139], 0\n"
    "ds_read_b128 v[168:171], v201 offset:38912\n"
    "v_mfma_f32_16x16x32_bf16 a[72:75], v[180:183], v[152:155], a[72:75]\n"
    "s_waitcnt lgkmcnt(3)\n"
    "ds_read_b128 v[172:175], v201 offset:39936\n"
    "v_mfma_f32_16x16x32_bf16 a[88:91], v[184:187], v[136:139], 0\n"
    "ds_read_b128 v[176:179], v201 offset:40960\n"
    "v_mfma_f32_16x16x32_bf16 a[88:91], v[188:191], v[152:155], a[88:91]\n"
    "s_waitcnt lgkmcnt(3)\n"
    "ds_read_b128 v[180:183], v201 offset:41984\n"
    "v_mfma_f32_16x16x32_bf16 a[104:107], v[160:163], v[136:139], 0\n"
    "ds_read_b128 v[184:187], v201 offset:43008\n"
    "v_mfma_f32_16x16x32_bf16 a[104:107], v[164:167], v[152:155], a[104:107]\n"
    "s_waitcnt lgkmcnt(3)\n"
    "ds_read_b128 v[188:191], v201 offset:44032\n"
    "v_mfma_f32_16x16x32_bf16 a[120:123], v[168:171], v[136:139], 0\n"
    "ds_read_b128 v[160:163], v201 offset:45056\n"
    "v_mfma_f32_16x16x32_bf16 a[120:123], v[172:175], v[152:155], a[120:123]\n"
    "s_waitcnt lgkmcnt(3)\n"
    "ds_read_b128 v[164:167], v201 offset:46080\n"
    "v_mfma_f32_16x16x32_bf16 a[136:139], v[176:179], v[136:139], 0\n"
    "ds_read_b128 v[168:171], v201 offset:47104\n"
    "v_mfma_f32_16x16x32_bf16 a[136:139], v[180:183], v[152:155], a[136:139]\n"
    "s_waitcnt lgkmcnt(3)\n"
    "ds_read_b128 v[172:175], v201 offset:48128\n"
    "v_mfma_f32_16x16x32_bf16 a[152:155], v[184:187], v[136:139], 0\n"
    "v_exp_f32 v42, v42\n"
    "v_mfma_f32_16x16x32_bf16 a[152:155], v[188:191], v[152:155], a[152:155]\n"
    "s_waitcnt lgkmcnt(2)\n"
    "v_exp_f32 v43, v43\n"
    "v_mfma_f32_16x16x32_bf16 a[168:171], v[160:163], v[136:139], 0\n"
    "v_exp_f32 v56, v56\n"
    "v_mfma_f32_16x16x32_bf16 a[168:171], v[164:167], v[152:155], a[168:171]\n"
    "s_waitcnt lgkmcnt(0)\n"
    "v_exp_f32 v57, v57\n"
    "v_mfma_f32_16x16x32_bf16 a[184:187], v[168:171], v[136:139], 0\n"
    "v_exp_f32 v58, v58\n"
    "v_exp_f32 v59, v59\n"
    "v_mfma_f32_16x16x32_bf16 a[184:187], v[172:175], v[152:155], a[184:187]\n"
    "v_add_f32 v121, v121, v40\n"
    "v_add_f32 v122, v122, v41\n"
    "v_add_f32 v121, v121, v42\n"
    "v_add_f32 v122, v122, v43\n"
    "v_add_f32 v121, v121, v56\n"
    "v_add_f32 v122, v122, v57\n"
    "s_waitcnt vmcnt(12)\n"
    "ds_read_b128 v[8:11], v158 offset:24576\n"
    "ds_read_b128 v[12:15], v159 offset:24576\n"
    "ds_read_b128 v[16:19], v158 offset:24704\n"
    "ds_read_b128 v[20:23], v159 offset:24704\n"
    "ds_read_b128 v[24:27], v158 offset:24832\n"
    "ds_read_b128 v[28:31], v159 offset:24832\n"
    "s_waitcnt lgkmcnt(0)\n"
    "ds_read_b128 v[160:163], v255\n"
    "ds_read_b128 v[164:167], v255 offset:1024\n"
    "ds_read_b128 v[168:171], v255 offset:2048\n"
    "ds_read_b128 v[172:175], v255 offset:3072\n"
    "ds_read_b128 v[176:179], v255 offset:4096\n"
    "ds_read_b128 v[180:183], v255 offset:5120\n"
    "s_waitcnt lgkmcnt(4)\n"
    "v_mfma_f32_16x16x32_bf16 v[64:67], v[160:163], v[8:11], 0\n"
    "ds_read_b128 v[184:187], v255 offset:6144\n"
    "v_mfma_f32_16x16x32_bf16 v[64:67], v[164:167], v[12:15], v[64:67]\n"
    "s_waitcnt lgkmcnt(3)\n"
    "s_add_u32 m0, s71, 0x6000\n"
    "ds_read_b128 v[188:191], v255 offset:7168\n"
    "v_mfma_f32_16x16x32_bf16 v[64:67], v[168:171], v[16:19], v[64:67]\n"
    "ds_read_b128 v[160:163], v255 offset:8192\n"
    "buffer_load_dwordx4 v200, s[36:39], s60 offen sc1 lds\n"
    "v_mfma_f32_16x16x32_bf16 v[64:67], v[172:175], v[20:23], v[64:67]\n"
    "s_waitcnt lgkmcnt(3)\n"
    "ds_read_b128 v[164:167], v255 offset:9216\n"
    "v_mfma_f32_16x16x32_bf16 v[64:67], v[176:179], v[24:27], v[64:67]\n"
    "ds_read_b128 v[168:171], v255 offset:10240\n"
    "v_mfma_f32_16x16x32_bf16 v[64:67], v[180:183], v[28:31], v[64:67]\n"
    "s_waitcnt lgkmcnt(3)\n"
    "ds_read_b128 v[172:175], v255 offset:11264\n"
    "buffer_load_dwordx4 v200, s[36:39], s60 offen offset:1024 sc1 lds\n"
    "v_mfma_f32_16x16x32_bf16 v[80:83], v[184:187], v[8:11], 0\n"
    "ds_read_b128 v[176:179], v200\n"
    "v_mfma_f32_16x16x32_bf16 v[80:83], v[188:191], v[12:15], v[80:83]\n"
    "s_waitcnt lgkmcnt(3)\n"
    "ds_read_b128 v[180:183], v200 offset:1024\n"
    "v_mfma_f32_16x16x32_bf16 v[80:83], v[160:163], v[16:19], v[80:83]\n"
    "ds_read_b128 v[184:187], v200 offset:2048\n"
    "buffer_load_dwordx4 v200, s[36:39], s60 offen offset:2048 sc1 lds\n"
    "v_mfma_f32_16x16x32_bf16 v[80:83], v[164:167], v[20:23], v[80:83]\n"
    "s_waitcnt lgkmcnt(3)\n"
    "ds_read_b128 v[188:191], v200 offset:3072\n"
    "v_mfma_f32_16x16x32_bf16 v[80:83], v[168:171], v[24:27], v[80:83]\n"
    "ds_read_b128 v[160:163], v200 offset:4096\n"
    "v_mfma_f32_16x16x32_bf16 v[80:83], v[172:175], v[28:31], v[80:83]\n"
    "s_waitcnt lgkmcnt(3)\n"
    "ds_read_b128 v[164:167], v200 offset:5120\n"
    "buffer_load_dwordx4 v200, s[36:39], s60 offen offset:3072 sc1 lds\n"
    "v_mfma_f32_16x16x32_bf16 v[76:79], v[176:179], v[8:11], 0\n"
    "s_add_u32 s65, s60, 0x1000\n"
    "ds_read_b128 v[168:171], v200 offset:6144\n"
    "v_mfma_f32_16x16x32_bf16 v[76:79], v[180:183], v[12:15], v[76:79]\n"
    "s_waitcnt lgkmcnt(3)\n"
    "s_add_u32 m0, s71, 0x7000\n"
    "ds_read_b128 v[172:175], v200 offset:7168\n"
    "v_mfma_f32_16x16x32_bf16 v[76:79], v[184:187], v[16:19], v[76:79]\n"
    "ds_read_b128 v[176:179], v200 offset:8192\n"
    "buffer_load_dwordx4 v200, s[36:39], s65 offen sc1 lds\n"
    "v_mfma_f32_16x16x32_bf16 v[76:79], v[188:191], v[20:23], v[76:79]\n"
    "s_waitcnt lgkmcnt(3)\n"
    "ds_read_b128 v[180:183], v200 offset:9216\n"
    "v_mfma_f32_16x16x32_bf16 v[76:79], v[160:163], v[24:27], v[76:79]\n"
    "ds_read_b128 v[184:187], v200 offset:10240\n"
    "v_mfma_f32_16x16x32_bf16 v[76:79], v[164:167], v[28:31], v[76:79]\n"
    "s_waitcnt lgkmcnt(3)\n"
    "ds_read_b128 v[188:191], v200 offset:11264\n"
    "buffer_load_dwordx4 v200, s[36:39], s65 offen offset:1024 sc1 lds\n"
    "v_max3_f32 v196, v64, v65, v66\n"
    "v_mfma_f32_16x16x32_bf16 v[92:95], v[168:171], v[8:11], 0\n"
    "ds_read_b128 v[160:163], v200 offset:12288\n"
    "v_max3_f32 v196, v196, v67, v80\n"
    "v_max3_f32 v196, v196, v81, v82\n"
    "v_max_f32 v196, v196, v83\n"
    "v_mfma_f32_16x16x32_bf16 v[92:95], v[172:175], v[12:15], v[92:95]\n"
    "s_waitcnt lgkmcnt(3)\n"
    "ds_read_b128 v[164:167], v200 offset:13312\n"
    "v_mov_b32 v197, v196\n"
    "s_nop 1\n"
    "v_permlane32_swap_b32 v196, v197\n"
    "v_max_f32 v196, v196, v197\n"
    "v_mfma_f32_16x16x32_bf16 v[92:95], v[176:179], v[16:19], v[92:95]\n"
    "ds_read_b128 v[168:171], v200 offset:14336\n"
    "v_mov_b32 v197, v196\n"
    "s_nop 1\n"
    "v_permlane16_swap_b32 v196, v197\n"
    "v_max_f32 v196, v196, v197\n"
    "v_mfma_f32_16x16x32_bf16 v[92:95], v[180:183], v[20:23], v[92:95]\n"
    "s_waitcnt lgkmcnt(3)\n"
    "ds_read_b128 v[172:175], v200 offset:15360\n"
    "v_add_f32 v196, 0x426c0000, v196\n"
    "v_sub_f32 v108, 0, v196\n"
    "v_sub_f32 v109, 0, v196\n"
    "v_mfma_f32_16x16x32_bf16 v[92:95], v[184:187], v[24:27], v[92:95]\n"
    "ds_read_b128 v[176:179], v200 offset:16384\n"
    "v_sub_f32 v110, 0, v196\n"
    "v_sub_f32 v111, 0, v196\n"
    "v_add_f32 v64, v64, v108\n"
    "v_mfma_f32_16x16x32_bf16 v[92:95], v[188:191], v[28:31], v[92:95]\n"
    "s_waitcnt lgkmcnt(3)\n"
    "ds_read_b128 v[180:183], v200 offset:17408\n"
    "v_add_f32 v65, v65, v108\n"
    "v_add_f32 v66, v66, v108\n"
    "v_add_f32 v67, v67, v108\n"
    "v_mfma_f32_16x16x32_bf16 v[44:47], v[160:163], v[8:11], v[108:111]\n"
    "ds_read_b128 v[184:187], v200 offset:18432\n"
    "v_add_f32 v80, v80, v108\n"
    "v_add_f32 v81, v81, v108\n"
    "v_add_f32 v82, v82, v108\n"
    "v_mfma_f32_16x16x32_bf16 v[44:47], v[164:167], v[12:15], v[44:47]\n"
    "s_waitcnt lgkmcnt(3)\n"
    "ds_read_b128 v[188:191], v200 offset:19456\n"
    "v_add_f32 v83, v83, v108\n"
    "v_exp_f32 v64, v64\n"
    "v_mfma_f32_16x16x32_bf16 v[44:47], v[168:171], v[16:19], v[44:47]\n"
    "ds_read_b128 v[160:163], v200 offset:20480\n"
    "buffer_load_dwordx2 v[218:219], v213, s[44:47], 0 offen sc1\n"
    "v_mfma_f32_16x16x32_bf16 v[44:47], v[172:175], v[20:23], v[44:47]\n"
    "s_waitcnt lgkmcnt(3)\n"
    "ds_read_b128 v[164:167], v200 offset:21504\n"
    "v_exp_f32 v65, v65\n"
    "v_mfma_f32_16x16x32_bf16 v[44:47], v[176:179], v[24:27], v[44:47]\n"
    "ds_read_b128 v[168:171], v200 offset:22528\n"
    "v_exp_f32 v66, v66\n"
    "v_mfma_f32_16x16x32_bf16 v[44:47], v[180:183], v[28:31], v[44:47]\n"
    "s_waitcnt lgkmcnt(3)\n"
    "ds_read_b128 v[172:175], v200 offset:23552\n"
    "v_exp_f32 v67, v67\n"
    "v_mfma_f32_16x16x32_bf16 v[60:63], v[184:187], v[8:11], v[108:111]\n"
    "ds_read_b128 v[176:179], v201 offset:24576\n"
    "v_exp_f32 v80, v80\n"
    "v_mfma_f32_16x16x32_bf16 v[60:63], v[188:191], v[12:15], v[60:63]\n"
    "s_waitcnt lgkmcnt(3)\n"
    "ds_read_b128 v[180:183], v201 offset:25600\n"
    "v_exp_f32 v81, v81\n"
    "v_mfma_f32_16x16x32_bf16 v[60:63], v[160:163], v[16:19], v[60:63]\n"
    "ds_read_b128 v[184:187], v201 offset:26624\n"
    "v_exp_f32 v82, v82\n"
    "v_mfma_f32_16x16x32_bf16 v[60:63], v[164:167], v[20:23], v[60:63]\n"
    "s_waitcnt lgkmcnt(3)\n"
    "ds_read_b128 v[188:191], v201 offset:27648\n"
    "v_exp_f32 v83, v83\n"
    "v_cvt_pk_bf16_f32 v140, v64, v65\n"
    "v_mfma_f32_16x16x32_bf16 v[60:63], v[168:171], v[24:27], v[60:63]\n"
    "ds_read_b128 v[160:163], v201 offset:28672\n"
    "v_cvt_pk_bf16_f32 v141, v66, v67\n"
    "v_cvt_pk_bf16_f32 v142, v80, v81\n"
    "v_cvt_pk_bf16_f32 v143, v82, v83\n"
    "v_add_f32 v76, v76, v108\n"
    "v_add_f32 v77, v77, v108\n"
    "v_add_f32 v78, v78, v108\n"
    "v_add_f32 v79, v79, v108\n"
    "v_add_f32 v92, v92, v108\n"
    "v_add_f32 v93, v93, v108\n"
    "v_add_f32 v94, v94, v108\n"
    "v_add_f32 v95, v95, v108\n"
    "v_exp_f32 v76, v76\n"
    "v_exp_f32 v77, v77\n"
    "v_exp_f32 v78, v78\n"
    "v_exp_f32 v79, v79\n"
    "v_exp_f32 v92, v92\n"
    "v_exp_f32 v93, v93\n"
    "v_exp_f32 v94, v94\n"
    "v_exp_f32 v95, v95\n"
    "v_cvt_pk_bf16_f32 v156, v76, v77\n"
    "v_cvt_pk_bf16_f32 v157, v78, v79\n"
    "v_cvt_pk_bf16_f32 v158, v92, v93\n"
    "v_cvt_pk_bf16_f32 v159, v94, v95\n"
    "v_mfma_f32_16x16x32_bf16 v[60:63], v[172:175], v[28:31], v[60:63]\n"
    "s_waitcnt lgkmcnt(3)\n"
    "ds_read_b128 v[164:167], v201 offset:29696\n"
    "v_exp_f32 v44, v44\n"
    "v_mfma_f32_16x16x32_bf16 a[12:15], v[176:179], v[140:143], 0\n"
    "ds_read_b128 v[168:171], v201 offset:30720\n"
    "v_mfma_f32_16x16x32_bf16 a[12:15], v[180:183], v[156:159], a[12:15]\n"
    "s_waitcnt lgkmcnt(3)\n"
    "ds_read_b128 v[172:175], v201 offset:31744\n"
    "v_mfma_f32_16x16x32_bf16 a[28:31], v[184:187], v[140:143], 0\n"
    "ds_read_b128 v[176:179], v201 offset:32768\n"
    "v_mfma_f32_16x16x32_bf16 a[28:31], v[188:191], v[156:159], a[28:31]\n"
    "s_waitcnt lgkmcnt(3)\n"
    "ds_read_b128 v[180:183], v201 offset:33792\n"
    "v_mfma_f32_16x16x32_bf16 a[44:47], v[160:163], v[140:143], 0\n"
    "ds_read_b128 v[184:187], v201 offset:34816\n"
    "v_mfma_f32_16x16x32_bf16 a[44:47], v[164:167], v[156:159], a[44:47]\n"
    "ds_read_b128 v[188:191], v201 offset:35840\n"
    "v_mfma_f32_16x16x32_bf16 v[124:127], v[192:195], v[140:143], 0\n"
    "v_exp_f32 v45, v45\n"
    "v_mfma_f32_16x16x32_bf16 v[124:127], v[192:195], v[156:159], v[124:127]\n"
    "s_waitcnt lgkmcnt(4)\n"
    "v_exp_f32 v46, v46\n"
    "v_mfma_f32_16x16x32_bf16 a[60:63], v[168:171], v[140:143], 0\n"
    "ds_read_b128 v[160:163], v201 offset:36864\n"
    "v_mfma_f32_16x16x32_bf16 a[60:63], v[172:175], v[156:159], a[60:63]\n"
    "s_waitcnt lgkmcnt(3)\n"
    "ds_read_b128 v[164:167], v201 offset:37888\n"
    "v_mfma_f32_16x16x32_bf16 a[76:79], v[176:179], v[140:143], 0\n"
    "ds_read_b128 v[168:171], v201 offset:38912\n"
    "v_mfma_f32_16x16x32_bf16 a[76:79], v[180:183], v[156:159], a[76:79]\n"
    "s_waitcnt lgkmcnt(3)\n"
    "ds_read_b128 v[172:175], v201 offset:39936\n"
    "v_mfma_f32_16x16x32_bf16 a[92:95], v[184:187], v[140:143], 0\n"
    "ds_read_b128 v[176:179], v201 offset:40960\n"
    "v_mfma_f32_16x16x32_bf16 a[92:95], v[188:191], v[156:159], a[92:95]\n"
    "s_waitcnt lgkmcnt(3)\n"
    "ds_read_b128 v[180:183], v201 offset:41984\n"
    "v_mfma_f32_16x16x32_bf16 a[108:111], v[160:163], v[140:143], 0\n"
    "ds_read_b128 v[184:187], v201 offset:43008\n"
    "v_mfma_f32_16x16x32_bf16 a[108:111], v[164:167], v[156:159], a[108:111]\n"
    "s_waitcnt lgkmcnt(3)\n"
    "ds_read_b128 v[188:191], v201 offset:44032\n"
    "v_mfma_f32_16x16x32_bf16 a[124:127], v[168:171], v[140:143], 0\n"
    "ds_read_b128 v[160:163], v201 offset:45056\n"
    "v_mfma_f32_16x16x32_bf16 a[124:127], v[172:175], v[156:159], a[124:127]\n"
    "s_waitcnt lgkmcnt(3)\n"
    "ds_read_b128 v[164:167], v201 offset:46080\n"
    "v_mfma_f32_16x16x32_bf16 a[140:143], v[176:179], v[140:143], 0\n"
    "ds_read_b128 v[168:171], v201 offset:47104\n"
    "v_mfma_f32_16x16x32_bf16 a[140:143], v[180:183], v[156:159], a[140:143]\n"
    "s_waitcnt lgkmcnt(3)\n"
    "ds_read_b128 v[172:175], v201 offset:48128\n"
    "v_mfma_f32_16x16x32_bf16 a[156:159], v[184:187], v[140:143], 0\n"
    "v_exp_f32 v47, v47\n"
    "v_mfma_f32_16x16x32_bf16 a[156:159], v[188:191], v[156:159], a[156:159]\n"
    "s_waitcnt lgkmcnt(2)\n"
    "v_exp_f32 v60, v60\n"
    "v_mfma_f32_16x16x32_bf16 a[172:175], v[160:163], v[140:143], 0\n"
    "v_exp_f32 v61, v61\n"
    "v_mfma_f32_16x16x32_bf16 a[172:175], v[164:167], v[156:159], a[172:175]\n"
    "s_waitcnt lgkmcnt(0)\n"
    "v_exp_f32 v62, v62\n"
    "v_mfma_f32_16x16x32_bf16 a[188:191], v[168:171], v[140:143], 0\n"
    "v_exp_f32 v63, v63\n"
    "v_mfma_f32_16x16x32_bf16 a[188:191], v[172:175], v[156:159], a[188:191]\n"
    "v_add_f32 v125, v125, v44\n"
    "v_add_f32 v126, v126, v45\n"
    "v_add_f32 v125, v125, v46\n"
    "v_add_f32 v126, v126, v47\n"
    "s_add_u32 s64, s64, 0x3c000\n"
    "s_add_u32 s62, s62, 0x6000\n"
    "s_add_u32 s63, s63, 0x6000\n"
    "s_add_u32 s60, s60, 0x6000\n"
    "s_add_u32 s61, s61, 0x6000\n"
    "v_add_u32 v209, 0x300, v209\n"
    "v_add_u32 v210, 48, v210\n"
    "v_mov_b32 v220, 5\n"
    "s_waitcnt vmcnt(0)\n"
    "v_cmp_ne_u32 s[80:81], s75, v219\n"
    "v_cmp_gt_u32 vcc, s66, v218\n"
    "s_or_b64 vcc, vcc, s[80:81]\n"
    "s_cbranch_vccnz LSPRE2\n"
    "LSPB2:\n"
    "s_barrier\n"
    "s_cmp_eq_u32 s58, 0\n"
    "s_cbranch_scc1 LENT0\n"
    "LENT1:\n"
    "s_mov_b32 s59, 1\n"
    "s_add_u32 m0, s72, 0x18000\n"
    "s_nop 0\n"
    "buffer_load_dwordx4 v202, s[32:35], s64 offen lds\n"
    "buffer_load_dwordx4 v203, s[32:35], s64 offen offset:1024 lds\n"
    "buffer_load_dwordx4 v204, s[32:35], s64 offen offset:2048 lds\n"
    "s_add_u32 s64, s64, 0xc000\n"
    "s_mov_b32 s94, 0x3000\n"
    "s_mov_b32 s95, 0x6000\n"
    "s_add_u32 s51, s72, 0x18000\n"
    "v_add_u32 v205, 0x3000, v205\n"
    "v_add_u32 v206, 0x3000, v206\n"
    "v_add_u32 v207, 0x3000, v207\n"
    "ds_read_u16_d16_hi v223, v209 offset:6336\n"
    "ds_read_u16_d16_hi v222, v209 offset:192\n"
    "ds_read_b128 v[160:163], v200 offset:24576\n"
    "v_add_f32 v121, v121, v58\n"
    "ds_read_b128 v[232:235], v205 offset:24576\n"
    "v_add_f32 v122, v122, v59\n"
    "ds_read_b128 v[164:167], v200 offset:25600\n"
    "ds_read_b128 v[236:239], v206 offset:24576\n"
    "v_add_f32 v125, v125, v60\n"
    "v_add_f32 v126, v126, v61\n"
    "v_add_f32 v125, v125, v62\n"
    "v_add_f32 v126, v126, v63\n"
    "ds_read_u16_d16_hi v225, v210 offset:528\n"
    "ds_read_u16_d16_hi v224, v210 offset:16\n"
    "ds_read_b128 v[168:171], v200 offset:26624\n"
    "ds_read2st64_b32 v[240:241], v207 offset0:96 offset1:99\n"
    "ds_read2st64_b32 v[242:243], v207 offset0:102 offset1:105\n"
    "ds_read2st64_b32 v[244:245], v207 offset0:108 offset1:111\n"
    "ds_read2st64_b32 v[246:247], v207 offset0:114 offset1:117\n"
    "v_cvt_pk_bf16_f32 v140, v44, v45\n"
    "s_waitcnt lgkmcnt(7)\n"
    "v_mul_f32 v226, s67, v223\n"
    "ds_read_b128 v[172:175], v200 offset:27648\n"
    "v_fma_f32 v227, -v222, v226, v217\n"
    "v_cvt_f32_i32 v232, v232\n"
    "v_cvt_f32_i32 v233, v233\n"
    "v_cvt_f32_i32 v234, v234\n"
    "v_cvt_f32_i32 v235, v235\n"
    "s_nop 7\n"
    ".p2align 6\n"
    "LLOOP1:\n"
    "v_mfma_f32_16x16x32_bf16 v[64:67], v[160:163], a[192:195], v[96:99]\n"
    "ds_read_b128 v[176:179], v200 offset:28672\n"
    "v_cvt_f32_i32 v236, v236\n"
    "v_mfma_f32_16x16x32_bf16 v[64:67], v[164:167], a[196:199], v[64:67]\n"
    "v_cvt_f32_i32 v237, v237\n"
    "v_cvt_f32_i32 v238, v238\n"
    "v_mfma_f32_16x16x32_bf16 v[68:71], v[164:167], a[220:223], v[100:103]\n"
    "v_cvt_f32_i32 v239, v239\n"
    "v_mul_f32_dpp v232, v226, v232 row_newbcast:0 row_mask:0xf bank_mask:0xf\n"
    "v_mfma_f32_16x16x32_bf16 v[68:71], v[160:163], a[216:219], v[68:71]\n"
    "v_mul_f32_dpp v233, v226, v233 row_newbcast:1 row_mask:0xf bank_mask:0xf\n"
    "v_mul_f32_dpp v234, v226, v234 row_newbcast:2 row_mask:0xf bank_mask:0xf\n"
    "v_mfma_f32_16x16x32_bf16 v[72:75], v[160:163], a[240:243], v[104:107]\n"
    "ds_read_b128 v[180:183], v200 offset:29696\n"
    "v_mul_f32_dpp v235, v226, v235 row_newbcast:3 row_mask:0xf bank_mask:0xf\n"
    "v_mfma_f32_16x16x32_bf16 v[72:75], v[164:167], a[244:247], v[72:75]\n"
    "v_mul_f32_dpp v236, v226, v236 row_newbcast:4 row_mask:0xf bank_mask:0xf\n"
    "v_mul_f32_dpp v237, v226, v237 row_newbcast:5 row_mask:0xf bank_mask:0xf\n"
    "v_mfma_f32_16x16x32_bf16 v[76:79], v[164:167], v[12:15], v[108:111]\n"
    "v_mul_f32_dpp v238, v226, v238 row_newbcast:6 row_mask:0xf bank_mask:0xf\n"
    "v_mul_f32_dpp v239, v226, v239 row_newbcast:7 row_mask:0xf bank_mask:0xf\n"
    "v_mfma_f32_16x16x32_bf16 v[76:79], v[160:163], v[8:11], v[76:79]\n"
    "s_waitcnt lgkmcnt(2)\n"
    "v_add_f32_dpp v232, v227, v232 row_newbcast:0 row_mask:0xf bank_mask:0xf\n"
    "v_add_f32_dpp v233, v227, v233 row_newbcast:1 row_mask:0xf bank_mask:0xf\n"
    "v_mfma_f32_16x16x32_bf16 v[76:79], v[168:171], v[16:19], v[76:79]\n"
    "ds_read_b128 v[160:163], v200 offset:34816\n"
    "v_add_f32_dpp v234, v227, v234 row_newbcast:2 row_mask:0xf bank_mask:0xf\n"
    "v_mfma_f32_16x16x32_bf16 v[76:79], v[172:175], v[20:23], v[76:79]\n"
    "v_add_f32_dpp v235, v227, v235 row_newbcast:3 row_mask:0xf bank_mask:0xf\n"
    "v_add_f32_dpp v236, v227, v236 row_newbcast:4 row_mask:0xf bank_mask:0xf\n"
    "v_mfma_f32_16x16x32_bf16 v[72:75], v[172:175], a[252:255], v[72:75]\n"
    "v_add_f32_dpp v237, v227, v237 row_newbcast:5 row_mask:0xf bank_mask:0xf\n"
    "v_add_f32_dpp v238, v227, v238 row_newbcast:6 row_mask:0xf bank_mask:0xf\n"
    "v_mfma_f32_16x16x32_bf16 v[72:75], v[168:171], a[248:251], v[72:75]\n"
    "v_add_f32_dpp v239, v227, v239 row_newbcast:7 row_mask:0xf bank_mask:0xf\n"
    "v_cvt_pk_bf16_f32 v232, v232, v233\n"
    "v_mfma_f32_16x16x32_bf16 v[68:71], v[168:171], a[224:227], v[68:71]\n"
    "ds_read_b128 v[164:167], v200 offset:35840\n"
    "v_cvt_pk_bf16_f32 v233, v234, v235\n"
    "v_mfma_f32_16x16x32_bf16 v[68:71], v[172:175], a[228:231], v[68:71]\n"
    "v_cvt_pk_bf16_f32 v234, v236, v237\n"
    "v_cvt_pk_bf16_f32 v235, v238, v239\n"
    "v_mfma_f32_16x16x32_bf16 v[64:67], v[172:175], a[204:207], v[64:67]\n"
    "buffer_store_dwordx4 v[232:235], v211, s[36:39], s62 offen sc1\n"
    "v_mul_f32 v228, s68, v225\n"
    "v_fma_f32 v229, -v224, v228, s69\n"
    "v_cvt_f32_i32 v240, v240\n"
    "v_mfma_f32_16x16x32_bf16 v[64:67], v[168:171], a[200:203], v[64:67]\n"
    "s_waitcnt lgkmcnt(2)\n"
    "v_cvt_f32_i32 v241, v241\n"
    "v_cvt_f32_i32 v242, v242\n"
    "v_mfma_f32_16x16x32_bf16 v[64:67], v[176:179], a[208:211], v[64:67]\n"
    "ds_read_b128 v[168:171], v200 offset:32768\n"
    "v_cvt_f32_i32 v243, v243\n"
    "v_mfma_f32_16x16x32_bf16 v[64:67], v[180:183], a[212:215], v[64:67]\n"
    "v_cvt_f32_i32 v244, v244\n"
    "v_cvt_f32_i32 v245, v245\n"
    "v_mfma_f32_16x16x32_bf16 v[68:71], v[180:183], a[236:239], v[68:71]\n"
    "v_cvt_f32_i32 v246, v246\n"
    "v_cvt_f32_i32 v247, v247\n"
    "v_mfma_f32_16x16x32_bf16 v[68:71], v[176:179], a[232:235], v[68:71]\n"
    "v_mul_f32_dpp v240, v228, v240 row_newbcast:0 row_mask:0xf bank_mask:0xf\n"
    "v_mul_f32_dpp v241, v228, v241 row_newbcast:1 row_mask:0xf bank_mask:0xf\n"
    "v_mfma_f32_16x16x32_bf16 v[72:75], v[176:179], v[0:3], v[72:75]\n"
    "ds_read_b128 v[172:175], v200 offset:33792\n"
    "v_mul_f32_dpp v242, v228, v242 row_newbcast:2 row_mask:0xf bank_mask:0xf\n"
    "v_mfma_f32_16x16x32_bf16 v[72:75], v[180:183], v[4:7], v[72:75]\n"
    "v_mul_f32_dpp v243, v228, v243 row_newbcast:3 row_mask:0xf bank_mask:0xf\n"
    "v_mul_f32_dpp v244, v228, v244 row_newbcast:4 row_mask:0xf bank_mask:0xf\n"
    "v_mfma_f32_16x16x32_bf16 v[76:79], v[180:183], v[28:31], v[76:79]\n"
    "v_mul_f32_dpp v245, v228, v245 row_newbcast:5 row_mask:0xf bank_mask:0xf\n"
    "v_mul_f32_dpp v246, v228, v246 row_newbcast:6 row_mask:0xf bank_mask:0xf\n"
    "v_mfma_f32_16x16x32_bf16 v[76:79], v[176:179], v[24:27], v[76:79]\n"
    "s_waitcnt lgkmcnt(2)\n"
    "v_mul_f32_dpp v247, v228, v247 row_newbcast:7 row_mask:0xf bank_mask:0xf\n"
    "v_add_f32_dpp v240, v229, v240 row_newbcast:0 row_mask:0xf bank_mask:0xf\n"
    "v_mfma_f32_16x16x32_bf16 v[92:95], v[160:163], v[24:27], v[108:111]\n"
    "ds_read_b128 v[176:179], v200 offset:30720\n"
    "v_add_f32_dpp v241, v229, v241 row_newbcast:1 row_mask:0xf bank_mask:0xf\n"
    "v_mfma_f32_16x16x32_bf16 v[92:95], v[164:167], v[28:31], v[92:95]\n"
    "v_add_f32_dpp v242, v229, v242 row_newbcast:2 row_mask:0xf bank_mask:0xf\n"
    "v_add_f32_dpp v243, v229, v243 row_newbcast:3 row_mask:0xf bank_mask:0xf\n"
    "v_mfma_f32_16x16x32_bf16 v[88:91], v[164:167], v[4:7], v[104:107]\n"
    "v_add_f32_dpp v244, v229, v244 row_newbcast:4 row_mask:0xf bank_mask:0xf\n"
    "v_add_f32_dpp v245, v229, v245 row_newbcast:5 row_mask:0xf bank_mask:0xf\n"
    "v_mfma_f32_16x16x32_bf16 v[88:91], v[160:163], v[0:3], v[88:91]\n"
    "v_add_f32_dpp v246, v229, v246 row_newbcast:6 row_mask:0xf bank_mask:0xf\n"
    "v_add_f32_dpp v247, v229, v247 row_newbcast:7 row_mask:0xf bank_mask:0xf\n"
    "v_mfma_f32_16x16x32_bf16 v[84:87], v[160:163], a[232:235], v[100:103]\n"
    "ds_read_b128 v[180:183], v200 offset:31744\n"
    "v_cvt_pk_bf16_f32 v141, v46, v47\n"
    "v_mfma_f32_16x16x32_bf16 v[84:87], v[164:167], a[236:239], v[84:87]\n"
    "v_cvt_pk_bf16_f32 v240, v240, v241\n"
    "v_cvt_pk_bf16_f32 v136, v40, v41\n"
    "v_mfma_f32_16x16x32_bf16 v[80:83], v[164:167], a[212:215], v[96:99]\n"
    "v_cvt_pk_bf16_f32 v241, v242, v243\n"
    "v_cvt_pk_bf16_f32 v137, v42, v43\n"
    "v_mfma_f32_16x16x32_bf16 v[80:83], v[160:163], a[208:211], v[80:83]\n"
    "s_waitcnt lgkmcnt(2)\n"
    "v_cvt_pk_bf16_f32 v242, v244, v245\n"
    "v_cvt_pk_bf16_f32 v132, v36, v37\n"
    "v_mfma_f32_16x16x32_bf16 v[80:83], v[168:171], a[200:203], v[80:83]\n"
    "ds_read_b128 v[160:163], v200 offset:36864\n"
    "v_cvt_pk_bf16_f32 v243, v246, v247\n"
    "v_mfma_f32_16x16x32_bf16 v[80:83], v[172:175], a[204:207], v[80:83]\n"
    "v_cvt_pk_bf16_f32 v133, v38, v39\n"
    "v_cvt_pk_bf16_f32 v128, v32, v33\n"
    "v_mfma_f32_16x16x32_bf16 v[84:87], v[172:175], a[228:231], v[84:87]\n"
    "v_cvt_pk_bf16_f32 v129, v34, v35\n"
    "v_cvt_pk_bf16_f32 v142, v60, v61\n"
    "v_mfma_f32_16x16x32_bf16 v[84:87], v[168:171], a[224:227], v[84:87]\n"
    "s_add_u32 s65, s60, s92\n"
    "buffer_store_dwordx4 v[240:243], v212, s[40:43], s63 offen sc1\n"
    "v_exp_f32 v64, v64\n"
    "v_cvt_pk_bf16_f32 v143, v62, v63\n"
    "v_mfma_f32_16x16x32_bf16 v[88:91], v[168:171], a[248:251], v[88:91]\n"
    "s_add_u32 m0, s90, 0\n"
    "ds_read_b128 v[164:167], v200 offset:37888\n"
    "v_cvt_pk_bf16_f32 v138, v56, v57\n"
    "v_mfma_f32_16x16x32_bf16 v[88:91], v[172:175], a[252:255], v[88:91]\n"
    "v_exp_f32 v65, v65\n"
    "v_mfma_f32_16x16x32_bf16 v[92:95], v[172:175], v[20:23], v[92:95]\n"
    "buffer_load_dwordx4 v200, s[36:39], s65 offen sc1 lds\n"
    "v_mfma_f32_16x16x32_bf16 v[92:95], v[168:171], v[16:19], v[92:95]\n"
    "s_waitcnt lgkmcnt(2)\n"
    "v_exp_f32 v66, v66\n"
    "v_mfma_f32_16x16x32_bf16 v[92:95], v[176:179], v[8:11], v[92:95]\n"
    "ds_read_b128 v[168:171], v200 offset:38912\n"
    "v_cvt_pk_bf16_f32 v139, v58, v59\n"
    "v_mfma_f32_16x16x32_bf16 v[92:95], v[180:183], v[12:15], v[92:95]\n"
    "v_exp_f32 v67, v67\n"
    "v_mfma_f32_16x16x32_bf16 v[88:91], v[180:183], a[244:247], v[88:91]\n"
    "buffer_load_dwordx4 v200, s[36:39], s65 offen offset:1024 sc1 lds\n"
    "v_mfma_f32_16x16x32_bf16 v[88:91], v[176:179], a[240:243], v[88:91]\n"
    "v_exp_f32 v68, v68\n"
    "v_mfma_f32_16x16x32_bf16 v[84:87], v[176:179], a[216:219], v[84:87]\n"
    "ds_read_b128 v[172:175], v200 offset:39936\n"
    "v_cvt_pk_bf16_f32 v134, v52, v53\n"
    "v_mfma_f32_16x16x32_bf16 v[84:87], v[180:183], a[220:223], v[84:87]\n"
    "v_exp_f32 v69, v69\n"
    "v_mfma_f32_16x16x32_bf16 v[80:83], v[180:183], a[196:199], v[80:83]\n"
    "buffer_load_dwordx4 v200, s[36:39], s65 offen offset:2048 sc1 lds\n"
    "v_mfma_f32_16x16x32_bf16 v[80:83], v[176:179], a[192:195], v[80:83]\n"
    "s_waitcnt lgkmcnt(2)\n"
    "v_exp_f32 v70, v70\n"
    "v_mfma_f32_16x16x32_bf16 v[32:35], v[160:163], a[192:195], v[96:99]\n"
    "ds_read_b128 v[176:179], v200 offset:40960\n"
    "v_cvt_pk_bf16_f32 v135, v54, v55\n"
    "v_mfma_f32_16x16x32_bf16 v[32:35], v[164:167], a[196:199], v[32:35]\n"
    "v_exp_f32 v71, v71\n"
    "v_mfma_f32_16x16x32_bf16 v[36:39], v[164:167], a[220:223], v[100:103]\n"
    "buffer_load_dwordx4 v200, s[36:39], s65 offen offset:3072 sc1 lds\n"
    "v_mfma_f32_16x16x32_bf16 v[36:39], v[160:163], a[216:219], v[36:39]\n"
    "s_add_u32 s65, s65, 0x1000\n"
    "v_exp_f32 v72, v72\n"
    "v_mfma_f32_16x16x32_bf16 v[40:43], v[160:163], a[240:243], v[104:107]\n"
    "s_add_u32 m0, s90, 0x1000\n"
    "ds_read_b128 v[180:183], v200 offset:41984\n"
    "v_cvt_pk_bf16_f32 v130, v48, v49\n"
    "v_mfma_f32_16x16x32_bf16 v[40:43], v[164:167], a[244:247], v[40:43]\n"
    "v_exp_f32 v73, v73\n"
    "v_mfma_f32_16x16x32_bf16 v[44:47], v[164:167], v[12:15], v[108:111]\n"
    "buffer_load_dwordx4 v200, s[36:39], s65 offen sc1 lds\n"
    "v_mfma_f32_16x16x32_bf16 v[44:47], v[160:163], v[8:11], v[44:47]\n"
    "s_waitcnt lgkmcnt(2)\n"
    "v_exp_f32 v74, v74\n"
    "v_mfma_f32_16x16x32_bf16 v[44:47], v[168:171], v[16:19], v[44:47]\n"
    "ds_read_b128 v[160:163], v200 offset:47104\n"
    "v_cvt_pk_bf16_f32 v131, v50, v51\n"
    "v_mfma_f32_16x16x32_bf16 v[44:47], v[172:175], v[20:23], v[44:47]\n"
    "v_exp_f32 v75, v75\n"
    "v_mfma_f32_16x16x32_bf16 v[40:43], v[172:175], a[252:255], v[40:43]\n"
    "buffer_load_dwordx4 v200, s[36:39], s65 offen offset:1024 sc1 lds\n"
    "v_mfma_f32_16x16x32_bf16 v[40:43], v[168:171], a[248:251], v[40:43]\n"
    "v_exp_f32 v76, v76\n"
    "v_mfma_f32_16x16x32_bf16 v[36:39], v[168:171], a[224:227], v[36:39]\n"
    "ds_read_b128 v[164:167], v200 offset:48128\n"
    "v_cvt_pk_bf16_f32 v144, v64, v65\n"
    "v_mfma_f32_16x16x32_bf16 v[36:39], v[172:175], a[228:231], v[36:39]\n"
    "v_exp_f32 v77, v77\n"
    "v_mfma_f32_16x16x32_bf16 v[32:35], v[172:175], a[204:207], v[32:35]\n"
    "buffer_load_dwordx4 v200, s[36:39], s65 offen offset:2048 sc1 lds\n"
    "v_mfma_f32_16x16x32_bf16 v[32:35], v[168:171], a[200:203], v[32:35]\n"
    "s_waitcnt lgkmcnt(2)\n"
    "v_exp_f32 v78, v78\n"
    "v_mfma_f32_16x16x32_bf16 v[32:35], v[176:179], a[208:211], v[32:35]\n"
    "s_add_u32 m0, s71, 0x12000\n"
    "ds_read_b128 v[168:171], v200 offset:45056\n"
    "v_cvt_pk_bf16_f32 v145, v66, v67\n"
    "v_mfma_f32_16x16x32_bf16 v[32:35], v[180:183], a[212:215], v[32:35]\n"
    "v_exp_f32 v79, v79\n"
    "v_mfma_f32_16x16x32_bf16 v[36:39], v[180:183], a[236:239], v[36:39]\n"
    "buffer_load_dwordx4 v200, s[40:43], s61 offen sc1 lds\n"
    "v_mfma_f32_16x16x32_bf16 v[36:39], v[176:179], a[232:235], v[36:39]\n"
    "v_exp_f32 v80, v80\n"
    "v_mfma_f32_16x16x32_bf16 v[40:43], v[176:179], v[0:3], v[40:43]\n"
    "ds_read_b128 v[172:175], v200 offset:46080\n"
    "v_cvt_pk_bf16_f32 v148, v68, v69\n"
    "v_mfma_f32_16x16x32_bf16 v[40:43], v[180:183], v[4:7], v[40:43]\n"
    "v_exp_f32 v81, v81\n"
    "v_mfma_f32_16x16x32_bf16 v[44:47], v[180:183], v[28:31], v[44:47]\n"
    "buffer_load_dwordx4 v200, s[40:43], s61 offen offset:1024 sc1 lds\n"
    "v_mfma_f32_16x16x32_bf16 v[44:47], v[176:179], v[24:27], v[44:47]\n"
    "s_waitcnt lgkmcnt(2)\n"
    "v_exp_f32 v82, v82\n"
    "v_mfma_f32_16x16x32_bf16 v[60:63], v[160:163], v[24:27], v[108:111]\n"
    "ds_read_b128 v[176:179], v200 offset:43008\n"
    "v_cvt_pk_bf16_f32 v149, v70, v71\n"
    "v_mfma_f32_16x16x32_bf16 v[60:63], v[164:167], v[28:31], v[60:63]\n"
    "v_exp_f32 v83, v83\n"
    "v_mfma_f32_16x16x32_bf16 v[56:59], v[164:167], v[4:7], v[104:107]\n"
    "buffer_load_dwordx4 v200, s[40:43], s61 offen offset:2048 sc1 lds\n"
    "v_mfma_f32_16x16x32_bf16 v[56:59], v[160:163], v[0:3], v[56:59]\n"
    "v_exp_f32 v84, v84\n"
    "v_mfma_f32_16x16x32_bf16 v[52:55], v[160:163], a[232:235], v[100:103]\n"
    "ds_read_b128 v[180:183], v200 offset:44032\n"
    "v_cvt_pk_bf16_f32 v152, v72, v73\n"
    "v_mfma_f32_16x16x32_bf16 v[52:55], v[164:167], a[236:239], v[52:55]\n"
    "v_exp_f32 v85, v85\n"
    "v_mfma_f32_16x16x32_bf16 v[48:51], v[164:167], a[212:215], v[96:99]\n"
    "buffer_load_dwordx4 v200, s[40:43], s61 offen offset:3072 sc1 lds\n"
    "v_mfma_f32_16x16x32_bf16 v[48:51], v[160:163], a[208:211], v[48:51]\n"
    "s_waitcnt lgkmcnt(2)\n"
    "s_add_u32 s65, s61, 0x1000\n"
    "v_cvt_pk_bf16_f32 v153, v74, v75\n"
    "v_mfma_f32_16x16x32_bf16 v[48:51], v[168:171], a[200:203], v[48:51]\n"
    "s_add_u32 m0, s71, 0x13000\n"
    "ds_read_b128 v[160:163], v201\n"
    "v_cvt_pk_bf16_f32 v156, v76, v77\n"
    "v_mfma_f32_16x16x32_bf16 v[48:51], v[172:175], a[204:207], v[48:51]\n"
    "v_exp_f32 v86, v86\n"
    "v_mfma_f32_16x16x32_bf16 v[52:55], v[172:175], a[228:231], v[52:55]\n"
    "buffer_load_dwordx4 v200, s[40:43], s65 offen sc1 lds\n"
    "v_mfma_f32_16x16x32_bf16 v[52:55], v[168:171], a[224:227], v[52:55]\n"
    "v_exp_f32 v87, v87\n"
    "v_mfma_f32_16x16x32_bf16 v[56:59], v[168:171], a[248:251], v[56:59]\n"
    "ds_read_b128 v[164:167], v201 offset:1024\n"
    "v_cvt_pk_bf16_f32 v157, v78, v79\n"
    "v_mfma_f32_16x16x32_bf16 v[56:59], v[172:175], a[252:255], v[56:59]\n"
    "v_exp_f32 v88, v88\n"
    "v_mfma_f32_16x16x32_bf16 v[60:63], v[172:175], v[20:23], v[60:63]\n"
    "buffer_load_dwordx4 v200, s[40:43], s65 offen offset:1024 sc1 lds\n"
    "v_mfma_f32_16x16x32_bf16 v[60:63], v[168:171], v[16:19], v[60:63]\n"
    "s_waitcnt lgkmcnt(2)\n"
    "v_exp_f32 v89, v89\n"
    "v_mfma_f32_16x16x32_bf16 v[60:63], v[176:179], v[8:11], v[60:63]\n"
    "ds_read_b128 v[168:171], v201 offset:2048\n"
    "v_cvt_pk_bf16_f32 v146, v80, v81\n"
    "v_mfma_f32_16x16x32_bf16 v[60:63], v[180:183], v[12:15], v[60:63]\n"
    "v_exp_f32 v90, v90\n"
    "v_mfma_f32_16x16x32_bf16 v[56:59], v[180:183], a[244:247], v[56:59]\n"
    "v_exp_f32 v91, v91\n"
    "v_mfma_f32_16x16x32_bf16 v[56:59], v[176:179], a[240:243], v[56:59]\n"
    "v_cvt_pk_bf16_f32 v147, v82, v83\n"
    "v_cvt_pk_bf16_f32 v150, v84, v85\n"
    "v_mfma_f32_16x16x32_bf16 v[52:55], v[176:179], a[216:219], v[52:55]\n"
    "ds_read_b128 v[172:175], v201 offset:3072\n"
    "v_cvt_pk_bf16_f32 v151, v86, v87\n"
    "v_mfma_f32_16x16x32_bf16 v[52:55], v[180:183], a[220:223], v[52:55]\n"
    "v_exp_f32 v92, v92\n"
    "v_mfma_f32_16x16x32_bf16 v[48:51], v[180:183], a[196:199], v[48:51]\n"
    "v_exp_f32 v93, v93\n"
    "v_mfma_f32_16x16x32_bf16 v[48:51], v[176:179], a[192:195], v[48:51]\n"
    "s_waitcnt lgkmcnt(2)\n"
    "v_exp_f32 v94, v94\n"
    "v_mfma_f32_16x16x32_bf16 a[0:3], v[160:163], v[128:131], a[0:3]\n"
    "ds_read_b128 v[176:179], v201 offset:4096\n"
    "v_cvt_pk_bf16_f32 v154, v88, v89\n"
    "v_mfma_f32_16x16x32_bf16 a[0:3], v[164:167], v[144:147], a[0:3]\n"
    "v_cvt_pk_bf16_f32 v155, v90, v91\n"
    "v_cvt_pk_bf16_f32 v158, v92, v93\n"
    "v_mfma_f32_16x16x32_bf16 a[4:7], v[160:163], v[132:135], a[4:7]\n"
    "v_exp_f32 v95, v95\n"
    "v_mfma_f32_16x16x32_bf16 a[4:7], v[164:167], v[148:151], a[4:7]\n"
    "v_cvt_pk_bf16_f32 v159, v94, v95\n"
    "v_add_f32 v113, v113, v64\n"
    "v_mfma_f32_16x16x32_bf16 a[8:11], v[160:163], v[136:139], a[8:11]\n"
    "ds_read_b128 v[180:183], v201 offset:5120\n"
    "v_add_f32 v114, v114, v65\n"
    "v_mfma_f32_16x16x32_bf16 a[8:11], v[164:167], v[152:155], a[8:11]\n"
    "v_exp_f32 v32, v32\n"
    "v_mfma_f32_16x16x32_bf16 a[12:15], v[160:163], v[140:143], a[12:15]\n"
    "v_exp_f32 v33, v33\n"
    "v_mfma_f32_16x16x32_bf16 a[12:15], v[164:167], v[156:159], a[12:15]\n"
    "s_waitcnt lgkmcnt(2)\n"
    "v_exp_f32 v34, v34\n"
    "v_mfma_f32_16x16x32_bf16 a[28:31], v[168:171], v[140:143], a[28:31]\n"
    "ds_read_b128 v[160:163], v201 offset:6144\n"
    "v_add_f32 v113, v113, v66\n"
    "v_mfma_f32_16x16x32_bf16 a[28:31], v[172:175], v[156:159], a[28:31]\n"
    "v_exp_f32 v35, v35\n"
    "v_mfma_f32_16x16x32_bf16 a[24:27], v[172:175], v[152:155], a[24:27]\n"
    "v_exp_f32 v36, v36\n"
    "v_mfma_f32_16x16x32_bf16 a[24:27], v[168:171], v[136:139], a[24:27]\n"
    "v_exp_f32 v37, v37\n"
    "v_mfma_f32_16x16x32_bf16 a[20:23], v[168:171], v[132:135], a[20:23]\n"
    "ds_read_b128 v[164:167], v201 offset:7168\n"
    "v_add_f32 v114, v114, v67\n"
    "v_mfma_f32_16x16x32_bf16 a[20:23], v[172:175], v[148:151], a[20:23]\n"
    "v_exp_f32 v38, v38\n"
    "v_mfma_f32_16x16x32_bf16 a[16:19], v[172:175], v[144:147], a[16:19]\n"
    "v_exp_f32 v39, v39\n"
    "v_mfma_f32_16x16x32_bf16 a[16:19], v[168:171], v[128:131], a[16:19]\n"
    "s_waitcnt lgkmcnt(2)\n"
    "v_exp_f32 v40, v40\n"
    "v_mfma_f32_16x16x32_bf16 a[32:35], v[176:179], v[128:131], a[32:35]\n"
    "ds_read_b128 v[168:171], v201 offset:8192\n"
    "v_add_f32 v117, v117, v68\n"
    "v_mfma_f32_16x16x32_bf16 a[32:35], v[180:183], v[144:147], a[32:35]\n"
    "v_exp_f32 v41, v41\n"
    "v_mfma_f32_16x16x32_bf16 a[36:39], v[180:183], v[148:151], a[36:39]\n"
    "v_exp_f32 v42, v42\n"
    "v_mfma_f32_16x16x32_bf16 a[36:39], v[176:179], v[132:135], a[36:39]\n"
    "v_exp_f32 v43, v43\n"
    "v_mfma_f32_16x16x32_bf16 a[40:43], v[176:179], v[136:139], a[40:43]\n"
    "ds_read_b128 v[172:175], v201 offset:9216\n"
    "v_add_f32 v118, v118, v69\n"
    "v_mfma_f32_16x16x32_bf16 a[40:43], v[180:183], v[152:155], a[40:43]\n"
    "v_exp_f32 v44, v44\n"
    "v_mfma_f32_16x16x32_bf16 a[44:47], v[180:183], v[156:159], a[44:47]\n"
    "v_exp_f32 v45, v45\n"
    "v_mfma_f32_16x16x32_bf16 a[44:47], v[176:179], v[140:143], a[44:47]\n"
    "s_waitcnt lgkmcnt(2)\n"
    "v_exp_f32 v46, v46\n"
    "v_mfma_f32_16x16x32_bf16 a[60:63], v[160:163], v[140:143], a[60:63]\n"
    "ds_read_b128 v[176:179], v201 offset:10240\n"
    "v_add_f32 v117, v117, v70\n"
    "v_mfma_f32_16x16x32_bf16 a[60:63], v[164:167], v[156:159], a[60:63]\n"
    "v_exp_f32 v47, v47\n"
    "v_mfma_f32_16x16x32_bf16 a[56:59], v[164:167], v[152:155], a[56:59]\n"
    "v_exp_f32 v48, v48\n"
    "v_mfma_f32_16x16x32_bf16 a[56:59], v[160:163], v[136:139], a[56:59]\n"
    "s_add_u32 m0, s51, s95\n"
    "v_exp_f32 v49, v49\n"
    "v_mfma_f32_16x16x32_bf16 a[52:55], v[160:163], v[132:135], a[52:55]\n"
    "ds_read_b128 v[180:183], v201 offset:11264\n"
    "v_add_f32 v113, v113, v32\n"
    "v_mfma_f32_16x16x32_bf16 a[52:55], v[164:167], v[148:151], a[52:55]\n"
    "buffer_load_dwordx4 v202, s[32:35], s64 offen lds\n"
    "v_mfma_f32_16x16x32_bf16 a[48:51], v[164:167], v[144:147], a[48:51]\n"
    "v_exp_f32 v50, v50\n"
    "v_mfma_f32_16x16x32_bf16 a[48:51], v[160:163], v[128:131], a[48:51]\n"
    "s_waitcnt lgkmcnt(2)\n"
    "v_exp_f32 v51, v51\n"
    "v_mfma_f32_16x16x32_bf16 a[64:67], v[168:171], v[128:131], a[64:67]\n"
    "ds_read_b128 v[160:163], v201 offset:12288\n"
    "v_add_f32 v118, v118, v71\n"
    "v_mfma_f32_16x16x32_bf16 a[64:67], v[172:175], v[144:147], a[64:67]\n"
    "v_exp_f32 v52, v52\n"
    "v_mfma_f32_16x16x32_bf16 a[68:71], v[172:175], v[148:151], a[68:71]\n"
    "buffer_load_dwordx4 v203, s[32:35], s64 offen offset:1024 lds\n"
    "v_mfma_f32_16x16x32_bf16 a[68:71], v[168:171], v[132:135], a[68:71]\n"
    "v_add_f32 v114, v114, v33\n"
    "v_add_f32 v121, v121, v72\n"
    "v_mfma_f32_16x16x32_bf16 a[72:75], v[168:171], v[136:139], a[72:75]\n"
    "ds_read_b128 v[164:167], v201 offset:13312\n"
    "v_add_f32 v113, v113, v34\n"
    "v_mfma_f32_16x16x32_bf16 a[72:75], v[172:175], v[152:155], a[72:75]\n"
    "v_exp_f32 v53, v53\n"
    "v_mfma_f32_16x16x32_bf16 a[76:79], v[172:175], v[156:159], a[76:79]\n"
    "v_add_f32 v122, v122, v73\n"
    "v_add_f32 v114, v114, v35\n"
    "v_mfma_f32_16x16x32_bf16 a[76:79], v[168:171], v[140:143], a[76:79]\n"
    "s_waitcnt lgkmcnt(2)\n"
    "buffer_load_dwordx4 v204, s[32:35], s64 offen offset:2048 lds\n"
    "v_mfma_f32_16x16x32_bf16 a[92:95], v[176:179], v[140:143], a[92:95]\n"
    "ds_read_b128 v[168:171], v201 offset:14336\n"
    "v_add_f32 v121, v121, v74\n"
    "v_mfma_f32_16x16x32_bf16 a[92:95], v[180:183], v[156:159], a[92:95]\n"
    "v_add_f32 v117, v117, v36\n"
    "v_add_f32 v122, v122, v75\n"
    "v_mfma_f32_16x16x32_bf16 a[88:91], v[180:183], v[152:155], a[88:91]\n"
    "v_exp_f32 v54, v54\n"
    "v_mfma_f32_16x16x32_bf16 a[88:91], v[176:179], v[136:139], a[88:91]\n"
    "v_add_f32 v118, v118, v37\n"
    "v_add_f32 v125, v125, v76\n"
    "v_mfma_f32_16x16x32_bf16 a[84:87], v[176:179], v[132:135], a[84:87]\n"
    "s_mov_b32 s95, s94\n"
    "ds_read_b128 v[172:175], v201 offset:15360\n"
    "v_add_f32 v117, v117, v38\n"
    "v_mfma_f32_16x16x32_bf16 a[84:87], v[180:183], v[148:151], a[84:87]\n"
    "s_sub_u32 s94, s94, 0x3000\n"
    "v_exp_f32 v55, v55\n"
    "v_mfma_f32_16x16x32_bf16 a[80:83], v[180:183], v[144:147], a[80:83]\n"
    "s_min_u32 s94, s94, 0x6000\n"
    "v_add_f32 v126, v126, v77\n"
    "v_add_f32 v118, v118, v39\n"
    "v_mfma_f32_16x16x32_bf16 a[80:83], v[176:179], v[128:131], a[80:83]\n"
    "s_waitcnt lgkmcnt(2)\n"
    "v_add_f32 v125, v125, v78\n"
    "v_add_f32 v121, v121, v40\n"
    "v_mfma_f32_16x16x32_bf16 a[96:99], v[160:163], v[128:131], a[96:99]\n"
    "s_sub_u32 s93, s94, s95\n"
    "ds_read_b128 v[176:179], v201 offset:16384\n"
    "v_add_f32 v126, v126, v79\n"
    "v_mfma_f32_16x16x32_bf16 a[96:99], v[164:167], v[144:147], a[96:99]\n"
    "v_exp_f32 v56, v56\n"
    "v_mfma_f32_16x16x32_bf16 a[100:103], v[164:167], v[148:151], a[100:103]\n"
    "v_add_f32 v122, v122, v41\n"
    "v_add_f32 v113, v113, v80\n"
    "v_mfma_f32_16x16x32_bf16 a[100:103], v[160:163], v[132:135], a[100:103]\n"
    "v_add_f32 v121, v121, v42\n"
    "v_add_f32 v114, v114, v81\n"
    "v_mfma_f32_16x16x32_bf16 a[104:107], v[160:163], v[136:139], a[104:107]\n"
    "ds_read_b128 v[180:183], v201 offset:17408\n"
    "v_add_f32 v122, v122, v43\n"
    "v_mfma_f32_16x16x32_bf16 a[104:107], v[164:167], v[152:155], a[104:107]\n"
    "v_exp_f32 v57, v57\n"
    "v_mfma_f32_16x16x32_bf16 a[108:111], v[164:167], v[156:159], a[108:111]\n"
    "v_add_f32 v113, v113, v82\n"
    "v_add_f32 v125, v125, v44\n"
    "v_mfma_f32_16x16x32_bf16 a[108:111], v[160:163], v[140:143], a[108:111]\n"
    "s_waitcnt lgkmcnt(2)\n"
    "v_exp_f32 v58, v58\n"
    "v_mfma_f32_16x16x32_bf16 a[124:127], v[168:171], v[140:143], a[124:127]\n"
    "ds_read_b128 v[160:163], v201 offset:18432\n"
    "v_add_f32 v114, v114, v83\n"
    "v_mfma_f32_16x16x32_bf16 a[124:127], v[172:175], v[156:159], a[124:127]\n"
    "v_add_f32 v126, v126, v45\n"
    "v_add_f32 v117, v117, v84\n"
    "v_mfma_f32_16x16x32_bf16 a[120:123], v[172:175], v[152:155], a[120:123]\n"
    "v_add_f32 v125, v125, v46\n"
    "v_add_f32 v118, v118, v85\n"
    "v_mfma_f32_16x16x32_bf16 a[120:123], v[168:171], v[136:139], a[120:123]\n"
    "v_exp_f32 v59, v59\n"
    "v_mfma_f32_16x16x32_bf16 a[116:119], v[168:171], v[132:135], a[116:119]\n"
    "ds_read_b128 v[164:167], v201 offset:19456\n"
    "v_add_f32 v126, v126, v47\n"
    "v_mfma_f32_16x16x32_bf16 a[116:119], v[172:175], v[148:151], a[116:119]\n"
    "v_add_f32 v117, v117, v86\n"
    "v_add_f32 v113, v113, v48\n"
    "v_mfma_f32_16x16x32_bf16 a[112:115], v[172:175], v[144:147], a[112:115]\n"
    "v_exp_f32 v60, v60\n"
    "v_mfma_f32_16x16x32_bf16 a[112:115], v[168:171], v[128:131], a[112:115]\n"
    "s_waitcnt lgkmcnt(2)\n"
    "v_add_u32 v205, s93, v205\n"
    "v_add_f32 v118, v118, v87\n"
    "v_mfma_f32_16x16x32_bf16 a[128:131], v[176:179], v[128:131], a[128:131]\n"
    "ds_read_b128 v[168:171], v201 offset:20480\n"
    "v_add_f32 v114, v114, v49\n"
    "v_mfma_f32_16x16x32_bf16 a[128:131], v[180:183], v[144:147], a[128:131]\n"
    "v_add_u32 v209, 0x180, v209\n"
    "v_add_u32 v206, s93, v206\n"
    "v_mfma_f32_16x16x32_bf16 a[132:135], v[180:183], v[148:151], a[132:135]\n"
    "s_add_u32 s64, s64, 0xc000\n"
    "v_add_f32 v121, v121, v88\n"
    "v_add_f32 v113, v113, v50\n"
    "v_mfma_f32_16x16x32_bf16 a[132:135], v[176:179], v[132:135], a[132:135]\n"
    "s_add_u32 s62, s62, 0x6000\n"
    "v_exp_f32 v61, v61\n"
    "v_mfma_f32_16x16x32_bf16 a[136:139], v[176:179], v[136:139], a[136:139]\n"
    "s_add_u32 s63, s63, 0x6000\n"
    "ds_read_b128 v[172:175], v201 offset:21504\n"
    "v_add_u32 v210, 32, v210\n"
    "v_mfma_f32_16x16x32_bf16 a[136:139], v[180:183], v[152:155], a[136:139]\n"
    "s_add_u32 s60, s60, 0x6000\n"
    "v_add_u32 v207, s93, v207\n"
    "v_add_f32 v122, v122, v89\n"
    "v_mfma_f32_16x16x32_bf16 a[140:143], v[180:183], v[156:159], a[140:143]\n"
    "s_add_u32 s61, s61, 0x6000\n"
    "v_add_f32 v114, v114, v51\n"
    "v_add_f32 v121, v121, v90\n"
    "v_mfma_f32_16x16x32_bf16 a[140:143], v[176:179], v[140:143], a[140:143]\n"
    "s_waitcnt lgkmcnt(2)\n"
    "v_add_f32 v117, v117, v52\n"
    "v_add_f32 v122, v122, v91\n"
    "v_mfma_f32_16x16x32_bf16 a[156:159], v[160:163], v[140:143], a[156:159]\n"
    "ds_read_b128 v[176:179], v201 offset:22528\n"
    "v_add_f32 v118, v118, v53\n"
    "v_mfma_f32_16x16x32_bf16 a[156:159], v[164:167], v[156:159], a[156:159]\n"
    "s_waitcnt vmcnt(3)\n"
    "ds_read_b128 v[180:183], v201 offset:23552\n"
    "v_add_f32 v125, v125, v92\n"
    "v_mfma_f32_16x16x32_bf16 a[152:155], v[164:167], v[152:155], a[152:155]\n"
    "v_exp_f32 v62, v62\n"
    "v_mfma_f32_16x16x32_bf16 a[152:155], v[160:163], v[136:139], a[152:155]\n"
    "v_add_f32 v117, v117, v54\n"
    "v_add_f32 v126, v126, v93\n"
    "v_mfma_f32_16x16x32_bf16 a[148:151], v[160:163], v[132:135], a[148:151]\n"
    "v_exp_f32 v63, v63\n"
    "v_mfma_f32_16x16x32_bf16 a[148:151], v[164:167], v[148:151], a[148:151]\n"
    "v_add_f32 v118, v118, v55\n"
    "v_add_f32 v125, v125, v94\n"
    "v_mfma_f32_16x16x32_bf16 a[144:147], v[164:167], v[144:147], a[144:147]\n"
    "v_add_f32 v121, v121, v56\n"
    "v_add_f32 v126, v126, v95\n"
    "v_add_f32 v122, v122, v57\n"
    "v_mfma_f32_16x16x32_bf16 a[144:147], v[160:163], v[128:131], a[144:147]\n"
    "s_waitcnt lgkmcnt(0)\n"
    "v_mfma_f32_16x16x32_bf16 a[160:163], v[168:171], v[128:131], a[160:163]\n"
    "s_barrier\n"
    "v_mfma_f32_16x16x32_bf16 a[160:163], v[172:175], v[144:147], a[160:163]\n"
    "ds_read_u16_d16_hi v223, v209 offset:6144\n"
    "ds_read_u16_d16_hi v222, v209\n"
    "v_mfma_f32_16x16x32_bf16 a[164:167], v[172:175], v[148:151], a[164:167]\n"
    "ds_read_b128 v[160:163], v200\n"
    "v_add_f32 v121, v121, v58\n"
    "v_mfma_f32_16x16x32_bf16 a[164:167], v[168:171], v[132:135], a[164:167]\n"
    "ds_read_b128 v[232:235], v205 offset:24576\n"
    "v_add_f32 v122, v122, v59\n"
    "v_mfma_f32_16x16x32_bf16 a[168:171], v[168:171], v[136:139], a[168:171]\n"
    "ds_read_b128 v[164:167], v200 offset:1024\n"
    "ds_read_b128 v[236:239], v206 offset:24576\n"
    "v_mfma_f32_16x16x32_bf16 a[168:171], v[172:175], v[152:155], a[168:171]\n"
    "v_add_f32 v125, v125, v60\n"
    "v_add_f32 v126, v126, v61\n"
    "v_mfma_f32_16x16x32_bf16 a[172:175], v[172:175], v[156:159], a[172:175]\n"
    "v_add_f32 v125, v125, v62\n"
    "v_add_f32 v126, v126, v63\n"
    "v_mfma_f32_16x16x32_bf16 a[172:175], v[168:171], v[140:143], a[172:175]\n"
    "ds_read_u16_d16_hi v225, v210 offset:512\n"
    "ds_read_u16_d16_hi v224, v210\n"
    "v_mfma_f32_16x16x32_bf16 a[188:191], v[176:179], v[140:143], a[188:191]\n"
    "ds_read_b128 v[168:171], v200 offset:2048\n"
    "ds_read2st64_b32 v[240:241], v207 offset0:96 offset1:99\n"
    "v_mfma_f32_16x16x32_bf16 a[188:191], v[180:183], v[156:159], a[188:191]\n"
    "ds_read2st64_b32 v[242:243], v207 offset0:102 offset1:105\n"
    "ds_read2st64_b32 v[244:245], v207 offset0:108 offset1:111\n"
    "v_mfma_f32_16x16x32_bf16 a[184:187], v[176:179], v[136:139], a[184:187]\n"
    "ds_read2st64_b32 v[246:247], v207 offset0:114 offset1:117\n"
    "v_cvt_pk_bf16_f32 v140, v44, v45\n"
    "v_mfma_f32_16x16x32_bf16 a[184:187], v[180:183], v[152:155], a[184:187]\n"
    "s_waitcnt lgkmcnt(7)\n"
    "v_mul_f32 v226, s67, v223\n"
    "v_mfma_f32_16x16x32_bf16 a[180:183], v[176:179], v[132:135], a[180:183]\n"
    "ds_read_b128 v[172:175], v200 offset:3072\n"
    "v_fma_f32 v227, -v222, v226, v217\n"
    "v_mfma_f32_16x16x32_bf16 a[180:183], v[180:183], v[148:151], a[180:183]\n"
    "v_cvt_f32_i32 v232, v232\n"
    "v_cvt_f32_i32 v233, v233\n"
    "v_mfma_f32_16x16x32_bf16 a[176:179], v[176:179], v[128:131], a[176:179]\n"
    "v_cvt_f32_i32 v234, v234\n"
    "v_cvt_f32_i32 v235, v235\n"
    "v_mfma_f32_16x16x32_bf16 a[176:179], v[180:183], v[144:147], a[176:179]\n"
    "v_cvt_f32_i32 v236, v236\n"
    "v_cvt_f32_i32 v237, v237\n"
    "LM01:\n"
    "v_mfma_f32_16x16x32_bf16 v[64:67], v[160:163], a[192:195], v[96:99]\n"
    "ds_read_b128 v[176:179], v200 offset:4096\n"
    "v_cvt_f32_i32 v238, v238\n"
    "v_mfma_f32_16x16x32_bf16 v[64:67], v[164:167], a[196:199], v[64:67]\n"
    "v_cvt_f32_i32 v239, v239\n"
    "v_mul_f32_dpp v232, v226, v232 row_newbcast:0 row_mask:0xf bank_mask:0xf\n"
    "v_mfma_f32_16x16x32_bf16 v[68:71], v[164:167], a[220:223], v[100:103]\n"
    "v_mul_f32_dpp v233, v226, v233 row_newbcast:1 row_mask:0xf bank_mask:0xf\n"
    "v_mul_f32_dpp v234, v226, v234 row_newbcast:2 row_mask:0xf bank_mask:0xf\n"
    "v_mfma_f32_16x16x32_bf16 v[68:71], v[160:163], a[216:219], v[68:71]\n"
    "v_mul_f32_dpp v235, v226, v235 row_newbcast:3 row_mask:0xf bank_mask:0xf\n"
    "v_mul_f32_dpp v236, v226, v236 row_newbcast:4 row_mask:0xf bank_mask:0xf\n"
    "v_mfma_f32_16x16x32_bf16 v[72:75], v[160:163], a[240:243], v[104:107]\n"
    "ds_read_b128 v[180:183], v200 offset:5120\n"
    "v_mul_f32_dpp v237, v226, v237 row_newbcast:5 row_mask:0xf bank_mask:0xf\n"
    "v_mfma_f32_16x16x32_bf16 v[72:75], v[164:167], a[244:247], v[72:75]\n"
    "v_mul_f32_dpp v238, v226, v238 row_newbcast:6 row_mask:0xf bank_mask:0xf\n"
    "v_mul_f32_dpp v239, v226, v239 row_newbcast:7 row_mask:0xf bank_mask:0xf\n"
    "v_mfma_f32_16x16x32_bf16 v[76:79], v[164:167], v[12:15], v[108:111]\n"
    "v_add_f32_dpp v232, v227, v232 row_newbcast:0 row_mask:0xf bank_mask:0xf\n"
    "v_add_f32_dpp v233, v227, v233 row_newbcast:1 row_mask:0xf bank_mask:0xf\n"
    "v_mfma_f32_16x16x32_bf16 v[76:79], v[160:163], v[8:11], v[76:79]\n"
    "s_waitcnt lgkmcnt(2)\n"
    "v_add_f32_dpp v234, v227, v234 row_newbcast:2 row_mask:0xf bank_mask:0xf\n"
    "v_add_f32_dpp v235, v227, v235 row_newbcast:3 row_mask:0xf bank_mask:0xf\n"
    "v_mfma_f32_16x16x32_bf16 v[76:79], v[168:171], v[16:19], v[76:79]\n"
    "ds_read_b128 v[160:163], v200 offset:10240\n"
    "v_add_f32_dpp v236, v227, v236 row_newbcast:4 row_mask:0xf bank_mask:0xf\n"
    "v_mfma_f32_16x16x32_bf16 v[76:79], v[172:175], v[20:23], v[76:79]\n"
    "v_add_f32_dpp v237, v227, v237 row_newbcast:5 row_mask:0xf bank_mask:0xf\n"
    "v_add_f32_dpp v238, v227, v238 row_newbcast:6 row_mask:0xf bank_mask:0xf\n"
    "v_mfma_f32_16x16x32_bf16 v[72:75], v[172:175], a[252:255], v[72:75]\n"
    "v_add_f32_dpp v239, v227, v239 row_newbcast:7 row_mask:0xf bank_mask:0xf\n"
    "v_cvt_pk_bf16_f32 v232, v232, v233\n"
    "v_mfma_f32_16x16x32_bf16 v[72:75], v[168:171], a[248:251], v[72:75]\n"
    "v_cvt_pk_bf16_f32 v233, v234, v235\n"
    "v_cvt_pk_bf16_f32 v234, v236, v237\n"
    "v_mfma_f32_16x16x32_bf16 v[68:71], v[168:171], a[224:227], v[68:71]\n"
    "ds_read_b128 v[164:167], v200 offset:11264\n"
    "v_cvt_pk_bf16_f32 v235, v238, v239\n"
    "v_mfma_f32_16x16x32_bf16 v[68:71], v[172:175], a[228:231], v[68:71]\n"
    "buffer_store_dwordx4 v[232:235], v211, s[36:39], s62 offen sc1\n"
    "v_mul_f32 v228, s68, v225\n"
    "v_fma_f32 v229, -v224, v228, s69\n"
    "v_cvt_f32_i32 v240, v240\n"
    "v_mfma_f32_16x16x32_bf16 v[64:67], v[172:175], a[204:207], v[64:67]\n"
    "v_cvt_f32_i32 v241, v241\n"
    "v_cvt_f32_i32 v242, v242\n"
    "v_mfma_f32_16x16x32_bf16 v[64:67], v[168:171], a[200:203], v[64:67]\n"
    "s_waitcnt lgkmcnt(2)\n"
    "v_cvt_f32_i32 v243, v243\n"
    "v_cvt_f32_i32 v244, v244\n"
    "v_mfma_f32_16x16x32_bf16 v[64:67], v[176:179], a[208:211], v[64:67]\n"
    "ds_read_b128 v[168:171], v200 offset:8192\n"
    "v_cvt_f32_i32 v245, v245\n"
    "v_mfma_f32_16x16x32_bf16 v[64:67], v[180:183], a[212:215], v[64:67]\n"
    "v_cvt_f32_i32 v246, v246\n"
    "v_cvt_f32_i32 v247, v247\n"
    "v_mfma_f32_16x16x32_bf16 v[68:71], v[180:183], a[236:239], v[68:71]\n"
    "v_mul_f32_dpp v240, v228, v240 row_newbcast:0 row_mask:0xf bank_mask:0xf\n"
    "v_mul_f32_dpp v241, v228, v241 row_newbcast:1 row_mask:0xf bank_mask:0xf\n"
    "v_mfma_f32_16x16x32_bf16 v[68:71], v[176:179], a[232:235], v[68:71]\n"
    "v_mul_f32_dpp v242, v228, v242 row_newbcast:2 row_mask:0xf bank_mask:0xf\n"
    "v_mul_f32_dpp v243, v228, v243 row_newbcast:3 row_mask:0xf bank_mask:0xf\n"
    "v_mfma_f32_16x16x32_bf16 v[72:75], v[176:179], v[0:3], v[72:75]\n"
    "ds_read_b128 v[172:175], v200 offset:9216\n"
    "v_mul_f32_dpp v244, v228, v244 row_newbcast:4 row_mask:0xf bank_mask:0xf\n"
    "v_mfma_f32_16x16x32_bf16 v[72:75], v[180:183], v[4:7], v[72:75]\n"
    "v_mul_f32_dpp v245, v228, v245 row_newbcast:5 row_mask:0xf bank_mask:0xf\n"
    "v_mul_f32_dpp v246, v228, v246 row_newbcast:6 row_mask:0xf bank_mask:0xf\n"
    "v_mfma_f32_16x16x32_bf16 v[76:79], v[180:183], v[28:31], v[76:79]\n"
    "v_mul_f32_dpp v247, v228, v247 row_newbcast:7 row_mask:0xf bank_mask:0xf\n"
    "v_add_f32_dpp v240, v229, v240 row_newbcast:0 row_mask:0xf bank_mask:0xf\n"
    "v_mfma_f32_16x16x32_bf16 v[76:79], v[176:179], v[24:27], v[76:79]\n"
    "s_waitcnt lgkmcnt(2)\n"
    "v_add_f32_dpp v241, v229, v241 row_newbcast:1 row_mask:0xf bank_mask:0xf\n"
    "v_add_f32_dpp v242, v229, v242 row_newbcast:2 row_mask:0xf bank_mask:0xf\n"
    "v_mfma_f32_16x16x32_bf16 v[92:95], v[160:163], v[24:27], v[108:111]\n"
    "ds_read_b128 v[176:179], v200 offset:6144\n"
    "v_add_f32_dpp v243, v229, v243 row_newbcast:3 row_mask:0xf bank_mask:0xf\n"
    "v_mfma_f32_16x16x32_bf16 v[92:95], v[164:167], v[28:31], v[92:95]\n"
    "v_add_f32_dpp v244, v229, v244 row_newbcast:4 row_mask:0xf bank_mask:0xf\n"
    "v_add_f32_dpp v245, v229, v245 row_newbcast:5 row_mask:0xf bank_mask:0xf\n"
    "v_mfma_f32_16x16x32_bf16 v[88:91], v[164:167], v[4:7], v[104:107]\n"
    "v_add_f32_dpp v246, v229, v246 row_newbcast:6 row_mask:0xf bank_mask:0xf\n"
    "v_add_f32_dpp v247, v229, v247 row_newbcast:7 row_mask:0xf bank_mask:0xf\n"
    "v_mfma_f32_16x16x32_bf16 v[88:91], v[160:163], v[0:3], v[88:91]\n"
    "v_cvt_pk_bf16_f32 v141, v46, v47\n"
    "v_cvt_pk_bf16_f32 v240, v240, v241\n"
    "v_mfma_f32_16x16x32_bf16 v[84:87], v[160:163], a[232:235], v[100:103]\n"
    "ds_read_b128 v[180:183], v200 offset:7168\n"
    "v_cvt_pk_bf16_f32 v136, v40, v41\n"
    "v_mfma_f32_16x16x32_bf16 v[84:87], v[164:167], a[236:239], v[84:87]\n"
    "v_cvt_pk_bf16_f32 v241, v242, v243\n"
    "v_cvt_pk_bf16_f32 v137, v42, v43\n"
    "v_mfma_f32_16x16x32_bf16 v[80:83], v[164:167], a[212:215], v[96:99]\n"
    "v_cvt_pk_bf16_f32 v242, v244, v245\n"
    "v_cvt_pk_bf16_f32 v132, v36, v37\n"
    "v_mfma_f32_16x16x32_bf16 v[80:83], v[160:163], a[208:211], v[80:83]\n"
    "s_waitcnt lgkmcnt(2)\n"
    "v_cvt_pk_bf16_f32 v243, v246, v247\n"
    "v_cvt_pk_bf16_f32 v133, v38, v39\n"
    "v_mfma_f32_16x16x32_bf16 v[80:83], v[168:171], a[200:203], v[80:83]\n"
    "ds_read_b128 v[160:163], v200 offset:12288\n"
    "v_cvt_pk_bf16_f32 v128, v32, v33\n"
    "v_mfma_f32_16x16x32_bf16 v[80:83], v[172:175], a[204:207], v[80:83]\n"
    "v_cvt_pk_bf16_f32 v129, v34, v35\n"
    "v_cvt_pk_bf16_f32 v142, v60, v61\n"
    "v_mfma_f32_16x16x32_bf16 v[84:87], v[172:175], a[228:231], v[84:87]\n"
    "buffer_store_dwordx4 v[240:243], v212, s[40:43], s63 offen sc1\n"
    "v_exp_f32 v64, v64\n"
    "v_cvt_pk_bf16_f32 v143, v62, v63\n"
    "v_mfma_f32_16x16x32_bf16 v[84:87], v[168:171], a[224:227], v[84:87]\n"
    "s_add_u32 s65, s60, s92\n"
    "v_exp_f32 v65, v65\n"
    "v_mfma_f32_16x16x32_bf16 v[88:91], v[168:171], a[248:251], v[88:91]\n"
    "s_add_u32 m0, s90, 0x6000\n"
    "ds_read_b128 v[164:167], v200 offset:13312\n"
    "v_cvt_pk_bf16_f32 v138, v56, v57\n"
    "v_mfma_f32_16x16x32_bf16 v[88:91], v[172:175], a[252:255], v[88:91]\n"
    "v_exp_f32 v66, v66\n"
    "v_mfma_f32_16x16x32_bf16 v[92:95], v[172:175], v[20:23], v[92:95]\n"
    "buffer_load_dwordx4 v200, s[36:39], s65 offen sc1 lds\n"
    "v_mfma_f32_16x16x32_bf16 v[92:95], v[168:171], v[16:19], v[92:95]\n"
    "s_waitcnt lgkmcnt(2)\n"
    "v_exp_f32 v67, v67\n"
    "v_mfma_f32_16x16x32_bf16 v[92:95], v[176:179], v[8:11], v[92:95]\n"
    "ds_read_b128 v[168:171], v200 offset:14336\n"
    "v_cvt_pk_bf16_f32 v139, v58, v59\n"
    "v_mfma_f32_16x16x32_bf16 v[92:95], v[180:183], v[12:15], v[92:95]\n"
    "v_exp_f32 v68, v68\n"
    "v_mfma_f32_16x16x32_bf16 v[88:91], v[180:183], a[244:247], v[88:91]\n"
    "buffer_load_dwordx4 v200, s[36:39], s65 offen offset:1024 sc1 lds\n"
    "v_mfma_f32_16x16x32_bf16 v[88:91], v[176:179], a[240:243], v[88:91]\n"
    "v_exp_f32 v69, v69\n"
    "v_mfma_f32_16x16x32_bf16 v[84:87], v[176:179], a[216:219], v[84:87]\n"
    "ds_read_b128 v[172:175], v200 offset:15360\n"
    "v_cvt_pk_bf16_f32 v134, v52, v53\n"
    "v_mfma_f32_16x16x32_bf16 v[84:87], v[180:183], a[220:223], v[84:87]\n"
    "v_exp_f32 v70, v70\n"
    "v_mfma_f32_16x16x32_bf16 v[80:83], v[180:183], a[196:199], v[80:83]\n"
    "buffer_load_dwordx4 v200, s[36:39], s65 offen offset:2048 sc1 lds\n"
    "v_mfma_f32_16x16x32_bf16 v[80:83], v[176:179], a[192:195], v[80:83]\n"
    "s_waitcnt lgkmcnt(2)\n"
    "v_exp_f32 v71, v71\n"
    "v_mfma_f32_16x16x32_bf16 v[32:35], v[160:163], a[192:195], v[96:99]\n"
    "ds_read_b128 v[176:179], v200 offset:16384\n"
    "v_cvt_pk_bf16_f32 v135, v54, v55\n"
    "v_mfma_f32_16x16x32_bf16 v[32:35], v[164:167], a[196:199], v[32:35]\n"
    "v_exp_f32 v72, v72\n"
    "v_mfma_f32_16x16x32_bf16 v[36:39], v[164:167], a[220:223], v[100:103]\n"
    "buffer_load_dwordx4 v200, s[36:39], s65 offen offset:3072 sc1 lds\n"
    "v_mfma_f32_16x16x32_bf16 v[36:39], v[160:163], a[216:219], v[36:39]\n"
    "s_add_u32 s65, s65, 0x1000\n"
    "v_exp_f32 v73, v73\n"
    "v_mfma_f32_16x16x32_bf16 v[40:43], v[160:163], a[240:243], v[104:107]\n"
    "s_add_u32 m0, s90, 0x7000\n"
    "ds_read_b128 v[180:183], v200 offset:17408\n"
    "v_cvt_pk_bf16_f32 v130, v48, v49\n"
    "v_mfma_f32_16x16x32_bf16 v[40:43], v[164:167], a[244:247], v[40:43]\n"
    "v_exp_f32 v74, v74\n"
    "v_mfma_f32_16x16x32_bf16 v[44:47], v[164:167], v[12:15], v[108:111]\n"
    "buffer_load_dwordx4 v200, s[36:39], s65 offen sc1 lds\n"
    "v_mfma_f32_16x16x32_bf16 v[44:47], v[160:163], v[8:11], v[44:47]\n"
    "s_waitcnt lgkmcnt(2)\n"
    "v_exp_f32 v75, v75\n"
    "v_mfma_f32_16x16x32_bf16 v[44:47], v[168:171], v[16:19], v[44:47]\n"
    "ds_read_b128 v[160:163], v200 offset:22528\n"
    "v_cvt_pk_bf16_f32 v131, v50, v51\n"
    "v_mfma_f32_16x16x32_bf16 v[44:47], v[172:175], v[20:23], v[44:47]\n"
    "v_exp_f32 v76, v76\n"
    "v_mfma_f32_16x16x32_bf16 v[40:43], v[172:175], a[252:255], v[40:43]\n"
    "buffer_load_dwordx4 v200, s[36:39], s65 offen offset:1024 sc1 lds\n"
    "v_mfma_f32_16x16x32_bf16 v[40:43], v[168:171], a[248:251], v[40:43]\n"
    "v_exp_f32 v77, v77\n"
    "v_mfma_f32_16x16x32_bf16 v[36:39], v[168:171], a[224:227], v[36:39]\n"
    "ds_read_b128 v[164:167], v200 offset:23552\n"
    "v_cvt_pk_bf16_f32 v144, v64, v65\n"
    "v_mfma_f32_16x16x32_bf16 v[36:39], v[172:175], a[228:231], v[36:39]\n"
    "v_exp_f32 v78, v78\n"
    "v_mfma_f32_16x16x32_bf16 v[32:35], v[172:175], a[204:207], v[32:35]\n"
    "buffer_load_dwordx4 v200, s[36:39], s65 offen offset:2048 sc1 lds\n"
    "v_mfma_f32_16x16x32_bf16 v[32:35], v[168:171], a[200:203], v[32:35]\n"
    "s_waitcnt lgkmcnt(2)\n"
    "v_exp_f32 v79, v79\n"
    "v_mfma_f32_16x16x32_bf16 v[32:35], v[176:179], a[208:211], v[32:35]\n"
    "s_add_u32 m0, s71, 0xc000\n"
    "ds_read_b128 v[168:171], v200 offset:20480\n"
    "v_cvt_pk_bf16_f32 v145, v66, v67\n"
    "v_mfma_f32_16x16x32_bf16 v[32:35], v[180:183], a[212:215], v[32:35]\n"
    "v_exp_f32 v80, v80\n"
    "v_mfma_f32_16x16x32_bf16 v[36:39], v[180:183], a[236:239], v[36:39]\n"
    "buffer_load_dwordx4 v200, s[40:43], s61 offen sc1 lds\n"
    "v_mfma_f32_16x16x32_bf16 v[36:39], v[176:179], a[232:235], v[36:39]\n"
    "v_exp_f32 v81, v81\n"
    "v_mfma_f32_16x16x32_bf16 v[40:43], v[176:179], v[0:3], v[40:43]\n"
    "ds_read_b128 v[172:175], v200 offset:21504\n"
    "v_cvt_pk_bf16_f32 v148, v68, v69\n"
    "v_mfma_f32_16x16x32_bf16 v[40:43], v[180:183], v[4:7], v[40:43]\n"
    "v_exp_f32 v82, v82\n"
    "v_mfma_f32_16x16x32_bf16 v[44:47], v[180:183], v[28:31], v[44:47]\n"
    "buffer_load_dwordx4 v200, s[40:43], s61 offen offset:1024 sc1 lds\n"
    "v_mfma_f32_16x16x32_bf16 v[44:47], v[176:179], v[24:27], v[44:47]\n"
    "s_waitcnt lgkmcnt(2)\n"
    "v_exp_f32 v83, v83\n"
    "v_mfma_f32_16x16x32_bf16 v[60:63], v[160:163], v[24:27], v[108:111]\n"
    "ds_read_b128 v[176:179], v200 offset:18432\n"
    "v_cvt_pk_bf16_f32 v149, v70, v71\n"
    "v_mfma_f32_16x16x32_bf16 v[60:63], v[164:167], v[28:31], v[60:63]\n"
    "v_exp_f32 v84, v84\n"
    "v_mfma_f32_16x16x32_bf16 v[56:59], v[164:167], v[4:7], v[104:107]\n"
    "buffer_load_dwordx4 v200, s[40:43], s61 offen offset:2048 sc1 lds\n"
    "v_mfma_f32_16x16x32_bf16 v[56:59], v[160:163], v[0:3], v[56:59]\n"
    "v_exp_f32 v85, v85\n"
    "v_mfma_f32_16x16x32_bf16 v[52:55], v[160:163], a[232:235], v[100:103]\n"
    "ds_read_b128 v[180:183], v200 offset:19456\n"
    "v_cvt_pk_bf16_f32 v152, v72, v73\n"
    "v_mfma_f32_16x16x32_bf16 v[52:55], v[164:167], a[236:239], v[52:55]\n"
    "v_exp_f32 v86, v86\n"
    "v_mfma_f32_16x16x32_bf16 v[48:51], v[164:167], a[212:215], v[96:99]\n"
    "buffer_load_dwordx4 v200, s[40:43], s61 offen offset:3072 sc1 lds\n"
    "v_mfma_f32_16x16x32_bf16 v[48:51], v[160:163], a[208:211], v[48:51]\n"
    "s_waitcnt lgkmcnt(2)\n"
    "s_add_u32 s65, s61, 0x1000\n"
    "v_cvt_pk_bf16_f32 v153, v74, v75\n"
    "v_mfma_f32_16x16x32_bf16 v[48:51], v[168:171], a[200:203], v[48:51]\n"
    "s_add_u32 m0, s71, 0xd000\n"
    "ds_read_b128 v[160:163], v201 offset:24576\n"
    "v_cvt_pk_bf16_f32 v156, v76, v77\n"
    "v_mfma_f32_16x16x32_bf16 v[48:51], v[172:175], a[204:207], v[48:51]\n"
    "v_exp_f32 v87, v87\n"
    "v_mfma_f32_16x16x32_bf16 v[52:55], v[172:175], a[228:231], v[52:55]\n"
    "buffer_load_dwordx4 v200, s[40:43], s65 offen sc1 lds\n"
    "v_mfma_f32_16x16x32_bf16 v[52:55], v[168:171], a[224:227], v[52:55]\n"
    "v_exp_f32 v88, v88\n"
    "v_mfma_f32_16x16x32_bf16 v[56:59], v[168:171], a[248:251], v[56:59]\n"
    "ds_read_b128 v[164:167], v201 offset:25600\n"
    "v_cvt_pk_bf16_f32 v157, v78, v79\n"
    "v_mfma_f32_16x16x32_bf16 v[56:59], v[172:175], a[252:255], v[56:59]\n"
    "v_exp_f32 v89, v89\n"
    "v_mfma_f32_16x16x32_bf16 v[60:63], v[172:175], v[20:23], v[60:63]\n"
    "buffer_load_dwordx4 v200, s[40:43], s65 offen offset:1024 sc1 lds\n"
    "v_mfma_f32_16x16x32_bf16 v[60:63], v[168:171], v[16:19], v[60:63]\n"
    "s_waitcnt lgkmcnt(2)\n"
    "v_exp_f32 v90, v90\n"
    "v_mfma_f32_16x16x32_bf16 v[60:63], v[176:179], v[8:11], v[60:63]\n"
    "ds_read_b128 v[168:171], v201 offset:26624\n"
    "v_cvt_pk_bf16_f32 v146, v80, v81\n"
    "v_mfma_f32_16x16x32_bf16 v[60:63], v[180:183], v[12:15], v[60:63]\n"
    "v_exp_f32 v91, v91\n"
    "v_mfma_f32_16x16x32_bf16 v[56:59], v[180:183], a[244:247], v[56:59]\n"
    "v_cvt_pk_bf16_f32 v147, v82, v83\n"
    "v_cvt_pk_bf16_f32 v150, v84, v85\n"
    "v_mfma_f32_16x16x32_bf16 v[56:59], v[176:179], a[240:243], v[56:59]\n"
    "v_exp_f32 v92, v92\n"
    "v_mfma_f32_16x16x32_bf16 v[52:55], v[176:179], a[216:219], v[52:55]\n"
    "ds_read_b128 v[172:175], v201 offset:27648\n"
    "v_cvt_pk_bf16_f32 v151, v86, v87\n"
    "v_mfma_f32_16x16x32_bf16 v[52:55], v[180:183], a[220:223], v[52:55]\n"
    "v_exp_f32 v93, v93\n"
    "v_mfma_f32_16x16x32_bf16 v[48:51], v[180:183], a[196:199], v[48:51]\n"
    "v_exp_f32 v94, v94\n"
    "v_mfma_f32_16x16x32_bf16 v[48:51], v[176:179], a[192:195], v[48:51]\n"
    "s_waitcnt lgkmcnt(2)\n"
    "v_cvt_pk_bf16_f32 v154, v88, v89\n"
    "v_cvt_pk_bf16_f32 v155, v90, v91\n"
    "v_mfma_f32_16x16x32_bf16 a[0:3], v[160:163], v[128:131], a[0:3]\n"
    "ds_read_b128 v[176:179], v201 offset:28672\n"
    "v_cvt_pk_bf16_f32 v158, v92, v93\n"
    "v_mfma_f32_16x16x32_bf16 a[0:3], v[164:167], v[144:147], a[0:3]\n"
    "v_exp_f32 v95, v95\n"
    "v_mfma_f32_16x16x32_bf16 a[4:7], v[160:163], v[132:135], a[4:7]\n"
    "v_cvt_pk_bf16_f32 v159, v94, v95\n"
    "v_add_f32 v113, v113, v64\n"
    "v_mfma_f32_16x16x32_bf16 a[4:7], v[164:167], v[148:151], a[4:7]\n"
    "v_exp_f32 v32, v32\n"
    "v_mfma_f32_16x16x32_bf16 a[8:11], v[160:163], v[136:139], a[8:11]\n"
    "ds_read_b128 v[180:183], v201 offset:29696\n"
    "v_add_f32 v114, v114, v65\n"
    "v_mfma_f32_16x16x32_bf16 a[8:11], v[164:167], v[152:155], a[8:11]\n"
    "v_exp_f32 v33, v33\n"
    "v_mfma_f32_16x16x32_bf16 a[12:15], v[160:163], v[140:143], a[12:15]\n"
    "v_exp_f32 v34, v34\n"
    "v_mfma_f32_16x16x32_bf16 a[12:15], v[164:167], v[156:159], a[12:15]\n"
    "s_waitcnt lgkmcnt(2)\n"
    "v_exp_f32 v35, v35\n"
    "v_mfma_f32_16x16x32_bf16 a[28:31], v[168:171], v[140:143], a[28:31]\n"
    "ds_read_b128 v[160:163], v201 offset:30720\n"
    "v_add_f32 v113, v113, v66\n"
    "v_mfma_f32_16x16x32_bf16 a[28:31], v[172:175], v[156:159], a[28:31]\n"
    "v_exp_f32 v36, v36\n"
    "v_mfma_f32_16x16x32_bf16 a[24:27], v[172:175], v[152:155], a[24:27]\n"
    "v_exp_f32 v37, v37\n"
    "v_mfma_f32_16x16x32_bf16 a[24:27], v[168:171], v[136:139], a[24:27]\n"
    "v_exp_f32 v38, v38\n"
    "v_mfma_f32_16x16x32_bf16 a[20:23], v[168:171], v[132:135], a[20:23]\n"
    "ds_read_b128 v[164:167], v201 offset:31744\n"
    "v_add_f32 v114, v114, v67\n"
    "v_mfma_f32_16x16x32_bf16 a[20:23], v[172:175], v[148:151], a[20:23]\n"
    "v_exp_f32 v39, v39\n"
    "v_mfma_f32_16x16x32_bf16 a[16:19], v[172:175], v[144:147], a[16:19]\n"
    "v_exp_f32 v40, v40\n"
    "v_mfma_f32_16x16x32_bf16 a[16:19], v[168:171], v[128:131], a[16:19]\n"
    "s_waitcnt lgkmcnt(2)\n"
    "v_exp_f32 v41, v41\n"
    "v_mfma_f32_16x16x32_bf16 a[32:35], v[176:179], v[128:131], a[32:35]\n"
    "ds_read_b128 v[168:171], v201 offset:32768\n"
    "v_add_f32 v117, v117, v68\n"
    "v_mfma_f32_16x16x32_bf16 a[32:35], v[180:183], v[144:147], a[32:35]\n"
    "v_exp_f32 v42, v42\n"
    "v_mfma_f32_16x16x32_bf16 a[36:39], v[180:183], v[148:151], a[36:39]\n"
    "v_exp_f32 v43, v43\n"
    "v_mfma_f32_16x16x32_bf16 a[36:39], v[176:179], v[132:135], a[36:39]\n"
    "v_exp_f32 v44, v44\n"
    "v_mfma_f32_16x16x32_bf16 a[40:43], v[176:179], v[136:139], a[40:43]\n"
    "ds_read_b128 v[172:175], v201 offset:33792\n"
    "v_add_f32 v118, v118, v69\n"
    "v_mfma_f32_16x16x32_bf16 a[40:43], v[180:183], v[152:155], a[40:43]\n"
    "v_exp_f32 v45, v45\n"
    "v_mfma_f32_16x16x32_bf16 a[44:47], v[180:183], v[156:159], a[44:47]\n"
    "v_exp_f32 v46, v46\n"
    "v_mfma_f32_16x16x32_bf16 a[44:47], v[176:179], v[140:143], a[44:47]\n"
    "s_waitcnt lgkmcnt(2)\n"
    "v_exp_f32 v47, v47\n"
    "v_mfma_f32_16x16x32_bf16 a[60:63], v[160:163], v[140:143], a[60:63]\n"
    "ds_read_b128 v[176:179], v201 offset:34816\n"
    "v_add_f32 v117, v117, v70\n"
    "v_mfma_f32_16x16x32_bf16 a[60:63], v[164:167], v[156:159], a[60:63]\n"
    "v_exp_f32 v48, v48\n"
    "v_mfma_f32_16x16x32_bf16 a[56:59], v[164:167], v[152:155], a[56:59]\n"
    "v_exp_f32 v49, v49\n"
    "v_mfma_f32_16x16x32_bf16 a[56:59], v[160:163], v[136:139], a[56:59]\n"
    "s_add_u32 m0, s51, s95\n"
    "v_exp_f32 v50, v50\n"
    "v_mfma_f32_16x16x32_bf16 a[52:55], v[160:163], v[132:135], a[52:55]\n"
    "ds_read_b128 v[180:183], v201 offset:35840\n"
    "v_add_f32 v113, v113, v32\n"
    "v_mfma_f32_16x16x32_bf16 a[52:55], v[164:167], v[148:151], a[52:55]\n"
    "buffer_load_dwordx4 v202, s[32:35], s64 offen lds\n"
    "v_mfma_f32_16x16x32_bf16 a[48:51], v[164:167], v[144:147], a[48:51]\n"
    "v_exp_f32 v51, v51\n"
    "v_mfma_f32_16x16x32_bf16 a[48:51], v[160:163], v[128:131], a[48:51]\n"
    "s_waitcnt lgkmcnt(2)\n"
    "v_exp_f32 v52, v52\n"
    "v_mfma_f32_16x16x32_bf16 a[64:67], v[168:171], v[128:131], a[64:67]\n"
    "ds_read_b128 v[160:163], v201 offset:36864\n"
    "v_add_f32 v118, v118, v71\n"
    "v_mfma_f32_16x16x32_bf16 a[64:67], v[172:175], v[144:147], a[64:67]\n"
    "v_add_f32 v114, v114, v33\n"
    "v_add_f32 v121, v121, v72\n"
    "v_mfma_f32_16x16x32_bf16 a[68:71], v[172:175], v[148:151], a[68:71]\n"
    "buffer_load_dwordx4 v203, s[32:35], s64 offen offset:1024 lds\n"
    "v_mfma_f32_16x16x32_bf16 a[68:71], v[168:171], v[132:135], a[68:71]\n"
    "v_add_f32 v113, v113, v34\n"
    "v_add_f32 v122, v122, v73\n"
    "v_mfma_f32_16x16x32_bf16 a[72:75], v[168:171], v[136:139], a[72:75]\n"
    "ds_read_b128 v[164:167], v201 offset:37888\n"
    "v_add_f32 v114, v114, v35\n"
    "v_mfma_f32_16x16x32_bf16 a[72:75], v[172:175], v[152:155], a[72:75]\n"
    "v_exp_f32 v53, v53\n"
    "v_mfma_f32_16x16x32_bf16 a[76:79], v[172:175], v[156:159], a[76:79]\n"
    "v_add_f32 v121, v121, v74\n"
    "v_add_f32 v117, v117, v36\n"
    "v_mfma_f32_16x16x32_bf16 a[76:79], v[168:171], v[140:143], a[76:79]\n"
    "s_waitcnt lgkmcnt(2)\n"
    "buffer_load_dwordx4 v204, s[32:35], s64 offen offset:2048 lds\n"
    "v_mfma_f32_16x16x32_bf16 a[92:95], v[176:179], v[140:143], a[92:95]\n"
    "ds_read_b128 v[168:171], v201 offset:38912\n"
    "v_add_f32 v122, v122, v75\n"
    "v_mfma_f32_16x16x32_bf16 a[92:95], v[180:183], v[156:159], a[92:95]\n"
    "v_exp_f32 v54, v54\n"
    "v_mfma_f32_16x16x32_bf16 a[88:91], v[180:183], v[152:155], a[88:91]\n"
    "v_add_f32 v118, v118, v37\n"
    "v_add_f32 v125, v125, v76\n"
    "v_mfma_f32_16x16x32_bf16 a[88:91], v[176:179], v[136:139], a[88:91]\n"
    "v_add_f32 v117, v117, v38\n"
    "v_add_f32 v126, v126, v77\n"
    "v_mfma_f32_16x16x32_bf16 a[84:87], v[176:179], v[132:135], a[84:87]\n"
    "s_mov_b32 s95, s94\n"
    "ds_read_b128 v[172:175], v201 offset:39936\n"
    "v_add_f32 v118, v118, v39\n"
    "v_mfma_f32_16x16x32_bf16 a[84:87], v[180:183], v[148:151], a[84:87]\n"
    "s_sub_u32 s94, s94, 0x3000\n"
    "v_exp_f32 v55, v55\n"
    "v_mfma_f32_16x16x32_bf16 a[80:83], v[180:183], v[144:147], a[80:83]\n"
    "s_min_u32 s94, s94, 0x6000\n"
    "v_add_f32 v125, v125, v78\n"
    "v_add_f32 v121, v121, v40\n"
    "v_mfma_f32_16x16x32_bf16 a[80:83], v[176:179], v[128:131], a[80:83]\n"
    "s_waitcnt lgkmcnt(2)\n"
    "v_exp_f32 v56, v56\n"
    "v_mfma_f32_16x16x32_bf16 a[96:99], v[160:163], v[128:131], a[96:99]\n"
    "s_sub_u32 s93, s94, s95\n"
    "ds_read_b128 v[176:179], v201 offset:40960\n"
    "v_add_f32 v126, v126, v79\n"
    "v_mfma_f32_16x16x32_bf16 a[96:99], v[164:167], v[144:147], a[96:99]\n"
    "v_add_f32 v122, v122, v41\n"
    "v_add_f32 v113, v113, v80\n"
    "v_mfma_f32_16x16x32_bf16 a[100:103], v[164:167], v[148:151], a[100:103]\n"
    "v_add_f32 v121, v121, v42\n"
    "v_add_f32 v114, v114, v81\n"
    "v_mfma_f32_16x16x32_bf16 a[100:103], v[160:163], v[132:135], a[100:103]\n"
    "v_exp_f32 v57, v57\n"
    "v_mfma_f32_16x16x32_bf16 a[104:107], v[160:163], v[136:139], a[104:107]\n"
    "ds_read_b128 v[180:183], v201 offset:41984\n"
    "v_add_f32 v122, v122, v43\n"
    "v_mfma_f32_16x16x32_bf16 a[104:107], v[164:167], v[152:155], a[104:107]\n"
    "v_add_f32 v113, v113, v82\n"
    "v_add_f32 v125, v125, v44\n"
    "v_mfma_f32_16x16x32_bf16 a[108:111], v[164:167], v[156:159], a[108:111]\n"
    "v_exp_f32 v58, v58\n"
    "v_mfma_f32_16x16x32_bf16 a[108:111], v[160:163], v[140:143], a[108:111]\n"
    "s_waitcnt lgkmcnt(2)\n"
    "v_add_f32 v114, v114, v83\n"
    "v_add_f32 v126, v126, v45\n"
    "v_mfma_f32_16x16x32_bf16 a[124:127], v[168:171], v[140:143], a[124:127]\n"
    "ds_read_b128 v[160:163], v201 offset:43008\n"
    "v_add_f32 v117, v117, v84\n"
    "v_mfma_f32_16x16x32_bf16 a[124:127], v[172:175], v[156:159], a[124:127]\n"
    "v_add_f32 v125, v125, v46\n"
    "v_add_f32 v118, v118, v85\n"
    "v_mfma_f32_16x16x32_bf16 a[120:123], v[172:175], v[152:155], a[120:123]\n"
    "v_exp_f32 v59, v59\n"
    "v_mfma_f32_16x16x32_bf16 a[120:123], v[168:171], v[136:139], a[120:123]\n"
    "v_add_f32 v126, v126, v47\n"
    "v_add_f32 v117, v117, v86\n"
    "v_mfma_f32_16x16x32_bf16 a[116:119], v[168:171], v[132:135], a[116:119]\n"
    "ds_read_b128 v[164:167], v201 offset:44032\n"
    "v_add_f32 v113, v113, v48\n"
    "v_mfma_f32_16x16x32_bf16 a[116:119], v[172:175], v[148:151], a[116:119]\n"
    "v_exp_f32 v60, v60\n"
    "v_mfma_f32_16x16x32_bf16 a[112:115], v[172:175], v[144:147], a[112:115]\n"
    "v_add_u32 v205, s93, v205\n"
    "v_add_f32 v118, v118, v87\n"
    "v_mfma_f32_16x16x32_bf16 a[112:115], v[168:171], v[128:131], a[112:115]\n"
    "s_waitcnt lgkmcnt(2)\n"
    "v_add_f32 v114, v114, v49\n"
    "v_add_u32 v206, s93, v206\n"
    "v_mfma_f32_16x16x32_bf16 a[128:131], v[176:179], v[128:131], a[128:131]\n"
    "ds_read_b128 v[168:171], v201 offset:45056\n"
    "v_add_f32 v121, v121, v88\n"
    "v_mfma_f32_16x16x32_bf16 a[128:131], v[180:183], v[144:147], a[128:131]\n"
    "v_add_f32 v113, v113, v50\n"
    "v_add_u32 v207, s93, v207\n"
    "v_mfma_f32_16x16x32_bf16 a[132:135], v[180:183], v[148:151], a[132:135]\n"
    "s_add_u32 s64, s64, 0xc000\n"
    "v_exp_f32 v61, v61\n"
    "v_mfma_f32_16x16x32_bf16 a[132:135], v[176:179], v[132:135], a[132:135]\n"
    "s_add_u32 s62, s62, 0x6000\n"
    "v_add_f32 v122, v122, v89\n"
    "v_add_f32 v114, v114, v51\n"
    "v_mfma_f32_16x16x32_bf16 a[136:139], v[176:179], v[136:139], a[136:139]\n"
    "s_add_u32 s63, s63, 0x6000\n"
    "ds_read_b128 v[172:175], v201 offset:46080\n"
    "v_add_f32 v121, v121, v90\n"
    "v_mfma_f32_16x16x32_bf16 a[136:139], v[180:183], v[152:155], a[136:139]\n"
    "s_add_u32 s60, s60, 0x6000\n"
    "v_add_f32 v117, v117, v52\n"
    "v_add_f32 v122, v122, v91\n"
    "v_mfma_f32_16x16x32_bf16 a[140:143], v[180:183], v[156:159], a[140:143]\n"
    "s_add_u32 s61, s61, 0x6000\n"
    "v_exp_f32 v62, v62\n"
    "v_mfma_f32_16x16x32_bf16 a[140:143], v[176:179], v[140:143], a[140:143]\n"
    "s_waitcnt lgkmcnt(2)\n"
    "v_add_f32 v118, v118, v53\n"
    "v_add_f32 v125, v125, v92\n"
    "v_mfma_f32_16x16x32_bf16 a[156:159], v[160:163], v[140:143], a[156:159]\n"
    "ds_read_b128 v[176:179], v201 offset:47104\n"
    "v_add_f32 v117, v117, v54\n"
    "v_mfma_f32_16x16x32_bf16 a[156:159], v[164:167], v[156:159], a[156:159]\n"
    "s_waitcnt vmcnt(3)\n"
    "ds_read_b128 v[180:183], v201 offset:48128\n"
    "v_add_f32 v126, v126, v93\n"
    "v_mfma_f32_16x16x32_bf16 a[152:155], v[164:167], v[152:155], a[152:155]\n"
    "v_exp_f32 v63, v63\n"
    "v_mfma_f32_16x16x32_bf16 a[152:155], v[160:163], v[136:139], a[152:155]\n"
    "v_add_f32 v118, v118, v55\n"
    "v_add_f32 v125, v125, v94\n"
    "v_mfma_f32_16x16x32_bf16 a[148:151], v[160:163], v[132:135], a[148:151]\n"
    "v_add_f32 v121, v121, v56\n"
    "v_add_f32 v126, v126, v95\n"
    "v_mfma_f32_16x16x32_bf16 a[148:151], v[164:167], v[148:151], a[148:151]\n"
    "v_add_f32 v122, v122, v57\n"
    "v_mfma_f32_16x16x32_bf16 a[144:147], v[164:167], v[144:147], a[144:147]\n"
    "v_mfma_f32_16x16x32_bf16 a[144:147], v[160:163], v[128:131], a[144:147]\n"
    "s_waitcnt lgkmcnt(0)\n"
    "v_mfma_f32_16x16x32_bf16 a[160:163], v[168:171], v[128:131], a[160:163]\n"
    "s_barrier\n"
    "LMID1:\n"
    "v_mfma_f32_16x16x32_bf16 a[160:163], v[172:175], v[144:147], a[160:163]\n"
    "ds_read_u16_d16_hi v223, v209 offset:6336\n"
    "ds_read_u16_d16_hi v222, v209 offset:192\n"
    "v_mfma_f32_16x16x32_bf16 a[164:167], v[172:175], v[148:151], a[164:167]\n"
    "ds_read_b128 v[160:163], v200 offset:24576\n"
    "v_add_f32 v121, v121, v58\n"
    "v_mfma_f32_16x16x32_bf16 a[164:167], v[168:171], v[132:135], a[164:167]\n"
    "ds_read_b128 v[232:235], v205 offset:24576\n"
    "v_add_f32 v122, v122, v59\n"
    "v_mfma_f32_16x16x32_bf16 a[168:171], v[168:171], v[136:139], a[168:171]\n"
    "ds_read_b128 v[164:167], v200 offset:25600\n"
    "ds_read_b128 v[236:239], v206 offset:24576\n"
    "v_mfma_f32_16x16x32_bf16 a[168:171], v[172:175], v[152:155], a[168:171]\n"
    "v_add_f32 v125, v125, v60\n"
    "v_add_f32 v126, v126, v61\n"
    "v_mfma_f32_16x16x32_bf16 a[172:175], v[172:175], v[156:159], a[172:175]\n"
    "v_add_f32 v125, v125, v62\n"
    "v_add_f32 v126, v126, v63\n"
    "v_mfma_f32_16x16x32_bf16 a[172:175], v[168:171], v[140:143], a[172:175]\n"
    "ds_read_u16_d16_hi v225, v210 offset:528\n"
    "ds_read_u16_d16_hi v224, v210 offset:16\n"
    "v_mfma_f32_16x16x32_bf16 a[188:191], v[176:179], v[140:143], a[188:191]\n"
    "ds_read_b128 v[168:171], v200 offset:26624\n"
    "ds_read2st64_b32 v[240:241], v207 offset0:96 offset1:99\n"
    "v_mfma_f32_16x16x32_bf16 a[188:191], v[180:183], v[156:159], a[188:191]\n"
    "ds_read2st64_b32 v[242:243], v207 offset0:102 offset1:105\n"
    "ds_read2st64_b32 v[244:245], v207 offset0:108 offset1:111\n"
    "v_mfma_f32_16x16x32_bf16 a[184:187], v[176:179], v[136:139], a[184:187]\n"
    "ds_read2st64_b32 v[246:247], v207 offset0:114 offset1:117\n"
    "v_cvt_pk_bf16_f32 v140, v44, v45\n"
    "v_mfma_f32_16x16x32_bf16 a[184:187], v[180:183], v[152:155], a[184:187]\n"
    "s_waitcnt lgkmcnt(7)\n"
    "v_mul_f32 v226, s67, v223\n"
    "v_mfma_f32_16x16x32_bf16 a[180:183], v[176:179], v[132:135], a[180:183]\n"
    "s_add_u32 s59, s59, 1\n"
    "ds_read_b128 v[172:175], v200 offset:27648\n"
    "v_fma_f32 v227, -v222, v226, v217\n"
    "v_mfma_f32_16x16x32_bf16 a[180:183], v[180:183], v[148:151], a[180:183]\n"
    "v_cvt_f32_i32 v232, v232\n"
    "v_cvt_f32_i32 v233, v233\n"
    "v_mfma_f32_16x16x32_bf16 a[176:179], v[176:179], v[128:131], a[176:179]\n"
    "s_cmp_lt_u32 s59, 15\n"
    "v_cvt_f32_i32 v234, v234\n"
    "v_cvt_f32_i32 v235, v235\n"
    "v_mfma_f32_16x16x32_bf16 a[176:179], v[180:183], v[144:147], a[176:179]\n"
    "s_cbranch_scc1 LLOOP1\n"
    "s_branch LTAIL\n"
    "LENT0:\n"
    "s_mov_b32 s59, 1\n"
    "s_add_u32 m0, s72, 0x18000\n"
    "s_nop 0\n"
    "buffer_load_dwordx4 v202, s[32:35], s64 offen lds\n"
    "buffer_load_dwordx4 v203, s[32:35], s64 offen offset:1024 lds\n"
    "buffer_load_dwordx4 v204, s[32:35], s64 offen offset:2048 lds\n"
    "s_add_u32 s64, s64, 0xc000\n"
    "s_mov_b32 s94, 0x3000\n"
    "s_mov_b32 s95, 0x6000\n"
    "s_add_u32 s51, s72, 0x18000\n"
    "v_add_u32 v205, 0x3000, v205\n"
    "v_add_u32 v206, 0x3000, v206\n"
    "v_add_u32 v207, 0x3000, v207\n"
    "buffer_store_dwordx2 v[220:221], v214, s[44:47], 0 offen sc1\n"
    "ds_read_b128 v[160:163], v200 offset:24576\n"
    "ds_read_u16_d16_hi v223, v209 offset:6336\n"
    "ds_read_u16_d16_hi v222, v209 offset:192\n"
    "v_add_f32 v121, v121, v58\n"
    "ds_read_b128 v[164:167], v200 offset:25600\n"
    "ds_read_b128 v[232:235], v205 offset:24576\n"
    "v_add_f32 v122, v122, v59\n"
    "ds_read_b128 v[236:239], v206 offset:24576\n"
    "v_add_f32 v125, v125, v60\n"
    "v_add_f32 v126, v126, v61\n"
    "v_add_f32 v125, v125, v62\n"
    "v_add_f32 v126, v126, v63\n"
    "ds_read_b128 v[168:171], v200 offset:26624\n"
    "ds_read_u16_d16_hi v225, v210 offset:528\n"
    "ds_read_u16_d16_hi v224, v210 offset:16\n"
    "ds_read2st64_b32 v[240:241], v207 offset0:96 offset1:99\n"
    "ds_read2st64_b32 v[242:243], v207 offset0:102 offset1:105\n"
    "ds_read2st64_b32 v[244:245], v207 offset0:108 offset1:111\n"
    "ds_read2st64_b32 v[246:247], v207 offset0:114 offset1:117\n"
    "v_cvt_pk_bf16_f32 v140, v44, v45\n"
    "ds_read_b128 v[172:175], v200 offset:27648\n"
    "s_waitcnt lgkmcnt(8)\n"
    "v_mul_f32 v226, s67, v223\n"
    "v_fma_f32 v227, -v222, v226, v217\n"
    "v_cvt_f32_i32 v232, v232\n"
    "v_cvt_f32_i32 v233, v233\n"
    "s_nop 7\n"
    ".p2align 6\n"
    "LLOOP:\n"
    "v_mfma_f32_16x16x32_bf16 v[64:67], v[160:163], a[192:195], v[96:99]\n"
    "ds_read_b128 v[176:179], v200 offset:28672\n"
    "v_cvt_f32_i32 v234, v234\n"
    "v_mfma_f32_16x16x32_bf16 v[64:67], v[164:167], a[196:199], v[64:67]\n"
    "v_cvt_f32_i32 v235, v235\n"
    "v_cvt_f32_i32 v236, v236\n"
    "v_mfma_f32_16x16x32_bf16 v[68:71], v[164:167], a[220:223], v[100:103]\n"
    "v_cvt_f32_i32 v237, v237\n"
    "v_cvt_f32_i32 v238, v238\n"
    "v_mfma_f32_16x16x32_bf16 v[68:71], v[160:163], a[216:219], v[68:71]\n"
    "v_cvt_f32_i32 v239, v239\n"
    "v_mul_f32_dpp v232, v226, v232 row_newbcast:0 row_mask:0xf bank_mask:0xf\n"
    "v_mfma_f32_16x16x32_bf16 v[72:75], v[160:163], a[240:243], v[104:107]\n"
    "ds_read_b128 v[180:183], v200 offset:29696\n"
    "v_mul_f32_dpp v233, v226, v233 row_newbcast:1 row_mask:0xf bank_mask:0xf\n"
    "v_mfma_f32_16x16x32_bf16 v[72:75], v[164:167], a[244:247], v[72:75]\n"
    "v_mul_f32_dpp v234, v226, v234 row_newbcast:2 row_mask:0xf bank_mask:0xf\n"
    "v_mul_f32_dpp v235, v226, v235 row_newbcast:3 row_mask:0xf bank_mask:0xf\n"
    "v_mfma_f32_16x16x32_bf16 v[76:79], v[164:167], v[12:15], v[108:111]\n"
    "v_mul_f32_dpp v236, v226, v236 row_newbcast:4 row_mask:0xf bank_mask:0xf\n"
    "v_mul_f32_dpp v237, v226, v237 row_newbcast:5 row_mask:0xf bank_mask:0xf\n"
    "v_mfma_f32_16x16x32_bf16 v[76:79], v[160:163], v[8:11], v[76:79]\n"
    "s_waitcnt lgkmcnt(2)\n"
    "v_mul_f32_dpp v238, v226, v238 row_newbcast:6 row_mask:0xf bank_mask:0xf\n"
    "v_mul_f32_dpp v239, v226, v239 row_newbcast:7 row_mask:0xf bank_mask:0xf\n"
    "v_mfma_f32_16x16x32_bf16 v[76:79], v[168:171], v[16:19], v[76:79]\n"
    "ds_read_b128 v[160:163], v200 offset:34816\n"
    "v_add_f32_dpp v232, v227, v232 row_newbcast:0 row_mask:0xf bank_mask:0xf\n"
    "v_mfma_f32_16x16x32_bf16 v[76:79], v[172:175], v[20:23], v[76:79]\n"
    "v_add_f32_dpp v233, v227, v233 row_newbcast:1 row_mask:0xf bank_mask:0xf\n"
    "v_add_f32_dpp v234, v227, v234 row_newbcast:2 row_mask:0xf bank_mask:0xf\n"
    "v_mfma_f32_16x16x32_bf16 v[72:75], v[172:175], a[252:255], v[72:75]\n"
    "v_add_f32_dpp v235, v227, v235 row_newbcast:3 row_mask:0xf bank_mask:0xf\n"
    "v_add_f32_dpp v236, v227, v236 row_newbcast:4 row_mask:0xf bank_mask:0xf\n"
    "v_mfma_f32_16x16x32_bf16 v[72:75], v[168:171], a[248:251], v[72:75]\n"
    "v_add_f32_dpp v237, v227, v237 row_newbcast:5 row_mask:0xf bank_mask:0xf\n"
    "v_add_f32_dpp v238, v227, v238 row_newbcast:6 row_mask:0xf bank_mask:0xf\n"
    "v_mfma_f32_16x16x32_bf16 v[68:71], v[168:171], a[224:227], v[68:71]\n"
    "ds_read_b128 v[164:167], v200 offset:35840\n"
    "v_add_f32_dpp v239, v227, v239 row_newbcast:7 row_mask:0xf bank_mask:0xf\n"
    "v_mfma_f32_16x16x32_bf16 v[68:71], v[172:175], a[228:231], v[68:71]\n"
    "v_cvt_pk_bf16_f32 v232, v232, v233\n"
    "v_cvt_pk_bf16_f32 v233, v234, v235\n"
    "v_mfma_f32_16x16x32_bf16 v[64:67], v[172:175], a[204:207], v[64:67]\n"
    "v_cvt_pk_bf16_f32 v234, v236, v237\n"
    "v_cvt_pk_bf16_f32 v235, v238, v239\n"
    "v_mfma_f32_16x16x32_bf16 v[64:67], v[168:171], a[200:203], v[64:67]\n"
    "s_waitcnt lgkmcnt(2)\n"
    "v_mul_f32 v228, s68, v225\n"
    "v_fma_f32 v229, -v224, v228, s69\n"
    "v_mfma_f32_16x16x32_bf16 v[64:67], v[176:179], a[208:211], v[64:67]\n"
    "ds_read_b128 v[168:171], v200 offset:32768\n"
    "v_cvt_f32_i32 v240, v240\n"
    "v_mfma_f32_16x16x32_bf16 v[64:67], v[180:183], a[212:215], v[64:67]\n"
    "buffer_store_dwordx4 v[232:235], v211, s[36:39], s62 offen sc1\n"
    "v_cvt_f32_i32 v241, v241\n"
    "v_cvt_f32_i32 v242, v242\n"
    "v_cvt_f32_i32 v243, v243\n"
    "v_mfma_f32_16x16x32_bf16 v[68:71], v[180:183], a[236:239], v[68:71]\n"
    "v_cvt_f32_i32 v244, v244\n"
    "v_cvt_f32_i32 v245, v245\n"
    "v_mfma_f32_16x16x32_bf16 v[68:71], v[176:179], a[232:235], v[68:71]\n"
    "v_cvt_f32_i32 v246, v246\n"
    "v_cvt_f32_i32 v247, v247\n"
    "v_mfma_f32_16x16x32_bf16 v[72:75], v[176:179], v[0:3], v[72:75]\n"
    "ds_read_b128 v[172:175], v200 offset:33792\n"
    "v_mul_f32_dpp v240, v228, v240 row_newbcast:0 row_mask:0xf bank_mask:0xf\n"
    "v_mfma_f32_16x16x32_bf16 v[72:75], v[180:183], v[4:7], v[72:75]\n"
    "v_mul_f32_dpp v241, v228, v241 row_newbcast:1 row_mask:0xf bank_mask:0xf\n"
    "v_mul_f32_dpp v242, v228, v242 row_newbcast:2 row_mask:0xf bank_mask:0xf\n"
    "v_mfma_f32_16x16x32_bf16 v[76:79], v[180:183], v[28:31], v[76:79]\n"
    "v_mul_f32_dpp v243, v228, v243 row_newbcast:3 row_mask:0xf bank_mask:0xf\n"
    "v_mul_f32_dpp v244, v228, v244 row_newbcast:4 row_mask:0xf bank_mask:0xf\n"
    "v_mfma_f32_16x16x32_bf16 v[76:79], v[176:179], v[24:27], v[76:79]\n"
    "s_waitcnt lgkmcnt(2)\n"
    "v_mul_f32_dpp v245, v228, v245 row_newbcast:5 row_mask:0xf bank_mask:0xf\n"
    "v_mul_f32_dpp v246, v228, v246 row_newbcast:6 row_mask:0xf bank_mask:0xf\n"
    "v_mfma_f32_16x16x32_bf16 v[92:95], v[160:163], v[24:27], v[108:111]\n"
    "ds_read_b128 v[176:179], v200 offset:30720\n"
    "v_mul_f32_dpp v247, v228, v247 row_newbcast:7 row_mask:0xf bank_mask:0xf\n"
    "v_mfma_f32_16x16x32_bf16 v[92:95], v[164:167], v[28:31], v[92:95]\n"
    "v_add_f32_dpp v240, v229, v240 row_newbcast:0 row_mask:0xf bank_mask:0xf\n"
    "v_add_f32_dpp v241, v229, v241 row_newbcast:1 row_mask:0xf bank_mask:0xf\n"
    "v_mfma_f32_16x16x32_bf16 v[88:91], v[164:167], v[4:7], v[104:107]\n"
    "v_add_f32_dpp v242, v229, v242 row_newbcast:2 row_mask:0xf bank_mask:0xf\n"
    "v_add_f32_dpp v243, v229, v243 row_newbcast:3 row_mask:0xf bank_mask:0xf\n"
    "v_mfma_f32_16x16x32_bf16 v[88:91], v[160:163], v[0:3], v[88:91]\n"
    "v_add_f32_dpp v244, v229, v244 row_newbcast:4 row_mask:0xf bank_mask:0xf\n"
    "v_add_f32_dpp v245, v229, v245 row_newbcast:5 row_mask:0xf bank_mask:0xf\n"
    "v_mfma_f32_16x16x32_bf16 v[84:87], v[160:163], a[232:235], v[100:103]\n"
    "ds_read_b128 v[180:183], v200 offset:31744\n"
    "v_add_f32_dpp v246, v229, v246 row_newbcast:6 row_mask:0xf bank_mask:0xf\n"
    "v_mfma_f32_16x16x32_bf16 v[84:87], v[164:167], a[236:239], v[84:87]\n"
    "v_add_f32_dpp v247, v229, v247 row_newbcast:7 row_mask:0xf bank_mask:0xf\n"
    "v_cvt_pk_bf16_f32 v141, v46, v47\n"
    "v_mfma_f32_16x16x32_bf16 v[80:83], v[164:167], a[212:215], v[96:99]\n"
    "v_cvt_pk_bf16_f32 v240, v240, v241\n"
    "v_cvt_pk_bf16_f32 v136, v40, v41\n"
    "v_mfma_f32_16x16x32_bf16 v[80:83], v[160:163], a[208:211], v[80:83]\n"
    "s_waitcnt lgkmcnt(2)\n"
    "v_cvt_pk_bf16_f32 v241, v242, v243\n"
    "v_cvt_pk_bf16_f32 v137, v42, v43\n"
    "v_mfma_f32_16x16x32_bf16 v[80:83], v[168:171], a[200:203], v[80:83]\n"
    "ds_read_b128 v[160:163], v200 offset:36864\n"
    "v_cvt_pk_bf16_f32 v242, v244, v245\n"
    "v_mfma_f32_16x16x32_bf16 v[80:83], v[172:175], a[204:207], v[80:83]\n"
    "v_cvt_pk_bf16_f32 v132, v36, v37\n"
    "v_cvt_pk_bf16_f32 v243, v246, v247\n"
    "v_mfma_f32_16x16x32_bf16 v[84:87], v[172:175], a[228:231], v[84:87]\n"
    "v_cvt_pk_bf16_f32 v133, v38, v39\n"
    "v_cvt_pk_bf16_f32 v128, v32, v33\n"
    "v_mfma_f32_16x16x32_bf16 v[84:87], v[168:171], a[224:227], v[84:87]\n"
    "v_cvt_pk_bf16_f32 v129, v34, v35\n"
    "v_cvt_pk_bf16_f32 v142, v60, v61\n"
    "v_mfma_f32_16x16x32_bf16 v[88:91], v[168:171], a[248:251], v[88:91]\n"
    "ds_read_b128 v[164:167], v200 offset:37888\n"
    "v_cvt_pk_bf16_f32 v143, v62, v63\n"
    "v_mfma_f32_16x16x32_bf16 v[88:91], v[172:175], a[252:255], v[88:91]\n"
    "v_exp_f32 v64, v64\n"
    "v_mfma_f32_16x16x32_bf16 v[92:95], v[172:175], v[20:23], v[92:95]\n"
    "v_exp_f32 v65, v65\n"
    "v_mfma_f32_16x16x32_bf16 v[92:95], v[168:171], v[16:19], v[92:95]\n"
    "s_waitcnt lgkmcnt(2)\n"
    "v_exp_f32 v66, v66\n"
    "v_mfma_f32_16x16x32_bf16 v[92:95], v[176:179], v[8:11], v[92:95]\n"
    "ds_read_b128 v[168:171], v200 offset:38912\n"
    "v_cvt_pk_bf16_f32 v138, v56, v57\n"
    "v_mfma_f32_16x16x32_bf16 v[92:95], v[180:183], v[12:15], v[92:95]\n"
    "buffer_store_dwordx4 v[240:243], v212, s[40:43], s63 offen sc1\n"
    "v_exp_f32 v67, v67\n"
    "v_cvt_pk_bf16_f32 v139, v58, v59\n"
    "v_mfma_f32_16x16x32_bf16 v[88:91], v[180:183], a[244:247], v[88:91]\n"
    "v_exp_f32 v68, v68\n"
    "v_mfma_f32_16x16x32_bf16 v[88:91], v[176:179], a[240:243], v[88:91]\n"
    "v_exp_f32 v69, v69\n"
    "v_mfma_f32_16x16x32_bf16 v[84:87], v[176:179], a[216:219], v[84:87]\n"
    "ds_read_b128 v[172:175], v200 offset:39936\n"
    "v_cvt_pk_bf16_f32 v134, v52, v53\n"
    "v_mfma_f32_16x16x32_bf16 v[84:87], v[180:183], a[220:223], v[84:87]\n"
    "v_exp_f32 v70, v70\n"
    "v_mfma_f32_16x16x32_bf16 v[80:83], v[180:183], a[196:199], v[80:83]\n"
    "v_exp_f32 v71, v71\n"
    "v_mfma_f32_16x16x32_bf16 v[80:83], v[176:179], a[192:195], v[80:83]\n"
    "s_waitcnt lgkmcnt(2)\n"
    "v_exp_f32 v72, v72\n"
    "v_mfma_f32_16x16x32_bf16 v[32:35], v[160:163], a[192:195], v[96:99]\n"
    "ds_read_b128 v[176:179], v200 offset:40960\n"
    "v_cvt_pk_bf16_f32 v135, v54, v55\n"
    "v_mfma_f32_16x16x32_bf16 v[32:35], v[164:167], a[196:199], v[32:35]\n"
    "v_exp_f32 v73, v73\n"
    "v_mfma_f32_16x16x32_bf16 v[36:39], v[164:167], a[220:223], v[100:103]\n"
    "v_exp_f32 v74, v74\n"
    "v_mfma_f32_16x16x32_bf16 v[36:39], v[160:163], a[216:219], v[36:39]\n"
    "v_exp_f32 v75, v75\n"
    "v_mfma_f32_16x16x32_bf16 v[40:43], v[160:163], a[240:243], v[104:107]\n"
    "ds_read_b128 v[180:183], v200 offset:41984\n"
    "v_cvt_pk_bf16_f32 v130, v48, v49\n"
    "v_mfma_f32_16x16x32_bf16 v[40:43], v[164:167], a[244:247], v[40:43]\n"
    "v_exp_f32 v76, v76\n"
    "v_mfma_f32_16x16x32_bf16 v[44:47], v[164:167], v[12:15], v[108:111]\n"
    "v_exp_f32 v77, v77\n"
    "v_mfma_f32_16x16x32_bf16 v[44:47], v[160:163], v[8:11], v[44:47]\n"
    "s_waitcnt lgkmcnt(2)\n"
    "v_exp_f32 v78, v78\n"
    "v_mfma_f32_16x16x32_bf16 v[44:47], v[168:171], v[16:19], v[44:47]\n"
    "ds_read_b128 v[160:163], v200 offset:47104\n"
    "v_cvt_pk_bf16_f32 v131, v50, v51\n"
    "v_mfma_f32_16x16x32_bf16 v[44:47], v[172:175], v[20:23], v[44:47]\n"
    "v_exp_f32 v79, v79\n"
    "v_mfma_f32_16x16x32_bf16 v[40:43], v[172:175], a[252:255], v[40:43]\n"
    "v_exp_f32 v80, v80\n"
    "v_mfma_f32_16x16x32_bf16 v[40:43], v[168:171], a[248:251], v[40:43]\n"
    "v_exp_f32 v81, v81\n"
    "v_mfma_f32_16x16x32_bf16 v[36:39], v[168:171], a[224:227], v[36:39]\n"
    "ds_read_b128 v[164:167], v200 offset:48128\n"
    "v_cvt_pk_bf16_f32 v144, v64, v65\n"
    "v_mfma_f32_16x16x32_bf16 v[36:39], v[172:175], a[228:231], v[36:39]\n"
    "v_exp_f32 v82, v82\n"
    "v_mfma_f32_16x16x32_bf16 v[32:35], v[172:175], a[204:207], v[32:35]\n"
    "v_exp_f32 v83, v83\n"
    "v_mfma_f32_16x16x32_bf16 v[32:35], v[168:171], a[200:203], v[32:35]\n"
    "s_waitcnt lgkmcnt(2)\n"
    "v_exp_f32 v84, v84\n"
    "v_mfma_f32_16x16x32_bf16 v[32:35], v[176:179], a[208:211], v[32:35]\n"
    "ds_read_b128 v[168:171], v200 offset:45056\n"
    "v_cvt_pk_bf16_f32 v145, v66, v67\n"
    "v_mfma_f32_16x16x32_bf16 v[32:35], v[180:183], a[212:215], v[32:35]\n"
    "v_exp_f32 v85, v85\n"
    "v_mfma_f32_16x16x32_bf16 v[36:39], v[180:183], a[236:239], v[36:39]\n"
    "v_exp_f32 v86, v86\n"
    "v_mfma_f32_16x16x32_bf16 v[36:39], v[176:179], a[232:235], v[36:39]\n"
    "v_exp_f32 v87, v87\n"
    "v_mfma_f32_16x16x32_bf16 v[40:43], v[176:179], v[0:3], v[40:43]\n"
    "ds_read_b128 v[172:175], v200 offset:46080\n"
    "v_cvt_pk_bf16_f32 v148, v68, v69\n"
    "v_mfma_f32_16x16x32_bf16 v[40:43], v[180:183], v[4:7], v[40:43]\n"
    "v_exp_f32 v88, v88\n"
    "v_mfma_f32_16x16x32_bf16 v[44:47], v[180:183], v[28:31], v[44:47]\n"
    "v_cvt_pk_bf16_f32 v149, v70, v71\n"
    "v_cvt_pk_bf16_f32 v152, v72, v73\n"
    "v_mfma_f32_16x16x32_bf16 v[44:47], v[176:179], v[24:27], v[44:47]\n"
    "s_waitcnt lgkmcnt(2)\n"
    "v_exp_f32 v89, v89\n"
    "v_mfma_f32_16x16x32_bf16 v[60:63], v[160:163], v[24:27], v[108:111]\n"
    "s_mov_b32 m0, 0\n"
    "ds_read_b128 v[176:179], v200 offset:43008\n"
    "v_cvt_pk_bf16_f32 v153, v74, v75\n"
    "v_mfma_f32_16x16x32_bf16 v[60:63], v[164:167], v[28:31], v[60:63]\n"
    "v_exp_f32 v90, v90\n"
    "v_mfma_f32_16x16x32_bf16 v[56:59], v[164:167], v[4:7], v[104:107]\n"
    "buffer_load_dwordx4 v200, s[36:39], s60 offen sc1 lds\n"
    "v_mfma_f32_16x16x32_bf16 v[56:59], v[160:163], v[0:3], v[56:59]\n"
    "v_cvt_pk_bf16_f32 v156, v76, v77\n"
    "v_cvt_pk_bf16_f32 v157, v78, v79\n"
    "v_mfma_f32_16x16x32_bf16 v[52:55], v[160:163], a[232:235], v[100:103]\n"
    "ds_read_b128 v[180:183], v200 offset:44032\n"
    "v_cvt_pk_bf16_f32 v146, v80, v81\n"
    "v_mfma_f32_16x16x32_bf16 v[52:55], v[164:167], a[236:239], v[52:55]\n"
    "v_exp_f32 v91, v91\n"
    "v_mfma_f32_16x16x32_bf16 v[48:51], v[164:167], a[212:215], v[96:99]\n"
    "buffer_load_dwordx4 v200, s[36:39], s60 offen offset:1024 sc1 lds\n"
    "v_mfma_f32_16x16x32_bf16 v[48:51], v[160:163], a[208:211], v[48:51]\n"
    "s_waitcnt lgkmcnt(2)\n"
    "v_cvt_pk_bf16_f32 v147, v82, v83\n"
    "v_cvt_pk_bf16_f32 v150, v84, v85\n"
    "v_mfma_f32_16x16x32_bf16 v[48:51], v[168:171], a[200:203], v[48:51]\n"
    "ds_read_b128 v[160:163], v201\n"
    "v_cvt_pk_bf16_f32 v151, v86, v87\n"
    "v_mfma_f32_16x16x32_bf16 v[48:51], v[172:175], a[204:207], v[48:51]\n"
    "v_exp_f32 v92, v92\n"
    "v_mfma_f32_16x16x32_bf16 v[52:55], v[172:175], a[228:231], v[52:55]\n"
    "buffer_load_dwordx4 v200, s[36:39], s60 offen offset:2048 sc1 lds\n"
    "v_mfma_f32_16x16x32_bf16 v[52:55], v[168:171], a[224:227], v[52:55]\n"
    "v_exp_f32 v93, v93\n"
    "v_mfma_f32_16x16x32_bf16 v[56:59], v[168:171], a[248:251], v[56:59]\n"
    "s_add_u32 m0, s71, 0x12000\n"
    "ds_read_b128 v[164:167], v201 offset:1024\n"
    "v_cvt_pk_bf16_f32 v154, v88, v89\n"
    "v_mfma_f32_16x16x32_bf16 v[56:59], v[172:175], a[252:255], v[56:59]\n"
    "v_exp_f32 v94, v94\n"
    "v_mfma_f32_16x16x32_bf16 v[60:63], v[172:175], v[20:23], v[60:63]\n"
    "buffer_load_dwordx4 v200, s[40:43], s61 offen sc1 lds\n"
    "v_mfma_f32_16x16x32_bf16 v[60:63], v[168:171], v[16:19], v[60:63]\n"
    "s_waitcnt lgkmcnt(2)\n"
    "v_cvt_pk_bf16_f32 v155, v90, v91\n"
    "v_cvt_pk_bf16_f32 v158, v92, v93\n"
    "v_mfma_f32_16x16x32_bf16 v[60:63], v[176:179], v[8:11], v[60:63]\n"
    "ds_read_b128 v[168:171], v201 offset:2048\n"
    "v_add_f32 v113, v113, v64\n"
    "v_mfma_f32_16x16x32_bf16 v[60:63], v[180:183], v[12:15], v[60:63]\n"
    "v_exp_f32 v95, v95\n"
    "v_mfma_f32_16x16x32_bf16 v[56:59], v[180:183], a[244:247], v[56:59]\n"
    "buffer_load_dwordx4 v200, s[40:43], s61 offen offset:1024 sc1 lds\n"
    "v_mfma_f32_16x16x32_bf16 v[56:59], v[176:179], a[240:243], v[56:59]\n"
    "v_cvt_pk_bf16_f32 v159, v94, v95\n"
    "v_add_f32 v114, v114, v65\n"
    "v_mfma_f32_16x16x32_bf16 v[52:55], v[176:179], a[216:219], v[52:55]\n"
    "ds_read_b128 v[172:175], v201 offset:3072\n"
    "v_add_f32 v113, v113, v66\n"
    "v_mfma_f32_16x16x32_bf16 v[52:55], v[180:183], a[220:223], v[52:55]\n"
    "v_exp_f32 v32, v32\n"
    "v_mfma_f32_16x16x32_bf16 v[48:51], v[180:183], a[196:199], v[48:51]\n"
    "buffer_load_dwordx4 v200, s[40:43], s61 offen offset:2048 sc1 lds\n"
    "v_mfma_f32_16x16x32_bf16 v[48:51], v[176:179], a[192:195], v[48:51]\n"
    "s_waitcnt lgkmcnt(2)\n"
    "v_exp_f32 v33, v33\n"
    "v_mfma_f32_16x16x32_bf16 a[0:3], v[160:163], v[128:131], a[0:3]\n"
    "ds_read_b128 v[176:179], v201 offset:4096\n"
    "v_add_f32 v114, v114, v67\n"
    "v_mfma_f32_16x16x32_bf16 a[0:3], v[164:167], v[144:147], a[0:3]\n"
    "v_exp_f32 v34, v34\n"
    "v_mfma_f32_16x16x32_bf16 a[4:7], v[160:163], v[132:135], a[4:7]\n"
    "buffer_load_dwordx4 v200, s[40:43], s61 offen offset:3072 sc1 lds\n"
    "v_mfma_f32_16x16x32_bf16 a[4:7], v[164:167], v[148:151], a[4:7]\n"
    "s_add_u32 s65, s61, 0x1000\n"
    "v_exp_f32 v35, v35\n"
    "v_mfma_f32_16x16x32_bf16 a[8:11], v[160:163], v[136:139], a[8:11]\n"
    "s_add_u32 m0, s71, 0x13000\n"
    "ds_read_b128 v[180:183], v201 offset:5120\n"
    "v_add_f32 v117, v117, v68\n"
    "v_mfma_f32_16x16x32_bf16 a[8:11], v[164:167], v[152:155], a[8:11]\n"
    "v_exp_f32 v36, v36\n"
    "v_mfma_f32_16x16x32_bf16 a[12:15], v[160:163], v[140:143], a[12:15]\n"
    "buffer_load_dwordx4 v200, s[40:43], s65 offen sc1 lds\n"
    "v_mfma_f32_16x16x32_bf16 a[12:15], v[164:167], v[156:159], a[12:15]\n"
    "s_waitcnt lgkmcnt(2)\n"
    "v_exp_f32 v37, v37\n"
    "v_mfma_f32_16x16x32_bf16 a[28:31], v[168:171], v[140:143], a[28:31]\n"
    "ds_read_b128 v[160:163], v201 offset:6144\n"
    "v_add_f32 v118, v118, v69\n"
    "v_mfma_f32_16x16x32_bf16 a[28:31], v[172:175], v[156:159], a[28:31]\n"
    "v_exp_f32 v38, v38\n"
    "v_mfma_f32_16x16x32_bf16 a[24:27], v[172:175], v[152:155], a[24:27]\n"
    "buffer_load_dwordx4 v200, s[40:43], s65 offen offset:1024 sc1 lds\n"
    "v_mfma_f32_16x16x32_bf16 a[24:27], v[168:171], v[136:139], a[24:27]\n"
    "v_exp_f32 v39, v39\n"
    "v_mfma_f32_16x16x32_bf16 a[20:23], v[168:171], v[132:135], a[20:23]\n"
    "ds_read_b128 v[164:167], v201 offset:7168\n"
    "v_add_f32 v117, v117, v70\n"
    "v_mfma_f32_16x16x32_bf16 a[20:23], v[172:175], v[148:151], a[20:23]\n"
    "v_exp_f32 v40, v40\n"
    "v_mfma_f32_16x16x32_bf16 a[16:19], v[172:175], v[144:147], a[16:19]\n"
    "v_exp_f32 v41, v41\n"
    "v_mfma_f32_16x16x32_bf16 a[16:19], v[168:171], v[128:131], a[16:19]\n"
    "s_waitcnt lgkmcnt(2)\n"
    "v_exp_f32 v42, v42\n"
    "v_mfma_f32_16x16x32_bf16 a[32:35], v[176:179], v[128:131], a[32:35]\n"
    "ds_read_b128 v[168:171], v201 offset:8192\n"
    "v_add_f32 v113, v113, v32\n"
    "v_mfma_f32_16x16x32_bf16 a[32:35], v[180:183], v[144:147], a[32:35]\n"
    "buffer_load_dwordx2 v[218:219], v213, s[44:47], 0 offen sc1\n"
    "v_mfma_f32_16x16x32_bf16 a[36:39], v[180:183], v[148:151], a[36:39]\n"
    "v_exp_f32 v43, v43\n"
    "v_mfma_f32_16x16x32_bf16 a[36:39], v[176:179], v[132:135], a[36:39]\n"
    "v_exp_f32 v44, v44\n"
    "v_mfma_f32_16x16x32_bf16 a[40:43], v[176:179], v[136:139], a[40:43]\n"
    "ds_read_b128 v[172:175], v201 offset:9216\n"
    "v_add_f32 v118, v118, v71\n"
    "v_mfma_f32_16x16x32_bf16 a[40:43], v[180:183], v[152:155], a[40:43]\n"
    "v_exp_f32 v45, v45\n"
    "v_mfma_f32_16x16x32_bf16 a[44:47], v[180:183], v[156:159], a[44:47]\n"
    "v_exp_f32 v46, v46\n"
    "v_mfma_f32_16x16x32_bf16 a[44:47], v[176:179], v[140:143], a[44:47]\n"
    "s_waitcnt lgkmcnt(2)\n"
    "v_exp_f32 v47, v47\n"
    "v_mfma_f32_16x16x32_bf16 a[60:63], v[160:163], v[140:143], a[60:63]\n"
    "ds_read_b128 v[176:179], v201 offset:10240\n"
    "v_add_f32 v114, v114, v33\n"
    "v_mfma_f32_16x16x32_bf16 a[60:63], v[164:167], v[156:159], a[60:63]\n"
    "v_exp_f32 v48, v48\n"
    "v_mfma_f32_16x16x32_bf16 a[56:59], v[164:167], v[152:155], a[56:59]\n"
    "v_exp_f32 v49, v49\n"
    "v_mfma_f32_16x16x32_bf16 a[56:59], v[160:163], v[136:139], a[56:59]\n"
    "s_add_u32 m0, s51, s95\n"
    "v_exp_f32 v50, v50\n"
    "v_mfma_f32_16x16x32_bf16 a[52:55], v[160:163], v[132:135], a[52:55]\n"
    "ds_read_b128 v[180:183], v201 offset:11264\n"
    "v_add_f32 v121, v121, v72\n"
    "v_mfma_f32_16x16x32_bf16 a[52:55], v[164:167], v[148:151], a[52:55]\n"
    "buffer_load_dwordx4 v202, s[32:35], s64 offen lds\n"
    "v_mfma_f32_16x16x32_bf16 a[48:51], v[164:167], v[144:147], a[48:51]\n"
    "v_exp_f32 v51, v51\n"
    "v_mfma_f32_16x16x32_bf16 a[48:51], v[160:163], v[128:131], a[48:51]\n"
    "s_waitcnt lgkmcnt(2)\n"
    "v_exp_f32 v52, v52\n"
    "v_mfma_f32_16x16x32_bf16 a[64:67], v[168:171], v[128:131], a[64:67]\n"
    "ds_read_b128 v[160:163], v201 offset:12288\n"
    "v_add_f32 v113, v113, v34\n"
    "v_mfma_f32_16x16x32_bf16 a[64:67], v[172:175], v[144:147], a[64:67]\n"
    "v_exp_f32 v53, v53\n"
    "v_mfma_f32_16x16x32_bf16 a[68:71], v[172:175], v[148:151], a[68:71]\n"
    "buffer_load_dwordx4 v203, s[32:35], s64 offen offset:1024 lds\n"
    "v_mfma_f32_16x16x32_bf16 a[68:71], v[168:171], v[132:135], a[68:71]\n"
    "v_add_f32 v122, v122, v73\n"
    "v_add_f32 v114, v114, v35\n"
    "v_mfma_f32_16x16x32_bf16 a[72:75], v[168:171], v[136:139], a[72:75]\n"
    "ds_read_b128 v[164:167], v201 offset:13312\n"
    "v_add_f32 v121, v121, v74\n"
    "v_mfma_f32_16x16x32_bf16 a[72:75], v[172:175], v[152:155], a[72:75]\n"
    "v_add_f32 v117, v117, v36\n"
    "v_add_f32 v122, v122, v75\n"
    "v_mfma_f32_16x16x32_bf16 a[76:79], v[172:175], v[156:159], a[76:79]\n"
    "v_exp_f32 v54, v54\n"
    "v_mfma_f32_16x16x32_bf16 a[76:79], v[168:171], v[140:143], a[76:79]\n"
    "s_waitcnt lgkmcnt(2)\n"
    "buffer_load_dwordx4 v204, s[32:35], s64 offen offset:2048 lds\n"
    "v_mfma_f32_16x16x32_bf16 a[92:95], v[176:179], v[140:143], a[92:95]\n"
    "ds_read_b128 v[168:171], v201 offset:14336\n"
    "v_add_f32 v118, v118, v37\n"
    "v_mfma_f32_16x16x32_bf16 a[92:95], v[180:183], v[156:159], a[92:95]\n"
    "v_add_f32 v125, v125, v76\n"
    "v_add_f32 v117, v117, v38\n"
    "v_mfma_f32_16x16x32_bf16 a[88:91], v[180:183], v[152:155], a[88:91]\n"
    "v_exp_f32 v55, v55\n"
    "v_mfma_f32_16x16x32_bf16 a[88:91], v[176:179], v[136:139], a[88:91]\n"
    "v_add_f32 v126, v126, v77\n"
    "v_add_f32 v118, v118, v39\n"
    "v_mfma_f32_16x16x32_bf16 a[84:87], v[176:179], v[132:135], a[84:87]\n"
    "s_mov_b32 s95, s94\n"
    "ds_read_b128 v[172:175], v201 offset:15360\n"
    "v_add_f32 v125, v125, v78\n"
    "v_mfma_f32_16x16x32_bf16 a[84:87], v[180:183], v[148:151], a[84:87]\n"
    "s_sub_u32 s94, s94, 0x3000\n"
    "v_add_f32 v121, v121, v40\n"
    "v_add_f32 v126, v126, v79\n"
    "v_mfma_f32_16x16x32_bf16 a[80:83], v[180:183], v[144:147], a[80:83]\n"
    "s_min_u32 s94, s94, 0x6000\n"
    "v_exp_f32 v56, v56\n"
    "v_mfma_f32_16x16x32_bf16 a[80:83], v[176:179], v[128:131], a[80:83]\n"
    "s_waitcnt lgkmcnt(2)\n"
    "v_add_f32 v122, v122, v41\n"
    "v_add_f32 v113, v113, v80\n"
    "v_mfma_f32_16x16x32_bf16 a[96:99], v[160:163], v[128:131], a[96:99]\n"
    "s_sub_u32 s93, s94, s95\n"
    "ds_read_b128 v[176:179], v201 offset:16384\n"
    "v_add_f32 v121, v121, v42\n"
    "v_mfma_f32_16x16x32_bf16 a[96:99], v[164:167], v[144:147], a[96:99]\n"
    "v_exp_f32 v57, v57\n"
    "v_mfma_f32_16x16x32_bf16 a[100:103], v[164:167], v[148:151], a[100:103]\n"
    "v_add_f32 v114, v114, v81\n"
    "v_add_f32 v122, v122, v43\n"
    "v_mfma_f32_16x16x32_bf16 a[100:103], v[160:163], v[132:135], a[100:103]\n"
    "v_add_f32 v113, v113, v82\n"
    "v_add_f32 v125, v125, v44\n"
    "v_mfma_f32_16x16x32_bf16 a[104:107], v[160:163], v[136:139], a[104:107]\n"
    "ds_read_b128 v[180:183], v201 offset:17408\n"
    "v_add_f32 v114, v114, v83\n"
    "v_mfma_f32_16x16x32_bf16 a[104:107], v[164:167], v[152:155], a[104:107]\n"
    "v_exp_f32 v58, v58\n"
    "v_mfma_f32_16x16x32_bf16 a[108:111], v[164:167], v[156:159], a[108:111]\n"
    "v_add_f32 v126, v126, v45\n"
    "v_add_f32 v117, v117, v84\n"
    "v_mfma_f32_16x16x32_bf16 a[108:111], v[160:163], v[140:143], a[108:111]\n"
    "s_waitcnt lgkmcnt(2)\n"
    "v_add_f32 v125, v125, v46\n"
    "v_add_f32 v118, v118, v85\n"
    "v_mfma_f32_16x16x32_bf16 a[124:127], v[168:171], v[140:143], a[124:127]\n"
    "ds_read_b128 v[160:163], v201 offset:18432\n"
    "v_add_f32 v126, v126, v47\n"
    "v_mfma_f32_16x16x32_bf16 a[124:127], v[172:175], v[156:159], a[124:127]\n"
    "v_exp_f32 v59, v59\n"
    "v_mfma_f32_16x16x32_bf16 a[120:123], v[172:175], v[152:155], a[120:123]\n"
    "v_add_f32 v117, v117, v86\n"
    "v_add_f32 v113, v113, v48\n"
    "v_mfma_f32_16x16x32_bf16 a[120:123], v[168:171], v[136:139], a[120:123]\n"
    "v_exp_f32 v60, v60\n"
    "v_mfma_f32_16x16x32_bf16 a[116:119], v[168:171], v[132:135], a[116:119]\n"
    "ds_read_b128 v[164:167], v201 offset:19456\n"
    "v_add_u32 v220, 1, v220\n"
    "v_mfma_f32_16x16x32_bf16 a[116:119], v[172:175], v[148:151], a[116:119]\n"
    "v_add_u32 v205, s93, v205\n"
    "v_add_f32 v118, v118, v87\n"
    "v_mfma_f32_16x16x32_bf16 a[112:115], v[172:175], v[144:147], a[112:115]\n"
    "v_add_f32 v114, v114, v49\n"
    "v_add_u32 v209, 0x180, v209\n"
    "v_mfma_f32_16x16x32_bf16 a[112:115], v[168:171], v[128:131], a[112:115]\n"
    "s_waitcnt lgkmcnt(2)\n"
    "v_add_u32 v206, s93, v206\n"
    "v_add_f32 v121, v121, v88\n"
    "v_mfma_f32_16x16x32_bf16 a[128:131], v[176:179], v[128:131], a[128:131]\n"
    "s_add_u32 s64, s64, 0xc000\n"
    "ds_read_b128 v[168:171], v201 offset:20480\n"
    "v_add_f32 v113, v113, v50\n"
    "v_mfma_f32_16x16x32_bf16 a[128:131], v[180:183], v[144:147], a[128:131]\n"
    "s_add_u32 s62, s62, 0x6000\n"
    "v_exp_f32 v61, v61\n"
    "v_mfma_f32_16x16x32_bf16 a[132:135], v[180:183], v[148:151], a[132:135]\n"
    "s_add_u32 s63, s63, 0x6000\n"
    "v_add_u32 v210, 32, v210\n"
    "v_add_u32 v207, s93, v207\n"
    "v_mfma_f32_16x16x32_bf16 a[132:135], v[176:179], v[132:135], a[132:135]\n"
    "s_add_u32 s60, s60, 0x6000\n"
    "v_add_f32 v122, v122, v89\n"
    "v_add_f32 v114, v114, v51\n"
    "v_mfma_f32_16x16x32_bf16 a[136:139], v[176:179], v[136:139], a[136:139]\n"
    "s_add_u32 s61, s61, 0x6000\n"
    "ds_read_b128 v[172:175], v201 offset:21504\n"
    "v_add_f32 v121, v121, v90\n"
    "v_mfma_f32_16x16x32_bf16 a[136:139], v[180:183], v[152:155], a[136:139]\n"
    "s_add_u32 s66, s66, s74\n"
    "v_add_f32 v117, v117, v52\n"
    "v_add_f32 v122, v122, v91\n"
    "v_mfma_f32_16x16x32_bf16 a[140:143], v[180:183], v[156:159], a[140:143]\n"
    "s_min_u32 s66, s66, 33\n"
    "v_exp_f32 v62, v62\n"
    "v_mfma_f32_16x16x32_bf16 a[140:143], v[176:179], v[140:143], a[140:143]\n"
    "s_waitcnt lgkmcnt(2)\n"
    "v_add_f32 v118, v118, v53\n"
    "v_add_f32 v125, v125, v92\n"
    "v_mfma_f32_16x16x32_bf16 a[156:159], v[160:163], v[140:143], a[156:159]\n"
    "ds_read_b128 v[176:179], v201 offset:22528\n"
    "v_add_f32 v117, v117, v54\n"
    "v_mfma_f32_16x16x32_bf16 a[156:159], v[164:167], v[156:159], a[156:159]\n"
    "s_waitcnt vmcnt(3)\n"
    "ds_read_b128 v[180:183], v201 offset:23552\n"
    "v_add_f32 v126, v126, v93\n"
    "v_mfma_f32_16x16x32_bf16 a[152:155], v[164:167], v[152:155], a[152:155]\n"
    "v_cmp_ne_u32 s[80:81], s75, v219\n"
    "v_add_f32 v118, v118, v55\n"
    "v_mfma_f32_16x16x32_bf16 a[152:155], v[160:163], v[136:139], a[152:155]\n"
    "v_cmp_gt_u32 vcc, s66, v218\n"
    "v_add_f32 v125, v125, v94\n"
    "v_mfma_f32_16x16x32_bf16 a[148:151], v[160:163], v[132:135], a[148:151]\n"
    "v_exp_f32 v63, v63\n"
    "v_mfma_f32_16x16x32_bf16 a[148:151], v[164:167], v[148:151], a[148:151]\n"
    "s_or_b64 vcc, vcc, s[80:81]\n"
    "v_add_f32 v121, v121, v56\n"
    "v_add_f32 v126, v126, v95\n"
    "v_mfma_f32_16x16x32_bf16 a[144:147], v[164:167], v[144:147], a[144:147]\n"
    "s_cbranch_vccnz LSPRE4\n"
    "LSPB4:\n"
    "v_add_f32 v122, v122, v57\n"
    "v_mfma_f32_16x16x32_bf16 a[144:147], v[160:163], v[128:131], a[144:147]\n"
    "s_waitcnt lgkmcnt(0)\n"
    "v_mfma_f32_16x16x32_bf16 a[160:163], v[168:171], v[128:131], a[160:163]\n"
    "s_barrier\n"
    "v_mfma_f32_16x16x32_bf16 a[160:163], v[172:175], v[144:147], a[160:163]\n"
    "buffer_store_dwordx2 v[220:221], v214, s[44:47], 0 offen sc1\n"
    "v_mfma_f32_16x16x32_bf16 a[164:167], v[172:175], v[148:151], a[164:167]\n"
    "ds_read_b128 v[160:163], v200\n"
    "ds_read_u16_d16_hi v223, v209 offset:6144\n"
    "v_mfma_f32_16x16x32_bf16 a[164:167], v[168:171], v[132:135], a[164:167]\n"
    "ds_read_u16_d16_hi v222, v209\n"
    "v_add_f32 v121, v121, v58\n"
    "v_mfma_f32_16x16x32_bf16 a[168:171], v[168:171], v[136:139], a[168:171]\n"
    "ds_read_b128 v[164:167], v200 offset:1024\n"
    "ds_read_b128 v[232:235], v205 offset:24576\n"
    "v_mfma_f32_16x16x32_bf16 a[168:171], v[172:175], v[152:155], a[168:171]\n"
    "v_add_f32 v122, v122, v59\n"
    "ds_read_b128 v[236:239], v206 offset:24576\n"
    "v_mfma_f32_16x16x32_bf16 a[172:175], v[172:175], v[156:159], a[172:175]\n"
    "v_add_f32 v125, v125, v60\n"
    "v_add_f32 v126, v126, v61\n"
    "v_mfma_f32_16x16x32_bf16 a[172:175], v[168:171], v[140:143], a[172:175]\n"
    "v_add_f32 v125, v125, v62\n"
    "v_add_f32 v126, v126, v63\n"
    "v_mfma_f32_16x16x32_bf16 a[188:191], v[176:179], v[140:143], a[188:191]\n"
    "ds_read_b128 v[168:171], v200 offset:2048\n"
    "ds_read_u16_d16_hi v225, v210 offset:512\n"
    "v_mfma_f32_16x16x32_bf16 a[188:191], v[180:183], v[156:159], a[188:191]\n"
    "ds_read_u16_d16_hi v224, v210\n"
    "ds_read2st64_b32 v[240:241], v207 offset0:96 offset1:99\n"
    "v_mfma_f32_16x16x32_bf16 a[184:187], v[176:179], v[136:139], a[184:187]\n"
    "ds_read2st64_b32 v[242:243], v207 offset0:102 offset1:105\n"
    "ds_read2st64_b32 v[244:245], v207 offset0:108 offset1:111\n"
    "v_mfma_f32_16x16x32_bf16 a[184:187], v[180:183], v[152:155], a[184:187]\n"
    "ds_read2st64_b32 v[246:247], v207 offset0:114 offset1:117\n"
    "v_cvt_pk_bf16_f32 v140, v44, v45\n"
    "v_mfma_f32_16x16x32_bf16 a[180:183], v[176:179], v[132:135], a[180:183]\n"
    "ds_read_b128 v[172:175], v200 offset:3072\n"
    "s_waitcnt lgkmcnt(8)\n"
    "v_mfma_f32_16x16x32_bf16 a[180:183], v[180:183], v[148:151], a[180:183]\n"
    "v_mul_f32 v226, s67, v223\n"
    "v_fma_f32 v227, -v222, v226, v217\n"
    "v_mfma_f32_16x16x32_bf16 a[176:179], v[176:179], v[128:131], a[176:179]\n"
    "v_cvt_f32_i32 v232, v232\n"
    "v_cvt_f32_i32 v233, v233\n"
    "v_mfma_f32_16x16x32_bf16 a[176:179], v[180:183], v[144:147], a[176:179]\n"
    "v_cvt_f32_i32 v234, v234\n"
    "v_cvt_f32_i32 v235, v235\n"
    "LM0:\n"
    "v_mfma_f32_16x16x32_bf16 v[64:67], v[160:163], a[192:195], v[96:99]\n"
    "ds_read_b128 v[176:179], v200 offset:4096\n"
    "v_cvt_f32_i32 v236, v236\n"
    "v_mfma_f32_16x16x32_bf16 v[64:67], v[164:167], a[196:199], v[64:67]\n"
    "v_cvt_f32_i32 v237, v237\n"
    "v_cvt_f32_i32 v238, v238\n"
    "v_mfma_f32_16x16x32_bf16 v[68:71], v[164:167], a[220:223], v[100:103]\n"
    "v_cvt_f32_i32 v239, v239\n"
    "v_mul_f32_dpp v232, v226, v232 row_newbcast:0 row_mask:0xf bank_mask:0xf\n"
    "v_mfma_f32_16x16x32_bf16 v[68:71], v[160:163], a[216:219], v[68:71]\n"
    "v_mul_f32_dpp v233, v226, v233 row_newbcast:1 row_mask:0xf bank_mask:0xf\n"
    "v_mul_f32_dpp v234, v226, v234 row_newbcast:2 row_mask:0xf bank_mask:0xf\n"
    "v_mfma_f32_16x16x32_bf16 v[72:75], v[160:163], a[240:243], v[104:107]\n"
    "ds_read_b128 v[180:183], v200 offset:5120\n"
    "v_mul_f32_dpp v235, v226, v235 row_newbcast:3 row_mask:0xf bank_mask:0xf\n"
    "v_mfma_f32_16x16x32_bf16 v[72:75], v[164:167], a[244:247], v[72:75]\n"
    "v_mul_f32_dpp v236, v226, v236 row_newbcast:4 row_mask:0xf bank_mask:0xf\n"
    "v_mul_f32_dpp v237, v226, v237 row_newbcast:5 row_mask:0xf bank_mask:0xf\n"
    "v_mfma_f32_16x16x32_bf16 v[76:79], v[164:167], v[12:15], v[108:111]\n"
    "v_mul_f32_dpp v238, v226, v238 row_newbcast:6 row_mask:0xf bank_mask:0xf\n"
    "v_mul_f32_dpp v239, v226, v239 row_newbcast:7 row_mask:0xf bank_mask:0xf\n"
    "v_mfma_f32_16x16x32_bf16 v[76:79], v[160:163], v[8:11], v[76:79]\n"
    "s_waitcnt lgkmcnt(2)\n"
    "v_add_f32_dpp v232, v227, v232 row_newbcast:0 row_mask:0xf bank_mask:0xf\n"
    "v_add_f32_dpp v233, v227, v233 row_newbcast:1 row_mask:0xf bank_mask:0xf\n"
    "v_mfma_f32_16x16x32_bf16 v[76:79], v[168:171], v[16:19], v[76:79]\n"
    "ds_read_b128 v[160:163], v200 offset:10240\n"
    "v_add_f32_dpp v234, v227, v234 row_newbcast:2 row_mask:0xf bank_mask:0xf\n"
    "v_mfma_f32_16x16x32_bf16 v[76:79], v[172:175], v[20:23], v[76:79]\n"
    "v_add_f32_dpp v235, v227, v235 row_newbcast:3 row_mask:0xf bank_mask:0xf\n"
    "v_add_f32_dpp v236, v227, v236 row_newbcast:4 row_mask:0xf bank_mask:0xf\n"
    "v_mfma_f32_16x16x32_bf16 v[72:75], v[172:175], a[252:255], v[72:75]\n"
    "v_add_f32_dpp v237, v227, v237 row_newbcast:5 row_mask:0xf bank_mask:0xf\n"
    "v_add_f32_dpp v238, v227, v238 row_newbcast:6 row_mask:0xf bank_mask:0xf\n"
    "v_mfma_f32_16x16x32_bf16 v[72:75], v[168:171], a[248:251], v[72:75]\n"
    "v_add_f32_dpp v239, v227, v239 row_newbcast:7 row_mask:0xf bank_mask:0xf\n"
    "v_cvt_pk_bf16_f32 v232, v232, v233\n"
    "v_mfma_f32_16x16x32_bf16 v[68:71], v[168:171], a[224:227], v[68:71]\n"
    "ds_read_b128 v[164:167], v200 offset:11264\n"
    "v_cvt_pk_bf16_f32 v233, v234, v235\n"
    "v_mfma_f32_16x16x32_bf16 v[68:71], v[172:175], a[228:231], v[68:71]\n"
    "v_cvt_pk_bf16_f32 v234, v236, v237\n"
    "v_cvt_pk_bf16_f32 v235, v238, v239\n"
    "v_mfma_f32_16x16x32_bf16 v[64:67], v[172:175], a[204:207], v[64:67]\n"
    "v_mul_f32 v228, s68, v225\n"
    "v_fma_f32 v229, -v224, v228, s69\n"
    "v_mfma_f32_16x16x32_bf16 v[64:67], v[168:171], a[200:203], v[64:67]\n"
    "s_waitcnt lgkmcnt(2)\n"
    "v_cvt_f32_i32 v240, v240\n"
    "v_cvt_f32_i32 v241, v241\n"
    "v_mfma_f32_16x16x32_bf16 v[64:67], v[176:179], a[208:211], v[64:67]\n"
    "ds_read_b128 v[168:171], v200 offset:8192\n"
    "v_cvt_f32_i32 v242, v242\n"
    "v_mfma_f32_16x16x32_bf16 v[64:67], v[180:183], a[212:215], v[64:67]\n"
    "buffer_store_dwordx4 v[232:235], v211, s[36:39], s62 offen sc1\n"
    "v_cvt_f32_i32 v243, v243\n"
    "v_cvt_f32_i32 v244, v244\n"
    "v_cvt_f32_i32 v245, v245\n"
    "v_mfma_f32_16x16x32_bf16 v[68:71], v[180:183], a[236:239], v[68:71]\n"
    "v_cvt_f32_i32 v246, v246\n"
    "v_cvt_f32_i32 v247, v247\n"
    "v_mfma_f32_16x16x32_bf16 v[68:71], v[176:179], a[232:235], v[68:71]\n"
    "v_mul_f32_dpp v240, v228, v240 row_newbcast:0 row_mask:0xf bank_mask:0xf\n"
    "v_mul_f32_dpp v241, v228, v241 row_newbcast:1 row_mask:0xf bank_mask:0xf\n"
    "v_mfma_f32_16x16x32_bf16 v[72:75], v[176:179], v[0:3], v[72:75]\n"
    "ds_read_b128 v[172:175], v200 offset:9216\n"
    "v_mul_f32_dpp v242, v228, v242 row_newbcast:2 row_mask:0xf bank_mask:0xf\n"
    "v_mfma_f32_16x16x32_bf16 v[72:75], v[180:183], v[4:7], v[72:75]\n"
    "v_mul_f32_dpp v243, v228, v243 row_newbcast:3 row_mask:0xf bank_mask:0xf\n"
    "v_mul_f32_dpp v244, v228, v244 row_newbcast:4 row_mask:0xf bank_mask:0xf\n"
    "v_mfma_f32_16x16x32_bf16 v[76:79], v[180:183], v[28:31], v[76:79]\n"
    "v_mul_f32_dpp v245, v228, v245 row_newbcast:5 row_mask:0xf bank_mask:0xf\n"
    "v_mul_f32_dpp v246, v228, v246 row_newbcast:6 row_mask:0xf bank_mask:0xf\n"
    "v_mfma_f32_16x16x32_bf16 v[76:79], v[176:179], v[24:27], v[76:79]\n"
    "s_waitcnt lgkmcnt(2)\n"
    "v_mul_f32_dpp v247, v228, v247 row_newbcast:7 row_mask:0xf bank_mask:0xf\n"
    "v_add_f32_dpp v240, v229, v240 row_newbcast:0 row_mask:0xf bank_mask:0xf\n"
    "v_mfma_f32_16x16x32_bf16 v[92:95], v[160:163], v[24:27], v[108:111]\n"
    "ds_read_b128 v[176:179], v200 offset:6144\n"
    "v_add_f32_dpp v241, v229, v241 row_newbcast:1 row_mask:0xf bank_mask:0xf\n"
    "v_mfma_f32_16x16x32_bf16 v[92:95], v[164:167], v[28:31], v[92:95]\n"
    "v_add_f32_dpp v242, v229, v242 row_newbcast:2 row_mask:0xf bank_mask:0xf\n"
    "v_add_f32_dpp v243, v229, v243 row_newbcast:3 row_mask:0xf bank_mask:0xf\n"
    "v_mfma_f32_16x16x32_bf16 v[88:91], v[164:167], v[4:7], v[104:107]\n"
    "v_add_f32_dpp v244, v229, v244 row_newbcast:4 row_mask:0xf bank_mask:0xf\n"
    "v_add_f32_dpp v245, v229, v245 row_newbcast:5 row_mask:0xf bank_mask:0xf\n"
    "v_mfma_f32_16x16x32_bf16 v[88:91], v[160:163], v[0:3], v[88:91]\n"
    "v_add_f32_dpp v246, v229, v246 row_newbcast:6 row_mask:0xf bank_mask:0xf\n"
    "v_add_f32_dpp v247, v229, v247 row_newbcast:7 row_mask:0xf bank_mask:0xf\n"
    "v_mfma_f32_16x16x32_bf16 v[84:87], v[160:163], a[232:235], v[100:103]\n"
    "ds_read_b128 v[180:183], v200 offset:7168\n"
    "v_cvt_pk_bf16_f32 v141, v46, v47\n"
    "v_mfma_f32_16x16x32_bf16 v[84:87], v[164:167], a[236:239], v[84:87]\n"
    "v_cvt_pk_bf16_f32 v240, v240, v241\n"
    "v_cvt_pk_bf16_f32 v136, v40, v41\n"
    "v_mfma_f32_16x16x32_bf16 v[80:83], v[164:167], a[212:215], v[96:99]\n"
    "v_cvt_pk_bf16_f32 v241, v242, v243\n"
    "v_cvt_pk_bf16_f32 v137, v42, v43\n"
    "v_mfma_f32_16x16x32_bf16 v[80:83], v[160:163], a[208:211], v[80:83]\n"
    "s_waitcnt lgkmcnt(2)\n"
    "v_cvt_pk_bf16_f32 v242, v244, v245\n"
    "v_cvt_pk_bf16_f32 v132, v36, v37\n"
    "v_mfma_f32_16x16x32_bf16 v[80:83], v[168:171], a[200:203], v[80:83]\n"
    "ds_read_b128 v[160:163], v200 offset:12288\n"
    "v_cvt_pk_bf16_f32 v243, v246, v247\n"
    "v_mfma_f32_16x16x32_bf16 v[80:83], v[172:175], a[204:207], v[80:83]\n"
    "v_cvt_pk_bf16_f32 v133, v38, v39\n"
    "v_cvt_pk_bf16_f32 v128, v32, v33\n"
    "v_mfma_f32_16x16x32_bf16 v[84:87], v[172:175], a[228:231], v[84:87]\n"
    "v_cvt_pk_bf16_f32 v129, v34, v35\n"
    "v_cvt_pk_bf16_f32 v142, v60, v61\n"
    "v_mfma_f32_16x16x32_bf16 v[84:87], v[168:171], a[224:227], v[84:87]\n"
    "v_exp_f32 v64, v64\n"
    "v_mfma_f32_16x16x32_bf16 v[88:91], v[168:171], a[248:251], v[88:91]\n"
    "ds_read_b128 v[164:167], v200 offset:13312\n"
    "v_cvt_pk_bf16_f32 v143, v62, v63\n"
    "v_mfma_f32_16x16x32_bf16 v[88:91], v[172:175], a[252:255], v[88:91]\n"
    "v_exp_f32 v65, v65\n"
    "v_mfma_f32_16x16x32_bf16 v[92:95], v[172:175], v[20:23], v[92:95]\n"
    "v_exp_f32 v66, v66\n"
    "v_mfma_f32_16x16x32_bf16 v[92:95], v[168:171], v[16:19], v[92:95]\n"
    "s_waitcnt lgkmcnt(2)\n"
    "v_exp_f32 v67, v67\n"
    "v_mfma_f32_16x16x32_bf16 v[92:95], v[176:179], v[8:11], v[92:95]\n"
    "ds_read_b128 v[168:171], v200 offset:14336\n"
    "v_cvt_pk_bf16_f32 v138, v56, v57\n"
    "v_mfma_f32_16x16x32_bf16 v[92:95], v[180:183], v[12:15], v[92:95]\n"
    "buffer_store_dwordx4 v[240:243], v212, s[40:43], s63 offen sc1\n"
    "v_exp_f32 v68, v68\n"
    "v_cvt_pk_bf16_f32 v139, v58, v59\n"
    "v_mfma_f32_16x16x32_bf16 v[88:91], v[180:183], a[244:247], v[88:91]\n"
    "v_exp_f32 v69, v69\n"
    "v_mfma_f32_16x16x32_bf16 v[88:91], v[176:179], a[240:243], v[88:91]\n"
    "v_exp_f32 v70, v70\n"
    "v_mfma_f32_16x16x32_bf16 v[84:87], v[176:179], a[216:219], v[84:87]\n"
    "ds_read_b128 v[172:175], v200 offset:15360\n"
    "v_cvt_pk_bf16_f32 v134, v52, v53\n"
    "v_mfma_f32_16x16x32_bf16 v[84:87], v[180:183], a[220:223], v[84:87]\n"
    "v_exp_f32 v71, v71\n"
    "v_mfma_f32_16x16x32_bf16 v[80:83], v[180:183], a[196:199], v[80:83]\n"
    "v_exp_f32 v72, v72\n"
    "v_mfma_f32_16x16x32_bf16 v[80:83], v[176:179], a[192:195], v[80:83]\n"
    "s_waitcnt lgkmcnt(2)\n"
    "v_exp_f32 v73, v73\n"
    "v_mfma_f32_16x16x32_bf16 v[32:35], v[160:163], a[192:195], v[96:99]\n"
    "ds_read_b128 v[176:179], v200 offset:16384\n"
    "v_cvt_pk_bf16_f32 v135, v54, v55\n"
    "v_mfma_f32_16x16x32_bf16 v[32:35], v[164:167], a[196:199], v[32:35]\n"
    "v_exp_f32 v74, v74\n"
    "v_mfma_f32_16x16x32_bf16 v[36:39], v[164:167], a[220:223], v[100:103]\n"
    "v_exp_f32 v75, v75\n"
    "v_mfma_f32_16x16x32_bf16 v[36:39], v[160:163], a[216:219], v[36:39]\n"
    "v_exp_f32 v76, v76\n"
    "v_mfma_f32_16x16x32_bf16 v[40:43], v[160:163], a[240:243], v[104:107]\n"
    "ds_read_b128 v[180:183], v200 offset:17408\n"
    "v_cvt_pk_bf16_f32 v130, v48, v49\n"
    "v_mfma_f32_16x16x32_bf16 v[40:43], v[164:167], a[244:247], v[40:43]\n"
    "v_exp_f32 v77, v77\n"
    "v_mfma_f32_16x16x32_bf16 v[44:47], v[164:167], v[12:15], v[108:111]\n"
    "v_exp_f32 v78, v78\n"
    "v_mfma_f32_16x16x32_bf16 v[44:47], v[160:163], v[8:11], v[44:47]\n"
    "s_waitcnt lgkmcnt(2)\n"
    "v_cvt_pk_bf16_f32 v131, v50, v51\n"
    "v_cvt_pk_bf16_f32 v144, v64, v65\n"
    "v_mfma_f32_16x16x32_bf16 v[44:47], v[168:171], v[16:19], v[44:47]\n"
    "ds_read_b128 v[160:163], v200 offset:22528\n"
    "v_cvt_pk_bf16_f32 v145, v66, v67\n"
    "v_mfma_f32_16x16x32_bf16 v[44:47], v[172:175], v[20:23], v[44:47]\n"
    "v_exp_f32 v79, v79\n"
    "v_mfma_f32_16x16x32_bf16 v[40:43], v[172:175], a[252:255], v[40:43]\n"
    "v_exp_f32 v80, v80\n"
    "v_mfma_f32_16x16x32_bf16 v[40:43], v[168:171], a[248:251], v[40:43]\n"
    "v_exp_f32 v81, v81\n"
    "v_mfma_f32_16x16x32_bf16 v[36:39], v[168:171], a[224:227], v[36:39]\n"
    "ds_read_b128 v[164:167], v200 offset:23552\n"
    "v_cvt_pk_bf16_f32 v148, v68, v69\n"
    "v_mfma_f32_16x16x32_bf16 v[36:39], v[172:175], a[228:231], v[36:39]\n"
    "v_exp_f32 v82, v82\n"
    "v_mfma_f32_16x16x32_bf16 v[32:35], v[172:175], a[204:207], v[32:35]\n"
    "v_exp_f32 v83, v83\n"
    "v_mfma_f32_16x16x32_bf16 v[32:35], v[168:171], a[200:203], v[32:35]\n"
    "s_waitcnt lgkmcnt(2)\n"
    "v_exp_f32 v84, v84\n"
    "v_mfma_f32_16x16x32_bf16 v[32:35], v[176:179], a[208:211], v[32:35]\n"
    "ds_read_b128 v[168:171], v200 offset:20480\n"
    "v_cvt_pk_bf16_f32 v149, v70, v71\n"
    "v_mfma_f32_16x16x32_bf16 v[32:35], v[180:183], a[212:215], v[32:35]\n"
    "v_exp_f32 v85, v85\n"
    "v_mfma_f32_16x16x32_bf16 v[36:39], v[180:183], a[236:239], v[36:39]\n"
    "v_exp_f32 v86, v86\n"
    "v_mfma_f32_16x16x32_bf16 v[36:39], v[176:179], a[232:235], v[36:39]\n"
    "v_exp_f32 v87, v87\n"
    "v_mfma_f32_16x16x32_bf16 v[40:43], v[176:179], v[0:3], v[40:43]\n"
    "ds_read_b128 v[172:175], v200 offset:21504\n"
    "v_cvt_pk_bf16_f32 v152, v72, v73\n"
    "v_mfma_f32_16x16x32_bf16 v[40:43], v[180:183], v[4:7], v[40:43]\n"
    "v_exp_f32 v88, v88\n"
    "v_mfma_f32_16x16x32_bf16 v[44:47], v[180:183], v[28:31], v[44:47]\n"
    "v_exp_f32 v89, v89\n"
    "v_mfma_f32_16x16x32_bf16 v[44:47], v[176:179], v[24:27], v[44:47]\n"
    "s_waitcnt lgkmcnt(2)\n"
    "v_cvt_pk_bf16_f32 v153, v74, v75\n"
    "v_cvt_pk_bf16_f32 v156, v76, v77\n"
    "v_mfma_f32_16x16x32_bf16 v[60:63], v[160:163], v[24:27], v[108:111]\n"
    "s_mov_b32 m0, 0x6000\n"
    "ds_read_b128 v[176:179], v200 offset:18432\n"
    "v_cvt_pk_bf16_f32 v157, v78, v79\n"
    "v_mfma_f32_16x16x32_bf16 v[60:63], v[164:167], v[28:31], v[60:63]\n"
    "v_exp_f32 v90, v90\n"
    "v_mfma_f32_16x16x32_bf16 v[56:59], v[164:167], v[4:7], v[104:107]\n"
    "buffer_load_dwordx4 v200, s[36:39], s60 offen sc1 lds\n"
    "v_mfma_f32_16x16x32_bf16 v[56:59], v[160:163], v[0:3], v[56:59]\n"
    "v_exp_f32 v91, v91\n"
    "v_mfma_f32_16x16x32_bf16 v[52:55], v[160:163], a[232:235], v[100:103]\n"
    "ds_read_b128 v[180:183], v200 offset:19456\n"
    "v_cvt_pk_bf16_f32 v146, v80, v81\n"
    "v_mfma_f32_16x16x32_bf16 v[52:55], v[164:167], a[236:239], v[52:55]\n"
    "v_cvt_pk_bf16_f32 v147, v82, v83\n"
    "v_cvt_pk_bf16_f32 v150, v84, v85\n"
    "v_mfma_f32_16x16x32_bf16 v[48:51], v[164:167], a[212:215], v[96:99]\n"
    "buffer_load_dwordx4 v200, s[36:39], s60 offen offset:1024 sc1 lds\n"
    "v_mfma_f32_16x16x32_bf16 v[48:51], v[160:163], a[208:211], v[48:51]\n"
    "s_waitcnt lgkmcnt(2)\n"
    "v_exp_f32 v92, v92\n"
    "v_mfma_f32_16x16x32_bf16 v[48:51], v[168:171], a[200:203], v[48:51]\n"
    "ds_read_b128 v[160:163], v201 offset:24576\n"
    "v_cvt_pk_bf16_f32 v151, v86, v87\n"
    "v_mfma_f32_16x16x32_bf16 v[48:51], v[172:175], a[204:207], v[48:51]\n"
    "v_exp_f32 v93, v93\n"
    "v_mfma_f32_16x16x32_bf16 v[52:55], v[172:175], a[228:231], v[52:55]\n"
    "buffer_load_dwordx4 v200, s[36:39], s60 offen offset:2048 sc1 lds\n"
    "v_mfma_f32_16x16x32_bf16 v[52:55], v[168:171], a[224:227], v[52:55]\n"
    "v_exp_f32 v94, v94\n"
    "v_mfma_f32_16x16x32_bf16 v[56:59], v[168:171], a[248:251], v[56:59]\n"
    "s_add_u32 m0, s71, 0xc000\n"
    "ds_read_b128 v[164:167], v201 offset:25600\n"
    "v_cvt_pk_bf16_f32 v154, v88, v89\n"
    "v_mfma_f32_16x16x32_bf16 v[56:59], v[172:175], a[252:255], v[56:59]\n"
    "v_cvt_pk_bf16_f32 v155, v90, v91\n"
    "v_cvt_pk_bf16_f32 v158, v92, v93\n"
    "v_mfma_f32_16x16x32_bf16 v[60:63], v[172:175], v[20:23], v[60:63]\n"
    "buffer_load_dwordx4 v200, s[40:43], s61 offen sc1 lds\n"
    "v_mfma_f32_16x16x32_bf16 v[60:63], v[168:171], v[16:19], v[60:63]\n"
    "s_waitcnt lgkmcnt(2)\n"
    "v_exp_f32 v95, v95\n"
    "v_mfma_f32_16x16x32_bf16 v[60:63], v[176:179], v[8:11], v[60:63]\n"
    "ds_read_b128 v[168:171], v201 offset:26624\n"
    "v_cvt_pk_bf16_f32 v159, v94, v95\n"
    "v_mfma_f32_16x16x32_bf16 v[60:63], v[180:183], v[12:15], v[60:63]\n"
    "v_exp_f32 v32, v32\n"
    "v_mfma_f32_16x16x32_bf16 v[56:59], v[180:183], a[244:247], v[56:59]\n"
    "buffer_load_dwordx4 v200, s[40:43], s61 offen offset:1024 sc1 lds\n"
    "v_mfma_f32_16x16x32_bf16 v[56:59], v[176:179], a[240:243], v[56:59]\n"
    "v_exp_f32 v33, v33\n"
    "v_mfma_f32_16x16x32_bf16 v[52:55], v[176:179], a[216:219], v[52:55]\n"
    "ds_read_b128 v[172:175], v201 offset:27648\n"
    "v_add_f32 v113, v113, v64\n"
    "v_mfma_f32_16x16x32_bf16 v[52:55], v[180:183], a[220:223], v[52:55]\n"
    "v_exp_f32 v34, v34\n"
    "v_mfma_f32_16x16x32_bf16 v[48:51], v[180:183], a[196:199], v[48:51]\n"
    "buffer_load_dwordx4 v200, s[40:43], s61 offen offset:2048 sc1 lds\n"
    "v_mfma_f32_16x16x32_bf16 v[48:51], v[176:179], a[192:195], v[48:51]\n"
    "s_waitcnt lgkmcnt(2)\n"
    "v_exp_f32 v35, v35\n"
    "v_mfma_f32_16x16x32_bf16 a[0:3], v[160:163], v[128:131], a[0:3]\n"
    "ds_read_b128 v[176:179], v201 offset:28672\n"
    "v_add_f32 v114, v114, v65\n"
    "v_mfma_f32_16x16x32_bf16 a[0:3], v[164:167], v[144:147], a[0:3]\n"
    "v_exp_f32 v36, v36\n"
    "v_mfma_f32_16x16x32_bf16 a[4:7], v[160:163], v[132:135], a[4:7]\n"
    "buffer_load_dwordx4 v200, s[40:43], s61 offen offset:3072 sc1 lds\n"
    "v_mfma_f32_16x16x32_bf16 a[4:7], v[164:167], v[148:151], a[4:7]\n"
    "s_add_u32 s65, s61, 0x1000\n"
    "v_exp_f32 v37, v37\n"
    "v_mfma_f32_16x16x32_bf16 a[8:11], v[160:163], v[136:139], a[8:11]\n"
    "s_add_u32 m0, s71, 0xd000\n"
    "ds_read_b128 v[180:183], v201 offset:29696\n"
    "v_add_f32 v113, v113, v66\n"
    "v_mfma_f32_16x16x32_bf16 a[8:11], v[164:167], v[152:155], a[8:11]\n"
    "v_exp_f32 v38, v38\n"
    "v_mfma_f32_16x16x32_bf16 a[12:15], v[160:163], v[140:143], a[12:15]\n"
    "buffer_load_dwordx4 v200, s[40:43], s65 offen sc1 lds\n"
    "v_mfma_f32_16x16x32_bf16 a[12:15], v[164:167], v[156:159], a[12:15]\n"
    "s_waitcnt lgkmcnt(2)\n"
    "v_exp_f32 v39, v39\n"
    "v_mfma_f32_16x16x32_bf16 a[28:31], v[168:171], v[140:143], a[28:31]\n"
    "ds_read_b128 v[160:163], v201 offset:30720\n"
    "v_add_f32 v114, v114, v67\n"
    "v_mfma_f32_16x16x32_bf16 a[28:31], v[172:175], v[156:159], a[28:31]\n"
    "v_exp_f32 v40, v40\n"
    "v_mfma_f32_16x16x32_bf16 a[24:27], v[172:175], v[152:155], a[24:27]\n"
    "buffer_load_dwordx4 v200, s[40:43], s65 offen offset:1024 sc1 lds\n"
    "v_mfma_f32_16x16x32_bf16 a[24:27], v[168:171], v[136:139], a[24:27]\n"
    "v_exp_f32 v41, v41\n"
    "v_mfma_f32_16x16x32_bf16 a[20:23], v[168:171], v[132:135], a[20:23]\n"
    "ds_read_b128 v[164:167], v201 offset:31744\n"
    "v_add_f32 v117, v117, v68\n"
    "v_mfma_f32_16x16x32_bf16 a[20:23], v[172:175], v[148:151], a[20:23]\n"
    "v_exp_f32 v42, v42\n"
    "v_mfma_f32_16x16x32_bf16 a[16:19], v[172:175], v[144:147], a[16:19]\n"
    "v_exp_f32 v43, v43\n"
    "v_mfma_f32_16x16x32_bf16 a[16:19], v[168:171], v[128:131], a[16:19]\n"
    "s_waitcnt lgkmcnt(2)\n"
    "v_exp_f32 v44, v44\n"
    "v_mfma_f32_16x16x32_bf16 a[32:35], v[176:179], v[128:131], a[32:35]\n"
    "ds_read_b128 v[168:171], v201 offset:32768\n"
    "v_add_f32 v118, v118, v69\n"
    "v_mfma_f32_16x16x32_bf16 a[32:35], v[180:183], v[144:147], a[32:35]\n"
    "buffer_load_dwordx2 v[218:219], v213, s[44:47], 0 offen sc1\n"
    "v_mfma_f32_16x16x32_bf16 a[36:39], v[180:183], v[148:151], a[36:39]\n"
    "v_exp_f32 v45, v45\n"
    "v_mfma_f32_16x16x32_bf16 a[36:39], v[176:179], v[132:135], a[36:39]\n"
    "v_exp_f32 v46, v46\n"
    "v_mfma_f32_16x16x32_bf16 a[40:43], v[176:179], v[136:139], a[40:43]\n"
    "ds_read_b128 v[172:175], v201 offset:33792\n"
    "v_add_f32 v117, v117, v70\n"
    "v_mfma_f32_16x16x32_bf16 a[40:43], v[180:183], v[152:155], a[40:43]\n"
    "v_exp_f32 v47, v47\n"
    "v_mfma_f32_16x16x32_bf16 a[44:47], v[180:183], v[156:159], a[44:47]\n"
    "v_exp_f32 v48, v48\n"
    "v_mfma_f32_16x16x32_bf16 a[44:47], v[176:179], v[140:143], a[44:47]\n"
    "s_waitcnt lgkmcnt(2)\n"
    "v_exp_f32 v49, v49\n"
    "v_mfma_f32_16x16x32_bf16 a[60:63], v[160:163], v[140:143], a[60:63]\n"
    "ds_read_b128 v[176:179], v201 offset:34816\n"
    "v_add_f32 v113, v113, v32\n"
    "v_mfma_f32_16x16x32_bf16 a[60:63], v[164:167], v[156:159], a[60:63]\n"
    "v_exp_f32 v50, v50\n"
    "v_mfma_f32_16x16x32_bf16 a[56:59], v[164:167], v[152:155], a[56:59]\n"
    "v_exp_f32 v51, v51\n"
    "v_mfma_f32_16x16x32_bf16 a[56:59], v[160:163], v[136:139], a[56:59]\n"
    "s_add_u32 m0, s51, s95\n"
    "v_exp_f32 v52, v52\n"
    "v_mfma_f32_16x16x32_bf16 a[52:55], v[160:163], v[132:135], a[52:55]\n"
    "ds_read_b128 v[180:183], v201 offset:35840\n"
    "v_add_f32 v118, v118, v71\n"
    "v_mfma_f32_16x16x32_bf16 a[52:55], v[164:167], v[148:151], a[52:55]\n"
    "buffer_load_dwordx4 v202, s[32:35], s64 offen lds\n"
    "v_mfma_f32_16x16x32_bf16 a[48:51], v[164:167], v[144:147], a[48:51]\n"
    "v_add_f32 v114, v114, v33\n"
    "v_add_f32 v121, v121, v72\n"
    "v_mfma_f32_16x16x32_bf16 a[48:51], v[160:163], v[128:131], a[48:51]\n"
    "s_waitcnt lgkmcnt(2)\n"
    "v_add_f32 v113, v113, v34\n"
    "v_add_f32 v122, v122, v73\n"
    "v_mfma_f32_16x16x32_bf16 a[64:67], v[168:171], v[128:131], a[64:67]\n"
    "ds_read_b128 v[160:163], v201 offset:36864\n"
    "v_add_f32 v114, v114, v35\n"
    "v_mfma_f32_16x16x32_bf16 a[64:67], v[172:175], v[144:147], a[64:67]\n"
    "v_exp_f32 v53, v53\n"
    "v_mfma_f32_16x16x32_bf16 a[68:71], v[172:175], v[148:151], a[68:71]\n"
    "buffer_load_dwordx4 v203, s[32:35], s64 offen offset:1024 lds\n"
    "v_mfma_f32_16x16x32_bf16 a[68:71], v[168:171], v[132:135], a[68:71]\n"
    "v_add_f32 v121, v121, v74\n"
    "v_add_f32 v117, v117, v36\n"
    "v_mfma_f32_16x16x32_bf16 a[72:75], v[168:171], v[136:139], a[72:75]\n"
    "ds_read_b128 v[164:167], v201 offset:37888\n"
    "v_add_f32 v122, v122, v75\n"
    "v_mfma_f32_16x16x32_bf16 a[72:75], v[172:175], v[152:155], a[72:75]\n"
    "v_exp_f32 v54, v54\n"
    "v_mfma_f32_16x16x32_bf16 a[76:79], v[172:175], v[156:159], a[76:79]\n"
    "v_add_f32 v118, v118, v37\n"
    "v_add_f32 v125, v125, v76\n"
    "v_mfma_f32_16x16x32_bf16 a[76:79], v[168:171], v[140:143], a[76:79]\n"
    "s_waitcnt lgkmcnt(2)\n"
    "buffer_load_dwordx4 v204, s[32:35], s64 offen offset:2048 lds\n"
    "v_mfma_f32_16x16x32_bf16 a[92:95], v[176:179], v[140:143], a[92:95]\n"
    "ds_read_b128 v[168:171], v201 offset:38912\n"
    "v_add_f32 v117, v117, v38\n"
    "v_mfma_f32_16x16x32_bf16 a[92:95], v[180:183], v[156:159], a[92:95]\n"
    "v_exp_f32 v55, v55\n"
    "v_mfma_f32_16x16x32_bf16 a[88:91], v[180:183], v[152:155], a[88:91]\n"
    "v_add_f32 v126, v126, v77\n"
    "v_add_f32 v118, v118, v39\n"
    "v_mfma_f32_16x16x32_bf16 a[88:91], v[176:179], v[136:139], a[88:91]\n"
    "v_add_f32 v125, v125, v78\n"
    "v_add_f32 v121, v121, v40\n"
    "v_mfma_f32_16x16x32_bf16 a[84:87], v[176:179], v[132:135], a[84:87]\n"
    "s_mov_b32 s95, s94\n"
    "ds_read_b128 v[172:175], v201 offset:39936\n"
    "v_add_f32 v126, v126, v79\n"
    "v_mfma_f32_16x16x32_bf16 a[84:87], v[180:183], v[148:151], a[84:87]\n"
    "s_sub_u32 s94, s94, 0x3000\n"
    "v_exp_f32 v56, v56\n"
    "v_mfma_f32_16x16x32_bf16 a[80:83], v[180:183], v[144:147], a[80:83]\n"
    "s_min_u32 s94, s94, 0x6000\n"
    "v_add_f32 v122, v122, v41\n"
    "v_add_f32 v113, v113, v80\n"
    "v_mfma_f32_16x16x32_bf16 a[80:83], v[176:179], v[128:131], a[80:83]\n"
    "s_waitcnt lgkmcnt(2)\n"
    "v_add_f32 v121, v121, v42\n"
    "v_add_f32 v114, v114, v81\n"
    "v_mfma_f32_16x16x32_bf16 a[96:99], v[160:163], v[128:131], a[96:99]\n"
    "s_sub_u32 s93, s94, s95\n"
    "ds_read_b128 v[176:179], v201 offset:40960\n"
    "v_add_f32 v122, v122, v43\n"
    "v_mfma_f32_16x16x32_bf16 a[96:99], v[164:167], v[144:147], a[96:99]\n"
    "v_exp_f32 v57, v57\n"
    "v_mfma_f32_16x16x32_bf16 a[100:103], v[164:167], v[148:151], a[100:103]\n"
    "v_add_f32 v113, v113, v82\n"
    "v_add_f32 v125, v125, v44\n"
    "v_mfma_f32_16x16x32_bf16 a[100:103], v[160:163], v[132:135], a[100:103]\n"
    "v_exp_f32 v58, v58\n"
    "v_mfma_f32_16x16x32_bf16 a[104:107], v[160:163], v[136:139], a[104:107]\n"
    "ds_read_b128 v[180:183], v201 offset:41984\n"
    "v_add_f32 v114, v114, v83\n"
    "v_mfma_f32_16x16x32_bf16 a[104:107], v[164:167], v[152:155], a[104:107]\n"
    "v_add_f32 v126, v126, v45\n"
    "v_add_f32 v117, v117, v84\n"
    "v_mfma_f32_16x16x32_bf16 a[108:111], v[164:167], v[156:159], a[108:111]\n"
    "v_add_f32 v125, v125, v46\n"
    "v_add_f32 v118, v118, v85\n"
    "v_mfma_f32_16x16x32_bf16 a[108:111], v[160:163], v[140:143], a[108:111]\n"
    "s_waitcnt lgkmcnt(2)\n"
    "v_exp_f32 v59, v59\n"
    "v_mfma_f32_16x16x32_bf16 a[124:127], v[168:171], v[140:143], a[124:127]\n"
    "ds_read_b128 v[160:163], v201 offset:43008\n"
    "v_add_f32 v126, v126, v47\n"
    "v_mfma_f32_16x16x32_bf16 a[124:127], v[172:175], v[156:159], a[124:127]\n"
    "v_add_f32 v117, v117, v86\n"
    "v_add_f32 v113, v113, v48\n"
    "v_mfma_f32_16x16x32_bf16 a[120:123], v[172:175], v[152:155], a[120:123]\n"
    "v_exp_f32 v60, v60\n"
    "v_mfma_f32_16x16x32_bf16 a[120:123], v[168:171], v[136:139], a[120:123]\n"
    "v_add_u32 v205, s93, v205\n"
    "v_add_f32 v118, v118, v87\n"
    "v_mfma_f32_16x16x32_bf16 a[116:119], v[168:171], v[132:135], a[116:119]\n"
    "ds_read_b128 v[164:167], v201 offset:44032\n"
    "v_add_f32 v114, v114, v49\n"
    "v_mfma_f32_16x16x32_bf16 a[116:119], v[172:175], v[148:151], a[116:119]\n"
    "v_add_u32 v206, s93, v206\n"
    "v_add_f32 v121, v121, v88\n"
    "v_mfma_f32_16x16x32_bf16 a[112:115], v[172:175], v[144:147], a[112:115]\n"
    "v_add_f32 v113, v113, v50\n"
    "v_add_u32 v220, 1, v220\n"
    "v_mfma_f32_16x16x32_bf16 a[112:115], v[168:171], v[128:131], a[112:115]\n"
    "s_waitcnt lgkmcnt(2)\n"
    "v_exp_f32 v61, v61\n"
    "v_mfma_f32_16x16x32_bf16 a[128:131], v[176:179], v[128:131], a[128:131]\n"
    "s_add_u32 s64, s64, 0xc000\n"
    "ds_read_b128 v[168:171], v201 offset:45056\n"
    "v_add_u32 v207, s93, v207\n"
    "v_mfma_f32_16x16x32_bf16 a[128:131], v[180:183], v[144:147], a[128:131]\n"
    "s_add_u32 s62, s62, 0x6000\n"
    "v_add_f32 v122, v122, v89\n"
    "v_add_f32 v114, v114, v51\n"
    "v_mfma_f32_16x16x32_bf16 a[132:135], v[180:183], v[148:151], a[132:135]\n"
    "s_add_u32 s63, s63, 0x6000\n"
    "v_add_f32 v121, v121, v90\n"
    "v_add_f32 v117, v117, v52\n"
    "v_mfma_f32_16x16x32_bf16 a[132:135], v[176:179], v[132:135], a[132:135]\n"
    "s_add_u32 s60, s60, 0x6000\n"
    "v_exp_f32 v62, v62\n"
    "v_mfma_f32_16x16x32_bf16 a[136:139], v[176:179], v[136:139], a[136:139]\n"
    "s_add_u32 s61, s61, 0x6000\n"
    "ds_read_b128 v[172:175], v201 offset:46080\n"
    "v_add_f32 v122, v122, v91\n"
    "v_mfma_f32_16x16x32_bf16 a[136:139], v[180:183], v[152:155], a[136:139]\n"
    "s_add_u32 s66, s66, s74\n"
    "v_add_f32 v118, v118, v53\n"
    "v_add_f32 v125, v125, v92\n"
    "v_mfma_f32_16x16x32_bf16 a[140:143], v[180:183], v[156:159], a[140:143]\n"
    "s_min_u32 s66, s66, 33\n"
    "v_add_f32 v117, v117, v54\n"
    "v_add_f32 v126, v126, v93\n"
    "v_mfma_f32_16x16x32_bf16 a[140:143], v[176:179], v[140:143], a[140:143]\n"
    "s_waitcnt lgkmcnt(2)\n"
    "v_exp_f32 v63, v63\n"
    "v_mfma_f32_16x16x32_bf16 a[156:159], v[160:163], v[140:143], a[156:159]\n"
    "ds_read_b128 v[176:179], v201 offset:47104\n"
    "v_add_f32 v118, v118, v55\n"
    "v_mfma_f32_16x16x32_bf16 a[156:159], v[164:167], v[156:159], a[156:159]\n"
    "s_waitcnt vmcnt(3)\n"
    "ds_read_b128 v[180:183], v201 offset:48128\n"
    "v_add_f32 v125, v125, v94\n"
    "v_mfma_f32_16x16x32_bf16 a[152:155], v[164:167], v[152:155], a[152:155]\n"
    "v_cmp_ne_u32 s[80:81], s75, v219\n"
    "v_add_f32 v121, v121, v56\n"
    "v_mfma_f32_16x16x32_bf16 a[152:155], v[160:163], v[136:139], a[152:155]\n"
    "v_cmp_gt_u32 vcc, s66, v218\n"
    "v_add_f32 v126, v126, v95\n"
    "v_mfma_f32_16x16x32_bf16 a[148:151], v[160:163], v[132:135], a[148:151]\n"
    "v_add_f32 v122, v122, v57\n"
    "v_mfma_f32_16x16x32_bf16 a[148:151], v[164:167], v[148:151], a[148:151]\n"
    "s_or_b64 vcc, vcc, s[80:81]\n"
    "v_mfma_f32_16x16x32_bf16 a[144:147], v[164:167], v[144:147], a[144:147]\n"
    "s_cbranch_vccnz LSPRE3\n"
    "LSPB3:\n"
    "v_mfma_f32_16x16x32_bf16 a[144:147], v[160:163], v[128:131], a[144:147]\n"
    "s_waitcnt lgkmcnt(0)\n"
    "v_mfma_f32_16x16x32_bf16 a[160:163], v[168:171], v[128:131], a[160:163]\n"
    "s_barrier\n"
    "LMID:\n"
    "v_mfma_f32_16x16x32_bf16 a[160:163], v[172:175], v[144:147], a[160:163]\n"
    "buffer_store_dwordx2 v[220:221], v214, s[44:47], 0 offen sc1\n"
    "v_mfma_f32_16x16x32_bf16 a[164:167], v[172:175], v[148:151], a[164:167]\n"
    "ds_read_b128 v[160:163], v200 offset:24576\n"
    "ds_read_u16_d16_hi v223, v209 offset:6336\n"
    "v_mfma_f32_16x16x32_bf16 a[164:167], v[168:171], v[132:135], a[164:167]\n"
    "ds_read_u16_d16_hi v222, v209 offset:192\n"
    "v_add_f32 v121, v121, v58\n"
    "v_mfma_f32_16x16x32_bf16 a[168:171], v[168:171], v[136:139], a[168:171]\n"
    "ds_read_b128 v[164:167], v200 offset:25600\n"
    "ds_read_b128 v[232:235], v205 offset:24576\n"
    "v_mfma_f32_16x16x32_bf16 a[168:171], v[172:175], v[152:155], a[168:171]\n"
    "v_add_f32 v122, v122, v59\n"
    "ds_read_b128 v[236:239], v206 offset:24576\n"
    "v_mfma_f32_16x16x32_bf16 a[172:175], v[172:175], v[156:159], a[172:175]\n"
    "v_add_f32 v125, v125, v60\n"
    "v_add_f32 v126, v126, v61\n"
    "v_mfma_f32_16x16x32_bf16 a[172:175], v[168:171], v[140:143], a[172:175]\n"
    "v_add_f32 v125, v125, v62\n"
    "v_add_f32 v126, v126, v63\n"
    "v_mfma_f32_16x16x32_bf16 a[188:191], v[176:179], v[140:143], a[188:191]\n"
    "ds_read_b128 v[168:171], v200 offset:26624\n"
    "ds_read_u16_d16_hi v225, v210 offset:528\n"
    "v_mfma_f32_16x16x32_bf16 a[188:191], v[180:183], v[156:159], a[188:191]\n"
    "ds_read_u16_d16_hi v224, v210 offset:16\n"
    "ds_read2st64_b32 v[240:241], v207 offset0:96 offset1:99\n"
    "v_mfma_f32_16x16x32_bf16 a[184:187], v[176:179], v[136:139], a[184:187]\n"
    "ds_read2st64_b32 v[242:243], v207 offset0:102 offset1:105\n"
    "ds_read2st64_b32 v[244:245], v207 offset0:108 offset1:111\n"
    "v_mfma_f32_16x16x32_bf16 a[184:187], v[180:183], v[152:155], a[184:187]\n"
    "ds_read2st64_b32 v[246:247], v207 offset0:114 offset1:117\n"
    "v_cvt_pk_bf16_f32 v140, v44, v45\n"
    "v_mfma_f32_16x16x32_bf16 a[180:183], v[176:179], v[132:135], a[180:183]\n"
    "s_add_u32 s59, s59, 1\n"
    "ds_read_b128 v[172:175], v200 offset:27648\n"
    "s_waitcnt lgkmcnt(8)\n"
    "v_mfma_f32_16x16x32_bf16 a[180:183], v[180:183], v[148:151], a[180:183]\n"
    "v_mul_f32 v226, s67, v223\n"
    "v_fma_f32 v227, -v222, v226, v217\n"
    "v_mfma_f32_16x16x32_bf16 a[176:179], v[176:179], v[128:131], a[176:179]\n"
    "s_cmp_lt_u32 s59, 15\n"
    "v_cvt_f32_i32 v232, v232\n"
    "v_cvt_f32_i32 v233, v233\n"
    "v_mfma_f32_16x16x32_bf16 a[176:179], v[180:183], v[144:147], a[176:179]\n"
    "s_cbranch_scc1 LLOOP\n"
    "LTAIL:\n"
    "s_waitcnt lgkmcnt(0)\n"
    "s_nop 1\n"
    "v_mfma_f32_16x16x32_bf16 v[64:67], v[160:163], a[192:195], v[96:99]\n"
    "ds_read_b128 v[176:179], v200 offset:28672\n"
    "v_cvt_pk_bf16_f32 v128, v32, v33\n"
    "v_mfma_f32_16x16x32_bf16 v[64:67], v[164:167], a[196:199], v[64:67]\n"
    "s_add_u32 m0, s71, 0\n"
    "v_cvt_pk_bf16_f32 v129, v34, v35\n"
    "v_cvt_pk_bf16_f32 v132, v36, v37\n"
    "v_mfma_f32_16x16x32_bf16 v[68:71], v[164:167], a[220:223], v[100:103]\n"
    "buffer_load_dwordx4 v200, s[36:39], s60 offen sc1 lds\n"
    "v_mfma_f32_16x16x32_bf16 v[68:71], v[160:163], a[216:219], v[68:71]\n"
    "v_cvt_pk_bf16_f32 v133, v38, v39\n"
    "v_cvt_pk_bf16_f32 v136, v40, v41\n"
    "v_mfma_f32_16x16x32_bf16 v[72:75], v[160:163], a[240:243], v[104:107]\n"
    "ds_read_b128 v[180:183], v200 offset:29696\n"
    "v_cvt_pk_bf16_f32 v137, v42, v43\n"
    "v_mfma_f32_16x16x32_bf16 v[72:75], v[164:167], a[244:247], v[72:75]\n"
    "v_cvt_pk_bf16_f32 v141, v46, v47\n"
    "v_cvt_pk_bf16_f32 v130, v48, v49\n"
    "v_mfma_f32_16x16x32_bf16 v[76:79], v[164:167], v[12:15], v[108:111]\n"
    "buffer_load_dwordx4 v200, s[36:39], s60 offen offset:1024 sc1 lds\n"
    "v_mfma_f32_16x16x32_bf16 v[76:79], v[160:163], v[8:11], v[76:79]\n"
    "v_cvt_pk_bf16_f32 v131, v50, v51\n"
    "v_cvt_pk_bf16_f32 v134, v52, v53\n"
    "v_mfma_f32_16x16x32_bf16 v[76:79], v[168:171], v[16:19], v[76:79]\n"
    "ds_read_b128 v[160:163], v200 offset:34816\n"
    "v_cvt_pk_bf16_f32 v135, v54, v55\n"
    "v_mfma_f32_16x16x32_bf16 v[76:79], v[172:175], v[20:23], v[76:79]\n"
    "v_cvt_pk_bf16_f32 v138, v56, v57\n"
    "v_cvt_pk_bf16_f32 v139, v58, v59\n"
    "v_mfma_f32_16x16x32_bf16 v[72:75], v[172:175], a[252:255], v[72:75]\n"
    "buffer_load_dwordx4 v200, s[36:39], s60 offen offset:2048 sc1 lds\n"
    "v_mfma_f32_16x16x32_bf16 v[72:75], v[168:171], a[248:251], v[72:75]\n"
    "v_cvt_pk_bf16_f32 v142, v60, v61\n"
    "v_cvt_pk_bf16_f32 v143, v62, v63\n"
    "v_mfma_f32_16x16x32_bf16 v[68:71], v[168:171], a[224:227], v[68:71]\n"
    "ds_read_b128 v[164:167], v200 offset:35840\n"
    "v_add_f32 v113, v113, v114\n"
    "v_mfma_f32_16x16x32_bf16 v[68:71], v[172:175], a[228:231], v[68:71]\n"
    "v_add_f32 v117, v117, v118\n"
    "v_add_f32 v121, v121, v122\n"
    "v_mfma_f32_16x16x32_bf16 v[64:67], v[172:175], a[204:207], v[64:67]\n"
    "buffer_load_dwordx4 v200, s[36:39], s60 offen offset:3072 sc1 lds\n"
    "v_mfma_f32_16x16x32_bf16 v[64:67], v[168:171], a[200:203], v[64:67]\n"
    "s_waitcnt lgkmcnt(2)\n"
    "v_add_f32 v125, v125, v126\n"
    "v_mov_b32 v115, v113\n"
    "v_mfma_f32_16x16x32_bf16 v[64:67], v[176:179], a[208:211], v[64:67]\n"
    "s_add_u32 s65, s60, 0x1000\n"
    "ds_read_b128 v[168:171], v200 offset:32768\n"
    "v_mov_b32 v119, v117\n"
    "v_mfma_f32_16x16x32_bf16 v[64:67], v[180:183], a[212:215], v[64:67]\n"
    "s_add_u32 m0, s71, 0x1000\n"
    "v_mov_b32 v123, v121\n"
    "v_mov_b32 v127, v125\n"
    "v_mfma_f32_16x16x32_bf16 v[68:71], v[180:183], a[236:239], v[68:71]\n"
    "buffer_load_dwordx4 v200, s[36:39], s65 offen sc1 lds\n"
    "v_mfma_f32_16x16x32_bf16 v[68:71], v[176:179], a[232:235], v[68:71]\n"
    "v_permlane32_swap_b32 v113, v115\n"
    "v_permlane32_swap_b32 v117, v119\n"
    "v_mfma_f32_16x16x32_bf16 v[72:75], v[176:179], v[0:3], v[72:75]\n"
    "ds_read_b128 v[172:175], v200 offset:33792\n"
    "v_permlane32_swap_b32 v121, v123\n"
    "v_mfma_f32_16x16x32_bf16 v[72:75], v[180:183], v[4:7], v[72:75]\n"
    "v_permlane32_swap_b32 v125, v127\n"
    "v_add_f32 v113, v113, v115\n"
    "v_mfma_f32_16x16x32_bf16 v[76:79], v[180:183], v[28:31], v[76:79]\n"
    "buffer_load_dwordx4 v200, s[36:39], s65 offen offset:1024 sc1 lds\n"
    "v_mfma_f32_16x16x32_bf16 v[76:79], v[176:179], v[24:27], v[76:79]\n"
    "s_waitcnt lgkmcnt(2)\n"
    "v_add_f32 v117, v117, v119\n"
    "v_add_f32 v121, v121, v123\n"
    "v_mfma_f32_16x16x32_bf16 v[92:95], v[160:163], v[24:27], v[108:111]\n"
    "ds_read_b128 v[176:179], v200 offset:30720\n"
    "v_add_f32 v125, v125, v127\n"
    "v_mfma_f32_16x16x32_bf16 v[92:95], v[164:167], v[28:31], v[92:95]\n"
    "s_add_u32 m0, s71, 0x12000\n"
    "v_exp_f32 v64, v64\n"
    "v_mfma_f32_16x16x32_bf16 v[88:91], v[164:167], v[4:7], v[104:107]\n"
    "buffer_load_dwordx4 v200, s[40:43], s61 offen sc1 lds\n"
    "v_mfma_f32_16x16x32_bf16 v[88:91], v[160:163], v[0:3], v[88:91]\n"
    "v_exp_f32 v65, v65\n"
    "v_mfma_f32_16x16x32_bf16 v[84:87], v[160:163], a[232:235], v[100:103]\n"
    "ds_read_b128 v[180:183], v200 offset:31744\n"
    "v_cvt_pk_bf16_f32 v144, v64, v65\n"
    "v_mfma_f32_16x16x32_bf16 v[84:87], v[164:167], a[236:239], v[84:87]\n"
    "v_exp_f32 v66, v66\n"
    "v_mfma_f32_16x16x32_bf16 v[80:83], v[164:167], a[212:215], v[96:99]\n"
    "buffer_load_dwordx4 v200, s[40:43], s61 offen offset:1024 sc1 lds\n"
    "v_mfma_f32_16x16x32_bf16 v[80:83], v[160:163], a[208:211], v[80:83]\n"
    "s_waitcnt lgkmcnt(2)\n"
    "v_exp_f32 v67, v67\n"
    "v_mfma_f32_16x16x32_bf16 v[80:83], v[168:171], a[200:203], v[80:83]\n"
    "ds_read_b128 v[160:163], v200 offset:36864\n"
    "v_cvt_pk_bf16_f32 v145, v66, v67\n"
    "v_mfma_f32_16x16x32_bf16 v[80:83], v[172:175], a[204:207], v[80:83]\n"
    "v_exp_f32 v68, v68\n"
    "v_mfma_f32_16x16x32_bf16 v[84:87], v[172:175], a[228:231], v[84:87]\n"
    "buffer_load_dwordx4 v200, s[40:43], s61 offen offset:2048 sc1 lds\n"
    "v_mfma_f32_16x16x32_bf16 v[84:87], v[168:171], a[224:227], v[84:87]\n"
    "v_exp_f32 v69, v69\n"
    "v_mfma_f32_16x16x32_bf16 v[88:91], v[168:171], a[248:251], v[88:91]\n"
    "ds_read_b128 v[164:167], v200 offset:37888\n"
    "v_cvt_pk_bf16_f32 v148, v68, v69\n"
    "v_mfma_f32_16x16x32_bf16 v[88:91], v[172:175], a[252:255], v[88:91]\n"
    "v_exp_f32 v70, v70\n"
    "v_mfma_f32_16x16x32_bf16 v[92:95], v[172:175], v[20:23], v[92:95]\n"
    "buffer_load_dwordx4 v200, s[40:43], s61 offen offset:3072 sc1 lds\n"
    "v_mfma_f32_16x16x32_bf16 v[92:95], v[168:171], v[16:19], v[92:95]\n"
    "s_waitcnt lgkmcnt(2)\n"
    "v_exp_f32 v71, v71\n"
    "v_mfma_f32_16x16x32_bf16 v[92:95], v[176:179], v[8:11], v[92:95]\n"
    "s_add_u32 s65, s61, 0x1000\n"
    "ds_read_b128 v[168:171], v200 offset:38912\n"
    "v_cvt_pk_bf16_f32 v149, v70, v71\n"
    "v_mfma_f32_16x16x32_bf16 v[92:95], v[180:183], v[12:15], v[92:95]\n"
    "s_add_u32 m0, s71, 0x13000\n"
    "v_exp_f32 v72, v72\n"
    "v_mfma_f32_16x16x32_bf16 v[88:91], v[180:183], a[244:247], v[88:91]\n"
    "buffer_load_dwordx4 v200, s[40:43], s65 offen sc1 lds\n"
    "v_mfma_f32_16x16x32_bf16 v[88:91], v[176:179], a[240:243], v[88:91]\n"
    "v_exp_f32 v73, v73\n"
    "v_mfma_f32_16x16x32_bf16 v[84:87], v[176:179], a[216:219], v[84:87]\n"
    "ds_read_b128 v[172:175], v200 offset:39936\n"
    "v_cvt_pk_bf16_f32 v152, v72, v73\n"
    "v_mfma_f32_16x16x32_bf16 v[84:87], v[180:183], a[220:223], v[84:87]\n"
    "v_exp_f32 v74, v74\n"
    "v_mfma_f32_16x16x32_bf16 v[80:83], v[180:183], a[196:199], v[80:83]\n"
    "buffer_load_dwordx4 v200, s[40:43], s65 offen offset:1024 sc1 lds\n"
    "v_mfma_f32_16x16x32_bf16 v[80:83], v[176:179], a[192:195], v[80:83]\n"
    "s_waitcnt lgkmcnt(2)\n"
    "v_exp_f32 v75, v75\n"
    "v_mfma_f32_16x16x32_bf16 v[32:35], v[160:163], a[192:195], v[96:99]\n"
    "ds_read_b128 v[176:179], v200 offset:40960\n"
    "v_cvt_pk_bf16_f32 v153, v74, v75\n"
    "v_mfma_f32_16x16x32_bf16 v[32:35], v[164:167], a[196:199], v[32:35]\n"
    "v_exp_f32 v76, v76\n"
    "v_mfma_f32_16x16x32_bf16 v[36:39], v[164:167], a[220:223], v[100:103]\n"
    "v_exp_f32 v77, v77\n"
    "v_mfma_f32_16x16x32_bf16 v[36:39], v[160:163], a[216:219], v[36:39]\n"
    "v_exp_f32 v78, v78\n"
    "v_mfma_f32_16x16x32_bf16 v[40:43], v[160:163], a[240:243], v[104:107]\n"
    "ds_read_b128 v[180:183], v200 offset:41984\n"
    "v_cvt_pk_bf16_f32 v156, v76, v77\n"
    "v_mfma_f32_16x16x32_bf16 v[40:43], v[164:167], a[244:247], v[40:43]\n"
    "v_exp_f32 v79, v79\n"
    "v_mfma_f32_16x16x32_bf16 v[44:47], v[164:167], v[12:15], v[108:111]\n"
    "v_exp_f32 v80, v80\n"
    "v_mfma_f32_16x16x32_bf16 v[44:47], v[160:163], v[8:11], v[44:47]\n"
    "s_waitcnt lgkmcnt(2)\n"
    "v_exp_f32 v81, v81\n"
    "v_mfma_f32_16x16x32_bf16 v[44:47], v[168:171], v[16:19], v[44:47]\n"
    "ds_read_b128 v[160:163], v200 offset:47104\n"
    "v_cvt_pk_bf16_f32 v157, v78, v79\n"
    "v_mfma_f32_16x16x32_bf16 v[44:47], v[172:175], v[20:23], v[44:47]\n"
    "v_exp_f32 v82, v82\n"
    "v_mfma_f32_16x16x32_bf16 v[40:43], v[172:175], a[252:255], v[40:43]\n"
    "v_exp_f32 v83, v83\n"
    "v_mfma_f32_16x16x32_bf16 v[40:43], v[168:171], a[248:251], v[40:43]\n"
    "v_exp_f32 v84, v84\n"
    "v_mfma_f32_16x16x32_bf16 v[36:39], v[168:171], a[224:227], v[36:39]\n"
    "ds_read_b128 v[164:167], v200 offset:48128\n"
    "v_cvt_pk_bf16_f32 v146, v80, v81\n"
    "v_mfma_f32_16x16x32_bf16 v[36:39], v[172:175], a[228:231], v[36:39]\n"
    "v_exp_f32 v85, v85\n"
    "v_mfma_f32_16x16x32_bf16 v[32:35], v[172:175], a[204:207], v[32:35]\n"
    "v_exp_f32 v86, v86\n"
    "v_mfma_f32_16x16x32_bf16 v[32:35], v[168:171], a[200:203], v[32:35]\n"
    "s_waitcnt lgkmcnt(2)\n"
    "v_exp_f32 v87, v87\n"
    "v_mfma_f32_16x16x32_bf16 v[32:35], v[176:179], a[208:211], v[32:35]\n"
    "ds_read_b128 v[168:171], v200 offset:45056\n"
    "v_cvt_pk_bf16_f32 v147, v82, v83\n"
    "v_mfma_f32_16x16x32_bf16 v[32:35], v[180:183], a[212:215], v[32:35]\n"
    "v_exp_f32 v88, v88\n"
    "v_mfma_f32_16x16x32_bf16 v[36:39], v[180:183], a[236:239], v[36:39]\n"
    "v_exp_f32 v89, v89\n"
    "v_mfma_f32_16x16x32_bf16 v[36:39], v[176:179], a[232:235], v[36:39]\n"
    "v_exp_f32 v90, v90\n"
    "v_mfma_f32_16x16x32_bf16 v[40:43], v[176:179], v[0:3], v[40:43]\n"
    "ds_read_b128 v[172:175], v200 offset:46080\n"
    "v_cvt_pk_bf16_f32 v150, v84, v85\n"
    "v_mfma_f32_16x16x32_bf16 v[40:43], v[180:183], v[4:7], v[40:43]\n"
    "v_exp_f32 v91, v91\n"
    "v_mfma_f32_16x16x32_bf16 v[44:47], v[180:183], v[28:31], v[44:47]\n"
    "v_exp_f32 v92, v92\n"
    "v_mfma_f32_16x16x32_bf16 v[44:47], v[176:179], v[24:27], v[44:47]\n"
    "s_waitcnt lgkmcnt(2)\n"
    "v_exp_f32 v93, v93\n"
    "v_mfma_f32_16x16x32_bf16 v[60:63], v[160:163], v[24:27], v[108:111]\n"
    "ds_read_b128 v[176:179], v200 offset:43008\n"
    "v_cvt_pk_bf16_f32 v151, v86, v87\n"
    "v_mfma_f32_16x16x32_bf16 v[60:63], v[164:167], v[28:31], v[60:63]\n"
    "v_exp_f32 v94, v94\n"
    "v_mfma_f32_16x16x32_bf16 v[56:59], v[164:167], v[4:7], v[104:107]\n"
    "v_exp_f32 v95, v95\n"
    "v_mfma_f32_16x16x32_bf16 v[56:59], v[160:163], v[0:3], v[56:59]\n"
    "v_exp_f32 v32, v32\n"
    "v_mfma_f32_16x16x32_bf16 v[52:55], v[160:163], a[232:235], v[100:103]\n"
    "ds_read_b128 v[180:183], v200 offset:44032\n"
    "v_cvt_pk_bf16_f32 v154, v88, v89\n"
    "v_mfma_f32_16x16x32_bf16 v[52:55], v[164:167], a[236:239], v[52:55]\n"
    "v_exp_f32 v33, v33\n"
    "v_mfma_f32_16x16x32_bf16 v[48:51], v[164:167], a[212:215], v[96:99]\n"
    "s_waitcnt vmcnt(6)\n"
    "v_exp_f32 v34, v34\n"
    "v_mfma_f32_16x16x32_bf16 v[48:51], v[160:163], a[208:211], v[48:51]\n"
    "s_waitcnt lgkmcnt(2)\n"
    "v_exp_f32 v35, v35\n"
    "v_mfma_f32_16x16x32_bf16 v[48:51], v[168:171], a[200:203], v[48:51]\n"
    "s_barrier\n"
    "v_exp_f32 v36, v36\n"
    "v_mfma_f32_16x16x32_bf16 v[48:51], v[172:175], a[204:207], v[48:51]\n"
    "ds_read_b128 v[160:163], v200\n"
    "v_cvt_pk_bf16_f32 v155, v90, v91\n"
    "v_mfma_f32_16x16x32_bf16 v[52:55], v[172:175], a[228:231], v[52:55]\n"
    "v_exp_f32 v37, v37\n"
    "v_mfma_f32_16x16x32_bf16 v[52:55], v[168:171], a[224:227], v[52:55]\n"
    "v_exp_f32 v38, v38\n"
    "v_mfma_f32_16x16x32_bf16 v[56:59], v[168:171], a[248:251], v[56:59]\n"
    "ds_read_b128 v[164:167], v200 offset:1024\n"
    "v_cvt_pk_bf16_f32 v158, v92, v93\n"
    "v_mfma_f32_16x16x32_bf16 v[56:59], v[172:175], a[252:255], v[56:59]\n"
    "buffer_load_dwordx2 v[218:219], v213, s[44:47], 0 offen sc1\n"
    "v_mfma_f32_16x16x32_bf16 v[60:63], v[172:175], v[20:23], v[60:63]\n"
    "v_exp_f32 v39, v39\n"
    "v_mfma_f32_16x16x32_bf16 v[60:63], v[168:171], v[16:19], v[60:63]\n"
    "s_waitcnt lgkmcnt(2)\n"
    "v_exp_f32 v40, v40\n"
    "v_mfma_f32_16x16x32_bf16 v[60:63], v[176:179], v[8:11], v[60:63]\n"
    "ds_read_b128 v[168:171], v200 offset:2048\n"
    "v_cvt_pk_bf16_f32 v159, v94, v95\n"
    "v_mfma_f32_16x16x32_bf16 v[60:63], v[180:183], v[12:15], v[60:63]\n"
    "v_exp_f32 v41, v41\n"
    "v_mfma_f32_16x16x32_bf16 v[56:59], v[180:183], a[244:247], v[56:59]\n"
    "v_exp_f32 v42, v42\n"
    "v_mfma_f32_16x16x32_bf16 v[56:59], v[176:179], a[240:243], v[56:59]\n"
    "v_exp_f32 v43, v43\n"
    "v_mfma_f32_16x16x32_bf16 v[52:55], v[176:179], a[216:219], v[52:55]\n"
    "ds_read_b128 v[172:175], v200 offset:3072\n"
    "v_cvt_pk_bf16_f32 v32, v32, v33\n"
    "v_mfma_f32_16x16x32_bf16 v[52:55], v[180:183], a[220:223], v[52:55]\n"
    "v_exp_f32 v44, v44\n"
    "v_mfma_f32_16x16x32_bf16 v[48:51], v[180:183], a[196:199], v[48:51]\n"
    "v_exp_f32 v45, v45\n"
    "v_mfma_f32_16x16x32_bf16 v[48:51], v[176:179], a[192:195], v[48:51]\n"
    "s_waitcnt lgkmcnt(2)\n"
    "v_exp_f32 v46, v46\n"
    "v_mfma_f32_16x16x32_bf16 v[64:67], v[160:163], a[192:195], v[96:99]\n"
    "ds_read_b128 v[176:179], v200 offset:4096\n"
    "v_cvt_pk_bf16_f32 v33, v34, v35\n"
    "v_mfma_f32_16x16x32_bf16 v[64:67], v[164:167], a[196:199], v[64:67]\n"
    "v_exp_f32 v47, v47\n"
    "v_mfma_f32_16x16x32_bf16 v[68:71], v[164:167], a[220:223], v[100:103]\n"
    "v_cvt_pk_bf16_f32 v36, v36, v37\n"
    "v_cvt_pk_bf16_f32 v37, v38, v39\n"
    "v_mfma_f32_16x16x32_bf16 v[68:71], v[160:163], a[216:219], v[68:71]\n"
    "v_cvt_pk_bf16_f32 v40, v40, v41\n"
    "v_cvt_pk_bf16_f32 v41, v42, v43\n"
    "v_mfma_f32_16x16x32_bf16 v[72:75], v[160:163], a[240:243], v[104:107]\n"
    "ds_read_b128 v[180:183], v200 offset:5120\n"
    "v_cvt_pk_bf16_f32 v44, v44, v45\n"
    "v_mfma_f32_16x16x32_bf16 v[72:75], v[164:167], a[244:247], v[72:75]\n"
    "v_cvt_pk_bf16_f32 v45, v46, v47\n"
    "v_mov_b32 v115, v113\n"
    "v_mfma_f32_16x16x32_bf16 v[76:79], v[164:167], v[12:15], v[108:111]\n"
    "v_exp_f32 v48, v48\n"
    "v_mfma_f32_16x16x32_bf16 v[76:79], v[160:163], v[8:11], v[76:79]\n"
    "s_waitcnt lgkmcnt(2)\n"
    "v_exp_f32 v49, v49\n"
    "v_mfma_f32_16x16x32_bf16 v[76:79], v[168:171], v[16:19], v[76:79]\n"
    "ds_read_b128 v[160:163], v200 offset:10240\n"
    "v_cvt_pk_bf16_f32 v34, v48, v49\n"
    "v_mfma_f32_16x16x32_bf16 v[76:79], v[172:175], v[20:23], v[76:79]\n"
    "v_exp_f32 v50, v50\n"
    "v_mfma_f32_16x16x32_bf16 v[72:75], v[172:175], a[252:255], v[72:75]\n"
    "v_exp_f32 v51, v51\n"
    "v_mfma_f32_16x16x32_bf16 v[72:75], v[168:171], a[248:251], v[72:75]\n"
    "v_exp_f32 v52, v52\n"
    "v_mfma_f32_16x16x32_bf16 v[68:71], v[168:171], a[224:227], v[68:71]\n"
    "ds_read_b128 v[164:167], v200 offset:11264\n"
    "v_cvt_pk_bf16_f32 v35, v50, v51\n"
    "v_mfma_f32_16x16x32_bf16 v[68:71], v[172:175], a[228:231], v[68:71]\n"
    "v_exp_f32 v53, v53\n"
    "v_mfma_f32_16x16x32_bf16 v[64:67], v[172:175], a[204:207], v[64:67]\n"
    "v_exp_f32 v54, v54\n"
    "v_mfma_f32_16x16x32_bf16 v[64:67], v[168:171], a[200:203], v[64:67]\n"
    "s_waitcnt lgkmcnt(2)\n"
    "v_exp_f32 v55, v55\n"
    "v_mfma_f32_16x16x32_bf16 v[64:67], v[176:179], a[208:211], v[64:67]\n"
    "s_add_u32 s66, s66, s74\n"
    "ds_read_b128 v[168:171], v200 offset:8192\n"
    "v_cvt_pk_bf16_f32 v38, v52, v53\n"
    "v_mfma_f32_16x16x32_bf16 v[64:67], v[180:183], a[212:215], v[64:67]\n"
    "s_add_u32 s60, s72, 0xbd000\n"
    "v_exp_f32 v56, v56\n"
    "v_mfma_f32_16x16x32_bf16 v[68:71], v[180:183], a[236:239], v[68:71]\n"
    "s_add_u32 s61, s71, 0xba000\n"
    "v_exp_f32 v57, v57\n"
    "v_mfma_f32_16x16x32_bf16 v[68:71], v[176:179], a[232:235], v[68:71]\n"
    "v_exp_f32 v58, v58\n"
    "v_mfma_f32_16x16x32_bf16 v[72:75], v[176:179], v[0:3], v[72:75]\n"
    "ds_read_b128 v[172:175], v200 offset:9216\n"
    "v_cvt_pk_bf16_f32 v39, v54, v55\n"
    "v_mfma_f32_16x16x32_bf16 v[72:75], v[180:183], v[4:7], v[72:75]\n"
    "v_exp_f32 v59, v59\n"
    "v_mfma_f32_16x16x32_bf16 v[76:79], v[180:183], v[28:31], v[76:79]\n"
    "s_waitcnt vmcnt(0)\n"
    "v_cmp_ne_u32 s[80:81], s75, v219\n"
    "v_cvt_pk_bf16_f32 v42, v56, v57\n"
    "v_mfma_f32_16x16x32_bf16 v[76:79], v[176:179], v[24:27], v[76:79]\n"
    "s_waitcnt lgkmcnt(2)\n"
    "v_cmp_gt_u32 vcc, s66, v218\n"
    "v_cvt_pk_bf16_f32 v43, v58, v59\n"
    "v_mfma_f32_16x16x32_bf16 v[92:95], v[160:163], v[24:27], v[108:111]\n"
    "ds_read_b128 v[176:179], v200 offset:6144\n"
    "v_mov_b32 v119, v117\n"
    "v_mfma_f32_16x16x32_bf16 v[92:95], v[164:167], v[28:31], v[92:95]\n"
    "s_or_b64 vcc, vcc, s[80:81]\n"
    "v_exp_f32 v64, v64\n"
    "v_mfma_f32_16x16x32_bf16 v[88:91], v[164:167], v[4:7], v[104:107]\n"
    "s_cbranch_vccnz LSPRE5\n"
    "LSPB5:\n"
    "v_exp_f32 v65, v65\n"
    "v_mfma_f32_16x16x32_bf16 v[88:91], v[160:163], v[0:3], v[88:91]\n"
    "v_exp_f32 v66, v66\n"
    "v_mfma_f32_16x16x32_bf16 v[84:87], v[160:163], a[232:235], v[100:103]\n"
    "s_barrier\n"
    "ds_read_b128 v[180:183], v200 offset:7168\n"
    "v_cvt_pk_bf16_f32 v64, v64, v65\n"
    "v_mfma_f32_16x16x32_bf16 v[84:87], v[164:167], a[236:239], v[84:87]\n"
    "s_add_u32 m0, s72, 0x6000\n"
    "v_exp_f32 v67, v67\n"
    "v_mfma_f32_16x16x32_bf16 v[80:83], v[164:167], a[212:215], v[96:99]\n"
    "buffer_load_dwordx4 v200, s[36:39], s60 offen sc1 lds\n"
    "v_mfma_f32_16x16x32_bf16 v[80:83], v[160:163], a[208:211], v[80:83]\n"
    "s_waitcnt lgkmcnt(2)\n"
    "v_exp_f32 v68, v68\n"
    "v_mfma_f32_16x16x32_bf16 v[80:83], v[168:171], a[200:203], v[80:83]\n"
    "ds_read_b128 v[160:163], v200 offset:12288\n"
    "v_cvt_pk_bf16_f32 v65, v66, v67\n"
    "v_mfma_f32_16x16x32_bf16 v[80:83], v[172:175], a[204:207], v[80:83]\n"
    "buffer_load_dwordx4 v200, s[36:39], s60 offen offset:1024 sc1 lds\n"
    "v_mfma_f32_16x16x32_bf16 v[84:87], v[172:175], a[228:231], v[84:87]\n"
    "v_exp_f32 v69, v69\n"
    "v_mfma_f32_16x16x32_bf16 v[84:87], v[168:171], a[224:227], v[84:87]\n"
    "buffer_load_dwordx4 v200, s[36:39], s60 offen offset:2048 sc1 lds\n"
    "v_mfma_f32_16x16x32_bf16 v[88:91], v[168:171], a[248:251], v[88:91]\n"
    "ds_read_b128 v[164:167], v200 offset:13312\n"
    "v_cvt_pk_bf16_f32 v68, v68, v69\n"
    "v_mfma_f32_16x16x32_bf16 v[88:91], v[172:175], a[252:255], v[88:91]\n"
    "v_exp_f32 v60, v60\n"
    "v_mfma_f32_16x16x32_bf16 v[92:95], v[172:175], v[20:23], v[92:95]\n"
    "s_add_u32 m0, s71, 0x18000\n"
    "v_exp_f32 v70, v70\n"
    "v_mfma_f32_16x16x32_bf16 v[92:95], v[168:171], v[16:19], v[92:95]\n"
    "s_waitcnt lgkmcnt(2)\n"
    "buffer_load_dwordx4 v200, s[40:43], s61 offen sc1 lds\n"
    "v_mfma_f32_16x16x32_bf16 v[92:95], v[176:179], v[8:11], v[92:95]\n"
    "ds_read_b128 v[168:171], v200 offset:14336\n"
    "v_mov_b32 v123, v121\n"
    "v_mfma_f32_16x16x32_bf16 v[92:95], v[180:183], v[12:15], v[92:95]\n"
    "v_exp_f32 v61, v61\n"
    "v_mfma_f32_16x16x32_bf16 v[88:91], v[180:183], a[244:247], v[88:91]\n"
    "v_exp_f32 v71, v71\n"
    "v_mfma_f32_16x16x32_bf16 v[88:91], v[176:179], a[240:243], v[88:91]\n"
    "v_exp_f32 v62, v62\n"
    "v_mfma_f32_16x16x32_bf16 v[84:87], v[176:179], a[216:219], v[84:87]\n"
    "ds_read_b128 v[172:175], v200 offset:15360\n"
    "v_cvt_pk_bf16_f32 v46, v60, v61\n"
    "v_mfma_f32_16x16x32_bf16 v[84:87], v[180:183], a[220:223], v[84:87]\n"
    "v_exp_f32 v72, v72\n"
    "v_mfma_f32_16x16x32_bf16 v[80:83], v[180:183], a[196:199], v[80:83]\n"
    "v_exp_f32 v63, v63\n"
    "v_mfma_f32_16x16x32_bf16 v[80:83], v[176:179], a[192:195], v[80:83]\n"
    "s_waitcnt lgkmcnt(2)\n"
    "buffer_load_dwordx4 v200, s[40:43], s61 offen offset:1024 sc1 lds\n"
    "v_mfma_f32_16x16x32_bf16 v[232:235], v[160:163], a[192:195], v[96:99]\n"
    "ds_read_b128 v[176:179], v200 offset:16384\n"
    "v_cvt_pk_bf16_f32 v47, v62, v63\n"
    "v_mfma_f32_16x16x32_bf16 v[232:235], v[164:167], a[196:199], v[232:235]\n"
    "v_exp_f32 v73, v73\n"
    "v_mfma_f32_16x16x32_bf16 v[236:239], v[164:167], a[220:223], v[100:103]\n"
    "v_exp_f32 v74, v74\n"
    "v_mfma_f32_16x16x32_bf16 v[236:239], v[160:163], a[216:219], v[236:239]\n"
    "v_exp_f32 v75, v75\n"
    "v_mfma_f32_16x16x32_bf16 v[240:243], v[160:163], a[240:243], v[104:107]\n"
    "ds_read_b128 v[180:183], v200 offset:17408\n"
    "v_cvt_pk_bf16_f32 v69, v70, v71\n"
    "v_mfma_f32_16x16x32_bf16 v[240:243], v[164:167], a[244:247], v[240:243]\n"
    "v_exp_f32 v76, v76\n"
    "v_mfma_f32_16x16x32_bf16 v[244:247], v[164:167], v[12:15], v[108:111]\n"
    "v_exp_f32 v77, v77\n"
    "v_mfma_f32_16x16x32_bf16 v[244:247], v[160:163], v[8:11], v[244:247]\n"
    "s_waitcnt lgkmcnt(2)\n"
    "buffer_load_dwordx4 v200, s[40:43], s61 offen offset:2048 sc1 lds\n"
    "v_mfma_f32_16x16x32_bf16 v[244:247], v[168:171], v[16:19], v[244:247]\n"
    "ds_read_b128 v[160:163], v200 offset:22528\n"
    "v_cvt_pk_bf16_f32 v72, v72, v73\n"
    "v_mfma_f32_16x16x32_bf16 v[244:247], v[172:175], v[20:23], v[244:247]\n"
    "v_exp_f32 v78, v78\n"
    "v_mfma_f32_16x16x32_bf16 v[240:243], v[172:175], a[252:255], v[240:243]\n"
    "v_exp_f32 v79, v79\n"
    "v_mfma_f32_16x16x32_bf16 v[240:243], v[168:171], a[248:251], v[240:243]\n"
    "v_exp_f32 v80, v80\n"
    "v_mfma_f32_16x16x32_bf16 v[236:239], v[168:171], a[224:227], v[236:239]\n"
    "ds_read_b128 v[164:167], v200 offset:23552\n"
    "v_cvt_pk_bf16_f32 v73, v74, v75\n"
    "v_mfma_f32_16x16x32_bf16 v[236:239], v[172:175], a[228:231], v[236:239]\n"
    "v_exp_f32 v81, v81\n"
    "v_mfma_f32_16x16x32_bf16 v[232:235], v[172:175], a[204:207], v[232:235]\n"
    "v_exp_f32 v82, v82\n"
    "v_mfma_f32_16x16x32_bf16 v[232:235], v[168:171], a[200:203], v[232:235]\n"
    "s_waitcnt lgkmcnt(2)\n"
    "buffer_load_dwordx4 v200, s[40:43], s61 offen offset:3072 sc1 lds\n"
    "v_mfma_f32_16x16x32_bf16 v[232:235], v[176:179], a[208:211], v[232:235]\n"
    "ds_read_b128 v[168:171], v200 offset:20480\n"
    "v_cvt_pk_bf16_f32 v76, v76, v77\n"
    "v_mfma_f32_16x16x32_bf16 v[232:235], v[180:183], a[212:215], v[232:235]\n"
    "v_exp_f32 v83, v83\n"
    "v_mfma_f32_16x16x32_bf16 v[236:239], v[180:183], a[236:239], v[236:239]\n"
    "v_exp_f32 v84, v84\n"
    "v_mfma_f32_16x16x32_bf16 v[236:239], v[176:179], a[232:235], v[236:239]\n"
    "v_exp_f32 v85, v85\n"
    "v_mfma_f32_16x16x32_bf16 v[240:243], v[176:179], v[0:3], v[240:243]\n"
    "ds_read_b128 v[172:175], v200 offset:21504\n"
    "v_cvt_pk_bf16_f32 v77, v78, v79\n"
    "v_mfma_f32_16x16x32_bf16 v[240:243], v[180:183], v[4:7], v[240:243]\n"
    "s_add_u32 s65, s61, 0x1000\n"
    "v_exp_f32 v86, v86\n"
    "v_mfma_f32_16x16x32_bf16 v[244:247], v[180:183], v[28:31], v[244:247]\n"
    "s_add_u32 m0, s71, 0x19000\n"
    "v_exp_f32 v87, v87\n"
    "v_mfma_f32_16x16x32_bf16 v[244:247], v[176:179], v[24:27], v[244:247]\n"
    "s_waitcnt lgkmcnt(2)\n"
    "buffer_load_dwordx4 v200, s[40:43], s65 offen sc1 lds\n"
    "v_mfma_f32_16x16x32_bf16 v[60:63], v[160:163], v[24:27], v[108:111]\n"
    "ds_read_b128 v[176:179], v200 offset:18432\n"
    "v_cvt_pk_bf16_f32 v66, v80, v81\n"
    "v_mfma_f32_16x16x32_bf16 v[60:63], v[164:167], v[28:31], v[60:63]\n"
    "v_exp_f32 v88, v88\n"
    "v_mfma_f32_16x16x32_bf16 v[56:59], v[164:167], v[4:7], v[104:107]\n"
    "v_exp_f32 v89, v89\n"
    "v_mfma_f32_16x16x32_bf16 v[56:59], v[160:163], v[0:3], v[56:59]\n"
    "v_exp_f32 v90, v90\n"
    "v_mfma_f32_16x16x32_bf16 v[52:55], v[160:163], a[232:235], v[100:103]\n"
    "ds_read_b128 v[180:183], v200 offset:19456\n"
    "v_cvt_pk_bf16_f32 v67, v82, v83\n"
    "v_mfma_f32_16x16x32_bf16 v[52:55], v[164:167], a[236:239], v[52:55]\n"
    "v_exp_f32 v91, v91\n"
    "v_mfma_f32_16x16x32_bf16 v[48:51], v[164:167], a[212:215], v[96:99]\n"
    "s_waitcnt vmcnt(5)\n"
    "v_exp_f32 v92, v92\n"
    "v_mfma_f32_16x16x32_bf16 v[48:51], v[160:163], a[208:211], v[48:51]\n"
    "s_waitcnt lgkmcnt(2)\n"
    "buffer_load_dwordx4 v200, s[40:43], s65 offen offset:1024 sc1 lds\n"
    "v_mfma_f32_16x16x32_bf16 v[48:51], v[168:171], a[200:203], v[48:51]\n"
    "s_barrier\n"
    "v_exp_f32 v93, v93\n"
    "v_mfma_f32_16x16x32_bf16 v[48:51], v[172:175], a[204:207], v[48:51]\n"
    "ds_read_b128 v[160:163], v200 offset:24576\n"
    "v_cvt_pk_bf16_f32 v70, v84, v85\n"
    "v_mfma_f32_16x16x32_bf16 v[52:55], v[172:175], a[228:231], v[52:55]\n"
    "v_exp_f32 v94, v94\n"
    "v_mfma_f32_16x16x32_bf16 v[52:55], v[168:171], a[224:227], v[52:55]\n"
    "v_exp_f32 v95, v95\n"
    "v_mfma_f32_16x16x32_bf16 v[56:59], v[168:171], a[248:251], v[56:59]\n"
    "ds_read_b128 v[164:167], v200 offset:25600\n"
    "v_cvt_pk_bf16_f32 v71, v86, v87\n"
    "v_mfma_f32_16x16x32_bf16 v[56:59], v[172:175], a[252:255], v[56:59]\n"
    "v_exp_f32 v232, v232\n"
    "v_mfma_f32_16x16x32_bf16 v[60:63], v[172:175], v[20:23], v[60:63]\n"
    "v_exp_f32 v233, v233\n"
    "v_mfma_f32_16x16x32_bf16 v[60:63], v[168:171], v[16:19], v[60:63]\n"
    "s_waitcnt lgkmcnt(2)\n"
    "v_exp_f32 v234, v234\n"
    "v_mfma_f32_16x16x32_bf16 v[60:63], v[176:179], v[8:11], v[60:63]\n"
    "s_lshl_b32 s76, s56, 3\n"
    "ds_read_b128 v[168:171], v200 offset:26624\n"
    "v_cvt_pk_bf16_f32 v74, v88, v89\n"
    "v_mfma_f32_16x16x32_bf16 v[60:63], v[180:183], v[12:15], v[60:63]\n"
    "s_add_u32 s76, s76, s57\n"
    "v_exp_f32 v235, v235\n"
    "v_mfma_f32_16x16x32_bf16 v[56:59], v[180:183], a[244:247], v[56:59]\n"
    "s_mul_i32 s76, s76, 0x18000\n"
    "v_exp_f32 v236, v236\n"
    "v_mfma_f32_16x16x32_bf16 v[56:59], v[176:179], a[240:243], v[56:59]\n"
    "s_add_u32 s76, s76, s88\n"
    "v_exp_f32 v237, v237\n"
    "v_mfma_f32_16x16x32_bf16 v[52:55], v[176:179], a[216:219], v[52:55]\n"
    "s_add_u32 s28, s24, s76\n"
    "ds_read_b128 v[172:175], v200 offset:27648\n"
    "v_cvt_pk_bf16_f32 v75, v90, v91\n"
    "v_mfma_f32_16x16x32_bf16 v[52:55], v[180:183], a[220:223], v[52:55]\n"
    "s_addc_u32 s29, s25, 0\n"
    "v_exp_f32 v238, v238\n"
    "v_mfma_f32_16x16x32_bf16 v[48:51], v[180:183], a[196:199], v[48:51]\n"
    "s_and_b32 s29, s29, 0xffff\n"
    "v_exp_f32 v239, v239\n"
    "v_mfma_f32_16x16x32_bf16 v[48:51], v[176:179], a[192:195], v[48:51]\n"
    "s_waitcnt lgkmcnt(2)\n"
    "v_exp_f32 v240, v240\n"
    "v_mfma_f32_16x16x32_bf16 v[80:83], v[160:163], a[192:195], v[96:99]\n"
    "s_mov_b32 s30, 0x6000\n"
    "ds_read_b128 v[176:179], v200 offset:28672\n"
    "v_cvt_pk_bf16_f32 v78, v92, v93\n"
    "v_mfma_f32_16x16x32_bf16 v[80:83], v[164:167], a[196:199], v[80:83]\n"
    "s_mov_b32 s31, 0x20000\n"
    "v_cvt_pk_bf16_f32 v79, v94, v95\n"
    "v_mov_b32 v127, v125\n"
    "v_mfma_f32_16x16x32_bf16 v[84:87], v[164:167], a[220:223], v[100:103]\n"
    "s_xor_b32 s94, s70, 0x80000000\n"
    "v_exp_f32 v241, v241\n"
    "v_mfma_f32_16x16x32_bf16 v[84:87], v[160:163], a[216:219], v[84:87]\n"
    "s_xor_b32 s95, s70, 0x80000000\n"
    "v_exp_f32 v242, v242\n"
    "v_mfma_f32_16x16x32_bf16 v[88:91], v[160:163], a[240:243], v[104:107]\n"
    "s_mov_b32 s62, 0x1800\n"
    "ds_read_b128 v[180:183], v200 offset:29696\n"
    "v_permlane16_swap_b32 v113, v115\n"
    "v_mfma_f32_16x16x32_bf16 v[88:91], v[164:167], a[244:247], v[88:91]\n"
    "s_mov_b32 s63, 0x3000\n"
    "v_exp_f32 v243, v243\n"
    "v_mfma_f32_16x16x32_bf16 v[92:95], v[164:167], v[12:15], v[108:111]\n"
    "s_mov_b32 s64, 0x4800\n"
    "v_exp_f32 v244, v244\n"
    "v_mfma_f32_16x16x32_bf16 v[92:95], v[160:163], v[8:11], v[92:95]\n"
    "s_waitcnt lgkmcnt(2)\n"
    "v_exp_f32 v245, v245\n"
    "v_mfma_f32_16x16x32_bf16 v[92:95], v[168:171], v[16:19], v[92:95]\n"
    "ds_read_b128 v[160:163], v200 offset:34816\n"
    "v_permlane16_swap_b32 v117, v119\n"
    "v_mfma_f32_16x16x32_bf16 v[92:95], v[172:175], v[20:23], v[92:95]\n"
    "v_exp_f32 v246, v246\n"
    "v_mfma_f32_16x16x32_bf16 v[88:91], v[172:175], a[252:255], v[88:91]\n"
    "v_exp_f32 v247, v247\n"
    "v_mfma_f32_16x16x32_bf16 v[88:91], v[168:171], a[248:251], v[88:91]\n"
    "v_exp_f32 v48, v48\n"
    "v_mfma_f32_16x16x32_bf16 v[84:87], v[168:171], a[224:227], v[84:87]\n"
    "ds_read_b128 v[164:167], v200 offset:35840\n"
    "v_permlane16_swap_b32 v121, v123\n"
    "v_mfma_f32_16x16x32_bf16 v[84:87], v[172:175], a[228:231], v[84:87]\n"
    "v_exp_f32 v49, v49\n"
    "v_mfma_f32_16x16x32_bf16 v[80:83], v[172:175], a[204:207], v[80:83]\n"
    "v_exp_f32 v50, v50\n"
    "v_mfma_f32_16x16x32_bf16 v[80:83], v[168:171], a[200:203], v[80:83]\n"
    "s_waitcnt lgkmcnt(2)\n"
    "v_exp_f32 v51, v51\n"
    "v_mfma_f32_16x16x32_bf16 v[80:83], v[176:179], a[208:211], v[80:83]\n"
    "ds_read_b128 v[168:171], v200 offset:32768\n"
    "v_permlane16_swap_b32 v125, v127\n"
    "v_mfma_f32_16x16x32_bf16 v[80:83], v[180:183], a[212:215], v[80:83]\n"
    "v_exp_f32 v52, v52\n"
    "v_mfma_f32_16x16x32_bf16 v[84:87], v[180:183], a[236:239], v[84:87]\n"
    "v_exp_f32 v53, v53\n"
    "v_mfma_f32_16x16x32_bf16 v[84:87], v[176:179], a[232:235], v[84:87]\n"
    "v_exp_f32 v54, v54\n"
    "v_mfma_f32_16x16x32_bf16 v[88:91], v[176:179], v[0:3], v[88:91]\n"
    "ds_read_b128 v[172:175], v200 offset:33792\n"
    "v_add_f32 v113, v113, v115\n"
    "v_mfma_f32_16x16x32_bf16 v[88:91], v[180:183], v[4:7], v[88:91]\n"
    "v_add_f32 v117, v117, v119\n"
    "v_add_f32 v121, v121, v123\n"
    "v_mfma_f32_16x16x32_bf16 v[92:95], v[180:183], v[28:31], v[92:95]\n"
    "v_add_f32 v125, v125, v127\n"
    "v_add_f32 v112, v112, v113\n"
    "v_mfma_f32_16x16x32_bf16 v[92:95], v[176:179], v[24:27], v[92:95]\n"
    "s_waitcnt lgkmcnt(2)\n"
    "v_add_f32 v116, v116, v117\n"
    "v_add_f32 v120, v120, v121\n"
    "v_mfma_f32_16x16x32_bf16 v[252:255], v[160:163], v[24:27], v[108:111]\n"
    "ds_read_b128 v[176:179], v200 offset:30720\n"
    "v_add_f32 v124, v124, v125\n"
    "v_mfma_f32_16x16x32_bf16 v[252:255], v[164:167], v[28:31], v[252:255]\n"
    "v_and_b32 v198, 15, v231\n"
    "v_mul_u32_u24 v205, 0x180, v198\n"
    "v_mfma_f32_16x16x32_bf16 v[248:251], v[164:167], v[4:7], v[104:107]\n"
    "v_exp_f32 v55, v55\n"
    "v_mfma_f32_16x16x32_bf16 v[248:251], v[160:163], v[0:3], v[248:251]\n"
    "v_lshrrev_b32 v199, 4, v231\n"
    "v_lshl_add_u32 v205, v199, 4, v205\n"
    "v_mfma_f32_16x16x32_bf16 v[188:191], v[160:163], a[232:235], v[100:103]\n"
    "ds_read_b128 v[180:183], v200 offset:31744\n"
    "v_add_u32 v207, 0x18000, v200\n"
    "v_mfma_f32_16x16x32_bf16 v[188:191], v[164:167], a[236:239], v[188:191]\n"
    "v_exp_f32 v56, v56\n"
    "v_mfma_f32_16x16x32_bf16 v[184:187], v[164:167], a[212:215], v[96:99]\n"
    "v_exp_f32 v57, v57\n"
    "v_mfma_f32_16x16x32_bf16 v[184:187], v[160:163], a[208:211], v[184:187]\n"
    "s_waitcnt lgkmcnt(2)\n"
    "v_exp_f32 v58, v58\n"
    "v_mfma_f32_16x16x32_bf16 v[184:187], v[168:171], a[200:203], v[184:187]\n"
    "ds_read_b128 v[160:163], v201\n"
    "v_cvt_pk_bf16_f32 v232, v232, v233\n"
    "v_mfma_f32_16x16x32_bf16 v[184:187], v[172:175], a[204:207], v[184:187]\n"
    "v_exp_f32 v59, v59\n"
    "v_mfma_f32_16x16x32_bf16 v[188:191], v[172:175], a[228:231], v[188:191]\n"
    "v_exp_f32 v60, v60\n"
    "v_mfma_f32_16x16x32_bf16 v[188:191], v[168:171], a[224:227], v[188:191]\n"
    "v_exp_f32 v61, v61\n"
    "v_mfma_f32_16x16x32_bf16 v[248:251], v[168:171], a[248:251], v[248:251]\n"
    "ds_read_b128 v[164:167], v201 offset:1024\n"
    "v_cvt_pk_bf16_f32 v233, v234, v235\n"
    "v_mfma_f32_16x16x32_bf16 v[248:251], v[172:175], a[252:255], v[248:251]\n"
    "v_exp_f32 v62, v62\n"
    "v_mfma_f32_16x16x32_bf16 v[252:255], v[172:175], v[20:23], v[252:255]\n"
    "v_exp_f32 v63, v63\n"
    "v_mfma_f32_16x16x32_bf16 v[252:255], v[168:171], v[16:19], v[252:255]\n"
    "s_waitcnt lgkmcnt(2)\n"
    "v_exp_f32 v80, v80\n"
    "v_mfma_f32_16x16x32_bf16 v[252:255], v[176:179], v[8:11], v[252:255]\n"
    "ds_read_b128 v[168:171], v201 offset:24576\n"
    "v_cvt_pk_bf16_f32 v236, v236, v237\n"
    "v_mfma_f32_16x16x32_bf16 v[252:255], v[180:183], v[12:15], v[252:255]\n"
    "v_exp_f32 v81, v81\n"
    "v_mfma_f32_16x16x32_bf16 v[248:251], v[180:183], a[244:247], v[248:251]\n"
    "v_exp_f32 v82, v82\n"
    "v_mfma_f32_16x16x32_bf16 v[248:251], v[176:179], a[240:243], v[248:251]\n"
    "v_exp_f32 v83, v83\n"
    "v_mfma_f32_16x16x32_bf16 v[188:191], v[176:179], a[216:219], v[188:191]\n"
    "ds_read_b128 v[172:175], v201 offset:25600\n"
    "v_cvt_pk_bf16_f32 v237, v238, v239\n"
    "v_mfma_f32_16x16x32_bf16 v[188:191], v[180:183], a[220:223], v[188:191]\n"
    "v_exp_f32 v84, v84\n"
    "v_mfma_f32_16x16x32_bf16 v[184:187], v[180:183], a[196:199], v[184:187]\n"
    "v_exp_f32 v85, v85\n"
    "v_mfma_f32_16x16x32_bf16 v[184:187], v[176:179], a[192:195], v[184:187]\n"
    "s_waitcnt lgkmcnt(2)\n"
    "v_exp_f32 v86, v86\n"
    "v_mfma_f32_16x16x32_bf16 a[0:3], v[160:163], v[128:131], a[0:3]\n"
    "ds_read_b128 v[176:179], v201 offset:2048\n"
    "v_cvt_pk_bf16_f32 v240, v240, v241\n"
    "v_mfma_f32_16x16x32_bf16 a[0:3], v[164:167], v[144:147], a[0:3]\n"
    "v_exp_f32 v87, v87\n"
    "v_mfma_f32_16x16x32_bf16 a[4:7], v[160:163], v[132:135], a[4:7]\n"
    "v_exp_f32 v88, v88\n"
    "v_mfma_f32_16x16x32_bf16 a[4:7], v[164:167], v[148:151], a[4:7]\n"
    "v_cvt_pk_bf16_f32 v241, v242, v243\n"
    "v_cvt_pk_bf16_f32 v244, v244, v245\n"
    "v_mfma_f32_16x16x32_bf16 a[8:11], v[160:163], v[136:139], a[8:11]\n"
    "ds_read_b128 v[180:183], v201 offset:3072\n"
    "v_cvt_pk_bf16_f32 v245, v246, v247\n"
    "v_mfma_f32_16x16x32_bf16 a[8:11], v[164:167], v[152:155], a[8:11]\n"
    "v_cvt_pk_bf16_f32 v234, v48, v49\n"
    "v_cvt_pk_bf16_f32 v235, v50, v51\n"
    "v_mfma_f32_16x16x32_bf16 a[12:15], v[160:163], v[140:143], a[12:15]\n"
    "v_exp_f32 v89, v89\n"
    "v_mfma_f32_16x16x32_bf16 a[12:15], v[164:167], v[156:159], a[12:15]\n"
    "v_cvt_pk_bf16_f32 v238, v52, v53\n"
    "v_cvt_pk_bf16_f32 v239, v54, v55\n"
    "v_mfma_f32_16x16x32_bf16 v[112:115], v[192:195], v[144:147], v[112:115]\n"
    "v_cvt_pk_bf16_f32 v242, v56, v57\n"
    "v_cvt_pk_bf16_f32 v243, v58, v59\n"
    "v_mfma_f32_16x16x32_bf16 v[112:115], v[192:195], v[32:35], v[112:115]\n"
    "v_cvt_pk_bf16_f32 v246, v60, v61\n"
    "v_cvt_pk_bf16_f32 v247, v62, v63\n"
    "v_mfma_f32_16x16x32_bf16 v[112:115], v[192:195], v[64:67], v[112:115]\n"
    "v_exp_f32 v90, v90\n"
    "v_mfma_f32_16x16x32_bf16 v[116:119], v[192:195], v[148:151], v[116:119]\n"
    "v_exp_f32 v91, v91\n"
    "v_mfma_f32_16x16x32_bf16 v[116:119], v[192:195], v[36:39], v[116:119]\n"
    "v_exp_f32 v92, v92\n"
    "v_mfma_f32_16x16x32_bf16 v[116:119], v[192:195], v[68:71], v[116:119]\n"
    "s_waitcnt lgkmcnt(2)\n"
    "v_exp_f32 v93, v93\n"
    "v_mfma_f32_16x16x32_bf16 a[0:3], v[168:171], v[32:35], a[0:3]\n"
    "ds_read_b128 v[160:163], v201 offset:26624\n"
    "v_cvt_pk_bf16_f32 v80, v80, v81\n"
    "v_mfma_f32_16x16x32_bf16 a[0:3], v[172:175], v[64:67], a[0:3]\n"
    "v_exp_f32 v94, v94\n"
    "v_mfma_f32_16x16x32_bf16 a[4:7], v[168:171], v[36:39], a[4:7]\n"
    "v_exp_f32 v95, v95\n"
    "v_mfma_f32_16x16x32_bf16 a[4:7], v[172:175], v[68:71], a[4:7]\n"
    "v_exp_f32 v184, v184\n"
    "v_mfma_f32_16x16x32_bf16 a[8:11], v[168:171], v[40:43], a[8:11]\n"
    "ds_read_b128 v[164:167], v201 offset:27648\n"
    "v_cvt_pk_bf16_f32 v81, v82, v83\n"
    "v_mfma_f32_16x16x32_bf16 a[8:11], v[172:175], v[72:75], a[8:11]\n"
    "v_exp_f32 v185, v185\n"
    "v_mfma_f32_16x16x32_bf16 a[12:15], v[168:171], v[44:47], a[12:15]\n"
    "v_exp_f32 v186, v186\n"
    "v_mfma_f32_16x16x32_bf16 a[12:15], v[172:175], v[76:79], a[12:15]\n"
    "v_exp_f32 v187, v187\n"
    "v_mfma_f32_16x16x32_bf16 v[120:123], v[192:195], v[152:155], v[120:123]\n"
    "v_exp_f32 v188, v188\n"
    "v_mfma_f32_16x16x32_bf16 v[120:123], v[192:195], v[40:43], v[120:123]\n"
    "v_exp_f32 v189, v189\n"
    "v_mfma_f32_16x16x32_bf16 v[120:123], v[192:195], v[72:75], v[120:123]\n"
    "v_exp_f32 v190, v190\n"
    "v_mfma_f32_16x16x32_bf16 v[124:127], v[192:195], v[156:159], v[124:127]\n"
    "v_exp_f32 v191, v191\n"
    "v_mfma_f32_16x16x32_bf16 v[124:127], v[192:195], v[44:47], v[124:127]\n"
    "s_waitcnt vmcnt(0)\n"
    "v_exp_f32 v248, v248\n"
    "v_mfma_f32_16x16x32_bf16 v[124:127], v[192:195], v[76:79], v[124:127]\n"
    "s_waitcnt lgkmcnt(2)\n"
    "v_exp_f32 v249, v249\n"
    "v_mfma_f32_16x16x32_bf16 a[16:19], v[176:179], v[128:131], a[16:19]\n"
    "s_barrier\n"
    "v_exp_f32 v250, v250\n"
    "v_mfma_f32_16x16x32_bf16 a[16:19], v[180:183], v[144:147], a[16:19]\n"
    "ds_read_b128 v[168:171], v207\n"
    "v_cvt_pk_bf16_f32 v84, v84, v85\n"
    "v_mfma_f32_16x16x32_bf16 a[20:23], v[176:179], v[132:135], a[20:23]\n"
    "v_exp_f32 v251, v251\n"
    "v_mfma_f32_16x16x32_bf16 a[20:23], v[180:183], v[148:151], a[20:23]\n"
    "v_exp_f32 v252, v252\n"
    "v_mfma_f32_16x16x32_bf16 a[24:27], v[176:179], v[136:139], a[24:27]\n"
    "ds_read_b128 v[172:175], v207 offset:1024\n"
    "v_cvt_pk_bf16_f32 v85, v86, v87\n"
    "v_mfma_f32_16x16x32_bf16 a[24:27], v[180:183], v[152:155], a[24:27]\n"
    "v_exp_f32 v253, v253\n"
    "v_mfma_f32_16x16x32_bf16 a[28:31], v[176:179], v[140:143], a[28:31]\n"
    "v_cvt_pk_bf16_f32 v88, v88, v89\n"
    "v_cvt_pk_bf16_f32 v89, v90, v91\n"
    "v_mfma_f32_16x16x32_bf16 a[28:31], v[180:183], v[156:159], a[28:31]\n"
    "s_waitcnt lgkmcnt(2)\n"
    "v_exp_f32 v254, v254\n"
    "v_mfma_f32_16x16x32_bf16 a[16:19], v[160:163], v[32:35], a[16:19]\n"
    "ds_read_b128 v[176:179], v207 offset:2048\n"
    "v_cvt_pk_bf16_f32 v92, v92, v93\n"
    "v_mfma_f32_16x16x32_bf16 a[16:19], v[164:167], v[64:67], a[16:19]\n"
    "ds_read_b128 v[180:183], v207 offset:3072\n"
    "v_cvt_pk_bf16_f32 v93, v94, v95\n"
    "v_mfma_f32_16x16x32_bf16 a[20:23], v[160:163], v[36:39], a[20:23]\n"
    "v_cvt_pk_bf16_f32 v82, v184, v185\n"
    "v_cvt_pk_bf16_f32 v83, v186, v187\n"
    "v_mfma_f32_16x16x32_bf16 a[20:23], v[164:167], v[68:71], a[20:23]\n"
    "v_exp_f32 v255, v255\n"
    "v_mfma_f32_16x16x32_bf16 a[24:27], v[160:163], v[40:43], a[24:27]\n"
    "v_cvt_pk_bf16_f32 v86, v188, v189\n"
    "v_cvt_pk_bf16_f32 v87, v190, v191\n"
    "v_mfma_f32_16x16x32_bf16 a[24:27], v[164:167], v[72:75], a[24:27]\n"
    "v_cvt_pk_bf16_f32 v90, v248, v249\n"
    "v_cvt_pk_bf16_f32 v91, v250, v251\n"
    "v_mfma_f32_16x16x32_bf16 a[28:31], v[160:163], v[44:47], a[28:31]\n"
    "v_cvt_pk_bf16_f32 v94, v252, v253\n"
    "v_cvt_pk_bf16_f32 v95, v254, v255\n"
    "v_mfma_f32_16x16x32_bf16 a[28:31], v[164:167], v[76:79], a[28:31]\n"
    "v_mfma_f32_16x16x32_bf16 v[112:115], v[192:195], v[232:235], v[112:115]\n"
    "ds_read_b128 v[160:163], v201 offset:4096\n"
    "v_mfma_f32_16x16x32_bf16 v[112:115], v[192:195], v[80:83], v[112:115]\n"
    "s_waitcnt lgkmcnt(3)\n"
    "v_mfma_f32_16x16x32_bf16 a[0:3], v[168:171], v[232:235], a[0:3]\n"
    "v_mfma_f32_16x16x32_bf16 a[0:3], v[172:175], v[80:83], a[0:3]\n"
    "s_waitcnt lgkmcnt(1)\n"
    "v_mfma_f32_16x16x32_bf16 a[16:19], v[176:179], v[232:235], a[16:19]\n"
    "ds_read_b128 v[164:167], v201 offset:5120\n"
    "v_mfma_f32_16x16x32_bf16 a[16:19], v[180:183], v[80:83], a[16:19]\n"
    "v_mfma_f32_16x16x32_bf16 v[116:119], v[192:195], v[236:239], v[116:119]\n"
    "v_mul_f32 v222, s68, v112\n"
    "v_rcp_f32 v222, v222\n"
    "v_mfma_f32_16x16x32_bf16 v[116:119], v[192:195], v[84:87], v[116:119]\n"
    "v_accvgpr_read_b32 v96, a0\n"
    "v_accvgpr_read_b32 v97, a1\n"
    "v_accvgpr_read_b32 v98, a2\n"
    "v_mfma_f32_16x16x32_bf16 a[4:7], v[168:171], v[236:239], a[4:7]\n"
    "v_accvgpr_read_b32 v99, a3\n"
    "v_fma_f32 v96, v96, v222, s94\n"
    "v_fma_f32 v97, v97, v222, s94\n"
    "v_mfma_f32_16x16x32_bf16 a[4:7], v[172:175], v[84:87], a[4:7]\n"
    "v_fma_f32 v98, v98, v222, s94\n"
    "v_fma_f32 v99, v99, v222, s94\n"
    "v_cvt_pk_bf16_f32 v48, v96, v97\n"
    "v_mfma_f32_16x16x32_bf16 a[20:23], v[176:179], v[236:239], a[20:23]\n"
    "v_cvt_pk_bf16_f32 v49, v98, v99\n"
    "v_accvgpr_read_b32 v100, a16\n"
    "v_accvgpr_read_b32 v101, a17\n"
    "v_mfma_f32_16x16x32_bf16 a[20:23], v[180:183], v[84:87], a[20:23]\n"
    "v_accvgpr_read_b32 v102, a18\n"
    "v_accvgpr_read_b32 v103, a19\n"
    "v_fma_f32 v100, v100, v222, s94\n"
    "v_mfma_f32_16x16x32_bf16 v[120:123], v[192:195], v[240:243], v[120:123]\n"
    "v_fma_f32 v101, v101, v222, s94\n"
    "v_fma_f32 v102, v102, v222, s94\n"
    "v_fma_f32 v103, v103, v222, s94\n"
    "v_mfma_f32_16x16x32_bf16 v[120:123], v[192:195], v[88:91], v[120:123]\n"
    "v_cvt_pk_bf16_f32 v50, v100, v101\n"
    "v_cvt_pk_bf16_f32 v51, v102, v103\n"
    "v_mfma_f32_16x16x32_bf16 a[8:11], v[168:171], v[240:243], a[8:11]\n"
    "buffer_store_dwordx4 v[48:51], v205, s[28:31], 0 offen sc0 sc1\n"
    "v_mul_f32 v224, s68, v116\n"
    "v_rcp_f32 v224, v224\n"
    "v_accvgpr_read_b32 v104, a4\n"
    "v_mfma_f32_16x16x32_bf16 a[8:11], v[172:175], v[88:91], a[8:11]\n"
    "v_accvgpr_read_b32 v105, a5\n"
    "v_accvgpr_read_b32 v106, a6\n"
    "v_accvgpr_read_b32 v107, a7\n"
    "v_mfma_f32_16x16x32_bf16 a[24:27], v[176:179], v[240:243], a[24:27]\n"
    "v_fma_f32 v104, v104, v224, s94\n"
    "v_fma_f32 v105, v105, v224, s94\n"
    "v_fma_f32 v106, v106, v224, s94\n"
    "v_mfma_f32_16x16x32_bf16 a[24:27], v[180:183], v[88:91], a[24:27]\n"
    "v_fma_f32 v107, v107, v224, s94\n"
    "v_cvt_pk_bf16_f32 v52, v104, v105\n"
    "v_cvt_pk_bf16_f32 v53, v106, v107\n"
    "v_mfma_f32_16x16x32_bf16 v[124:127], v[192:195], v[244:247], v[124:127]\n"
    "v_accvgpr_read_b32 v108, a20\n"
    "v_accvgpr_read_b32 v109, a21\n"
    "v_accvgpr_read_b32 v110, a22\n"
    "v_mfma_f32_16x16x32_bf16 v[124:127], v[192:195], v[92:95], v[124:127]\n"
    "v_accvgpr_read_b32 v111, a23\n"
    "v_fma_f32 v108, v108, v224, s94\n"
    "v_fma_f32 v109, v109, v224, s94\n"
    "v_mfma_f32_16x16x32_bf16 a[12:15], v[168:171], v[244:247], a[12:15]\n"
    "v_fma_f32 v110, v110, v224, s94\n"
    "v_fma_f32 v111, v111, v224, s94\n"
    "v_mfma_f32_16x16x32_bf16 a[12:15], v[172:175], v[92:95], a[12:15]\n"
    "v_cvt_pk_bf16_f32 v54, v108, v109\n"
    "v_cvt_pk_bf16_f32 v55, v110, v111\n"
    "v_mfma_f32_16x16x32_bf16 a[28:31], v[176:179], v[244:247], a[28:31]\n"
    "ds_read_b128 v[168:171], v201 offset:28672\n"
    "v_mfma_f32_16x16x32_bf16 a[28:31], v[180:183], v[92:95], a[28:31]\n"
    "s_waitcnt lgkmcnt(1)\n"
    "ds_read_b128 v[172:175], v201 offset:29696\n"
    "v_mfma_f32_16x16x32_bf16 a[32:35], v[160:163], v[128:131], a[32:35]\n"
    "ds_read_b128 v[176:179], v207 offset:4096\n"
    "v_mfma_f32_16x16x32_bf16 a[32:35], v[164:167], v[144:147], a[32:35]\n"
    "buffer_store_dwordx4 v[52:55], v205, s[28:31], s62 offen sc0 sc1\n"
    "v_mul_f32 v226, s68, v120\n"
    "v_rcp_f32 v226, v226\n"
    "v_mfma_f32_16x16x32_bf16 a[36:39], v[160:163], v[132:135], a[36:39]\n"
    "v_accvgpr_read_b32 v96, a8\n"
    "v_accvgpr_read_b32 v97, a9\n"
    "v_mfma_f32_16x16x32_bf16 a[36:39], v[164:167], v[148:151], a[36:39]\n"
    "v_accvgpr_read_b32 v98, a10\n"
    "v_accvgpr_read_b32 v99, a11\n"
    "v_mfma_f32_16x16x32_bf16 a[40:43], v[160:163], v[136:139], a[40:43]\n"
    "ds_read_b128 v[180:183], v207 offset:5120\n"
    "v_fma_f32 v96, v96, v226, s94\n"
    "v_mfma_f32_16x16x32_bf16 a[40:43], v[164:167], v[152:155], a[40:43]\n"
    "v_fma_f32 v97, v97, v226, s94\n"
    "v_fma_f32 v98, v98, v226, s94\n"
    "v_mfma_f32_16x16x32_bf16 a[44:47], v[160:163], v[140:143], a[44:47]\n"
    "v_fma_f32 v99, v99, v226, s94\n"
    "v_cvt_pk_bf16_f32 v56, v96, v97\n"
    "v_mfma_f32_16x16x32_bf16 a[44:47], v[164:167], v[156:159], a[44:47]\n"
    "s_waitcnt lgkmcnt(2)\n"
    "v_cvt_pk_bf16_f32 v57, v98, v99\n"
    "v_accvgpr_read_b32 v100, a24\n"
    "v_mfma_f32_16x16x32_bf16 a[44:47], v[168:171], v[44:47], a[44:47]\n"
    "ds_read_b128 v[160:163], v201 offset:6144\n"
    "v_accvgpr_read_b32 v101, a25\n"
    "v_mfma_f32_16x16x32_bf16 a[44:47], v[172:175], v[76:79], a[44:47]\n"
    "v_accvgpr_read_b32 v102, a26\n"
    "v_accvgpr_read_b32 v103, a27\n"
    "v_mfma_f32_16x16x32_bf16 a[40:43], v[168:171], v[40:43], a[40:43]\n"
    "v_fma_f32 v100, v100, v226, s94\n"
    "v_fma_f32 v101, v101, v226, s94\n"
    "v_mfma_f32_16x16x32_bf16 a[40:43], v[172:175], v[72:75], a[40:43]\n"
    "v_fma_f32 v102, v102, v226, s94\n"
    "v_fma_f32 v103, v103, v226, s94\n"
    "v_mfma_f32_16x16x32_bf16 a[36:39], v[168:171], v[36:39], a[36:39]\n"
    "ds_read_b128 v[164:167], v201 offset:7168\n"
    "v_cvt_pk_bf16_f32 v58, v100, v101\n"
    "v_mfma_f32_16x16x32_bf16 a[36:39], v[172:175], v[68:71], a[36:39]\n"
    "v_cvt_pk_bf16_f32 v59, v102, v103\n"
    "v_mfma_f32_16x16x32_bf16 a[32:35], v[168:171], v[32:35], a[32:35]\n"
    "buffer_store_dwordx4 v[56:59], v205, s[28:31], s63 offen sc0 sc1\n"
    "v_mul_f32 v228, s68, v124\n"
    "v_rcp_f32 v228, v228\n"
    "v_mfma_f32_16x16x32_bf16 a[32:35], v[172:175], v[64:67], a[32:35]\n"
    "s_waitcnt lgkmcnt(2)\n"
    "v_accvgpr_read_b32 v104, a12\n"
    "v_accvgpr_read_b32 v105, a13\n"
    "v_mfma_f32_16x16x32_bf16 a[32:35], v[176:179], v[232:235], a[32:35]\n"
    "ds_read_b128 v[168:171], v201 offset:30720\n"
    "v_accvgpr_read_b32 v106, a14\n"
    "v_mfma_f32_16x16x32_bf16 a[32:35], v[180:183], v[80:83], a[32:35]\n"
    "v_accvgpr_read_b32 v107, a15\n"
    "v_fma_f32 v104, v104, v228, s94\n"
    "v_mfma_f32_16x16x32_bf16 a[36:39], v[176:179], v[236:239], a[36:39]\n"
    "v_fma_f32 v105, v105, v228, s94\n"
    "v_fma_f32 v106, v106, v228, s94\n"
    "v_mfma_f32_16x16x32_bf16 a[36:39], v[180:183], v[84:87], a[36:39]\n"
    "v_fma_f32 v107, v107, v228, s94\n"
    "v_cvt_pk_bf16_f32 v60, v104, v105\n"
    "v_mfma_f32_16x16x32_bf16 a[40:43], v[176:179], v[240:243], a[40:43]\n"
    "ds_read_b128 v[172:175], v201 offset:31744\n"
    "v_cvt_pk_bf16_f32 v61, v106, v107\n"
    "v_mfma_f32_16x16x32_bf16 a[40:43], v[180:183], v[88:91], a[40:43]\n"
    "v_accvgpr_read_b32 v108, a28\n"
    "v_accvgpr_read_b32 v109, a29\n"
    "v_mfma_f32_16x16x32_bf16 a[44:47], v[176:179], v[244:247], a[44:47]\n"
    "v_accvgpr_read_b32 v110, a30\n"
    "v_accvgpr_read_b32 v111, a31\n"
    "v_mfma_f32_16x16x32_bf16 a[44:47], v[180:183], v[92:95], a[44:47]\n"
    "s_waitcnt lgkmcnt(2)\n"
    "v_fma_f32 v108, v108, v228, s94\n"
    "v_fma_f32 v109, v109, v228, s94\n"
    "v_mfma_f32_16x16x32_bf16 a[48:51], v[160:163], v[128:131], a[48:51]\n"
    "ds_read_b128 v[176:179], v207 offset:6144\n"
    "v_fma_f32 v110, v110, v228, s94\n"
    "v_mfma_f32_16x16x32_bf16 a[48:51], v[164:167], v[144:147], a[48:51]\n"
    "v_fma_f32 v111, v111, v228, s94\n"
    "v_cvt_pk_bf16_f32 v62, v108, v109\n"
    "v_mfma_f32_16x16x32_bf16 a[52:55], v[160:163], v[132:135], a[52:55]\n"
    "v_cvt_pk_bf16_f32 v63, v110, v111\n"
    "v_mfma_f32_16x16x32_bf16 a[52:55], v[164:167], v[148:151], a[52:55]\n"
    "buffer_store_dwordx4 v[60:63], v205, s[28:31], s64 offen sc0 sc1\n"
    "v_accvgpr_read_b32 v96, a32\n"
    "v_accvgpr_read_b32 v97, a33\n"
    "v_accvgpr_read_b32 v98, a34\n"
    "v_mfma_f32_16x16x32_bf16 a[56:59], v[160:163], v[136:139], a[56:59]\n"
    "ds_read_b128 v[180:183], v207 offset:7168\n"
    "v_accvgpr_read_b32 v99, a35\n"
    "v_mfma_f32_16x16x32_bf16 a[56:59], v[164:167], v[152:155], a[56:59]\n"
    "v_fma_f32 v96, v96, v222, s94\n"
    "v_fma_f32 v97, v97, v222, s94\n"
    "v_mfma_f32_16x16x32_bf16 a[60:63], v[160:163], v[140:143], a[60:63]\n"
    "v_fma_f32 v98, v98, v222, s94\n"
    "v_fma_f32 v99, v99, v222, s94\n"
    "v_mfma_f32_16x16x32_bf16 a[60:63], v[164:167], v[156:159], a[60:63]\n"
    "s_waitcnt lgkmcnt(2)\n"
    "v_cvt_pk_bf16_f32 v184, v96, v97\n"
    "v_cvt_pk_bf16_f32 v185, v98, v99\n"
    "v_mfma_f32_16x16x32_bf16 a[60:63], v[168:171], v[44:47], a[60:63]\n"
    "ds_read_b128 v[160:163], v201 offset:8192\n"
    "v_accvgpr_read_b32 v100, a36\n"
    "v_mfma_f32_16x16x32_bf16 a[60:63], v[172:175], v[76:79], a[60:63]\n"
    "v_accvgpr_read_b32 v101, a37\n"
    "v_accvgpr_read_b32 v102, a38\n"
    "v_mfma_f32_16x16x32_bf16 a[56:59], v[168:171], v[40:43], a[56:59]\n"
    "v_accvgpr_read_b32 v103, a39\n"
    "v_fma_f32 v100, v100, v224, s94\n"
    "v_mfma_f32_16x16x32_bf16 a[56:59], v[172:175], v[72:75], a[56:59]\n"
    "v_fma_f32 v101, v101, v224, s94\n"
    "v_fma_f32 v102, v102, v224, s94\n"
    "v_mfma_f32_16x16x32_bf16 a[52:55], v[168:171], v[36:39], a[52:55]\n"
    "ds_read_b128 v[164:167], v201 offset:9216\n"
    "v_fma_f32 v103, v103, v224, s94\n"
    "v_mfma_f32_16x16x32_bf16 a[52:55], v[172:175], v[68:71], a[52:55]\n"
    "v_cvt_pk_bf16_f32 v188, v100, v101\n"
    "v_cvt_pk_bf16_f32 v189, v102, v103\n"
    "v_mfma_f32_16x16x32_bf16 a[48:51], v[168:171], v[32:35], a[48:51]\n"
    "v_accvgpr_read_b32 v104, a40\n"
    "v_accvgpr_read_b32 v105, a41\n"
    "v_mfma_f32_16x16x32_bf16 a[48:51], v[172:175], v[64:67], a[48:51]\n"
    "s_waitcnt lgkmcnt(2)\n"
    "v_accvgpr_read_b32 v106, a42\n"
    "v_accvgpr_read_b32 v107, a43\n"
    "v_mfma_f32_16x16x32_bf16 a[48:51], v[176:179], v[232:235], a[48:51]\n"
    "ds_read_b128 v[168:171], v201 offset:32768\n"
    "v_fma_f32 v104, v104, v226, s94\n"
    "v_mfma_f32_16x16x32_bf16 a[48:51], v[180:183], v[80:83], a[48:51]\n"
    "v_fma_f32 v105, v105, v226, s94\n"
    "v_fma_f32 v106, v106, v226, s94\n"
    "v_mfma_f32_16x16x32_bf16 a[52:55], v[176:179], v[236:239], a[52:55]\n"
    "v_fma_f32 v107, v107, v226, s94\n"
    "v_cvt_pk_bf16_f32 v248, v104, v105\n"
    "v_mfma_f32_16x16x32_bf16 a[52:55], v[180:183], v[84:87], a[52:55]\n"
    "v_cvt_pk_bf16_f32 v249, v106, v107\n"
    "v_accvgpr_read_b32 v108, a44\n"
    "v_mfma_f32_16x16x32_bf16 a[56:59], v[176:179], v[240:243], a[56:59]\n"
    "ds_read_b128 v[172:175], v201 offset:33792\n"
    "v_accvgpr_read_b32 v109, a45\n"
    "v_mfma_f32_16x16x32_bf16 a[56:59], v[180:183], v[88:91], a[56:59]\n"
    "v_accvgpr_read_b32 v110, a46\n"
    "v_accvgpr_read_b32 v111, a47\n"
    "v_mfma_f32_16x16x32_bf16 a[60:63], v[176:179], v[244:247], a[60:63]\n"
    "v_fma_f32 v108, v108, v228, s94\n"
    "v_fma_f32 v109, v109, v228, s94\n"
    "v_mfma_f32_16x16x32_bf16 a[60:63], v[180:183], v[92:95], a[60:63]\n"
    "s_waitcnt lgkmcnt(2)\n"
    "v_fma_f32 v110, v110, v228, s94\n"
    "v_fma_f32 v111, v111, v228, s94\n"
    "v_mfma_f32_16x16x32_bf16 a[64:67], v[160:163], v[128:131], a[64:67]\n"
    "ds_read_b128 v[176:179], v207 offset:8192\n"
    "v_cvt_pk_bf16_f32 v252, v108, v109\n"
    "v_mfma_f32_16x16x32_bf16 a[64:67], v[164:167], v[144:147], a[64:67]\n"
    "v_cvt_pk_bf16_f32 v253, v110, v111\n"
    "v_accvgpr_read_b32 v96, a48\n"
    "v_mfma_f32_16x16x32_bf16 a[68:71], v[160:163], v[132:135], a[68:71]\n"
    "v_accvgpr_read_b32 v97, a49\n"
    "v_accvgpr_read_b32 v98, a50\n"
    "v_mfma_f32_16x16x32_bf16 a[68:71], v[164:167], v[148:151], a[68:71]\n"
    "v_accvgpr_read_b32 v99, a51\n"
    "v_fma_f32 v96, v96, v222, s94\n"
    "v_mfma_f32_16x16x32_bf16 a[72:75], v[160:163], v[136:139], a[72:75]\n"
    "ds_read_b128 v[180:183], v207 offset:9216\n"
    "v_fma_f32 v97, v97, v222, s94\n"
    "v_mfma_f32_16x16x32_bf16 a[72:75], v[164:167], v[152:155], a[72:75]\n"
    "v_fma_f32 v98, v98, v222, s94\n"
    "v_fma_f32 v99, v99, v222, s94\n"
    "v_mfma_f32_16x16x32_bf16 a[76:79], v[160:163], v[140:143], a[76:79]\n"
    "v_cvt_pk_bf16_f32 v186, v96, v97\n"
    "v_cvt_pk_bf16_f32 v187, v98, v99\n"
    "v_mfma_f32_16x16x32_bf16 a[76:79], v[164:167], v[156:159], a[76:79]\n"
    "s_waitcnt lgkmcnt(2)\n"
    "buffer_store_dwordx4 v[184:187], v205, s[28:31], 0 offen offset:64 sc0 sc1\n"
    "v_accvgpr_read_b32 v100, a52\n"
    "v_accvgpr_read_b32 v101, a53\n"
    "v_accvgpr_read_b32 v102, a54\n"
    "v_mfma_f32_16x16x32_bf16 a[76:79], v[168:171], v[44:47], a[76:79]\n"
    "ds_read_b128 v[160:163], v201 offset:10240\n"
    "v_accvgpr_read_b32 v103, a55\n"
    "v_mfma_f32_16x16x32_bf16 a[76:79], v[172:175], v[76:79], a[76:79]\n"
    "v_fma_f32 v100, v100, v224, s94\n"
    "v_fma_f32 v101, v101, v224, s94\n"
    "v_mfma_f32_16x16x32_bf16 a[72:75], v[168:171], v[40:43], a[72:75]\n"
    "v_fma_f32 v102, v102, v224, s94\n"
    "v_fma_f32 v103, v103, v224, s94\n"
    "v_mfma_f32_16x16x32_bf16 a[72:75], v[172:175], v[72:75], a[72:75]\n"
    "v_cvt_pk_bf16_f32 v190, v100, v101\n"
    "v_cvt_pk_bf16_f32 v191, v102, v103\n"
    "v_mfma_f32_16x16x32_bf16 a[68:71], v[168:171], v[36:39], a[68:71]\n"
    "ds_read_b128 v[164:167], v201 offset:11264\n"
    "v_mfma_f32_16x16x32_bf16 a[68:71], v[172:175], v[68:71], a[68:71]\n"
    "buffer_store_dwordx4 v[188:191], v205, s[28:31], s62 offen offset:64 sc0 sc1\n"
    "v_accvgpr_read_b32 v104, a56\n"
    "v_accvgpr_read_b32 v105, a57\n"
    "v_accvgpr_read_b32 v106, a58\n"
    "v_mfma_f32_16x16x32_bf16 a[64:67], v[168:171], v[32:35], a[64:67]\n"
    "v_accvgpr_read_b32 v107, a59\n"
    "v_fma_f32 v104, v104, v226, s94\n"
    "v_mfma_f32_16x16x32_bf16 a[64:67], v[172:175], v[64:67], a[64:67]\n"
    "s_waitcnt lgkmcnt(2)\n"
    "v_fma_f32 v105, v105, v226, s94\n"
    "v_fma_f32 v106, v106, v226, s94\n"
    "v_mfma_f32_16x16x32_bf16 a[64:67], v[176:179], v[232:235], a[64:67]\n"
    "ds_read_b128 v[168:171], v201 offset:34816\n"
    "v_fma_f32 v107, v107, v226, s94\n"
    "v_mfma_f32_16x16x32_bf16 a[64:67], v[180:183], v[80:83], a[64:67]\n"
    "v_cvt_pk_bf16_f32 v250, v104, v105\n"
    "v_cvt_pk_bf16_f32 v251, v106, v107\n"
    "v_mfma_f32_16x16x32_bf16 a[68:71], v[176:179], v[236:239], a[68:71]\n"
    "buffer_store_dwordx4 v[248:251], v205, s[28:31], s63 offen offset:64 sc0 sc1\n"
    "v_accvgpr_read_b32 v108, a60\n"
    "v_accvgpr_read_b32 v109, a61\n"
    "v_accvgpr_read_b32 v110, a62\n"
    "v_mfma_f32_16x16x32_bf16 a[68:71], v[180:183], v[84:87], a[68:71]\n"
    "v_accvgpr_read_b32 v111, a63\n"
    "v_fma_f32 v108, v108, v228, s94\n"
    "v_mfma_f32_16x16x32_bf16 a[72:75], v[176:179], v[240:243], a[72:75]\n"
    "ds_read_b128 v[172:175], v201 offset:35840\n"
    "v_fma_f32 v109, v109, v228, s94\n"
    "v_mfma_f32_16x16x32_bf16 a[72:75], v[180:183], v[88:91], a[72:75]\n"
    "v_fma_f32 v110, v110, v228, s94\n"
    "v_fma_f32 v111, v111, v228, s94\n"
    "v_mfma_f32_16x16x32_bf16 a[76:79], v[176:179], v[244:247], a[76:79]\n"
    "v_cvt_pk_bf16_f32 v254, v108, v109\n"
    "v_cvt_pk_bf16_f32 v255, v110, v111\n"
    "v_mfma_f32_16x16x32_bf16 a[76:79], v[180:183], v[92:95], a[76:79]\n"
    "s_waitcnt lgkmcnt(2)\n"
    "buffer_store_dwordx4 v[252:255], v205, s[28:31], s64 offen offset:64 sc0 sc1\n"
    "v_accvgpr_read_b32 v96, a64\n"
    "v_accvgpr_read_b32 v97, a65\n"
    "v_accvgpr_read_b32 v98, a66\n"
    "v_mfma_f32_16x16x32_bf16 a[80:83], v[160:163], v[128:131], a[80:83]\n"
    "ds_read_b128 v[176:179], v207 offset:10240\n"
    "v_accvgpr_read_b32 v99, a67\n"
    "v_mfma_f32_16x16x32_bf16 a[80:83], v[164:167], v[144:147], a[80:83]\n"
    "v_fma_f32 v96, v96, v222, s94\n"
    "v_fma_f32 v97, v97, v222, s94\n"
    "v_mfma_f32_16x16x32_bf16 a[84:87], v[160:163], v[132:135], a[84:87]\n"
    "v_fma_f32 v98, v98, v222, s94\n"
    "v_fma_f32 v99, v99, v222, s94\n"
    "v_mfma_f32_16x16x32_bf16 a[84:87], v[164:167], v[148:151], a[84:87]\n"
    "v_cvt_pk_bf16_f32 v48, v96, v97\n"
    "v_cvt_pk_bf16_f32 v49, v98, v99\n"
    "v_mfma_f32_16x16x32_bf16 a[88:91], v[160:163], v[136:139], a[88:91]\n"
    "ds_read_b128 v[180:183], v207 offset:11264\n"
    "v_accvgpr_read_b32 v100, a68\n"
    "v_mfma_f32_16x16x32_bf16 a[88:91], v[164:167], v[152:155], a[88:91]\n"
    "v_accvgpr_read_b32 v101, a69\n"
    "v_accvgpr_read_b32 v102, a70\n"
    "v_mfma_f32_16x16x32_bf16 a[92:95], v[160:163], v[140:143], a[92:95]\n"
    "v_accvgpr_read_b32 v103, a71\n"
    "v_fma_f32 v100, v100, v224, s94\n"
    "v_mfma_f32_16x16x32_bf16 a[92:95], v[164:167], v[156:159], a[92:95]\n"
    "s_waitcnt lgkmcnt(2)\n"
    "v_fma_f32 v101, v101, v224, s94\n"
    "v_fma_f32 v102, v102, v224, s94\n"
    "v_mfma_f32_16x16x32_bf16 a[92:95], v[168:171], v[44:47], a[92:95]\n"
    "ds_read_b128 v[160:163], v201 offset:12288\n"
    "v_fma_f32 v103, v103, v224, s94\n"
    "v_mfma_f32_16x16x32_bf16 a[92:95], v[172:175], v[76:79], a[92:95]\n"
    "v_cvt_pk_bf16_f32 v52, v100, v101\n"
    "v_cvt_pk_bf16_f32 v53, v102, v103\n"
    "v_mfma_f32_16x16x32_bf16 a[88:91], v[168:171], v[40:43], a[88:91]\n"
    "v_accvgpr_read_b32 v104, a72\n"
    "v_accvgpr_read_b32 v105, a73\n"
    "v_mfma_f32_16x16x32_bf16 a[88:91], v[172:175], v[72:75], a[88:91]\n"
    "v_accvgpr_read_b32 v106, a74\n"
    "v_accvgpr_read_b32 v107, a75\n"
    "v_mfma_f32_16x16x32_bf16 a[84:87], v[168:171], v[36:39], a[84:87]\n"
    "ds_read_b128 v[164:167], v201 offset:13312\n"
    "v_fma_f32 v104, v104, v226, s94\n"
    "v_mfma_f32_16x16x32_bf16 a[84:87], v[172:175], v[68:71], a[84:87]\n"
    "v_fma_f32 v105, v105, v226, s94\n"
    "v_fma_f32 v106, v106, v226, s94\n"
    "v_mfma_f32_16x16x32_bf16 a[80:83], v[168:171], v[32:35], a[80:83]\n"
    "v_fma_f32 v107, v107, v226, s94\n"
    "v_cvt_pk_bf16_f32 v56, v104, v105\n"
    "v_mfma_f32_16x16x32_bf16 a[80:83], v[172:175], v[64:67], a[80:83]\n"
    "s_waitcnt lgkmcnt(2)\n"
    "v_cvt_pk_bf16_f32 v57, v106, v107\n"
    "v_accvgpr_read_b32 v108, a76\n"
    "v_mfma_f32_16x16x32_bf16 a[80:83], v[176:179], v[232:235], a[80:83]\n"
    "ds_read_b128 v[168:171], v201 offset:36864\n"
    "v_accvgpr_read_b32 v109, a77\n"
    "v_mfma_f32_16x16x32_bf16 a[80:83], v[180:183], v[80:83], a[80:83]\n"
    "v_accvgpr_read_b32 v110, a78\n"
    "v_accvgpr_read_b32 v111, a79\n"
    "v_mfma_f32_16x16x32_bf16 a[84:87], v[176:179], v[236:239], a[84:87]\n"
    "v_fma_f32 v108, v108, v228, s94\n"
    "v_fma_f32 v109, v109, v228, s94\n"
    "v_mfma_f32_16x16x32_bf16 a[84:87], v[180:183], v[84:87], a[84:87]\n"
    "v_fma_f32 v110, v110, v228, s94\n"
    "v_fma_f32 v111, v111, v228, s94\n"
    "v_mfma_f32_16x16x32_bf16 a[88:91], v[176:179], v[240:243], a[88:91]\n"
    "ds_read_b128 v[172:175], v201 offset:37888\n"
    "v_cvt_pk_bf16_f32 v60, v108, v109\n"
    "v_mfma_f32_16x16x32_bf16 a[88:91], v[180:183], v[88:91], a[88:91]\n"
    "v_cvt_pk_bf16_f32 v61, v110, v111\n"
    "v_mfma_f32_16x16x32_bf16 a[92:95], v[176:179], v[244:247], a[92:95]\n"
    "v_mfma_f32_16x16x32_bf16 a[92:95], v[180:183], v[92:95], a[92:95]\n"
    "s_waitcnt lgkmcnt(2)\n"
    "v_accvgpr_read_b32 v96, a80\n"
    "v_accvgpr_read_b32 v97, a81\n"
    "v_mfma_f32_16x16x32_bf16 a[96:99], v[160:163], v[128:131], a[96:99]\n"
    "ds_read_b128 v[176:179], v207 offset:12288\n"
    "v_accvgpr_read_b32 v98, a82\n"
    "v_mfma_f32_16x16x32_bf16 a[96:99], v[164:167], v[144:147], a[96:99]\n"
    "v_accvgpr_read_b32 v99, a83\n"
    "v_fma_f32 v96, v96, v222, s94\n"
    "v_mfma_f32_16x16x32_bf16 a[100:103], v[160:163], v[132:135], a[100:103]\n"
    "v_fma_f32 v97, v97, v222, s94\n"
    "v_fma_f32 v98, v98, v222, s94\n"
    "v_mfma_f32_16x16x32_bf16 a[100:103], v[164:167], v[148:151], a[100:103]\n"
    "v_fma_f32 v99, v99, v222, s94\n"
    "v_cvt_pk_bf16_f32 v50, v96, v97\n"
    "v_mfma_f32_16x16x32_bf16 a[104:107], v[160:163], v[136:139], a[104:107]\n"
    "ds_read_b128 v[180:183], v207 offset:13312\n"
    "v_cvt_pk_bf16_f32 v51, v98, v99\n"
    "v_mfma_f32_16x16x32_bf16 a[104:107], v[164:167], v[152:155], a[104:107]\n"
    "buffer_store_dwordx4 v[48:51], v205, s[28:31], 0 offen offset:128 sc0 sc1\n"
    "v_accvgpr_read_b32 v100, a84\n"
    "v_accvgpr_read_b32 v101, a85\n"
    "v_accvgpr_read_b32 v102, a86\n"
    "v_mfma_f32_16x16x32_bf16 a[108:111], v[160:163], v[140:143], a[108:111]\n"
    "v_accvgpr_read_b32 v103, a87\n"
    "v_fma_f32 v100, v100, v224, s94\n"
    "v_mfma_f32_16x16x32_bf16 a[108:111], v[164:167], v[156:159], a[108:111]\n"
    "s_waitcnt lgkmcnt(2)\n"
    "s_cmp_eq_u32 s57, 0\n"
    "s_cselect_b32 s77, s74, 0\n"
    "v_mov_b32 v196, 0x40000000\n"
    "v_mov_b32 v197, 0x800\n"
    "v_cmp_gt_u32 vcc, s77, v231\n"
    "v_mov_b32 v198, 0\n"
    "v_mov_b32 v198, 0\n"
    "v_cndmask_b32 v197, v196, v197, vcc\n"
    "buffer_store_dword v221, v197, s[44:47], 0 offen sc1\n"
    "v_mfma_f32_16x16x32_bf16 a[108:111], v[168:171], v[44:47], a[108:111]\n"
    "ds_read_b128 v[160:163], v201 offset:14336\n"
    "v_fma_f32 v101, v101, v224, s94\n"
    "v_mfma_f32_16x16x32_bf16 a[108:111], v[172:175], v[76:79], a[108:111]\n"
    "v_fma_f32 v102, v102, v224, s94\n"
    "v_fma_f32 v103, v103, v224, s94\n"
    "v_mfma_f32_16x16x32_bf16 a[104:107], v[168:171], v[40:43], a[104:107]\n"
    "v_cvt_pk_bf16_f32 v54, v100, v101\n"
    "v_cvt_pk_bf16_f32 v55, v102, v103\n"
    "v_mfma_f32_16x16x32_bf16 a[104:107], v[172:175], v[72:75], a[104:107]\n"
    "buffer_store_dwordx4 v[52:55], v205, s[28:31], s62 offen offset:128 sc0 sc1\n"
    "v_accvgpr_read_b32 v104, a88\n"
    "v_accvgpr_read_b32 v105, a89\n"
    "v_accvgpr_read_b32 v106, a90\n"
    "v_mfma_f32_16x16x32_bf16 a[100:103], v[168:171], v[36:39], a[100:103]\n"
    "ds_read_b128 v[164:167], v201 offset:15360\n"
    "v_accvgpr_read_b32 v107, a91\n"
    "v_mfma_f32_16x16x32_bf16 a[100:103], v[172:175], v[68:71], a[100:103]\n"
    "v_fma_f32 v104, v104, v226, s94\n"
    "v_fma_f32 v105, v105, v226, s94\n"
    "v_mfma_f32_16x16x32_bf16 a[96:99], v[168:171], v[32:35], a[96:99]\n"
    "v_fma_f32 v106, v106, v226, s94\n"
    "v_fma_f32 v107, v107, v226, s94\n"
    "v_mfma_f32_16x16x32_bf16 a[96:99], v[172:175], v[64:67], a[96:99]\n"
    "s_waitcnt lgkmcnt(2)\n"
    "v_cvt_pk_bf16_f32 v58, v104, v105\n"
    "v_cvt_pk_bf16_f32 v59, v106, v107\n"
    "v_mfma_f32_16x16x32_bf16 a[96:99], v[176:179], v[232:235], a[96:99]\n"
    "ds_read_b128 v[168:171], v201 offset:38912\n"
    "v_mfma_f32_16x16x32_bf16 a[96:99], v[180:183], v[80:83], a[96:99]\n"
    "buffer_store_dwordx4 v[56:59], v205, s[28:31], s63 offen offset:128 sc0 sc1\n"
    "v_accvgpr_read_b32 v108, a92\n"
    "v_accvgpr_read_b32 v109, a93\n"
    "v_accvgpr_read_b32 v110, a94\n"
    "v_mfma_f32_16x16x32_bf16 a[100:103], v[176:179], v[236:239], a[100:103]\n"
    "v_accvgpr_read_b32 v111, a95\n"
    "v_fma_f32 v108, v108, v228, s94\n"
    "v_mfma_f32_16x16x32_bf16 a[100:103], v[180:183], v[84:87], a[100:103]\n"
    "v_fma_f32 v109, v109, v228, s94\n"
    "v_fma_f32 v110, v110, v228, s94\n"
    "v_mfma_f32_16x16x32_bf16 a[104:107], v[176:179], v[240:243], a[104:107]\n"
    "ds_read_b128 v[172:175], v201 offset:39936\n"
    "v_fma_f32 v111, v111, v228, s94\n"
    "v_mfma_f32_16x16x32_bf16 a[104:107], v[180:183], v[88:91], a[104:107]\n"
    "v_cvt_pk_bf16_f32 v62, v108, v109\n"
    "v_cvt_pk_bf16_f32 v63, v110, v111\n"
    "v_mfma_f32_16x16x32_bf16 a[108:111], v[176:179], v[244:247], a[108:111]\n"
    "buffer_store_dwordx4 v[60:63], v205, s[28:31], s64 offen offset:128 sc0 sc1\n"
    "v_mfma_f32_16x16x32_bf16 a[108:111], v[180:183], v[92:95], a[108:111]\n"
    "s_waitcnt lgkmcnt(2)\n"
    "v_accvgpr_read_b32 v96, a96\n"
    "v_accvgpr_read_b32 v97, a97\n"
    "v_mfma_f32_16x16x32_bf16 a[112:115], v[160:163], v[128:131], a[112:115]\n"
    "ds_read_b128 v[176:179], v207 offset:14336\n"
    "v_accvgpr_read_b32 v98, a98\n"
    "v_mfma_f32_16x16x32_bf16 a[112:115], v[164:167], v[144:147], a[112:115]\n"
    "v_accvgpr_read_b32 v99, a99\n"
    "v_fma_f32 v96, v96, v222, s94\n"
    "v_mfma_f32_16x16x32_bf16 a[116:119], v[160:163], v[132:135], a[116:119]\n"
    "v_fma_f32 v97, v97, v222, s94\n"
    "v_fma_f32 v98, v98, v222, s94\n"
    "v_mfma_f32_16x16x32_bf16 a[116:119], v[164:167], v[148:151], a[116:119]\n"
    "v_fma_f32 v99, v99, v222, s94\n"
    "v_cvt_pk_bf16_f32 v184, v96, v97\n"
    "v_mfma_f32_16x16x32_bf16 a[120:123], v[160:163], v[136:139], a[120:123]\n"
    "ds_read_b128 v[180:183], v207 offset:15360\n"
    "v_cvt_pk_bf16_f32 v185, v98, v99\n"
    "v_mfma_f32_16x16x32_bf16 a[120:123], v[164:167], v[152:155], a[120:123]\n"
    "v_accvgpr_read_b32 v100, a100\n"
    "v_accvgpr_read_b32 v101, a101\n"
    "v_mfma_f32_16x16x32_bf16 a[124:127], v[160:163], v[140:143], a[124:127]\n"
    "v_accvgpr_read_b32 v102, a102\n"
    "v_accvgpr_read_b32 v103, a103\n"
    "v_mfma_f32_16x16x32_bf16 a[124:127], v[164:167], v[156:159], a[124:127]\n"
    "s_waitcnt lgkmcnt(2)\n"
    "v_fma_f32 v100, v100, v224, s94\n"
    "v_fma_f32 v101, v101, v224, s94\n"
    "v_mfma_f32_16x16x32_bf16 a[124:127], v[168:171], v[44:47], a[124:127]\n"
    "ds_read_b128 v[160:163], v201 offset:16384\n"
    "v_fma_f32 v102, v102, v224, s94\n"
    "v_mfma_f32_16x16x32_bf16 a[124:127], v[172:175], v[76:79], a[124:127]\n"
    "v_fma_f32 v103, v103, v224, s94\n"
    "v_cvt_pk_bf16_f32 v188, v100, v101\n"
    "v_mfma_f32_16x16x32_bf16 a[120:123], v[168:171], v[40:43], a[120:123]\n"
    "v_cvt_pk_bf16_f32 v189, v102, v103\n"
    "v_accvgpr_read_b32 v104, a104\n"
    "v_mfma_f32_16x16x32_bf16 a[120:123], v[172:175], v[72:75], a[120:123]\n"
    "v_accvgpr_read_b32 v105, a105\n"
    "v_accvgpr_read_b32 v106, a106\n"
    "v_mfma_f32_16x16x32_bf16 a[116:119], v[168:171], v[36:39], a[116:119]\n"
    "ds_read_b128 v[164:167], v201 offset:17408\n"
    "v_accvgpr_read_b32 v107, a107\n"
    "v_mfma_f32_16x16x32_bf16 a[116:119], v[172:175], v[68:71], a[116:119]\n"
    "v_fma_f32 v104, v104, v226, s94\n"
    "v_fma_f32 v105, v105, v226, s94\n"
    "v_mfma_f32_16x16x32_bf16 a[112:115], v[168:171], v[32:35], a[112:115]\n"
    "v_fma_f32 v106, v106, v226, s94\n"
    "v_fma_f32 v107, v107, v226, s94\n"
    "v_mfma_f32_16x16x32_bf16 a[112:115], v[172:175], v[64:67], a[112:115]\n"
    "s_waitcnt lgkmcnt(2)\n"
    "v_cvt_pk_bf16_f32 v248, v104, v105\n"
    "v_cvt_pk_bf16_f32 v249, v106, v107\n"
    "v_mfma_f32_16x16x32_bf16 a[112:115], v[176:179], v[232:235], a[112:115]\n"
    "ds_read_b128 v[168:171], v201 offset:40960\n"
    "v_accvgpr_read_b32 v108, a108\n"
    "v_mfma_f32_16x16x32_bf16 a[112:115], v[180:183], v[80:83], a[112:115]\n"
    "v_accvgpr_read_b32 v109, a109\n"
    "v_accvgpr_read_b32 v110, a110\n"
    "v_mfma_f32_16x16x32_bf16 a[116:119], v[176:179], v[236:239], a[116:119]\n"
    "v_accvgpr_read_b32 v111, a111\n"
    "v_fma_f32 v108, v108, v228, s94\n"
    "v_mfma_f32_16x16x32_bf16 a[116:119], v[180:183], v[84:87], a[116:119]\n"
    "v_fma_f32 v109, v109, v228, s94\n"
    "v_fma_f32 v110, v110, v228, s94\n"
    "v_mfma_f32_16x16x32_bf16 a[120:123], v[176:179], v[240:243], a[120:123]\n"
    "ds_read_b128 v[172:175], v201 offset:41984\n"
    "v_fma_f32 v111, v111, v228, s94\n"
    "v_mfma_f32_16x16x32_bf16 a[120:123], v[180:183], v[88:91], a[120:123]\n"
    "v_cvt_pk_bf16_f32 v252, v108, v109\n"
    "v_cvt_pk_bf16_f32 v253, v110, v111\n"
    "v_mfma_f32_16x16x32_bf16 a[124:127], v[176:179], v[244:247], a[124:127]\n"
    "v_mfma_f32_16x16x32_bf16 a[124:127], v[180:183], v[92:95], a[124:127]\n"
    "s_waitcnt lgkmcnt(2)\n"
    "v_accvgpr_read_b32 v96, a112\n"
    "v_accvgpr_read_b32 v97, a113\n"
    "v_mfma_f32_16x16x32_bf16 a[128:131], v[160:163], v[128:131], a[128:131]\n"
    "ds_read_b128 v[176:179], v207 offset:16384\n"
    "v_accvgpr_read_b32 v98, a114\n"
    "v_mfma_f32_16x16x32_bf16 a[128:131], v[164:167], v[144:147], a[128:131]\n"
    "v_accvgpr_read_b32 v99, a115\n"
    "v_fma_f32 v96, v96, v222, s94\n"
    "v_mfma_f32_16x16x32_bf16 a[132:135], v[160:163], v[132:135], a[132:135]\n"
    "v_fma_f32 v97, v97, v222, s94\n"
    "v_fma_f32 v98, v98, v222, s94\n"
    "v_mfma_f32_16x16x32_bf16 a[132:135], v[164:167], v[148:151], a[132:135]\n"
    "v_fma_f32 v99, v99, v222, s94\n"
    "v_cvt_pk_bf16_f32 v186, v96, v97\n"
    "v_mfma_f32_16x16x32_bf16 a[136:139], v[160:163], v[136:139], a[136:139]\n"
    "ds_read_b128 v[180:183], v207 offset:17408\n"
    "v_cvt_pk_bf16_f32 v187, v98, v99\n"
    "v_mfma_f32_16x16x32_bf16 a[136:139], v[164:167], v[152:155], a[136:139]\n"
    "buffer_store_dwordx4 v[184:187], v205, s[28:31], 0 offen offset:192 sc0 sc1\n"
    "v_accvgpr_read_b32 v100, a116\n"
    "v_accvgpr_read_b32 v101, a117\n"
    "v_accvgpr_read_b32 v102, a118\n"
    "v_mfma_f32_16x16x32_bf16 a[140:143], v[160:163], v[140:143], a[140:143]\n"
    "v_accvgpr_read_b32 v103, a119\n"
    "v_fma_f32 v100, v100, v224, s94\n"
    "v_mfma_f32_16x16x32_bf16 a[140:143], v[164:167], v[156:159], a[140:143]\n"
    "s_waitcnt lgkmcnt(2)\n"
    "v_fma_f32 v101, v101, v224, s94\n"
    "v_fma_f32 v102, v102, v224, s94\n"
    "v_mfma_f32_16x16x32_bf16 a[140:143], v[168:171], v[44:47], a[140:143]\n"
    "ds_read_b128 v[160:163], v201 offset:18432\n"
    "v_fma_f32 v103, v103, v224, s94\n"
    "v_mfma_f32_16x16x32_bf16 a[140:143], v[172:175], v[76:79], a[140:143]\n"
    "v_cvt_pk_bf16_f32 v190, v100, v101\n"
    "v_cvt_pk_bf16_f32 v191, v102, v103\n"
    "v_mfma_f32_16x16x32_bf16 a[136:139], v[168:171], v[40:43], a[136:139]\n"
    "buffer_store_dwordx4 v[188:191], v205, s[28:31], s62 offen offset:192 sc0 sc1\n"
    "v_accvgpr_read_b32 v104, a120\n"
    "v_accvgpr_read_b32 v105, a121\n"
    "v_accvgpr_read_b32 v106, a122\n"
    "v_mfma_f32_16x16x32_bf16 a[136:139], v[172:175], v[72:75], a[136:139]\n"
    "v_accvgpr_read_b32 v107, a123\n"
    "v_fma_f32 v104, v104, v226, s94\n"
    "v_mfma_f32_16x16x32_bf16 a[132:135], v[168:171], v[36:39], a[132:135]\n"
    "ds_read_b128 v[164:167], v201 offset:19456\n"
    "v_fma_f32 v105, v105, v226, s94\n"
    "v_mfma_f32_16x16x32_bf16 a[132:135], v[172:175], v[68:71], a[132:135]\n"
    "v_fma_f32 v106, v106, v226, s94\n"
    "v_fma_f32 v107, v107, v226, s94\n"
    "v_mfma_f32_16x16x32_bf16 a[128:131], v[168:171], v[32:35], a[128:131]\n"
    "v_cvt_pk_bf16_f32 v250, v104, v105\n"
    "v_cvt_pk_bf16_f32 v251, v106, v107\n"
    "v_mfma_f32_16x16x32_bf16 a[128:131], v[172:175], v[64:67], a[128:131]\n"
    "s_waitcnt lgkmcnt(2)\n"
    "buffer_store_dwordx4 v[248:251], v205, s[28:31], s63 offen offset:192 sc0 sc1\n"
    "v_accvgpr_read_b32 v108, a124\n"
    "v_accvgpr_read_b32 v109, a125\n"
    "v_accvgpr_read_b32 v110, a126\n"
    "v_mfma_f32_16x16x32_bf16 a[128:131], v[176:179], v[232:235], a[128:131]\n"
    "ds_read_b128 v[168:171], v201 offset:43008\n"
    "v_accvgpr_read_b32 v111, a127\n"
    "v_mfma_f32_16x16x32_bf16 a[128:131], v[180:183], v[80:83], a[128:131]\n"
    "v_fma_f32 v108, v108, v228, s94\n"
    "v_fma_f32 v109, v109, v228, s94\n"
    "v_mfma_f32_16x16x32_bf16 a[132:135], v[176:179], v[236:239], a[132:135]\n"
    "v_fma_f32 v110, v110, v228, s94\n"
    "v_fma_f32 v111, v111, v228, s94\n"
    "v_mfma_f32_16x16x32_bf16 a[132:135], v[180:183], v[84:87], a[132:135]\n"
    "v_cvt_pk_bf16_f32 v254, v108, v109\n"
    "v_cvt_pk_bf16_f32 v255, v110, v111\n"
    "v_mfma_f32_16x16x32_bf16 a[136:139], v[176:179], v[240:243], a[136:139]\n"
    "ds_read_b128 v[172:175], v201 offset:44032\n"
    "v_mfma_f32_16x16x32_bf16 a[136:139], v[180:183], v[88:91], a[136:139]\n"
    "buffer_store_dwordx4 v[252:255], v205, s[28:31], s64 offen offset:192 sc0 sc1\n"
    "v_mfma_f32_16x16x32_bf16 a[140:143], v[176:179], v[244:247], a[140:143]\n"
    "v_mfma_f32_16x16x32_bf16 a[140:143], v[180:183], v[92:95], a[140:143]\n"
    "s_waitcnt lgkmcnt(2)\n"
    "v_accvgpr_read_b32 v96, a128\n"
    "v_accvgpr_read_b32 v97, a129\n"
    "v_mfma_f32_16x16x32_bf16 a[144:147], v[160:163], v[128:131], a[144:147]\n"
    "ds_read_b128 v[176:179], v207 offset:18432\n"
    "v_accvgpr_read_b32 v98, a130\n"
    "v_mfma_f32_16x16x32_bf16 a[144:147], v[164:167], v[144:147], a[144:147]\n"
    "v_accvgpr_read_b32 v99, a131\n"
    "v_fma_f32 v96, v96, v222, s94\n"
    "v_mfma_f32_16x16x32_bf16 a[148:151], v[160:163], v[132:135], a[148:151]\n"
    "v_fma_f32 v97, v97, v222, s94\n"
    "v_fma_f32 v98, v98, v222, s94\n"
    "v_mfma_f32_16x16x32_bf16 a[148:151], v[164:167], v[148:151], a[148:151]\n"
    "v_fma_f32 v99, v99, v222, s94\n"
    "v_cvt_pk_bf16_f32 v48, v96, v97\n"
    "v_mfma_f32_16x16x32_bf16 a[152:155], v[160:163], v[136:139], a[152:155]\n"
    "ds_read_b128 v[180:183], v207 offset:19456\n"
    "v_cvt_pk_bf16_f32 v49, v98, v99\n"
    "v_mfma_f32_16x16x32_bf16 a[152:155], v[164:167], v[152:155], a[152:155]\n"
    "v_accvgpr_read_b32 v100, a132\n"
    "v_accvgpr_read_b32 v101, a133\n"
    "v_mfma_f32_16x16x32_bf16 a[156:159], v[160:163], v[140:143], a[156:159]\n"
    "v_accvgpr_read_b32 v102, a134\n"
    "v_accvgpr_read_b32 v103, a135\n"
    "v_mfma_f32_16x16x32_bf16 a[156:159], v[164:167], v[156:159], a[156:159]\n"
    "s_waitcnt lgkmcnt(2)\n"
    "v_fma_f32 v100, v100, v224, s94\n"
    "v_fma_f32 v101, v101, v224, s94\n"
    "v_mfma_f32_16x16x32_bf16 a[156:159], v[168:171], v[44:47], a[156:159]\n"
    "ds_read_b128 v[160:163], v201 offset:20480\n"
    "v_fma_f32 v102, v102, v224, s94\n"
    "v_mfma_f32_16x16x32_bf16 a[156:159], v[172:175], v[76:79], a[156:159]\n"
    "v_fma_f32 v103, v103, v224, s94\n"
    "v_cvt_pk_bf16_f32 v52, v100, v101\n"
    "v_mfma_f32_16x16x32_bf16 a[152:155], v[168:171], v[40:43], a[152:155]\n"
    "v_cvt_pk_bf16_f32 v53, v102, v103\n"
    "v_accvgpr_read_b32 v104, a136\n"
    "v_mfma_f32_16x16x32_bf16 a[152:155], v[172:175], v[72:75], a[152:155]\n"
    "v_accvgpr_read_b32 v105, a137\n"
    "v_accvgpr_read_b32 v106, a138\n"
    "v_mfma_f32_16x16x32_bf16 a[148:151], v[168:171], v[36:39], a[148:151]\n"
    "ds_read_b128 v[164:167], v201 offset:21504\n"
    "v_accvgpr_read_b32 v107, a139\n"
    "v_mfma_f32_16x16x32_bf16 a[148:151], v[172:175], v[68:71], a[148:151]\n"
    "v_fma_f32 v104, v104, v226, s94\n"
    "v_fma_f32 v105, v105, v226, s94\n"
    "v_mfma_f32_16x16x32_bf16 a[144:147], v[168:171], v[32:35], a[144:147]\n"
    "v_fma_f32 v106, v106, v226, s94\n"
    "v_fma_f32 v107, v107, v226, s94\n"
    "v_mfma_f32_16x16x32_bf16 a[144:147], v[172:175], v[64:67], a[144:147]\n"
    "s_waitcnt lgkmcnt(2)\n"
    "v_cvt_pk_bf16_f32 v56, v104, v105\n"
    "v_cvt_pk_bf16_f32 v57, v106, v107\n"
    "v_mfma_f32_16x16x32_bf16 a[144:147], v[176:179], v[232:235], a[144:147]\n"
    "ds_read_b128 v[168:171], v201 offset:45056\n"
    "v_accvgpr_read_b32 v108, a140\n"
    "v_mfma_f32_16x16x32_bf16 a[144:147], v[180:183], v[80:83], a[144:147]\n"
    "v_accvgpr_read_b32 v109, a141\n"
    "v_accvgpr_read_b32 v110, a142\n"
    "v_mfma_f32_16x16x32_bf16 a[148:151], v[176:179], v[236:239], a[148:151]\n"
    "v_accvgpr_read_b32 v111, a143\n"
    "v_fma_f32 v108, v108, v228, s94\n"
    "v_mfma_f32_16x16x32_bf16 a[148:151], v[180:183], v[84:87], a[148:151]\n"
    "v_fma_f32 v109, v109, v228, s94\n"
    "v_fma_f32 v110, v110, v228, s94\n"
    "v_mfma_f32_16x16x32_bf16 a[152:155], v[176:179], v[240:243], a[152:155]\n"
    "ds_read_b128 v[172:175], v201 offset:46080\n"
    "v_fma_f32 v111, v111, v228, s94\n"
    "v_mfma_f32_16x16x32_bf16 a[152:155], v[180:183], v[88:91], a[152:155]\n"
    "v_cvt_pk_bf16_f32 v60, v108, v109\n"
    "v_cvt_pk_bf16_f32 v61, v110, v111\n"
    "v_mfma_f32_16x16x32_bf16 a[156:159], v[176:179], v[244:247], a[156:159]\n"
    "v_mfma_f32_16x16x32_bf16 a[156:159], v[180:183], v[92:95], a[156:159]\n"
    "s_waitcnt lgkmcnt(2)\n"
    "v_accvgpr_read_b32 v96, a144\n"
    "v_accvgpr_read_b32 v97, a145\n"
    "v_mfma_f32_16x16x32_bf16 a[160:163], v[160:163], v[128:131], a[160:163]\n"
    "ds_read_b128 v[176:179], v207 offset:20480\n"
    "v_accvgpr_read_b32 v98, a146\n"
    "v_mfma_f32_16x16x32_bf16 a[160:163], v[164:167], v[144:147], a[160:163]\n"
    "v_accvgpr_read_b32 v99, a147\n"
    "v_fma_f32 v96, v96, v222, s94\n"
    "v_mfma_f32_16x16x32_bf16 a[164:167], v[160:163], v[132:135], a[164:167]\n"
    "v_fma_f32 v97, v97, v222, s94\n"
    "v_fma_f32 v98, v98, v222, s94\n"
    "v_mfma_f32_16x16x32_bf16 a[164:167], v[164:167], v[148:151], a[164:167]\n"
    "v_fma_f32 v99, v99, v222, s94\n"
    "v_cvt_pk_bf16_f32 v50, v96, v97\n"
    "v_mfma_f32_16x16x32_bf16 a[168:171], v[160:163], v[136:139], a[168:171]\n"
    "ds_read_b128 v[180:183], v207 offset:21504\n"
    "v_cvt_pk_bf16_f32 v51, v98, v99\n"
    "v_mfma_f32_16x16x32_bf16 a[168:171], v[164:167], v[152:155], a[168:171]\n"
    "buffer_store_dwordx4 v[48:51], v205, s[28:31], 0 offen offset:256 sc0 sc1\n"
    "v_accvgpr_read_b32 v100, a148\n"
    "v_accvgpr_read_b32 v101, a149\n"
    "v_accvgpr_read_b32 v102, a150\n"
    "v_mfma_f32_16x16x32_bf16 a[172:175], v[160:163], v[140:143], a[172:175]\n"
    "v_accvgpr_read_b32 v103, a151\n"
    "v_fma_f32 v100, v100, v224, s94\n"
    "v_mfma_f32_16x16x32_bf16 a[172:175], v[164:167], v[156:159], a[172:175]\n"
    "s_waitcnt lgkmcnt(2)\n"
    "v_fma_f32 v101, v101, v224, s94\n"
    "v_fma_f32 v102, v102, v224, s94\n"
    "v_mfma_f32_16x16x32_bf16 a[172:175], v[168:171], v[44:47], a[172:175]\n"
    "ds_read_b128 v[160:163], v201 offset:22528\n"
    "v_fma_f32 v103, v103, v224, s94\n"
    "v_mfma_f32_16x16x32_bf16 a[172:175], v[172:175], v[76:79], a[172:175]\n"
    "v_cvt_pk_bf16_f32 v54, v100, v101\n"
    "v_cvt_pk_bf16_f32 v55, v102, v103\n"
    "v_mfma_f32_16x16x32_bf16 a[168:171], v[168:171], v[40:43], a[168:171]\n"
    "buffer_store_dwordx4 v[52:55], v205, s[28:31], s62 offen offset:256 sc0 sc1\n"
    "v_accvgpr_read_b32 v104, a152\n"
    "v_accvgpr_read_b32 v105, a153\n"
    "v_accvgpr_read_b32 v106, a154\n"
    "v_mfma_f32_16x16x32_bf16 a[168:171], v[172:175], v[72:75], a[168:171]\n"
    "v_accvgpr_read_b32 v107, a155\n"
    "v_fma_f32 v104, v104, v226, s94\n"
    "v_mfma_f32_16x16x32_bf16 a[164:167], v[168:171], v[36:39], a[164:167]\n"
    "ds_read_b128 v[164:167], v201 offset:23552\n"
    "v_fma_f32 v105, v105, v226, s94\n"
    "v_mfma_f32_16x16x32_bf16 a[164:167], v[172:175], v[68:71], a[164:167]\n"
    "v_fma_f32 v106, v106, v226, s94\n"
    "v_fma_f32 v107, v107, v226, s94\n"
    "v_mfma_f32_16x16x32_bf16 a[160:163], v[168:171], v[32:35], a[160:163]\n"
    "v_cvt_pk_bf16_f32 v58, v104, v105\n"
    "v_cvt_pk_bf16_f32 v59, v106, v107\n"
    "v_mfma_f32_16x16x32_bf16 a[160:163], v[172:175], v[64:67], a[160:163]\n"
    "s_waitcnt lgkmcnt(2)\n"
    "buffer_store_dwordx4 v[56:59], v205, s[28:31], s63 offen offset:256 sc0 sc1\n"
    "v_accvgpr_read_b32 v108, a156\n"
    "v_accvgpr_read_b32 v109, a157\n"
    "v_accvgpr_read_b32 v110, a158\n"
    "v_mfma_f32_16x16x32_bf16 a[160:163], v[176:179], v[232:235], a[160:163]\n"
    "ds_read_b128 v[168:171], v201 offset:47104\n"
    "v_accvgpr_read_b32 v111, a159\n"
    "v_mfma_f32_16x16x32_bf16 a[160:163], v[180:183], v[80:83], a[160:163]\n"
    "v_fma_f32 v108, v108, v228, s94\n"
    "v_fma_f32 v109, v109, v228, s94\n"
    "v_mfma_f32_16x16x32_bf16 a[164:167], v[176:179], v[236:239], a[164:167]\n"
    "v_fma_f32 v110, v110, v228, s94\n"
    "v_fma_f32 v111, v111, v228, s94\n"
    "v_mfma_f32_16x16x32_bf16 a[164:167], v[180:183], v[84:87], a[164:167]\n"
    "v_cvt_pk_bf16_f32 v62, v108, v109\n"
    "v_cvt_pk_bf16_f32 v63, v110, v111\n"
    "v_mfma_f32_16x16x32_bf16 a[168:171], v[176:179], v[240:243], a[168:171]\n"
    "ds_read_b128 v[172:175], v201 offset:48128\n"
    "v_mfma_f32_16x16x32_bf16 a[168:171], v[180:183], v[88:91], a[168:171]\n"
    "buffer_store_dwordx4 v[60:63], v205, s[28:31], s64 offen offset:256 sc0 sc1\n"
    "v_mfma_f32_16x16x32_bf16 a[172:175], v[176:179], v[244:247], a[172:175]\n"
    "v_mfma_f32_16x16x32_bf16 a[172:175], v[180:183], v[92:95], a[172:175]\n"
    "s_waitcnt lgkmcnt(2)\n"
    "v_accvgpr_read_b32 v96, a160\n"
    "v_accvgpr_read_b32 v97, a161\n"
    "v_mfma_f32_16x16x32_bf16 a[176:179], v[160:163], v[128:131], a[176:179]\n"
    "ds_read_b128 v[176:179], v207 offset:22528\n"
    "v_accvgpr_read_b32 v98, a162\n"
    "v_mfma_f32_16x16x32_bf16 a[176:179], v[164:167], v[144:147], a[176:179]\n"
    "v_accvgpr_read_b32 v99, a163\n"
    "v_fma_f32 v96, v96, v222, s94\n"
    "v_mfma_f32_16x16x32_bf16 a[180:183], v[160:163], v[132:135], a[180:183]\n"
    "v_fma_f32 v97, v97, v222, s94\n"
    "v_fma_f32 v98, v98, v222, s94\n"
    "v_mfma_f32_16x16x32_bf16 a[180:183], v[164:167], v[148:151], a[180:183]\n"
    "v_fma_f32 v99, v99, v222, s94\n"
    "v_cvt_pk_bf16_f32 v184, v96, v97\n"
    "v_mfma_f32_16x16x32_bf16 a[184:187], v[160:163], v[136:139], a[184:187]\n"
    "ds_read_b128 v[180:183], v207 offset:23552\n"
    "v_cvt_pk_bf16_f32 v185, v98, v99\n"
    "v_mfma_f32_16x16x32_bf16 a[184:187], v[164:167], v[152:155], a[184:187]\n"
    "v_accvgpr_read_b32 v100, a164\n"
    "v_accvgpr_read_b32 v101, a165\n"
    "v_mfma_f32_16x16x32_bf16 a[188:191], v[160:163], v[140:143], a[188:191]\n"
    "v_accvgpr_read_b32 v102, a166\n"
    "v_accvgpr_read_b32 v103, a167\n"
    "v_mfma_f32_16x16x32_bf16 a[188:191], v[164:167], v[156:159], a[188:191]\n"
    "s_waitcnt lgkmcnt(2)\n"
    "v_fma_f32 v100, v100, v224, s94\n"
    "v_fma_f32 v101, v101, v224, s94\n"
    "v_mfma_f32_16x16x32_bf16 a[188:191], v[168:171], v[44:47], a[188:191]\n"
    "v_fma_f32 v102, v102, v224, s94\n"
    "v_fma_f32 v103, v103, v224, s94\n"
    "v_mfma_f32_16x16x32_bf16 a[188:191], v[172:175], v[76:79], a[188:191]\n"
    "v_cvt_pk_bf16_f32 v188, v100, v101\n"
    "v_cvt_pk_bf16_f32 v189, v102, v103\n"
    "v_mfma_f32_16x16x32_bf16 a[184:187], v[168:171], v[40:43], a[184:187]\n"
    "v_accvgpr_read_b32 v104, a168\n"
    "v_accvgpr_read_b32 v105, a169\n"
    "v_mfma_f32_16x16x32_bf16 a[184:187], v[172:175], v[72:75], a[184:187]\n"
    "v_accvgpr_read_b32 v106, a170\n"
    "v_accvgpr_read_b32 v107, a171\n"
    "v_mfma_f32_16x16x32_bf16 a[180:183], v[168:171], v[36:39], a[180:183]\n"
    "v_fma_f32 v104, v104, v226, s94\n"
    "v_fma_f32 v105, v105, v226, s94\n"
    "v_mfma_f32_16x16x32_bf16 a[180:183], v[172:175], v[68:71], a[180:183]\n"
    "v_fma_f32 v106, v106, v226, s94\n"
    "v_fma_f32 v107, v107, v226, s94\n"
    "v_mfma_f32_16x16x32_bf16 a[176:179], v[168:171], v[32:35], a[176:179]\n"
    "v_cvt_pk_bf16_f32 v248, v104, v105\n"
    "v_cvt_pk_bf16_f32 v249, v106, v107\n"
    "v_mfma_f32_16x16x32_bf16 a[176:179], v[172:175], v[64:67], a[176:179]\n"
    "s_waitcnt lgkmcnt(0)\n"
    "v_accvgpr_read_b32 v108, a172\n"
    "v_accvgpr_read_b32 v109, a173\n"
    "v_mfma_f32_16x16x32_bf16 a[176:179], v[176:179], v[232:235], a[176:179]\n"
    "v_accvgpr_read_b32 v110, a174\n"
    "v_accvgpr_read_b32 v111, a175\n"
    "v_mfma_f32_16x16x32_bf16 a[176:179], v[180:183], v[80:83], a[176:179]\n"
    "v_fma_f32 v108, v108, v228, s94\n"
    "v_fma_f32 v109, v109, v228, s94\n"
    "v_mfma_f32_16x16x32_bf16 a[180:183], v[176:179], v[236:239], a[180:183]\n"
    "v_fma_f32 v110, v110, v228, s94\n"
    "v_fma_f32 v111, v111, v228, s94\n"
    "v_mfma_f32_16x16x32_bf16 a[180:183], v[180:183], v[84:87], a[180:183]\n"
    "v_cvt_pk_bf16_f32 v252, v108, v109\n"
    "v_cvt_pk_bf16_f32 v253, v110, v111\n"
    "v_mfma_f32_16x16x32_bf16 a[184:187], v[176:179], v[240:243], a[184:187]\n"
    "v_mfma_f32_16x16x32_bf16 a[184:187], v[180:183], v[88:91], a[184:187]\n"
    "v_mfma_f32_16x16x32_bf16 a[188:191], v[176:179], v[244:247], a[188:191]\n"
    "v_mfma_f32_16x16x32_bf16 a[188:191], v[180:183], v[92:95], a[188:191]\n"
    "v_accvgpr_read_b32 v96, a176\n"
    "v_accvgpr_read_b32 v97, a177\n"
    "v_accvgpr_read_b32 v98, a178\n"
    "v_accvgpr_read_b32 v99, a179\n"
    "v_fma_f32 v96, v96, v222, s94\n"
    "v_fma_f32 v97, v97, v222, s94\n"
    "v_fma_f32 v98, v98, v222, s94\n"
    "v_fma_f32 v99, v99, v222, s94\n"
    "v_cvt_pk_bf16_f32 v186, v96, v97\n"
    "v_cvt_pk_bf16_f32 v187, v98, v99\n"
    "buffer_store_dwordx4 v[184:187], v205, s[28:31], 0 offen offset:320 sc0 sc1\n"
    "v_accvgpr_read_b32 v100, a180\n"
    "v_accvgpr_read_b32 v101, a181\n"
    "v_accvgpr_read_b32 v102, a182\n"
    "v_accvgpr_read_b32 v103, a183\n"
    "v_fma_f32 v100, v100, v224, s94\n"
    "v_fma_f32 v101, v101, v224, s94\n"
    "v_fma_f32 v102, v102, v224, s94\n"
    "v_fma_f32 v103, v103, v224, s94\n"
    "v_cvt_pk_bf16_f32 v190, v100, v101\n"
    "v_cvt_pk_bf16_f32 v191, v102, v103\n"
    "buffer_store_dwordx4 v[188:191], v205, s[28:31], s62 offen offset:320 sc0 sc1\n"
    "v_accvgpr_read_b32 v104, a184\n"
    "v_accvgpr_read_b32 v105, a185\n"
    "v_accvgpr_read_b32 v106, a186\n"
    "v_accvgpr_read_b32 v107, a187\n"
    "v_fma_f32 v104, v104, v226, s94\n"
    "v_fma_f32 v105, v105, v226, s94\n"
    "v_fma_f32 v106, v106, v226, s94\n"
    "v_fma_f32 v107, v107, v226, s94\n"
    "v_cvt_pk_bf16_f32 v250, v104, v105\n"
    "v_cvt_pk_bf16_f32 v251, v106, v107\n"
    "buffer_store_dwordx4 v[248:251], v205, s[28:31], s63 offen offset:320 sc0 sc1\n"
    "v_accvgpr_read_b32 v108, a188\n"
    "v_accvgpr_read_b32 v109, a189\n"
    "v_accvgpr_read_b32 v110, a190\n"
    "v_accvgpr_read_b32 v111, a191\n"
    "v_fma_f32 v108, v108, v228, s94\n"
    "v_fma_f32 v109, v109, v228, s94\n"
    "v_fma_f32 v110, v110, v228, s94\n"
    "v_fma_f32 v111, v111, v228, s94\n"
    "v_cvt_pk_bf16_f32 v254, v108, v109\n"
    "v_cvt_pk_bf16_f32 v255, v110, v111\n"
    "buffer_store_dwordx4 v[252:255], v205, s[28:31], s64 offen offset:320 sc0 sc1\n"
    "v_add_f32 v196, v112, v116\n"
    "v_add_f32 v197, v120, v124\n"
    "v_add_f32 v196, v196, v197\n"
    "v_cmp_ngt_f32 vcc, 0x45800000, v196\n"
    "s_barrier\n"
    "s_cbranch_vccnz LSLOW\n"
    "LFIN:\n"
    "s_branch LEND\n"
    "LSPRE0:\n"
    "LSPIN0:\n"
    "s_sleep 1\n"
    "buffer_load_dwordx2 v[218:219], v213, s[44:47], 0 offen sc1\n"
    "s_waitcnt vmcnt(0)\n"
    "s_add_u32 s73, s73, 1\n"
    "s_cmp_gt_u32 s73, 200000\n"
    "s_cbranch_scc1 LSPB0\n"
    "v_cmp_ne_u32 s[80:81], s75, v219\n"
    "v_cmp_gt_u32 vcc, s66, v218\n"
    "s_or_b64 vcc, vcc, s[80:81]\n"
    "s_cbranch_vccnz LSPIN0\n"
    "s_branch LSPB0\n"
    "LSPRE1:\n"
    "LSPIN1:\n"
    "s_sleep 1\n"
    "buffer_load_dwordx2 v[218:219], v213, s[44:47], 0 offen sc1\n"
    "s_waitcnt vmcnt(0)\n"
    "s_add_u32 s73, s73, 1\n"
    "s_cmp_gt_u32 s73, 200000\n"
    "s_cbranch_scc1 LSPB1\n"
    "v_cmp_ne_u32 s[80:81], s75, v219\n"
    "v_cmp_gt_u32 vcc, s66, v218\n"
    "s_or_b64 vcc, vcc, s[80:81]\n"
    "s_cbranch_vccnz LSPIN1\n"
    "s_branch LSPB1\n"
    "LSPRE2:\n"
    "LSPIN2:\n"
    "s_sleep 1\n"
    "buffer_load_dwordx2 v[218:219], v213, s[44:47], 0 offen sc1\n"
    "s_waitcnt vmcnt(0)\n"
    "s_add_u32 s73, s73, 1\n"
    "s_cmp_gt_u32 s73, 200000\n"
    "s_cbranch_scc1 LSPB2\n"
    "v_cmp_ne_u32 s[80:81], s75, v219\n"
    "v_cmp_gt_u32 vcc, s66, v218\n"
    "s_or_b64 vcc, vcc, s[80:81]\n"
    "s_cbranch_vccnz LSPIN2\n"
    "s_branch LSPB2\n"
    "LSPRE3:\n"
    "LSPIN3:\n"
    "s_sleep 1\n"
    "buffer_load_dwordx2 v[218:219], v213, s[44:47], 0 offen sc1\n"
    "s_waitcnt vmcnt(0)\n"
    "s_add_u32 s73, s73, 1\n"
    "s_cmp_gt_u32 s73, 200000\n"
    "s_cbranch_scc1 LSPB3\n"
    "v_cmp_ne_u32 s[80:81], s75, v219\n"
    "v_cmp_gt_u32 vcc, s66, v218\n"
    "s_or_b64 vcc, vcc, s[80:81]\n"
    "s_cbranch_vccnz LSPIN3\n"
    "s_branch LSPB3\n"
    "LSPRE4:\n"
    "LSPIN4:\n"
    "s_sleep 1\n"
    "buffer_load_dwordx2 v[218:219], v213, s[44:47], 0 offen sc1\n"
    "s_waitcnt vmcnt(0)\n"
    "s_add_u32 s73, s73, 1\n"
    "s_cmp_gt_u32 s73, 200000\n"
    "s_cbranch_scc1 LSPB4\n"
    "v_cmp_ne_u32 s[80:81], s75, v219\n"
    "v_cmp_gt_u32 vcc, s66, v218\n"
    "s_or_b64 vcc, vcc, s[80:81]\n"
    "s_cbranch_vccnz LSPIN4\n"
    "s_branch LSPB4\n"
    "LSPRE5:\n"
    "LSPIN5:\n"
    "s_sleep 1\n"
    "buffer_load_dwordx2 v[218:219], v213, s[44:47], 0 offen sc1\n"
    "s_waitcnt vmcnt(0)\n"
    "s_add_u32 s73, s73, 1\n"
    "s_cmp_gt_u32 s73, 200000\n"
    "s_cbranch_scc1 LSPB5\n"
    "v_cmp_ne_u32 s[80:81], s75, v219\n"
    "v_cmp_gt_u32 vcc, s66, v218\n"
    "s_or_b64 vcc, vcc, s[80:81]\n"
    "s_cbranch_vccnz LSPIN5\n"
    "s_branch LSPB5\n"
    "LSLOW:\n"
    "s_waitcnt vmcnt(0)\n"
    "s_nop 15\n"
    "s_mul_i32 s62, s58, 0x9000\n"
    "v_add_u32 v205, s62, v200\n"
    "s_mov_b32 s63, 0\n"
    "v_mov_b32 v128, 0\n"
    "v_mov_b32 v129, 0\n"
    "v_mov_b32 v130, 0\n"
    "v_mov_b32 v131, 0\n"
    "v_mov_b32 v96, 0xff61b1e6\n"
    "v_mov_b32 v100, 0xff61b1e6\n"
    "v_mov_b32 v104, 0xff61b1e6\n"
    "v_mov_b32 v108, 0xff61b1e6\n"
    "v_mfma_f32_16x16x32_bf16 a[0:3], v[128:131], v[128:131], 0\n"
    "v_mfma_f32_16x16x32_bf16 a[4:7], v[128:131], v[128:131], 0\n"
    "v_mfma_f32_16x16x32_bf16 a[8:11], v[128:131], v[128:131], 0\n"
    "v_mfma_f32_16x16x32_bf16 a[12:15], v[128:131], v[128:131], 0\n"
    "v_mfma_f32_16x16x32_bf16 a[16:19], v[128:131], v[128:131], 0\n"
    "v_mfma_f32_16x16x32_bf16 a[20:23], v[128:131], v[128:131], 0\n"
    "v_mfma_f32_16x16x32_bf16 a[24:27], v[128:131], v[128:131], 0\n"
    "v_mfma_f32_16x16x32_bf16 a[28:31], v[128:131], v[128:131], 0\n"
    "v_mfma_f32_16x16x32_bf16 a[32:35], v[128:131], v[128:131], 0\n"
    "v_mfma_f32_16x16x32_bf16 a[36:39], v[128:131], v[128:131], 0\n"
    "v_mfma_f32_16x16x32_bf16 a[40:43], v[128:131], v[128:131], 0\n"
    "v_mfma_f32_16x16x32_bf16 a[44:47], v[128:131], v[128:131], 0\n"
    "v_mfma_f32_16x16x32_bf16 a[48:51], v[128:131], v[128:131], 0\n"
    "v_mfma_f32_16x16x32_bf16 a[52:55], v[128:131], v[128:131], 0\n"
    "v_mfma_f32_16x16x32_bf16 a[56:59], v[128:131], v[128:131], 0\n"
    "v_mfma_f32_16x16x32_bf16 a[60:63], v[128:131], v[128:131], 0\n"
    "v_mfma_f32_16x16x32_bf16 a[64:67], v[128:131], v[128:131], 0\n"
    "v_mfma_f32_16x16x32_bf16 a[68:71], v[128:131], v[128:131], 0\n"
    "v_mfma_f32_16x16x32_bf16 a[72:75], v[128:131], v[128:131], 0\n"
    "v_mfma_f32_16x16x32_bf16 a[76:79], v[128:131], v[128:131], 0\n"
    "v_mfma_f32_16x16x32_bf16 a[80:83], v[128:131], v[128:131], 0\n"
    "v_mfma_f32_16x16x32_bf16 a[84:87], v[128:131], v[128:131], 0\n"
    "v_mfma_f32_16x16x32_bf16 a[88:91], v[128:131], v[128:131], 0\n"
    "v_mfma_f32_16x16x32_bf16 a[92:95], v[128:131], v[128:131], 0\n"
    "v_mfma_f32_16x16x32_bf16 a[96:99], v[128:131], v[128:131], 0\n"
    "v_mfma_f32_16x16x32_bf16 a[100:103], v[128:131], v[128:131], 0\n"
    "v_mfma_f32_16x16x32_bf16 a[104:107], v[128:131], v[128:131], 0\n"
    "v_mfma_f32_16x16x32_bf16 a[108:111], v[128:131], v[128:131], 0\n"
    "v_mfma_f32_16x16x32_bf16 a[112:115], v[128:131], v[128:131], 0\n"
    "v_mfma_f32_16x16x32_bf16 a[116:119], v[128:131], v[128:131], 0\n"
    "v_mfma_f32_16x16x32_bf16 a[120:123], v[128:131], v[128:131], 0\n"
    "v_mfma_f32_16x16x32_bf16 a[124:127], v[128:131], v[128:131], 0\n"
    "v_mfma_f32_16x16x32_bf16 a[128:131], v[128:131], v[128:131], 0\n"
    "v_mfma_f32_16x16x32_bf16 a[132:135], v[128:131], v[128:131], 0\n"
    "v_mfma_f32_16x16x32_bf16 a[136:139], v[128:131], v[128:131], 0\n"
    "v_mfma_f32_16x16x32_bf16 a[140:143], v[128:131], v[128:131], 0\n"
    "v_mfma_f32_16x16x32_bf16 a[144:147], v[128:131], v[128:131], 0\n"
    "v_mfma_f32_16x16x32_bf16 a[148:151], v[128:131], v[128:131], 0\n"
    "v_mfma_f32_16x16x32_bf16 a[152:155], v[128:131], v[128:131], 0\n"
    "v_mfma_f32_16x16x32_bf16 a[156:159], v[128:131], v[128:131], 0\n"
    "v_mfma_f32_16x16x32_bf16 a[160:163], v[128:131], v[128:131], 0\n"
    "v_mfma_f32_16x16x32_bf16 a[164:167], v[128:131], v[128:131], 0\n"
    "v_mfma_f32_16x16x32_bf16 a[168:171], v[128:131], v[128:131], 0\n"
    "v_mfma_f32_16x16x32_bf16 a[172:175], v[128:131], v[128:131], 0\n"
    "v_mfma_f32_16x16x32_bf16 a[176:179], v[128:131], v[128:131], 0\n"
    "v_mfma_f32_16x16x32_bf16 a[180:183], v[128:131], v[128:131], 0\n"
    "v_mfma_f32_16x16x32_bf16 a[184:187], v[128:131], v[128:131], 0\n"
    "v_mfma_f32_16x16x32_bf16 a[188:191], v[128:131], v[128:131], 0\n"
    "v_mfma_f32_16x16x32_bf16 v[112:115], v[128:131], v[128:131], 0\n"
    "v_mfma_f32_16x16x32_bf16 v[116:119], v[128:131], v[128:131], 0\n"
    "v_mfma_f32_16x16x32_bf16 v[120:123], v[128:131], v[128:131], 0\n"
    "v_mfma_f32_16x16x32_bf16 v[124:127], v[128:131], v[128:131], 0\n"
    "s_nop 15\n"
    "LSLOOP:\n"
    "s_add_u32 s78, s63, 0\n"
    "s_add_u32 m0, s62, 0\n"
    "s_nop 0\n"
    "buffer_load_dwordx4 v200, s[36:39], s78 offen sc1 lds\n"
    "buffer_load_dwordx4 v200, s[36:39], s78 offen offset:1024 sc1 lds\n"
    "buffer_load_dwordx4 v200, s[36:39], s78 offen offset:2048 sc1 lds\n"
    "buffer_load_dwordx4 v200, s[36:39], s78 offen offset:3072 sc1 lds\n"
    "s_add_u32 s78, s63, 0x1000\n"
    "s_add_u32 m0, s62, 0x1000\n"
    "s_nop 0\n"
    "buffer_load_dwordx4 v200, s[36:39], s78 offen sc1 lds\n"
    "buffer_load_dwordx4 v200, s[36:39], s78 offen offset:1024 sc1 lds\n"
    "buffer_load_dwordx4 v200, s[36:39], s78 offen offset:2048 sc1 lds\n"
    "buffer_load_dwordx4 v200, s[36:39], s78 offen offset:3072 sc1 lds\n"
    "s_add_u32 s78, s63, 0x2000\n"
    "s_add_u32 m0, s62, 0x2000\n"
    "s_nop 0\n"
    "buffer_load_dwordx4 v200, s[36:39], s78 offen sc1 lds\n"
    "buffer_load_dwordx4 v200, s[36:39], s78 offen offset:1024 sc1 lds\n"
    "buffer_load_dwordx4 v200, s[36:39], s78 offen offset:2048 sc1 lds\n"
    "buffer_load_dwordx4 v200, s[36:39], s78 offen offset:3072 sc1 lds\n"
    "s_add_u32 s78, s63, 0\n"
    "s_add_u32 m0, s62, 0x3000\n"
    "s_nop 0\n"
    "buffer_load_dwordx4 v200, s[40:43], s78 offen sc1 lds\n"
    "buffer_load_dwordx4 v200, s[40:43], s78 offen offset:1024 sc1 lds\n"
    "buffer_load_dwordx4 v200, s[40:43], s78 offen offset:2048 sc1 lds\n"
    "buffer_load_dwordx4 v200, s[40:43], s78 offen offset:3072 sc1 lds\n"
    "s_add_u32 s78, s63, 0x1000\n"
    "s_add_u32 m0, s62, 0x4000\n"
    "s_nop 0\n"
    "buffer_load_dwordx4 v200, s[40:43], s78 offen sc1 lds\n"
    "buffer_load_dwordx4 v200, s[40:43], s78 offen offset:1024 sc1 lds\n"
    "buffer_load_dwordx4 v200, s[40:43], s78 offen offset:2048 sc1 lds\n"
    "buffer_load_dwordx4 v200, s[40:43], s78 offen offset:3072 sc1 lds\n"
    "s_add_u32 s78, s63, 0x2000\n"
    "s_add_u32 m0, s62, 0x5000\n"
    "s_nop 0\n"
    "buffer_load_dwordx4 v200, s[40:43], s78 offen sc1 lds\n"
    "buffer_load_dwordx4 v200, s[40:43], s78 offen offset:1024 sc1 lds\n"
    "buffer_load_dwordx4 v200, s[40:43], s78 offen offset:2048 sc1 lds\n"
    "buffer_load_dwordx4 v200, s[40:43], s78 offen offset:3072 sc1 lds\n"
    "s_add_u32 s78, s63, 0x3000\n"
    "s_add_u32 m0, s62, 0x6000\n"
    "s_nop 0\n"
    "buffer_load_dwordx4 v200, s[40:43], s78 offen sc1 lds\n"
    "buffer_load_dwordx4 v200, s[40:43], s78 offen offset:1024 sc1 lds\n"
    "buffer_load_dwordx4 v200, s[40:43], s78 offen offset:2048 sc1 lds\n"
    "buffer_load_dwordx4 v200, s[40:43], s78 offen offset:3072 sc1 lds\n"
    "s_add_u32 s78, s63, 0x4000\n"
    "s_add_u32 m0, s62, 0x7000\n"
    "s_nop 0\n"
    "buffer_load_dwordx4 v200, s[40:43], s78 offen sc1 lds\n"
    "buffer_load_dwordx4 v200, s[40:43], s78 offen offset:1024 sc1 lds\n"
    "buffer_load_dwordx4 v200, s[40:43], s78 offen offset:2048 sc1 lds\n"
    "buffer_load_dwordx4 v200, s[40:43], s78 offen offset:3072 sc1 lds\n"
    "s_add_u32 s78, s63, 0x5000\n"
    "s_add_u32 m0, s62, 0x8000\n"
    "s_nop 0\n"
    "buffer_load_dwordx4 v200, s[40:43], s78 offen sc1 lds\n"
    "buffer_load_dwordx4 v200, s[40:43], s78 offen offset:1024 sc1 lds\n"
    "buffer_load_dwordx4 v200, s[40:43], s78 offen offset:2048 sc1 lds\n"
    "buffer_load_dwordx4 v200, s[40:43], s78 offen offset:3072 sc1 lds\n"
    "s_waitcnt vmcnt(0)\n"
    "ds_read_b128 v[160:163], v205\n"
    "ds_read_b128 v[164:167], v205 offset:1024\n"
    "s_waitcnt lgkmcnt(1)\n"
    "v_mfma_f32_16x16x32_bf16 v[32:35], v[160:163], a[192:195], 0\n"
    "s_waitcnt lgkmcnt(0)\n"
    "v_mfma_f32_16x16x32_bf16 v[32:35], v[164:167], a[196:199], v[32:35]\n"
    "v_mfma_f32_16x16x32_bf16 v[36:39], v[164:167], a[220:223], 0\n"
    "v_mfma_f32_16x16x32_bf16 v[36:39], v[160:163], a[216:219], v[36:39]\n"
    "v_mfma_f32_16x16x32_bf16 v[40:43], v[160:163], a[240:243], 0\n"
    "v_mfma_f32_16x16x32_bf16 v[40:43], v[164:167], a[244:247], v[40:43]\n"
    "v_mfma_f32_16x16x32_bf16 v[44:47], v[164:167], v[12:15], 0\n"
    "v_mfma_f32_16x16x32_bf16 v[44:47], v[160:163], v[8:11], v[44:47]\n"
    "ds_read_b128 v[168:171], v205 offset:2048\n"
    "ds_read_b128 v[172:175], v205 offset:3072\n"
    "s_waitcnt lgkmcnt(1)\n"
    "v_mfma_f32_16x16x32_bf16 v[44:47], v[168:171], v[16:19], v[44:47]\n"
    "s_waitcnt lgkmcnt(0)\n"
    "v_mfma_f32_16x16x32_bf16 v[44:47], v[172:175], v[20:23], v[44:47]\n"
    "v_mfma_f32_16x16x32_bf16 v[40:43], v[172:175], a[252:255], v[40:43]\n"
    "v_mfma_f32_16x16x32_bf16 v[40:43], v[168:171], a[248:251], v[40:43]\n"
    "v_mfma_f32_16x16x32_bf16 v[36:39], v[168:171], a[224:227], v[36:39]\n"
    "v_mfma_f32_16x16x32_bf16 v[36:39], v[172:175], a[228:231], v[36:39]\n"
    "v_mfma_f32_16x16x32_bf16 v[32:35], v[172:175], a[204:207], v[32:35]\n"
    "v_mfma_f32_16x16x32_bf16 v[32:35], v[168:171], a[200:203], v[32:35]\n"
    "ds_read_b128 v[176:179], v205 offset:4096\n"
    "ds_read_b128 v[180:183], v205 offset:5120\n"
    "s_waitcnt lgkmcnt(1)\n"
    "v_mfma_f32_16x16x32_bf16 v[32:35], v[176:179], a[208:211], v[32:35]\n"
    "s_waitcnt lgkmcnt(0)\n"
    "v_mfma_f32_16x16x32_bf16 v[32:35], v[180:183], a[212:215], v[32:35]\n"
    "v_mfma_f32_16x16x32_bf16 v[36:39], v[180:183], a[236:239], v[36:39]\n"
    "v_mfma_f32_16x16x32_bf16 v[36:39], v[176:179], a[232:235], v[36:39]\n"
    "v_mfma_f32_16x16x32_bf16 v[40:43], v[176:179], v[0:3], v[40:43]\n"
    "v_mfma_f32_16x16x32_bf16 v[40:43], v[180:183], v[4:7], v[40:43]\n"
    "v_mfma_f32_16x16x32_bf16 v[44:47], v[180:183], v[28:31], v[44:47]\n"
    "v_mfma_f32_16x16x32_bf16 v[44:47], v[176:179], v[24:27], v[44:47]\n"
    "ds_read_b128 v[160:163], v205 offset:10240\n"
    "ds_read_b128 v[164:167], v205 offset:11264\n"
    "s_waitcnt lgkmcnt(1)\n"
    "v_mfma_f32_16x16x32_bf16 v[60:63], v[160:163], v[24:27], 0\n"
    "s_waitcnt lgkmcnt(0)\n"
    "v_mfma_f32_16x16x32_bf16 v[60:63], v[164:167], v[28:31], v[60:63]\n"
    "v_mfma_f32_16x16x32_bf16 v[56:59], v[164:167], v[4:7], 0\n"
    "v_mfma_f32_16x16x32_bf16 v[56:59], v[160:163], v[0:3], v[56:59]\n"
    "v_mfma_f32_16x16x32_bf16 v[52:55], v[160:163], a[232:235], 0\n"
    "v_mfma_f32_16x16x32_bf16 v[52:55], v[164:167], a[236:239], v[52:55]\n"
    "v_mfma_f32_16x16x32_bf16 v[48:51], v[164:167], a[212:215], 0\n"
    "v_mfma_f32_16x16x32_bf16 v[48:51], v[160:163], a[208:211], v[48:51]\n"
    "ds_read_b128 v[168:171], v205 offset:8192\n"
    "ds_read_b128 v[172:175], v205 offset:9216\n"
    "s_waitcnt lgkmcnt(1)\n"
    "v_mfma_f32_16x16x32_bf16 v[48:51], v[168:171], a[200:203], v[48:51]\n"
    "s_waitcnt lgkmcnt(0)\n"
    "v_mfma_f32_16x16x32_bf16 v[48:51], v[172:175], a[204:207], v[48:51]\n"
    "v_mfma_f32_16x16x32_bf16 v[52:55], v[172:175], a[228:231], v[52:55]\n"
    "v_mfma_f32_16x16x32_bf16 v[52:55], v[168:171], a[224:227], v[52:55]\n"
    "v_mfma_f32_16x16x32_bf16 v[56:59], v[168:171], a[248:251], v[56:59]\n"
    "v_mfma_f32_16x16x32_bf16 v[56:59], v[172:175], a[252:255], v[56:59]\n"
    "v_mfma_f32_16x16x32_bf16 v[60:63], v[172:175], v[20:23], v[60:63]\n"
    "v_mfma_f32_16x16x32_bf16 v[60:63], v[168:171], v[16:19], v[60:63]\n"
    "ds_read_b128 v[176:179], v205 offset:6144\n"
    "ds_read_b128 v[180:183], v205 offset:7168\n"
    "s_waitcnt lgkmcnt(1)\n"
    "v_mfma_f32_16x16x32_bf16 v[60:63], v[176:179], v[8:11], v[60:63]\n"
    "s_waitcnt lgkmcnt(0)\n"
    "v_mfma_f32_16x16x32_bf16 v[60:63], v[180:183], v[12:15], v[60:63]\n"
    "v_mfma_f32_16x16x32_bf16 v[56:59], v[180:183], a[244:247], v[56:59]\n"
    "v_mfma_f32_16x16x32_bf16 v[56:59], v[176:179], a[240:243], v[56:59]\n"
    "v_mfma_f32_16x16x32_bf16 v[52:55], v[176:179], a[216:219], v[52:55]\n"
    "v_mfma_f32_16x16x32_bf16 v[52:55], v[180:183], a[220:223], v[52:55]\n"
    "v_mfma_f32_16x16x32_bf16 v[48:51], v[180:183], a[196:199], v[48:51]\n"
    "v_mfma_f32_16x16x32_bf16 v[48:51], v[176:179], a[192:195], v[48:51]\n"
    "s_add_u32 s78, s63, 0x3000\n"
    "s_add_u32 m0, s62, 0\n"
    "s_nop 0\n"
    "buffer_load_dwordx4 v200, s[36:39], s78 offen sc1 lds\n"
    "buffer_load_dwordx4 v200, s[36:39], s78 offen offset:1024 sc1 lds\n"
    "buffer_load_dwordx4 v200, s[36:39], s78 offen offset:2048 sc1 lds\n"
    "buffer_load_dwordx4 v200, s[36:39], s78 offen offset:3072 sc1 lds\n"
    "s_add_u32 s78, s63, 0x4000\n"
    "s_add_u32 m0, s62, 0x1000\n"
    "s_nop 0\n"
    "buffer_load_dwordx4 v200, s[36:39], s78 offen sc1 lds\n"
    "buffer_load_dwordx4 v200, s[36:39], s78 offen offset:1024 sc1 lds\n"
    "buffer_load_dwordx4 v200, s[36:39], s78 offen offset:2048 sc1 lds\n"
    "buffer_load_dwordx4 v200, s[36:39], s78 offen offset:3072 sc1 lds\n"
    "s_add_u32 s78, s63, 0x5000\n"
    "s_add_u32 m0, s62, 0x2000\n"
    "s_nop 0\n"
    "buffer_load_dwordx4 v200, s[36:39], s78 offen sc1 lds\n"
    "buffer_load_dwordx4 v200, s[36:39], s78 offen offset:1024 sc1 lds\n"
    "buffer_load_dwordx4 v200, s[36:39], s78 offen offset:2048 sc1 lds\n"
    "buffer_load_dwordx4 v200, s[36:39], s78 offen offset:3072 sc1 lds\n"
    "s_waitcnt vmcnt(0)\n"
    "ds_read_b128 v[160:163], v205\n"
    "ds_read_b128 v[164:167], v205 offset:1024\n"
    "s_waitcnt lgkmcnt(1)\n"
    "v_mfma_f32_16x16x32_bf16 v[64:67], v[160:163], a[192:195], 0\n"
    "s_waitcnt lgkmcnt(0)\n"
    "v_mfma_f32_16x16x32_bf16 v[64:67], v[164:167], a[196:199], v[64:67]\n"
    "v_mfma_f32_16x16x32_bf16 v[68:71], v[164:167], a[220:223], 0\n"
    "v_mfma_f32_16x16x32_bf16 v[68:71], v[160:163], a[216:219], v[68:71]\n"
    "v_mfma_f32_16x16x32_bf16 v[72:75], v[160:163], a[240:243], 0\n"
    "v_mfma_f32_16x16x32_bf16 v[72:75], v[164:167], a[244:247], v[72:75]\n"
    "v_mfma_f32_16x16x32_bf16 v[76:79], v[164:167], v[12:15], 0\n"
    "v_mfma_f32_16x16x32_bf16 v[76:79], v[160:163], v[8:11], v[76:79]\n"
    "ds_read_b128 v[168:171], v205 offset:2048\n"
    "ds_read_b128 v[172:175], v205 offset:3072\n"
    "s_waitcnt lgkmcnt(1)\n"
    "v_mfma_f32_16x16x32_bf16 v[76:79], v[168:171], v[16:19], v[76:79]\n"
    "s_waitcnt lgkmcnt(0)\n"
    "v_mfma_f32_16x16x32_bf16 v[76:79], v[172:175], v[20:23], v[76:79]\n"
    "v_mfma_f32_16x16x32_bf16 v[72:75], v[172:175], a[252:255], v[72:75]\n"
    "v_mfma_f32_16x16x32_bf16 v[72:75], v[168:171], a[248:251], v[72:75]\n"
    "v_mfma_f32_16x16x32_bf16 v[68:71], v[168:171], a[224:227], v[68:71]\n"
    "v_mfma_f32_16x16x32_bf16 v[68:71], v[172:175], a[228:231], v[68:71]\n"
    "v_mfma_f32_16x16x32_bf16 v[64:67], v[172:175], a[204:207], v[64:67]\n"
    "v_mfma_f32_16x16x32_bf16 v[64:67], v[168:171], a[200:203], v[64:67]\n"
    "ds_read_b128 v[176:179], v205 offset:4096\n"
    "ds_read_b128 v[180:183], v205 offset:5120\n"
    "s_waitcnt lgkmcnt(1)\n"
    "v_mfma_f32_16x16x32_bf16 v[64:67], v[176:179], a[208:211], v[64:67]\n"
    "s_waitcnt lgkmcnt(0)\n"
    "v_mfma_f32_16x16x32_bf16 v[64:67], v[180:183], a[212:215], v[64:67]\n"
    "v_mfma_f32_16x16x32_bf16 v[68:71], v[180:183], a[236:239], v[68:71]\n"
    "v_mfma_f32_16x16x32_bf16 v[68:71], v[176:179], a[232:235], v[68:71]\n"
    "v_mfma_f32_16x16x32_bf16 v[72:75], v[176:179], v[0:3], v[72:75]\n"
    "v_mfma_f32_16x16x32_bf16 v[72:75], v[180:183], v[4:7], v[72:75]\n"
    "v_mfma_f32_16x16x32_bf16 v[76:79], v[180:183], v[28:31], v[76:79]\n"
    "v_mfma_f32_16x16x32_bf16 v[76:79], v[176:179], v[24:27], v[76:79]\n"
    "ds_read_b128 v[160:163], v205 offset:10240\n"
    "ds_read_b128 v[164:167], v205 offset:11264\n"
    "s_waitcnt lgkmcnt(1)\n"
    "v_mfma_f32_16x16x32_bf16 v[92:95], v[160:163], v[24:27], 0\n"
    "s_waitcnt lgkmcnt(0)\n"
    "v_mfma_f32_16x16x32_bf16 v[92:95], v[164:167], v[28:31], v[92:95]\n"
    "v_mfma_f32_16x16x32_bf16 v[88:91], v[164:167], v[4:7], 0\n"
    "v_mfma_f32_16x16x32_bf16 v[88:91], v[160:163], v[0:3], v[88:91]\n"
    "v_mfma_f32_16x16x32_bf16 v[84:87], v[160:163], a[232:235], 0\n"
    "v_mfma_f32_16x16x32_bf16 v[84:87], v[164:167], a[236:239], v[84:87]\n"
    "v_mfma_f32_16x16x32_bf16 v[80:83], v[164:167], a[212:215], 0\n"
    "v_mfma_f32_16x16x32_bf16 v[80:83], v[160:163], a[208:211], v[80:83]\n"
    "ds_read_b128 v[168:171], v205 offset:8192\n"
    "ds_read_b128 v[172:175], v205 offset:9216\n"
    "s_waitcnt lgkmcnt(1)\n"
    "v_mfma_f32_16x16x32_bf16 v[80:83], v[168:171], a[200:203], v[80:83]\n"
    "s_waitcnt lgkmcnt(0)\n"
    "v_mfma_f32_16x16x32_bf16 v[80:83], v[172:175], a[204:207], v[80:83]\n"
    "v_mfma_f32_16x16x32_bf16 v[84:87], v[172:175], a[228:231], v[84:87]\n"
    "v_mfma_f32_16x16x32_bf16 v[84:87], v[168:171], a[224:227], v[84:87]\n"
    "v_mfma_f32_16x16x32_bf16 v[88:91], v[168:171], a[248:251], v[88:91]\n"
    "v_mfma_f32_16x16x32_bf16 v[88:91], v[172:175], a[252:255], v[88:91]\n"
    "v_mfma_f32_16x16x32_bf16 v[92:95], v[172:175], v[20:23], v[92:95]\n"
    "v_mfma_f32_16x16x32_bf16 v[92:95], v[168:171], v[16:19], v[92:95]\n"
    "ds_read_b128 v[176:179], v205 offset:6144\n"
    "ds_read_b128 v[180:183], v205 offset:7168\n"
    "s_waitcnt lgkmcnt(1)\n"
    "v_mfma_f32_16x16x32_bf16 v[92:95], v[176:179], v[8:11], v[92:95]\n"
    "s_waitcnt lgkmcnt(0)\n"
    "v_mfma_f32_16x16x32_bf16 v[92:95], v[180:183], v[12:15], v[92:95]\n"
    "v_mfma_f32_16x16x32_bf16 v[88:91], v[180:183], a[244:247], v[88:91]\n"
    "v_mfma_f32_16x16x32_bf16 v[88:91], v[176:179], a[240:243], v[88:91]\n"
    "v_mfma_f32_16x16x32_bf16 v[84:87], v[176:179], a[216:219], v[84:87]\n"
    "v_mfma_f32_16x16x32_bf16 v[84:87], v[180:183], a[220:223], v[84:87]\n"
    "v_mfma_f32_16x16x32_bf16 v[80:83], v[180:183], a[196:199], v[80:83]\n"
    "v_mfma_f32_16x16x32_bf16 v[80:83], v[176:179], a[192:195], v[80:83]\n"
    "v_max3_f32 v196, v32, v33, v34\n"
    "v_max3_f32 v196, v196, v35, v48\n"
    "v_max3_f32 v196, v196, v49, v50\n"
    "v_max3_f32 v196, v196, v51, v64\n"
    "v_max3_f32 v196, v196, v65, v66\n"
    "s_nop 2\n"
    "v_max3_f32 v196, v196, v67, v80\n"
    "v_max3_f32 v196, v196, v81, v82\n"
    "v_max_f32 v196, v196, v83\n"
    "v_mov_b32 v197, v196\n"
    "s_nop 1\n"
    "v_permlane32_swap_b32 v196, v197\n"
    "v_max_f32 v196, v196, v197\n"
    "v_mov_b32 v197, v196\n"
    "s_nop 1\n"
    "v_permlane16_swap_b32 v196, v197\n"
    "v_max_f32 v196, v196, v197\n"
    "v_max_f32 v196, v196, v96\n"
    "v_sub_f32 v197, v96, v196\n"
    "v_mov_b32 v96, v196\n"
    "v_exp_f32 v197, v197\n"
    "v_sub_f32 v32, v32, v196\n"
    "v_sub_f32 v33, v33, v196\n"
    "v_sub_f32 v34, v34, v196\n"
    "v_sub_f32 v35, v35, v196\n"
    "v_sub_f32 v48, v48, v196\n"
    "v_sub_f32 v49, v49, v196\n"
    "v_sub_f32 v50, v50, v196\n"
    "v_sub_f32 v51, v51, v196\n"
    "v_sub_f32 v64, v64, v196\n"
    "v_sub_f32 v65, v65, v196\n"
    "v_sub_f32 v66, v66, v196\n"
    "v_sub_f32 v67, v67, v196\n"
    "v_sub_f32 v80, v80, v196\n"
    "v_sub_f32 v81, v81, v196\n"
    "v_sub_f32 v82, v82, v196\n"
    "v_sub_f32 v83, v83, v196\n"
    "v_mul_f32 v112, v112, v197\n"
    "v_mul_f32 v113, v113, v197\n"
    "v_mul_f32 v114, v114, v197\n"
    "v_mul_f32 v115, v115, v197\n"
    "v_accvgpr_read_b32 v198, a0\n"
    "v_accvgpr_read_b32 v199, a1\n"
    "v_mul_f32 v198, v198, v197\n"
    "v_mul_f32 v199, v199, v197\n"
    "v_accvgpr_write_b32 a0, v198\n"
    "v_accvgpr_write_b32 a1, v199\n"
    "v_accvgpr_read_b32 v198, a2\n"
    "v_accvgpr_read_b32 v199, a3\n"
    "v_mul_f32 v198, v198, v197\n"
    "v_mul_f32 v199, v199, v197\n"
    "v_accvgpr_write_b32 a2, v198\n"
    "v_accvgpr_write_b32 a3, v199\n"
    "v_accvgpr_read_b32 v198, a16\n"
    "v_accvgpr_read_b32 v199, a17\n"
    "v_mul_f32 v198, v198, v197\n"
    "v_mul_f32 v199, v199, v197\n"
    "v_accvgpr_write_b32 a16, v198\n"
    "v_accvgpr_write_b32 a17, v199\n"
    "v_accvgpr_read_b32 v198, a18\n"
    "v_accvgpr_read_b32 v199, a19\n"
    "v_mul_f32 v198, v198, v197\n"
    "v_mul_f32 v199, v199, v197\n"
    "v_accvgpr_write_b32 a18, v198\n"
    "v_accvgpr_write_b32 a19, v199\n"
    "v_accvgpr_read_b32 v198, a32\n"
    "v_accvgpr_read_b32 v199, a33\n"
    "v_mul_f32 v198, v198, v197\n"
    "v_mul_f32 v199, v199, v197\n"
    "v_accvgpr_write_b32 a32, v198\n"
    "v_accvgpr_write_b32 a33, v199\n"
    "v_accvgpr_read_b32 v198, a34\n"
    "v_accvgpr_read_b32 v199, a35\n"
    "v_mul_f32 v198, v198, v197\n"
    "v_mul_f32 v199, v199, v197\n"
    "v_accvgpr_write_b32 a34, v198\n"
    "v_accvgpr_write_b32 a35, v199\n"
    "v_accvgpr_read_b32 v198, a48\n"
    "v_accvgpr_read_b32 v199, a49\n"
    "v_mul_f32 v198, v198, v197\n"
    "v_mul_f32 v199, v199, v197\n"
    "v_accvgpr_write_b32 a48, v198\n"
    "v_accvgpr_write_b32 a49, v199\n"
    "v_accvgpr_read_b32 v198, a50\n"
    "v_accvgpr_read_b32 v199, a51\n"
    "v_mul_f32 v198, v198, v197\n"
    "v_mul_f32 v199, v199, v197\n"
    "v_accvgpr_write_b32 a50, v198\n"
    "v_accvgpr_write_b32 a51, v199\n"
    "v_accvgpr_read_b32 v198, a64\n"
    "v_accvgpr_read_b32 v199, a65\n"
    "v_mul_f32 v198, v198, v197\n"
    "v_mul_f32 v199, v199, v197\n"
    "v_accvgpr_write_b32 a64, v198\n"
    "v_accvgpr_write_b32 a65, v199\n"
    "v_accvgpr_read_b32 v198, a66\n"
    "v_accvgpr_read_b32 v199, a67\n"
    "v_mul_f32 v198, v198, v197\n"
    "v_mul_f32 v199, v199, v197\n"
    "v_accvgpr_write_b32 a66, v198\n"
    "v_accvgpr_write_b32 a67, v199\n"
    "v_accvgpr_read_b32 v198, a80\n"
    "v_accvgpr_read_b32 v199, a81\n"
    "v_mul_f32 v198, v198, v197\n"
    "v_mul_f32 v199, v199, v197\n"
    "v_accvgpr_write_b32 a80, v198\n"
    "v_accvgpr_write_b32 a81, v199\n"
    "v_accvgpr_read_b32 v198, a82\n"
    "v_accvgpr_read_b32 v199, a83\n"
    "v_mul_f32 v198, v198, v197\n"
    "v_mul_f32 v199, v199, v197\n"
    "v_accvgpr_write_b32 a82, v198\n"
    "v_accvgpr_write_b32 a83, v199\n"
    "v_accvgpr_read_b32 v198, a96\n"
    "v_accvgpr_read_b32 v199, a97\n"
    "v_mul_f32 v198, v198, v197\n"
    "v_mul_f32 v199, v199, v197\n"
    "v_accvgpr_write_b32 a96, v198\n"
    "v_accvgpr_write_b32 a97, v199\n"
    "v_accvgpr_read_b32 v198, a98\n"
    "v_accvgpr_read_b32 v199, a99\n"
    "v_mul_f32 v198, v198, v197\n"
    "v_mul_f32 v199, v199, v197\n"
    "v_accvgpr_write_b32 a98, v198\n"
    "v_accvgpr_write_b32 a99, v199\n"
    "v_accvgpr_read_b32 v198, a112\n"
    "v_accvgpr_read_b32 v199, a113\n"
    "v_mul_f32 v198, v198, v197\n"
    "v_mul_f32 v199, v199, v197\n"
    "v_accvgpr_write_b32 a112, v198\n"
    "v_accvgpr_write_b32 a113, v199\n"
    "v_accvgpr_read_b32 v198, a114\n"
    "v_accvgpr_read_b32 v199, a115\n"
    "v_mul_f32 v198, v198, v197\n"
    "v_mul_f32 v199, v199, v197\n"
    "v_accvgpr_write_b32 a114, v198\n"
    "v_accvgpr_write_b32 a115, v199\n"
    "v_accvgpr_read_b32 v198, a128\n"
    "v_accvgpr_read_b32 v199, a129\n"
    "v_mul_f32 v198, v198, v197\n"
    "v_mul_f32 v199, v199, v197\n"
    "v_accvgpr_write_b32 a128, v198\n"
    "v_accvgpr_write_b32 a129, v199\n"
    "v_accvgpr_read_b32 v198, a130\n"
    "v_accvgpr_read_b32 v199, a131\n"
    "v_mul_f32 v198, v198, v197\n"
    "v_mul_f32 v199, v199, v197\n"
    "v_accvgpr_write_b32 a130, v198\n"
    "v_accvgpr_write_b32 a131, v199\n"
    "v_accvgpr_read_b32 v198, a144\n"
    "v_accvgpr_read_b32 v199, a145\n"
    "v_mul_f32 v198, v198, v197\n"
    "v_mul_f32 v199, v199, v197\n"
    "v_accvgpr_write_b32 a144, v198\n"
    "v_accvgpr_write_b32 a145, v199\n"
    "v_accvgpr_read_b32 v198, a146\n"
    "v_accvgpr_read_b32 v199, a147\n"
    "v_mul_f32 v198, v198, v197\n"
    "v_mul_f32 v199, v199, v197\n"
    "v_accvgpr_write_b32 a146, v198\n"
    "v_accvgpr_write_b32 a147, v199\n"
    "v_accvgpr_read_b32 v198, a160\n"
    "v_accvgpr_read_b32 v199, a161\n"
    "v_mul_f32 v198, v198, v197\n"
    "v_mul_f32 v199, v199, v197\n"
    "v_accvgpr_write_b32 a160, v198\n"
    "v_accvgpr_write_b32 a161, v199\n"
    "v_accvgpr_read_b32 v198, a162\n"
    "v_accvgpr_read_b32 v199, a163\n"
    "v_mul_f32 v198, v198, v197\n"
    "v_mul_f32 v199, v199, v197\n"
    "v_accvgpr_write_b32 a162, v198\n"
    "v_accvgpr_write_b32 a163, v199\n"
    "v_accvgpr_read_b32 v198, a176\n"
    "v_accvgpr_read_b32 v199, a177\n"
    "v_mul_f32 v198, v198, v197\n"
    "v_mul_f32 v199, v199, v197\n"
    "v_accvgpr_write_b32 a176, v198\n"
    "v_accvgpr_write_b32 a177, v199\n"
    "v_accvgpr_read_b32 v198, a178\n"
    "v_accvgpr_read_b32 v199, a179\n"
    "v_mul_f32 v198, v198, v197\n"
    "v_mul_f32 v199, v199, v197\n"
    "v_accvgpr_write_b32 a178, v198\n"
    "v_accvgpr_write_b32 a179, v199\n"
    "v_max3_f32 v196, v36, v37, v38\n"
    "v_max3_f32 v196, v196, v39, v52\n"
    "v_max3_f32 v196, v196, v53, v54\n"
    "v_max3_f32 v196, v196, v55, v68\n"
    "v_max3_f32 v196, v196, v69, v70\n"
    "v_max3_f32 v196, v196, v71, v84\n"
    "v_max3_f32 v196, v196, v85, v86\n"
    "v_max_f32 v196, v196, v87\n"
    "v_mov_b32 v197, v196\n"
    "s_nop 1\n"
    "v_permlane32_swap_b32 v196, v197\n"
    "v_max_f32 v196, v196, v197\n"
    "v_mov_b32 v197, v196\n"
    "s_nop 1\n"
    "v_permlane16_swap_b32 v196, v197\n"
    "v_max_f32 v196, v196, v197\n"
    "v_max_f32 v196, v196, v100\n"
    "v_sub_f32 v197, v100, v196\n"
    "v_mov_b32 v100, v196\n"
    "v_exp_f32 v197, v197\n"
    "v_sub_f32 v36, v36, v196\n"
    "v_sub_f32 v37, v37, v196\n"
    "v_sub_f32 v38, v38, v196\n"
    "v_sub_f32 v39, v39, v196\n"
    "v_sub_f32 v52, v52, v196\n"
    "v_sub_f32 v53, v53, v196\n"
    "v_sub_f32 v54, v54, v196\n"
    "v_sub_f32 v55, v55, v196\n"
    "v_sub_f32 v68, v68, v196\n"
    "v_sub_f32 v69, v69, v196\n"
    "v_sub_f32 v70, v70, v196\n"
    "v_sub_f32 v71, v71, v196\n"
    "v_sub_f32 v84, v84, v196\n"
    "v_sub_f32 v85, v85, v196\n"
    "v_sub_f32 v86, v86, v196\n"
    "v_sub_f32 v87, v87, v196\n"
    "v_mul_f32 v116, v116, v197\n"
    "v_mul_f32 v117, v117, v197\n"
    "v_mul_f32 v118, v118, v197\n"
    "v_mul_f32 v119, v119, v197\n"
    "v_accvgpr_read_b32 v198, a4\n"
    "v_accvgpr_read_b32 v199, a5\n"
    "v_mul_f32 v198, v198, v197\n"
    "v_mul_f32 v199, v199, v197\n"
    "v_accvgpr_write_b32 a4, v198\n"
    "v_accvgpr_write_b32 a5, v199\n"
    "v_accvgpr_read_b32 v198, a6\n"
    "v_accvgpr_read_b32 v199, a7\n"
    "v_mul_f32 v198, v198, v197\n"
    "v_mul_f32 v199, v199, v197\n"
    "v_accvgpr_write_b32 a6, v198\n"
    "v_accvgpr_write_b32 a7, v199\n"
    "v_accvgpr_read_b32 v198, a20\n"
    "v_accvgpr_read_b32 v199, a21\n"
    "v_mul_f32 v198, v198, v197\n"
    "v_mul_f32 v199, v199, v197\n"
    "v_accvgpr_write_b32 a20, v198\n"
    "v_accvgpr_write_b32 a21, v199\n"
    "v_accvgpr_read_b32 v198, a22\n"
    "v_accvgpr_read_b32 v199, a23\n"
    "v_mul_f32 v198, v198, v197\n"
    "v_mul_f32 v199, v199, v197\n"
    "v_accvgpr_write_b32 a22, v198\n"
    "v_accvgpr_write_b32 a23, v199\n"
    "v_accvgpr_read_b32 v198, a36\n"
    "v_accvgpr_read_b32 v199, a37\n"
    "v_mul_f32 v198, v198, v197\n"
    "v_mul_f32 v199, v199, v197\n"
    "v_accvgpr_write_b32 a36, v198\n"
    "v_accvgpr_write_b32 a37, v199\n"
    "v_accvgpr_read_b32 v198, a38\n"
    "v_accvgpr_read_b32 v199, a39\n"
    "v_mul_f32 v198, v198, v197\n"
    "v_mul_f32 v199, v199, v197\n"
    "v_accvgpr_write_b32 a38, v198\n"
    "v_accvgpr_write_b32 a39, v199\n"
    "v_accvgpr_read_b32 v198, a52\n"
    "v_accvgpr_read_b32 v199, a53\n"
    "v_mul_f32 v198, v198, v197\n"
    "v_mul_f32 v199, v199, v197\n"
    "v_accvgpr_write_b32 a52, v198\n"
    "v_accvgpr_write_b32 a53, v199\n"
    "v_accvgpr_read_b32 v198, a54\n"
    "v_accvgpr_read_b32 v199, a55\n"
    "v_mul_f32 v198, v198, v197\n"
    "v_mul_f32 v199, v199, v197\n"
    "v_accvgpr_write_b32 a54, v198\n"
    "v_accvgpr_write_b32 a55, v199\n"
    "v_accvgpr_read_b32 v198, a68\n"
    "v_accvgpr_read_b32 v199, a69\n"
    "v_mul_f32 v198, v198, v197\n"
    "v_mul_f32 v199, v199, v197\n"
    "v_accvgpr_write_b32 a68, v198\n"
    "v_accvgpr_write_b32 a69, v199\n"
    "v_accvgpr_read_b32 v198, a70\n"
    "v_accvgpr_read_b32 v199, a71\n"
    "v_mul_f32 v198, v198, v197\n"
    "v_mul_f32 v199, v199, v197\n"
    "v_accvgpr_write_b32 a70, v198\n"
    "v_accvgpr_write_b32 a71, v199\n"
    "v_accvgpr_read_b32 v198, a84\n"
    "v_accvgpr_read_b32 v199, a85\n"
    "v_mul_f32 v198, v198, v197\n"
    "v_mul_f32 v199, v199, v197\n"
    "v_accvgpr_write_b32 a84, v198\n"
    "v_accvgpr_write_b32 a85, v199\n"
    "v_accvgpr_read_b32 v198, a86\n"
    "v_accvgpr_read_b32 v199, a87\n"
    "v_mul_f32 v198, v198, v197\n"
    "v_mul_f32 v199, v199, v197\n"
    "v_accvgpr_write_b32 a86, v198\n"
    "v_accvgpr_write_b32 a87, v199\n"
    "v_accvgpr_read_b32 v198, a100\n"
    "v_accvgpr_read_b32 v199, a101\n"
    "v_mul_f32 v198, v198, v197\n"
    "v_mul_f32 v199, v199, v197\n"
    "v_accvgpr_write_b32 a100, v198\n"
    "v_accvgpr_write_b32 a101, v199\n"
    "v_accvgpr_read_b32 v198, a102\n"
    "v_accvgpr_read_b32 v199, a103\n"
    "v_mul_f32 v198, v198, v197\n"
    "v_mul_f32 v199, v199, v197\n"
    "v_accvgpr_write_b32 a102, v198\n"
    "v_accvgpr_write_b32 a103, v199\n"
    "v_accvgpr_read_b32 v198, a116\n"
    "v_accvgpr_read_b32 v199, a117\n"
    "v_mul_f32 v198, v198, v197\n"
    "v_mul_f32 v199, v199, v197\n"
    "v_accvgpr_write_b32 a116, v198\n"
    "v_accvgpr_write_b32 a117, v199\n"
    "v_accvgpr_read_b32 v198, a118\n"
    "v_accvgpr_read_b32 v199, a119\n"
    "v_mul_f32 v198, v198, v197\n"
    "v_mul_f32 v199, v199, v197\n"
    "v_accvgpr_write_b32 a118, v198\n"
    "v_accvgpr_write_b32 a119, v199\n"
    "v_accvgpr_read_b32 v198, a132\n"
    "v_accvgpr_read_b32 v199, a133\n"
    "v_mul_f32 v198, v198, v197\n"
    "v_mul_f32 v199, v199, v197\n"
    "v_accvgpr_write_b32 a132, v198\n"
    "v_accvgpr_write_b32 a133, v199\n"
    "v_accvgpr_read_b32 v198, a134\n"
    "v_accvgpr_read_b32 v199, a135\n"
    "v_mul_f32 v198, v198, v197\n"
    "v_mul_f32 v199, v199, v197\n"
    "v_accvgpr_write_b32 a134, v198\n"
    "v_accvgpr_write_b32 a135, v199\n"
    "v_accvgpr_read_b32 v198, a148\n"
    "v_accvgpr_read_b32 v199, a149\n"
    "v_mul_f32 v198, v198, v197\n"
    "v_mul_f32 v199, v199, v197\n"
    "v_accvgpr_write_b32 a148, v198\n"
    "v_accvgpr_write_b32 a149, v199\n"
    "v_accvgpr_read_b32 v198, a150\n"
    "v_accvgpr_read_b32 v199, a151\n"
    "v_mul_f32 v198, v198, v197\n"
    "v_mul_f32 v199, v199, v197\n"
    "v_accvgpr_write_b32 a150, v198\n"
    "v_accvgpr_write_b32 a151, v199\n"
    "v_accvgpr_read_b32 v198, a164\n"
    "v_accvgpr_read_b32 v199, a165\n"
    "v_mul_f32 v198, v198, v197\n"
    "v_mul_f32 v199, v199, v197\n"
    "v_accvgpr_write_b32 a164, v198\n"
    "v_accvgpr_write_b32 a165, v199\n"
    "v_accvgpr_read_b32 v198, a166\n"
    "v_accvgpr_read_b32 v199, a167\n"
    "v_mul_f32 v198, v198, v197\n"
    "v_mul_f32 v199, v199, v197\n"
    "v_accvgpr_write_b32 a166, v198\n"
    "v_accvgpr_write_b32 a167, v199\n"
    "v_accvgpr_read_b32 v198, a180\n"
    "v_accvgpr_read_b32 v199, a181\n"
    "v_mul_f32 v198, v198, v197\n"
    "v_mul_f32 v199, v199, v197\n"
    "v_accvgpr_write_b32 a180, v198\n"
    "v_accvgpr_write_b32 a181, v199\n"
    "v_accvgpr_read_b32 v198, a182\n"
    "v_accvgpr_read_b32 v199, a183\n"
    "v_mul_f32 v198, v198, v197\n"
    "v_mul_f32 v199, v199, v197\n"
    "v_accvgpr_write_b32 a182, v198\n"
    "v_accvgpr_write_b32 a183, v199\n"
    "v_max3_f32 v196, v40, v41, v42\n"
    "v_max3_f32 v196, v196, v43, v56\n"
    "v_max3_f32 v196, v196, v57, v58\n"
    "v_max3_f32 v196, v196, v59, v72\n"
    "v_max3_f32 v196, v196, v73, v74\n"
    "v_max3_f32 v196, v196, v75, v88\n"
    "v_max3_f32 v196, v196, v89, v90\n"
    "v_max_f32 v196, v196, v91\n"
    "v_mov_b32 v197, v196\n"
    "s_nop 1\n"
    "v_permlane32_swap_b32 v196, v197\n"
    "v_max_f32 v196, v196, v197\n"
    "v_mov_b32 v197, v196\n"
    "s_nop 1\n"
    "v_permlane16_swap_b32 v196, v197\n"
    "v_max_f32 v196, v196, v197\n"
    "v_max_f32 v196, v196, v104\n"
    "v_sub_f32 v197, v104, v196\n"
    "v_mov_b32 v104, v196\n"
    "v_exp_f32 v197, v197\n"
    "v_sub_f32 v40, v40, v196\n"
    "v_sub_f32 v41, v41, v196\n"
    "v_sub_f32 v42, v42, v196\n"
    "v_sub_f32 v43, v43, v196\n"
    "v_sub_f32 v56, v56, v196\n"
    "v_sub_f32 v57, v57, v196\n"
    "v_sub_f32 v58, v58, v196\n"
    "v_sub_f32 v59, v59, v196\n"
    "v_sub_f32 v72, v72, v196\n"
    "v_sub_f32 v73, v73, v196\n"
    "v_sub_f32 v74, v74, v196\n"
    "v_sub_f32 v75, v75, v196\n"
    "v_sub_f32 v88, v88, v196\n"
    "v_sub_f32 v89, v89, v196\n"
    "v_sub_f32 v90, v90, v196\n"
    "v_sub_f32 v91, v91, v196\n"
    "v_mul_f32 v120, v120, v197\n"
    "v_mul_f32 v121, v121, v197\n"
    "v_mul_f32 v122, v122, v197\n"
    "v_mul_f32 v123, v123, v197\n"
    "v_accvgpr_read_b32 v198, a8\n"
    "v_accvgpr_read_b32 v199, a9\n"
    "v_mul_f32 v198, v198, v197\n"
    "v_mul_f32 v199, v199, v197\n"
    "v_accvgpr_write_b32 a8, v198\n"
    "v_accvgpr_write_b32 a9, v199\n"
    "v_accvgpr_read_b32 v198, a10\n"
    "v_accvgpr_read_b32 v199, a11\n"
    "v_mul_f32 v198, v198, v197\n"
    "v_mul_f32 v199, v199, v197\n"
    "v_accvgpr_write_b32 a10, v198\n"
    "v_accvgpr_write_b32 a11, v199\n"
    "v_accvgpr_read_b32 v198, a24\n"
    "v_accvgpr_read_b32 v199, a25\n"
    "v_mul_f32 v198, v198, v197\n"
    "v_mul_f32 v199, v199, v197\n"
    "v_accvgpr_write_b32 a24, v198\n"
    "v_accvgpr_write_b32 a25, v199\n"
    "v_accvgpr_read_b32 v198, a26\n"
    "v_accvgpr_read_b32 v199, a27\n"
    "v_mul_f32 v198, v198, v197\n"
    "v_mul_f32 v199, v199, v197\n"
    "v_accvgpr_write_b32 a26, v198\n"
    "v_accvgpr_write_b32 a27, v199\n"
    "v_accvgpr_read_b32 v198, a40\n"
    "v_accvgpr_read_b32 v199, a41\n"
    "v_mul_f32 v198, v198, v197\n"
    "v_mul_f32 v199, v199, v197\n"
    "v_accvgpr_write_b32 a40, v198\n"
    "v_accvgpr_write_b32 a41, v199\n"
    "v_accvgpr_read_b32 v198, a42\n"
    "v_accvgpr_read_b32 v199, a43\n"
    "v_mul_f32 v198, v198, v197\n"
    "v_mul_f32 v199, v199, v197\n"
    "v_accvgpr_write_b32 a42, v198\n"
    "v_accvgpr_write_b32 a43, v199\n"
    "v_accvgpr_read_b32 v198, a56\n"
    "v_accvgpr_read_b32 v199, a57\n"
    "v_mul_f32 v198, v198, v197\n"
    "v_mul_f32 v199, v199, v197\n"
    "v_accvgpr_write_b32 a56, v198\n"
    "v_accvgpr_write_b32 a57, v199\n"
    "v_accvgpr_read_b32 v198, a58\n"
    "v_accvgpr_read_b32 v199, a59\n"
    "v_mul_f32 v198, v198, v197\n"
    "v_mul_f32 v199, v199, v197\n"
    "v_accvgpr_write_b32 a58, v198\n"
    "v_accvgpr_write_b32 a59, v199\n"
    "v_accvgpr_read_b32 v198, a72\n"
    "v_accvgpr_read_b32 v199, a73\n"
    "v_mul_f32 v198, v198, v197\n"
    "v_mul_f32 v199, v199, v197\n"
    "v_accvgpr_write_b32 a72, v198\n"
    "v_accvgpr_write_b32 a73, v199\n"
    "v_accvgpr_read_b32 v198, a74\n"
    "v_accvgpr_read_b32 v199, a75\n"
    "v_mul_f32 v198, v198, v197\n"
    "v_mul_f32 v199, v199, v197\n"
    "v_accvgpr_write_b32 a74, v198\n"
    "v_accvgpr_write_b32 a75, v199\n"
    "v_accvgpr_read_b32 v198, a88\n"
    "v_accvgpr_read_b32 v199, a89\n"
    "v_mul_f32 v198, v198, v197\n"
    "v_mul_f32 v199, v199, v197\n"
    "v_accvgpr_write_b32 a88, v198\n"
    "v_accvgpr_write_b32 a89, v199\n"
    "v_accvgpr_read_b32 v198, a90\n"
    "v_accvgpr_read_b32 v199, a91\n"
    "v_mul_f32 v198, v198, v197\n"
    "v_mul_f32 v199, v199, v197\n"
    "v_accvgpr_write_b32 a90, v198\n"
    "v_accvgpr_write_b32 a91, v199\n"
    "v_accvgpr_read_b32 v198, a104\n"
    "v_accvgpr_read_b32 v199, a105\n"
    "v_mul_f32 v198, v198, v197\n"
    "v_mul_f32 v199, v199, v197\n"
    "v_accvgpr_write_b32 a104, v198\n"
    "v_accvgpr_write_b32 a105, v199\n"
    "v_accvgpr_read_b32 v198, a106\n"
    "v_accvgpr_read_b32 v199, a107\n"
    "v_mul_f32 v198, v198, v197\n"
    "v_mul_f32 v199, v199, v197\n"
    "v_accvgpr_write_b32 a106, v198\n"
    "v_accvgpr_write_b32 a107, v199\n"
    "v_accvgpr_read_b32 v198, a120\n"
    "v_accvgpr_read_b32 v199, a121\n"
    "v_mul_f32 v198, v198, v197\n"
    "v_mul_f32 v199, v199, v197\n"
    "v_accvgpr_write_b32 a120, v198\n"
    "v_accvgpr_write_b32 a121, v199\n"
    "v_accvgpr_read_b32 v198, a122\n"
    "v_accvgpr_read_b32 v199, a123\n"
    "v_mul_f32 v198, v198, v197\n"
    "v_mul_f32 v199, v199, v197\n"
    "v_accvgpr_write_b32 a122, v198\n"
    "v_accvgpr_write_b32 a123, v199\n"
    "v_accvgpr_read_b32 v198, a136\n"
    "v_accvgpr_read_b32 v199, a137\n"
    "v_mul_f32 v198, v198, v197\n"
    "v_mul_f32 v199, v199, v197\n"
    "v_accvgpr_write_b32 a136, v198\n"
    "v_accvgpr_write_b32 a137, v199\n"
    "v_accvgpr_read_b32 v198, a138\n"
    "v_accvgpr_read_b32 v199, a139\n"
    "v_mul_f32 v198, v198, v197\n"
    "v_mul_f32 v199, v199, v197\n"
    "v_accvgpr_write_b32 a138, v198\n"
    "v_accvgpr_write_b32 a139, v199\n"
    "v_accvgpr_read_b32 v198, a152\n"
    "v_accvgpr_read_b32 v199, a153\n"
    "v_mul_f32 v198, v198, v197\n"
    "v_mul_f32 v199, v199, v197\n"
    "v_accvgpr_write_b32 a152, v198\n"
    "v_accvgpr_write_b32 a153, v199\n"
    "v_accvgpr_read_b32 v198, a154\n"
    "v_accvgpr_read_b32 v199, a155\n"
    "v_mul_f32 v198, v198, v197\n"
    "v_mul_f32 v199, v199, v197\n"
    "v_accvgpr_write_b32 a154, v198\n"
    "v_accvgpr_write_b32 a155, v199\n"
    "v_accvgpr_read_b32 v198, a168\n"
    "v_accvgpr_read_b32 v199, a169\n"
    "v_mul_f32 v198, v198, v197\n"
    "v_mul_f32 v199, v199, v197\n"
    "v_accvgpr_write_b32 a168, v198\n"
    "v_accvgpr_write_b32 a169, v199\n"
    "v_accvgpr_read_b32 v198, a170\n"
    "v_accvgpr_read_b32 v199, a171\n"
    "v_mul_f32 v198, v198, v197\n"
    "v_mul_f32 v199, v199, v197\n"
    "v_accvgpr_write_b32 a170, v198\n"
    "v_accvgpr_write_b32 a171, v199\n"
    "v_accvgpr_read_b32 v198, a184\n"
    "v_accvgpr_read_b32 v199, a185\n"
    "v_mul_f32 v198, v198, v197\n"
    "v_mul_f32 v199, v199, v197\n"
    "v_accvgpr_write_b32 a184, v198\n"
    "v_accvgpr_write_b32 a185, v199\n"
    "v_accvgpr_read_b32 v198, a186\n"
    "v_accvgpr_read_b32 v199, a187\n"
    "v_mul_f32 v198, v198, v197\n"
    "v_mul_f32 v199, v199, v197\n"
    "v_accvgpr_write_b32 a186, v198\n"
    "v_accvgpr_write_b32 a187, v199\n"
    "v_max3_f32 v196, v44, v45, v46\n"
    "v_max3_f32 v196, v196, v47, v60\n"
    "v_max3_f32 v196, v196, v61, v62\n"
    "v_max3_f32 v196, v196, v63, v76\n"
    "v_max3_f32 v196, v196, v77, v78\n"
    "v_max3_f32 v196, v196, v79, v92\n"
    "v_max3_f32 v196, v196, v93, v94\n"
    "v_max_f32 v196, v196, v95\n"
    "v_mov_b32 v197, v196\n"
    "s_nop 1\n"
    "v_permlane32_swap_b32 v196, v197\n"
    "v_max_f32 v196, v196, v197\n"
    "v_mov_b32 v197, v196\n"
    "s_nop 1\n"
    "v_permlane16_swap_b32 v196, v197\n"
    "v_max_f32 v196, v196, v197\n"
    "v_max_f32 v196, v196, v108\n"
    "v_sub_f32 v197, v108, v196\n"
    "v_mov_b32 v108, v196\n"
    "v_exp_f32 v197, v197\n"
    "v_sub_f32 v44, v44, v196\n"
    "v_sub_f32 v45, v45, v196\n"
    "v_sub_f32 v46, v46, v196\n"
    "v_sub_f32 v47, v47, v196\n"
    "v_sub_f32 v60, v60, v196\n"
    "v_sub_f32 v61, v61, v196\n"
    "v_sub_f32 v62, v62, v196\n"
    "v_sub_f32 v63, v63, v196\n"
    "v_sub_f32 v76, v76, v196\n"
    "v_sub_f32 v77, v77, v196\n"
    "v_sub_f32 v78, v78, v196\n"
    "v_sub_f32 v79, v79, v196\n"
    "v_sub_f32 v92, v92, v196\n"
    "v_sub_f32 v93, v93, v196\n"
    "v_sub_f32 v94, v94, v196\n"
    "v_sub_f32 v95, v95, v196\n"
    "v_mul_f32 v124, v124, v197\n"
    "v_mul_f32 v125, v125, v197\n"
    "v_mul_f32 v126, v126, v197\n"
    "v_mul_f32 v127, v127, v197\n"
    "v_accvgpr_read_b32 v198, a12\n"
    "v_accvgpr_read_b32 v199, a13\n"
    "v_mul_f32 v198, v198, v197\n"
    "v_mul_f32 v199, v199, v197\n"
    "v_accvgpr_write_b32 a12, v198\n"
    "v_accvgpr_write_b32 a13, v199\n"
    "v_accvgpr_read_b32 v198, a14\n"
    "v_accvgpr_read_b32 v199, a15\n"
    "v_mul_f32 v198, v198, v197\n"
    "v_mul_f32 v199, v199, v197\n"
    "v_accvgpr_write_b32 a14, v198\n"
    "v_accvgpr_write_b32 a15, v199\n"
    "v_accvgpr_read_b32 v198, a28\n"
    "v_accvgpr_read_b32 v199, a29\n"
    "v_mul_f32 v198, v198, v197\n"
    "v_mul_f32 v199, v199, v197\n"
    "v_accvgpr_write_b32 a28, v198\n"
    "v_accvgpr_write_b32 a29, v199\n"
    "v_accvgpr_read_b32 v198, a30\n"
    "v_accvgpr_read_b32 v199, a31\n"
    "v_mul_f32 v198, v198, v197\n"
    "v_mul_f32 v199, v199, v197\n"
    "v_accvgpr_write_b32 a30, v198\n"
    "v_accvgpr_write_b32 a31, v199\n"
    "v_accvgpr_read_b32 v198, a44\n"
    "v_accvgpr_read_b32 v199, a45\n"
    "v_mul_f32 v198, v198, v197\n"
    "v_mul_f32 v199, v199, v197\n"
    "v_accvgpr_write_b32 a44, v198\n"
    "v_accvgpr_write_b32 a45, v199\n"
    "v_accvgpr_read_b32 v198, a46\n"
    "v_accvgpr_read_b32 v199, a47\n"
    "v_mul_f32 v198, v198, v197\n"
    "v_mul_f32 v199, v199, v197\n"
    "v_accvgpr_write_b32 a46, v198\n"
    "v_accvgpr_write_b32 a47, v199\n"
    "v_accvgpr_read_b32 v198, a60\n"
    "v_accvgpr_read_b32 v199, a61\n"
    "v_mul_f32 v198, v198, v197\n"
    "v_mul_f32 v199, v199, v197\n"
    "v_accvgpr_write_b32 a60, v198\n"
    "v_accvgpr_write_b32 a61, v199\n"
    "v_accvgpr_read_b32 v198, a62\n"
    "v_accvgpr_read_b32 v199, a63\n"
    "v_mul_f32 v198, v198, v197\n"
    "v_mul_f32 v199, v199, v197\n"
    "v_accvgpr_write_b32 a62, v198\n"
    "v_accvgpr_write_b32 a63, v199\n"
    "v_accvgpr_read_b32 v198, a76\n"
    "v_accvgpr_read_b32 v199, a77\n"
    "v_mul_f32 v198, v198, v197\n"
    "v_mul_f32 v199, v199, v197\n"
    "v_accvgpr_write_b32 a76, v198\n"
    "v_accvgpr_write_b32 a77, v199\n"
    "v_accvgpr_read_b32 v198, a78\n"
    "v_accvgpr_read_b32 v199, a79\n"
    "v_mul_f32 v198, v198, v197\n"
    "v_mul_f32 v199, v199, v197\n"
    "v_accvgpr_write_b32 a78, v198\n"
    "v_accvgpr_write_b32 a79, v199\n"
    "v_accvgpr_read_b32 v198, a92\n"
    "v_accvgpr_read_b32 v199, a93\n"
    "v_mul_f32 v198, v198, v197\n"
    "v_mul_f32 v199, v199, v197\n"
    "v_accvgpr_write_b32 a92, v198\n"
    "v_accvgpr_write_b32 a93, v199\n"
    "v_accvgpr_read_b32 v198, a94\n"
    "v_accvgpr_read_b32 v199, a95\n"
    "v_mul_f32 v198, v198, v197\n"
    "v_mul_f32 v199, v199, v197\n"
    "v_accvgpr_write_b32 a94, v198\n"
    "v_accvgpr_write_b32 a95, v199\n"
    "v_accvgpr_read_b32 v198, a108\n"
    "v_accvgpr_read_b32 v199, a109\n"
    "v_mul_f32 v198, v198, v197\n"
    "v_mul_f32 v199, v199, v197\n"
    "v_accvgpr_write_b32 a108, v198\n"
    "v_accvgpr_write_b32 a109, v199\n"
    "v_accvgpr_read_b32 v198, a110\n"
    "v_accvgpr_read_b32 v199, a111\n"
    "v_mul_f32 v198, v198, v197\n"
    "v_mul_f32 v199, v199, v197\n"
    "v_accvgpr_write_b32 a110, v198\n"
    "v_accvgpr_write_b32 a111, v199\n"
    "v_accvgpr_read_b32 v198, a124\n"
    "v_accvgpr_read_b32 v199, a125\n"
    "v_mul_f32 v198, v198, v197\n"
    "v_mul_f32 v199, v199, v197\n"
    "v_accvgpr_write_b32 a124, v198\n"
    "v_accvgpr_write_b32 a125, v199\n"
    "v_accvgpr_read_b32 v198, a126\n"
    "v_accvgpr_read_b32 v199, a127\n"
    "v_mul_f32 v198, v198, v197\n"
    "v_mul_f32 v199, v199, v197\n"
    "v_accvgpr_write_b32 a126, v198\n"
    "v_accvgpr_write_b32 a127, v199\n"
    "v_accvgpr_read_b32 v198, a140\n"
    "v_accvgpr_read_b32 v199, a141\n"
    "v_mul_f32 v198, v198, v197\n"
    "v_mul_f32 v199, v199, v197\n"
    "v_accvgpr_write_b32 a140, v198\n"
    "v_accvgpr_write_b32 a141, v199\n"
    "v_accvgpr_read_b32 v198, a142\n"
    "v_accvgpr_read_b32 v199, a143\n"
    "v_mul_f32 v198, v198, v197\n"
    "v_mul_f32 v199, v199, v197\n"
    "v_accvgpr_write_b32 a142, v198\n"
    "v_accvgpr_write_b32 a143, v199\n"
    "v_accvgpr_read_b32 v198, a156\n"
    "v_accvgpr_read_b32 v199, a157\n"
    "v_mul_f32 v198, v198, v197\n"
    "v_mul_f32 v199, v199, v197\n"
    "v_accvgpr_write_b32 a156, v198\n"
    "v_accvgpr_write_b32 a157, v199\n"
    "v_accvgpr_read_b32 v198, a158\n"
    "v_accvgpr_read_b32 v199, a159\n"
    "v_mul_f32 v198, v198, v197\n"
    "v_mul_f32 v199, v199, v197\n"
    "v_accvgpr_write_b32 a158, v198\n"
    "v_accvgpr_write_b32 a159, v199\n"
    "v_accvgpr_read_b32 v198, a172\n"
    "v_accvgpr_read_b32 v199, a173\n"
    "v_mul_f32 v198, v198, v197\n"
    "v_mul_f32 v199, v199, v197\n"
    "v_accvgpr_write_b32 a172, v198\n"
    "v_accvgpr_write_b32 a173, v199\n"
    "v_accvgpr_read_b32 v198, a174\n"
    "v_accvgpr_read_b32 v199, a175\n"
    "v_mul_f32 v198, v198, v197\n"
    "v_mul_f32 v199, v199, v197\n"
    "v_accvgpr_write_b32 a174, v198\n"
    "v_accvgpr_write_b32 a175, v199\n"
    "v_accvgpr_read_b32 v198, a188\n"
    "v_accvgpr_read_b32 v199, a189\n"
    "v_mul_f32 v198, v198, v197\n"
    "v_mul_f32 v199, v199, v197\n"
    "v_accvgpr_write_b32 a188, v198\n"
    "v_accvgpr_write_b32 a189, v199\n"
    "v_accvgpr_read_b32 v198, a190\n"
    "v_accvgpr_read_b32 v199, a191\n"
    "v_mul_f32 v198, v198, v197\n"
    "v_mul_f32 v199, v199, v197\n"
    "v_accvgpr_write_b32 a190, v198\n"
    "v_accvgpr_write_b32 a191, v199\n"
    "v_exp_f32 v32, v32\n"
    "v_exp_f32 v33, v33\n"
    "v_exp_f32 v34, v34\n"
    "v_cvt_pk_bf16_f32 v128, v32, v33\n"
    "v_exp_f32 v35, v35\n"
    "v_exp_f32 v36, v36\n"
    "v_cvt_pk_bf16_f32 v129, v34, v35\n"
    "v_exp_f32 v37, v37\n"
    "v_exp_f32 v38, v38\n"
    "v_cvt_pk_bf16_f32 v132, v36, v37\n"
    "v_exp_f32 v39, v39\n"
    "v_exp_f32 v40, v40\n"
    "v_cvt_pk_bf16_f32 v133, v38, v39\n"
    "v_exp_f32 v41, v41\n"
    "v_exp_f32 v42, v42\n"
    "v_cvt_pk_bf16_f32 v136, v40, v41\n"
    "v_exp_f32 v43, v43\n"
    "v_exp_f32 v44, v44\n"
    "v_cvt_pk_bf16_f32 v137, v42, v43\n"
    "v_exp_f32 v45, v45\n"
    "v_exp_f32 v46, v46\n"
    "v_cvt_pk_bf16_f32 v140, v44, v45\n"
    "v_exp_f32 v47, v47\n"
    "v_exp_f32 v48, v48\n"
    "v_cvt_pk_bf16_f32 v141, v46, v47\n"
    "v_exp_f32 v49, v49\n"
    "v_exp_f32 v50, v50\n"
    "v_cvt_pk_bf16_f32 v130, v48, v49\n"
    "v_exp_f32 v51, v51\n"
    "v_exp_f32 v52, v52\n"
    "v_cvt_pk_bf16_f32 v131, v50, v51\n"
    "v_exp_f32 v53, v53\n"
    "v_exp_f32 v54, v54\n"
    "v_cvt_pk_bf16_f32 v134, v52, v53\n"
    "v_exp_f32 v55, v55\n"
    "v_exp_f32 v56, v56\n"
    "v_cvt_pk_bf16_f32 v135, v54, v55\n"
    "v_exp_f32 v57, v57\n"
    "v_exp_f32 v58, v58\n"
    "v_cvt_pk_bf16_f32 v138, v56, v57\n"
    "v_exp_f32 v59, v59\n"
    "v_exp_f32 v60, v60\n"
    "v_cvt_pk_bf16_f32 v139, v58, v59\n"
    "v_exp_f32 v61, v61\n"
    "v_exp_f32 v62, v62\n"
    "v_cvt_pk_bf16_f32 v142, v60, v61\n"
    "v_exp_f32 v63, v63\n"
    "s_nop 0\n"
    "v_cvt_pk_bf16_f32 v143, v62, v63\n"
    "v_exp_f32 v64, v64\n"
    "v_exp_f32 v65, v65\n"
    "v_exp_f32 v66, v66\n"
    "v_cvt_pk_bf16_f32 v144, v64, v65\n"
    "v_exp_f32 v67, v67\n"
    "v_exp_f32 v68, v68\n"
    "v_cvt_pk_bf16_f32 v145, v66, v67\n"
    "v_exp_f32 v69, v69\n"
    "v_exp_f32 v70, v70\n"
    "v_cvt_pk_bf16_f32 v148, v68, v69\n"
    "v_exp_f32 v71, v71\n"
    "v_exp_f32 v72, v72\n"
    "v_cvt_pk_bf16_f32 v149, v70, v71\n"
    "v_exp_f32 v73, v73\n"
    "v_exp_f32 v74, v74\n"
    "v_cvt_pk_bf16_f32 v152, v72, v73\n"
    "v_exp_f32 v75, v75\n"
    "v_exp_f32 v76, v76\n"
    "v_cvt_pk_bf16_f32 v153, v74, v75\n"
    "v_exp_f32 v77, v77\n"
    "v_exp_f32 v78, v78\n"
    "v_cvt_pk_bf16_f32 v156, v76, v77\n"
    "v_exp_f32 v79, v79\n"
    "v_exp_f32 v80, v80\n"
    "v_cvt_pk_bf16_f32 v157, v78, v79\n"
    "v_exp_f32 v81, v81\n"
    "v_exp_f32 v82, v82\n"
    "v_cvt_pk_bf16_f32 v146, v80, v81\n"
    "v_exp_f32 v83, v83\n"
    "v_exp_f32 v84, v84\n"
    "v_cvt_pk_bf16_f32 v147, v82, v83\n"
    "v_exp_f32 v85, v85\n"
    "v_exp_f32 v86, v86\n"
    "v_cvt_pk_bf16_f32 v150, v84, v85\n"
    "v_exp_f32 v87, v87\n"
    "v_exp_f32 v88, v88\n"
    "v_cvt_pk_bf16_f32 v151, v86, v87\n"
    "v_exp_f32 v89, v89\n"
    "v_exp_f32 v90, v90\n"
    "v_cvt_pk_bf16_f32 v154, v88, v89\n"
    "v_exp_f32 v91, v91\n"
    "v_exp_f32 v92, v92\n"
    "v_cvt_pk_bf16_f32 v155, v90, v91\n"
    "v_exp_f32 v93, v93\n"
    "v_exp_f32 v94, v94\n"
    "v_cvt_pk_bf16_f32 v158, v92, v93\n"
    "v_exp_f32 v95, v95\n"
    "s_nop 0\n"
    "v_cvt_pk_bf16_f32 v159, v94, v95\n"
    "ds_read_b128 v[160:163], v205 offset:12288\n"
    "ds_read_b128 v[164:167], v205 offset:13312\n"
    "s_waitcnt lgkmcnt(1)\n"
    "v_mfma_f32_16x16x32_bf16 a[0:3], v[160:163], v[128:131], a[0:3]\n"
    "s_waitcnt lgkmcnt(0)\n"
    "v_mfma_f32_16x16x32_bf16 a[0:3], v[164:167], v[144:147], a[0:3]\n"
    "v_mfma_f32_16x16x32_bf16 a[4:7], v[160:163], v[132:135], a[4:7]\n"
    "v_mfma_f32_16x16x32_bf16 a[4:7], v[164:167], v[148:151], a[4:7]\n"
    "v_mfma_f32_16x16x32_bf16 a[8:11], v[160:163], v[136:139], a[8:11]\n"
    "v_mfma_f32_16x16x32_bf16 a[8:11], v[164:167], v[152:155], a[8:11]\n"
    "v_mfma_f32_16x16x32_bf16 a[12:15], v[160:163], v[140:143], a[12:15]\n"
    "v_mfma_f32_16x16x32_bf16 a[12:15], v[164:167], v[156:159], a[12:15]\n"
    "ds_read_b128 v[168:171], v205 offset:14336\n"
    "ds_read_b128 v[172:175], v205 offset:15360\n"
    "s_waitcnt lgkmcnt(1)\n"
    "v_mfma_f32_16x16x32_bf16 a[28:31], v[168:171], v[140:143], a[28:31]\n"
    "s_waitcnt lgkmcnt(0)\n"
    "v_mfma_f32_16x16x32_bf16 a[28:31], v[172:175], v[156:159], a[28:31]\n"
    "v_mfma_f32_16x16x32_bf16 a[24:27], v[172:175], v[152:155], a[24:27]\n"
    "v_mfma_f32_16x16x32_bf16 a[24:27], v[168:171], v[136:139], a[24:27]\n"
    "v_mfma_f32_16x16x32_bf16 a[20:23], v[168:171], v[132:135], a[20:23]\n"
    "v_mfma_f32_16x16x32_bf16 a[20:23], v[172:175], v[148:151], a[20:23]\n"
    "v_mfma_f32_16x16x32_bf16 a[16:19], v[172:175], v[144:147], a[16:19]\n"
    "v_mfma_f32_16x16x32_bf16 a[16:19], v[168:171], v[128:131], a[16:19]\n"
    "ds_read_b128 v[176:179], v205 offset:16384\n"
    "ds_read_b128 v[180:183], v205 offset:17408\n"
    "s_waitcnt lgkmcnt(1)\n"
    "v_mfma_f32_16x16x32_bf16 a[32:35], v[176:179], v[128:131], a[32:35]\n"
    "s_waitcnt lgkmcnt(0)\n"
    "v_mfma_f32_16x16x32_bf16 a[32:35], v[180:183], v[144:147], a[32:35]\n"
    "v_mfma_f32_16x16x32_bf16 a[36:39], v[180:183], v[148:151], a[36:39]\n"
    "v_mfma_f32_16x16x32_bf16 a[36:39], v[176:179], v[132:135], a[36:39]\n"
    "v_mfma_f32_16x16x32_bf16 a[40:43], v[176:179], v[136:139], a[40:43]\n"
    "v_mfma_f32_16x16x32_bf16 a[40:43], v[180:183], v[152:155], a[40:43]\n"
    "v_mfma_f32_16x16x32_bf16 a[44:47], v[180:183], v[156:159], a[44:47]\n"
    "v_mfma_f32_16x16x32_bf16 a[44:47], v[176:179], v[140:143], a[44:47]\n"
    "ds_read_b128 v[160:163], v205 offset:18432\n"
    "ds_read_b128 v[164:167], v205 offset:19456\n"
    "s_waitcnt lgkmcnt(1)\n"
    "v_mfma_f32_16x16x32_bf16 a[60:63], v[160:163], v[140:143], a[60:63]\n"
    "s_waitcnt lgkmcnt(0)\n"
    "v_mfma_f32_16x16x32_bf16 a[60:63], v[164:167], v[156:159], a[60:63]\n"
    "v_mfma_f32_16x16x32_bf16 a[56:59], v[164:167], v[152:155], a[56:59]\n"
    "v_mfma_f32_16x16x32_bf16 a[56:59], v[160:163], v[136:139], a[56:59]\n"
    "v_mfma_f32_16x16x32_bf16 a[52:55], v[160:163], v[132:135], a[52:55]\n"
    "v_mfma_f32_16x16x32_bf16 a[52:55], v[164:167], v[148:151], a[52:55]\n"
    "v_mfma_f32_16x16x32_bf16 a[48:51], v[164:167], v[144:147], a[48:51]\n"
    "v_mfma_f32_16x16x32_bf16 a[48:51], v[160:163], v[128:131], a[48:51]\n"
    "ds_read_b128 v[168:171], v205 offset:20480\n"
    "ds_read_b128 v[172:175], v205 offset:21504\n"
    "s_waitcnt lgkmcnt(1)\n"
    "v_mfma_f32_16x16x32_bf16 a[64:67], v[168:171], v[128:131], a[64:67]\n"
    "s_waitcnt lgkmcnt(0)\n"
    "v_mfma_f32_16x16x32_bf16 a[64:67], v[172:175], v[144:147], a[64:67]\n"
    "v_mfma_f32_16x16x32_bf16 a[68:71], v[172:175], v[148:151], a[68:71]\n"
    "v_mfma_f32_16x16x32_bf16 a[68:71], v[168:171], v[132:135], a[68:71]\n"
    "v_mfma_f32_16x16x32_bf16 a[72:75], v[168:171], v[136:139], a[72:75]\n"
    "v_mfma_f32_16x16x32_bf16 a[72:75], v[172:175], v[152:155], a[72:75]\n"
    "v_mfma_f32_16x16x32_bf16 a[76:79], v[172:175], v[156:159], a[76:79]\n"
    "v_mfma_f32_16x16x32_bf16 a[76:79], v[168:171], v[140:143], a[76:79]\n"
    "ds_read_b128 v[176:179], v205 offset:22528\n"
    "ds_read_b128 v[180:183], v205 offset:23552\n"
    "s_waitcnt lgkmcnt(1)\n"
    "v_mfma_f32_16x16x32_bf16 a[92:95], v[176:179], v[140:143], a[92:95]\n"
    "s_waitcnt lgkmcnt(0)\n"
    "v_mfma_f32_16x16x32_bf16 a[92:95], v[180:183], v[156:159], a[92:95]\n"
    "v_mfma_f32_16x16x32_bf16 a[88:91], v[180:183], v[152:155], a[88:91]\n"
    "v_mfma_f32_16x16x32_bf16 a[88:91], v[176:179], v[136:139], a[88:91]\n"
    "v_mfma_f32_16x16x32_bf16 a[84:87], v[176:179], v[132:135], a[84:87]\n"
    "v_mfma_f32_16x16x32_bf16 a[84:87], v[180:183], v[148:151], a[84:87]\n"
    "v_mfma_f32_16x16x32_bf16 a[80:83], v[180:183], v[144:147], a[80:83]\n"
    "v_mfma_f32_16x16x32_bf16 a[80:83], v[176:179], v[128:131], a[80:83]\n"
    "ds_read_b128 v[160:163], v205 offset:24576\n"
    "ds_read_b128 v[164:167], v205 offset:25600\n"
    "s_waitcnt lgkmcnt(1)\n"
    "v_mfma_f32_16x16x32_bf16 a[96:99], v[160:163], v[128:131], a[96:99]\n"
    "s_waitcnt lgkmcnt(0)\n"
    "v_mfma_f32_16x16x32_bf16 a[96:99], v[164:167], v[144:147], a[96:99]\n"
    "v_mfma_f32_16x16x32_bf16 a[100:103], v[164:167], v[148:151], a[100:103]\n"
    "v_mfma_f32_16x16x32_bf16 a[100:103], v[160:163], v[132:135], a[100:103]\n"
    "v_mfma_f32_16x16x32_bf16 a[104:107], v[160:163], v[136:139], a[104:107]\n"
    "v_mfma_f32_16x16x32_bf16 a[104:107], v[164:167], v[152:155], a[104:107]\n"
    "v_mfma_f32_16x16x32_bf16 a[108:111], v[164:167], v[156:159], a[108:111]\n"
    "v_mfma_f32_16x16x32_bf16 a[108:111], v[160:163], v[140:143], a[108:111]\n"
    "ds_read_b128 v[168:171], v205 offset:26624\n"
    "ds_read_b128 v[172:175], v205 offset:27648\n"
    "s_waitcnt lgkmcnt(1)\n"
    "v_mfma_f32_16x16x32_bf16 a[124:127], v[168:171], v[140:143], a[124:127]\n"
    "s_waitcnt lgkmcnt(0)\n"
    "v_mfma_f32_16x16x32_bf16 a[124:127], v[172:175], v[156:159], a[124:127]\n"
    "v_mfma_f32_16x16x32_bf16 a[120:123], v[172:175], v[152:155], a[120:123]\n"
    "v_mfma_f32_16x16x32_bf16 a[120:123], v[168:171], v[136:139], a[120:123]\n"
    "v_mfma_f32_16x16x32_bf16 a[116:119], v[168:171], v[132:135], a[116:119]\n"
    "v_mfma_f32_16x16x32_bf16 a[116:119], v[172:175], v[148:151], a[116:119]\n"
    "v_mfma_f32_16x16x32_bf16 a[112:115], v[172:175], v[144:147], a[112:115]\n"
    "v_mfma_f32_16x16x32_bf16 a[112:115], v[168:171], v[128:131], a[112:115]\n"
    "ds_read_b128 v[176:179], v205 offset:28672\n"
    "ds_read_b128 v[180:183], v205 offset:29696\n"
    "s_waitcnt lgkmcnt(1)\n"
    "v_mfma_f32_16x16x32_bf16 a[128:131], v[176:179], v[128:131], a[128:131]\n"
    "s_waitcnt lgkmcnt(0)\n"
    "v_mfma_f32_16x16x32_bf16 a[128:131], v[180:183], v[144:147], a[128:131]\n"
    "v_mfma_f32_16x16x32_bf16 a[132:135], v[180:183], v[148:151], a[132:135]\n"
    "v_mfma_f32_16x16x32_bf16 a[132:135], v[176:179], v[132:135], a[132:135]\n"
    "v_mfma_f32_16x16x32_bf16 a[136:139], v[176:179], v[136:139], a[136:139]\n"
    "v_mfma_f32_16x16x32_bf16 a[136:139], v[180:183], v[152:155], a[136:139]\n"
    "v_mfma_f32_16x16x32_bf16 a[140:143], v[180:183], v[156:159], a[140:143]\n"
    "v_mfma_f32_16x16x32_bf16 a[140:143], v[176:179], v[140:143], a[140:143]\n"
    "ds_read_b128 v[160:163], v205 offset:30720\n"
    "ds_read_b128 v[164:167], v205 offset:31744\n"
    "s_waitcnt lgkmcnt(1)\n"
    "v_mfma_f32_16x16x32_bf16 a[156:159], v[160:163], v[140:143], a[156:159]\n"
    "s_waitcnt lgkmcnt(0)\n"
    "v_mfma_f32_16x16x32_bf16 a[156:159], v[164:167], v[156:159], a[156:159]\n"
    "v_mfma_f32_16x16x32_bf16 a[152:155], v[164:167], v[152:155], a[152:155]\n"
    "v_mfma_f32_16x16x32_bf16 a[152:155], v[160:163], v[136:139], a[152:155]\n"
    "v_mfma_f32_16x16x32_bf16 a[148:151], v[160:163], v[132:135], a[148:151]\n"
    "v_mfma_f32_16x16x32_bf16 a[148:151], v[164:167], v[148:151], a[148:151]\n"
    "v_mfma_f32_16x16x32_bf16 a[144:147], v[164:167], v[144:147], a[144:147]\n"
    "v_mfma_f32_16x16x32_bf16 a[144:147], v[160:163], v[128:131], a[144:147]\n"
    "ds_read_b128 v[168:171], v205 offset:32768\n"
    "ds_read_b128 v[172:175], v205 offset:33792\n"
    "s_waitcnt lgkmcnt(1)\n"
    "v_mfma_f32_16x16x32_bf16 a[160:163], v[168:171], v[128:131], a[160:163]\n"
    "s_waitcnt lgkmcnt(0)\n"
    "v_mfma_f32_16x16x32_bf16 a[160:163], v[172:175], v[144:147], a[160:163]\n"
    "v_mfma_f32_16x16x32_bf16 a[164:167], v[172:175], v[148:151], a[164:167]\n"
    "v_mfma_f32_16x16x32_bf16 a[164:167], v[168:171], v[132:135], a[164:167]\n"
    "v_mfma_f32_16x16x32_bf16 a[168:171], v[168:171], v[136:139], a[168:171]\n"
    "v_mfma_f32_16x16x32_bf16 a[168:171], v[172:175], v[152:155], a[168:171]\n"
    "v_mfma_f32_16x16x32_bf16 a[172:175], v[172:175], v[156:159], a[172:175]\n"
    "v_mfma_f32_16x16x32_bf16 a[172:175], v[168:171], v[140:143], a[172:175]\n"
    "ds_read_b128 v[176:179], v205 offset:34816\n"
    "ds_read_b128 v[180:183], v205 offset:35840\n"
    "s_waitcnt lgkmcnt(1)\n"
    "v_mfma_f32_16x16x32_bf16 a[188:191], v[176:179], v[140:143], a[188:191]\n"
    "s_waitcnt lgkmcnt(0)\n"
    "v_mfma_f32_16x16x32_bf16 a[188:191], v[180:183], v[156:159], a[188:191]\n"
    "v_mfma_f32_16x16x32_bf16 a[184:187], v[176:179], v[136:139], a[184:187]\n"
    "v_mfma_f32_16x16x32_bf16 a[184:187], v[180:183], v[152:155], a[184:187]\n"
    "v_mfma_f32_16x16x32_bf16 a[180:183], v[176:179], v[132:135], a[180:183]\n"
    "v_mfma_f32_16x16x32_bf16 a[180:183], v[180:183], v[148:151], a[180:183]\n"
    "v_mfma_f32_16x16x32_bf16 a[176:179], v[176:179], v[128:131], a[176:179]\n"
    "v_mfma_f32_16x16x32_bf16 a[176:179], v[180:183], v[144:147], a[176:179]\n"
    "v_mfma_f32_16x16x32_bf16 v[112:115], v[192:195], v[128:131], v[112:115]\n"
    "v_mfma_f32_16x16x32_bf16 v[112:115], v[192:195], v[144:147], v[112:115]\n"
    "v_mfma_f32_16x16x32_bf16 v[116:119], v[192:195], v[132:135], v[116:119]\n"
    "v_mfma_f32_16x16x32_bf16 v[116:119], v[192:195], v[148:151], v[116:119]\n"
    "v_mfma_f32_16x16x32_bf16 v[120:123], v[192:195], v[136:139], v[120:123]\n"
    "v_mfma_f32_16x16x32_bf16 v[120:123], v[192:195], v[152:155], v[120:123]\n"
    "v_mfma_f32_16x16x32_bf16 v[124:127], v[192:195], v[140:143], v[124:127]\n"
    "v_mfma_f32_16x16x32_bf16 v[124:127], v[192:195], v[156:159], v[124:127]\n"
    "s_nop 15\n"
    "s_add_u32 s63, s63, 0x6000\n"
    "s_cmp_lt_u32 s63, 0xc0000\n"
    "s_cbranch_scc1 LSLOOP\n"
    "s_branch LEPI\n"
    "LEPI:\n"
    "s_lshl_b32 s76, s56, 3\n"
    "s_add_u32 s76, s76, s57\n"
    "s_mul_i32 s76, s76, 0x18000\n"
    "s_add_u32 s76, s76, s88\n"
    "s_add_u32 s28, s24, s76\n"
    "s_addc_u32 s29, s25, 0\n"
    "s_and_b32 s29, s29, 0xffff\n"
    "s_mov_b32 s30, 0x6000\n"
    "s_mov_b32 s31, 0x20000\n"
    "s_xor_b32 s94, s70, 0x80000000\n"
    "s_xor_b32 s95, s70, 0x80000000\n"
    "s_mov_b32 s62, 0x1800\n"
    "s_mov_b32 s63, 0x3000\n"
    "s_mov_b32 s64, 0x4800\n"
    "v_and_b32 v198, 15, v231\n"
    "v_mul_u32_u24 v205, 0x180, v198\n"
    "v_lshrrev_b32 v199, 4, v231\n"
    "v_lshl_add_u32 v205, v199, 4, v205\n"
    "v_mul_f32 v222, s68, v112\n"
    "v_rcp_f32 v222, v222\n"
    "v_mul_f32 v224, s68, v116\n"
    "v_rcp_f32 v224, v224\n"
    "v_mul_f32 v226, s68, v120\n"
    "v_rcp_f32 v226, v226\n"
    "v_mul_f32 v228, s68, v124\n"
    "v_rcp_f32 v228, v228\n"
    "v_mov_b32 v223, v222\n"
    "v_mov_b32 v225, v224\n"
    "v_mov_b32 v227, v226\n"
    "v_mov_b32 v229, v228\n"
    "v_accvgpr_read_b32 v96, a0\n"
    "v_accvgpr_read_b32 v97, a1\n"
    "v_accvgpr_read_b32 v98, a2\n"
    "v_accvgpr_read_b32 v99, a3\n"
    "v_pk_fma_f32 v[96:97], v[96:97], v[222:223], s[94:95]\n"
    "v_pk_fma_f32 v[98:99], v[98:99], v[222:223], s[94:95]\n"
    "v_cvt_pk_bf16_f32 v32, v96, v97\n"
    "v_cvt_pk_bf16_f32 v33, v98, v99\n"
    "v_accvgpr_read_b32 v100, a16\n"
    "v_accvgpr_read_b32 v101, a17\n"
    "v_accvgpr_read_b32 v102, a18\n"
    "v_accvgpr_read_b32 v103, a19\n"
    "v_pk_fma_f32 v[100:101], v[100:101], v[222:223], s[94:95]\n"
    "v_pk_fma_f32 v[102:103], v[102:103], v[222:223], s[94:95]\n"
    "v_cvt_pk_bf16_f32 v34, v100, v101\n"
    "v_cvt_pk_bf16_f32 v35, v102, v103\n"
    "buffer_store_dwordx4 v[32:35], v205, s[28:31], 0 offen sc0 sc1\n"
    "v_accvgpr_read_b32 v104, a32\n"
    "v_accvgpr_read_b32 v105, a33\n"
    "v_accvgpr_read_b32 v106, a34\n"
    "v_accvgpr_read_b32 v107, a35\n"
    "v_pk_fma_f32 v[104:105], v[104:105], v[222:223], s[94:95]\n"
    "v_pk_fma_f32 v[106:107], v[106:107], v[222:223], s[94:95]\n"
    "v_cvt_pk_bf16_f32 v36, v104, v105\n"
    "v_cvt_pk_bf16_f32 v37, v106, v107\n"
    "v_accvgpr_read_b32 v108, a48\n"
    "v_accvgpr_read_b32 v109, a49\n"
    "v_accvgpr_read_b32 v110, a50\n"
    "v_accvgpr_read_b32 v111, a51\n"
    "v_pk_fma_f32 v[108:109], v[108:109], v[222:223], s[94:95]\n"
    "v_pk_fma_f32 v[110:111], v[110:111], v[222:223], s[94:95]\n"
    "v_cvt_pk_bf16_f32 v38, v108, v109\n"
    "v_cvt_pk_bf16_f32 v39, v110, v111\n"
    "buffer_store_dwordx4 v[36:39], v205, s[28:31], 0 offen offset:64 sc0 sc1\n"
    "v_accvgpr_read_b32 v96, a64\n"
    "v_accvgpr_read_b32 v97, a65\n"
    "v_accvgpr_read_b32 v98, a66\n"
    "v_accvgpr_read_b32 v99, a67\n"
    "v_pk_fma_f32 v[96:97], v[96:97], v[222:223], s[94:95]\n"
    "v_pk_fma_f32 v[98:99], v[98:99], v[222:223], s[94:95]\n"
    "v_cvt_pk_bf16_f32 v40, v96, v97\n"
    "v_cvt_pk_bf16_f32 v41, v98, v99\n"
    "v_accvgpr_read_b32 v100, a80\n"
    "v_accvgpr_read_b32 v101, a81\n"
    "v_accvgpr_read_b32 v102, a82\n"
    "v_accvgpr_read_b32 v103, a83\n"
    "v_pk_fma_f32 v[100:101], v[100:101], v[222:223], s[94:95]\n"
    "v_pk_fma_f32 v[102:103], v[102:103], v[222:223], s[94:95]\n"
    "v_cvt_pk_bf16_f32 v42, v100, v101\n"
    "v_cvt_pk_bf16_f32 v43, v102, v103\n"
    "buffer_store_dwordx4 v[40:43], v205, s[28:31], 0 offen offset:128 sc0 sc1\n"
    "v_accvgpr_read_b32 v104, a96\n"
    "v_accvgpr_read_b32 v105, a97\n"
    "v_accvgpr_read_b32 v106, a98\n"
    "v_accvgpr_read_b32 v107, a99\n"
    "v_pk_fma_f32 v[104:105], v[104:105], v[222:223], s[94:95]\n"
    "v_pk_fma_f32 v[106:107], v[106:107], v[222:223], s[94:95]\n"
    "v_cvt_pk_bf16_f32 v44, v104, v105\n"
    "v_cvt_pk_bf16_f32 v45, v106, v107\n"
    "v_accvgpr_read_b32 v108, a112\n"
    "v_accvgpr_read_b32 v109, a113\n"
    "v_accvgpr_read_b32 v110, a114\n"
    "v_accvgpr_read_b32 v111, a115\n"
    "v_pk_fma_f32 v[108:109], v[108:109], v[222:223], s[94:95]\n"
    "v_pk_fma_f32 v[110:111], v[110:111], v[222:223], s[94:95]\n"
    "v_cvt_pk_bf16_f32 v46, v108, v109\n"
    "v_cvt_pk_bf16_f32 v47, v110, v111\n"
    "buffer_store_dwordx4 v[44:47], v205, s[28:31], 0 offen offset:192 sc0 sc1\n"
    "v_accvgpr_read_b32 v96, a128\n"
    "v_accvgpr_read_b32 v97, a129\n"
    "v_accvgpr_read_b32 v98, a130\n"
    "v_accvgpr_read_b32 v99, a131\n"
    "v_pk_fma_f32 v[96:97], v[96:97], v[222:223], s[94:95]\n"
    "v_pk_fma_f32 v[98:99], v[98:99], v[222:223], s[94:95]\n"
    "v_cvt_pk_bf16_f32 v48, v96, v97\n"
    "v_cvt_pk_bf16_f32 v49, v98, v99\n"
    "v_accvgpr_read_b32 v100, a144\n"
    "v_accvgpr_read_b32 v101, a145\n"
    "v_accvgpr_read_b32 v102, a146\n"
    "v_accvgpr_read_b32 v103, a147\n"
    "v_pk_fma_f32 v[100:101], v[100:101], v[222:223], s[94:95]\n"
    "v_pk_fma_f32 v[102:103], v[102:103], v[222:223], s[94:95]\n"
    "v_cvt_pk_bf16_f32 v50, v100, v101\n"
    "v_cvt_pk_bf16_f32 v51, v102, v103\n"
    "buffer_store_dwordx4 v[48:51], v205, s[28:31], 0 offen offset:256 sc0 sc1\n"
    "v_accvgpr_read_b32 v104, a160\n"
    "v_accvgpr_read_b32 v105, a161\n"
    "v_accvgpr_read_b32 v106, a162\n"
    "v_accvgpr_read_b32 v107, a163\n"
    "v_pk_fma_f32 v[104:105], v[104:105], v[222:223], s[94:95]\n"
    "v_pk_fma_f32 v[106:107], v[106:107], v[222:223], s[94:95]\n"
    "v_cvt_pk_bf16_f32 v52, v104, v105\n"
    "v_cvt_pk_bf16_f32 v53, v106, v107\n"
    "v_accvgpr_read_b32 v108, a176\n"
    "v_accvgpr_read_b32 v109, a177\n"
    "v_accvgpr_read_b32 v110, a178\n"
    "v_accvgpr_read_b32 v111, a179\n"
    "v_pk_fma_f32 v[108:109], v[108:109], v[222:223], s[94:95]\n"
    "v_pk_fma_f32 v[110:111], v[110:111], v[222:223], s[94:95]\n"
    "v_cvt_pk_bf16_f32 v54, v108, v109\n"
    "v_cvt_pk_bf16_f32 v55, v110, v111\n"
    "buffer_store_dwordx4 v[52:55], v205, s[28:31], 0 offen offset:320 sc0 sc1\n"
    "v_accvgpr_read_b32 v96, a4\n"
    "v_accvgpr_read_b32 v97, a5\n"
    "v_accvgpr_read_b32 v98, a6\n"
    "v_accvgpr_read_b32 v99, a7\n"
    "v_pk_fma_f32 v[96:97], v[96:97], v[224:225], s[94:95]\n"
    "v_pk_fma_f32 v[98:99], v[98:99], v[224:225], s[94:95]\n"
    "v_cvt_pk_bf16_f32 v56, v96, v97\n"
    "v_cvt_pk_bf16_f32 v57, v98, v99\n"
    "v_accvgpr_read_b32 v100, a20\n"
    "v_accvgpr_read_b32 v101, a21\n"
    "v_accvgpr_read_b32 v102, a22\n"
    "v_accvgpr_read_b32 v103, a23\n"
    "v_pk_fma_f32 v[100:101], v[100:101], v[224:225], s[94:95]\n"
    "v_pk_fma_f32 v[102:103], v[102:103], v[224:225], s[94:95]\n"
    "v_cvt_pk_bf16_f32 v58, v100, v101\n"
    "v_cvt_pk_bf16_f32 v59, v102, v103\n"
    "buffer_store_dwordx4 v[56:59], v205, s[28:31], s62 offen sc0 sc1\n"
    "v_accvgpr_read_b32 v104, a36\n"
    "v_accvgpr_read_b32 v105, a37\n"
    "v_accvgpr_read_b32 v106, a38\n"
    "v_accvgpr_read_b32 v107, a39\n"
    "v_pk_fma_f32 v[104:105], v[104:105], v[224:225], s[94:95]\n"
    "v_pk_fma_f32 v[106:107], v[106:107], v[224:225], s[94:95]\n"
    "v_cvt_pk_bf16_f32 v60, v104, v105\n"
    "v_cvt_pk_bf16_f32 v61, v106, v107\n"
    "v_accvgpr_read_b32 v108, a52\n"
    "v_accvgpr_read_b32 v109, a53\n"
    "v_accvgpr_read_b32 v110, a54\n"
    "v_accvgpr_read_b32 v111, a55\n"
    "v_pk_fma_f32 v[108:109], v[108:109], v[224:225], s[94:95]\n"
    "v_pk_fma_f32 v[110:111], v[110:111], v[224:225], s[94:95]\n"
    "v_cvt_pk_bf16_f32 v62, v108, v109\n"
    "v_cvt_pk_bf16_f32 v63, v110, v111\n"
    "buffer_store_dwordx4 v[60:63], v205, s[28:31], s62 offen offset:64 sc0 sc1\n"
    "v_accvgpr_read_b32 v96, a68\n"
    "v_accvgpr_read_b32 v97, a69\n"
    "v_accvgpr_read_b32 v98, a70\n"
    "v_accvgpr_read_b32 v99, a71\n"
    "v_pk_fma_f32 v[96:97], v[96:97], v[224:225], s[94:95]\n"
    "v_pk_fma_f32 v[98:99], v[98:99], v[224:225], s[94:95]\n"
    "v_cvt_pk_bf16_f32 v32, v96, v97\n"
    "v_cvt_pk_bf16_f32 v33, v98, v99\n"
    "v_accvgpr_read_b32 v100, a84\n"
    "v_accvgpr_read_b32 v101, a85\n"
    "v_accvgpr_read_b32 v102, a86\n"
    "v_accvgpr_read_b32 v103, a87\n"
    "v_pk_fma_f32 v[100:101], v[100:101], v[224:225], s[94:95]\n"
    "v_pk_fma_f32 v[102:103], v[102:103], v[224:225], s[94:95]\n"
    "v_cvt_pk_bf16_f32 v34, v100, v101\n"
    "v_cvt_pk_bf16_f32 v35, v102, v103\n"
    "buffer_store_dwordx4 v[32:35], v205, s[28:31], s62 offen offset:128 sc0 sc1\n"
    "v_accvgpr_read_b32 v104, a100\n"
    "v_accvgpr_read_b32 v105, a101\n"
    "v_accvgpr_read_b32 v106, a102\n"
    "v_accvgpr_read_b32 v107, a103\n"
    "v_pk_fma_f32 v[104:105], v[104:105], v[224:225], s[94:95]\n"
    "v_pk_fma_f32 v[106:107], v[106:107], v[224:225], s[94:95]\n"
    "v_cvt_pk_bf16_f32 v36, v104, v105\n"
    "v_cvt_pk_bf16_f32 v37, v106, v107\n"
    "v_accvgpr_read_b32 v108, a116\n"
    "v_accvgpr_read_b32 v109, a117\n"
    "v_accvgpr_read_b32 v110, a118\n"
    "v_accvgpr_read_b32 v111, a119\n"
    "v_pk_fma_f32 v[108:109], v[108:109], v[224:225], s[94:95]\n"
    "v_pk_fma_f32 v[110:111], v[110:111], v[224:225], s[94:95]\n"
    "v_cvt_pk_bf16_f32 v38, v108, v109\n"
    "v_cvt_pk_bf16_f32 v39, v110, v111\n"
    "buffer_store_dwordx4 v[36:39], v205, s[28:31], s62 offen offset:192 sc0 sc1\n"
    "v_accvgpr_read_b32 v96, a132\n"
    "v_accvgpr_read_b32 v97, a133\n"
    "v_accvgpr_read_b32 v98, a134\n"
    "v_accvgpr_read_b32 v99, a135\n"
    "v_pk_fma_f32 v[96:97], v[96:97], v[224:225], s[94:95]\n"
    "v_pk_fma_f32 v[98:99], v[98:99], v[224:225], s[94:95]\n"
    "v_cvt_pk_bf16_f32 v40, v96, v97\n"
    "v_cvt_pk_bf16_f32 v41, v98, v99\n"
    "v_accvgpr_read_b32 v100, a148\n"
    "v_accvgpr_read_b32 v101, a149\n"
    "v_accvgpr_read_b32 v102, a150\n"
    "v_accvgpr_read_b32 v103, a151\n"
    "v_pk_fma_f32 v[100:101], v[100:101], v[224:225], s[94:95]\n"
    "v_pk_fma_f32 v[102:103], v[102:103], v[224:225], s[94:95]\n"
    "v_cvt_pk_bf16_f32 v42, v100, v101\n"
    "v_cvt_pk_bf16_f32 v43, v102, v103\n"
    "buffer_store_dwordx4 v[40:43], v205, s[28:31], s62 offen offset:256 sc0 sc1\n"
    "v_accvgpr_read_b32 v104, a164\n"
    "v_accvgpr_read_b32 v105, a165\n"
    "v_accvgpr_read_b32 v106, a166\n"
    "v_accvgpr_read_b32 v107, a167\n"
    "v_pk_fma_f32 v[104:105], v[104:105], v[224:225], s[94:95]\n"
    "v_pk_fma_f32 v[106:107], v[106:107], v[224:225], s[94:95]\n"
    "v_cvt_pk_bf16_f32 v44, v104, v105\n"
    "v_cvt_pk_bf16_f32 v45, v106, v107\n"
    "v_accvgpr_read_b32 v108, a180\n"
    "v_accvgpr_read_b32 v109, a181\n"
    "v_accvgpr_read_b32 v110, a182\n"
    "v_accvgpr_read_b32 v111, a183\n"
    "v_pk_fma_f32 v[108:109], v[108:109], v[224:225], s[94:95]\n"
    "v_pk_fma_f32 v[110:111], v[110:111], v[224:225], s[94:95]\n"
    "v_cvt_pk_bf16_f32 v46, v108, v109\n"
    "v_cvt_pk_bf16_f32 v47, v110, v111\n"
    "buffer_store_dwordx4 v[44:47], v205, s[28:31], s62 offen offset:320 sc0 sc1\n"
    "v_accvgpr_read_b32 v96, a8\n"
    "v_accvgpr_read_b32 v97, a9\n"
    "v_accvgpr_read_b32 v98, a10\n"
    "v_accvgpr_read_b32 v99, a11\n"
    "v_pk_fma_f32 v[96:97], v[96:97], v[226:227], s[94:95]\n"
    "v_pk_fma_f32 v[98:99], v[98:99], v[226:227], s[94:95]\n"
    "v_cvt_pk_bf16_f32 v48, v96, v97\n"
    "v_cvt_pk_bf16_f32 v49, v98, v99\n"
    "v_accvgpr_read_b32 v100, a24\n"
    "v_accvgpr_read_b32 v101, a25\n"
    "v_accvgpr_read_b32 v102, a26\n"
    "v_accvgpr_read_b32 v103, a27\n"
    "v_pk_fma_f32 v[100:101], v[100:101], v[226:227], s[94:95]\n"
    "v_pk_fma_f32 v[102:103], v[102:103], v[226:227], s[94:95]\n"
    "v_cvt_pk_bf16_f32 v50, v100, v101\n"
    "v_cvt_pk_bf16_f32 v51, v102, v103\n"
    "buffer_store_dwordx4 v[48:51], v205, s[28:31], s63 offen sc0 sc1\n"
    "v_accvgpr_read_b32 v104, a40\n"
    "v_accvgpr_read_b32 v105, a41\n"
    "v_accvgpr_read_b32 v106, a42\n"
    "v_accvgpr_read_b32 v107, a43\n"
    "v_pk_fma_f32 v[104:105], v[104:105], v[226:227], s[94:95]\n"
    "v_pk_fma_f32 v[106:107], v[106:107], v[226:227], s[94:95]\n"
    "v_cvt_pk_bf16_f32 v52, v104, v105\n"
    "v_cvt_pk_bf16_f32 v53, v106, v107\n"
    "v_accvgpr_read_b32 v108, a56\n"
    "v_accvgpr_read_b32 v109, a57\n"
    "v_accvgpr_read_b32 v110, a58\n"
    "v_accvgpr_read_b32 v111, a59\n"
    "v_pk_fma_f32 v[108:109], v[108:109], v[226:227], s[94:95]\n"
    "v_pk_fma_f32 v[110:111], v[110:111], v[226:227], s[94:95]\n"
    "v_cvt_pk_bf16_f32 v54, v108, v109\n"
    "v_cvt_pk_bf16_f32 v55, v110, v111\n"
    "buffer_store_dwordx4 v[52:55], v205, s[28:31], s63 offen offset:64 sc0 sc1\n"
    "v_accvgpr_read_b32 v96, a72\n"
    "v_accvgpr_read_b32 v97, a73\n"
    "v_accvgpr_read_b32 v98, a74\n"
    "v_accvgpr_read_b32 v99, a75\n"
    "v_pk_fma_f32 v[96:97], v[96:97], v[226:227], s[94:95]\n"
    "v_pk_fma_f32 v[98:99], v[98:99], v[226:227], s[94:95]\n"
    "v_cvt_pk_bf16_f32 v56, v96, v97\n"
    "v_cvt_pk_bf16_f32 v57, v98, v99\n"
    "v_accvgpr_read_b32 v100, a88\n"
    "v_accvgpr_read_b32 v101, a89\n"
    "v_accvgpr_read_b32 v102, a90\n"
    "v_accvgpr_read_b32 v103, a91\n"
    "v_pk_fma_f32 v[100:101], v[100:101], v[226:227], s[94:95]\n"
    "v_pk_fma_f32 v[102:103], v[102:103], v[226:227], s[94:95]\n"
    "v_cvt_pk_bf16_f32 v58, v100, v101\n"
    "v_cvt_pk_bf16_f32 v59, v102, v103\n"
    "buffer_store_dwordx4 v[56:59], v205, s[28:31], s63 offen offset:128 sc0 sc1\n"
    "v_accvgpr_read_b32 v104, a104\n"
    "v_accvgpr_read_b32 v105, a105\n"
    "v_accvgpr_read_b32 v106, a106\n"
    "v_accvgpr_read_b32 v107, a107\n"
    "v_pk_fma_f32 v[104:105], v[104:105], v[226:227], s[94:95]\n"
    "v_pk_fma_f32 v[106:107], v[106:107], v[226:227], s[94:95]\n"
    "v_cvt_pk_bf16_f32 v60, v104, v105\n"
    "v_cvt_pk_bf16_f32 v61, v106, v107\n"
    "v_accvgpr_read_b32 v108, a120\n"
    "v_accvgpr_read_b32 v109, a121\n"
    "v_accvgpr_read_b32 v110, a122\n"
    "v_accvgpr_read_b32 v111, a123\n"
    "v_pk_fma_f32 v[108:109], v[108:109], v[226:227], s[94:95]\n"
    "v_pk_fma_f32 v[110:111], v[110:111], v[226:227], s[94:95]\n"
    "v_cvt_pk_bf16_f32 v62, v108, v109\n"
    "v_cvt_pk_bf16_f32 v63, v110, v111\n"
    "buffer_store_dwordx4 v[60:63], v205, s[28:31], s63 offen offset:192 sc0 sc1\n"
    "v_accvgpr_read_b32 v96, a136\n"
    "v_accvgpr_read_b32 v97, a137\n"
    "v_accvgpr_read_b32 v98, a138\n"
    "v_accvgpr_read_b32 v99, a139\n"
    "v_pk_fma_f32 v[96:97], v[96:97], v[226:227], s[94:95]\n"
    "v_pk_fma_f32 v[98:99], v[98:99], v[226:227], s[94:95]\n"
    "v_cvt_pk_bf16_f32 v32, v96, v97\n"
    "v_cvt_pk_bf16_f32 v33, v98, v99\n"
    "v_accvgpr_read_b32 v100, a152\n"
    "v_accvgpr_read_b32 v101, a153\n"
    "v_accvgpr_read_b32 v102, a154\n"
    "v_accvgpr_read_b32 v103, a155\n"
    "v_pk_fma_f32 v[100:101], v[100:101], v[226:227], s[94:95]\n"
    "v_pk_fma_f32 v[102:103], v[102:103], v[226:227], s[94:95]\n"
    "v_cvt_pk_bf16_f32 v34, v100, v101\n"
    "v_cvt_pk_bf16_f32 v35, v102, v103\n"
    "buffer_store_dwordx4 v[32:35], v205, s[28:31], s63 offen offset:256 sc0 sc1\n"
    "v_accvgpr_read_b32 v104, a168\n"
    "v_accvgpr_read_b32 v105, a169\n"
    "v_accvgpr_read_b32 v106, a170\n"
    "v_accvgpr_read_b32 v107, a171\n"
    "v_pk_fma_f32 v[104:105], v[104:105], v[226:227], s[94:95]\n"
    "v_pk_fma_f32 v[106:107], v[106:107], v[226:227], s[94:95]\n"
    "v_cvt_pk_bf16_f32 v36, v104, v105\n"
    "v_cvt_pk_bf16_f32 v37, v106, v107\n"
    "v_accvgpr_read_b32 v108, a184\n"
    "v_accvgpr_read_b32 v109, a185\n"
    "v_accvgpr_read_b32 v110, a186\n"
    "v_accvgpr_read_b32 v111, a187\n"
    "v_pk_fma_f32 v[108:109], v[108:109], v[226:227], s[94:95]\n"
    "v_pk_fma_f32 v[110:111], v[110:111], v[226:227], s[94:95]\n"
    "v_cvt_pk_bf16_f32 v38, v108, v109\n"
    "v_cvt_pk_bf16_f32 v39, v110, v111\n"
    "buffer_store_dwordx4 v[36:39], v205, s[28:31], s63 offen offset:320 sc0 sc1\n"
    "v_accvgpr_read_b32 v96, a12\n"
    "v_accvgpr_read_b32 v97, a13\n"
    "v_accvgpr_read_b32 v98, a14\n"
    "v_accvgpr_read_b32 v99, a15\n"
    "v_pk_fma_f32 v[96:97], v[96:97], v[228:229], s[94:95]\n"
    "v_pk_fma_f32 v[98:99], v[98:99], v[228:229], s[94:95]\n"
    "v_cvt_pk_bf16_f32 v40, v96, v97\n"
    "v_cvt_pk_bf16_f32 v41, v98, v99\n"
    "v_accvgpr_read_b32 v100, a28\n"
    "v_accvgpr_read_b32 v101, a29\n"
    "v_accvgpr_read_b32 v102, a30\n"
    "v_accvgpr_read_b32 v103, a31\n"
    "v_pk_fma_f32 v[100:101], v[100:101], v[228:229], s[94:95]\n"
    "v_pk_fma_f32 v[102:103], v[102:103], v[228:229], s[94:95]\n"
    "v_cvt_pk_bf16_f32 v42, v100, v101\n"
    "v_cvt_pk_bf16_f32 v43, v102, v103\n"
    "buffer_store_dwordx4 v[40:43], v205, s[28:31], s64 offen sc0 sc1\n"
    "v_accvgpr_read_b32 v104, a44\n"
    "v_accvgpr_read_b32 v105, a45\n"
    "v_accvgpr_read_b32 v106, a46\n"
    "v_accvgpr_read_b32 v107, a47\n"
    "v_pk_fma_f32 v[104:105], v[104:105], v[228:229], s[94:95]\n"
    "v_pk_fma_f32 v[106:107], v[106:107], v[228:229], s[94:95]\n"
    "v_cvt_pk_bf16_f32 v44, v104, v105\n"
    "v_cvt_pk_bf16_f32 v45, v106, v107\n"
    "v_accvgpr_read_b32 v108, a60\n"
    "v_accvgpr_read_b32 v109, a61\n"
    "v_accvgpr_read_b32 v110, a62\n"
    "v_accvgpr_read_b32 v111, a63\n"
    "v_pk_fma_f32 v[108:109], v[108:109], v[228:229], s[94:95]\n"
    "v_pk_fma_f32 v[110:111], v[110:111], v[228:229], s[94:95]\n"
    "v_cvt_pk_bf16_f32 v46, v108, v109\n"
    "v_cvt_pk_bf16_f32 v47, v110, v111\n"
    "buffer_store_dwordx4 v[44:47], v205, s[28:31], s64 offen offset:64 sc0 sc1\n"
    "v_accvgpr_read_b32 v96, a76\n"
    "v_accvgpr_read_b32 v97, a77\n"
    "v_accvgpr_read_b32 v98, a78\n"
    "v_accvgpr_read_b32 v99, a79\n"
    "v_pk_fma_f32 v[96:97], v[96:97], v[228:229], s[94:95]\n"
    "v_pk_fma_f32 v[98:99], v[98:99], v[228:229], s[94:95]\n"
    "v_cvt_pk_bf16_f32 v48, v96, v97\n"
    "v_cvt_pk_bf16_f32 v49, v98, v99\n"
    "v_accvgpr_read_b32 v100, a92\n"
    "v_accvgpr_read_b32 v101, a93\n"
    "v_accvgpr_read_b32 v102, a94\n"
    "v_accvgpr_read_b32 v103, a95\n"
    "v_pk_fma_f32 v[100:101], v[100:101], v[228:229], s[94:95]\n"
    "v_pk_fma_f32 v[102:103], v[102:103], v[228:229], s[94:95]\n"
    "v_cvt_pk_bf16_f32 v50, v100, v101\n"
    "v_cvt_pk_bf16_f32 v51, v102, v103\n"
    "buffer_store_dwordx4 v[48:51], v205, s[28:31], s64 offen offset:128 sc0 sc1\n"
    "v_accvgpr_read_b32 v104, a108\n"
    "v_accvgpr_read_b32 v105, a109\n"
    "v_accvgpr_read_b32 v106, a110\n"
    "v_accvgpr_read_b32 v107, a111\n"
    "v_pk_fma_f32 v[104:105], v[104:105], v[228:229], s[94:95]\n"
    "v_pk_fma_f32 v[106:107], v[106:107], v[228:229], s[94:95]\n"
    "v_cvt_pk_bf16_f32 v52, v104, v105\n"
    "v_cvt_pk_bf16_f32 v53, v106, v107\n"
    "v_accvgpr_read_b32 v108, a124\n"
    "v_accvgpr_read_b32 v109, a125\n"
    "v_accvgpr_read_b32 v110, a126\n"
    "v_accvgpr_read_b32 v111, a127\n"
    "v_pk_fma_f32 v[108:109], v[108:109], v[228:229], s[94:95]\n"
    "v_pk_fma_f32 v[110:111], v[110:111], v[228:229], s[94:95]\n"
    "v_cvt_pk_bf16_f32 v54, v108, v109\n"
    "v_cvt_pk_bf16_f32 v55, v110, v111\n"
    "buffer_store_dwordx4 v[52:55], v205, s[28:31], s64 offen offset:192 sc0 sc1\n"
    "v_accvgpr_read_b32 v96, a140\n"
    "v_accvgpr_read_b32 v97, a141\n"
    "v_accvgpr_read_b32 v98, a142\n"
    "v_accvgpr_read_b32 v99, a143\n"
    "v_pk_fma_f32 v[96:97], v[96:97], v[228:229], s[94:95]\n"
    "v_pk_fma_f32 v[98:99], v[98:99], v[228:229], s[94:95]\n"
    "v_cvt_pk_bf16_f32 v56, v96, v97\n"
    "v_cvt_pk_bf16_f32 v57, v98, v99\n"
    "v_accvgpr_read_b32 v100, a156\n"
    "v_accvgpr_read_b32 v101, a157\n"
    "v_accvgpr_read_b32 v102, a158\n"
    "v_accvgpr_read_b32 v103, a159\n"
    "v_pk_fma_f32 v[100:101], v[100:101], v[228:229], s[94:95]\n"
    "v_pk_fma_f32 v[102:103], v[102:103], v[228:229], s[94:95]\n"
    "v_cvt_pk_bf16_f32 v58, v100, v101\n"
    "v_cvt_pk_bf16_f32 v59, v102, v103\n"
    "buffer_store_dwordx4 v[56:59], v205, s[28:31], s64 offen offset:256 sc0 sc1\n"
    "v_accvgpr_read_b32 v104, a172\n"
    "v_accvgpr_read_b32 v105, a173\n"
    "v_accvgpr_read_b32 v106, a174\n"
    "v_accvgpr_read_b32 v107, a175\n"
    "v_pk_fma_f32 v[104:105], v[104:105], v[228:229], s[94:95]\n"
    "v_pk_fma_f32 v[106:107], v[106:107], v[228:229], s[94:95]\n"
    "v_cvt_pk_bf16_f32 v60, v104, v105\n"
    "v_cvt_pk_bf16_f32 v61, v106, v107\n"
    "v_accvgpr_read_b32 v108, a188\n"
    "v_accvgpr_read_b32 v109, a189\n"
    "v_accvgpr_read_b32 v110, a190\n"
    "v_accvgpr_read_b32 v111, a191\n"
    "v_pk_fma_f32 v[108:109], v[108:109], v[228:229], s[94:95]\n"
    "v_pk_fma_f32 v[110:111], v[110:111], v[228:229], s[94:95]\n"
    "v_cvt_pk_bf16_f32 v62, v108, v109\n"
    "v_cvt_pk_bf16_f32 v63, v110, v111\n"
    "buffer_store_dwordx4 v[60:63], v205, s[28:31], s64 offen offset:320 sc0 sc1\n"
    "s_branch LFIN\n"
    "LEND:\n"
    :
    : "s"(__builtin_amdgcn_kernarg_segment_ptr()), "s"(blockIdx.x), "s"(wid)
    : "v0", "v1", "v2", "v3", "v4", "v5", "v6", "v7", "v8", "v9", "v10", "v11", "v12", "v13", "v14", "v15", "v16", "v17", "v18", "v19", "v20", "v21", "v22", "v23", "v24", "v25", "v26", "v27", "v28", "v29", "v30", "v31", "v32", "v33", "v34", "v35", "v36", "v37", "v38", "v39", "v40", "v41", "v42", "v43", "v44", "v45", "v46", "v47", "v48", "v49", "v50", "v51", "v52", "v53", "v54", "v55", "v56", "v57", "v58", "v59", "v60", "v61", "v62", "v63", "v64", "v65", "v66", "v67", "v68", "v69", "v70", "v71", "v72", "v73", "v74", "v75", "v76", "v77", "v78", "v79", "v80", "v81", "v82", "v83", "v84", "v85", "v86", "v87", "v88", "v89", "v90", "v91", "v92", "v93", "v94", "v95", "v96", "v97", "v98", "v99", "v100", "v101", "v102", "v103", "v104", "v105", "v106", "v107", "v108", "v109", "v110", "v111", "v112", "v113", "v114", "v115", "v116", "v117", "v118", "v119", "v120", "v121", "v122", "v123", "v124", "v125", "v126", "v127", "v128", "v129", "v130", "v131", "v132", "v133", "v134", "v135", "v136", "v137", "v138", "v139", "v140", "v141", "v142", "v143", "v144", "v145", "v146", "v147", "v148", "v149", "v150", "v151", "v152", "v153", "v154", "v155", "v156", "v157", "v158", "v159", "v160", "v161", "v162", "v163", "v164", "v165", "v166", "v167", "v168", "v169", "v170", "v171", "v172", "v173", "v174", "v175", "v176", "v177", "v178", "v179", "v180", "v181", "v182", "v183", "v184", "v185", "v186", "v187", "v188", "v189", "v190", "v191", "v192", "v193", "v194", "v195", "v196", "v197", "v198", "v199", "v200", "v201", "v202", "v203", "v204", "v205", "v206", "v207", "v208", "v209", "v210", "v211", "v212", "v213", "v214", "v215", "v216", "v217", "v218", "v219", "v220", "v221", "v222", "v223", "v224", "v225", "v226", "v227", "v228", "v229", "v230", "v231", "v232", "v233", "v234", "v235", "v236", "v237", "v238", "v239", "v240", "v241", "v242", "v243", "v244", "v245", "v246", "v247", "v248", "v249", "v250", "v251", "v252", "v253", "v254", "v255", "a0", "a1", "a2", "a3", "a4", "a5", "a6", "a7", "a8", "a9", "a10", "a11", "a12", "a13", "a14", "a15", "a16", "a17", "a18", "a19", "a20", "a21", "a22", "a23", "a24", "a25", "a26", "a27", "a28", "a29", "a30", "a31", "a32", "a33", "a34", "a35", "a36", "a37", "a38", "a39", "a40", "a41", "a42", "a43", "a44", "a45", "a46", "a47", "a48", "a49", "a50", "a51", "a52", "a53", "a54", "a55", "a56", "a57", "a58", "a59", "a60", "a61", "a62", "a63", "a64", "a65", "a66", "a67", "a68", "a69", "a70", "a71", "a72", "a73", "a74", "a75", "a76", "a77", "a78", "a79", "a80", "a81", "a82", "a83", "a84", "a85", "a86", "a87", "a88", "a89", "a90", "a91", "a92", "a93", "a94", "a95", "a96", "a97", "a98", "a99", "a100", "a101", "a102", "a103", "a104", "a105", "a106", "a107", "a108", "a109", "a110", "a111", "a112", "a113", "a114", "a115", "a116", "a117", "a118", "a119", "a120", "a121", "a122", "a123", "a124", "a125", "a126", "a127", "a128", "a129", "a130", "a131", "a132", "a133", "a134", "a135", "a136", "a137", "a138", "a139", "a140", "a141", "a142", "a143", "a144", "a145", "a146", "a147", "a148", "a149", "a150", "a151", "a152", "a153", "a154", "a155", "a156", "a157", "a158", "a159", "a160", "a161", "a162", "a163", "a164", "a165", "a166", "a167", "a168", "a169", "a170", "a171", "a172", "a173", "a174", "a175", "a176", "a177", "a178", "a179", "a180", "a181", "a182", "a183", "a184", "a185", "a186", "a187", "a188", "a189", "a190", "a191", "a192", "a193", "a194", "a195", "a196", "a197", "a198", "a199", "a200", "a201", "a202", "a203", "a204", "a205", "a206", "a207", "a208", "a209", "a210", "a211", "a212", "a213", "a214", "a215", "a216", "a217", "a218", "a219", "a220", "a221", "a222", "a223", "a224", "a225", "a226", "a227", "a228", "a229", "a230", "a231", "a232", "a233", "a234", "a235", "a236", "a237", "a238", "a239", "a240", "a241", "a242", "a243", "a244", "a245", "a246", "a247", "a248", "a249", "a250", "a251", "a252", "a253", "a254", "a255", "s8", "s9", "s10", "s11", "s12", "s13", "s14", "s15", "s16", "s17", "s18", "s19", "s20", "s21", "s22", "s23", "s24", "s25", "s26", "s27", "s28", "s29", "s30", "s31", "s32", "s33", "s34", "s35", "s36", "s37", "s38", "s39", "s40", "s41", "s42", "s43", "s44", "s45", "s46", "s47", "s48", "s49", "s50", "s51", "s52", "s53", "s54", "s55", "s56", "s57", "s58", "s59", "s60", "s61", "s62", "s63", "s64", "s65", "s66", "s67", "s68", "s69", "s70", "s71", "s72", "s73", "s74", "s75", "s76", "s77", "s78", "s79", "s80", "s81", "s82", "s83", "s84", "s85", "s86", "s87", "s88", "s89", "s90", "s91", "s92", "s93", "s94", "s95", "s96", "s97", "s98", "s99", "s100", "s101", "vcc", "scc", "memory");
}

extern "C" void kernel_launch(void* const* d_in, const int* in_sizes, int n_in, void* d_out, int out_size, void* d_ws, size_t ws_size,
                              hipStream_t stream) {
  (void)hipFuncSetAttribute((const void*)attn_fused, hipFuncAttributeMaxDynamicSharedMemorySize, 153088);
  attn_fused<<<256, 256, 153088, stream>>>(d_in[0], d_in[1], d_in[2], d_in[3], d_in[4], d_in[5], d_in[6], d_ws, d_out);
}
```

```cpp
#include <hip/hip_runtime.h>

__global__ __launch_bounds__(256, 1) void attn_fused(const void* q, const void* qk, const void* qv, const void* ks, const void* kz,
                                                     const void* vs, const void* vz, void* ws, void* out) {
  const int wid = __builtin_amdgcn_readfirstlane(threadIdx.x >> 6);
  asm volatile(
    "s_load_dwordx16 s[8:23], %0, 0x0\n"
    "s_mov_b32 s58, %2\n"
    "s_and_b32 s76, %1, 7\n"
    "s_lshr_b32 s77, %1, 3\n"
    "s_and_b32 s78, s77, 3\n"
    "s_lshl_b32 s56, s76, 2\n"
    "s_add_u32 s56, s56, s78\n"
    "s_lshr_b32 s57, s77, 2\n"
    "v_mbcnt_lo_u32_b32 v231, -1, 0\n"
    "v_mbcnt_hi_u32_b32 v231, -1, v231\n"
    "s_getpc_b64 s[44:45]\n"
    "LPC:\n"
    "s_mov_b32 s46, LSLOW-LPC\n"
    "s_mov_b32 s47, 0x20000\n"
    "s_lshr_b32 s76, %1, 3\n"
    "s_lshl_b32 s76, s76, 2\n"
    "s_add_u32 s76, s76, s58\n"
    "s_lshl_b32 s76, s76, 9\n"
    "v_lshlrev_b32 v196, 3, v231\n"
    "buffer_load_dword v251, v196, s[44:47], s76 offen\n"
    "s_mul_i32 s72, s58, 0xc00\n"
    "s_lshr_b32 s86, s57, 2\n"
    "s_mul_i32 s64, s86, 0xc000\n"
    "s_lshl_b32 s86, s86, 3\n"
    "s_and_b32 s79, s57, 3\n"
    "s_add_u32 s86, s86, s79\n"
    "s_lshl_b32 s86, s86, 2\n"
    "s_xor_b32 s76, s57, 4\n"
    "s_and_b32 s77, s76, 6\n"
    "s_mul_i32 s77, s77, 15\n"
    "s_add_u32 s76, s76, s77\n"
    "s_and_b32 s77, s58, 1\n"
    "s_mul_i32 s78, s77, 12\n"
    "s_add_u32 s78, s78, s86\n"
    "s_lshl_b32 s78, s78, 1\n"
    "s_mul_i32 s77, s77, 0xc0\n"
    "s_movk_i32 s90, 0x556\n"
    "s_lshl_b32 s60, s56, 12\n"
    "s_cmp_lt_u32 s58, 2\n"
    "s_cselect_b32 s76, s76, s78\n"
    "s_cmovk_i32 s90, 0xaab\n"
    "s_cselect_b32 s91, 24, 0\n"
    "s_cselect_b32 s92, 1, 10\n"
    "s_cselect_b32 s89, 7, 0\n"
    "s_cselect_b32 s64, s64, 0\n"
    "s_add_u32 s60, s60, s76\n"
    "s_mul_i32 s60, s60, 0x180\n"
    "s_mul_i32 s76, s76, 0x180\n"
    "s_sub_u32 s34, 0x180000, s76\n"
    "s_mov_b32 s35, 0x20000\n"
    "v_add_u32 v144, s77, v231\n"
    "v_add_u32 v145, 64, v144\n"
    "v_add_u32 v146, 64, v145\n"
    "v_mul_u32_u24 v197, s90, v144\n"
    "v_lshrrev_b32 v197, 16, v197\n"
    "v_mul_u32_u24 v198, s92, v197\n"
    "v_lshrrev_b32 v198, 1, v198\n"
    "v_and_b32 v198, s89, v198\n"
    "v_xor_b32 v198, v198, v144\n"
    "v_mul_u32_u24 v197, s91, v197\n"
    "v_add_lshl_u32 v202, v197, v198, 4\n"
    "v_mul_u32_u24 v197, s90, v145\n"
    "v_lshrrev_b32 v197, 16, v197\n"
    "v_mul_u32_u24 v198, s92, v197\n"
    "v_lshrrev_b32 v198, 1, v198\n"
    "v_and_b32 v198, s89, v198\n"
    "v_xor_b32 v198, v198, v144\n"
    "v_mul_u32_u24 v197, s91, v197\n"
    "v_add_lshl_u32 v203, v197, v198, 4\n"
    "v_mul_u32_u24 v197, s90, v146\n"
    "v_lshrrev_b32 v197, 16, v197\n"
    "v_mul_u32_u24 v198, s92, v197\n"
    "v_lshrrev_b32 v198, 1, v198\n"
    "v_and_b32 v198, s89, v198\n"
    "v_xor_b32 v198, v198, v144\n"
    "v_mul_u32_u24 v197, s91, v197\n"
    "v_add_lshl_u32 v204, v197, v198, 4\n"
    "s_waitcnt lgkmcnt(0)\n"
    "s_cmp_lt_u32 s58, 2\n"
    "s_cselect_b64 s[80:81], s[10:11], s[12:13]\n"
    "s_cselect_b64 s[82:83], s[16:17], s[14:15]\n"
    "s_add_u32 s32, s80, s60\n"
    "s_addc_u32 s33, s81, 0\n"
    "s_cmp_eq_u32 s64, 0\n"
    "s_cbranch_scc1 LNOR\n"
    "s_add_u32 m0, s72, 0x12000\n"
    "s_nop 0\n"
    "buffer_load_dwordx4 v202, s[32:35], 0 offen nt lds\n"
    "buffer_load_dwordx4 v203, s[32:35], 0 offen offset:1024 nt lds\n"
    "buffer_load_dwordx4 v204, s[32:35], 0 offen offset:2048 nt lds\n"
    "LNOR:\n"
    "s_add_u32 m0, s72, 0x15000\n"
    "s_nop 0\n"
    "buffer_load_dwordx4 v202, s[32:35], s64 offen nt lds\n"
    "buffer_load_dwordx4 v203, s[32:35], s64 offen offset:1024 nt lds\n"
    "buffer_load_dwordx4 v204, s[32:35], s64 offen offset:2048 nt lds\n"
    "LR0:\n"
    "s_load_dwordx2 s[24:25], %0, 0x40\n"
    "s_mul_i32 s71, s58, 0x1800\n"
    "s_lshr_b32 s87, s57, 2\n"
    "s_mul_i32 s87, s87, 0x6000\n"
    "s_mul_i32 s61, s56, 0x3000\n"
    "s_lshl_b32 s62, s56, 12\n"
    "s_lshl_b32 s59, s56, 12\n"
    "s_mov_b32 s50, 0x3000\n"
    "s_mov_b32 s51, 0x20000\n"
    "s_and_b32 s77, s57, 1\n"
    "s_mul_i32 s77, s77, 0xc0\n"
    "v_mul_u32_u24 v197, 0x1556, v144\n"
    "v_lshrrev_b32 v197, 16, v197\n"
    "v_mul_u32_u24 v196, 12, v197\n"
    "v_sub_u32 v196, v144, v196\n"
    "v_mul_u32_u24 v248, 0x180, v197\n"
    "v_lshl_add_u32 v248, v196, 4, v248\n"
    "v_add_u32 v248, s77, v248\n"
    "s_add_u32 s48, s82, s61\n"
    "s_addc_u32 s49, s83, 0\n"
    "s_add_u32 m0, s72, 0x21000\n"
    "s_nop 0\n"
    "buffer_load_dwordx4 v248, s[48:51], 0 offen lds\n"
    "s_add_u32 s40, s20, s62\n"
    "s_addc_u32 s41, s21, 0\n"
    "s_add_u32 s52, s18, s62\n"
    "s_addc_u32 s53, s19, 0\n"
    "s_mov_b32 s42, 0x1000\n"
    "s_mov_b32 s43, 0x20000\n"
    "s_mov_b32 s54, 0x1000\n"
    "s_mov_b32 s55, 0x20000\n"
    "v_lshlrev_b32 v196, 1, v231\n"
    "buffer_load_ushort v44, v196, s[40:43], 0 offen\n"
    "buffer_load_ushort v45, v196, s[52:55], 0 offen\n"
    "s_lshl_b32 s77, s58, 6\n"
    "v_add_u32 v41, s77, v231\n"
    "v_bfe_u32 v197, v231, 2, 1\n"
    "v_mul_u32_u24 v197, 12, v197\n"
    "v_and_b32 v198, 7, v231\n"
    "v_add3_u32 v197, v197, v198, s86\n"
    "v_lshrrev_b32 v40, 3, v41\n"
    "v_lshlrev_b32 v40, 7, v40\n"
    "v_lshl_add_u32 v40, v197, 1, v40\n"
    "buffer_load_ushort v42, v40, s[40:43], 0 offen\n"
    "buffer_load_ushort v43, v40, s[52:55], 0 offen\n"
    "s_add_u32 s44, s22, s59\n"
    "s_addc_u32 s45, s23, 0\n"
    "s_mov_b32 s46, 0x900\n"
    "s_mov_b32 s47, 0x20000\n"
    "v_mov_b32 v196, 0x40000000\n"
    "s_cmp_eq_u32 s58, 0\n"
    "s_cselect_b32 s77, 64, 0\n"
    "s_cselect_b32 s74, 1, 0\n"
    "v_and_b32 v213, 7, v231\n"
    "v_mul_u32_u24 v213, 0x100, v213\n"
    "v_cmp_gt_u32 vcc, s77, v231\n"
    "v_mov_b32 v197, 0x800\n"
    "s_mul_i32 s77, s57, 0x100\n"
    "v_cndmask_b32 v213, v196, v213, vcc\n"
    "v_cndmask_b32 v197, v196, v197, vcc\n"
    "buffer_load_dword v221, v197, s[44:47], 0 offen sc1\n"
    "v_cmp_gt_u32 vcc, s74, v231\n"
    "v_mov_b32 v214, s77\n"
    "s_mul_i32 s66, s74, 3\n"
    "v_cndmask_b32 v214, v196, v214, vcc\n"
    "s_add_u32 s78, s64, 0xc000\n"
    "s_add_u32 m0, s72, 0x1e000\n"
    "s_nop 0\n"
    "buffer_load_dwordx4 v202, s[32:35], s78 offen nt lds\n"
    "buffer_load_dwordx4 v203, s[32:35], s78 offen offset:1024 nt lds\n"
    "buffer_load_dwordx4 v204, s[32:35], s78 offen offset:2048 nt lds\n"
    "s_lshl_b32 s76, s56, 3\n"
    "s_add_u32 s76, s76, s57\n"
    "s_mul_i32 s76, s76, 0x18000\n"
    "s_add_u32 s28, s8, s76\n"
    "s_addc_u32 s29, s9, 0\n"
    "s_mov_b32 s30, 0x18000\n"
    "s_mov_b32 s31, 0x20000\n"
    "s_mul_i32 s88, s58, 0x6000\n"
    "v_mul_u32_u24 v197, 0xaab, v231\n"
    "v_lshrrev_b32 v197, 16, v197\n"
    "v_mul_u32_u24 v196, 24, v197\n"
    "v_sub_u32 v196, v231, v196\n"
    "v_mul_u32_u24 v152, 0x180, v197\n"
    "v_lshrrev_b32 v197, 1, v197\n"
    "v_xor_b32 v196, v196, v197\n"
    "v_lshl_add_u32 v152, v196, 4, v152\n"
    "v_add_u32 v198, 64, v231\n"
    "v_mul_u32_u24 v197, 0xaab, v198\n"
    "v_lshrrev_b32 v197, 16, v197\n"
    "v_mul_u32_u24 v196, 24, v197\n"
    "v_sub_u32 v196, v198, v196\n"
    "v_mul_u32_u24 v153, 0x180, v197\n"
    "v_lshrrev_b32 v197, 1, v197\n"
    "v_xor_b32 v196, v196, v197\n"
    "v_lshl_add_u32 v153, v196, 4, v153\n"
    "v_add_u32 v198, 0x80, v231\n"
    "v_mul_u32_u24 v197, 0xaab, v198\n"
    "v_lshrrev_b32 v197, 16, v197\n"
    "v_mul_u32_u24 v196, 24, v197\n"
    "v_sub_u32 v196, v198, v196\n"
    "v_mul_u32_u24 v154, 0x180, v197\n"
    "v_lshrrev_b32 v197, 1, v197\n"
    "v_xor_b32 v196, v196, v197\n"
    "v_lshl_add_u32 v154, v196, 4, v154\n"
    "s_mov_b32 s76, 0xc00\n"
    "v_xad_u32 v155, v152, 64, s76\n"
    "v_xad_u32 v156, v153, 64, s76\n"
    "v_xad_u32 v157, v154, 64, s76\n"
    "s_add_u32 s78, s88, 0\n"
    "s_add_u32 m0, s71, 0\n"
    "s_nop 0\n"
    "buffer_load_dwordx4 v152, s[28:31], s78 offen nt lds\n"
    "s_add_u32 s78, s88, 0\n"
    "s_add_u32 m0, s71, 0x400\n"
    "s_nop 0\n"
    "buffer_load_dwordx4 v153, s[28:31], s78 offen nt lds\n"
    "s_add_u32 s78, s88, 0\n"
    "s_add_u32 m0, s71, 0x800\n"
    "s_nop 0\n"
    "buffer_load_dwordx4 v154, s[28:31], s78 offen nt lds\n"
    "v_mov_b32 v196, 0x40000000\n"
    "v_and_b32 v46, 15, v231\n"
    "v_lshrrev_b32 v47, 4, v231\n"
    "s_mov_b32 s75, 0x3dd53b95\n"
    "v_min_u32 v199, 2, v47\n"
    "v_lshlrev_b32 v197, 3, v199\n"
    "s_lshl_b32 s76, s58, 1\n"
    "v_add_u32 v197, s76, v197\n"
    "v_lshrrev_b32 v198, 1, v46\n"
    "v_xor_b32 v197, v197, v198\n"
    "v_mul_u32_u24 v205, 0x180, v46\n"
    "v_lshl_add_u32 v205, v197, 4, v205\n"
    "v_add_u32 v205, 0x12000, v205\n"
    "v_xor_b32 v206, 16, v205\n"
    "v_and_b32 v197, 7, v231\n"
    "v_lshl_add_u32 v209, v199, 5, v197\n"
    "s_lshl_b32 s76, s58, 3\n"
    "v_add_lshl_u32 v209, v209, s76, 1\n"
    "v_add_u32 v217, 0x21000, v209\n"
    "s_lshr_b32 s76, s57, 2\n"
    "s_mul_i32 s76, s76, 0xc0\n"
    "s_add_u32 s76, s76, 0x20f40\n"
    "v_add_u32 v209, s76, v209\n"
    "v_lshrrev_b32 v198, 3, v231\n"
    "s_mul_i32 s76, s58, 12\n"
    "v_lshl_add_u32 v199, v198, 1, s76\n"
    "v_mul_u32_u24 v207, 5, v197\n"
    "v_and_b32 v207, 9, v207\n"
    "v_xor_b32 v199, v199, v207\n"
    "v_mul_u32_u24 v207, 0x300, v197\n"
    "v_lshl_add_u32 v207, v199, 4, v207\n"
    "v_add_u32 v207, 0x13800, v207\n"
    "v_xor_b32 v208, 16, v207\n"
    "s_mul_i32 s76, s58, 0xc0\n"
    "s_add_u32 s76, s76, 0x13800\n"
    "v_lshl_add_u32 v207, v231, 2, s76\n"
    "v_lshlrev_b32 v210, 1, v197\n"
    "v_add_u32 v210, 0x24000, v210\n"
    "s_mul_i32 s76, s58, 0x480\n"
    "s_add_u32 s76, s76, 0x24400\n"
    "v_mul_u32_u24 v215, 0x90, v197\n"
    "v_lshl_add_u32 v215, v198, 4, v215\n"
    "v_add_u32 v215, s76, v215\n"
    "v_and_b32 v197, 1, v197\n"
    "v_lshlrev_b32 v197, 3, v197\n"
    "v_sub_u32 v215, v215, v197\n"
    "v_bfe_u32 v197, v231, 2, 2\n"
    "v_mul_u32_u24 v216, 0x90, v197\n"
    "v_lshl_add_u32 v216, v47, 5, v216\n"
    "v_and_b32 v197, 1, v197\n"
    "v_lshlrev_b32 v197, 3, v197\n"
    "v_sub_u32 v216, v216, v197\n"
    "v_and_b32 v197, 3, v231\n"
    "v_lshl_add_u32 v216, v197, 3, v216\n"
    "v_add_u32 v216, s76, v216\n"
    "s_lshr_b32 s76, s57, 1\n"
    "s_xor_b32 s76, s76, 2\n"
    "s_mul_i32 s76, s76, 0x1800\n"
    "s_and_b32 s77, s57, 1\n"
    "s_mul_i32 s77, s77, 0xc00\n"
    "s_add_u32 s76, s76, s77\n"
    "s_lshl_b32 s77, s58, 8\n"
    "s_add_u32 s76, s76, s77\n"
    "v_lshlrev_b32 v197, 4, v46\n"
    "v_lshl_add_u32 v211, v47, 10, v197\n"
    "v_add_u32 v211, s76, v211\n"
    "s_lshr_b32 s76, s57, 2\n"
    "s_lshl_b32 s76, s76, 10\n"
    "s_lshl_b32 s77, s79, 8\n"
    "s_add_u32 s76, s76, s77\n"
    "s_mul_i32 s77, s58, 48\n"
    "v_add_u32 v198, s77, v231\n"
    "v_lshrrev_b32 v197, 5, v198\n"
    "v_lshl_add_u32 v212, v197, 12, s76\n"
    "v_bfe_u32 v197, v198, 2, 1\n"
    "v_lshl_add_u32 v212, v197, 11, v212\n"
    "v_bfe_u32 v197, v198, 3, 2\n"
    "v_lshl_add_u32 v212, v197, 6, v212\n"
    "v_cmp_gt_u32 vcc, 48, v231\n"
    "v_and_b32 v197, 3, v198\n"
    "v_lshl_add_u32 v212, v197, 4, v212\n"
    "v_cndmask_b32 v211, v196, v211, vcc\n"
    "v_cndmask_b32 v212, v196, v212, vcc\n"
    "v_mov_b32 v222, 0\n"
    "v_mov_b32 v223, 0\n"
    "v_mov_b32 v224, 0\n"
    "v_mov_b32 v225, 0\n"
    "v_mov_b32 v160, 0\n"
    "v_mov_b32 v161, 0\n"
    "v_mov_b32 v162, 0\n"
    "v_mov_b32 v163, 0\n"
    "v_mov_b32 v164, 0\n"
    "v_mov_b32 v165, 0\n"
    "v_mov_b32 v172, 0\n"
    "v_mov_b32 v173, 0\n"
    "v_mov_b32 v174, 0\n"
    "v_mov_b32 v175, 0\n"
    "v_lshlrev_b32 v41, 1, v41\n"
    "v_add_u32 v41, 0x24000, v41\n"
    "s_mul_i32 s76, s56, 0xc0000\n"
    "s_add_u32 s77, s76, 0x40000\n"
    "s_add_u32 s36, s22, s77\n"
    "s_addc_u32 s37, s23, 0\n"
    "s_mov_b32 s38, 0xc0000\n"
    "s_mov_b32 s39, 0x20000\n"
    "s_cmp_eq_u32 s87, 0\n"
    "s_cselect_b32 s62, 0x40000000, 0\n"
    "s_waitcnt vmcnt(10)\n"
    "v_lshlrev_b32 v44, 16, v44\n"
    "s_waitcnt vmcnt(9)\n"
    "v_lshlrev_b32 v45, 16, v45\n"
    "v_mul_f32 v197, v44, v45\n"
    "s_waitcnt vmcnt(8)\n"
    "ds_write_b16 v41, v42\n"
    "s_waitcnt vmcnt(7)\n"
    "ds_write_b16 v41, v43 offset:512\n"
    "v_min_f32_dpp v197, v197, v197 row_ror:8 row_mask:0xf bank_mask:0xf\n"
    "s_mul_i32 s76, s56, 0xc0000\n"
    "s_add_u32 s77, s76, 0x1840000\n"
    "v_min_f32_dpp v197, v197, v197 row_ror:4 row_mask:0xf bank_mask:0xf\n"
    "s_add_u32 s40, s22, s77\n"
    "s_addc_u32 s41, s23, 0\n"
    "v_min_f32_dpp v197, v197, v197 row_ror:2 row_mask:0xf bank_mask:0xf\n"
    "s_mov_b32 s42, 0xc0000\n"
    "s_mov_b32 s43, 0x20000\n"
    "v_min_f32_dpp v197, v197, v197 row_ror:1 row_mask:0xf bank_mask:0xf\n"
    "v_mov_b32 v198, v197\n"
    "s_nop 1\n"
    "v_permlane32_swap_b32 v197, v198\n"
    "v_min_f32 v197, v197, v198\n"
    "v_mov_b32 v198, v197\n"
    "s_nop 1\n"
    "v_permlane16_swap_b32 v197, v198\n"
    "v_min_f32 v197, v197, v198\n"
    "v_rcp_f32 v198, v197\n"
    "v_subrev_u32 v199, 0x37800000, v197\n"
    "v_mul_f32 v197, 0x3fcccccd, v197\n"
    "v_cmp_gt_u32 vcc, 0x10000000, v199\n"
    "v_mul_f32 v198, 0x3f3425ed, v198\n"
    "v_mov_b32 v199, 0x3f800000\n"
    "v_cndmask_b32 v197, 0, v197, vcc\n"
    "v_cndmask_b32 v198, v199, v198, vcc\n"
    "v_readfirstlane_b32 s70, v197\n"
    "v_mul_f32 v197, v197, v198\n"
    "v_readfirstlane_b32 s68, v198\n"
    "v_readfirstlane_b32 s69, v197\n"
    "s_waitcnt lgkmcnt(0)\n"
    "s_barrier\n"
    "ds_read_u16_d16_hi v172, v217 offset:6144\n"
    "ds_read_u16_d16_hi v173, v217\n"
    "ds_read_u16_d16_hi v174, v217 offset:6336\n"
    "ds_read_u16_d16_hi v175, v217 offset:192\n"
    "ds_read_u16_d16_hi v223, v209 offset:6144\n"
    "ds_read_u16_d16_hi v222, v209\n"
    "ds_read_b128 v[232:235], v205\n"
    "ds_read_b128 v[236:239], v206\n"
    "ds_read_u16_d16_hi v161, v209 offset:6336\n"
    "ds_read_u16_d16_hi v160, v209 offset:192\n"
    "ds_read_b128 v[64:67], v205 offset:12288\n"
    "ds_read_b128 v[68:71], v206 offset:12288\n"
    "s_waitcnt lgkmcnt(10)\n"
    "v_mul_f32 v197, v172, v173\n"
    "s_waitcnt lgkmcnt(8)\n"
    "v_mul_f32 v198, v174, v175\n"
    "v_min_f32 v197, v197, v198\n"
    "v_max_f32 v197, 0, v197\n"
    "v_mul_f32 v217, 0x3e15434e, v197\n"
    "ds_read_u16_d16_hi v225, v210 offset:512\n"
    "ds_read_u16_d16_hi v224, v210\n"
    "ds_read2st64_b32 v[240:241], v207 offset0:48 offset1:51\n"
    "ds_read2st64_b32 v[242:243], v207 offset0:54 offset1:57\n"
    "ds_read2st64_b32 v[244:245], v207 offset0:60 offset1:63\n"
    "ds_read2st64_b32 v[246:247], v207 offset0:66 offset1:69\n"
    "s_waitcnt lgkmcnt(13)\n"
    "v_mul_f32 v226, s75, v223\n"
    "s_waitcnt lgkmcnt(12)\n"
    "v_fma_f32 v227, -v222, v226, v217\n"
    "s_waitcnt lgkmcnt(11)\n"
    "v_cvt_f32_i32 v232, v232\n"
    "v_cvt_f32_i32 v233, v233\n"
    "v_cvt_f32_i32 v234, v234\n"
    "v_cvt_f32_i32 v235, v235\n"
    "s_waitcnt lgkmcnt(10)\n"
    "v_cvt_f32_i32 v236, v236\n"
    "v_cvt_f32_i32 v237, v237\n"
    "v_cvt_f32_i32 v238, v238\n"
    "v_cvt_f32_i32 v239, v239\n"
    "v_mul_f32_dpp v232, v226, v232 row_newbcast:0 row_mask:0xf bank_mask:0xf\n"
    "v_mul_f32_dpp v233, v226, v233 row_newbcast:1 row_mask:0xf bank_mask:0xf\n"
    "v_mul_f32_dpp v234, v226, v234 row_newbcast:2 row_mask:0xf bank_mask:0xf\n"
    "v_mul_f32_dpp v235, v226, v235 row_newbcast:3 row_mask:0xf bank_mask:0xf\n"
    "v_mul_f32_dpp v236, v226, v236 row_newbcast:4 row_mask:0xf bank_mask:0xf\n"
    "v_mul_f32_dpp v237, v226, v237 row_newbcast:5 row_mask:0xf bank_mask:0xf\n"
    "v_mul_f32_dpp v238, v226, v238 row_newbcast:6 row_mask:0xf bank_mask:0xf\n"
    "v_mul_f32_dpp v239, v226, v239 row_newbcast:7 row_mask:0xf bank_mask:0xf\n"
    "v_add_f32_dpp v232, v227, v232 row_newbcast:0 row_mask:0xf bank_mask:0xf\n"
    "v_add_f32_dpp v233, v227, v233 row_newbcast:1 row_mask:0xf bank_mask:0xf\n"
    "v_add_f32_dpp v234, v227, v234 row_newbcast:2 row_mask:0xf bank_mask:0xf\n"
    "v_add_f32_dpp v235, v227, v235 row_newbcast:3 row_mask:0xf bank_mask:0xf\n"
    "v_add_f32_dpp v236, v227, v236 row_newbcast:4 row_mask:0xf bank_mask:0xf\n"
    "v_add_f32_dpp v237, v227, v237 row_newbcast:5 row_mask:0xf bank_mask:0xf\n"
    "v_add_f32_dpp v238, v227, v238 row_newbcast:6 row_mask:0xf bank_mask:0xf\n"
    "v_add_f32_dpp v239, v227, v239 row_newbcast:7 row_mask:0xf bank_mask:0xf\n"
    "v_cvt_pk_bf16_f32 v232, v232, v233\n"
    "v_cvt_pk_bf16_f32 v233, v234, v235\n"
    "v_cvt_pk_bf16_f32 v234, v236, v237\n"
    "v_cvt_pk_bf16_f32 v235, v238, v239\n"
    "buffer_store_dwordx4 v[232:235], v211, s[36:39], s62 offen sc1\n"
    "s_waitcnt lgkmcnt(9)\n"
    "v_mul_f32 v166, s75, v161\n"
    "s_waitcnt lgkmcnt(8)\n"
    "v_fma_f32 v167, -v160, v166, v217\n"
    "s_waitcnt lgkmcnt(7)\n"
    "v_cvt_f32_i32 v64, v64\n"
    "v_cvt_f32_i32 v65, v65\n"
    "v_cvt_f32_i32 v66, v66\n"
    "v_cvt_f32_i32 v67, v67\n"
    "s_waitcnt lgkmcnt(6)\n"
    "v_cvt_f32_i32 v68, v68\n"
    "v_cvt_f32_i32 v69, v69\n"
    "v_cvt_f32_i32 v70, v70\n"
    "v_cvt_f32_i32 v71, v71\n"
    "v_mul_f32_dpp v64, v166, v64 row_newbcast:0 row_mask:0xf bank_mask:0xf\n"
    "v_mul_f32_dpp v65, v166, v65 row_newbcast:1 row_mask:0xf bank_mask:0xf\n"
    "v_mul_f32_dpp v66, v166, v66 row_newbcast:2 row_mask:0xf bank_mask:0xf\n"
    "v_mul_f32_dpp v67, v166, v67 row_newbcast:3 row_mask:0xf bank_mask:0xf\n"
    "v_mul_f32_dpp v68, v166, v68 row_newbcast:4 row_mask:0xf bank_mask:0xf\n"
    "v_mul_f32_dpp v69, v166, v69 row_newbcast:5 row_mask:0xf bank_mask:0xf\n"
    "v_mul_f32_dpp v70, v166, v70 row_newbcast:6 row_mask:0xf bank_mask:0xf\n"
    "v_mul_f32_dpp v71, v166, v71 row_newbcast:7 row_mask:0xf bank_mask:0xf\n"
    "v_add_f32_dpp v64, v167, v64 row_newbcast:0 row_mask:0xf bank_mask:0xf\n"
    "v_add_f32_dpp v65, v167, v65 row_newbcast:1 row_mask:0xf bank_mask:0xf\n"
    "v_add_f32_dpp v66, v167, v66 row_newbcast:2 row_mask:0xf bank_mask:0xf\n"
    "v_add_f32_dpp v67, v167, v67 row_newbcast:3 row_mask:0xf bank_mask:0xf\n"
    "v_add_f32_dpp v68, v167, v68 row_newbcast:4 row_mask:0xf bank_mask:0xf\n"
    "v_add_f32_dpp v69, v167, v69 row_newbcast:5 row_mask:0xf bank_mask:0xf\n"
    "v_add_f32_dpp v70, v167, v70 row_newbcast:6 row_mask:0xf bank_mask:0xf\n"
    "v_add_f32_dpp v71, v167, v71 row_newbcast:7 row_mask:0xf bank_mask:0xf\n"
    "v_cvt_pk_bf16_f32 v64, v64, v65\n"
    "v_cvt_pk_bf16_f32 v65, v66, v67\n"
    "v_cvt_pk_bf16_f32 v66, v68, v69\n"
    "v_cvt_pk_bf16_f32 v67, v70, v71\n"
    "s_add_u32 s78, s87, 0\n"
    "buffer_store_dwordx4 v[64:67], v211, s[36:39], s78 offen sc1\n"
    "s_waitcnt lgkmcnt(5)\n"
    "v_mul_f32 v228, s68, v225\n"
    "s_waitcnt lgkmcnt(4)\n"
    "v_fma_f32 v229, -v224, v228, s69\n"
    "s_waitcnt lgkmcnt(3)\n"
    "v_cvt_f32_i32 v240, v240\n"
    "v_cvt_f32_i32 v241, v241\n"
    "s_waitcnt lgkmcnt(2)\n"
    "v_cvt_f32_i32 v242, v242\n"
    "v_cvt_f32_i32 v243, v243\n"
    "s_waitcnt lgkmcnt(1)\n"
    "v_cvt_f32_i32 v244, v244\n"
    "v_cvt_f32_i32 v245, v245\n"
    "s_waitcnt lgkmcnt(0)\n"
    "v_cvt_f32_i32 v246, v246\n"
    "v_cvt_f32_i32 v247, v247\n"
    "v_mul_f32_dpp v240, v228, v240 row_newbcast:0 row_mask:0xf bank_mask:0xf\n"
    "v_mul_f32_dpp v241, v228, v241 row_newbcast:1 row_mask:0xf bank_mask:0xf\n"
    "v_mul_f32_dpp v242, v228, v242 row_newbcast:2 row_mask:0xf bank_mask:0xf\n"
    "v_mul_f32_dpp v243, v228, v243 row_newbcast:3 row_mask:0xf bank_mask:0xf\n"
    "v_mul_f32_dpp v244, v228, v244 row_newbcast:4 row_mask:0xf bank_mask:0xf\n"
    "v_mul_f32_dpp v245, v228, v245 row_newbcast:5 row_mask:0xf bank_mask:0xf\n"
    "v_mul_f32_dpp v246, v228, v246 row_newbcast:6 row_mask:0xf bank_mask:0xf\n"
    "v_mul_f32_dpp v247, v228, v247 row_newbcast:7 row_mask:0xf bank_mask:0xf\n"
    "v_add_f32_dpp v240, v229, v240 row_newbcast:0 row_mask:0xf bank_mask:0xf\n"
    "v_add_f32_dpp v241, v229, v241 row_newbcast:1 row_mask:0xf bank_mask:0xf\n"
    "v_add_f32_dpp v242, v229, v242 row_newbcast:2 row_mask:0xf bank_mask:0xf\n"
    "v_add_f32_dpp v243, v229, v243 row_newbcast:3 row_mask:0xf bank_mask:0xf\n"
    "v_add_f32_dpp v244, v229, v244 row_newbcast:4 row_mask:0xf bank_mask:0xf\n"
    "v_add_f32_dpp v245, v229, v245 row_newbcast:5 row_mask:0xf bank_mask:0xf\n"
    "v_add_f32_dpp v246, v229, v246 row_newbcast:6 row_mask:0xf bank_mask:0xf\n"
    "v_add_f32_dpp v247, v229, v247 row_newbcast:7 row_mask:0xf bank_mask:0xf\n"
    "v_cvt_pk_bf16_f32 v240, v240, v241\n"
    "v_cvt_pk_bf16_f32 v241, v242, v243\n"
    "v_cvt_pk_bf16_f32 v242, v244, v245\n"
    "v_cvt_pk_bf16_f32 v243, v246, v247\n"
    "s_waitcnt vmcnt(8)\n"
    "v_add_u32 v221, s74, v221\n"
    "s_mov_b32 s73, 0\n"
    "v_readfirstlane_b32 s67, v221\n"
    "s_mov_b32 s78, 0\n"
    "buffer_store_dwordx4 v[240:243], v212, s[40:43], s78 offen sc1\n"
    "s_add_u32 s78, s88, 0\n"
    "s_add_u32 m0, s71, 0xc00\n"
    "s_nop 0\n"
    "buffer_load_dwordx4 v155, s[28:31], s78 offen nt lds\n"
    "s_add_u32 s78, s88, 0\n"
    "s_add_u32 m0, s71, 0x1000\n"
    "s_nop 0\n"
    "buffer_load_dwordx4 v156, s[28:31], s78 offen nt lds\n"
    "s_add_u32 s78, s88, 0\n"
    "s_add_u32 m0, s71, 0x1400\n"
    "s_nop 0\n"
    "buffer_load_dwordx4 v157, s[28:31], s78 offen nt lds\n"
    "s_add_u32 s78, s88, 0x1800\n"
    "s_add_u32 m0, s71, 0x6000\n"
    "s_nop 0\n"
    "buffer_load_dwordx4 v152, s[28:31], s78 offen nt lds\n"
    "v_lshlrev_b32 v200, 4, v231\n"
    "v_add_u32 v201, 0xc000, v200\n"
    "v_mul_u32_u24 v198, 0x180, v46\n"
    "v_add_u32 v198, s71, v198\n"
    "v_lshrrev_b32 v197, 1, v46\n"
    "v_xor_b32 v197, v197, v47\n"
    "v_lshl_add_u32 v158, v197, 4, v198\n"
    "v_xor_b32 v159, 64, v158\n"
    "v_and_b32 v197, 3, v231\n"
    "v_cmp_eq_u32 vcc, 0, v197\n"
    "v_mov_b32 v198, 0x3f803f80\n"
    "s_nop 0\n"
    "v_cndmask_b32 v192, 0, v198, vcc\n"
    "v_cndmask_b32 v193, 0, v198, vcc\n"
    "v_cndmask_b32 v194, 0, v198, vcc\n"
    "v_cndmask_b32 v195, 0, v198, vcc\n"
    "s_and_b32 s77, s57, 1\n"
    "s_mul_i32 s77, s77, 0xc0\n"
    "s_sub_u32 s77, s77, 0x400\n"
    "v_mul_u32_u24 v197, 0x1556, v145\n"
    "v_lshrrev_b32 v197, 16, v197\n"
    "v_mul_u32_u24 v196, 12, v197\n"
    "v_sub_u32 v196, v145, v196\n"
    "v_mul_u32_u24 v249, 0x180, v197\n"
    "v_lshl_add_u32 v249, v196, 4, v249\n"
    "v_add_u32 v249, s77, v249\n"
    "s_and_b32 s77, s57, 1\n"
    "s_mul_i32 s77, s77, 0xc0\n"
    "s_sub_u32 s77, s77, 0x800\n"
    "v_mul_u32_u24 v197, 0x1556, v146\n"
    "v_lshrrev_b32 v197, 16, v197\n"
    "v_mul_u32_u24 v196, 12, v197\n"
    "v_sub_u32 v196, v146, v196\n"
    "v_mul_u32_u24 v250, 0x180, v197\n"
    "v_lshl_add_u32 v250, v196, 4, v250\n"
    "v_add_u32 v250, s77, v250\n"
    "s_mul_i32 s66, s74, 2\n"
    "s_waitcnt vmcnt(4)\n"
    "s_barrier\n"
    "v_mov_b32 v220, 2\n"
    "buffer_store_dwordx2 v[220:221], v214, s[44:47], 0 offen sc1\n"
    "s_add_u32 s78, s64, 0x18000\n"
    "s_add_u32 m0, s72, 0x1b000\n"
    "s_nop 0\n"
    "buffer_load_dwordx4 v202, s[32:35], s78 offen nt lds\n"
    "buffer_load_dwordx4 v203, s[32:35], s78 offen offset:1024 nt lds\n"
    "buffer_load_dwordx4 v204, s[32:35], s78 offen offset:2048 nt lds\n"
    "ds_read_u16_d16_hi v163, v209 offset:6528\n"
    "ds_read_u16_d16_hi v162, v209 offset:384\n"
    "ds_read_b128 v[72:75], v205 offset:49152\n"
    "ds_read_b128 v[76:79], v206 offset:49152\n"
    "ds_read_u16_d16_hi v165, v210 offset:528\n"
    "ds_read_u16_d16_hi v164, v210 offset:16\n"
    "ds_read2st64_b32 v[80:81], v207 offset0:192 offset1:195\n"
    "ds_read2st64_b32 v[82:83], v207 offset0:198 offset1:201\n"
    "ds_read2st64_b32 v[84:85], v207 offset0:204 offset1:207\n"
    "ds_read2st64_b32 v[86:87], v207 offset0:210 offset1:213\n"
    "s_waitcnt lgkmcnt(9)\n"
    "v_mul_f32 v168, s75, v163\n"
    "s_waitcnt lgkmcnt(8)\n"
    "v_fma_f32 v169, -v162, v168, v217\n"
    "s_waitcnt lgkmcnt(7)\n"
    "v_cvt_f32_i32 v72, v72\n"
    "v_cvt_f32_i32 v73, v73\n"
    "v_cvt_f32_i32 v74, v74\n"
    "v_cvt_f32_i32 v75, v75\n"
    "s_waitcnt lgkmcnt(6)\n"
    "v_cvt_f32_i32 v76, v76\n"
    "v_cvt_f32_i32 v77, v77\n"
    "v_cvt_f32_i32 v78, v78\n"
    "v_cvt_f32_i32 v79, v79\n"
    "v_mul_f32_dpp v72, v168, v72 row_newbcast:0 row_mask:0xf bank_mask:0xf\n"
    "v_mul_f32_dpp v73, v168, v73 row_newbcast:1 row_mask:0xf bank_mask:0xf\n"
    "v_mul_f32_dpp v74, v168, v74 row_newbcast:2 row_mask:0xf bank_mask:0xf\n"
    "v_mul_f32_dpp v75, v168, v75 row_newbcast:3 row_mask:0xf bank_mask:0xf\n"
    "v_mul_f32_dpp v76, v168, v76 row_newbcast:4 row_mask:0xf bank_mask:0xf\n"
    "v_mul_f32_dpp v77, v168, v77 row_newbcast:5 row_mask:0xf bank_mask:0xf\n"
    "v_mul_f32_dpp v78, v168, v78 row_newbcast:6 row_mask:0xf bank_mask:0xf\n"
    "v_mul_f32_dpp v79, v168, v79 row_newbcast:7 row_mask:0xf bank_mask:0xf\n"
    "v_add_f32_dpp v72, v169, v72 row_newbcast:0 row_mask:0xf bank_mask:0xf\n"
    "v_add_f32_dpp v73, v169, v73 row_newbcast:1 row_mask:0xf bank_mask:0xf\n"
    "v_add_f32_dpp v74, v169, v74 row_newbcast:2 row_mask:0xf bank_mask:0xf\n"
    "v_add_f32_dpp v75, v169, v75 row_newbcast:3 row_mask:0xf bank_mask:0xf\n"
    "v_add_f32_dpp v76, v169, v76 row_newbcast:4 row_mask:0xf bank_mask:0xf\n"
    "v_add_f32_dpp v77, v169, v77 row_newbcast:5 row_mask:0xf bank_mask:0xf\n"
    "v_add_f32_dpp v78, v169, v78 row_newbcast:6 row_mask:0xf bank_mask:0xf\n"
    "v_add_f32_dpp v79, v169, v79 row_newbcast:7 row_mask:0xf bank_mask:0xf\n"
    "v_cvt_pk_bf16_f32 v72, v72, v73\n"
    "v_cvt_pk_bf16_f32 v73, v74, v75\n"
    "v_cvt_pk_bf16_f32 v74, v76, v77\n"
    "v_cvt_pk_bf16_f32 v75, v78, v79\n"
    "s_waitcnt lgkmcnt(5)\n"
    "v_mul_f32 v170, s68, v165\n"
    "s_waitcnt lgkmcnt(4)\n"
    "v_fma_f32 v171, -v164, v170, s69\n"
    "s_waitcnt lgkmcnt(3)\n"
    "v_cvt_f32_i32 v80, v80\n"
    "v_cvt_f32_i32 v81, v81\n"
    "s_waitcnt lgkmcnt(2)\n"
    "v_cvt_f32_i32 v82, v82\n"
    "v_cvt_f32_i32 v83, v83\n"
    "s_waitcnt lgkmcnt(1)\n"
    "v_cvt_f32_i32 v84, v84\n"
    "v_cvt_f32_i32 v85, v85\n"
    "s_waitcnt lgkmcnt(0)\n"
    "v_cvt_f32_i32 v86, v86\n"
    "v_cvt_f32_i32 v87, v87\n"
    "v_mul_f32_dpp v80, v170, v80 row_newbcast:0 row_mask:0xf bank_mask:0xf\n"
    "v_mul_f32_dpp v81, v170, v81 row_newbcast:1 row_mask:0xf bank_mask:0xf\n"
    "v_mul_f32_dpp v82, v170, v82 row_newbcast:2 row_mask:0xf bank_mask:0xf\n"
    "v_mul_f32_dpp v83, v170, v83 row_newbcast:3 row_mask:0xf bank_mask:0xf\n"
    "v_mul_f32_dpp v84, v170, v84 row_newbcast:4 row_mask:0xf bank_mask:0xf\n"
    "v_mul_f32_dpp v85, v170, v85 row_newbcast:5 row_mask:0xf bank_mask:0xf\n"
    "v_mul_f32_dpp v86, v170, v86 row_newbcast:6 row_mask:0xf bank_mask:0xf\n"
    "v_mul_f32_dpp v87, v170, v87 row_newbcast:7 row_mask:0xf bank_mask:0xf\n"
    "v_add_f32_dpp v80, v171, v80 row_newbcast:0 row_mask:0xf bank_mask:0xf\n"
    "v_add_f32_dpp v81, v171, v81 row_newbcast:1 row_mask:0xf bank_mask:0xf\n"
    "v_add_f32_dpp v82, v171, v82 row_newbcast:2 row_mask:0xf bank_mask:0xf\n"
    "v_add_f32_dpp v83, v171, v83 row_newbcast:3 row_mask:0xf bank_mask:0xf\n"
    "v_add_f32_dpp v84, v171, v84 row_newbcast:4 row_mask:0xf bank_mask:0xf\n"
    "v_add_f32_dpp v85, v171, v85 row_newbcast:5 row_mask:0xf bank_mask:0xf\n"
    "v_add_f32_dpp v86, v171, v86 row_newbcast:6 row_mask:0xf bank_mask:0xf\n"
    "v_add_f32_dpp v87, v171, v87 row_newbcast:7 row_mask:0xf bank_mask:0xf\n"
    "v_cvt_pk_bf16_f32 v80, v80, v81\n"
    "v_cvt_pk_bf16_f32 v81, v82, v83\n"
    "v_cvt_pk_bf16_f32 v82, v84, v85\n"
    "v_cvt_pk_bf16_f32 v83, v86, v87\n"
    "buffer_load_dwordx2 v[218:219], v213, s[44:47], 0 offen sc1\n"
    "s_add_u32 s78, s87, 0x6000\n"
    "buffer_store_dwordx4 v[72:75], v211, s[36:39], s78 offen sc1\n"
    "s_mov_b32 s78, 0x6000\n"
    "buffer_store_dwordx4 v[80:83], v212, s[40:43], s78 offen sc1\n"
    "s_add_u32 s78, s88, 0x1800\n"
    "s_add_u32 m0, s71, 0x6400\n"
    "s_nop 0\n"
    "buffer_load_dwordx4 v153, s[28:31], s78 offen nt lds\n"
    "s_add_u32 s78, s88, 0x1800\n"
    "s_add_u32 m0, s71, 0x6800\n"
    "s_nop 0\n"
    "buffer_load_dwordx4 v154, s[28:31], s78 offen nt lds\n"
    "s_add_u32 s78, s88, 0x1800\n"
    "s_add_u32 m0, s71, 0x6c00\n"
    "s_nop 0\n"
    "buffer_load_dwordx4 v155, s[28:31], s78 offen nt lds\n"
    "s_add_u32 s78, s88, 0x1800\n"
    "s_add_u32 m0, s71, 0x7000\n"
    "s_nop 0\n"
    "buffer_load_dwordx4 v156, s[28:31], s78 offen nt lds\n"
    "s_add_u32 s78, s88, 0x1800\n"
    "s_add_u32 m0, s71, 0x7400\n"
    "s_nop 0\n"
    "buffer_load_dwordx4 v157, s[28:31], s78 offen nt lds\n"
    "s_waitcnt vmcnt(13)\n"
    "ds_read_b128 a[192:195], v158\n"
    "ds_read_b128 a[196:199], v159\n"
    "ds_read_b128 a[200:203], v158 offset:128\n"
    "ds_read_b128 a[204:207], v159 offset:128\n"
    "ds_read_b128 a[208:211], v158 offset:256\n"
    "ds_read_b128 a[212:215], v159 offset:256\n"
    "s_waitcnt vmcnt(7)\n"
    "v_cmp_ne_u32 s[80:81], s67, v219\n"
    "v_cmp_gt_u32 vcc, s66, v218\n"
    "s_or_b64 vcc, vcc, s[80:81]\n"
    "s_cbranch_vccnz LSPRE0\n"
    "LSPB0:\n"
    "s_waitcnt vmcnt(5)\n"
    "s_waitcnt lgkmcnt(0)\n"
    "s_barrier\n"
    "v_mov_b32 v220, 3\n"
    "buffer_store_dwordx2 v[220:221], v214, s[44:47], 0 offen sc1\n"
    "s_add_u32 m0, s72, 0x1e000\n"
    "s_nop 0\n"
    "buffer_load_dwordx4 v200, s[36:39], s72 offen sc1 lds\n"
    "s_add_u32 m0, s72, 0x1e000\n"
    "s_nop 0\n"
    "buffer_load_dwordx4 v200, s[36:39], s72 offen offset:1024 sc1 lds\n"
    "s_add_u32 m0, s72, 0x1e000\n"
    "s_nop 0\n"
    "buffer_load_dwordx4 v200, s[36:39], s72 offen offset:2048 sc1 lds\n"
    "s_add_u32 s65, s71, 0x3000\n"
    "s_add_u32 m0, s71, 0\n"
    "s_nop 0\n"
    "buffer_load_dwordx4 v200, s[36:39], s65 offen sc1 lds\n"
    "s_add_u32 s65, s71, 0x3400\n"
    "s_add_u32 m0, s71, 0x400\n"
    "s_nop 0\n"
    "buffer_load_dwordx4 v200, s[36:39], s65 offen sc1 lds\n"
    "s_add_u32 s65, s71, 0x3800\n"
    "s_add_u32 m0, s71, 0x800\n"
    "s_nop 0\n"
    "buffer_load_dwordx4 v200, s[36:39], s65 offen sc1 lds\n"
    "s_add_u32 s65, s71, 0x3c00\n"
    "s_add_u32 m0, s71, 0xc00\n"
    "s_nop 0\n"
    "buffer_load_dwordx4 v200, s[36:39], s65 offen sc1 lds\n"
    "s_add_u32 s65, s71, 0x4000\n"
    "s_add_u32 m0, s71, 0x1000\n"
    "s_nop 0\n"
    "buffer_load_dwordx4 v200, s[36:39], s65 offen sc1 lds\n"
    "s_add_u32 s65, s71, 0x4400\n"
    "s_add_u32 m0, s71, 0x1400\n"
    "s_nop 0\n"
    "buffer_load_dwordx4 v200, s[36:39], s65 offen sc1 lds\n"
    "s_add_u32 s65, s71, 0\n"
    "s_add_u32 m0, s71, 0x12000\n"
    "s_nop 0\n"
    "buffer_load_dwordx4 v200, s[40:43], s65 offen sc1 lds\n"
    "s_add_u32 s65, s71, 0x400\n"
    "s_add_u32 m0, s71, 0x12400\n"
    "s_nop 0\n"
    "buffer_load_dwordx4 v200, s[40:43], s65 offen sc1 lds\n"
    "s_add_u32 s65, s71, 0x800\n"
    "s_add_u32 m0, s71, 0x12800\n"
    "s_nop 0\n"
    "buffer_load_dwordx4 v200, s[40:43], s65 offen sc1 lds\n"
    "s_add_u32 s65, s71, 0xc00\n"
    "s_add_u32 m0, s71, 0x12c00\n"
    "s_nop 0\n"
    "buffer_load_dwordx4 v200, s[40:43], s65 offen sc1 lds\n"
    "s_add_u32 s65, s71, 0x1000\n"
    "s_add_u32 m0, s71, 0x13000\n"
    "s_nop 0\n"
    "buffer_load_dwordx4 v200, s[40:43], s65 offen sc1 lds\n"
    "s_add_u32 s65, s71, 0x1400\n"
    "s_add_u32 m0, s71, 0x13400\n"
    "s_nop 0\n"
    "buffer_load_dwordx4 v200, s[40:43], s65 offen sc1 lds\n"
    "s_add_u32 s78, s64, 0x24000\n"
    "s_add_u32 m0, s72, 0x18000\n"
    "s_nop 0\n"
    "buffer_load_dwordx4 v202, s[32:35], s78 offen nt lds\n"
    "buffer_load_dwordx4 v203, s[32:35], s78 offen offset:1024 nt lds\n"
    "buffer_load_dwordx4 v204, s[32:35], s78 offen offset:2048 nt lds\n"
    "s_add_u32 s78, s88, 0x3000\n"
    "s_add_u32 m0, s71, 0xc000\n"
    "s_nop 0\n"
    "buffer_load_dwordx4 v152, s[28:31], s78 offen nt lds\n"
    "s_add_u32 s78, s88, 0x3000\n"
    "s_add_u32 m0, s71, 0xc400\n"
    "s_nop 0\n"
    "buffer_load_dwordx4 v153, s[28:31], s78 offen nt lds\n"
    "s_add_u32 s78, s88, 0x3000\n"
    "s_add_u32 m0, s71, 0xc800\n"
    "s_nop 0\n"
    "buffer_load_dwordx4 v154, s[28:31], s78 offen nt lds\n"
    "s_mul_i32 s66, s74, 3\n"
    "buffer_load_dwordx2 v[218:219], v213, s[44:47], 0 offen sc1\n"
    "s_waitcnt vmcnt(23)\n"
    "ds_read_b128 a[216:219], v158 offset:24576\n"
    "ds_read_b128 a[220:223], v159 offset:24576\n"
    "ds_read_b128 a[224:227], v158 offset:24704\n"
    "ds_read_b128 a[228:231], v159 offset:24704\n"
    "ds_read_b128 a[232:235], v158 offset:24832\n"
    "ds_read_b128 a[236:239], v159 offset:24832\n"
    "s_add_u32 m0, s72, 0x21000\n"
    "s_nop 0\n"
    "buffer_load_dwordx4 v249, s[48:51], 0 offen offset:1024 lds\n"
    "v_add_u32 v255, 0x1e000, v200\n"
    "s_waitcnt vmcnt(14) lgkmcnt(0)\n"
    "s_add_u32 s60, s71, 0x9000\n"
    "s_add_u32 s61, s71, 0x6000\n"
    "s_add_u32 s62, s87, 0x12000\n"
    "s_mov_b32 s63, 0x12000\n"
    "s_mov_b32 s59, 0\n"
    "s_mul_i32 s66, s74, 3\n"
    "s_mul_i32 s90, s58, 0x1c00\n"
    "s_sub_u32 s90, s90, 0x1000\n"
    "s_sub_u32 s92, s90, s71\n"
    "s_barrier\n"
    "ds_read_b128 v[160:163], v255\n"
    "ds_read_b128 v[164:167], v255 offset:1024\n"
    "ds_read_b128 v[168:171], v255 offset:2048\n"
    "ds_read_b128 v[172:175], v255 offset:3072\n"
    "ds_read_b128 v[176:179], v255 offset:4096\n"
    "ds_read_b128 v[180:183], v255 offset:5120\n"
    "s_add_u32 s76, s87, 0xc000\n"
    "s_mov_b32 s77, 0xc000\n"
    "s_waitcnt lgkmcnt(4)\n"
    "v_mfma_f32_16x16x32_bf16 v[40:43], v[160:163], a[192:195], 0\n"
    "ds_read_b128 v[184:187], v255 offset:6144\n"
    "ds_read_u16_d16_hi v225, v210 offset:544\n"
    "v_mfma_f32_16x16x32_bf16 v[40:43], v[164:167], a[196:199], v[40:43]\n"
    "s_add_u32 s78, s88, 0x3000\n"
    "ds_read_u16_d16_hi v224, v210 offset:32\n"
    "ds_read2st64_b32 v[240:241], v207 offset0:144 offset1:147\n"
    "v_mfma_f32_16x16x32_bf16 v[44:47], v[160:163], a[216:219], 0\n"
    "s_add_u32 m0, s71, 0xcc00\n"
    "ds_read_b128 v[188:191], v255 offset:7168\n"
    "ds_read2st64_b32 v[242:243], v207 offset0:150 offset1:153\n"
    "v_mfma_f32_16x16x32_bf16 v[44:47], v[164:167], a[220:223], v[44:47]\n"
    "s_waitcnt lgkmcnt(8)\n"
    "buffer_load_dwordx4 v155, s[28:31], s78 offen nt lds\n"
    "v_mfma_f32_16x16x32_bf16 v[40:43], v[168:171], a[200:203], v[40:43]\n"
    "ds_read_b128 v[160:163], v255 offset:8192\n"
    "ds_read2st64_b32 v[244:245], v207 offset0:156 offset1:159\n"
    "v_mfma_f32_16x16x32_bf16 v[40:43], v[172:175], a[204:207], v[40:43]\n"
    "ds_read2st64_b32 v[246:247], v207 offset0:162 offset1:165\n"
    "ds_read_u16_d16_hi v223, v209 offset:6720\n"
    "v_mfma_f32_16x16x32_bf16 v[44:47], v[168:171], a[224:227], v[44:47]\n"
    "ds_read_b128 v[164:167], v255 offset:9216\n"
    "ds_read_u16_d16_hi v222, v209 offset:576\n"
    "v_mfma_f32_16x16x32_bf16 v[44:47], v[172:175], a[228:231], v[44:47]\n"
    "s_waitcnt lgkmcnt(12)\n"
    "ds_read_b128 v[232:235], v205 offset:36864\n"
    "ds_read_b128 v[236:239], v206 offset:36864\n"
    "v_mfma_f32_16x16x32_bf16 v[40:43], v[176:179], a[208:211], v[40:43]\n"
    "ds_read_b128 v[168:171], v255 offset:10240\n"
    "v_mfma_f32_16x16x32_bf16 v[40:43], v[180:183], a[212:215], v[40:43]\n"
    "v_mfma_f32_16x16x32_bf16 v[44:47], v[176:179], a[232:235], v[44:47]\n"
    "s_waitcnt lgkmcnt(11)\n"
    "ds_read_b128 v[172:175], v255 offset:11264\n"
    "v_mfma_f32_16x16x32_bf16 v[44:47], v[180:183], a[236:239], v[44:47]\n"
    "s_waitcnt lgkmcnt(11)\n"
    "v_mfma_f32_16x16x32_bf16 v[56:59], v[184:187], a[192:195], 0\n"
    "ds_read_b128 v[176:179], v200\n"
    "v_mul_f32 v228, s68, v225\n"
    "v_mfma_f32_16x16x32_bf16 v[56:59], v[188:191], a[196:199], v[56:59]\n"
    "s_add_u32 s78, s88, 0x3000\n"
    "v_fma_f32 v229, -v224, v228, s69\n"
    "v_cvt_f32_i32 v240, v240\n"
    "v_mfma_f32_16x16x32_bf16 v[60:63], v[184:187], a[216:219], 0\n"
    "s_add_u32 m0, s71, 0xd000\n"
    "ds_read_b128 v[180:183], v200 offset:1024\n"
    "v_cvt_f32_i32 v241, v241\n"
    "v_mfma_f32_16x16x32_bf16 v[60:63], v[188:191], a[220:223], v[60:63]\n"
    "s_waitcnt lgkmcnt(7)\n"
    "buffer_load_dwordx4 v156, s[28:31], s78 offen nt lds\n"
    "v_mfma_f32_16x16x32_bf16 v[56:59], v[160:163], a[200:203], v[56:59]\n"
    "ds_read_b128 v[184:187], v200 offset:2048\n"
    "v_cvt_f32_i32 v242, v242\n"
    "v_mfma_f32_16x16x32_bf16 v[56:59], v[164:167], a[204:207], v[56:59]\n"
    "v_cvt_f32_i32 v243, v243\n"
    "v_cvt_f32_i32 v244, v244\n"
    "v_mfma_f32_16x16x32_bf16 v[60:63], v[160:163], a[224:227], v[60:63]\n"
    "ds_read_b128 v[188:191], v200 offset:3072\n"
    "v_cvt_f32_i32 v245, v245\n"
    "v_mfma_f32_16x16x32_bf16 v[60:63], v[164:167], a[228:231], v[60:63]\n"
    "s_waitcnt lgkmcnt(4)\n"
    "v_cvt_f32_i32 v246, v246\n"
    "v_mul_f32 v226, s75, v223\n"
    "v_mfma_f32_16x16x32_bf16 v[56:59], v[168:171], a[208:211], v[56:59]\n"
    "ds_read_b128 v[160:163], v200 offset:4096\n"
    "v_cvt_f32_i32 v247, v247\n"
    "v_mfma_f32_16x16x32_bf16 v[56:59], v[172:175], a[212:215], v[56:59]\n"
    "v_fma_f32 v227, -v222, v226, v217\n"
    "v_mul_f32_dpp v240, v228, v240 row_newbcast:0 row_mask:0xf bank_mask:0xf\n"
    "v_mfma_f32_16x16x32_bf16 v[60:63], v[168:171], a[232:235], v[60:63]\n"
    "ds_read_b128 v[164:167], v200 offset:5120\n"
    "v_cvt_f32_i32 v232, v232\n"
    "v_mfma_f32_16x16x32_bf16 v[60:63], v[172:175], a[236:239], v[60:63]\n"
    "s_waitcnt lgkmcnt(4)\n"
    "v_mul_f32_dpp v241, v228, v241 row_newbcast:1 row_mask:0xf bank_mask:0xf\n"
    "v_cvt_f32_i32 v233, v233\n"
    "v_mfma_f32_16x16x32_bf16 v[64:67], v[176:179], a[192:195], 0\n"
    "ds_read_b128 v[168:171], v200 offset:6144\n"
    "v_mul_f32_dpp v242, v228, v242 row_newbcast:2 row_mask:0xf bank_mask:0xf\n"
    "v_cvt_f32_i32 v234, v234\n"
    "v_mul_f32_dpp v243, v228, v243 row_newbcast:3 row_mask:0xf bank_mask:0xf\n"
    "v_mfma_f32_16x16x32_bf16 v[64:67], v[180:183], a[196:199], v[64:67]\n"
    "s_add_u32 s78, s88, 0x3000\n"
    "v_cvt_f32_i32 v235, v235\n"
    "v_mul_f32_dpp v244, v228, v244 row_newbcast:4 row_mask:0xf bank_mask:0xf\n"
    "v_cvt_f32_i32 v236, v236\n"
    "v_mul_f32_dpp v245, v228, v245 row_newbcast:5 row_mask:0xf bank_mask:0xf\n"
    "v_mfma_f32_16x16x32_bf16 v[68:71], v[176:179], a[216:219], 0\n"
    "s_add_u32 m0, s71, 0xd400\n"
    "ds_read_b128 v[172:175], v200 offset:7168\n"
    "v_cvt_f32_i32 v237, v237\n"
    "v_mul_f32_dpp v246, v228, v246 row_newbcast:6 row_mask:0xf bank_mask:0xf\n"
    "v_cvt_f32_i32 v238, v238\n"
    "v_mfma_f32_16x16x32_bf16 v[68:71], v[180:183], a[220:223], v[68:71]\n"
    "s_waitcnt lgkmcnt(4)\n"
    "buffer_load_dwordx4 v157, s[28:31], s78 offen nt lds\n"
    "v_mul_f32_dpp v247, v228, v247 row_newbcast:7 row_mask:0xf bank_mask:0xf\n"
    "v_cvt_f32_i32 v239, v239\n"
    "v_mfma_f32_16x16x32_bf16 v[64:67], v[184:187], a[200:203], v[64:67]\n"
    "ds_read_b128 v[176:179], v200 offset:8192\n"
    "v_add_f32_dpp v240, v229, v240 row_newbcast:0 row_mask:0xf bank_mask:0xf\n"
    "v_mul_f32_dpp v232, v226, v232 row_newbcast:0 row_mask:0xf bank_mask:0xf\n"
    "v_add_f32_dpp v241, v229, v241 row_newbcast:1 row_mask:0xf bank_mask:0xf\n"
    "v_mfma_f32_16x16x32_bf16 v[64:67], v[188:191], a[204:207], v[64:67]\n"
    "v_mul_f32_dpp v233, v226, v233 row_newbcast:1 row_mask:0xf bank_mask:0xf\n"
    "v_add_f32_dpp v242, v229, v242 row_newbcast:2 row_mask:0xf bank_mask:0xf\n"
    "v_mul_f32_dpp v234, v226, v234 row_newbcast:2 row_mask:0xf bank_mask:0xf\n"
    "v_add_f32_dpp v243, v229, v243 row_newbcast:3 row_mask:0xf bank_mask:0xf\n"
    "v_mfma_f32_16x16x32_bf16 v[68:71], v[184:187], a[224:227], v[68:71]\n"
    "ds_read_b128 v[180:183], v200 offset:9216\n"
    "v_mul_f32_dpp v235, v226, v235 row_newbcast:3 row_mask:0xf bank_mask:0xf\n"
    "v_add_f32_dpp v244, v229, v244 row_newbcast:4 row_mask:0xf bank_mask:0xf\n"
    "v_mul_f32_dpp v236, v226, v236 row_newbcast:4 row_mask:0xf bank_mask:0xf\n"
    "v_add_f32_dpp v245, v229, v245 row_newbcast:5 row_mask:0xf bank_mask:0xf\n"
    "v_mul_f32_dpp v237, v226, v237 row_newbcast:5 row_mask:0xf bank_mask:0xf\n"
    "v_add_f32_dpp v246, v229, v246 row_newbcast:6 row_mask:0xf bank_mask:0xf\n"
    "v_mul_f32_dpp v238, v226, v238 row_newbcast:6 row_mask:0xf bank_mask:0xf\n"
    "v_add_f32_dpp v247, v229, v247 row_newbcast:7 row_mask:0xf bank_mask:0xf\n"
    "v_mul_f32_dpp v239, v226, v239 row_newbcast:7 row_mask:0xf bank_mask:0xf\n"
    "v_cvt_pk_bf16_f32 v240, v240, v241\n"
    "v_add_f32_dpp v232, v227, v232 row_newbcast:0 row_mask:0xf bank_mask:0xf\n"
    "v_cvt_pk_bf16_f32 v241, v242, v243\n"
    "v_add_f32_dpp v233, v227, v233 row_newbcast:1 row_mask:0xf bank_mask:0xf\n"
    "v_cvt_pk_bf16_f32 v242, v244, v245\n"
    "v_add_f32_dpp v234, v227, v234 row_newbcast:2 row_mask:0xf bank_mask:0xf\n"
    "v_cvt_pk_bf16_f32 v243, v246, v247\n"
    "v_add_f32_dpp v235, v227, v235 row_newbcast:3 row_mask:0xf bank_mask:0xf\n"
    "v_add_f32_dpp v236, v227, v236 row_newbcast:4 row_mask:0xf bank_mask:0xf\n"
    "v_add_f32_dpp v237, v227, v237 row_newbcast:5 row_mask:0xf bank_mask:0xf\n"
    "v_add_f32_dpp v238, v227, v238 row_newbcast:6 row_mask:0xf bank_mask:0xf\n"
    "v_add_f32_dpp v239, v227, v239 row_newbcast:7 row_mask:0xf bank_mask:0xf\n"
    "v_cvt_pk_bf16_f32 v232, v232, v233\n"
    "v_cvt_pk_bf16_f32 v233, v234, v235\n"
    "v_cvt_pk_bf16_f32 v234, v236, v237\n"
    "v_cvt_pk_bf16_f32 v235, v238, v239\n"
    "v_mfma_f32_16x16x32_bf16 v[68:71], v[188:191], a[228:231], v[68:71]\n"
    "s_waitcnt lgkmcnt(4)\n"
    "v_max3_f32 v196, v40, v41, v42\n"
    "v_max3_f32 v196, v196, v43, v56\n"
    "v_max3_f32 v196, v196, v57, v58\n"
    "v_max_f32 v196, v196, v59\n"
    "buffer_store_dwordx4 v[232:235], v211, s[36:39], s76 offen sc1\n"
    "v_mfma_f32_16x16x32_bf16 v[64:67], v[160:163], a[208:211], v[64:67]\n"
    "ds_read_b128 v[184:187], v200 offset:10240\n"
    "v_max3_f32 v198, v44, v45, v46\n"
    "v_mov_b32 v197, v196\n"
    "v_max3_f32 v198, v198, v47, v60\n"
    "v_mfma_f32_16x16x32_bf16 v[64:67], v[164:167], a[212:215], v[64:67]\n"
    "v_permlane32_swap_b32 v196, v197\n"
    "v_max3_f32 v198, v198, v61, v62\n"
    "v_max_f32 v196, v196, v197\n"
    "v_max_f32 v198, v198, v63\n"
    "v_mfma_f32_16x16x32_bf16 v[68:71], v[160:163], a[232:235], v[68:71]\n"
    "ds_read_b128 v[188:191], v200 offset:11264\n"
    "v_mov_b32 v197, v196\n"
    "v_mov_b32 v199, v198\n"
    "s_nop 0\n"
    "v_permlane16_swap_b32 v196, v197\n"
    "v_mfma_f32_16x16x32_bf16 v[68:71], v[164:167], a[236:239], v[68:71]\n"
    "s_waitcnt lgkmcnt(4)\n"
    "v_permlane32_swap_b32 v198, v199\n"
    "v_max_f32 v196, v196, v197\n"
    "v_max_f32 v198, v198, v199\n"
    "v_add_f32 v196, 0x426c0000, v196\n"
    "v_mfma_f32_16x16x32_bf16 v[80:83], v[168:171], a[192:195], 0\n"
    "ds_read_b128 v[160:163], v200 offset:12288\n"
    "v_mov_b32 v199, v198\n"
    "v_sub_f32 v96, 0, v196\n"
    "s_nop 0\n"
    "v_permlane16_swap_b32 v198, v199\n"
    "v_mfma_f32_16x16x32_bf16 v[80:83], v[172:175], a[196:199], v[80:83]\n"
    "s_add_u32 s78, s88, 0x4800\n"
    "buffer_store_dwordx4 v[240:243], v212, s[40:43], s77 offen sc1\n"
    "v_sub_f32 v97, 0, v196\n"
    "v_max_f32 v198, v198, v199\n"
    "v_sub_f32 v98, 0, v196\n"
    "v_add_f32 v198, 0x426c0000, v198\n"
    "v_sub_f32 v99, 0, v196\n"
    "v_mfma_f32_16x16x32_bf16 v[84:87], v[168:171], a[216:219], 0\n"
    "s_add_u32 m0, s71, 0x6000\n"
    "ds_read_b128 v[164:167], v200 offset:13312\n"
    "v_sub_f32 v100, 0, v198\n"
    "v_add_f32 v40, v40, v96\n"
    "v_sub_f32 v101, 0, v198\n"
    "v_mfma_f32_16x16x32_bf16 v[84:87], v[172:175], a[220:223], v[84:87]\n"
    "s_waitcnt lgkmcnt(4)\n"
    "buffer_load_dwordx4 v152, s[28:31], s78 offen nt lds\n"
    "v_add_f32 v41, v41, v96\n"
    "v_sub_f32 v102, 0, v198\n"
    "v_mfma_f32_16x16x32_bf16 v[80:83], v[176:179], a[200:203], v[80:83]\n"
    "ds_read_b128 v[168:171], v200 offset:14336\n"
    "v_add_f32 v42, v42, v96\n"
    "v_sub_f32 v103, 0, v198\n"
    "v_add_f32 v43, v43, v96\n"
    "v_mfma_f32_16x16x32_bf16 v[80:83], v[180:183], a[204:207], v[80:83]\n"
    "v_add_f32 v44, v44, v100\n"
    "v_add_f32 v56, v56, v96\n"
    "v_add_f32 v45, v45, v100\n"
    "v_add_f32 v57, v57, v96\n"
    "v_mfma_f32_16x16x32_bf16 v[84:87], v[176:179], a[224:227], v[84:87]\n"
    "ds_read_b128 v[172:175], v200 offset:15360\n"
    "v_add_f32 v46, v46, v100\n"
    "v_add_f32 v58, v58, v96\n"
    "v_add_f32 v47, v47, v100\n"
    "v_mfma_f32_16x16x32_bf16 v[84:87], v[180:183], a[228:231], v[84:87]\n"
    "s_waitcnt lgkmcnt(4)\n"
    "v_add_f32 v59, v59, v96\n"
    "v_add_f32 v60, v60, v100\n"
    "v_exp_f32 v40, v40\n"
    "v_mfma_f32_16x16x32_bf16 v[80:83], v[184:187], a[208:211], v[80:83]\n"
    "ds_read_b128 v[176:179], v200 offset:16384\n"
    "v_add_f32 v61, v61, v100\n"
    "v_add_f32 v62, v62, v100\n"
    "v_add_f32 v63, v63, v100\n"
    "v_mfma_f32_16x16x32_bf16 v[80:83], v[188:191], a[212:215], v[80:83]\n"
    "v_exp_f32 v41, v41\n"
    "v_exp_f32 v44, v44\n"
    "v_mfma_f32_16x16x32_bf16 v[84:87], v[184:187], a[232:235], v[84:87]\n"
    "ds_read_b128 v[180:183], v200 offset:17408\n"
    "v_exp_f32 v42, v42\n"
    "v_mfma_f32_16x16x32_bf16 v[84:87], v[188:191], a[236:239], v[84:87]\n"
    "s_waitcnt lgkmcnt(4)\n"
    "v_exp_f32 v45, v45\n"
    "v_exp_f32 v43, v43\n"
    "v_mfma_f32_16x16x32_bf16 v[32:35], v[160:163], a[192:195], v[96:99]\n"
    "ds_read_b128 v[184:187], v200 offset:18432\n"
    "v_exp_f32 v46, v46\n"
    "v_mfma_f32_16x16x32_bf16 v[32:35], v[164:167], a[196:199], v[32:35]\n"
    "s_add_u32 s78, s88, 0x4800\n"
    "v_exp_f32 v56, v56\n"
    "v_exp_f32 v47, v47\n"
    "v_mfma_f32_16x16x32_bf16 v[36:39], v[160:163], a[216:219], v[100:103]\n"
    "s_add_u32 m0, s71, 0x6400\n"
    "ds_read_b128 v[188:191], v200 offset:19456\n"
    "v_exp_f32 v57, v57\n"
    "v_mfma_f32_16x16x32_bf16 v[36:39], v[164:167], a[220:223], v[36:39]\n"
    "s_waitcnt lgkmcnt(4)\n"
    "buffer_load_dwordx4 v153, s[28:31], s78 offen nt lds\n"
    "v_exp_f32 v60, v60\n"
    "v_mfma_f32_16x16x32_bf16 v[32:35], v[168:171], a[200:203], v[32:35]\n"
    "ds_read_b128 v[160:163], v200 offset:20480\n"
    "v_exp_f32 v58, v58\n"
    "v_mfma_f32_16x16x32_bf16 v[32:35], v[172:175], a[204:207], v[32:35]\n"
    "v_exp_f32 v61, v61\n"
    "v_exp_f32 v59, v59\n"
    "v_mfma_f32_16x16x32_bf16 v[36:39], v[168:171], a[224:227], v[36:39]\n"
    "ds_read_b128 v[164:167], v200 offset:21504\n"
    "v_exp_f32 v62, v62\n"
    "v_cvt_pk_bf16_f32 v128, v40, v41\n"
    "v_mfma_f32_16x16x32_bf16 v[36:39], v[172:175], a[228:231], v[36:39]\n"
    "s_waitcnt lgkmcnt(4)\n"
    "v_cvt_pk_bf16_f32 v129, v42, v43\n"
    "v_exp_f32 v63, v63\n"
    "v_cvt_pk_bf16_f32 v130, v56, v57\n"
    "v_mfma_f32_16x16x32_bf16 v[32:35], v[176:179], a[208:211], v[32:35]\n"
    "ds_read_b128 v[168:171], v200 offset:22528\n"
    "v_cvt_pk_bf16_f32 v131, v58, v59\n"
    "v_cvt_pk_bf16_f32 v132, v44, v45\n"
    "v_add_f32 v64, v64, v96\n"
    "v_mfma_f32_16x16x32_bf16 v[32:35], v[180:183], a[212:215], v[32:35]\n"
    "v_cvt_pk_bf16_f32 v133, v46, v47\n"
    "v_add_f32 v65, v65, v96\n"
    "v_cvt_pk_bf16_f32 v134, v60, v61\n"
    "v_add_f32 v66, v66, v96\n"
    "v_mfma_f32_16x16x32_bf16 v[36:39], v[176:179], a[232:235], v[36:39]\n"
    "ds_read_b128 v[172:175], v200 offset:23552\n"
    "v_cvt_pk_bf16_f32 v135, v62, v63\n"
    "v_add_f32 v67, v67, v96\n"
    "v_add_f32 v68, v68, v100\n"
    "v_mfma_f32_16x16x32_bf16 v[36:39], v[180:183], a[236:239], v[36:39]\n"
    "s_waitcnt lgkmcnt(4)\n"
    "v_add_f32 v80, v80, v96\n"
    "v_add_f32 v69, v69, v100\n"
    "s_waitcnt vmcnt(12)\n"
    "s_barrier\n"
    "v_mfma_f32_16x16x32_bf16 v[48:51], v[184:187], a[192:195], v[96:99]\n"
    "ds_read_b128 v[176:179], v201 offset:24576\n"
    "v_add_f32 v81, v81, v96\n"
    "v_add_f32 v70, v70, v100\n"
    "v_add_f32 v82, v82, v96\n"
    "v_mfma_f32_16x16x32_bf16 v[48:51], v[188:191], a[196:199], v[48:51]\n"
    "s_add_u32 s78, s88, 0x4800\n"
    "v_add_f32 v71, v71, v100\n"
    "v_add_f32 v83, v83, v96\n"
    "v_add_f32 v84, v84, v100\n"
    "v_add_f32 v85, v85, v100\n"
    "v_mfma_f32_16x16x32_bf16 v[52:55], v[184:187], a[216:219], v[100:103]\n"
    "s_add_u32 m0, s71, 0x6800\n"
    "ds_read_b128 v[180:183], v201 offset:25600\n"
    "v_exp_f32 v64, v64\n"
    "v_add_f32 v86, v86, v100\n"
    "v_mfma_f32_16x16x32_bf16 v[52:55], v[188:191], a[220:223], v[52:55]\n"
    "s_waitcnt lgkmcnt(4)\n"
    "buffer_load_dwordx4 v154, s[28:31], s78 offen nt lds\n"
    "v_exp_f32 v65, v65\n"
    "v_mfma_f32_16x16x32_bf16 v[48:51], v[160:163], a[200:203], v[48:51]\n"
    "ds_read_b128 v[184:187], v201 offset:26624\n"
    "v_add_f32 v87, v87, v100\n"
    "v_exp_f32 v68, v68\n"
    "v_mfma_f32_16x16x32_bf16 v[48:51], v[164:167], a[204:207], v[48:51]\n"
    "v_exp_f32 v66, v66\n"
    "v_exp_f32 v69, v69\n"
    "v_mfma_f32_16x16x32_bf16 v[52:55], v[160:163], a[224:227], v[52:55]\n"
    "ds_read_b128 v[188:191], v201 offset:27648\n"
    "v_exp_f32 v67, v67\n"
    "ds_read_u16_d16_hi v225, v210 offset:560\n"
    "v_mfma_f32_16x16x32_bf16 v[52:55], v[164:167], a[228:231], v[52:55]\n"
    "s_waitcnt lgkmcnt(5)\n"
    "v_exp_f32 v70, v70\n"
    "v_exp_f32 v80, v80\n"
    "v_mfma_f32_16x16x32_bf16 v[48:51], v[168:171], a[208:211], v[48:51]\n"
    "ds_read_b128 v[160:163], v201 offset:28672\n"
    "v_exp_f32 v71, v71\n"
    "ds_read_u16_d16_hi v224, v210 offset:48\n"
    "v_mfma_f32_16x16x32_bf16 v[48:51], v[172:175], a[212:215], v[48:51]\n"
    "v_exp_f32 v81, v81\n"
    "v_exp_f32 v84, v84\n"
    "v_mfma_f32_16x16x32_bf16 v[52:55], v[168:171], a[232:235], v[52:55]\n"
    "ds_read_b128 v[164:167], v201 offset:29696\n"
    "v_exp_f32 v82, v82\n"
    "ds_read2st64_b32 v[240:241], v207 offset0:96 offset1:99\n"
    "v_exp_f32 v83, v83\n"
    "v_cvt_pk_bf16_f32 v144, v64, v65\n"
    "v_cvt_pk_bf16_f32 v145, v66, v67\n"
    "v_cvt_pk_bf16_f32 v146, v80, v81\n"
    "v_cvt_pk_bf16_f32 v147, v82, v83\n"
    "v_mfma_f32_16x16x32_bf16 v[52:55], v[172:175], a[236:239], v[52:55]\n"
    "s_waitcnt lgkmcnt(7)\n"
    "v_exp_f32 v85, v85\n"
    "v_exp_f32 v86, v86\n"
    "v_mfma_f32_16x16x32_bf16 a[0:3], v[176:179], v[128:131], 0\n"
    "ds_read_b128 v[168:171], v201 offset:30720\n"
    "ds_read2st64_b32 v[242:243], v207 offset0:102 offset1:105\n"
    "v_exp_f32 v87, v87\n"
    "v_cvt_pk_bf16_f32 v148, v68, v69\n"
    "v_cvt_pk_bf16_f32 v149, v70, v71\n"
    "v_cvt_pk_bf16_f32 v150, v84, v85\n"
    "v_cvt_pk_bf16_f32 v151, v86, v87\n"
    "v_mfma_f32_16x16x32_bf16 a[0:3], v[180:183], v[144:147], a[0:3]\n"
    "s_add_u32 s78, s88, 0x4800\n"
    "ds_read2st64_b32 v[244:245], v207 offset0:108 offset1:111\n"
    "ds_read2st64_b32 v[246:247], v207 offset0:114 offset1:117\n"
    "v_mfma_f32_16x16x32_bf16 a[4:7], v[176:179], v[132:135], 0\n"
    "s_add_u32 m0, s71, 0x6c00\n"
    "ds_read_b128 v[172:175], v201 offset:31744\n"
    "ds_read_u16_d16_hi v223, v209 offset:6912\n"
    "v_mfma_f32_16x16x32_bf16 a[4:7], v[180:183], v[148:151], a[4:7]\n"
    "s_waitcnt lgkmcnt(11)\n"
    "buffer_load_dwordx4 v155, s[28:31], s78 offen nt lds\n"
    "v_mfma_f32_16x16x32_bf16 a[16:19], v[184:187], v[128:131], 0\n"
    "ds_read_b128 v[176:179], v201 offset:32768\n"
    "ds_read_u16_d16_hi v222, v209 offset:768\n"
    "v_mfma_f32_16x16x32_bf16 a[16:19], v[188:191], v[144:147], a[16:19]\n"
    "v_exp_f32 v32, v32\n"
    "v_mfma_f32_16x16x32_bf16 a[20:23], v[184:187], v[132:135], 0\n"
    "ds_read_b128 v[180:183], v201 offset:33792\n"
    "ds_read_b128 v[232:235], v205 offset:24576\n"
    "v_mfma_f32_16x16x32_bf16 a[20:23], v[188:191], v[148:151], a[20:23]\n"
    "s_waitcnt lgkmcnt(11)\n"
    "ds_read_b128 v[236:239], v206 offset:24576\n"
    "v_mfma_f32_16x16x32_bf16 a[32:35], v[160:163], v[128:131], 0\n"
    "ds_read_b128 v[184:187], v201 offset:34816\n"
    "v_mul_f32 v228, s68, v225\n"
    "v_mfma_f32_16x16x32_bf16 a[32:35], v[164:167], v[144:147], a[32:35]\n"
    "v_exp_f32 v33, v33\n"
    "v_mfma_f32_16x16x32_bf16 a[36:39], v[160:163], v[132:135], 0\n"
    "ds_read_b128 v[188:191], v201 offset:35840\n"
    "v_fma_f32 v229, -v224, v228, s69\n"
    "v_mfma_f32_16x16x32_bf16 a[36:39], v[164:167], v[148:151], a[36:39]\n"
    "v_exp_f32 v34, v34\n"
    "v_mfma_f32_16x16x32_bf16 v[112:115], v[192:195], v[128:131], 0\n"
    "s_waitcnt lgkmcnt(13)\n"
    "v_cvt_f32_i32 v240, v240\n"
    "v_cvt_f32_i32 v241, v241\n"
    "v_mfma_f32_16x16x32_bf16 v[112:115], v[192:195], v[144:147], v[112:115]\n"
    "s_waitcnt lgkmcnt(8)\n"
    "s_add_u32 s78, s88, 0x4800\n"
    "v_cvt_f32_i32 v242, v242\n"
    "v_mfma_f32_16x16x32_bf16 a[48:51], v[168:171], v[128:131], 0\n"
    "s_add_u32 m0, s71, 0x7000\n"
    "ds_read_b128 v[160:163], v201 offset:36864\n"
    "v_cvt_f32_i32 v243, v243\n"
    "v_mfma_f32_16x16x32_bf16 a[48:51], v[172:175], v[144:147], a[48:51]\n"
    "buffer_load_dwordx4 v156, s[28:31], s78 offen nt lds\n"
    "v_mfma_f32_16x16x32_bf16 a[52:55], v[168:171], v[132:135], 0\n"
    "ds_read_b128 v[164:167], v201 offset:37888\n"
    "v_cvt_f32_i32 v244, v244\n"
    "v_mfma_f32_16x16x32_bf16 a[52:55], v[172:175], v[148:151], a[52:55]\n"
    "v_exp_f32 v35, v35\n"
    "v_mfma_f32_16x16x32_bf16 v[116:119], v[192:195], v[132:135], 0\n"
    "v_exp_f32 v36, v36\n"
    "v_mfma_f32_16x16x32_bf16 v[116:119], v[192:195], v[148:151], v[116:119]\n"
    "s_waitcnt lgkmcnt(6)\n"
    "v_exp_f32 v37, v37\n"
    "v_mfma_f32_16x16x32_bf16 a[64:67], v[176:179], v[128:131], 0\n"
    "ds_read_b128 v[168:171], v201 offset:38912\n"
    "v_cvt_f32_i32 v245, v245\n"
    "v_mfma_f32_16x16x32_bf16 a[64:67], v[180:183], v[144:147], a[64:67]\n"
    "v_cvt_f32_i32 v246, v246\n"
    "v_mul_f32 v226, s75, v223\n"
    "v_mfma_f32_16x16x32_bf16 a[68:71], v[176:179], v[132:135], 0\n"
    "ds_read_b128 v[172:175], v201 offset:39936\n"
    "v_cvt_f32_i32 v247, v247\n"
    "v_mfma_f32_16x16x32_bf16 a[68:71], v[180:183], v[148:151], a[68:71]\n"
    "s_waitcnt lgkmcnt(4)\n"
    "v_exp_f32 v38, v38\n"
    "v_mfma_f32_16x16x32_bf16 a[80:83], v[184:187], v[128:131], 0\n"
    "ds_read_b128 v[176:179], v201 offset:40960\n"
    "v_fma_f32 v227, -v222, v226, v217\n"
    "v_mfma_f32_16x16x32_bf16 a[80:83], v[188:191], v[144:147], a[80:83]\n"
    "s_add_u32 s78, s88, 0x4800\n"
    "v_mul_f32_dpp v240, v228, v240 row_newbcast:0 row_mask:0xf bank_mask:0xf\n"
    "v_cvt_f32_i32 v232, v232\n"
    "v_mfma_f32_16x16x32_bf16 a[84:87], v[184:187], v[132:135], 0\n"
    "s_add_u32 m0, s71, 0x7400\n"
    "ds_read_b128 v[180:183], v201 offset:41984\n"
    "v_mul_f32_dpp v241, v228, v241 row_newbcast:1 row_mask:0xf bank_mask:0xf\n"
    "v_mfma_f32_16x16x32_bf16 a[84:87], v[188:191], v[148:151], a[84:87]\n"
    "s_waitcnt lgkmcnt(4)\n"
    "buffer_load_dwordx4 v157, s[28:31], s78 offen nt lds\n"
    "v_mfma_f32_16x16x32_bf16 a[96:99], v[160:163], v[128:131], 0\n"
    "ds_read_b128 v[184:187], v201 offset:43008\n"
    "v_cvt_f32_i32 v233, v233\n"
    "v_mfma_f32_16x16x32_bf16 a[96:99], v[164:167], v[144:147], a[96:99]\n"
    "v_exp_f32 v39, v39\n"
    "v_mfma_f32_16x16x32_bf16 a[100:103], v[160:163], v[132:135], 0\n"
    "ds_read_b128 v[188:191], v201 offset:44032\n"
    "v_mul_f32_dpp v242, v228, v242 row_newbcast:2 row_mask:0xf bank_mask:0xf\n"
    "v_mfma_f32_16x16x32_bf16 a[100:103], v[164:167], v[148:151], a[100:103]\n"
    "s_waitcnt lgkmcnt(4)\n"
    "v_exp_f32 v48, v48\n"
    "v_mfma_f32_16x16x32_bf16 a[112:115], v[168:171], v[128:131], 0\n"
    "ds_read_b128 v[160:163], v201 offset:45056\n"
    "v_cvt_f32_i32 v234, v234\n"
    "v_mfma_f32_16x16x32_bf16 a[112:115], v[172:175], v[144:147], a[112:115]\n"
    "v_mul_f32_dpp v243, v228, v243 row_newbcast:3 row_mask:0xf bank_mask:0xf\n"
    "v_cvt_f32_i32 v235, v235\n"
    "v_mfma_f32_16x16x32_bf16 a[116:119], v[168:171], v[132:135], 0\n"
    "ds_read_b128 v[164:167], v201 offset:46080\n"
    "v_mul_f32_dpp v244, v228, v244 row_newbcast:4 row_mask:0xf bank_mask:0xf\n"
    "v_mfma_f32_16x16x32_bf16 a[116:119], v[172:175], v[148:151], a[116:119]\n"
    "s_waitcnt lgkmcnt(4)\n"
    "v_exp_f32 v49, v49\n"
    "v_mfma_f32_16x16x32_bf16 a[128:131], v[176:179], v[128:131], 0\n"
    "ds_read_b128 v[168:171], v201 offset:47104\n"
    "v_cvt_f32_i32 v236, v236\n"
    "v_mfma_f32_16x16x32_bf16 a[128:131], v[180:183], v[144:147], a[128:131]\n"
    "v_mul_f32_dpp v245, v228, v245 row_newbcast:5 row_mask:0xf bank_mask:0xf\n"
    "v_cvt_f32_i32 v237, v237\n"
    "v_mfma_f32_16x16x32_bf16 a[132:135], v[176:179], v[132:135], 0\n"
    "ds_read_b128 v[172:175], v201 offset:48128\n"
    "v_mul_f32_dpp v246, v228, v246 row_newbcast:6 row_mask:0xf bank_mask:0xf\n"
    "v_mfma_f32_16x16x32_bf16 a[132:135], v[180:183], v[148:151], a[132:135]\n"
    "s_waitcnt lgkmcnt(4)\n"
    "v_exp_f32 v50, v50\n"
    "v_mfma_f32_16x16x32_bf16 a[144:147], v[184:187], v[128:131], 0\n"
    "v_cvt_f32_i32 v238, v238\n"
    "v_mul_f32_dpp v247, v228, v247 row_newbcast:7 row_mask:0xf bank_mask:0xf\n"
    "v_mfma_f32_16x16x32_bf16 a[144:147], v[188:191], v[144:147], a[144:147]\n"
    "v_cvt_f32_i32 v239, v239\n"
    "v_add_f32_dpp v240, v229, v240 row_newbcast:0 row_mask:0xf bank_mask:0xf\n"
    "v_mfma_f32_16x16x32_bf16 a[148:151], v[184:187], v[132:135], 0\n"
    "v_exp_f32 v51, v51\n"
    "v_mfma_f32_16x16x32_bf16 a[148:151], v[188:191], v[148:151], a[148:151]\n"
    "s_waitcnt lgkmcnt(2)\n"
    "v_mul_f32_dpp v232, v226, v232 row_newbcast:0 row_mask:0xf bank_mask:0xf\n"
    "v_add_f32_dpp v241, v229, v241 row_newbcast:1 row_mask:0xf bank_mask:0xf\n"
    "v_mfma_f32_16x16x32_bf16 a[160:163], v[160:163], v[128:131], 0\n"
    "v_mul_f32_dpp v233, v226, v233 row_newbcast:1 row_mask:0xf bank_mask:0xf\n"
    "v_add_f32_dpp v242, v229, v242 row_newbcast:2 row_mask:0xf bank_mask:0xf\n"
    "v_mfma_f32_16x16x32_bf16 a[160:163], v[164:167], v[144:147], a[160:163]\n"
    "v_exp_f32 v52, v52\n"
    "v_mfma_f32_16x16x32_bf16 a[164:167], v[160:163], v[132:135], 0\n"
    "v_mul_f32_dpp v234, v226, v234 row_newbcast:2 row_mask:0xf bank_mask:0xf\n"
    "v_add_f32_dpp v243, v229, v243 row_newbcast:3 row_mask:0xf bank_mask:0xf\n"
    "v_mfma_f32_16x16x32_bf16 a[164:167], v[164:167], v[148:151], a[164:167]\n"
    "s_waitcnt lgkmcnt(0)\n"
    "v_mul_f32_dpp v235, v226, v235 row_newbcast:3 row_mask:0xf bank_mask:0xf\n"
    "v_add_f32_dpp v244, v229, v244 row_newbcast:4 row_mask:0xf bank_mask:0xf\n"
    "v_mfma_f32_16x16x32_bf16 a[176:179], v[168:171], v[128:131], 0\n"
    "v_exp_f32 v53, v53\n"
    "v_mfma_f32_16x16x32_bf16 a[176:179], v[172:175], v[144:147], a[176:179]\n"
    "v_mul_f32_dpp v236, v226, v236 row_newbcast:4 row_mask:0xf bank_mask:0xf\n"
    "v_add_f32_dpp v245, v229, v245 row_newbcast:5 row_mask:0xf bank_mask:0xf\n"
    "v_mfma_f32_16x16x32_bf16 a[180:183], v[168:171], v[132:135], 0\n"
    "v_exp_f32 v54, v54\n"
    "v_mul_f32_dpp v237, v226, v237 row_newbcast:5 row_mask:0xf bank_mask:0xf\n"
    "v_add_f32_dpp v246, v229, v246 row_newbcast:6 row_mask:0xf bank_mask:0xf\n"
    "v_mul_f32_dpp v238, v226, v238 row_newbcast:6 row_mask:0xf bank_mask:0xf\n"
    "v_add_f32_dpp v247, v229, v247 row_newbcast:7 row_mask:0xf bank_mask:0xf\n"
    "v_exp_f32 v55, v55\n"
    "v_mul_f32_dpp v239, v226, v239 row_newbcast:7 row_mask:0xf bank_mask:0xf\n"
    "v_cvt_pk_bf16_f32 v240, v240, v241\n"
    "v_add_f32_dpp v232, v227, v232 row_newbcast:0 row_mask:0xf bank_mask:0xf\n"
    "v_cvt_pk_bf16_f32 v241, v242, v243\n"
    "v_add_f32_dpp v233, v227, v233 row_newbcast:1 row_mask:0xf bank_mask:0xf\n"
    "v_cvt_pk_bf16_f32 v242, v244, v245\n"
    "v_add_f32_dpp v234, v227, v234 row_newbcast:2 row_mask:0xf bank_mask:0xf\n"
    "v_cvt_pk_bf16_f32 v243, v246, v247\n"
    "v_add_f32_dpp v235, v227, v235 row_newbcast:3 row_mask:0xf bank_mask:0xf\n"
    "v_add_f32_dpp v236, v227, v236 row_newbcast:4 row_mask:0xf bank_mask:0xf\n"
    "v_add_f32_dpp v237, v227, v237 row_newbcast:5 row_mask:0xf bank_mask:0xf\n"
    "v_add_f32_dpp v238, v227, v238 row_newbcast:6 row_mask:0xf bank_mask:0xf\n"
    "v_add_f32_dpp v239, v227, v239 row_newbcast:7 row_mask:0xf bank_mask:0xf\n"
    "v_cvt_pk_bf16_f32 v232, v232, v233\n"
    "v_cvt_pk_bf16_f32 v233, v234, v235\n"
    "v_cvt_pk_bf16_f32 v234, v236, v237\n"
    "v_cvt_pk_bf16_f32 v235, v238, v239\n"
    "v_mfma_f32_16x16x32_bf16 a[180:183], v[172:175], v[148:151], a[180:183]\n"
    "v_add_f32 v113, v113, v32\n"
    "v_add_f32 v114, v114, v33\n"
    "v_add_f32 v113, v113, v34\n"
    "v_add_f32 v114, v114, v35\n"
    "v_add_f32 v117, v117, v36\n"
    "v_add_f32 v118, v118, v37\n"
    "v_add_f32 v117, v117, v38\n"
    "v_add_f32 v118, v118, v39\n"
    "v_add_f32 v113, v113, v48\n"
    "v_add_f32 v114, v114, v49\n"
    "v_add_f32 v113, v113, v50\n"
    "v_add_f32 v114, v114, v51\n"
    "v_add_f32 v117, v117, v52\n"
    "buffer_store_dwordx4 v[232:235], v211, s[36:39], s62 offen sc1\n"
    "buffer_store_dwordx4 v[240:243], v212, s[40:43], s63 offen sc1\n"
    "v_add_f32 v118, v118, v53\n"
    "v_add_f32 v117, v117, v54\n"
    "v_add_f32 v118, v118, v55\n"
    "s_waitcnt vmcnt(10)\n"
    "ds_read_b128 a[240:243], v158 offset:49152\n"
    "ds_read_b128 a[244:247], v159 offset:49152\n"
    "ds_read_b128 a[248:251], v158 offset:49280\n"
    "ds_read_b128 a[252:255], v159 offset:49280\n"
    "ds_read_b128 v[0:3], v158 offset:49408\n"
    "ds_read_b128 v[4:7], v159 offset:49408\n"
    "s_waitcnt vmcnt(8)\n"
    "v_cmp_ne_u32 s[80:81], s67, v219\n"
    "v_cmp_gt_u32 vcc, s66, v218\n"
    "s_or_b64 vcc, vcc, s[80:81]\n"
    "s_cbranch_vccnz LSPRE1\n"
    "LSPB1:\n"
    "v_mov_b32 v220, 4\n"
    "s_add_u32 s66, s66, s74\n"
    "s_waitcnt lgkmcnt(0)\n"
    "s_barrier\n"
    "buffer_store_dwordx2 v[220:221], v214, s[44:47], 0 offen sc1\n"
    "ds_read_b128 v[160:163], v255\n"
    "ds_read_b128 v[164:167], v255 offset:1024\n"
    "ds_read_b128 v[168:171], v255 offset:2048\n"
    "ds_read_b128 v[172:175], v255 offset:3072\n"
    "ds_read_b128 v[176:179], v255 offset:4096\n"
    "ds_read_b128 v[180:183], v255 offset:5120\n"
    "s_waitcnt lgkmcnt(4)\n"
    "v_mfma_f32_16x16x32_bf16 v[44:47], v[160:163], a[240:243], 0\n"
    "ds_read_b128 v[184:187], v255 offset:6144\n"
    "v_mfma_f32_16x16x32_bf16 v[44:47], v[164:167], a[244:247], v[44:47]\n"
    "s_waitcnt lgkmcnt(3)\n"
    "ds_read_b128 v[188:191], v255 offset:7168\n"
    "v_mfma_f32_16x16x32_bf16 v[44:47], v[168:171], a[248:251], v[44:47]\n"
    "s_add_u32 m0, s71, 0xc000\n"
    "ds_read_b128 v[160:163], v255 offset:8192\n"
    "v_mfma_f32_16x16x32_bf16 v[44:47], v[172:175], a[252:255], v[44:47]\n"
    "s_waitcnt lgkmcnt(3)\n"
    "ds_read_b128 v[164:167], v255 offset:9216\n"
    "buffer_load_dwordx4 v200, s[40:43], s61 offen sc1 lds\n"
    "v_mfma_f32_16x16x32_bf16 v[44:47], v[176:179], v[0:3], v[44:47]\n"
    "ds_read_b128 v[168:171], v255 offset:10240\n"
    "v_mfma_f32_16x16x32_bf16 v[44:47], v[180:183], v[4:7], v[44:47]\n"
    "s_waitcnt lgkmcnt(3)\n"
    "ds_read_b128 v[172:175], v255 offset:11264\n"
    "v_mfma_f32_16x16x32_bf16 v[60:63], v[184:187], a[240:243], 0\n"
    "ds_read_b128 v[176:179], v200\n"
    "v_mfma_f32_16x16x32_bf16 v[60:63], v[188:191], a[244:247], v[60:63]\n"
    "s_waitcnt lgkmcnt(3)\n"
    "ds_read_b128 v[180:183], v200 offset:1024\n"
    "v_mfma_f32_16x16x32_bf16 v[60:63], v[160:163], a[248:251], v[60:63]\n"
    "ds_read_b128 v[184:187], v200 offset:2048\n"
    "buffer_load_dwordx4 v200, s[40:43], s61 offen offset:1024 sc1 lds\n"
    "v_mfma_f32_16x16x32_bf16 v[60:63], v[164:167], a[252:255], v[60:63]\n"
    "s_waitcnt lgkmcnt(3)\n"
    "ds_read_b128 v[188:191], v200 offset:3072\n"
    "v_mfma_f32_16x16x32_bf16 v[60:63], v[168:171], v[0:3], v[60:63]\n"
    "ds_read_b128 v[160:163], v200 offset:4096\n"
    "v_mfma_f32_16x16x32_bf16 v[60:63], v[172:175], v[4:7], v[60:63]\n"
    "s_waitcnt lgkmcnt(3)\n"
    "ds_read_b128 v[164:167], v200 offset:5120\n"
    "v_mfma_f32_16x16x32_bf16 v[72:75], v[176:179], a[240:243], 0\n"
    "ds_read_b128 v[168:171], v200 offset:6144\n"
    "v_mfma_f32_16x16x32_bf16 v[72:75], v[180:183], a[244:247], v[72:75]\n"
    "s_waitcnt lgkmcnt(3)\n"
    "ds_read_b128 v[172:175], v200 offset:7168\n"
    "buffer_load_dwordx4 v200, s[40:43], s61 offen offset:2048 sc1 lds\n"
    "v_mfma_f32_16x16x32_bf16 v[72:75], v[184:187], a[248:251], v[72:75]\n"
    "ds_read_b128 v[176:179], v200 offset:8192\n"
    "v_mfma_f32_16x16x32_bf16 v[72:75], v[188:191], a[252:255], v[72:75]\n"
    "s_waitcnt lgkmcnt(3)\n"
    "ds_read_b128 v[180:183], v200 offset:9216\n"
    "v_mfma_f32_16x16x32_bf16 v[72:75], v[160:163], v[0:3], v[72:75]\n"
    "ds_read_b128 v[184:187], v200 offset:10240\n"
    "v_mfma_f32_16x16x32_bf16 v[72:75], v[164:167], v[4:7], v[72:75]\n"
    "s_waitcnt lgkmcnt(3)\n"
    "ds_read_b128 v[188:191], v200 offset:11264\n"
    "v_max3_f32 v196, v44, v45, v46\n"
    "v_max3_f32 v196, v196, v47, v60\n"
    "v_max3_f32 v196, v196, v61, v62\n"
    "v_mfma_f32_16x16x32_bf16 v[88:91], v[168:171], a[240:243], 0\n"
    "ds_read_b128 v[160:163], v200 offset:12288\n"
    "buffer_load_dwordx4 v200, s[40:43], s61 offen offset:3072 sc1 lds\n"
    "v_max_f32 v196, v196, v63\n"
    "v_mfma_f32_16x16x32_bf16 v[88:91], v[172:175], a[244:247], v[88:91]\n"
    "s_waitcnt lgkmcnt(3)\n"
    "ds_read_b128 v[164:167], v200 offset:13312\n"
    "v_mov_b32 v197, v196\n"
    "s_nop 1\n"
    "v_permlane32_swap_b32 v196, v197\n"
    "v_max_f32 v196, v196, v197\n"
    "v_mfma_f32_16x16x32_bf16 v[88:91], v[176:179], a[248:251], v[88:91]\n"
    "ds_read_b128 v[168:171], v200 offset:14336\n"
    "v_mov_b32 v197, v196\n"
    "s_nop 1\n"
    "v_permlane16_swap_b32 v196, v197\n"
    "v_max_f32 v196, v196, v197\n"
    "v_mfma_f32_16x16x32_bf16 v[88:91], v[180:183], a[252:255], v[88:91]\n"
    "s_waitcnt lgkmcnt(3)\n"
    "s_add_u32 s65, s61, 0x1000\n"
    "ds_read_b128 v[172:175], v200 offset:15360\n"
    "v_add_f32 v196, 0x426c0000, v196\n"
    "v_sub_f32 v104, 0, v196\n"
    "v_mfma_f32_16x16x32_bf16 v[88:91], v[184:187], v[0:3], v[88:91]\n"
    "s_add_u32 m0, s71, 0xd000\n"
    "ds_read_b128 v[176:179], v200 offset:16384\n"
    "v_sub_f32 v105, 0, v196\n"
    "v_sub_f32 v106, 0, v196\n"
    "v_sub_f32 v107, 0, v196\n"
    "v_mfma_f32_16x16x32_bf16 v[88:91], v[188:191], v[4:7], v[88:91]\n"
    "s_waitcnt lgkmcnt(3)\n"
    "ds_read_b128 v[180:183], v200 offset:17408\n"
    "buffer_load_dwordx4 v200, s[40:43], s65 offen sc1 lds\n"
    "v_add_f32 v44, v44, v104\n"
    "v_mfma_f32_16x16x32_bf16 v[40:43], v[160:163], a[240:243], v[104:107]\n"
    "ds_read_b128 v[184:187], v200 offset:18432\n"
    "v_add_f32 v45, v45, v104\n"
    "v_add_f32 v46, v46, v104\n"
    "v_add_f32 v47, v47, v104\n"
    "v_mfma_f32_16x16x32_bf16 v[40:43], v[164:167], a[244:247], v[40:43]\n"
    "s_waitcnt lgkmcnt(3)\n"
    "ds_read_b128 v[188:191], v200 offset:19456\n"
    "v_add_f32 v60, v60, v104\n"
    "v_add_f32 v61, v61, v104\n"
    "v_add_f32 v62, v62, v104\n"
    "v_mfma_f32_16x16x32_bf16 v[40:43], v[168:171], a[248:251], v[40:43]\n"
    "ds_read_b128 v[160:163], v200 offset:20480\n"
    "v_add_f32 v63, v63, v104\n"
    "v_exp_f32 v44, v44\n"
    "v_mfma_f32_16x16x32_bf16 v[40:43], v[172:175], a[252:255], v[40:43]\n"
    "s_waitcnt lgkmcnt(3)\n"
    "ds_read_b128 v[164:167], v200 offset:21504\n"
    "v_exp_f32 v45, v45\n"
    "v_mfma_f32_16x16x32_bf16 v[40:43], v[176:179], v[0:3], v[40:43]\n"
    "ds_read_b128 v[168:171], v200 offset:22528\n"
    "buffer_load_dwordx4 v200, s[40:43], s65 offen offset:1024 sc1 lds\n"
    "v_mfma_f32_16x16x32_bf16 v[40:43], v[180:183], v[4:7], v[40:43]\n"
    "s_waitcnt lgkmcnt(3)\n"
    "ds_read_b128 v[172:175], v200 offset:23552\n"
    "v_exp_f32 v46, v46\n"
    "v_mfma_f32_16x16x32_bf16 v[56:59], v[184:187], a[240:243], v[104:107]\n"
    "ds_read_b128 v[176:179], v201 offset:24576\n"
    "v_exp_f32 v47, v47\n"
    "v_mfma_f32_16x16x32_bf16 v[56:59], v[188:191], a[244:247], v[56:59]\n"
    "s_waitcnt lgkmcnt(3)\n"
    "s_add_u32 s78, s64, 0x30000\n"
    "ds_read_b128 v[180:183], v201 offset:25600\n"
    "v_exp_f32 v60, v60\n"
    "v_mfma_f32_16x16x32_bf16 v[56:59], v[160:163], a[248:251], v[56:59]\n"
    "s_add_u32 m0, s72, 0x1b000\n"
    "ds_read_b128 v[184:187], v201 offset:26624\n"
    "v_exp_f32 v61, v61\n"
    "v_mfma_f32_16x16x32_bf16 v[56:59], v[164:167], a[252:255], v[56:59]\n"
    "s_waitcnt lgkmcnt(3)\n"
    "ds_read_b128 v[188:191], v201 offset:27648\n"
    "buffer_load_dwordx4 v202, s[32:35], s78 offen nt lds\n"
    "v_mfma_f32_16x16x32_bf16 v[56:59], v[168:171], v[0:3], v[56:59]\n"
    "ds_read_b128 v[160:163], v201 offset:28672\n"
    "v_exp_f32 v62, v62\n"
    "v_exp_f32 v63, v63\n"
    "v_cvt_pk_bf16_f32 v136, v44, v45\n"
    "v_cvt_pk_bf16_f32 v137, v46, v47\n"
    "v_cvt_pk_bf16_f32 v138, v60, v61\n"
    "v_cvt_pk_bf16_f32 v139, v62, v63\n"
    "v_add_f32 v72, v72, v104\n"
    "v_add_f32 v73, v73, v104\n"
    "v_add_f32 v74, v74, v104\n"
    "v_add_f32 v75, v75, v104\n"
    "v_add_f32 v88, v88, v104\n"
    "v_add_f32 v89, v89, v104\n"
    "v_add_f32 v90, v90, v104\n"
    "v_add_f32 v91, v91, v104\n"
    "v_exp_f32 v72, v72\n"
    "v_exp_f32 v73, v73\n"
    "v_exp_f32 v74, v74\n"
    "v_exp_f32 v75, v75\n"
    "v_exp_f32 v88, v88\n"
    "v_exp_f32 v89, v89\n"
    "v_exp_f32 v90, v90\n"
    "v_exp_f32 v91, v91\n"
    "v_cvt_pk_bf16_f32 v152, v72, v73\n"
    "v_cvt_pk_bf16_f32 v153, v74, v75\n"
    "v_cvt_pk_bf16_f32 v154, v88, v89\n"
    "v_cvt_pk_bf16_f32 v155, v90, v91\n"
    "v_mfma_f32_16x16x32_bf16 v[56:59], v[172:175], v[4:7], v[56:59]\n"
    "s_waitcnt lgkmcnt(3)\n"
    "ds_read_b128 v[164:167], v201 offset:29696\n"
    "v_exp_f32 v40, v40\n"
    "v_mfma_f32_16x16x32_bf16 a[8:11], v[176:179], v[136:139], 0\n"
    "ds_read_b128 v[168:171], v201 offset:30720\n"
    "v_mfma_f32_16x16x32_bf16 a[8:11], v[180:183], v[152:155], a[8:11]\n"
    "s_waitcnt lgkmcnt(3)\n"
    "ds_read_b128 v[172:175], v201 offset:31744\n"
    "v_mfma_f32_16x16x32_bf16 a[24:27], v[184:187], v[136:139], 0\n"
    "ds_read_b128 v[176:179], v201 offset:32768\n"
    "buffer_load_dwordx4 v203, s[32:35], s78 offen offset:1024 nt lds\n"
    "v_mfma_f32_16x16x32_bf16 a[24:27], v[188:191], v[152:155], a[24:27]\n"
    "s_waitcnt lgkmcnt(3)\n"
    "ds_read_b128 v[180:183], v201 offset:33792\n"
    "v_mfma_f32_16x16x32_bf16 a[40:43], v[160:163], v[136:139], 0\n"
    "ds_read_b128 v[184:187], v201 offset:34816\n"
    "v_mfma_f32_16x16x32_bf16 a[40:43], v[164:167], v[152:155], a[40:43]\n"
    "ds_read_b128 v[188:191], v201 offset:35840\n"
    "v_mfma_f32_16x16x32_bf16 v[120:123], v[192:195], v[136:139], 0\n"
    "v_exp_f32 v41, v41\n"
    "v_mfma_f32_16x16x32_bf16 v[120:123], v[192:195], v[152:155], v[120:123]\n"
    "s_waitcnt lgkmcnt(4)\n"
    "buffer_load_dwordx4 v204, s[32:35], s78 offen offset:2048 nt lds\n"
    "v_mfma_f32_16x16x32_bf16 a[56:59], v[168:171], v[136:139], 0\n"
    "ds_read_b128 v[160:163], v201 offset:36864\n"
    "v_mfma_f32_16x16x32_bf16 a[56:59], v[172:175], v[152:155], a[56:59]\n"
    "s_waitcnt lgkmcnt(3)\n"
    "ds_read_b128 v[164:167], v201 offset:37888\n"
    "v_mfma_f32_16x16x32_bf16 a[72:75], v[176:179], v[136:139], 0\n"
    "ds_read_b128 v[168:171], v201 offset:38912\n"
    "v_mfma_f32_16x16x32_bf16 a[72:75], v[180:183], v[152:155], a[72:75]\n"
    "s_waitcnt lgkmcnt(3)\n"
    "ds_read_b128 v[172:175], v201 offset:39936\n"
    "v_mfma_f32_16x16x32_bf16 a[88:91], v[184:187], v[136:139], 0\n"
    "ds_read_b128 v[176:179], v201 offset:40960\n"
    "v_mfma_f32_16x16x32_bf16 a[88:91], v[188:191], v[152:155], a[88:91]\n"
    "s_waitcnt lgkmcnt(3)\n"
    "ds_read_b128 v[180:183], v201 offset:41984\n"
    "v_mfma_f32_16x16x32_bf16 a[104:107], v[160:163], v[136:139], 0\n"
    "ds_read_b128 v[184:187], v201 offset:43008\n"
    "v_mfma_f32_16x16x32_bf16 a[104:107], v[164:167], v[152:155], a[104:107]\n"
    "s_waitcnt lgkmcnt(3)\n"
    "ds_read_b128 v[188:191], v201 offset:44032\n"
    "v_mfma_f32_16x16x32_bf16 a[120:123], v[168:171], v[136:139], 0\n"
    "ds_read_b128 v[160:163], v201 offset:45056\n"
    "v_mfma_f32_16x16x32_bf16 a[120:123], v[172:175], v[152:155], a[120:123]\n"
    "s_waitcnt lgkmcnt(3)\n"
    "ds_read_b128 v[164:167], v201 offset:46080\n"
    "v_mfma_f32_16x16x32_bf16 a[136:139], v[176:179], v[136:139], 0\n"
    "ds_read_b128 v[168:171], v201 offset:47104\n"
    "v_mfma_f32_16x16x32_bf16 a[136:139], v[180:183], v[152:155], a[136:139]\n"
    "s_waitcnt lgkmcnt(3)\n"
    "ds_read_b128 v[172:175], v201 offset:48128\n"
    "v_mfma_f32_16x16x32_bf16 a[152:155], v[184:187], v[136:139], 0\n"
    "v_exp_f32 v42, v42\n"
    "v_mfma_f32_16x16x32_bf16 a[152:155], v[188:191], v[152:155], a[152:155]\n"
    "s_waitcnt lgkmcnt(2)\n"
    "v_exp_f32 v43, v43\n"
    "v_mfma_f32_16x16x32_bf16 a[168:171], v[160:163], v[136:139], 0\n"
    "v_exp_f32 v56, v56\n"
    "v_mfma_f32_16x16x32_bf16 a[168:171], v[164:167], v[152:155], a[168:171]\n"
    "s_waitcnt lgkmcnt(0)\n"
    "v_exp_f32 v57, v57\n"
    "v_mfma_f32_16x16x32_bf16 a[184:187], v[168:171], v[136:139], 0\n"
    "v_exp_f32 v58, v58\n"
    "v_exp_f32 v59, v59\n"
    "v_mfma_f32_16x16x32_bf16 a[184:187], v[172:175], v[152:155], a[184:187]\n"
    "v_add_f32 v121, v121, v40\n"
    "v_add_f32 v122, v122, v41\n"
    "v_add_f32 v121, v121, v42\n"
    "v_add_f32 v122, v122, v43\n"
    "v_add_f32 v121, v121, v56\n"
    "v_add_f32 v122, v122, v57\n"
    "s_waitcnt vmcnt(12)\n"
    "ds_read_b128 v[8:11], v158 offset:24576\n"
    "ds_read_b128 v[12:15], v159 offset:24576\n"
    "ds_read_b128 v[16:19], v158 offset:24704\n"
    "ds_read_b128 v[20:23], v159 offset:24704\n"
    "ds_read_b128 v[24:27], v158 offset:24832\n"
    "ds_read_b128 v[28:31], v159 offset:24832\n"
    "s_waitcnt lgkmcnt(0)\n"
    "ds_read_b128 v[160:163], v255\n"
    "ds_read_b128 v[164:167], v255 offset:1024\n"
    "ds_read_b128 v[168:171], v255 offset:2048\n"
    "ds_read_b128 v[172:175], v255 offset:3072\n"
    "ds_read_b128 v[176:179], v255 offset:4096\n"
    "ds_read_b128 v[180:183], v255 offset:5120\n"
    "s_waitcnt lgkmcnt(4)\n"
    "v_mfma_f32_16x16x32_bf16 v[64:67], v[160:163], v[8:11], 0\n"
    "ds_read_b128 v[184:187], v255 offset:6144\n"
    "v_mfma_f32_16x16x32_bf16 v[64:67], v[164:167], v[12:15], v[64:67]\n"
    "s_waitcnt lgkmcnt(3)\n"
    "s_add_u32 m0, s71, 0x6000\n"
    "ds_read_b128 v[188:191], v255 offset:7168\n"
    "v_mfma_f32_16x16x32_bf16 v[64:67], v[168:171], v[16:19], v[64:67]\n"
    "ds_read_b128 v[160:163], v255 offset:8192\n"
    "buffer_load_dwordx4 v200, s[36:39], s60 offen sc1 lds\n"
    "v_mfma_f32_16x16x32_bf16 v[64:67], v[172:175], v[20:23], v[64:67]\n"
    "s_waitcnt lgkmcnt(3)\n"
    "ds_read_b128 v[164:167], v255 offset:9216\n"
    "v_mfma_f32_16x16x32_bf16 v[64:67], v[176:179], v[24:27], v[64:67]\n"
    "ds_read_b128 v[168:171], v255 offset:10240\n"
    "v_mfma_f32_16x16x32_bf16 v[64:67], v[180:183], v[28:31], v[64:67]\n"
    "s_waitcnt lgkmcnt(3)\n"
    "ds_read_b128 v[172:175], v255 offset:11264\n"
    "buffer_load_dwordx4 v200, s[36:39], s60 offen offset:1024 sc1 lds\n"
    "v_mfma_f32_16x16x32_bf16 v[80:83], v[184:187], v[8:11], 0\n"
    "ds_read_b128 v[176:179], v200\n"
    "v_mfma_f32_16x16x32_bf16 v[80:83], v[188:191], v[12:15], v[80:83]\n"
    "s_waitcnt lgkmcnt(3)\n"
    "ds_read_b128 v[180:183], v200 offset:1024\n"
    "v_mfma_f32_16x16x32_bf16 v[80:83], v[160:163], v[16:19], v[80:83]\n"
    "ds_read_b128 v[184:187], v200 offset:2048\n"
    "buffer_load_dwordx4 v200, s[36:39], s60 offen offset:2048 sc1 lds\n"
    "v_mfma_f32_16x16x32_bf16 v[80:83], v[164:167], v[20:23], v[80:83]\n"
    "s_waitcnt lgkmcnt(3)\n"
    "ds_read_b128 v[188:191], v200 offset:3072\n"
    "v_mfma_f32_16x16x32_bf16 v[80:83], v[168:171], v[24:27], v[80:83]\n"
    "ds_read_b128 v[160:163], v200 offset:4096\n"
    "v_mfma_f32_16x16x32_bf16 v[80:83], v[172:175], v[28:31], v[80:83]\n"
    "s_waitcnt lgkmcnt(3)\n"
    "ds_read_b128 v[164:167], v200 offset:5120\n"
    "buffer_load_dwordx4 v200, s[36:39], s60 offen offset:3072 sc1 lds\n"
    "v_mfma_f32_16x16x32_bf16 v[76:79], v[176:179], v[8:11], 0\n"
    "s_add_u32 s65, s60, 0x1000\n"
    "ds_read_b128 v[168:171], v200 offset:6144\n"
    "v_mfma_f32_16x16x32_bf16 v[76:79], v[180:183], v[12:15], v[76:79]\n"
    "s_waitcnt lgkmcnt(3)\n"
    "s_add_u32 m0, s71, 0x7000\n"
    "ds_read_b128 v[172:175], v200 offset:7168\n"
    "v_mfma_f32_16x16x32_bf16 v[76:79], v[184:187], v[16:19], v[76:79]\n"
    "ds_read_b128 v[176:179], v200 offset:8192\n"
    "buffer_load_dwordx4 v200, s[36:39], s65 offen sc1 lds\n"
    "v_mfma_f32_16x16x32_bf16 v[76:79], v[188:191], v[20:23], v[76:79]\n"
    "s_waitcnt lgkmcnt(3)\n"
    "ds_read_b128 v[180:183], v200 offset:9216\n"
    "v_mfma_f32_16x16x32_bf16 v[76:79], v[160:163], v[24:27], v[76:79]\n"
    "ds_read_b128 v[184:187], v200 offset:10240\n"
    "v_mfma_f32_16x16x32_bf16 v[76:79], v[164:167], v[28:31], v[76:79]\n"
    "s_waitcnt lgkmcnt(3)\n"
    "ds_read_b128 v[188:191], v200 offset:11264\n"
    "buffer_load_dwordx4 v200, s[36:39], s65 offen offset:1024 sc1 lds\n"
    "v_max3_f32 v196, v64, v65, v66\n"
    "v_mfma_f32_16x16x32_bf16 v[92:95], v[168:171], v[8:11], 0\n"
    "ds_read_b128 v[160:163], v200 offset:12288\n"
    "v_max3_f32 v196, v196, v67, v80\n"
    "v_max3_f32 v196, v196, v81, v82\n"
    "v_max_f32 v196, v196, v83\n"
    "v_mfma_f32_16x16x32_bf16 v[92:95], v[172:175], v[12:15], v[92:95]\n"
    "s_waitcnt lgkmcnt(3)\n"
    "ds_read_b128 v[164:167], v200 offset:13312\n"
    "v_mov_b32 v197, v196\n"
    "s_nop 1\n"
    "v_permlane32_swap_b32 v196, v197\n"
    "v_max_f32 v196, v196, v197\n"
    "v_mfma_f32_16x16x32_bf16 v[92:95], v[176:179], v[16:19], v[92:95]\n"
    "ds_read_b128 v[168:171], v200 offset:14336\n"
    "v_mov_b32 v197, v196\n"
    "s_nop 1\n"
    "v_permlane16_swap_b32 v196, v197\n"
    "v_max_f32 v196, v196, v197\n"
    "v_mfma_f32_16x16x32_bf16 v[92:95], v[180:183], v[20:23], v[92:95]\n"
    "s_waitcnt lgkmcnt(3)\n"
    "ds_read_b128 v[172:175], v200 offset:15360\n"
    "v_add_f32 v196, 0x426c0000, v196\n"
    "v_sub_f32 v108, 0, v196\n"
    "v_sub_f32 v109, 0, v196\n"
    "v_mfma_f32_16x16x32_bf16 v[92:95], v[184:187], v[24:27], v[92:95]\n"
    "ds_read_b128 v[176:179], v200 offset:16384\n"
    "v_sub_f32 v110, 0, v196\n"
    "v_sub_f32 v111, 0, v196\n"
    "v_add_f32 v64, v64, v108\n"
    "v_mfma_f32_16x16x32_bf16 v[92:95], v[188:191], v[28:31], v[92:95]\n"
    "s_waitcnt lgkmcnt(3)\n"
    "ds_read_b128 v[180:183], v200 offset:17408\n"
    "v_add_f32 v65, v65, v108\n"
    "v_add_f32 v66, v66, v108\n"
    "v_add_f32 v67, v67, v108\n"
    "v_mfma_f32_16x16x32_bf16 v[44:47], v[160:163], v[8:11], v[108:111]\n"
    "ds_read_b128 v[184:187], v200 offset:18432\n"
    "v_add_f32 v80, v80, v108\n"
    "v_add_f32 v81, v81, v108\n"
    "v_add_f32 v82, v82, v108\n"
    "v_mfma_f32_16x16x32_bf16 v[44:47], v[164:167], v[12:15], v[44:47]\n"
    "s_waitcnt lgkmcnt(3)\n"
    "ds_read_b128 v[188:191], v200 offset:19456\n"
    "v_add_f32 v83, v83, v108\n"
    "v_exp_f32 v64, v64\n"
    "v_mfma_f32_16x16x32_bf16 v[44:47], v[168:171], v[16:19], v[44:47]\n"
    "ds_read_b128 v[160:163], v200 offset:20480\n"
    "buffer_load_dwordx2 v[218:219], v213, s[44:47], 0 offen sc1\n"
    "v_mfma_f32_16x16x32_bf16 v[44:47], v[172:175], v[20:23], v[44:47]\n"
    "s_waitcnt lgkmcnt(3)\n"
    "ds_read_b128 v[164:167], v200 offset:21504\n"
    "v_exp_f32 v65, v65\n"
    "v_mfma_f32_16x16x32_bf16 v[44:47], v[176:179], v[24:27], v[44:47]\n"
    "ds_read_b128 v[168:171], v200 offset:22528\n"
    "v_exp_f32 v66, v66\n"
    "v_mfma_f32_16x16x32_bf16 v[44:47], v[180:183], v[28:31], v[44:47]\n"
    "s_waitcnt lgkmcnt(3)\n"
    "ds_read_b128 v[172:175], v200 offset:23552\n"
    "v_exp_f32 v67, v67\n"
    "v_mfma_f32_16x16x32_bf16 v[60:63], v[184:187], v[8:11], v[108:111]\n"
    "ds_read_b128 v[176:179], v201 offset:24576\n"
    "v_exp_f32 v80, v80\n"
    "v_mfma_f32_16x16x32_bf16 v[60:63], v[188:191], v[12:15], v[60:63]\n"
    "s_waitcnt lgkmcnt(3)\n"
    "ds_read_b128 v[180:183], v201 offset:25600\n"
    "v_exp_f32 v81, v81\n"
    "v_mfma_f32_16x16x32_bf16 v[60:63], v[160:163], v[16:19], v[60:63]\n"
    "ds_read_b128 v[184:187], v201 offset:26624\n"
    "v_exp_f32 v82, v82\n"
    "v_mfma_f32_16x16x32_bf16 v[60:63], v[164:167], v[20:23], v[60:63]\n"
    "s_waitcnt lgkmcnt(3)\n"
    "ds_read_b128 v[188:191], v201 offset:27648\n"
    "v_exp_f32 v83, v83\n"
    "v_cvt_pk_bf16_f32 v140, v64, v65\n"
    "v_mfma_f32_16x16x32_bf16 v[60:63], v[168:171], v[24:27], v[60:63]\n"
    "ds_read_b128 v[160:163], v201 offset:28672\n"
    "v_cvt_pk_bf16_f32 v141, v66, v67\n"
    "v_cvt_pk_bf16_f32 v142, v80, v81\n"
    "v_cvt_pk_bf16_f32 v143, v82, v83\n"
    "v_add_f32 v76, v76, v108\n"
    "v_add_f32 v77, v77, v108\n"
    "v_add_f32 v78, v78, v108\n"
    "v_add_f32 v79, v79, v108\n"
    "v_add_f32 v92, v92, v108\n"
    "v_add_f32 v93, v93, v108\n"
    "v_add_f32 v94, v94, v108\n"
    "v_add_f32 v95, v95, v108\n"
    "v_exp_f32 v76, v76\n"
    "v_exp_f32 v77, v77\n"
    "v_exp_f32 v78, v78\n"
    "v_exp_f32 v79, v79\n"
    "v_exp_f32 v92, v92\n"
    "v_exp_f32 v93, v93\n"
    "v_exp_f32 v94, v94\n"
    "v_exp_f32 v95, v95\n"
    "v_cvt_pk_bf16_f32 v156, v76, v77\n"
    "v_cvt_pk_bf16_f32 v157, v78, v79\n"
    "v_cvt_pk_bf16_f32 v158, v92, v93\n"
    "v_cvt_pk_bf16_f32 v159, v94, v95\n"
    "v_mfma_f32_16x16x32_bf16 v[60:63], v[172:175], v[28:31], v[60:63]\n"
    "s_waitcnt lgkmcnt(3)\n"
    "ds_read_b128 v[164:167], v201 offset:29696\n"
    "v_exp_f32 v44, v44\n"
    "v_mfma_f32_16x16x32_bf16 a[12:15], v[176:179], v[140:143], 0\n"
    "ds_read_b128 v[168:171], v201 offset:30720\n"
    "v_mfma_f32_16x16x32_bf16 a[12:15], v[180:183], v[156:159], a[12:15]\n"
    "s_waitcnt lgkmcnt(3)\n"
    "ds_read_b128 v[172:175], v201 offset:31744\n"
    "v_mfma_f32_16x16x32_bf16 a[28:31], v[184:187], v[140:143], 0\n"
    "ds_read_b128 v[176:179], v201 offset:32768\n"
    "v_mfma_f32_16x16x32_bf16 a[28:31], v[188:191], v[156:159], a[28:31]\n"
    "s_waitcnt lgkmcnt(3)\n"
    "ds_read_b128 v[180:183], v201 offset:33792\n"
    "v_mfma_f32_16x16x32_bf16 a[44:47], v[160:163], v[140:143], 0\n"
    "ds_read_b128 v[184:187], v201 offset:34816\n"
    "v_mfma_f32_16x16x32_bf16 a[44:47], v[164:167], v[156:159], a[44:47]\n"
    "ds_read_b128 v[188:191], v201 offset:35840\n"
    "v_mfma_f32_16x16x32_bf16 v[124:127], v[192:195], v[140:143], 0\n"
    "v_exp_f32 v45, v45\n"
    "v_mfma_f32_16x16x32_bf16 v[124:127], v[192:195], v[156:159], v[124:127]\n"
    "s_waitcnt lgkmcnt(4)\n"
    "v_exp_f32 v46, v46\n"
    "v_mfma_f32_16x16x32_bf16 a[60:63], v[168:171], v[140:143], 0\n"
    "ds_read_b128 v[160:163], v201 offset:36864\n"
    "v_mfma_f32_16x16x32_bf16 a[60:63], v[172:175], v[156:159], a[60:63]\n"
    "s_waitcnt lgkmcnt(3)\n"
    "ds_read_b128 v[164:167], v201 offset:37888\n"
    "v_mfma_f32_16x16x32_bf16 a[76:79], v[176:179], v[140:143], 0\n"
    "ds_read_b128 v[168:171], v201 offset:38912\n"
    "v_mfma_f32_16x16x32_bf16 a[76:79], v[180:183], v[156:159], a[76:79]\n"
    "s_waitcnt lgkmcnt(3)\n"
    "ds_read_b128 v[172:175], v201 offset:39936\n"
    "v_mfma_f32_16x16x32_bf16 a[92:95], v[184:187], v[140:143], 0\n"
    "ds_read_b128 v[176:179], v201 offset:40960\n"
    "v_mfma_f32_16x16x32_bf16 a[92:95], v[188:191], v[156:159], a[92:95]\n"
    "s_waitcnt lgkmcnt(3)\n"
    "ds_read_b128 v[180:183], v201 offset:41984\n"
    "v_mfma_f32_16x16x32_bf16 a[108:111], v[160:163], v[140:143], 0\n"
    "ds_read_b128 v[184:187], v201 offset:43008\n"
    "v_mfma_f32_16x16x32_bf16 a[108:111], v[164:167], v[156:159], a[108:111]\n"
    "s_waitcnt lgkmcnt(3)\n"
    "ds_read_b128 v[188:191], v201 offset:44032\n"
    "v_mfma_f32_16x16x32_bf16 a[124:127], v[168:171], v[140:143], 0\n"
    "ds_read_b128 v[160:163], v201 offset:45056\n"
    "v_mfma_f32_16x16x32_bf16 a[124:127], v[172:175], v[156:159], a[124:127]\n"
    "s_waitcnt lgkmcnt(3)\n"
    "ds_read_b128 v[164:167], v201 offset:46080\n"
    "v_mfma_f32_16x16x32_bf16 a[140:143], v[176:179], v[140:143], 0\n"
    "ds_read_b128 v[168:171], v201 offset:47104\n"
    "v_mfma_f32_16x16x32_bf16 a[140:143], v[180:183], v[156:159], a[140:143]\n"
    "s_waitcnt lgkmcnt(3)\n"
    "ds_read_b128 v[172:175], v201 offset:48128\n"
    "v_mfma_f32_16x16x32_bf16 a[156:159], v[184:187], v[140:143], 0\n"
    "v_exp_f32 v47, v47\n"
    "v_mfma_f32_16x16x32_bf16 a[156:159], v[188:191], v[156:159], a[156:159]\n"
    "s_waitcnt lgkmcnt(2)\n"
    "v_exp_f32 v60, v60\n"
    "v_mfma_f32_16x16x32_bf16 a[172:175], v[160:163], v[140:143], 0\n"
    "v_exp_f32 v61, v61\n"
    "v_mfma_f32_16x16x32_bf16 a[172:175], v[164:167], v[156:159], a[172:175]\n"
    "s_waitcnt lgkmcnt(0)\n"
    "v_exp_f32 v62, v62\n"
    "v_mfma_f32_16x16x32_bf16 a[188:191], v[168:171], v[140:143], 0\n"
    "v_exp_f32 v63, v63\n"
    "v_mfma_f32_16x16x32_bf16 a[188:191], v[172:175], v[156:159], a[188:191]\n"
    "v_add_f32 v125, v125, v44\n"
    "v_add_f32 v126, v126, v45\n"
    "v_add_f32 v125, v125, v46\n"
    "v_add_f32 v126, v126, v47\n"
    "s_add_u32 s64, s64, 0x3c000\n"
    "s_add_u32 s62, s62, 0x6000\n"
    "s_add_u32 s63, s63, 0x6000\n"
    "s_add_u32 s60, s60, 0x6000\n"
    "s_add_u32 s61, s61, 0x6000\n"
    "v_add_u32 v209, 0x300, v209\n"
    "v_add_u32 v210, 48, v210\n"
    "v_mov_b32 v220, 5\n"
    "s_waitcnt vmcnt(0)\n"
    "v_cmp_ne_u32 s[80:81], s67, v219\n"
    "v_cmp_gt_u32 vcc, s66, v218\n"
    "s_or_b64 vcc, vcc, s[80:81]\n"
    "s_cbranch_vccnz LSPRE2\n"
    "LSPB2:\n"
    "s_barrier\n"
    "s_cmp_eq_u32 s58, 0\n"
    "s_cbranch_scc1 LENT0\n"
    "LENT1:\n"
    "s_mov_b32 s59, 1\n"
    "s_add_u32 m0, s72, 0x21000\n"
    "s_nop 0\n"
    "buffer_load_dwordx4 v250, s[48:51], 0 offen offset:2048 lds\n"
    "s_add_u32 m0, s72, 0x18000\n"
    "s_nop 0\n"
    "buffer_load_dwordx4 v202, s[32:35], s64 offen lds\n"
    "buffer_load_dwordx4 v203, s[32:35], s64 offen offset:1024 lds\n"
    "buffer_load_dwordx4 v204, s[32:35], s64 offen offset:2048 lds\n"
    "s_add_u32 s64, s64, 0xc000\n"
    "s_mov_b32 s94, 0x3000\n"
    "s_mov_b32 s95, 0x6000\n"
    "s_add_u32 s51, s72, 0x18000\n"
    "v_add_u32 v205, 0x3000, v205\n"
    "v_add_u32 v206, 0x3000, v206\n"
    "v_add_u32 v207, 0x3000, v207\n"
    "ds_read_u16_d16_hi v223, v209 offset:6336\n"
    "ds_read_u16_d16_hi v222, v209 offset:192\n"
    "ds_read_b128 v[160:163], v200 offset:24576\n"
    "v_add_f32 v121, v121, v58\n"
    "ds_read_b128 v[232:235], v205 offset:24576\n"
    "v_add_f32 v122, v122, v59\n"
    "ds_read_b128 v[164:167], v200 offset:25600\n"
    "ds_read_b128 v[236:239], v206 offset:24576\n"
    "v_add_f32 v125, v125, v60\n"
    "v_add_f32 v126, v126, v61\n"
    "v_add_f32 v125, v125, v62\n"
    "v_add_f32 v126, v126, v63\n"
    "ds_read_u16_d16_hi v225, v210 offset:528\n"
    "ds_read_u16_d16_hi v224, v210 offset:16\n"
    "ds_read_b128 v[168:171], v200 offset:26624\n"
    "ds_read2st64_b32 v[240:241], v207 offset0:96 offset1:99\n"
    "ds_read2st64_b32 v[242:243], v207 offset0:102 offset1:105\n"
    "ds_read2st64_b32 v[244:245], v207 offset0:108 offset1:111\n"
    "ds_read2st64_b32 v[246:247], v207 offset0:114 offset1:117\n"
    "v_cvt_pk_bf16_f32 v140, v44, v45\n"
    "s_waitcnt lgkmcnt(7)\n"
    "v_mul_f32 v226, s75, v223\n"
    "ds_read_b128 v[172:175], v200 offset:27648\n"
    "v_fma_f32 v227, -v222, v226, v217\n"
    "v_cvt_f32_i32 v232, v232\n"
    "v_cvt_f32_i32 v233, v233\n"
    "v_cvt_f32_i32 v234, v234\n"
    "v_cvt_f32_i32 v235, v235\n"
    "s_nop 7\n"
    ".p2align 6\n"
    "LLOOP1:\n"
    "v_mfma_f32_16x16x32_bf16 v[64:67], v[160:163], a[192:195], v[96:99]\n"
    "ds_read_b128 v[176:179], v200 offset:28672\n"
    "v_cvt_f32_i32 v236, v236\n"
    "v_mfma_f32_16x16x32_bf16 v[64:67], v[164:167], a[196:199], v[64:67]\n"
    "v_cvt_f32_i32 v237, v237\n"
    "v_cvt_f32_i32 v238, v238\n"
    "v_mfma_f32_16x16x32_bf16 v[68:71], v[164:167], a[220:223], v[100:103]\n"
    "v_cvt_f32_i32 v239, v239\n"
    "v_mul_f32_dpp v232, v226, v232 row_newbcast:0 row_mask:0xf bank_mask:0xf\n"
    "v_mfma_f32_16x16x32_bf16 v[68:71], v[160:163], a[216:219], v[68:71]\n"
    "v_mul_f32_dpp v233, v226, v233 row_newbcast:1 row_mask:0xf bank_mask:0xf\n"
    "v_mul_f32_dpp v234, v226, v234 row_newbcast:2 row_mask:0xf bank_mask:0xf\n"
    "v_mfma_f32_16x16x32_bf16 v[72:75], v[160:163], a[240:243], v[104:107]\n"
    "ds_read_b128 v[180:183], v200 offset:29696\n"
    "v_mul_f32_dpp v235, v226, v235 row_newbcast:3 row_mask:0xf bank_mask:0xf\n"
    "v_mfma_f32_16x16x32_bf16 v[72:75], v[164:167], a[244:247], v[72:75]\n"
    "v_mul_f32_dpp v236, v226, v236 row_newbcast:4 row_mask:0xf bank_mask:0xf\n"
    "v_mul_f32_dpp v237, v226, v237 row_newbcast:5 row_mask:0xf bank_mask:0xf\n"
    "v_mfma_f32_16x16x32_bf16 v[76:79], v[164:167], v[12:15], v[108:111]\n"
    "v_mul_f32_dpp v238, v226, v238 row_newbcast:6 row_mask:0xf bank_mask:0xf\n"
    "v_mul_f32_dpp v239, v226, v239 row_newbcast:7 row_mask:0xf bank_mask:0xf\n"
    "v_mfma_f32_16x16x32_bf16 v[76:79], v[160:163], v[8:11], v[76:79]\n"
    "s_waitcnt lgkmcnt(2)\n"
    "v_add_f32_dpp v232, v227, v232 row_newbcast:0 row_mask:0xf bank_mask:0xf\n"
    "v_add_f32_dpp v233, v227, v233 row_newbcast:1 row_mask:0xf bank_mask:0xf\n"
    "v_mfma_f32_16x16x32_bf16 v[76:79], v[168:171], v[16:19], v[76:79]\n"
    "ds_read_b128 v[160:163], v200 offset:34816\n"
    "v_add_f32_dpp v234, v227, v234 row_newbcast:2 row_mask:0xf bank_mask:0xf\n"
    "v_mfma_f32_16x16x32_bf16 v[76:79], v[172:175], v[20:23], v[76:79]\n"
    "v_add_f32_dpp v235, v227, v235 row_newbcast:3 row_mask:0xf bank_mask:0xf\n"
    "v_add_f32_dpp v236, v227, v236 row_newbcast:4 row_mask:0xf bank_mask:0xf\n"
    "v_mfma_f32_16x16x32_bf16 v[72:75], v[172:175], a[252:255], v[72:75]\n"
    "v_add_f32_dpp v237, v227, v237 row_newbcast:5 row_mask:0xf bank_mask:0xf\n"
    "v_add_f32_dpp v238, v227, v238 row_newbcast:6 row_mask:0xf bank_mask:0xf\n"
    "v_mfma_f32_16x16x32_bf16 v[72:75], v[168:171], a[248:251], v[72:75]\n"
    "v_add_f32_dpp v239, v227, v239 row_newbcast:7 row_mask:0xf bank_mask:0xf\n"
    "v_cvt_pk_bf16_f32 v232, v232, v233\n"
    "v_mfma_f32_16x16x32_bf16 v[68:71], v[168:171], a[224:227], v[68:71]\n"
    "ds_read_b128 v[164:167], v200 offset:35840\n"
    "v_cvt_pk_bf16_f32 v233, v234, v235\n"
    "v_mfma_f32_16x16x32_bf16 v[68:71], v[172:175], a[228:231], v[68:71]\n"
    "v_cvt_pk_bf16_f32 v234, v236, v237\n"
    "v_cvt_pk_bf16_f32 v235, v238, v239\n"
    "v_mfma_f32_16x16x32_bf16 v[64:67], v[172:175], a[204:207], v[64:67]\n"
    "buffer_store_dwordx4 v[232:235], v211, s[36:39], s62 offen sc1\n"
    "v_mul_f32 v228, s68, v225\n"
    "v_fma_f32 v229, -v224, v228, s69\n"
    "v_cvt_f32_i32 v240, v240\n"
    "v_mfma_f32_16x16x32_bf16 v[64:67], v[168:171], a[200:203], v[64:67]\n"
    "s_waitcnt lgkmcnt(2)\n"
    "v_cvt_f32_i32 v241, v241\n"
    "v_cvt_f32_i32 v242, v242\n"
    "v_mfma_f32_16x16x32_bf16 v[64:67], v[176:179], a[208:211], v[64:67]\n"
    "ds_read_b128 v[168:171], v200 offset:32768\n"
    "v_cvt_f32_i32 v243, v243\n"
    "v_mfma_f32_16x16x32_bf16 v[64:67], v[180:183], a[212:215], v[64:67]\n"
    "v_cvt_f32_i32 v244, v244\n"
    "v_cvt_f32_i32 v245, v245\n"
    "v_mfma_f32_16x16x32_bf16 v[68:71], v[180:183], a[236:239], v[68:71]\n"
    "v_cvt_f32_i32 v246, v246\n"
    "v_cvt_f32_i32 v247, v247\n"
    "v_mfma_f32_16x16x32_bf16 v[68:71], v[176:179], a[232:235], v[68:71]\n"
    "v_mul_f32_dpp v240, v228, v240 row_newbcast:0 row_mask:0xf bank_mask:0xf\n"
    "v_mul_f32_dpp v241, v228, v241 row_newbcast:1 row_mask:0xf bank_mask:0xf\n"
    "v_mfma_f32_16x16x32_bf16 v[72:75], v[176:179], v[0:3], v[72:75]\n"
    "ds_read_b128 v[172:175], v200 offset:33792\n"
    "v_mul_f32_dpp v242, v228, v242 row_newbcast:2 row_mask:0xf bank_mask:0xf\n"
    "v_mfma_f32_16x16x32_bf16 v[72:75], v[180:183], v[4:7], v[72:75]\n"
    "v_mul_f32_dpp v243, v228, v243 row_newbcast:3 row_mask:0xf bank_mask:0xf\n"
    "v_mul_f32_dpp v244, v228, v244 row_newbcast:4 row_mask:0xf bank_mask:0xf\n"
    "v_mfma_f32_16x16x32_bf16 v[76:79], v[180:183], v[28:31], v[76:79]\n"
    "v_mul_f32_dpp v245, v228, v245 row_newbcast:5 row_mask:0xf bank_mask:0xf\n"
    "v_mul_f32_dpp v246, v228, v246 row_newbcast:6 row_mask:0xf bank_mask:0xf\n"
    "v_mfma_f32_16x16x32_bf16 v[76:79], v[176:179], v[24:27], v[76:79]\n"
    "s_waitcnt lgkmcnt(2)\n"
    "v_mul_f32_dpp v247, v228, v247 row_newbcast:7 row_mask:0xf bank_mask:0xf\n"
    "v_add_f32_dpp v240, v229, v240 row_newbcast:0 row_mask:0xf bank_mask:0xf\n"
    "v_mfma_f32_16x16x32_bf16 v[92:95], v[160:163], v[24:27], v[108:111]\n"
    "ds_read_b128 v[176:179], v200 offset:30720\n"
    "v_add_f32_dpp v241, v229, v241 row_newbcast:1 row_mask:0xf bank_mask:0xf\n"
    "v_mfma_f32_16x16x32_bf16 v[92:95], v[164:167], v[28:31], v[92:95]\n"
    "v_add_f32_dpp v242, v229, v242 row_newbcast:2 row_mask:0xf bank_mask:0xf\n"
    "v_add_f32_dpp v243, v229, v243 row_newbcast:3 row_mask:0xf bank_mask:0xf\n"
    "v_mfma_f32_16x16x32_bf16 v[88:91], v[164:167], v[4:7], v[104:107]\n"
    "v_add_f32_dpp v244, v229, v244 row_newbcast:4 row_mask:0xf bank_mask:0xf\n"
    "v_add_f32_dpp v245, v229, v245 row_newbcast:5 row_mask:0xf bank_mask:0xf\n"
    "v_mfma_f32_16x16x32_bf16 v[88:91], v[160:163], v[0:3], v[88:91]\n"
    "v_add_f32_dpp v246, v229, v246 row_newbcast:6 row_mask:0xf bank_mask:0xf\n"
    "v_add_f32_dpp v247, v229, v247 row_newbcast:7 row_mask:0xf bank_mask:0xf\n"
    "v_mfma_f32_16x16x32_bf16 v[84:87], v[160:163], a[232:235], v[100:103]\n"
    "ds_read_b128 v[180:183], v200 offset:31744\n"
    "v_cvt_pk_bf16_f32 v141, v46, v47\n"
    "v_mfma_f32_16x16x32_bf16 v[84:87], v[164:167], a[236:239], v[84:87]\n"
    "v_cvt_pk_bf16_f32 v240, v240, v241\n"
    "v_cvt_pk_bf16_f32 v136, v40, v41\n"
    "v_mfma_f32_16x16x32_bf16 v[80:83], v[164:167], a[212:215], v[96:99]\n"
    "v_cvt_pk_bf16_f32 v241, v242, v243\n"
    "v_cvt_pk_bf16_f32 v137, v42, v43\n"
    "v_mfma_f32_16x16x32_bf16 v[80:83], v[160:163], a[208:211], v[80:83]\n"
    "s_waitcnt lgkmcnt(2)\n"
    "v_cvt_pk_bf16_f32 v242, v244, v245\n"
    "v_cvt_pk_bf16_f32 v132, v36, v37\n"
    "v_mfma_f32_16x16x32_bf16 v[80:83], v[168:171], a[200:203], v[80:83]\n"
    "ds_read_b128 v[160:163], v200 offset:36864\n"
    "v_cvt_pk_bf16_f32 v243, v246, v247\n"
    "v_mfma_f32_16x16x32_bf16 v[80:83], v[172:175], a[204:207], v[80:83]\n"
    "v_cvt_pk_bf16_f32 v133, v38, v39\n"
    "v_cvt_pk_bf16_f32 v128, v32, v33\n"
    "v_mfma_f32_16x16x32_bf16 v[84:87], v[172:175], a[228:231], v[84:87]\n"
    "v_cvt_pk_bf16_f32 v129, v34, v35\n"
    "v_cvt_pk_bf16_f32 v142, v60, v61\n"
    "v_mfma_f32_16x16x32_bf16 v[84:87], v[168:171], a[224:227], v[84:87]\n"
    "s_add_u32 s65, s60, s92\n"
    "buffer_store_dwordx4 v[240:243], v212, s[40:43], s63 offen sc1\n"
    "v_exp_f32 v64, v64\n"
    "v_cvt_pk_bf16_f32 v143, v62, v63\n"
    "v_mfma_f32_16x16x32_bf16 v[88:91], v[168:171], a[248:251], v[88:91]\n"
    "s_add_u32 m0, s90, 0\n"
    "ds_read_b128 v[164:167], v200 offset:37888\n"
    "v_cvt_pk_bf16_f32 v138, v56, v57\n"
    "v_mfma_f32_16x16x32_bf16 v[88:91], v[172:175], a[252:255], v[88:91]\n"
    "v_exp_f32 v65, v65\n"
    "v_mfma_f32_16x16x32_bf16 v[92:95], v[172:175], v[20:23], v[92:95]\n"
    "buffer_load_dwordx4 v200, s[36:39], s65 offen sc1 lds\n"
    "v_mfma_f32_16x16x32_bf16 v[92:95], v[168:171], v[16:19], v[92:95]\n"
    "s_waitcnt lgkmcnt(2)\n"
    "v_exp_f32 v66, v66\n"
    "v_mfma_f32_16x16x32_bf16 v[92:95], v[176:179], v[8:11], v[92:95]\n"
    "ds_read_b128 v[168:171], v200 offset:38912\n"
    "v_cvt_pk_bf16_f32 v139, v58, v59\n"
    "v_mfma_f32_16x16x32_bf16 v[92:95], v[180:183], v[12:15], v[92:95]\n"
    "v_exp_f32 v67, v67\n"
    "v_mfma_f32_16x16x32_bf16 v[88:91], v[180:183], a[244:247], v[88:91]\n"
    "buffer_load_dwordx4 v200, s[36:39], s65 offen offset:1024 sc1 lds\n"
    "v_mfma_f32_16x16x32_bf16 v[88:91], v[176:179], a[240:243], v[88:91]\n"
    "v_exp_f32 v68, v68\n"
    "v_mfma_f32_16x16x32_bf16 v[84:87], v[176:179], a[216:219], v[84:87]\n"
    "ds_read_b128 v[172:175], v200 offset:39936\n"
    "v_cvt_pk_bf16_f32 v134, v52, v53\n"
    "v_mfma_f32_16x16x32_bf16 v[84:87], v[180:183], a[220:223], v[84:87]\n"
    "v_exp_f32 v69, v69\n"
    "v_mfma_f32_16x16x32_bf16 v[80:83], v[180:183], a[196:199], v[80:83]\n"
    "buffer_load_dwordx4 v200, s[36:39], s65 offen offset:2048 sc1 lds\n"
    "v_mfma_f32_16x16x32_bf16 v[80:83], v[176:179], a[192:195], v[80:83]\n"
    "s_waitcnt lgkmcnt(2)\n"
    "v_exp_f32 v70, v70\n"
    "v_mfma_f32_16x16x32_bf16 v[32:35], v[160:163], a[192:195], v[96:99]\n"
    "ds_read_b128 v[176:179], v200 offset:40960\n"
    "v_cvt_pk_bf16_f32 v135, v54, v55\n"
    "v_mfma_f32_16x16x32_bf16 v[32:35], v[164:167], a[196:199], v[32:35]\n"
    "v_exp_f32 v71, v71\n"
    "v_mfma_f32_16x16x32_bf16 v[36:39], v[164:167], a[220:223], v[100:103]\n"
    "buffer_load_dwordx4 v200, s[36:39], s65 offen offset:3072 sc1 lds\n"
    "v_mfma_f32_16x16x32_bf16 v[36:39], v[160:163], a[216:219], v[36:39]\n"
    "s_add_u32 s65, s65, 0x1000\n"
    "v_exp_f32 v72, v72\n"
    "v_mfma_f32_16x16x32_bf16 v[40:43], v[160:163], a[240:243], v[104:107]\n"
    "s_add_u32 m0, s90, 0x1000\n"
    "ds_read_b128 v[180:183], v200 offset:41984\n"
    "v_cvt_pk_bf16_f32 v130, v48, v49\n"
    "v_mfma_f32_16x16x32_bf16 v[40:43], v[164:167], a[244:247], v[40:43]\n"
    "v_exp_f32 v73, v73\n"
    "v_mfma_f32_16x16x32_bf16 v[44:47], v[164:167], v[12:15], v[108:111]\n"
    "buffer_load_dwordx4 v200, s[36:39], s65 offen sc1 lds\n"
    "v_mfma_f32_16x16x32_bf16 v[44:47], v[160:163], v[8:11], v[44:47]\n"
    "s_waitcnt lgkmcnt(2)\n"
    "v_exp_f32 v74, v74\n"
    "v_mfma_f32_16x16x32_bf16 v[44:47], v[168:171], v[16:19], v[44:47]\n"
    "ds_read_b128 v[160:163], v200 offset:47104\n"
    "v_cvt_pk_bf16_f32 v131, v50, v51\n"
    "v_mfma_f32_16x16x32_bf16 v[44:47], v[172:175], v[20:23], v[44:47]\n"
    "v_exp_f32 v75, v75\n"
    "v_mfma_f32_16x16x32_bf16 v[40:43], v[172:175], a[252:255], v[40:43]\n"
    "buffer_load_dwordx4 v200, s[36:39], s65 offen offset:1024 sc1 lds\n"
    "v_mfma_f32_16x16x32_bf16 v[40:43], v[168:171], a[248:251], v[40:43]\n"
    "v_exp_f32 v76, v76\n"
    "v_mfma_f32_16x16x32_bf16 v[36:39], v[168:171], a[224:227], v[36:39]\n"
    "ds_read_b128 v[164:167], v200 offset:48128\n"
    "v_cvt_pk_bf16_f32 v144, v64, v65\n"
    "v_mfma_f32_16x16x32_bf16 v[36:39], v[172:175], a[228:231], v[36:39]\n"
    "v_exp_f32 v77, v77\n"
    "v_mfma_f32_16x16x32_bf16 v[32:35], v[172:175], a[204:207], v[32:35]\n"
    "buffer_load_dwordx4 v200, s[36:39], s65 offen offset:2048 sc1 lds\n"
    "v_mfma_f32_16x16x32_bf16 v[32:35], v[168:171], a[200:203], v[32:35]\n"
    "s_waitcnt lgkmcnt(2)\n"
    "v_exp_f32 v78, v78\n"
    "v_mfma_f32_16x16x32_bf16 v[32:35], v[176:179], a[208:211], v[32:35]\n"
    "s_add_u32 m0, s71, 0x12000\n"
    "ds_read_b128 v[168:171], v200 offset:45056\n"
    "v_cvt_pk_bf16_f32 v145, v66, v67\n"
    "v_mfma_f32_16x16x32_bf16 v[32:35], v[180:183], a[212:215], v[32:35]\n"
    "v_exp_f32 v79, v79\n"
    "v_mfma_f32_16x16x32_bf16 v[36:39], v[180:183], a[236:239], v[36:39]\n"
    "buffer_load_dwordx4 v200, s[40:43], s61 offen sc1 lds\n"
    "v_mfma_f32_16x16x32_bf16 v[36:39], v[176:179], a[232:235], v[36:39]\n"
    "v_exp_f32 v80, v80\n"
    "v_mfma_f32_16x16x32_bf16 v[40:43], v[176:179], v[0:3], v[40:43]\n"
    "ds_read_b128 v[172:175], v200 offset:46080\n"
    "v_cvt_pk_bf16_f32 v148, v68, v69\n"
    "v_mfma_f32_16x16x32_bf16 v[40:43], v[180:183], v[4:7], v[40:43]\n"
    "v_exp_f32 v81, v81\n"
    "v_mfma_f32_16x16x32_bf16 v[44:47], v[180:183], v[28:31], v[44:47]\n"
    "buffer_load_dwordx4 v200, s[40:43], s61 offen offset:1024 sc1 lds\n"
    "v_mfma_f32_16x16x32_bf16 v[44:47], v[176:179], v[24:27], v[44:47]\n"
    "s_waitcnt lgkmcnt(2)\n"
    "v_exp_f32 v82, v82\n"
    "v_mfma_f32_16x16x32_bf16 v[60:63], v[160:163], v[24:27], v[108:111]\n"
    "ds_read_b128 v[176:179], v200 offset:43008\n"
    "v_cvt_pk_bf16_f32 v149, v70, v71\n"
    "v_mfma_f32_16x16x32_bf16 v[60:63], v[164:167], v[28:31], v[60:63]\n"
    "v_exp_f32 v83, v83\n"
    "v_mfma_f32_16x16x32_bf16 v[56:59], v[164:167], v[4:7], v[104:107]\n"
    "buffer_load_dwordx4 v200, s[40:43], s61 offen offset:2048 sc1 lds\n"
    "v_mfma_f32_16x16x32_bf16 v[56:59], v[160:163], v[0:3], v[56:59]\n"
    "v_exp_f32 v84, v84\n"
    "v_mfma_f32_16x16x32_bf16 v[52:55], v[160:163], a[232:235], v[100:103]\n"
    "ds_read_b128 v[180:183], v200 offset:44032\n"
    "v_cvt_pk_bf16_f32 v152, v72, v73\n"
    "v_mfma_f32_16x16x32_bf16 v[52:55], v[164:167], a[236:239], v[52:55]\n"
    "v_exp_f32 v85, v85\n"
    "v_mfma_f32_16x16x32_bf16 v[48:51], v[164:167], a[212:215], v[96:99]\n"
    "buffer_load_dwordx4 v200, s[40:43], s61 offen offset:3072 sc1 lds\n"
    "v_mfma_f32_16x16x32_bf16 v[48:51], v[160:163], a[208:211], v[48:51]\n"
    "s_waitcnt lgkmcnt(2)\n"
    "s_add_u32 s65, s61, 0x1000\n"
    "v_cvt_pk_bf16_f32 v153, v74, v75\n"
    "v_mfma_f32_16x16x32_bf16 v[48:51], v[168:171], a[200:203], v[48:51]\n"
    "s_add_u32 m0, s71, 0x13000\n"
    "ds_read_b128 v[160:163], v201\n"
    "v_cvt_pk_bf16_f32 v156, v76, v77\n"
    "v_mfma_f32_16x16x32_bf16 v[48:51], v[172:175], a[204:207], v[48:51]\n"
    "v_exp_f32 v86, v86\n"
    "v_mfma_f32_16x16x32_bf16 v[52:55], v[172:175], a[228:231], v[52:55]\n"
    "buffer_load_dwordx4 v200, s[40:43], s65 offen sc1 lds\n"
    "v_mfma_f32_16x16x32_bf16 v[52:55], v[168:171], a[224:227], v[52:55]\n"
    "v_exp_f32 v87, v87\n"
    "v_mfma_f32_16x16x32_bf16 v[56:59], v[168:171], a[248:251], v[56:59]\n"
    "ds_read_b128 v[164:167], v201 offset:1024\n"
    "v_cvt_pk_bf16_f32 v157, v78, v79\n"
    "v_mfma_f32_16x16x32_bf16 v[56:59], v[172:175], a[252:255], v[56:59]\n"
    "v_exp_f32 v88, v88\n"
    "v_mfma_f32_16x16x32_bf16 v[60:63], v[172:175], v[20:23], v[60:63]\n"
    "buffer_load_dwordx4 v200, s[40:43], s65 offen offset:1024 sc1 lds\n"
    "v_mfma_f32_16x16x32_bf16 v[60:63], v[168:171], v[16:19], v[60:63]\n"
    "s_waitcnt lgkmcnt(2)\n"
    "v_exp_f32 v89, v89\n"
    "v_mfma_f32_16x16x32_bf16 v[60:63], v[176:179], v[8:11], v[60:63]\n"
    "ds_read_b128 v[168:171], v201 offset:2048\n"
    "v_cvt_pk_bf16_f32 v146, v80, v81\n"
    "v_mfma_f32_16x16x32_bf16 v[60:63], v[180:183], v[12:15], v[60:63]\n"
    "v_exp_f32 v90, v90\n"
    "v_mfma_f32_16x16x32_bf16 v[56:59], v[180:183], a[244:247], v[56:59]\n"
    "v_exp_f32 v91, v91\n"
    "v_mfma_f32_16x16x32_bf16 v[56:59], v[176:179], a[240:243], v[56:59]\n"
    "v_cvt_pk_bf16_f32 v147, v82, v83\n"
    "v_cvt_pk_bf16_f32 v150, v84, v85\n"
    "v_mfma_f32_16x16x32_bf16 v[52:55], v[176:179], a[216:219], v[52:55]\n"
    "ds_read_b128 v[172:175], v201 offset:3072\n"
    "v_cvt_pk_bf16_f32 v151, v86, v87\n"
    "v_mfma_f32_16x16x32_bf16 v[52:55], v[180:183], a[220:223], v[52:55]\n"
    "v_exp_f32 v92, v92\n"
    "v_mfma_f32_16x16x32_bf16 v[48:51], v[180:183], a[196:199], v[48:51]\n"
    "v_exp_f32 v93, v93\n"
    "v_mfma_f32_16x16x32_bf16 v[48:51], v[176:179], a[192:195], v[48:51]\n"
    "s_waitcnt lgkmcnt(2)\n"
    "v_exp_f32 v94, v94\n"
    "v_mfma_f32_16x16x32_bf16 a[0:3], v[160:163], v[128:131], a[0:3]\n"
    "ds_read_b128 v[176:179], v201 offset:4096\n"
    "v_cvt_pk_bf16_f32 v154, v88, v89\n"
    "v_mfma_f32_16x16x32_bf16 a[0:3], v[164:167], v[144:147], a[0:3]\n"
    "v_cvt_pk_bf16_f32 v155, v90, v91\n"
    "v_cvt_pk_bf16_f32 v158, v92, v93\n"
    "v_mfma_f32_16x16x32_bf16 a[4:7], v[160:163], v[132:135], a[4:7]\n"
    "v_exp_f32 v95, v95\n"
    "v_mfma_f32_16x16x32_bf16 a[4:7], v[164:167], v[148:151], a[4:7]\n"
    "v_cvt_pk_bf16_f32 v159, v94, v95\n"
    "v_add_f32 v113, v113, v64\n"
    "v_mfma_f32_16x16x32_bf16 a[8:11], v[160:163], v[136:139], a[8:11]\n"
    "ds_read_b128 v[180:183], v201 offset:5120\n"
    "v_add_f32 v114, v114, v65\n"
    "v_mfma_f32_16x16x32_bf16 a[8:11], v[164:167], v[152:155], a[8:11]\n"
    "v_exp_f32 v32, v32\n"
    "v_mfma_f32_16x16x32_bf16 a[12:15], v[160:163], v[140:143], a[12:15]\n"
    "v_exp_f32 v33, v33\n"
    "v_mfma_f32_16x16x32_bf16 a[12:15], v[164:167], v[156:159], a[12:15]\n"
    "s_waitcnt lgkmcnt(2)\n"
    "v_exp_f32 v34, v34\n"
    "v_mfma_f32_16x16x32_bf16 a[28:31], v[168:171], v[140:143], a[28:31]\n"
    "ds_read_b128 v[160:163], v201 offset:6144\n"
    "v_add_f32 v113, v113, v66\n"
    "v_mfma_f32_16x16x32_bf16 a[28:31], v[172:175], v[156:159], a[28:31]\n"
    "v_exp_f32 v35, v35\n"
    "v_mfma_f32_16x16x32_bf16 a[24:27], v[172:175], v[152:155], a[24:27]\n"
    "v_exp_f32 v36, v36\n"
    "v_mfma_f32_16x16x32_bf16 a[24:27], v[168:171], v[136:139], a[24:27]\n"
    "v_exp_f32 v37, v37\n"
    "v_mfma_f32_16x16x32_bf16 a[20:23], v[168:171], v[132:135], a[20:23]\n"
    "ds_read_b128 v[164:167], v201 offset:7168\n"
    "v_add_f32 v114, v114, v67\n"
    "v_mfma_f32_16x16x32_bf16 a[20:23], v[172:175], v[148:151], a[20:23]\n"
    "v_exp_f32 v38, v38\n"
    "v_mfma_f32_16x16x32_bf16 a[16:19], v[172:175], v[144:147], a[16:19]\n"
    "v_exp_f32 v39, v39\n"
    "v_mfma_f32_16x16x32_bf16 a[16:19], v[168:171], v[128:131], a[16:19]\n"
    "s_waitcnt lgkmcnt(2)\n"
    "v_exp_f32 v40, v40\n"
    "v_mfma_f32_16x16x32_bf16 a[32:35], v[176:179], v[128:131], a[32:35]\n"
    "ds_read_b128 v[168:171], v201 offset:8192\n"
    "v_add_f32 v117, v117, v68\n"
    "v_mfma_f32_16x16x32_bf16 a[32:35], v[180:183], v[144:147], a[32:35]\n"
    "v_exp_f32 v41, v41\n"
    "v_mfma_f32_16x16x32_bf16 a[36:39], v[180:183], v[148:151], a[36:39]\n"
    "v_exp_f32 v42, v42\n"
    "v_mfma_f32_16x16x32_bf16 a[36:39], v[176:179], v[132:135], a[36:39]\n"
    "v_exp_f32 v43, v43\n"
    "v_mfma_f32_16x16x32_bf16 a[40:43], v[176:179], v[136:139], a[40:43]\n"
    "ds_read_b128 v[172:175], v201 offset:9216\n"
    "v_add_f32 v118, v118, v69\n"
    "v_mfma_f32_16x16x32_bf16 a[40:43], v[180:183], v[152:155], a[40:43]\n"
    "v_exp_f32 v44, v44\n"
    "v_mfma_f32_16x16x32_bf16 a[44:47], v[180:183], v[156:159], a[44:47]\n"
    "v_exp_f32 v45, v45\n"
    "v_mfma_f32_16x16x32_bf16 a[44:47], v[176:179], v[140:143], a[44:47]\n"
    "s_waitcnt lgkmcnt(2)\n"
    "v_exp_f32 v46, v46\n"
    "v_mfma_f32_16x16x32_bf16 a[60:63], v[160:163], v[140:143], a[60:63]\n"
    "ds_read_b128 v[176:179], v201 offset:10240\n"
    "v_add_f32 v117, v117, v70\n"
    "v_mfma_f32_16x16x32_bf16 a[60:63], v[164:167], v[156:159], a[60:63]\n"
    "v_exp_f32 v47, v47\n"
    "v_mfma_f32_16x16x32_bf16 a[56:59], v[164:167], v[152:155], a[56:59]\n"
    "v_exp_f32 v48, v48\n"
    "v_mfma_f32_16x16x32_bf16 a[56:59], v[160:163], v[136:139], a[56:59]\n"
    "v_exp_f32 v49, v49\n"
    "v_mfma_f32_16x16x32_bf16 a[52:55], v[160:163], v[132:135], a[52:55]\n"
    "ds_read_b128 v[180:183], v201 offset:11264\n"
    "v_add_f32 v113, v113, v32\n"
    "v_mfma_f32_16x16x32_bf16 a[52:55], v[164:167], v[148:151], a[52:55]\n"
    "v_exp_f32 v50, v50\n"
    "v_mfma_f32_16x16x32_bf16 a[48:51], v[164:167], v[144:147], a[48:51]\n"
    "v_exp_f32 v51, v51\n"
    "v_mfma_f32_16x16x32_bf16 a[48:51], v[160:163], v[128:131], a[48:51]\n"
    "s_waitcnt lgkmcnt(2)\n"
    "v_exp_f32 v52, v52\n"
    "v_mfma_f32_16x16x32_bf16 a[64:67], v[168:171], v[128:131], a[64:67]\n"
    "ds_read_b128 v[160:163], v201 offset:12288\n"
    "v_add_f32 v118, v118, v71\n"
    "v_mfma_f32_16x16x32_bf16 a[64:67], v[172:175], v[144:147], a[64:67]\n"
    "v_add_f32 v114, v114, v33\n"
    "v_add_f32 v121, v121, v72\n"
    "v_mfma_f32_16x16x32_bf16 a[68:71], v[172:175], v[148:151], a[68:71]\n"
    "v_add_f32 v113, v113, v34\n"
    "v_add_f32 v122, v122, v73\n"
    "v_mfma_f32_16x16x32_bf16 a[68:71], v[168:171], v[132:135], a[68:71]\n"
    "s_add_u32 m0, s51, s95\n"
    "v_exp_f32 v53, v53\n"
    "v_mfma_f32_16x16x32_bf16 a[72:75], v[168:171], v[136:139], a[72:75]\n"
    "ds_read_b128 v[164:167], v201 offset:13312\n"
    "v_add_f32 v114, v114, v35\n"
    "v_mfma_f32_16x16x32_bf16 a[72:75], v[172:175], v[152:155], a[72:75]\n"
    "buffer_load_dwordx4 v202, s[32:35], s64 offen lds\n"
    "v_mfma_f32_16x16x32_bf16 a[76:79], v[172:175], v[156:159], a[76:79]\n"
    "v_add_f32 v121, v121, v74\n"
    "v_add_f32 v117, v117, v36\n"
    "v_mfma_f32_16x16x32_bf16 a[76:79], v[168:171], v[140:143], a[76:79]\n"
    "s_waitcnt lgkmcnt(2)\n"
    "v_exp_f32 v54, v54\n"
    "v_mfma_f32_16x16x32_bf16 a[92:95], v[176:179], v[140:143], a[92:95]\n"
    "ds_read_b128 v[168:171], v201 offset:14336\n"
    "v_add_f32 v122, v122, v75\n"
    "v_mfma_f32_16x16x32_bf16 a[92:95], v[180:183], v[156:159], a[92:95]\n"
    "v_add_f32 v118, v118, v37\n"
    "v_add_f32 v125, v125, v76\n"
    "v_mfma_f32_16x16x32_bf16 a[88:91], v[180:183], v[152:155], a[88:91]\n"
    "buffer_load_dwordx4 v203, s[32:35], s64 offen offset:1024 lds\n"
    "v_mfma_f32_16x16x32_bf16 a[88:91], v[176:179], v[136:139], a[88:91]\n"
    "v_add_f32 v117, v117, v38\n"
    "v_add_f32 v126, v126, v77\n"
    "v_mfma_f32_16x16x32_bf16 a[84:87], v[176:179], v[132:135], a[84:87]\n"
    "ds_read_b128 v[172:175], v201 offset:15360\n"
    "v_add_f32 v118, v118, v39\n"
    "v_mfma_f32_16x16x32_bf16 a[84:87], v[180:183], v[148:151], a[84:87]\n"
    "v_exp_f32 v55, v55\n"
    "v_mfma_f32_16x16x32_bf16 a[80:83], v[180:183], v[144:147], a[80:83]\n"
    "v_add_f32 v125, v125, v78\n"
    "v_add_f32 v121, v121, v40\n"
    "v_mfma_f32_16x16x32_bf16 a[80:83], v[176:179], v[128:131], a[80:83]\n"
    "s_waitcnt lgkmcnt(2)\n"
    "buffer_load_dwordx4 v204, s[32:35], s64 offen offset:2048 lds\n"
    "v_mfma_f32_16x16x32_bf16 a[96:99], v[160:163], v[128:131], a[96:99]\n"
    "ds_read_b128 v[176:179], v201 offset:16384\n"
    "v_add_f32 v126, v126, v79\n"
    "v_mfma_f32_16x16x32_bf16 a[96:99], v[164:167], v[144:147], a[96:99]\n"
    "v_exp_f32 v56, v56\n"
    "v_mfma_f32_16x16x32_bf16 a[100:103], v[164:167], v[148:151], a[100:103]\n"
    "v_add_f32 v122, v122, v41\n"
    "v_add_f32 v113, v113, v80\n"
    "v_mfma_f32_16x16x32_bf16 a[100:103], v[160:163], v[132:135], a[100:103]\n"
    "v_add_f32 v121, v121, v42\n"
    "v_add_f32 v114, v114, v81\n"
    "v_mfma_f32_16x16x32_bf16 a[104:107], v[160:163], v[136:139], a[104:107]\n"
    "s_mov_b32 s95, s94\n"
    "ds_read_b128 v[180:183], v201 offset:17408\n"
    "v_add_f32 v122, v122, v43\n"
    "v_mfma_f32_16x16x32_bf16 a[104:107], v[164:167], v[152:155], a[104:107]\n"
    "s_sub_u32 s94, s94, 0x3000\n"
    "v_exp_f32 v57, v57\n"
    "v_mfma_f32_16x16x32_bf16 a[108:111], v[164:167], v[156:159], a[108:111]\n"
    "s_min_u32 s94, s94, 0x6000\n"
    "v_add_f32 v113, v113, v82\n"
    "v_add_f32 v125, v125, v44\n"
    "v_mfma_f32_16x16x32_bf16 a[108:111], v[160:163], v[140:143], a[108:111]\n"
    "s_waitcnt lgkmcnt(2)\n"
    "v_exp_f32 v58, v58\n"
    "v_mfma_f32_16x16x32_bf16 a[124:127], v[168:171], v[140:143], a[124:127]\n"
    "s_sub_u32 s93, s94, s95\n"
    "ds_read_b128 v[160:163], v201 offset:18432\n"
    "v_add_f32 v114, v114, v83\n"
    "v_mfma_f32_16x16x32_bf16 a[124:127], v[172:175], v[156:159], a[124:127]\n"
    "v_add_f32 v126, v126, v45\n"
    "v_add_f32 v117, v117, v84\n"
    "v_mfma_f32_16x16x32_bf16 a[120:123], v[172:175], v[152:155], a[120:123]\n"
    "v_add_f32 v125, v125, v46\n"
    "v_add_f32 v118, v118, v85\n"
    "v_mfma_f32_16x16x32_bf16 a[120:123], v[168:171], v[136:139], a[120:123]\n"
    "v_exp_f32 v59, v59\n"
    "v_mfma_f32_16x16x32_bf16 a[116:119], v[168:171], v[132:135], a[116:119]\n"
    "ds_read_b128 v[164:167], v201 offset:19456\n"
    "v_add_f32 v126, v126, v47\n"
    "v_mfma_f32_16x16x32_bf16 a[116:119], v[172:175], v[148:151], a[116:119]\n"
    "v_add_f32 v117, v117, v86\n"
    "v_add_f32 v113, v113, v48\n"
    "v_mfma_f32_16x16x32_bf16 a[112:115], v[172:175], v[144:147], a[112:115]\n"
    "v_exp_f32 v60, v60\n"
    "v_mfma_f32_16x16x32_bf16 a[112:115], v[168:171], v[128:131], a[112:115]\n"
    "s_waitcnt lgkmcnt(2)\n"
    "v_add_u32 v205, s93, v205\n"
    "v_add_f32 v118, v118, v87\n"
    "v_mfma_f32_16x16x32_bf16 a[128:131], v[176:179], v[128:131], a[128:131]\n"
    "ds_read_b128 v[168:171], v201 offset:20480\n"
    "v_add_f32 v114, v114, v49\n"
    "v_mfma_f32_16x16x32_bf16 a[128:131], v[180:183], v[144:147], a[128:131]\n"
    "v_add_u32 v209, 0x180, v209\n"
    "v_add_u32 v206, s93, v206\n"
    "v_mfma_f32_16x16x32_bf16 a[132:135], v[180:183], v[148:151], a[132:135]\n"
    "s_add_u32 s64, s64, 0xc000\n"
    "v_add_f32 v121, v121, v88\n"
    "v_add_f32 v113, v113, v50\n"
    "v_mfma_f32_16x16x32_bf16 a[132:135], v[176:179], v[132:135], a[132:135]\n"
    "s_add_u32 s62, s62, 0x6000\n"
    "v_exp_f32 v61, v61\n"
    "v_mfma_f32_16x16x32_bf16 a[136:139], v[176:179], v[136:139], a[136:139]\n"
    "s_add_u32 s63, s63, 0x6000\n"
    "ds_read_b128 v[172:175], v201 offset:21504\n"
    "v_add_u32 v210, 32, v210\n"
    "v_mfma_f32_16x16x32_bf16 a[136:139], v[180:183], v[152:155], a[136:139]\n"
    "s_add_u32 s60, s60, 0x6000\n"
    "v_add_u32 v207, s93, v207\n"
    "v_add_f32 v122, v122, v89\n"
    "v_mfma_f32_16x16x32_bf16 a[140:143], v[180:183], v[156:159], a[140:143]\n"
    "s_add_u32 s61, s61, 0x6000\n"
    "v_add_f32 v114, v114, v51\n"
    "v_add_f32 v121, v121, v90\n"
    "v_mfma_f32_16x16x32_bf16 a[140:143], v[176:179], v[140:143], a[140:143]\n"
    "s_waitcnt lgkmcnt(2)\n"
    "v_add_f32 v117, v117, v52\n"
    "v_add_f32 v122, v122, v91\n"
    "v_mfma_f32_16x16x32_bf16 a[156:159], v[160:163], v[140:143], a[156:159]\n"
    "ds_read_b128 v[176:179], v201 offset:22528\n"
    "v_add_f32 v118, v118, v53\n"
    "v_mfma_f32_16x16x32_bf16 a[156:159], v[164:167], v[156:159], a[156:159]\n"
    "s_waitcnt vmcnt(3)\n"
    "ds_read_b128 v[180:183], v201 offset:23552\n"
    "v_add_f32 v125, v125, v92\n"
    "v_mfma_f32_16x16x32_bf16 a[152:155], v[164:167], v[152:155], a[152:155]\n"
    "v_exp_f32 v62, v62\n"
    "v_mfma_f32_16x16x32_bf16 a[152:155], v[160:163], v[136:139], a[152:155]\n"
    "v_add_f32 v117, v117, v54\n"
    "v_add_f32 v126, v126, v93\n"
    "v_mfma_f32_16x16x32_bf16 a[148:151], v[160:163], v[132:135], a[148:151]\n"
    "v_exp_f32 v63, v63\n"
    "v_mfma_f32_16x16x32_bf16 a[148:151], v[164:167], v[148:151], a[148:151]\n"
    "v_add_f32 v118, v118, v55\n"
    "v_add_f32 v125, v125, v94\n"
    "v_mfma_f32_16x16x32_bf16 a[144:147], v[164:167], v[144:147], a[144:147]\n"
    "v_add_f32 v121, v121, v56\n"
    "v_add_f32 v126, v126, v95\n"
    "v_add_f32 v122, v122, v57\n"
    "v_mfma_f32_16x16x32_bf16 a[144:147], v[160:163], v[128:131], a[144:147]\n"
    "s_waitcnt lgkmcnt(0)\n"
    "v_mfma_f32_16x16x32_bf16 a[160:163], v[168:171], v[128:131], a[160:163]\n"
    "s_barrier\n"
    "v_mfma_f32_16x16x32_bf16 a[160:163], v[172:175], v[144:147], a[160:163]\n"
    "ds_read_u16_d16_hi v223, v209 offset:6144\n"
    "ds_read_u16_d16_hi v222, v209\n"
    "v_mfma_f32_16x16x32_bf16 a[164:167], v[172:175], v[148:151], a[164:167]\n"
    "ds_read_b128 v[160:163], v200\n"
    "v_add_f32 v121, v121, v58\n"
    "v_mfma_f32_16x16x32_bf16 a[164:167], v[168:171], v[132:135], a[164:167]\n"
    "ds_read_b128 v[232:235], v205 offset:24576\n"
    "v_add_f32 v122, v122, v59\n"
    "v_mfma_f32_16x16x32_bf16 a[168:171], v[168:171], v[136:139], a[168:171]\n"
    "ds_read_b128 v[164:167], v200 offset:1024\n"
    "ds_read_b128 v[236:239], v206 offset:24576\n"
    "v_mfma_f32_16x16x32_bf16 a[168:171], v[172:175], v[152:155], a[168:171]\n"
    "v_add_f32 v125, v125, v60\n"
    "v_add_f32 v126, v126, v61\n"
    "v_mfma_f32_16x16x32_bf16 a[172:175], v[172:175], v[156:159], a[172:175]\n"
    "v_add_f32 v125, v125, v62\n"
    "v_add_f32 v126, v126, v63\n"
    "v_mfma_f32_16x16x32_bf16 a[172:175], v[168:171], v[140:143], a[172:175]\n"
    "ds_read_u16_d16_hi v225, v210 offset:512\n"
    "ds_read_u16_d16_hi v224, v210\n"
    "v_mfma_f32_16x16x32_bf16 a[188:191], v[176:179], v[140:143], a[188:191]\n"
    "ds_read_b128 v[168:171], v200 offset:2048\n"
    "ds_read2st64_b32 v[240:241], v207 offset0:96 offset1:99\n"
    "v_mfma_f32_16x16x32_bf16 a[188:191], v[180:183], v[156:159], a[188:191]\n"
    "ds_read2st64_b32 v[242:243], v207 offset0:102 offset1:105\n"
    "ds_read2st64_b32 v[244:245], v207 offset0:108 offset1:111\n"
    "v_mfma_f32_16x16x32_bf16 a[184:187], v[176:179], v[136:139], a[184:187]\n"
    "ds_read2st64_b32 v[246:247], v207 offset0:114 offset1:117\n"
    "v_cvt_pk_bf16_f32 v140, v44, v45\n"
    "v_mfma_f32_16x16x32_bf16 a[184:187], v[180:183], v[152:155], a[184:187]\n"
    "s_waitcnt lgkmcnt(7)\n"
    "v_mul_f32 v226, s75, v223\n"
    "v_mfma_f32_16x16x32_bf16 a[180:183], v[176:179], v[132:135], a[180:183]\n"
    "ds_read_b128 v[172:175], v200 offset:3072\n"
    "v_fma_f32 v227, -v222, v226, v217\n"
    "v_mfma_f32_16x16x32_bf16 a[180:183], v[180:183], v[148:151], a[180:183]\n"
    "v_cvt_f32_i32 v232, v232\n"
    "v_cvt_f32_i32 v233, v233\n"
    "v_mfma_f32_16x16x32_bf16 a[176:179], v[176:179], v[128:131], a[176:179]\n"
    "v_cvt_f32_i32 v234, v234\n"
    "v_cvt_f32_i32 v235, v235\n"
    "v_mfma_f32_16x16x32_bf16 a[176:179], v[180:183], v[144:147], a[176:179]\n"
    "v_cvt_f32_i32 v236, v236\n"
    "v_cvt_f32_i32 v237, v237\n"
    "LM01:\n"
    "v_mfma_f32_16x16x32_bf16 v[64:67], v[160:163], a[192:195], v[96:99]\n"
    "ds_read_b128 v[176:179], v200 offset:4096\n"
    "v_cvt_f32_i32 v238, v238\n"
    "v_mfma_f32_16x16x32_bf16 v[64:67], v[164:167], a[196:199], v[64:67]\n"
    "v_cvt_f32_i32 v239, v239\n"
    "v_mul_f32_dpp v232, v226, v232 row_newbcast:0 row_mask:0xf bank_mask:0xf\n"
    "v_mfma_f32_16x16x32_bf16 v[68:71], v[164:167], a[220:223], v[100:103]\n"
    "v_mul_f32_dpp v233, v226, v233 row_newbcast:1 row_mask:0xf bank_mask:0xf\n"
    "v_mul_f32_dpp v234, v226, v234 row_newbcast:2 row_mask:0xf bank_mask:0xf\n"
    "v_mfma_f32_16x16x32_bf16 v[68:71], v[160:163], a[216:219], v[68:71]\n"
    "v_mul_f32_dpp v235, v226, v235 row_newbcast:3 row_mask:0xf bank_mask:0xf\n"
    "v_mul_f32_dpp v236, v226, v236 row_newbcast:4 row_mask:0xf bank_mask:0xf\n"
    "v_mfma_f32_16x16x32_bf16 v[72:75], v[160:163], a[240:243], v[104:107]\n"
    "ds_read_b128 v[180:183], v200 offset:5120\n"
    "v_mul_f32_dpp v237, v226, v237 row_newbcast:5 row_mask:0xf bank_mask:0xf\n"
    "v_mfma_f32_16x16x32_bf16 v[72:75], v[164:167], a[244:247], v[72:75]\n"
    "v_mul_f32_dpp v238, v226, v238 row_newbcast:6 row_mask:0xf bank_mask:0xf\n"
    "v_mul_f32_dpp v239, v226, v239 row_newbcast:7 row_mask:0xf bank_mask:0xf\n"
    "v_mfma_f32_16x16x32_bf16 v[76:79], v[164:167], v[12:15], v[108:111]\n"
    "v_add_f32_dpp v232, v227, v232 row_newbcast:0 row_mask:0xf bank_mask:0xf\n"
    "v_add_f32_dpp v233, v227, v233 row_newbcast:1 row_mask:0xf bank_mask:0xf\n"
    "v_mfma_f32_16x16x32_bf16 v[76:79], v[160:163], v[8:11], v[76:79]\n"
    "s_waitcnt lgkmcnt(2)\n"
    "v_add_f32_dpp v234, v227, v234 row_newbcast:2 row_mask:0xf bank_mask:0xf\n"
    "v_add_f32_dpp v235, v227, v235 row_newbcast:3 row_mask:0xf bank_mask:0xf\n"
    "v_mfma_f32_16x16x32_bf16 v[76:79], v[168:171], v[16:19], v[76:79]\n"
    "ds_read_b128 v[160:163], v200 offset:10240\n"
    "v_add_f32_dpp v236, v227, v236 row_newbcast:4 row_mask:0xf bank_mask:0xf\n"
    "v_mfma_f32_16x16x32_bf16 v[76:79], v[172:175], v[20:23], v[76:79]\n"
    "v_add_f32_dpp v237, v227, v237 row_newbcast:5 row_mask:0xf bank_mask:0xf\n"
    "v_add_f32_dpp v238, v227, v238 row_newbcast:6 row_mask:0xf bank_mask:0xf\n"
    "v_mfma_f32_16x16x32_bf16 v[72:75], v[172:175], a[252:255], v[72:75]\n"
    "v_add_f32_dpp v239, v227, v239 row_newbcast:7 row_mask:0xf bank_mask:0xf\n"
    "v_cvt_pk_bf16_f32 v232, v232, v233\n"
    "v_mfma_f32_16x16x32_bf16 v[72:75], v[168:171], a[248:251], v[72:75]\n"
    "v_cvt_pk_bf16_f32 v233, v234, v235\n"
    "v_cvt_pk_bf16_f32 v234, v236, v237\n"
    "v_mfma_f32_16x16x32_bf16 v[68:71], v[168:171], a[224:227], v[68:71]\n"
    "ds_read_b128 v[164:167], v200 offset:11264\n"
    "v_cvt_pk_bf16_f32 v235, v238, v239\n"
    "v_mfma_f32_16x16x32_bf16 v[68:71], v[172:175], a[228:231], v[68:71]\n"
    "buffer_store_dwordx4 v[232:235], v211, s[36:39], s62 offen sc1\n"
    "v_mul_f32 v228, s68, v225\n"
    "v_fma_f32 v229, -v224, v228, s69\n"
    "v_cvt_f32_i32 v240, v240\n"
    "v_mfma_f32_16x16x32_bf16 v[64:67], v[172:175], a[204:207], v[64:67]\n"
    "v_cvt_f32_i32 v241, v241\n"
    "v_cvt_f32_i32 v242, v242\n"
    "v_mfma_f32_16x16x32_bf16 v[64:67], v[168:171], a[200:203], v[64:67]\n"
    "s_waitcnt lgkmcnt(2)\n"
    "v_cvt_f32_i32 v243, v243\n"
    "v_cvt_f32_i32 v244, v244\n"
    "v_mfma_f32_16x16x32_bf16 v[64:67], v[176:179], a[208:211], v[64:67]\n"
    "ds_read_b128 v[168:171], v200 offset:8192\n"
    "v_cvt_f32_i32 v245, v245\n"
    "v_mfma_f32_16x16x32_bf16 v[64:67], v[180:183], a[212:215], v[64:67]\n"
    "v_cvt_f32_i32 v246, v246\n"
    "v_cvt_f32_i32 v247, v247\n"
    "v_mfma_f32_16x16x32_bf16 v[68:71], v[180:183], a[236:239], v[68:71]\n"
    "v_mul_f32_dpp v240, v228, v240 row_newbcast:0 row_mask:0xf bank_mask:0xf\n"
    "v_mul_f32_dpp v241, v228, v241 row_newbcast:1 row_mask:0xf bank_mask:0xf\n"
    "v_mfma_f32_16x16x32_bf16 v[68:71], v[176:179], a[232:235], v[68:71]\n"
    "v_mul_f32_dpp v242, v228, v242 row_newbcast:2 row_mask:0xf bank_mask:0xf\n"
    "v_mul_f32_dpp v243, v228, v243 row_newbcast:3 row_mask:0xf bank_mask:0xf\n"
    "v_mfma_f32_16x16x32_bf16 v[72:75], v[176:179], v[0:3], v[72:75]\n"
    "ds_read_b128 v[172:175], v200 offset:9216\n"
    "v_mul_f32_dpp v244, v228, v244 row_newbcast:4 row_mask:0xf bank_mask:0xf\n"
    "v_mfma_f32_16x16x32_bf16 v[72:75], v[180:183], v[4:7], v[72:75]\n"
    "v_mul_f32_dpp v245, v228, v245 row_newbcast:5 row_mask:0xf bank_mask:0xf\n"
    "v_mul_f32_dpp v246, v228, v246 row_newbcast:6 row_mask:0xf bank_mask:0xf\n"
    "v_mfma_f32_16x16x32_bf16 v[76:79], v[180:183], v[28:31], v[76:79]\n"
    "v_mul_f32_dpp v247, v228, v247 row_newbcast:7 row_mask:0xf bank_mask:0xf\n"
    "v_add_f32_dpp v240, v229, v240 row_newbcast:0 row_mask:0xf bank_mask:0xf\n"
    "v_mfma_f32_16x16x32_bf16 v[76:79], v[176:179], v[24:27], v[76:79]\n"
    "s_waitcnt lgkmcnt(2)\n"
    "v_add_f32_dpp v241, v229, v241 row_newbcast:1 row_mask:0xf bank_mask:0xf\n"
    "v_add_f32_dpp v242, v229, v242 row_newbcast:2 row_mask:0xf bank_mask:0xf\n"
    "v_mfma_f32_16x16x32_bf16 v[92:95], v[160:163], v[24:27], v[108:111]\n"
    "ds_read_b128 v[176:179], v200 offset:6144\n"
    "v_add_f32_dpp v243, v229, v243 row_newbcast:3 row_mask:0xf bank_mask:0xf\n"
    "v_mfma_f32_16x16x32_bf16 v[92:95], v[164:167], v[28:31], v[92:95]\n"
    "v_add_f32_dpp v244, v229, v244 row_newbcast:4 row_mask:0xf bank_mask:0xf\n"
    "v_add_f32_dpp v245, v229, v245 row_newbcast:5 row_mask:0xf bank_mask:0xf\n"
    "v_mfma_f32_16x16x32_bf16 v[88:91], v[164:167], v[4:7], v[104:107]\n"
    "v_add_f32_dpp v246, v229, v246 row_newbcast:6 row_mask:0xf bank_mask:0xf\n"
    "v_add_f32_dpp v247, v229, v247 row_newbcast:7 row_mask:0xf bank_mask:0xf\n"
    "v_mfma_f32_16x16x32_bf16 v[88:91], v[160:163], v[0:3], v[88:91]\n"
    "v_cvt_pk_bf16_f32 v141, v46, v47\n"
    "v_cvt_pk_bf16_f32 v240, v240, v241\n"
    "v_mfma_f32_16x16x32_bf16 v[84:87], v[160:163], a[232:235], v[100:103]\n"
    "ds_read_b128 v[180:183], v200 offset:7168\n"
    "v_cvt_pk_bf16_f32 v136, v40, v41\n"
    "v_mfma_f32_16x16x32_bf16 v[84:87], v[164:167], a[236:239], v[84:87]\n"
    "v_cvt_pk_bf16_f32 v241, v242, v243\n"
    "v_cvt_pk_bf16_f32 v137, v42, v43\n"
    "v_mfma_f32_16x16x32_bf16 v[80:83], v[164:167], a[212:215], v[96:99]\n"
    "v_cvt_pk_bf16_f32 v242, v244, v245\n"
    "v_cvt_pk_bf16_f32 v132, v36, v37\n"
    "v_mfma_f32_16x16x32_bf16 v[80:83], v[160:163], a[208:211], v[80:83]\n"
    "s_waitcnt lgkmcnt(2)\n"
    "v_cvt_pk_bf16_f32 v243, v246, v247\n"
    "v_cvt_pk_bf16_f32 v133, v38, v39\n"
    "v_mfma_f32_16x16x32_bf16 v[80:83], v[168:171], a[200:203], v[80:83]\n"
    "ds_read_b128 v[160:163], v200 offset:12288\n"
    "v_cvt_pk_bf16_f32 v128, v32, v33\n"
    "v_mfma_f32_16x16x32_bf16 v[80:83], v[172:175], a[204:207], v[80:83]\n"
    "v_cvt_pk_bf16_f32 v129, v34, v35\n"
    "v_cvt_pk_bf16_f32 v142, v60, v61\n"
    "v_mfma_f32_16x16x32_bf16 v[84:87], v[172:175], a[228:231], v[84:87]\n"
    "buffer_store_dwordx4 v[240:243], v212, s[40:43], s63 offen sc1\n"
    "v_exp_f32 v64, v64\n"
    "v_cvt_pk_bf16_f32 v143, v62, v63\n"
    "v_mfma_f32_16x16x32_bf16 v[84:87], v[168:171], a[224:227], v[84:87]\n"
    "s_add_u32 s65, s60, s92\n"
    "v_exp_f32 v65, v65\n"
    "v_mfma_f32_16x16x32_bf16 v[88:91], v[168:171], a[248:251], v[88:91]\n"
    "s_add_u32 m0, s90, 0x6000\n"
    "ds_read_b128 v[164:167], v200 offset:13312\n"
    "v_cvt_pk_bf16_f32 v138, v56, v57\n"
    "v_mfma_f32_16x16x32_bf16 v[88:91], v[172:175], a[252:255], v[88:91]\n"
    "v_exp_f32 v66, v66\n"
    "v_mfma_f32_16x16x32_bf16 v[92:95], v[172:175], v[20:23], v[92:95]\n"
    "buffer_load_dwordx4 v200, s[36:39], s65 offen sc1 lds\n"
    "v_mfma_f32_16x16x32_bf16 v[92:95], v[168:171], v[16:19], v[92:95]\n"
    "s_waitcnt lgkmcnt(2)\n"
    "v_exp_f32 v67, v67\n"
    "v_mfma_f32_16x16x32_bf16 v[92:95], v[176:179], v[8:11], v[92:95]\n"
    "ds_read_b128 v[168:171], v200 offset:14336\n"
    "v_cvt_pk_bf16_f32 v139, v58, v59\n"
    "v_mfma_f32_16x16x32_bf16 v[92:95], v[180:183], v[12:15], v[92:95]\n"
    "v_exp_f32 v68, v68\n"
    "v_mfma_f32_16x16x32_bf16 v[88:91], v[180:183], a[244:247], v[88:91]\n"
    "buffer_load_dwordx4 v200, s[36:39], s65 offen offset:1024 sc1 lds\n"
    "v_mfma_f32_16x16x32_bf16 v[88:91], v[176:179], a[240:243], v[88:91]\n"
    "v_exp_f32 v69, v69\n"
    "v_mfma_f32_16x16x32_bf16 v[84:87], v[176:179], a[216:219], v[84:87]\n"
    "ds_read_b128 v[172:175], v200 offset:15360\n"
    "v_cvt_pk_bf16_f32 v134, v52, v53\n"
    "v_mfma_f32_16x16x32_bf16 v[84:87], v[180:183], a[220:223], v[84:87]\n"
    "v_exp_f32 v70, v70\n"
    "v_mfma_f32_16x16x32_bf16 v[80:83], v[180:183], a[196:199], v[80:83]\n"
    "buffer_load_dwordx4 v200, s[36:39], s65 offen offset:2048 sc1 lds\n"
    "v_mfma_f32_16x16x32_bf16 v[80:83], v[176:179], a[192:195], v[80:83]\n"
    "s_waitcnt lgkmcnt(2)\n"
    "v_exp_f32 v71, v71\n"
    "v_mfma_f32_16x16x32_bf16 v[32:35], v[160:163], a[192:195], v[96:99]\n"
    "ds_read_b128 v[176:179], v200 offset:16384\n"
    "v_cvt_pk_bf16_f32 v135, v54, v55\n"
    "v_mfma_f32_16x16x32_bf16 v[32:35], v[164:167], a[196:199], v[32:35]\n"
    "v_exp_f32 v72, v72\n"
    "v_mfma_f32_16x16x32_bf16 v[36:39], v[164:167], a[220:223], v[100:103]\n"
    "buffer_load_dwordx4 v200, s[36:39], s65 offen offset:3072 sc1 lds\n"
    "v_mfma_f32_16x16x32_bf16 v[36:39], v[160:163], a[216:219], v[36:39]\n"
    "s_add_u32 s65, s65, 0x1000\n"
    "v_exp_f32 v73, v73\n"
    "v_mfma_f32_16x16x32_bf16 v[40:43], v[160:163], a[240:243], v[104:107]\n"
    "s_add_u32 m0, s90, 0x7000\n"
    "ds_read_b128 v[180:183], v200 offset:17408\n"
    "v_cvt_pk_bf16_f32 v130, v48, v49\n"
    "v_mfma_f32_16x16x32_bf16 v[40:43], v[164:167], a[244:247], v[40:43]\n"
    "v_exp_f32 v74, v74\n"
    "v_mfma_f32_16x16x32_bf16 v[44:47], v[164:167], v[12:15], v[108:111]\n"
    "buffer_load_dwordx4 v200, s[36:39], s65 offen sc1 lds\n"
    "v_mfma_f32_16x16x32_bf16 v[44:47], v[160:163], v[8:11], v[44:47]\n"
    "s_waitcnt lgkmcnt(2)\n"
    "v_exp_f32 v75, v75\n"
    "v_mfma_f32_16x16x32_bf16 v[44:47], v[168:171], v[16:19], v[44:47]\n"
    "ds_read_b128 v[160:163], v200 offset:22528\n"
    "v_cvt_pk_bf16_f32 v131, v50, v51\n"
    "v_mfma_f32_16x16x32_bf16 v[44:47], v[172:175], v[20:23], v[44:47]\n"
    "v_exp_f32 v76, v76\n"
    "v_mfma_f32_16x16x32_bf16 v[40:43], v[172:175], a[252:255], v[40:43]\n"
    "buffer_load_dwordx4 v200, s[36:39], s65 offen offset:1024 sc1 lds\n"
    "v_mfma_f32_16x16x32_bf16 v[40:43], v[168:171], a[248:251], v[40:43]\n"
    "v_exp_f32 v77, v77\n"
    "v_mfma_f32_16x16x32_bf16 v[36:39], v[168:171], a[224:227], v[36:39]\n"
    "ds_read_b128 v[164:167], v200 offset:23552\n"
    "v_cvt_pk_bf16_f32 v144, v64, v65\n"
    "v_mfma_f32_16x16x32_bf16 v[36:39], v[172:175], a[228:231], v[36:39]\n"
    "v_exp_f32 v78, v78\n"
    "v_mfma_f32_16x16x32_bf16 v[32:35], v[172:175], a[204:207], v[32:35]\n"
    "buffer_load_dwordx4 v200, s[36:39], s65 offen offset:2048 sc1 lds\n"
    "v_mfma_f32_16x16x32_bf16 v[32:35], v[168:171], a[200:203], v[32:35]\n"
    "s_waitcnt lgkmcnt(2)\n"
    "v_exp_f32 v79, v79\n"
    "v_mfma_f32_16x16x32_bf16 v[32:35], v[176:179], a[208:211], v[32:35]\n"
    "s_add_u32 m0, s71, 0xc000\n"
    "ds_read_b128 v[168:171], v200 offset:20480\n"
    "v_cvt_pk_bf16_f32 v145, v66, v67\n"
    "v_mfma_f32_16x16x32_bf16 v[32:35], v[180:183], a[212:215], v[32:35]\n"
    "v_exp_f32 v80, v80\n"
    "v_mfma_f32_16x16x32_bf16 v[36:39], v[180:183], a[236:239], v[36:39]\n"
    "buffer_load_dwordx4 v200, s[40:43], s61 offen sc1 lds\n"
    "v_mfma_f32_16x16x32_bf16 v[36:39], v[176:179], a[232:235], v[36:39]\n"
    "v_exp_f32 v81, v81\n"
    "v_mfma_f32_16x16x32_bf16 v[40:43], v[176:179], v[0:3], v[40:43]\n"
    "ds_read_b128 v[172:175], v200 offset:21504\n"
    "v_cvt_pk_bf16_f32 v148, v68, v69\n"
    "v_mfma_f32_16x16x32_bf16 v[40:43], v[180:183], v[4:7], v[40:43]\n"
    "v_exp_f32 v82, v82\n"
    "v_mfma_f32_16x16x32_bf16 v[44:47], v[180:183], v[28:31], v[44:47]\n"
    "buffer_load_dwordx4 v200, s[40:43], s61 offen offset:1024 sc1 lds\n"
    "v_mfma_f32_16x16x32_bf16 v[44:47], v[176:179], v[24:27], v[44:47]\n"
    "s_waitcnt lgkmcnt(2)\n"
    "v_exp_f32 v83, v83\n"
    "v_mfma_f32_16x16x32_bf16 v[60:63], v[160:163], v[24:27], v[108:111]\n"
    "ds_read_b128 v[176:179], v200 offset:18432\n"
    "v_cvt_pk_bf16_f32 v149, v70, v71\n"
    "v_mfma_f32_16x16x32_bf16 v[60:63], v[164:167], v[28:31], v[60:63]\n"
    "v_exp_f32 v84, v84\n"
    "v_mfma_f32_16x16x32_bf16 v[56:59], v[164:167], v[4:7], v[104:107]\n"
    "buffer_load_dwordx4 v200, s[40:43], s61 offen offset:2048 sc1 lds\n"
    "v_mfma_f32_16x16x32_bf16 v[56:59], v[160:163], v[0:3], v[56:59]\n"
    "v_exp_f32 v85, v85\n"
    "v_mfma_f32_16x16x32_bf16 v[52:55], v[160:163], a[232:235], v[100:103]\n"
    "ds_read_b128 v[180:183], v200 offset:19456\n"
    "v_cvt_pk_bf16_f32 v152, v72, v73\n"
    "v_mfma_f32_16x16x32_bf16 v[52:55], v[164:167], a[236:239], v[52:55]\n"
    "v_exp_f32 v86, v86\n"
    "v_mfma_f32_16x16x32_bf16 v[48:51], v[164:167], a[212:215], v[96:99]\n"
    "buffer_load_dwordx4 v200, s[40:43], s61 offen offset:3072 sc1 lds\n"
    "v_mfma_f32_16x16x32_bf16 v[48:51], v[160:163], a[208:211], v[48:51]\n"
    "s_waitcnt lgkmcnt(2)\n"
    "s_add_u32 s65, s61, 0x1000\n"
    "v_cvt_pk_bf16_f32 v153, v74, v75\n"
    "v_mfma_f32_16x16x32_bf16 v[48:51], v[168:171], a[200:203], v[48:51]\n"
    "s_add_u32 m0, s71, 0xd000\n"
    "ds_read_b128 v[160:163], v201 offset:24576\n"
    "v_cvt_pk_bf16_f32 v156, v76, v77\n"
    "v_mfma_f32_16x16x32_bf16 v[48:51], v[172:175], a[204:207], v[48:51]\n"
    "v_exp_f32 v87, v87\n"
    "v_mfma_f32_16x16x32_bf16 v[52:55], v[172:175], a[228:231], v[52:55]\n"
    "buffer_load_dwordx4 v200, s[40:43], s65 offen sc1 lds\n"
    "v_mfma_f32_16x16x32_bf16 v[52:55], v[168:171], a[224:227], v[52:55]\n"
    "v_exp_f32 v88, v88\n"
    "v_mfma_f32_16x16x32_bf16 v[56:59], v[168:171], a[248:251], v[56:59]\n"
    "ds_read_b128 v[164:167], v201 offset:25600\n"
    "v_cvt_pk_bf16_f32 v157, v78, v79\n"
    "v_mfma_f32_16x16x32_bf16 v[56:59], v[172:175], a[252:255], v[56:59]\n"
    "v_exp_f32 v89, v89\n"
    "v_mfma_f32_16x16x32_bf16 v[60:63], v[172:175], v[20:23], v[60:63]\n"
    "buffer_load_dwordx4 v200, s[40:43], s65 offen offset:1024 sc1 lds\n"
    "v_mfma_f32_16x16x32_bf16 v[60:63], v[168:171], v[16:19], v[60:63]\n"
    "s_waitcnt lgkmcnt(2)\n"
    "v_exp_f32 v90, v90\n"
    "v_mfma_f32_16x16x32_bf16 v[60:63], v[176:179], v[8:11], v[60:63]\n"
    "ds_read_b128 v[168:171], v201 offset:26624\n"
    "v_cvt_pk_bf16_f32 v146, v80, v81\n"
    "v_mfma_f32_16x16x32_bf16 v[60:63], v[180:183], v[12:15], v[60:63]\n"
    "v_exp_f32 v91, v91\n"
    "v_mfma_f32_16x16x32_bf16 v[56:59], v[180:183], a[244:247], v[56:59]\n"
    "v_cvt_pk_bf16_f32 v147, v82, v83\n"
    "v_cvt_pk_bf16_f32 v150, v84, v85\n"
    "v_mfma_f32_16x16x32_bf16 v[56:59], v[176:179], a[240:243], v[56:59]\n"
    "v_exp_f32 v92, v92\n"
    "v_mfma_f32_16x16x32_bf16 v[52:55], v[176:179], a[216:219], v[52:55]\n"
    "ds_read_b128 v[172:175], v201 offset:27648\n"
    "v_cvt_pk_bf16_f32 v151, v86, v87\n"
    "v_mfma_f32_16x16x32_bf16 v[52:55], v[180:183], a[220:223], v[52:55]\n"
    "v_exp_f32 v93, v93\n"
    "v_mfma_f32_16x16x32_bf16 v[48:51], v[180:183], a[196:199], v[48:51]\n"
    "v_exp_f32 v94, v94\n"
    "v_mfma_f32_16x16x32_bf16 v[48:51], v[176:179], a[192:195], v[48:51]\n"
    "s_waitcnt lgkmcnt(2)\n"
    "v_cvt_pk_bf16_f32 v154, v88, v89\n"
    "v_cvt_pk_bf16_f32 v155, v90, v91\n"
    "v_mfma_f32_16x16x32_bf16 a[0:3], v[160:163], v[128:131], a[0:3]\n"
    "ds_read_b128 v[176:179], v201 offset:28672\n"
    "v_cvt_pk_bf16_f32 v158, v92, v93\n"
    "v_mfma_f32_16x16x32_bf16 a[0:3], v[164:167], v[144:147], a[0:3]\n"
    "v_exp_f32 v95, v95\n"
    "v_mfma_f32_16x16x32_bf16 a[4:7], v[160:163], v[132:135], a[4:7]\n"
    "v_cvt_pk_bf16_f32 v159, v94, v95\n"
    "v_add_f32 v113, v113, v64\n"
    "v_mfma_f32_16x16x32_bf16 a[4:7], v[164:167], v[148:151], a[4:7]\n"
    "v_exp_f32 v32, v32\n"
    "v_mfma_f32_16x16x32_bf16 a[8:11], v[160:163], v[136:139], a[8:11]\n"
    "ds_read_b128 v[180:183], v201 offset:29696\n"
    "v_add_f32 v114, v114, v65\n"
    "v_mfma_f32_16x16x32_bf16 a[8:11], v[164:167], v[152:155], a[8:11]\n"
    "v_exp_f32 v33, v33\n"
    "v_mfma_f32_16x16x32_bf16 a[12:15], v[160:163], v[140:143], a[12:15]\n"
    "v_exp_f32 v34, v34\n"
    "v_mfma_f32_16x16x32_bf16 a[12:15], v[164:167], v[156:159], a[12:15]\n"
    "s_waitcnt lgkmcnt(2)\n"
    "v_exp_f32 v35, v35\n"
    "v_mfma_f32_16x16x32_bf16 a[28:31], v[168:171], v[140:143], a[28:31]\n"
    "ds_read_b128 v[160:163], v201 offset:30720\n"
    "v_add_f32 v113, v113, v66\n"
    "v_mfma_f32_16x16x32_bf16 a[28:31], v[172:175], v[156:159], a[28:31]\n"
    "v_exp_f32 v36, v36\n"
    "v_mfma_f32_16x16x32_bf16 a[24:27], v[172:175], v[152:155], a[24:27]\n"
    "v_exp_f32 v37, v37\n"
    "v_mfma_f32_16x16x32_bf16 a[24:27], v[168:171], v[136:139], a[24:27]\n"
    "v_exp_f32 v38, v38\n"
    "v_mfma_f32_16x16x32_bf16 a[20:23], v[168:171], v[132:135], a[20:23]\n"
    "ds_read_b128 v[164:167], v201 offset:31744\n"
    "v_add_f32 v114, v114, v67\n"
    "v_mfma_f32_16x16x32_bf16 a[20:23], v[172:175], v[148:151], a[20:23]\n"
    "v_exp_f32 v39, v39\n"
    "v_mfma_f32_16x16x32_bf16 a[16:19], v[172:175], v[144:147], a[16:19]\n"
    "v_exp_f32 v40, v40\n"
    "v_mfma_f32_16x16x32_bf16 a[16:19], v[168:171], v[128:131], a[16:19]\n"
    "s_waitcnt lgkmcnt(2)\n"
    "v_exp_f32 v41, v41\n"
    "v_mfma_f32_16x16x32_bf16 a[32:35], v[176:179], v[128:131], a[32:35]\n"
    "ds_read_b128 v[168:171], v201 offset:32768\n"
    "v_add_f32 v117, v117, v68\n"
    "v_mfma_f32_16x16x32_bf16 a[32:35], v[180:183], v[144:147], a[32:35]\n"
    "v_exp_f32 v42, v42\n"
    "v_mfma_f32_16x16x32_bf16 a[36:39], v[180:183], v[148:151], a[36:39]\n"
    "v_exp_f32 v43, v43\n"
    "v_mfma_f32_16x16x32_bf16 a[36:39], v[176:179], v[132:135], a[36:39]\n"
    "v_exp_f32 v44, v44\n"
    "v_mfma_f32_16x16x32_bf16 a[40:43], v[176:179], v[136:139], a[40:43]\n"
    "ds_read_b128 v[172:175], v201 offset:33792\n"
    "v_add_f32 v118, v118, v69\n"
    "v_mfma_f32_16x16x32_bf16 a[40:43], v[180:183], v[152:155], a[40:43]\n"
    "v_exp_f32 v45, v45\n"
    "v_mfma_f32_16x16x32_bf16 a[44:47], v[180:183], v[156:159], a[44:47]\n"
    "v_exp_f32 v46, v46\n"
    "v_mfma_f32_16x16x32_bf16 a[44:47], v[176:179], v[140:143], a[44:47]\n"
    "s_waitcnt lgkmcnt(2)\n"
    "v_exp_f32 v47, v47\n"
    "v_mfma_f32_16x16x32_bf16 a[60:63], v[160:163], v[140:143], a[60:63]\n"
    "ds_read_b128 v[176:179], v201 offset:34816\n"
    "v_add_f32 v117, v117, v70\n"
    "v_mfma_f32_16x16x32_bf16 a[60:63], v[164:167], v[156:159], a[60:63]\n"
    "v_exp_f32 v48, v48\n"
    "v_mfma_f32_16x16x32_bf16 a[56:59], v[164:167], v[152:155], a[56:59]\n"
    "v_exp_f32 v49, v49\n"
    "v_mfma_f32_16x16x32_bf16 a[56:59], v[160:163], v[136:139], a[56:59]\n"
    "v_exp_f32 v50, v50\n"
    "v_mfma_f32_16x16x32_bf16 a[52:55], v[160:163], v[132:135], a[52:55]\n"
    "ds_read_b128 v[180:183], v201 offset:35840\n"
    "v_add_f32 v113, v113, v32\n"
    "v_mfma_f32_16x16x32_bf16 a[52:55], v[164:167], v[148:151], a[52:55]\n"
    "v_exp_f32 v51, v51\n"
    "v_mfma_f32_16x16x32_bf16 a[48:51], v[164:167], v[144:147], a[48:51]\n"
    "v_exp_f32 v52, v52\n"
    "v_mfma_f32_16x16x32_bf16 a[48:51], v[160:163], v[128:131], a[48:51]\n"
    "s_waitcnt lgkmcnt(2)\n"
    "v_add_f32 v118, v118, v71\n"
    "v_add_f32 v114, v114, v33\n"
    "v_mfma_f32_16x16x32_bf16 a[64:67], v[168:171], v[128:131], a[64:67]\n"
    "ds_read_b128 v[160:163], v201 offset:36864\n"
    "v_add_f32 v121, v121, v72\n"
    "v_mfma_f32_16x16x32_bf16 a[64:67], v[172:175], v[144:147], a[64:67]\n"
    "v_add_f32 v113, v113, v34\n"
    "v_add_f32 v122, v122, v73\n"
    "v_mfma_f32_16x16x32_bf16 a[68:71], v[172:175], v[148:151], a[68:71]\n"
    "v_exp_f32 v53, v53\n"
    "v_mfma_f32_16x16x32_bf16 a[68:71], v[168:171], v[132:135], a[68:71]\n"
    "s_add_u32 m0, s51, s95\n"
    "v_add_f32 v114, v114, v35\n"
    "v_add_f32 v121, v121, v74\n"
    "v_mfma_f32_16x16x32_bf16 a[72:75], v[168:171], v[136:139], a[72:75]\n"
    "ds_read_b128 v[164:167], v201 offset:37888\n"
    "v_add_f32 v117, v117, v36\n"
    "v_mfma_f32_16x16x32_bf16 a[72:75], v[172:175], v[152:155], a[72:75]\n"
    "buffer_load_dwordx4 v202, s[32:35], s64 offen lds\n"
    "v_mfma_f32_16x16x32_bf16 a[76:79], v[172:175], v[156:159], a[76:79]\n"
    "v_exp_f32 v54, v54\n"
    "v_mfma_f32_16x16x32_bf16 a[76:79], v[168:171], v[140:143], a[76:79]\n"
    "s_waitcnt lgkmcnt(2)\n"
    "v_add_f32 v122, v122, v75\n"
    "v_add_f32 v118, v118, v37\n"
    "v_mfma_f32_16x16x32_bf16 a[92:95], v[176:179], v[140:143], a[92:95]\n"
    "ds_read_b128 v[168:171], v201 offset:38912\n"
    "v_add_f32 v125, v125, v76\n"
    "v_mfma_f32_16x16x32_bf16 a[92:95], v[180:183], v[156:159], a[92:95]\n"
    "v_add_f32 v117, v117, v38\n"
    "v_add_f32 v126, v126, v77\n"
    "v_mfma_f32_16x16x32_bf16 a[88:91], v[180:183], v[152:155], a[88:91]\n"
    "buffer_load_dwordx4 v203, s[32:35], s64 offen offset:1024 lds\n"
    "v_mfma_f32_16x16x32_bf16 a[88:91], v[176:179], v[136:139], a[88:91]\n"
    "v_exp_f32 v55, v55\n"
    "v_mfma_f32_16x16x32_bf16 a[84:87], v[176:179], v[132:135], a[84:87]\n"
    "ds_read_b128 v[172:175], v201 offset:39936\n"
    "v_add_f32 v118, v118, v39\n"
    "v_mfma_f32_16x16x32_bf16 a[84:87], v[180:183], v[148:151], a[84:87]\n"
    "v_add_f32 v125, v125, v78\n"
    "v_add_f32 v121, v121, v40\n"
    "v_mfma_f32_16x16x32_bf16 a[80:83], v[180:183], v[144:147], a[80:83]\n"
    "v_exp_f32 v56, v56\n"
    "v_mfma_f32_16x16x32_bf16 a[80:83], v[176:179], v[128:131], a[80:83]\n"
    "s_waitcnt lgkmcnt(2)\n"
    "buffer_load_dwordx4 v204, s[32:35], s64 offen offset:2048 lds\n"
    "v_mfma_f32_16x16x32_bf16 a[96:99], v[160:163], v[128:131], a[96:99]\n"
    "ds_read_b128 v[176:179], v201 offset:40960\n"
    "v_add_f32 v126, v126, v79\n"
    "v_mfma_f32_16x16x32_bf16 a[96:99], v[164:167], v[144:147], a[96:99]\n"
    "v_add_f32 v122, v122, v41\n"
    "v_add_f32 v113, v113, v80\n"
    "v_mfma_f32_16x16x32_bf16 a[100:103], v[164:167], v[148:151], a[100:103]\n"
    "v_add_f32 v121, v121, v42\n"
    "v_add_f32 v114, v114, v81\n"
    "v_mfma_f32_16x16x32_bf16 a[100:103], v[160:163], v[132:135], a[100:103]\n"
    "v_exp_f32 v57, v57\n"
    "v_mfma_f32_16x16x32_bf16 a[104:107], v[160:163], v[136:139], a[104:107]\n"
    "s_mov_b32 s95, s94\n"
    "ds_read_b128 v[180:183], v201 offset:41984\n"
    "v_add_f32 v122, v122, v43\n"
    "v_mfma_f32_16x16x32_bf16 a[104:107], v[164:167], v[152:155], a[104:107]\n"
    "s_sub_u32 s94, s94, 0x3000\n"
    "v_add_f32 v113, v113, v82\n"
    "v_add_f32 v125, v125, v44\n"
    "v_mfma_f32_16x16x32_bf16 a[108:111], v[164:167], v[156:159], a[108:111]\n"
    "s_min_u32 s94, s94, 0x6000\n"
    "v_exp_f32 v58, v58\n"
    "v_mfma_f32_16x16x32_bf16 a[108:111], v[160:163], v[140:143], a[108:111]\n"
    "s_waitcnt lgkmcnt(2)\n"
    "v_add_f32 v114, v114, v83\n"
    "v_add_f32 v126, v126, v45\n"
    "v_mfma_f32_16x16x32_bf16 a[124:127], v[168:171], v[140:143], a[124:127]\n"
    "s_sub_u32 s93, s94, s95\n"
    "ds_read_b128 v[160:163], v201 offset:43008\n"
    "v_add_f32 v117, v117, v84\n"
    "v_mfma_f32_16x16x32_bf16 a[124:127], v[172:175], v[156:159], a[124:127]\n"
    "v_add_f32 v125, v125, v46\n"
    "v_add_f32 v118, v118, v85\n"
    "v_mfma_f32_16x16x32_bf16 a[120:123], v[172:175], v[152:155], a[120:123]\n"
    "v_exp_f32 v59, v59\n"
    "v_mfma_f32_16x16x32_bf16 a[120:123], v[168:171], v[136:139], a[120:123]\n"
    "v_add_f32 v126, v126, v47\n"
    "v_add_f32 v117, v117, v86\n"
    "v_mfma_f32_16x16x32_bf16 a[116:119], v[168:171], v[132:135], a[116:119]\n"
    "ds_read_b128 v[164:167], v201 offset:44032\n"
    "v_add_f32 v113, v113, v48\n"
    "v_mfma_f32_16x16x32_bf16 a[116:119], v[172:175], v[148:151], a[116:119]\n"
    "v_exp_f32 v60, v60\n"
    "v_mfma_f32_16x16x32_bf16 a[112:115], v[172:175], v[144:147], a[112:115]\n"
    "v_add_u32 v205, s93, v205\n"
    "v_add_f32 v118, v118, v87\n"
    "v_mfma_f32_16x16x32_bf16 a[112:115], v[168:171], v[128:131], a[112:115]\n"
    "s_waitcnt lgkmcnt(2)\n"
    "v_add_f32 v114, v114, v49\n"
    "v_add_u32 v206, s93, v206\n"
    "v_mfma_f32_16x16x32_bf16 a[128:131], v[176:179], v[128:131], a[128:131]\n"
    "ds_read_b128 v[168:171], v201 offset:45056\n"
    "v_add_f32 v121, v121, v88\n"
    "v_mfma_f32_16x16x32_bf16 a[128:131], v[180:183], v[144:147], a[128:131]\n"
    "v_add_f32 v113, v113, v50\n"
    "v_add_u32 v207, s93, v207\n"
    "v_mfma_f32_16x16x32_bf16 a[132:135], v[180:183], v[148:151], a[132:135]\n"
    "s_add_u32 s64, s64, 0xc000\n"
    "v_exp_f32 v61, v61\n"
    "v_mfma_f32_16x16x32_bf16 a[132:135], v[176:179], v[132:135], a[132:135]\n"
    "s_add_u32 s62, s62, 0x6000\n"
    "v_add_f32 v122, v122, v89\n"
    "v_add_f32 v114, v114, v51\n"
    "v_mfma_f32_16x16x32_bf16 a[136:139], v[176:179], v[136:139], a[136:139]\n"
    "s_add_u32 s63, s63, 0x6000\n"
    "ds_read_b128 v[172:175], v201 offset:46080\n"
    "v_add_f32 v121, v121, v90\n"
    "v_mfma_f32_16x16x32_bf16 a[136:139], v[180:183], v[152:155], a[136:139]\n"
    "s_add_u32 s60, s60, 0x6000\n"
    "v_add_f32 v117, v117, v52\n"
    "v_add_f32 v122, v122, v91\n"
    "v_mfma_f32_16x16x32_bf16 a[140:143], v[180:183], v[156:159], a[140:143]\n"
    "s_add_u32 s61, s61, 0x6000\n"
    "v_exp_f32 v62, v62\n"
    "v_mfma_f32_16x16x32_bf16 a[140:143], v[176:179], v[140:143], a[140:143]\n"
    "s_waitcnt lgkmcnt(2)\n"
    "v_add_f32 v118, v118, v53\n"
    "v_add_f32 v125, v125, v92\n"
    "v_mfma_f32_16x16x32_bf16 a[156:159], v[160:163], v[140:143], a[156:159]\n"
    "ds_read_b128 v[176:179], v201 offset:47104\n"
    "v_add_f32 v117, v117, v54\n"
    "v_mfma_f32_16x16x32_bf16 a[156:159], v[164:167], v[156:159], a[156:159]\n"
    "s_waitcnt vmcnt(3)\n"
    "ds_read_b128 v[180:183], v201 offset:48128\n"
    "v_add_f32 v126, v126, v93\n"
    "v_mfma_f32_16x16x32_bf16 a[152:155], v[164:167], v[152:155], a[152:155]\n"
    "v_exp_f32 v63, v63\n"
    "v_mfma_f32_16x16x32_bf16 a[152:155], v[160:163], v[136:139], a[152:155]\n"
    "v_add_f32 v118, v118, v55\n"
    "v_add_f32 v125, v125, v94\n"
    "v_mfma_f32_16x16x32_bf16 a[148:151], v[160:163], v[132:135], a[148:151]\n"
    "v_add_f32 v121, v121, v56\n"
    "v_add_f32 v126, v126, v95\n"
    "v_mfma_f32_16x16x32_bf16 a[148:151], v[164:167], v[148:151], a[148:151]\n"
    "v_add_f32 v122, v122, v57\n"
    "v_mfma_f32_16x16x32_bf16 a[144:147], v[164:167], v[144:147], a[144:147]\n"
    "v_mfma_f32_16x16x32_bf16 a[144:147], v[160:163], v[128:131], a[144:147]\n"
    "s_waitcnt lgkmcnt(0)\n"
    "v_mfma_f32_16x16x32_bf16 a[160:163], v[168:171], v[128:131], a[160:163]\n"
    "s_barrier\n"
    "LMID1:\n"
    "v_mfma_f32_16x16x32_bf16 a[160:163], v[172:175], v[144:147], a[160:163]\n"
    "ds_read_u16_d16_hi v223, v209 offset:6336\n"
    "ds_read_u16_d16_hi v222, v209 offset:192\n"
    "v_mfma_f32_16x16x32_bf16 a[164:167], v[172:175], v[148:151], a[164:167]\n"
    "ds_read_b128 v[160:163], v200 offset:24576\n"
    "v_add_f32 v121, v121, v58\n"
    "v_mfma_f32_16x16x32_bf16 a[164:167], v[168:171], v[132:135], a[164:167]\n"
    "ds_read_b128 v[232:235], v205 offset:24576\n"
    "v_add_f32 v122, v122, v59\n"
    "v_mfma_f32_16x16x32_bf16 a[168:171], v[168:171], v[136:139], a[168:171]\n"
    "ds_read_b128 v[164:167], v200 offset:25600\n"
    "ds_read_b128 v[236:239], v206 offset:24576\n"
    "v_mfma_f32_16x16x32_bf16 a[168:171], v[172:175], v[152:155], a[168:171]\n"
    "v_add_f32 v125, v125, v60\n"
    "v_add_f32 v126, v126, v61\n"
    "v_mfma_f32_16x16x32_bf16 a[172:175], v[172:175], v[156:159], a[172:175]\n"
    "v_add_f32 v125, v125, v62\n"
    "v_add_f32 v126, v126, v63\n"
    "v_mfma_f32_16x16x32_bf16 a[172:175], v[168:171], v[140:143], a[172:175]\n"
    "ds_read_u16_d16_hi v225, v210 offset:528\n"
    "ds_read_u16_d16_hi v224, v210 offset:16\n"
    "v_mfma_f32_16x16x32_bf16 a[188:191], v[176:179], v[140:143], a[188:191]\n"
    "ds_read_b128 v[168:171], v200 offset:26624\n"
    "ds_read2st64_b32 v[240:241], v207 offset0:96 offset1:99\n"
    "v_mfma_f32_16x16x32_bf16 a[188:191], v[180:183], v[156:159], a[188:191]\n"
    "ds_read2st64_b32 v[242:243], v207 offset0:102 offset1:105\n"
    "ds_read2st64_b32 v[244:245], v207 offset0:108 offset1:111\n"
    "v_mfma_f32_16x16x32_bf16 a[184:187], v[176:179], v[136:139], a[184:187]\n"
    "ds_read2st64_b32 v[246:247], v207 offset0:114 offset1:117\n"
    "v_cvt_pk_bf16_f32 v140, v44, v45\n"
    "v_mfma_f32_16x16x32_bf16 a[184:187], v[180:183], v[152:155], a[184:187]\n"
    "s_waitcnt lgkmcnt(7)\n"
    "v_mul_f32 v226, s75, v223\n"
    "v_mfma_f32_16x16x32_bf16 a[180:183], v[176:179], v[132:135], a[180:183]\n"
    "s_add_u32 s59, s59, 1\n"
    "ds_read_b128 v[172:175], v200 offset:27648\n"
    "v_fma_f32 v227, -v222, v226, v217\n"
    "v_mfma_f32_16x16x32_bf16 a[180:183], v[180:183], v[148:151], a[180:183]\n"
    "v_cvt_f32_i32 v232, v232\n"
    "v_cvt_f32_i32 v233, v233\n"
    "v_mfma_f32_16x16x32_bf16 a[176:179], v[176:179], v[128:131], a[176:179]\n"
    "s_cmp_lt_u32 s59, 15\n"
    "v_cvt_f32_i32 v234, v234\n"
    "v_cvt_f32_i32 v235, v235\n"
    "v_mfma_f32_16x16x32_bf16 a[176:179], v[180:183], v[144:147], a[176:179]\n"
    "s_cbranch_scc1 LLOOP1\n"
    "s_branch LTAIL\n"
    "LENT0:\n"
    "s_mov_b32 s59, 1\n"
    "s_add_u32 m0, s72, 0x21000\n"
    "s_nop 0\n"
    "buffer_load_dwordx4 v250, s[48:51], 0 offen offset:2048 lds\n"
    "s_add_u32 m0, s72, 0x18000\n"
    "s_nop 0\n"
    "buffer_load_dwordx4 v202, s[32:35], s64 offen lds\n"
    "buffer_load_dwordx4 v203, s[32:35], s64 offen offset:1024 lds\n"
    "buffer_load_dwordx4 v204, s[32:35], s64 offen offset:2048 lds\n"
    "s_add_u32 s64, s64, 0xc000\n"
    "s_mov_b32 s94, 0x3000\n"
    "s_mov_b32 s95, 0x6000\n"
    "s_add_u32 s51, s72, 0x18000\n"
    "v_add_u32 v205, 0x3000, v205\n"
    "v_add_u32 v206, 0x3000, v206\n"
    "v_add_u32 v207, 0x3000, v207\n"
    "buffer_store_dwordx2 v[220:221], v214, s[44:47], 0 offen sc1\n"
    "ds_read_b128 v[160:163], v200 offset:24576\n"
    "ds_read_u16_d16_hi v223, v209 offset:6336\n"
    "ds_read_u16_d16_hi v222, v209 offset:192\n"
    "v_add_f32 v121, v121, v58\n"
    "ds_read_b128 v[164:167], v200 offset:25600\n"
    "ds_read_b128 v[232:235], v205 offset:24576\n"
    "v_add_f32 v122, v122, v59\n"
    "ds_read_b128 v[236:239], v206 offset:24576\n"
    "v_add_f32 v125, v125, v60\n"
    "v_add_f32 v126, v126, v61\n"
    "v_add_f32 v125, v125, v62\n"
    "v_add_f32 v126, v126, v63\n"
    "ds_read_b128 v[168:171], v200 offset:26624\n"
    "ds_read_u16_d16_hi v225, v210 offset:528\n"
    "ds_read_u16_d16_hi v224, v210 offset:16\n"
    "ds_read2st64_b32 v[240:241], v207 offset0:96 offset1:99\n"
    "ds_read2st64_b32 v[242:243], v207 offset0:102 offset1:105\n"
    "ds_read2st64_b32 v[244:245], v207 offset0:108 offset1:111\n"
    "ds_read2st64_b32 v[246:247], v207 offset0:114 offset1:117\n"
    "v_cvt_pk_bf16_f32 v140, v44, v45\n"
    "ds_read_b128 v[172:175], v200 offset:27648\n"
    "s_waitcnt lgkmcnt(8)\n"
    "v_mul_f32 v226, s75, v223\n"
    "v_fma_f32 v227, -v222, v226, v217\n"
    "v_cvt_f32_i32 v232, v232\n"
    "v_cvt_f32_i32 v233, v233\n"
    "s_nop 7\n"
    ".p2align 6\n"
    "LLOOP:\n"
    "v_mfma_f32_16x16x32_bf16 v[64:67], v[160:163], a[192:195], v[96:99]\n"
    "ds_read_b128 v[176:179], v200 offset:28672\n"
    "v_cvt_f32_i32 v234, v234\n"
    "v_mfma_f32_16x16x32_bf16 v[64:67], v[164:167], a[196:199], v[64:67]\n"
    "v_cvt_f32_i32 v235, v235\n"
    "v_cvt_f32_i32 v236, v236\n"
    "v_mfma_f32_16x16x32_bf16 v[68:71], v[164:167], a[220:223], v[100:103]\n"
    "v_cvt_f32_i32 v237, v237\n"
    "v_cvt_f32_i32 v238, v238\n"
    "v_mfma_f32_16x16x32_bf16 v[68:71], v[160:163], a[216:219], v[68:71]\n"
    "v_cvt_f32_i32 v239, v239\n"
    "v_mul_f32_dpp v232, v226, v232 row_newbcast:0 row_mask:0xf bank_mask:0xf\n"
    "v_mfma_f32_16x16x32_bf16 v[72:75], v[160:163], a[240:243], v[104:107]\n"
    "ds_read_b128 v[180:183], v200 offset:29696\n"
    "v_mul_f32_dpp v233, v226, v233 row_newbcast:1 row_mask:0xf bank_mask:0xf\n"
    "v_mfma_f32_16x16x32_bf16 v[72:75], v[164:167], a[244:247], v[72:75]\n"
    "v_mul_f32_dpp v234, v226, v234 row_newbcast:2 row_mask:0xf bank_mask:0xf\n"
    "v_mul_f32_dpp v235, v226, v235 row_newbcast:3 row_mask:0xf bank_mask:0xf\n"
    "v_mfma_f32_16x16x32_bf16 v[76:79], v[164:167], v[12:15], v[108:111]\n"
    "v_mul_f32_dpp v236, v226, v236 row_newbcast:4 row_mask:0xf bank_mask:0xf\n"
    "v_mul_f32_dpp v237, v226, v237 row_newbcast:5 row_mask:0xf bank_mask:0xf\n"
    "v_mfma_f32_16x16x32_bf16 v[76:79], v[160:163], v[8:11], v[76:79]\n"
    "s_waitcnt lgkmcnt(2)\n"
    "v_mul_f32_dpp v238, v226, v238 row_newbcast:6 row_mask:0xf bank_mask:0xf\n"
    "v_mul_f32_dpp v239, v226, v239 row_newbcast:7 row_mask:0xf bank_mask:0xf\n"
    "v_mfma_f32_16x16x32_bf16 v[76:79], v[168:171], v[16:19], v[76:79]\n"
    "ds_read_b128 v[160:163], v200 offset:34816\n"
    "v_add_f32_dpp v232, v227, v232 row_newbcast:0 row_mask:0xf bank_mask:0xf\n"
    "v_mfma_f32_16x16x32_bf16 v[76:79], v[172:175], v[20:23], v[76:79]\n"
    "v_add_f32_dpp v233, v227, v233 row_newbcast:1 row_mask:0xf bank_mask:0xf\n"
    "v_add_f32_dpp v234, v227, v234 row_newbcast:2 row_mask:0xf bank_mask:0xf\n"
    "v_mfma_f32_16x16x32_bf16 v[72:75], v[172:175], a[252:255], v[72:75]\n"
    "v_add_f32_dpp v235, v227, v235 row_newbcast:3 row_mask:0xf bank_mask:0xf\n"
    "v_add_f32_dpp v236, v227, v236 row_newbcast:4 row_mask:0xf bank_mask:0xf\n"
    "v_mfma_f32_16x16x32_bf16 v[72:75], v[168:171], a[248:251], v[72:75]\n"
    "v_add_f32_dpp v237, v227, v237 row_newbcast:5 row_mask:0xf bank_mask:0xf\n"
    "v_add_f32_dpp v238, v227, v238 row_newbcast:6 row_mask:0xf bank_mask:0xf\n"
    "v_mfma_f32_16x16x32_bf16 v[68:71], v[168:171], a[224:227], v[68:71]\n"
    "ds_read_b128 v[164:167], v200 offset:35840\n"
    "v_add_f32_dpp v239, v227, v239 row_newbcast:7 row_mask:0xf bank_mask:0xf\n"
    "v_mfma_f32_16x16x32_bf16 v[68:71], v[172:175], a[228:231], v[68:71]\n"
    "v_cvt_pk_bf16_f32 v232, v232, v233\n"
    "v_cvt_pk_bf16_f32 v233, v234, v235\n"
    "v_mfma_f32_16x16x32_bf16 v[64:67], v[172:175], a[204:207], v[64:67]\n"
    "v_cvt_pk_bf16_f32 v234, v236, v237\n"
    "v_cvt_pk_bf16_f32 v235, v238, v239\n"
    "v_mfma_f32_16x16x32_bf16 v[64:67], v[168:171], a[200:203], v[64:67]\n"
    "s_waitcnt lgkmcnt(2)\n"
    "v_mul_f32 v228, s68, v225\n"
    "v_fma_f32 v229, -v224, v228, s69\n"
    "v_mfma_f32_16x16x32_bf16 v[64:67], v[176:179], a[208:211], v[64:67]\n"
    "ds_read_b128 v[168:171], v200 offset:32768\n"
    "v_cvt_f32_i32 v240, v240\n"
    "v_mfma_f32_16x16x32_bf16 v[64:67], v[180:183], a[212:215], v[64:67]\n"
    "buffer_store_dwordx4 v[232:235], v211, s[36:39], s62 offen sc1\n"
    "v_cvt_f32_i32 v241, v241\n"
    "v_cvt_f32_i32 v242, v242\n"
    "v_cvt_f32_i32 v243, v243\n"
    "v_mfma_f32_16x16x32_bf16 v[68:71], v[180:183], a[236:239], v[68:71]\n"
    "v_cvt_f32_i32 v244, v244\n"
    "v_cvt_f32_i32 v245, v245\n"
    "v_mfma_f32_16x16x32_bf16 v[68:71], v[176:179], a[232:235], v[68:71]\n"
    "v_cvt_f32_i32 v246, v246\n"
    "v_cvt_f32_i32 v247, v247\n"
    "v_mfma_f32_16x16x32_bf16 v[72:75], v[176:179], v[0:3], v[72:75]\n"
    "ds_read_b128 v[172:175], v200 offset:33792\n"
    "v_mul_f32_dpp v240, v228, v240 row_newbcast:0 row_mask:0xf bank_mask:0xf\n"
    "v_mfma_f32_16x16x32_bf16 v[72:75], v[180:183], v[4:7], v[72:75]\n"
    "v_mul_f32_dpp v241, v228, v241 row_newbcast:1 row_mask:0xf bank_mask:0xf\n"
    "v_mul_f32_dpp v242, v228, v242 row_newbcast:2 row_mask:0xf bank_mask:0xf\n"
    "v_mfma_f32_16x16x32_bf16 v[76:79], v[180:183], v[28:31], v[76:79]\n"
    "v_mul_f32_dpp v243, v228, v243 row_newbcast:3 row_mask:0xf bank_mask:0xf\n"
    "v_mul_f32_dpp v244, v228, v244 row_newbcast:4 row_mask:0xf bank_mask:0xf\n"
    "v_mfma_f32_16x16x32_bf16 v[76:79], v[176:179], v[24:27], v[76:79]\n"
    "s_waitcnt lgkmcnt(2)\n"
    "v_mul_f32_dpp v245, v228, v245 row_newbcast:5 row_mask:0xf bank_mask:0xf\n"
    "v_mul_f32_dpp v246, v228, v246 row_newbcast:6 row_mask:0xf bank_mask:0xf\n"
    "v_mfma_f32_16x16x32_bf16 v[92:95], v[160:163], v[24:27], v[108:111]\n"
    "ds_read_b128 v[176:179], v200 offset:30720\n"
    "v_mul_f32_dpp v247, v228, v247 row_newbcast:7 row_mask:0xf bank_mask:0xf\n"
    "v_mfma_f32_16x16x32_bf16 v[92:95], v[164:167], v[28:31], v[92:95]\n"
    "v_add_f32_dpp v240, v229, v240 row_newbcast:0 row_mask:0xf bank_mask:0xf\n"
    "v_add_f32_dpp v241, v229, v241 row_newbcast:1 row_mask:0xf bank_mask:0xf\n"
    "v_mfma_f32_16x16x32_bf16 v[88:91], v[164:167], v[4:7], v[104:107]\n"
    "v_add_f32_dpp v242, v229, v242 row_newbcast:2 row_mask:0xf bank_mask:0xf\n"
    "v_add_f32_dpp v243, v229, v243 row_newbcast:3 row_mask:0xf bank_mask:0xf\n"
    "v_mfma_f32_16x16x32_bf16 v[88:91], v[160:163], v[0:3], v[88:91]\n"
    "v_add_f32_dpp v244, v229, v244 row_newbcast:4 row_mask:0xf bank_mask:0xf\n"
    "v_add_f32_dpp v245, v229, v245 row_newbcast:5 row_mask:0xf bank_mask:0xf\n"
    "v_mfma_f32_16x16x32_bf16 v[84:87], v[160:163], a[232:235], v[100:103]\n"
    "ds_read_b128 v[180:183], v200 offset:31744\n"
    "v_add_f32_dpp v246, v229, v246 row_newbcast:6 row_mask:0xf bank_mask:0xf\n"
    "v_mfma_f32_16x16x32_bf16 v[84:87], v[164:167], a[236:239], v[84:87]\n"
    "v_add_f32_dpp v247, v229, v247 row_newbcast:7 row_mask:0xf bank_mask:0xf\n"
    "v_cvt_pk_bf16_f32 v141, v46, v47\n"
    "v_mfma_f32_16x16x32_bf16 v[80:83], v[164:167], a[212:215], v[96:99]\n"
    "v_cvt_pk_bf16_f32 v240, v240, v241\n"
    "v_cvt_pk_bf16_f32 v136, v40, v41\n"
    "v_mfma_f32_16x16x32_bf16 v[80:83], v[160:163], a[208:211], v[80:83]\n"
    "s_waitcnt lgkmcnt(2)\n"
    "v_cvt_pk_bf16_f32 v241, v242, v243\n"
    "v_cvt_pk_bf16_f32 v137, v42, v43\n"
    "v_mfma_f32_16x16x32_bf16 v[80:83], v[168:171], a[200:203], v[80:83]\n"
    "ds_read_b128 v[160:163], v200 offset:36864\n"
    "v_cvt_pk_bf16_f32 v242, v244, v245\n"
    "v_mfma_f32_16x16x32_bf16 v[80:83], v[172:175], a[204:207], v[80:83]\n"
    "v_cvt_pk_bf16_f32 v132, v36, v37\n"
    "v_cvt_pk_bf16_f32 v243, v246, v247\n"
    "v_mfma_f32_16x16x32_bf16 v[84:87], v[172:175], a[228:231], v[84:87]\n"
    "v_cvt_pk_bf16_f32 v133, v38, v39\n"
    "v_cvt_pk_bf16_f32 v128, v32, v33\n"
    "v_mfma_f32_16x16x32_bf16 v[84:87], v[168:171], a[224:227], v[84:87]\n"
    "v_cvt_pk_bf16_f32 v129, v34, v35\n"
    "v_cvt_pk_bf16_f32 v142, v60, v61\n"
    "v_mfma_f32_16x16x32_bf16 v[88:91], v[168:171], a[248:251], v[88:91]\n"
    "ds_read_b128 v[164:167], v200 offset:37888\n"
    "v_cvt_pk_bf16_f32 v143, v62, v63\n"
    "v_mfma_f32_16x16x32_bf16 v[88:91], v[172:175], a[252:255], v[88:91]\n"
    "v_exp_f32 v64, v64\n"
    "v_mfma_f32_16x16x32_bf16 v[92:95], v[172:175], v[20:23], v[92:95]\n"
    "v_exp_f32 v65, v65\n"
    "v_mfma_f32_16x16x32_bf16 v[92:95], v[168:171], v[16:19], v[92:95]\n"
    "s_waitcnt lgkmcnt(2)\n"
    "v_exp_f32 v66, v66\n"
    "v_mfma_f32_16x16x32_bf16 v[92:95], v[176:179], v[8:11], v[92:95]\n"
    "ds_read_b128 v[168:171], v200 offset:38912\n"
    "v_cvt_pk_bf16_f32 v138, v56, v57\n"
    "v_mfma_f32_16x16x32_bf16 v[92:95], v[180:183], v[12:15], v[92:95]\n"
    "buffer_store_dwordx4 v[240:243], v212, s[40:43], s63 offen sc1\n"
    "v_exp_f32 v67, v67\n"
    "v_cvt_pk_bf16_f32 v139, v58, v59\n"
    "v_mfma_f32_16x16x32_bf16 v[88:91], v[180:183], a[244:247], v[88:91]\n"
    "v_exp_f32 v68, v68\n"
    "v_mfma_f32_16x16x32_bf16 v[88:91], v[176:179], a[240:243], v[88:91]\n"
    "v_exp_f32 v69, v69\n"
    "v_mfma_f32_16x16x32_bf16 v[84:87], v[176:179], a[216:219], v[84:87]\n"
    "ds_read_b128 v[172:175], v200 offset:39936\n"
    "v_cvt_pk_bf16_f32 v134, v52, v53\n"
    "v_mfma_f32_16x16x32_bf16 v[84:87], v[180:183], a[220:223], v[84:87]\n"
    "v_exp_f32 v70, v70\n"
    "v_mfma_f32_16x16x32_bf16 v[80:83], v[180:183], a[196:199], v[80:83]\n"
    "v_exp_f32 v71, v71\n"
    "v_mfma_f32_16x16x32_bf16 v[80:83], v[176:179], a[192:195], v[80:83]\n"
    "s_waitcnt lgkmcnt(2)\n"
    "v_exp_f32 v72, v72\n"
    "v_mfma_f32_16x16x32_bf16 v[32:35], v[160:163], a[192:195], v[96:99]\n"
    "ds_read_b128 v[176:179], v200 offset:40960\n"
    "v_cvt_pk_bf16_f32 v135, v54, v55\n"
    "v_mfma_f32_16x16x32_bf16 v[32:35], v[164:167], a[196:199], v[32:35]\n"
    "v_exp_f32 v73, v73\n"
    "v_mfma_f32_16x16x32_bf16 v[36:39], v[164:167], a[220:223], v[100:103]\n"
    "v_exp_f32 v74, v74\n"
    "v_mfma_f32_16x16x32_bf16 v[36:39], v[160:163], a[216:219], v[36:39]\n"
    "v_exp_f32 v75, v75\n"
    "v_mfma_f32_16x16x32_bf16 v[40:43], v[160:163], a[240:243], v[104:107]\n"
    "ds_read_b128 v[180:183], v200 offset:41984\n"
    "v_cvt_pk_bf16_f32 v130, v48, v49\n"
    "v_mfma_f32_16x16x32_bf16 v[40:43], v[164:167], a[244:247], v[40:43]\n"
    "v_exp_f32 v76, v76\n"
    "v_mfma_f32_16x16x32_bf16 v[44:47], v[164:167], v[12:15], v[108:111]\n"
    "v_exp_f32 v77, v77\n"
    "v_mfma_f32_16x16x32_bf16 v[44:47], v[160:163], v[8:11], v[44:47]\n"
    "s_waitcnt lgkmcnt(2)\n"
    "v_exp_f32 v78, v78\n"
    "v_mfma_f32_16x16x32_bf16 v[44:47], v[168:171], v[16:19], v[44:47]\n"
    "ds_read_b128 v[160:163], v200 offset:47104\n"
    "v_cvt_pk_bf16_f32 v131, v50, v51\n"
    "v_mfma_f32_16x16x32_bf16 v[44:47], v[172:175], v[20:23], v[44:47]\n"
    "v_exp_f32 v79, v79\n"
    "v_mfma_f32_16x16x32_bf16 v[40:43], v[172:175], a[252:255], v[40:43]\n"
    "v_exp_f32 v80, v80\n"
    "v_mfma_f32_16x16x32_bf16 v[40:43], v[168:171], a[248:251], v[40:43]\n"
    "v_exp_f32 v81, v81\n"
    "v_mfma_f32_16x16x32_bf16 v[36:39], v[168:171], a[224:227], v[36:39]\n"
    "ds_read_b128 v[164:167], v200 offset:48128\n"
    "v_cvt_pk_bf16_f32 v144, v64, v65\n"
    "v_mfma_f32_16x16x32_bf16 v[36:39], v[172:175], a[228:231], v[36:39]\n"
    "v_exp_f32 v82, v82\n"
    "v_mfma_f32_16x16x32_bf16 v[32:35], v[172:175], a[204:207], v[32:35]\n"
    "v_exp_f32 v83, v83\n"
    "v_mfma_f32_16x16x32_bf16 v[32:35], v[168:171], a[200:203], v[32:35]\n"
    "s_waitcnt lgkmcnt(2)\n"
    "v_exp_f32 v84, v84\n"
    "v_mfma_f32_16x16x32_bf16 v[32:35], v[176:179], a[208:211], v[32:35]\n"
    "ds_read_b128 v[168:171], v200 offset:45056\n"
    "v_cvt_pk_bf16_f32 v145, v66, v67\n"
    "v_mfma_f32_16x16x32_bf16 v[32:35], v[180:183], a[212:215], v[32:35]\n"
    "v_exp_f32 v85, v85\n"
    "v_mfma_f32_16x16x32_bf16 v[36:39], v[180:183], a[236:239], v[36:39]\n"
    "v_exp_f32 v86, v86\n"
    "v_mfma_f32_16x16x32_bf16 v[36:39], v[176:179], a[232:235], v[36:39]\n"
    "v_exp_f32 v87, v87\n"
    "v_mfma_f32_16x16x32_bf16 v[40:43], v[176:179], v[0:3], v[40:43]\n"
    "ds_read_b128 v[172:175], v200 offset:46080\n"
    "v_cvt_pk_bf16_f32 v148, v68, v69\n"
    "v_mfma_f32_16x16x32_bf16 v[40:43], v[180:183], v[4:7], v[40:43]\n"
    "v_exp_f32 v88, v88\n"
    "v_mfma_f32_16x16x32_bf16 v[44:47], v[180:183], v[28:31], v[44:47]\n"
    "v_cvt_pk_bf16_f32 v149, v70, v71\n"
    "v_cvt_pk_bf16_f32 v152, v72, v73\n"
    "v_mfma_f32_16x16x32_bf16 v[44:47], v[176:179], v[24:27], v[44:47]\n"
    "s_waitcnt lgkmcnt(2)\n"
    "v_exp_f32 v89, v89\n"
    "v_mfma_f32_16x16x32_bf16 v[60:63], v[160:163], v[24:27], v[108:111]\n"
    "s_mov_b32 m0, 0\n"
    "ds_read_b128 v[176:179], v200 offset:43008\n"
    "v_cvt_pk_bf16_f32 v153, v74, v75\n"
    "v_mfma_f32_16x16x32_bf16 v[60:63], v[164:167], v[28:31], v[60:63]\n"
    "v_exp_f32 v90, v90\n"
    "v_mfma_f32_16x16x32_bf16 v[56:59], v[164:167], v[4:7], v[104:107]\n"
    "buffer_load_dwordx4 v200, s[36:39], s60 offen sc1 lds\n"
    "v_mfma_f32_16x16x32_bf16 v[56:59], v[160:163], v[0:3], v[56:59]\n"
    "v_cvt_pk_bf16_f32 v156, v76, v77\n"
    "v_cvt_pk_bf16_f32 v157, v78, v79\n"
    "v_mfma_f32_16x16x32_bf16 v[52:55], v[160:163], a[232:235], v[100:103]\n"
    "ds_read_b128 v[180:183], v200 offset:44032\n"
    "v_cvt_pk_bf16_f32 v146, v80, v81\n"
    "v_mfma_f32_16x16x32_bf16 v[52:55], v[164:167], a[236:239], v[52:55]\n"
    "v_exp_f32 v91, v91\n"
    "v_mfma_f32_16x16x32_bf16 v[48:51], v[164:167], a[212:215], v[96:99]\n"
    "buffer_load_dwordx4 v200, s[36:39], s60 offen offset:1024 sc1 lds\n"
    "v_mfma_f32_16x16x32_bf16 v[48:51], v[160:163], a[208:211], v[48:51]\n"
    "s_waitcnt lgkmcnt(2)\n"
    "v_cvt_pk_bf16_f32 v147, v82, v83\n"
    "v_cvt_pk_bf16_f32 v150, v84, v85\n"
    "v_mfma_f32_16x16x32_bf16 v[48:51], v[168:171], a[200:203], v[48:51]\n"
    "ds_read_b128 v[160:163], v201\n"
    "v_cvt_pk_bf16_f32 v151, v86, v87\n"
    "v_mfma_f32_16x16x32_bf16 v[48:51], v[172:175], a[204:207], v[48:51]\n"
    "v_exp_f32 v92, v92\n"
    "v_mfma_f32_16x16x32_bf16 v[52:55], v[172:175], a[228:231], v[52:55]\n"
    "buffer_load_dwordx4 v200, s[36:39], s60 offen offset:2048 sc1 lds\n"
    "v_mfma_f32_16x16x32_bf16 v[52:55], v[168:171], a[224:227], v[52:55]\n"
    "v_exp_f32 v93, v93\n"
    "v_mfma_f32_16x16x32_bf16 v[56:59], v[168:171], a[248:251], v[56:59]\n"
    "s_add_u32 m0, s71, 0x12000\n"
    "ds_read_b128 v[164:167], v201 offset:1024\n"
    "v_cvt_pk_bf16_f32 v154, v88, v89\n"
    "v_mfma_f32_16x16x32_bf16 v[56:59], v[172:175], a[252:255], v[56:59]\n"
    "v_exp_f32 v94, v94\n"
    "v_mfma_f32_16x16x32_bf16 v[60:63], v[172:175], v[20:23], v[60:63]\n"
    "buffer_load_dwordx4 v200, s[40:43], s61 offen sc1 lds\n"
    "v_mfma_f32_16x16x32_bf16 v[60:63], v[168:171], v[16:19], v[60:63]\n"
    "s_waitcnt lgkmcnt(2)\n"
    "v_cvt_pk_bf16_f32 v155, v90, v91\n"
    "v_cvt_pk_bf16_f32 v158, v92, v93\n"
    "v_mfma_f32_16x16x32_bf16 v[60:63], v[176:179], v[8:11], v[60:63]\n"
    "ds_read_b128 v[168:171], v201 offset:2048\n"
    "v_add_f32 v113, v113, v64\n"
    "v_mfma_f32_16x16x32_bf16 v[60:63], v[180:183], v[12:15], v[60:63]\n"
    "v_exp_f32 v95, v95\n"
    "v_mfma_f32_16x16x32_bf16 v[56:59], v[180:183], a[244:247], v[56:59]\n"
    "buffer_load_dwordx4 v200, s[40:43], s61 offen offset:1024 sc1 lds\n"
    "v_mfma_f32_16x16x32_bf16 v[56:59], v[176:179], a[240:243], v[56:59]\n"
    "v_cvt_pk_bf16_f32 v159, v94, v95\n"
    "v_add_f32 v114, v114, v65\n"
    "v_mfma_f32_16x16x32_bf16 v[52:55], v[176:179], a[216:219], v[52:55]\n"
    "ds_read_b128 v[172:175], v201 offset:3072\n"
    "v_add_f32 v113, v113, v66\n"
    "v_mfma_f32_16x16x32_bf16 v[52:55], v[180:183], a[220:223], v[52:55]\n"
    "v_exp_f32 v32, v32\n"
    "v_mfma_f32_16x16x32_bf16 v[48:51], v[180:183], a[196:199], v[48:51]\n"
    "buffer_load_dwordx4 v200, s[40:43], s61 offen offset:2048 sc1 lds\n"
    "v_mfma_f32_16x16x32_bf16 v[48:51], v[176:179], a[192:195], v[48:51]\n"
    "s_waitcnt lgkmcnt(2)\n"
    "v_exp_f32 v33, v33\n"
    "v_mfma_f32_16x16x32_bf16 a[0:3], v[160:163], v[128:131], a[0:3]\n"
    "ds_read_b128 v[176:179], v201 offset:4096\n"
    "v_add_f32 v114, v114, v67\n"
    "v_mfma_f32_16x16x32_bf16 a[0:3], v[164:167], v[144:147], a[0:3]\n"
    "v_exp_f32 v34, v34\n"
    "v_mfma_f32_16x16x32_bf16 a[4:7], v[160:163], v[132:135], a[4:7]\n"
    "buffer_load_dwordx4 v200, s[40:43], s61 offen offset:3072 sc1 lds\n"
    "v_mfma_f32_16x16x32_bf16 a[4:7], v[164:167], v[148:151], a[4:7]\n"
    "s_add_u32 s65, s61, 0x1000\n"
    "v_exp_f32 v35, v35\n"
    "v_mfma_f32_16x16x32_bf16 a[8:11], v[160:163], v[136:139], a[8:11]\n"
    "s_add_u32 m0, s71, 0x13000\n"
    "ds_read_b128 v[180:183], v201 offset:5120\n"
    "v_add_f32 v117, v117, v68\n"
    "v_mfma_f32_16x16x32_bf16 a[8:11], v[164:167], v[152:155], a[8:11]\n"
    "v_exp_f32 v36, v36\n"
    "v_mfma_f32_16x16x32_bf16 a[12:15], v[160:163], v[140:143], a[12:15]\n"
    "buffer_load_dwordx4 v200, s[40:43], s65 offen sc1 lds\n"
    "v_mfma_f32_16x16x32_bf16 a[12:15], v[164:167], v[156:159], a[12:15]\n"
    "s_waitcnt lgkmcnt(2)\n"
    "v_exp_f32 v37, v37\n"
    "v_mfma_f32_16x16x32_bf16 a[28:31], v[168:171], v[140:143], a[28:31]\n"
    "ds_read_b128 v[160:163], v201 offset:6144\n"
    "v_add_f32 v118, v118, v69\n"
    "v_mfma_f32_16x16x32_bf16 a[28:31], v[172:175], v[156:159], a[28:31]\n"
    "v_exp_f32 v38, v38\n"
    "v_mfma_f32_16x16x32_bf16 a[24:27], v[172:175], v[152:155], a[24:27]\n"
    "buffer_load_dwordx4 v200, s[40:43], s65 offen offset:1024 sc1 lds\n"
    "v_mfma_f32_16x16x32_bf16 a[24:27], v[168:171], v[136:139], a[24:27]\n"
    "v_exp_f32 v39, v39\n"
    "v_mfma_f32_16x16x32_bf16 a[20:23], v[168:171], v[132:135], a[20:23]\n"
    "ds_read_b128 v[164:167], v201 offset:7168\n"
    "v_add_f32 v117, v117, v70\n"
    "v_mfma_f32_16x16x32_bf16 a[20:23], v[172:175], v[148:151], a[20:23]\n"
    "v_exp_f32 v40, v40\n"
    "v_mfma_f32_16x16x32_bf16 a[16:19], v[172:175], v[144:147], a[16:19]\n"
    "v_exp_f32 v41, v41\n"
    "v_mfma_f32_16x16x32_bf16 a[16:19], v[168:171], v[128:131], a[16:19]\n"
    "s_waitcnt lgkmcnt(2)\n"
    "v_exp_f32 v42, v42\n"
    "v_mfma_f32_16x16x32_bf16 a[32:35], v[176:179], v[128:131], a[32:35]\n"
    "ds_read_b128 v[168:171], v201 offset:8192\n"
    "v_add_f32 v113, v113, v32\n"
    "v_mfma_f32_16x16x32_bf16 a[32:35], v[180:183], v[144:147], a[32:35]\n"
    "buffer_load_dwordx2 v[218:219], v213, s[44:47], 0 offen sc1\n"
    "v_mfma_f32_16x16x32_bf16 a[36:39], v[180:183], v[148:151], a[36:39]\n"
    "v_exp_f32 v43, v43\n"
    "v_mfma_f32_16x16x32_bf16 a[36:39], v[176:179], v[132:135], a[36:39]\n"
    "v_exp_f32 v44, v44\n"
    "v_mfma_f32_16x16x32_bf16 a[40:43], v[176:179], v[136:139], a[40:43]\n"
    "ds_read_b128 v[172:175], v201 offset:9216\n"
    "v_add_f32 v118, v118, v71\n"
    "v_mfma_f32_16x16x32_bf16 a[40:43], v[180:183], v[152:155], a[40:43]\n"
    "v_exp_f32 v45, v45\n"
    "v_mfma_f32_16x16x32_bf16 a[44:47], v[180:183], v[156:159], a[44:47]\n"
    "v_exp_f32 v46, v46\n"
    "v_mfma_f32_16x16x32_bf16 a[44:47], v[176:179], v[140:143], a[44:47]\n"
    "s_waitcnt lgkmcnt(2)\n"
    "v_exp_f32 v47, v47\n"
    "v_mfma_f32_16x16x32_bf16 a[60:63], v[160:163], v[140:143], a[60:63]\n"
    "ds_read_b128 v[176:179], v201 offset:10240\n"
    "v_add_f32 v114, v114, v33\n"
    "v_mfma_f32_16x16x32_bf16 a[60:63], v[164:167], v[156:159], a[60:63]\n"
    "v_exp_f32 v48, v48\n"
    "v_mfma_f32_16x16x32_bf16 a[56:59], v[164:167], v[152:155], a[56:59]\n"
    "v_exp_f32 v49, v49\n"
    "v_mfma_f32_16x16x32_bf16 a[56:59], v[160:163], v[136:139], a[56:59]\n"
    "v_exp_f32 v50, v50\n"
    "v_mfma_f32_16x16x32_bf16 a[52:55], v[160:163], v[132:135], a[52:55]\n"
    "ds_read_b128 v[180:183], v201 offset:11264\n"
    "v_add_f32 v121, v121, v72\n"
    "v_mfma_f32_16x16x32_bf16 a[52:55], v[164:167], v[148:151], a[52:55]\n"
    "v_exp_f32 v51, v51\n"
    "v_mfma_f32_16x16x32_bf16 a[48:51], v[164:167], v[144:147], a[48:51]\n"
    "v_exp_f32 v52, v52\n"
    "v_mfma_f32_16x16x32_bf16 a[48:51], v[160:163], v[128:131], a[48:51]\n"
    "s_waitcnt lgkmcnt(2)\n"
    "v_add_f32 v113, v113, v34\n"
    "v_add_f32 v122, v122, v73\n"
    "v_mfma_f32_16x16x32_bf16 a[64:67], v[168:171], v[128:131], a[64:67]\n"
    "ds_read_b128 v[160:163], v201 offset:12288\n"
    "v_add_f32 v114, v114, v35\n"
    "v_mfma_f32_16x16x32_bf16 a[64:67], v[172:175], v[144:147], a[64:67]\n"
    "v_exp_f32 v53, v53\n"
    "v_mfma_f32_16x16x32_bf16 a[68:71], v[172:175], v[148:151], a[68:71]\n"
    "v_add_f32 v121, v121, v74\n"
    "v_add_f32 v117, v117, v36\n"
    "v_mfma_f32_16x16x32_bf16 a[68:71], v[168:171], v[132:135], a[68:71]\n"
    "s_add_u32 m0, s51, s95\n"
    "v_exp_f32 v54, v54\n"
    "v_mfma_f32_16x16x32_bf16 a[72:75], v[168:171], v[136:139], a[72:75]\n"
    "ds_read_b128 v[164:167], v201 offset:13312\n"
    "v_add_f32 v122, v122, v75\n"
    "v_mfma_f32_16x16x32_bf16 a[72:75], v[172:175], v[152:155], a[72:75]\n"
    "buffer_load_dwordx4 v202, s[32:35], s64 offen lds\n"
    "v_mfma_f32_16x16x32_bf16 a[76:79], v[172:175], v[156:159], a[76:79]\n"
    "v_add_f32 v118, v118, v37\n"
    "v_add_f32 v125, v125, v76\n"
    "v_mfma_f32_16x16x32_bf16 a[76:79], v[168:171], v[140:143], a[76:79]\n"
    "s_waitcnt lgkmcnt(2)\n"
    "v_add_f32 v117, v117, v38\n"
    "v_add_f32 v126, v126, v77\n"
    "v_mfma_f32_16x16x32_bf16 a[92:95], v[176:179], v[140:143], a[92:95]\n"
    "ds_read_b128 v[168:171], v201 offset:14336\n"
    "v_add_f32 v118, v118, v39\n"
    "v_mfma_f32_16x16x32_bf16 a[92:95], v[180:183], v[156:159], a[92:95]\n"
    "v_exp_f32 v55, v55\n"
    "v_mfma_f32_16x16x32_bf16 a[88:91], v[180:183], v[152:155], a[88:91]\n"
    "buffer_load_dwordx4 v203, s[32:35], s64 offen offset:1024 lds\n"
    "v_mfma_f32_16x16x32_bf16 a[88:91], v[176:179], v[136:139], a[88:91]\n"
    "v_add_f32 v125, v125, v78\n"
    "v_add_f32 v121, v121, v40\n"
    "v_mfma_f32_16x16x32_bf16 a[84:87], v[176:179], v[132:135], a[84:87]\n"
    "ds_read_b128 v[172:175], v201 offset:15360\n"
    "v_add_f32 v126, v126, v79\n"
    "v_mfma_f32_16x16x32_bf16 a[84:87], v[180:183], v[148:151], a[84:87]\n"
    "v_exp_f32 v56, v56\n"
    "v_mfma_f32_16x16x32_bf16 a[80:83], v[180:183], v[144:147], a[80:83]\n"
    "v_add_f32 v122, v122, v41\n"
    "v_add_f32 v113, v113, v80\n"
    "v_mfma_f32_16x16x32_bf16 a[80:83], v[176:179], v[128:131], a[80:83]\n"
    "s_waitcnt lgkmcnt(2)\n"
    "buffer_load_dwordx4 v204, s[32:35], s64 offen offset:2048 lds\n"
    "v_mfma_f32_16x16x32_bf16 a[96:99], v[160:163], v[128:131], a[96:99]\n"
    "ds_read_b128 v[176:179], v201 offset:16384\n"
    "v_add_f32 v121, v121, v42\n"
    "v_mfma_f32_16x16x32_bf16 a[96:99], v[164:167], v[144:147], a[96:99]\n"
    "v_exp_f32 v57, v57\n"
    "v_mfma_f32_16x16x32_bf16 a[100:103], v[164:167], v[148:151], a[100:103]\n"
    "v_add_f32 v114, v114, v81\n"
    "v_add_f32 v122, v122, v43\n"
    "v_mfma_f32_16x16x32_bf16 a[100:103], v[160:163], v[132:135], a[100:103]\n"
    "v_add_f32 v113, v113, v82\n"
    "v_add_f32 v125, v125, v44\n"
    "v_mfma_f32_16x16x32_bf16 a[104:107], v[160:163], v[136:139], a[104:107]\n"
    "s_mov_b32 s95, s94\n"
    "ds_read_b128 v[180:183], v201 offset:17408\n"
    "v_add_f32 v114, v114, v83\n"
    "v_mfma_f32_16x16x32_bf16 a[104:107], v[164:167], v[152:155], a[104:107]\n"
    "s_sub_u32 s94, s94, 0x3000\n"
    "v_exp_f32 v58, v58\n"
    "v_mfma_f32_16x16x32_bf16 a[108:111], v[164:167], v[156:159], a[108:111]\n"
    "s_min_u32 s94, s94, 0x6000\n"
    "v_add_f32 v126, v126, v45\n"
    "v_add_f32 v117, v117, v84\n"
    "v_mfma_f32_16x16x32_bf16 a[108:111], v[160:163], v[140:143], a[108:111]\n"
    "s_waitcnt lgkmcnt(2)\n"
    "v_add_f32 v125, v125, v46\n"
    "v_add_f32 v118, v118, v85\n"
    "v_mfma_f32_16x16x32_bf16 a[124:127], v[168:171], v[140:143], a[124:127]\n"
    "s_sub_u32 s93, s94, s95\n"
    "ds_read_b128 v[160:163], v201 offset:18432\n"
    "v_add_f32 v126, v126, v47\n"
    "v_mfma_f32_16x16x32_bf16 a[124:127], v[172:175], v[156:159], a[124:127]\n"
    "v_exp_f32 v59, v59\n"
    "v_mfma_f32_16x16x32_bf16 a[120:123], v[172:175], v[152:155], a[120:123]\n"
    "v_add_f32 v117, v117, v86\n"
    "v_add_f32 v113, v113, v48\n"
    "v_mfma_f32_16x16x32_bf16 a[120:123], v[168:171], v[136:139], a[120:123]\n"
    "v_exp_f32 v60, v60\n"
    "v_mfma_f32_16x16x32_bf16 a[116:119], v[168:171], v[132:135], a[116:119]\n"
    "ds_read_b128 v[164:167], v201 offset:19456\n"
    "v_add_u32 v220, 1, v220\n"
    "v_mfma_f32_16x16x32_bf16 a[116:119], v[172:175], v[148:151], a[116:119]\n"
    "v_add_u32 v205, s93, v205\n"
    "v_add_f32 v118, v118, v87\n"
    "v_mfma_f32_16x16x32_bf16 a[112:115], v[172:175], v[144:147], a[112:115]\n"
    "v_add_f32 v114, v114, v49\n"
    "v_add_u32 v209, 0x180, v209\n"
    "v_mfma_f32_16x16x32_bf16 a[112:115], v[168:171], v[128:131], a[112:115]\n"
    "s_waitcnt lgkmcnt(2)\n"
    "v_add_u32 v206, s93, v206\n"
    "v_add_f32 v121, v121, v88\n"
    "v_mfma_f32_16x16x32_bf16 a[128:131], v[176:179], v[128:131], a[128:131]\n"
    "s_add_u32 s64, s64, 0xc000\n"
    "ds_read_b128 v[168:171], v201 offset:20480\n"
    "v_add_f32 v113, v113, v50\n"
    "v_mfma_f32_16x16x32_bf16 a[128:131], v[180:183], v[144:147], a[128:131]\n"
    "s_add_u32 s62, s62, 0x6000\n"
    "v_exp_f32 v61, v61\n"
    "v_mfma_f32_16x16x32_bf16 a[132:135], v[180:183], v[148:151], a[132:135]\n"
    "s_add_u32 s63, s63, 0x6000\n"
    "v_add_u32 v210, 32, v210\n"
    "v_add_u32 v207, s93, v207\n"
    "v_mfma_f32_16x16x32_bf16 a[132:135], v[176:179], v[132:135], a[132:135]\n"
    "s_add_u32 s60, s60, 0x6000\n"
    "v_add_f32 v122, v122, v89\n"
    "v_add_f32 v114, v114, v51\n"
    "v_mfma_f32_16x16x32_bf16 a[136:139], v[176:179], v[136:139], a[136:139]\n"
    "s_add_u32 s61, s61, 0x6000\n"
    "ds_read_b128 v[172:175], v201 offset:21504\n"
    "v_add_f32 v121, v121, v90\n"
    "v_mfma_f32_16x16x32_bf16 a[136:139], v[180:183], v[152:155], a[136:139]\n"
    "s_add_u32 s66, s66, s74\n"
    "v_add_f32 v117, v117, v52\n"
    "v_add_f32 v122, v122, v91\n"
    "v_mfma_f32_16x16x32_bf16 a[140:143], v[180:183], v[156:159], a[140:143]\n"
    "s_min_u32 s66, s66, 33\n"
    "v_exp_f32 v62, v62\n"
    "v_mfma_f32_16x16x32_bf16 a[140:143], v[176:179], v[140:143], a[140:143]\n"
    "s_waitcnt lgkmcnt(2)\n"
    "v_add_f32 v118, v118, v53\n"
    "v_add_f32 v125, v125, v92\n"
    "v_mfma_f32_16x16x32_bf16 a[156:159], v[160:163], v[140:143], a[156:159]\n"
    "ds_read_b128 v[176:179], v201 offset:22528\n"
    "v_add_f32 v117, v117, v54\n"
    "v_mfma_f32_16x16x32_bf16 a[156:159], v[164:167], v[156:159], a[156:159]\n"
    "s_waitcnt vmcnt(3)\n"
    "ds_read_b128 v[180:183], v201 offset:23552\n"
    "v_add_f32 v126, v126, v93\n"
    "v_mfma_f32_16x16x32_bf16 a[152:155], v[164:167], v[152:155], a[152:155]\n"
    "v_cmp_ne_u32 s[80:81], s67, v219\n"
    "v_add_f32 v118, v118, v55\n"
    "v_mfma_f32_16x16x32_bf16 a[152:155], v[160:163], v[136:139], a[152:155]\n"
    "v_cmp_gt_u32 vcc, s66, v218\n"
    "v_add_f32 v125, v125, v94\n"
    "v_mfma_f32_16x16x32_bf16 a[148:151], v[160:163], v[132:135], a[148:151]\n"
    "v_exp_f32 v63, v63\n"
    "v_mfma_f32_16x16x32_bf16 a[148:151], v[164:167], v[148:151], a[148:151]\n"
    "s_or_b64 vcc, vcc, s[80:81]\n"
    "s_cbranch_vccnz LSPRE4\n"
    "LSPB4:\n"
    "v_add_f32 v121, v121, v56\n"
    "v_mfma_f32_16x16x32_bf16 a[144:147], v[164:167], v[144:147], a[144:147]\n"
    "v_add_f32 v126, v126, v95\n"
    "v_add_f32 v122, v122, v57\n"
    "v_mfma_f32_16x16x32_bf16 a[144:147], v[160:163], v[128:131], a[144:147]\n"
    "s_waitcnt lgkmcnt(0)\n"
    "v_mfma_f32_16x16x32_bf16 a[160:163], v[168:171], v[128:131], a[160:163]\n"
    "s_barrier\n"
    "v_mfma_f32_16x16x32_bf16 a[160:163], v[172:175], v[144:147], a[160:163]\n"
    "buffer_store_dwordx2 v[220:221], v214, s[44:47], 0 offen sc1\n"
    "v_mfma_f32_16x16x32_bf16 a[164:167], v[172:175], v[148:151], a[164:167]\n"
    "ds_read_b128 v[160:163], v200\n"
    "ds_read_u16_d16_hi v223, v209 offset:6144\n"
    "v_mfma_f32_16x16x32_bf16 a[164:167], v[168:171], v[132:135], a[164:167]\n"
    "ds_read_u16_d16_hi v222, v209\n"
    "v_add_f32 v121, v121, v58\n"
    "v_mfma_f32_16x16x32_bf16 a[168:171], v[168:171], v[136:139], a[168:171]\n"
    "ds_read_b128 v[164:167], v200 offset:1024\n"
    "ds_read_b128 v[232:235], v205 offset:24576\n"
    "v_mfma_f32_16x16x32_bf16 a[168:171], v[172:175], v[152:155], a[168:171]\n"
    "v_add_f32 v122, v122, v59\n"
    "ds_read_b128 v[236:239], v206 offset:24576\n"
    "v_mfma_f32_16x16x32_bf16 a[172:175], v[172:175], v[156:159], a[172:175]\n"
    "v_add_f32 v125, v125, v60\n"
    "v_add_f32 v126, v126, v61\n"
    "v_mfma_f32_16x16x32_bf16 a[172:175], v[168:171], v[140:143], a[172:175]\n"
    "v_add_f32 v125, v125, v62\n"
    "v_add_f32 v126, v126, v63\n"
    "v_mfma_f32_16x16x32_bf16 a[188:191], v[176:179], v[140:143], a[188:191]\n"
    "ds_read_b128 v[168:171], v200 offset:2048\n"
    "ds_read_u16_d16_hi v225, v210 offset:512\n"
    "v_mfma_f32_16x16x32_bf16 a[188:191], v[180:183], v[156:159], a[188:191]\n"
    "ds_read_u16_d16_hi v224, v210\n"
    "ds_read2st64_b32 v[240:241], v207 offset0:96 offset1:99\n"
    "v_mfma_f32_16x16x32_bf16 a[184:187], v[176:179], v[136:139], a[184:187]\n"
    "ds_read2st64_b32 v[242:243], v207 offset0:102 offset1:105\n"
    "ds_read2st64_b32 v[244:245], v207 offset0:108 offset1:111\n"
    "v_mfma_f32_16x16x32_bf16 a[184:187], v[180:183], v[152:155], a[184:187]\n"
    "ds_read2st64_b32 v[246:247], v207 offset0:114 offset1:117\n"
    "v_cvt_pk_bf16_f32 v140, v44, v45\n"
    "v_mfma_f32_16x16x32_bf16 a[180:183], v[176:179], v[132:135], a[180:183]\n"
    "ds_read_b128 v[172:175], v200 offset:3072\n"
    "s_waitcnt lgkmcnt(8)\n"
    "v_mfma_f32_16x16x32_bf16 a[180:183], v[180:183], v[148:151], a[180:183]\n"
    "v_mul_f32 v226, s75, v223\n"
    "v_fma_f32 v227, -v222, v226, v217\n"
    "v_mfma_f32_16x16x32_bf16 a[176:179], v[176:179], v[128:131], a[176:179]\n"
    "v_cvt_f32_i32 v232, v232\n"
    "v_cvt_f32_i32 v233, v233\n"
    "v_mfma_f32_16x16x32_bf16 a[176:179], v[180:183], v[144:147], a[176:179]\n"
    "v_cvt_f32_i32 v234, v234\n"
    "v_cvt_f32_i32 v235, v235\n"
    "LM0:\n"
    "v_mfma_f32_16x16x32_bf16 v[64:67], v[160:163], a[192:195], v[96:99]\n"
    "ds_read_b128 v[176:179], v200 offset:4096\n"
    "v_cvt_f32_i32 v236, v236\n"
    "v_mfma_f32_16x16x32_bf16 v[64:67], v[164:167], a[196:199], v[64:67]\n"
    "v_cvt_f32_i32 v237, v237\n"
    "v_cvt_f32_i32 v238, v238\n"
    "v_mfma_f32_16x16x32_bf16 v[68:71], v[164:167], a[220:223], v[100:103]\n"
    "v_cvt_f32_i32 v239, v239\n"
    "v_mul_f32_dpp v232, v226, v232 row_newbcast:0 row_mask:0xf bank_mask:0xf\n"
    "v_mfma_f32_16x16x32_bf16 v[68:71], v[160:163], a[216:219], v[68:71]\n"
    "v_mul_f32_dpp v233, v226, v233 row_newbcast:1 row_mask:0xf bank_mask:0xf\n"
    "v_mul_f32_dpp v234, v226, v234 row_newbcast:2 row_mask:0xf bank_mask:0xf\n"
    "v_mfma_f32_16x16x32_bf16 v[72:75], v[160:163], a[240:243], v[104:107]\n"
    "ds_read_b128 v[180:183], v200 offset:5120\n"
    "v_mul_f32_dpp v235, v226, v235 row_newbcast:3 row_mask:0xf bank_mask:0xf\n"
    "v_mfma_f32_16x16x32_bf16 v[72:75], v[164:167], a[244:247], v[72:75]\n"
    "v_mul_f32_dpp v236, v226, v236 row_newbcast:4 row_mask:0xf bank_mask:0xf\n"
    "v_mul_f32_dpp v237, v226, v237 row_newbcast:5 row_mask:0xf bank_mask:0xf\n"
    "v_mfma_f32_16x16x32_bf16 v[76:79], v[164:167], v[12:15], v[108:111]\n"
    "v_mul_f32_dpp v238, v226, v238 row_newbcast:6 row_mask:0xf bank_mask:0xf\n"
    "v_mul_f32_dpp v239, v226, v239 row_newbcast:7 row_mask:0xf bank_mask:0xf\n"
    "v_mfma_f32_16x16x32_bf16 v[76:79], v[160:163], v[8:11], v[76:79]\n"
    "s_waitcnt lgkmcnt(2)\n"
    "v_add_f32_dpp v232, v227, v232 row_newbcast:0 row_mask:0xf bank_mask:0xf\n"
    "v_add_f32_dpp v233, v227, v233 row_newbcast:1 row_mask:0xf bank_mask:0xf\n"
    "v_mfma_f32_16x16x32_bf16 v[76:79], v[168:171], v[16:19], v[76:79]\n"
    "ds_read_b128 v[160:163], v200 offset:10240\n"
    "v_add_f32_dpp v234, v227, v234 row_newbcast:2 row_mask:0xf bank_mask:0xf\n"
    "v_mfma_f32_16x16x32_bf16 v[76:79], v[172:175], v[20:23], v[76:79]\n"
    "v_add_f32_dpp v235, v227, v235 row_newbcast:3 row_mask:0xf bank_mask:0xf\n"
    "v_add_f32_dpp v236, v227, v236 row_newbcast:4 row_mask:0xf bank_mask:0xf\n"
    "v_mfma_f32_16x16x32_bf16 v[72:75], v[172:175], a[252:255], v[72:75]\n"
    "v_add_f32_dpp v237, v227, v237 row_newbcast:5 row_mask:0xf bank_mask:0xf\n"
    "v_add_f32_dpp v238, v227, v238 row_newbcast:6 row_mask:0xf bank_mask:0xf\n"
    "v_mfma_f32_16x16x32_bf16 v[72:75], v[168:171], a[248:251], v[72:75]\n"
    "v_add_f32_dpp v239, v227, v239 row_newbcast:7 row_mask:0xf bank_mask:0xf\n"
    "v_cvt_pk_bf16_f32 v232, v232, v233\n"
    "v_mfma_f32_16x16x32_bf16 v[68:71], v[168:171], a[224:227], v[68:71]\n"
    "ds_read_b128 v[164:167], v200 offset:11264\n"
    "v_cvt_pk_bf16_f32 v233, v234, v235\n"
    "v_mfma_f32_16x16x32_bf16 v[68:71], v[172:175], a[228:231], v[68:71]\n"
    "v_cvt_pk_bf16_f32 v234, v236, v237\n"
    "v_cvt_pk_bf16_f32 v235, v238, v239\n"
    "v_mfma_f32_16x16x32_bf16 v[64:67], v[172:175], a[204:207], v[64:67]\n"
    "v_mul_f32 v228, s68, v225\n"
    "v_fma_f32 v229, -v224, v228, s69\n"
    "v_mfma_f32_16x16x32_bf16 v[64:67], v[168:171], a[200:203], v[64:67]\n"
    "s_waitcnt lgkmcnt(2)\n"
    "v_cvt_f32_i32 v240, v240\n"
    "v_cvt_f32_i32 v241, v241\n"
    "v_mfma_f32_16x16x32_bf16 v[64:67], v[176:179], a[208:211], v[64:67]\n"
    "ds_read_b128 v[168:171], v200 offset:8192\n"
    "v_cvt_f32_i32 v242, v242\n"
    "v_mfma_f32_16x16x32_bf16 v[64:67], v[180:183], a[212:215], v[64:67]\n"
    "buffer_store_dwordx4 v[232:235], v211, s[36:39], s62 offen sc1\n"
    "v_cvt_f32_i32 v243, v243\n"
    "v_cvt_f32_i32 v244, v244\n"
    "v_cvt_f32_i32 v245, v245\n"
    "v_mfma_f32_16x16x32_bf16 v[68:71], v[180:183], a[236:239], v[68:71]\n"
    "v_cvt_f32_i32 v246, v246\n"
    "v_cvt_f32_i32 v247, v247\n"
    "v_mfma_f32_16x16x32_bf16 v[68:71], v[176:179], a[232:235], v[68:71]\n"
    "v_mul_f32_dpp v240, v228, v240 row_newbcast:0 row_mask:0xf bank_mask:0xf\n"
    "v_mul_f32_dpp v241, v228, v241 row_newbcast:1 row_mask:0xf bank_mask:0xf\n"
    "v_mfma_f32_16x16x32_bf16 v[72:75], v[176:179], v[0:3], v[72:75]\n"
    "ds_read_b128 v[172:175], v200 offset:9216\n"
    "v_mul_f32_dpp v242, v228, v242 row_newbcast:2 row_mask:0xf bank_mask:0xf\n"
    "v_mfma_f32_16x16x32_bf16 v[72:75], v[180:183], v[4:7], v[72:75]\n"
    "v_mul_f32_dpp v243, v228, v243 row_newbcast:3 row_mask:0xf bank_mask:0xf\n"
    "v_mul_f32_dpp v244, v228, v244 row_newbcast:4 row_mask:0xf bank_mask:0xf\n"
    "v_mfma_f32_16x16x32_bf16 v[76:79], v[180:183], v[28:31], v[76:79]\n"
    "v_mul_f32_dpp v245, v228, v245 row_newbcast:5 row_mask:0xf bank_mask:0xf\n"
    "v_mul_f32_dpp v246, v228, v246 row_newbcast:6 row_mask:0xf bank_mask:0xf\n"
    "v_mfma_f32_16x16x32_bf16 v[76:79], v[176:179], v[24:27], v[76:79]\n"
    "s_waitcnt lgkmcnt(2)\n"
    "v_mul_f32_dpp v247, v228, v247 row_newbcast:7 row_mask:0xf bank_mask:0xf\n"
    "v_add_f32_dpp v240, v229, v240 row_newbcast:0 row_mask:0xf bank_mask:0xf\n"
    "v_mfma_f32_16x16x32_bf16 v[92:95], v[160:163], v[24:27], v[108:111]\n"
    "ds_read_b128 v[176:179], v200 offset:6144\n"
    "v_add_f32_dpp v241, v229, v241 row_newbcast:1 row_mask:0xf bank_mask:0xf\n"
    "v_mfma_f32_16x16x32_bf16 v[92:95], v[164:167], v[28:31], v[92:95]\n"
    "v_add_f32_dpp v242, v229, v242 row_newbcast:2 row_mask:0xf bank_mask:0xf\n"
    "v_add_f32_dpp v243, v229, v243 row_newbcast:3 row_mask:0xf bank_mask:0xf\n"
    "v_mfma_f32_16x16x32_bf16 v[88:91], v[164:167], v[4:7], v[104:107]\n"
    "v_add_f32_dpp v244, v229, v244 row_newbcast:4 row_mask:0xf bank_mask:0xf\n"
    "v_add_f32_dpp v245, v229, v245 row_newbcast:5 row_mask:0xf bank_mask:0xf\n"
    "v_mfma_f32_16x16x32_bf16 v[88:91], v[160:163], v[0:3], v[88:91]\n"
    "v_add_f32_dpp v246, v229, v246 row_newbcast:6 row_mask:0xf bank_mask:0xf\n"
    "v_add_f32_dpp v247, v229, v247 row_newbcast:7 row_mask:0xf bank_mask:0xf\n"
    "v_mfma_f32_16x16x32_bf16 v[84:87], v[160:163], a[232:235], v[100:103]\n"
    "ds_read_b128 v[180:183], v200 offset:7168\n"
    "v_cvt_pk_bf16_f32 v141, v46, v47\n"
    "v_mfma_f32_16x16x32_bf16 v[84:87], v[164:167], a[236:239], v[84:87]\n"
    "v_cvt_pk_bf16_f32 v240, v240, v241\n"
    "v_cvt_pk_bf16_f32 v136, v40, v41\n"
    "v_mfma_f32_16x16x32_bf16 v[80:83], v[164:167], a[212:215], v[96:99]\n"
    "v_cvt_pk_bf16_f32 v241, v242, v243\n"
    "v_cvt_pk_bf16_f32 v137, v42, v43\n"
    "v_mfma_f32_16x16x32_bf16 v[80:83], v[160:163], a[208:211], v[80:83]\n"
    "s_waitcnt lgkmcnt(2)\n"
    "v_cvt_pk_bf16_f32 v242, v244, v245\n"
    "v_cvt_pk_bf16_f32 v132, v36, v37\n"
    "v_mfma_f32_16x16x32_bf16 v[80:83], v[168:171], a[200:203], v[80:83]\n"
    "ds_read_b128 v[160:163], v200 offset:12288\n"
    "v_cvt_pk_bf16_f32 v243, v246, v247\n"
    "v_mfma_f32_16x16x32_bf16 v[80:83], v[172:175], a[204:207], v[80:83]\n"
    "v_cvt_pk_bf16_f32 v133, v38, v39\n"
    "v_cvt_pk_bf16_f32 v128, v32, v33\n"
    "v_mfma_f32_16x16x32_bf16 v[84:87], v[172:175], a[228:231], v[84:87]\n"
    "v_cvt_pk_bf16_f32 v129, v34, v35\n"
    "v_cvt_pk_bf16_f32 v142, v60, v61\n"
    "v_mfma_f32_16x16x32_bf16 v[84:87], v[168:171], a[224:227], v[84:87]\n"
    "v_exp_f32 v64, v64\n"
    "v_mfma_f32_16x16x32_bf16 v[88:91], v[168:171], a[248:251], v[88:91]\n"
    "ds_read_b128 v[164:167], v200 offset:13312\n"
    "v_cvt_pk_bf16_f32 v143, v62, v63\n"
    "v_mfma_f32_16x16x32_bf16 v[88:91], v[172:175], a[252:255], v[88:91]\n"
    "v_exp_f32 v65, v65\n"
    "v_mfma_f32_16x16x32_bf16 v[92:95], v[172:175], v[20:23], v[92:95]\n"
    "v_exp_f32 v66, v66\n"
    "v_mfma_f32_16x16x32_bf16 v[92:95], v[168:171], v[16:19], v[92:95]\n"
    "s_waitcnt lgkmcnt(2)\n"
    "v_exp_f32 v67, v67\n"
    "v_mfma_f32_16x16x32_bf16 v[92:95], v[176:179], v[8:11], v[92:95]\n"
    "ds_read_b128 v[168:171], v200 offset:14336\n"
    "v_cvt_pk_bf16_f32 v138, v56, v57\n"
    "v_mfma_f32_16x16x32_bf16 v[92:95], v[180:183], v[12:15], v[92:95]\n"
    "buffer_store_dwordx4 v[240:243], v212, s[40:43], s63 offen sc1\n"
    "v_exp_f32 v68, v68\n"
    "v_cvt_pk_bf16_f32 v139, v58, v59\n"
    "v_mfma_f32_16x16x32_bf16 v[88:91], v[180:183], a[244:247], v[88:91]\n"
    "v_exp_f32 v69, v69\n"
    "v_mfma_f32_16x16x32_bf16 v[88:91], v[176:179], a[240:243], v[88:91]\n"
    "v_exp_f32 v70, v70\n"
    "v_mfma_f32_16x16x32_bf16 v[84:87], v[176:179], a[216:219], v[84:87]\n"
    "ds_read_b128 v[172:175], v200 offset:15360\n"
    "v_cvt_pk_bf16_f32 v134, v52, v53\n"
    "v_mfma_f32_16x16x32_bf16 v[84:87], v[180:183], a[220:223], v[84:87]\n"
    "v_exp_f32 v71, v71\n"
    "v_mfma_f32_16x16x32_bf16 v[80:83], v[180:183], a[196:199], v[80:83]\n"
    "v_exp_f32 v72, v72\n"
    "v_mfma_f32_16x16x32_bf16 v[80:83], v[176:179], a[192:195], v[80:83]\n"
    "s_waitcnt lgkmcnt(2)\n"
    "v_exp_f32 v73, v73\n"
    "v_mfma_f32_16x16x32_bf16 v[32:35], v[160:163], a[192:195], v[96:99]\n"
    "ds_read_b128 v[176:179], v200 offset:16384\n"
    "v_cvt_pk_bf16_f32 v135, v54, v55\n"
    "v_mfma_f32_16x16x32_bf16 v[32:35], v[164:167], a[196:199], v[32:35]\n"
    "v_exp_f32 v74, v74\n"
    "v_mfma_f32_16x16x32_bf16 v[36:39], v[164:167], a[220:223], v[100:103]\n"
    "v_exp_f32 v75, v75\n"
    "v_mfma_f32_16x16x32_bf16 v[36:39], v[160:163], a[216:219], v[36:39]\n"
    "v_exp_f32 v76, v76\n"
    "v_mfma_f32_16x16x32_bf16 v[40:43], v[160:163], a[240:243], v[104:107]\n"
    "ds_read_b128 v[180:183], v200 offset:17408\n"
    "v_cvt_pk_bf16_f32 v130, v48, v49\n"
    "v_mfma_f32_16x16x32_bf16 v[40:43], v[164:167], a[244:247], v[40:43]\n"
    "v_exp_f32 v77, v77\n"
    "v_mfma_f32_16x16x32_bf16 v[44:47], v[164:167], v[12:15], v[108:111]\n"
    "v_exp_f32 v78, v78\n"
    "v_mfma_f32_16x16x32_bf16 v[44:47], v[160:163], v[8:11], v[44:47]\n"
    "s_waitcnt lgkmcnt(2)\n"
    "v_cvt_pk_bf16_f32 v131, v50, v51\n"
    "v_cvt_pk_bf16_f32 v144, v64, v65\n"
    "v_mfma_f32_16x16x32_bf16 v[44:47], v[168:171], v[16:19], v[44:47]\n"
    "ds_read_b128 v[160:163], v200 offset:22528\n"
    "v_cvt_pk_bf16_f32 v145, v66, v67\n"
    "v_mfma_f32_16x16x32_bf16 v[44:47], v[172:175], v[20:23], v[44:47]\n"
    "v_exp_f32 v79, v79\n"
    "v_mfma_f32_16x16x32_bf16 v[40:43], v[172:175], a[252:255], v[40:43]\n"
    "v_exp_f32 v80, v80\n"
    "v_mfma_f32_16x16x32_bf16 v[40:43], v[168:171], a[248:251], v[40:43]\n"
    "v_exp_f32 v81, v81\n"
    "v_mfma_f32_16x16x32_bf16 v[36:39], v[168:171], a[224:227], v[36:39]\n"
    "ds_read_b128 v[164:167], v200 offset:23552\n"
    "v_cvt_pk_bf16_f32 v148, v68, v69\n"
    "v_mfma_f32_16x16x32_bf16 v[36:39], v[172:175], a[228:231], v[36:39]\n"
    "v_exp_f32 v82, v82\n"
    "v_mfma_f32_16x16x32_bf16 v[32:35], v[172:175], a[204:207], v[32:35]\n"
    "v_exp_f32 v83, v83\n"
    "v_mfma_f32_16x16x32_bf16 v[32:35], v[168:171], a[200:203], v[32:35]\n"
    "s_waitcnt lgkmcnt(2)\n"
    "v_exp_f32 v84, v84\n"
    "v_mfma_f32_16x16x32_bf16 v[32:35], v[176:179], a[208:211], v[32:35]\n"
    "ds_read_b128 v[168:171], v200 offset:20480\n"
    "v_cvt_pk_bf16_f32 v149, v70, v71\n"
    "v_mfma_f32_16x16x32_bf16 v[32:35], v[180:183], a[212:215], v[32:35]\n"
    "v_exp_f32 v85, v85\n"
    "v_mfma_f32_16x16x32_bf16 v[36:39], v[180:183], a[236:239], v[36:39]\n"
    "v_exp_f32 v86, v86\n"
    "v_mfma_f32_16x16x32_bf16 v[36:39], v[176:179], a[232:235], v[36:39]\n"
    "v_exp_f32 v87, v87\n"
    "v_mfma_f32_16x16x32_bf16 v[40:43], v[176:179], v[0:3], v[40:43]\n"
    "ds_read_b128 v[172:175], v200 offset:21504\n"
    "v_cvt_pk_bf16_f32 v152, v72, v73\n"
    "v_mfma_f32_16x16x32_bf16 v[40:43], v[180:183], v[4:7], v[40:43]\n"
    "v_exp_f32 v88, v88\n"
    "v_mfma_f32_16x16x32_bf16 v[44:47], v[180:183], v[28:31], v[44:47]\n"
    "v_exp_f32 v89, v89\n"
    "v_mfma_f32_16x16x32_bf16 v[44:47], v[176:179], v[24:27], v[44:47]\n"
    "s_waitcnt lgkmcnt(2)\n"
    "v_cvt_pk_bf16_f32 v153, v74, v75\n"
    "v_cvt_pk_bf16_f32 v156, v76, v77\n"
    "v_mfma_f32_16x16x32_bf16 v[60:63], v[160:163], v[24:27], v[108:111]\n"
    "s_mov_b32 m0, 0x6000\n"
    "ds_read_b128 v[176:179], v200 offset:18432\n"
    "v_cvt_pk_bf16_f32 v157, v78, v79\n"
    "v_mfma_f32_16x16x32_bf16 v[60:63], v[164:167], v[28:31], v[60:63]\n"
    "v_exp_f32 v90, v90\n"
    "v_mfma_f32_16x16x32_bf16 v[56:59], v[164:167], v[4:7], v[104:107]\n"
    "buffer_load_dwordx4 v200, s[36:39], s60 offen sc1 lds\n"
    "v_mfma_f32_16x16x32_bf16 v[56:59], v[160:163], v[0:3], v[56:59]\n"
    "v_exp_f32 v91, v91\n"
    "v_mfma_f32_16x16x32_bf16 v[52:55], v[160:163], a[232:235], v[100:103]\n"
    "ds_read_b128 v[180:183], v200 offset:19456\n"
    "v_cvt_pk_bf16_f32 v146, v80, v81\n"
    "v_mfma_f32_16x16x32_bf16 v[52:55], v[164:167], a[236:239], v[52:55]\n"
    "v_cvt_pk_bf16_f32 v147, v82, v83\n"
    "v_cvt_pk_bf16_f32 v150, v84, v85\n"
    "v_mfma_f32_16x16x32_bf16 v[48:51], v[164:167], a[212:215], v[96:99]\n"
    "buffer_load_dwordx4 v200, s[36:39], s60 offen offset:1024 sc1 lds\n"
    "v_mfma_f32_16x16x32_bf16 v[48:51], v[160:163], a[208:211], v[48:51]\n"
    "s_waitcnt lgkmcnt(2)\n"
    "v_exp_f32 v92, v92\n"
    "v_mfma_f32_16x16x32_bf16 v[48:51], v[168:171], a[200:203], v[48:51]\n"
    "ds_read_b128 v[160:163], v201 offset:24576\n"
    "v_cvt_pk_bf16_f32 v151, v86, v87\n"
    "v_mfma_f32_16x16x32_bf16 v[48:51], v[172:175], a[204:207], v[48:51]\n"
    "v_exp_f32 v93, v93\n"
    "v_mfma_f32_16x16x32_bf16 v[52:55], v[172:175], a[228:231], v[52:55]\n"
    "buffer_load_dwordx4 v200, s[36:39], s60 offen offset:2048 sc1 lds\n"
    "v_mfma_f32_16x16x32_bf16 v[52:55], v[168:171], a[224:227], v[52:55]\n"
    "v_exp_f32 v94, v94\n"
    "v_mfma_f32_16x16x32_bf16 v[56:59], v[168:171], a[248:251], v[56:59]\n"
    "s_add_u32 m0, s71, 0xc000\n"
    "ds_read_b128 v[164:167], v201 offset:25600\n"
    "v_cvt_pk_bf16_f32 v154, v88, v89\n"
    "v_mfma_f32_16x16x32_bf16 v[56:59], v[172:175], a[252:255], v[56:59]\n"
    "v_cvt_pk_bf16_f32 v155, v90, v91\n"
    "v_cvt_pk_bf16_f32 v158, v92, v93\n"
    "v_mfma_f32_16x16x32_bf16 v[60:63], v[172:175], v[20:23], v[60:63]\n"
    "buffer_load_dwordx4 v200, s[40:43], s61 offen sc1 lds\n"
    "v_mfma_f32_16x16x32_bf16 v[60:63], v[168:171], v[16:19], v[60:63]\n"
    "s_waitcnt lgkmcnt(2)\n"
    "v_exp_f32 v95, v95\n"
    "v_mfma_f32_16x16x32_bf16 v[60:63], v[176:179], v[8:11], v[60:63]\n"
    "ds_read_b128 v[168:171], v201 offset:26624\n"
    "v_cvt_pk_bf16_f32 v159, v94, v95\n"
    "v_mfma_f32_16x16x32_bf16 v[60:63], v[180:183], v[12:15], v[60:63]\n"
    "v_exp_f32 v32, v32\n"
    "v_mfma_f32_16x16x32_bf16 v[56:59], v[180:183], a[244:247], v[56:59]\n"
    "buffer_load_dwordx4 v200, s[40:43], s61 offen offset:1024 sc1 lds\n"
    "v_mfma_f32_16x16x32_bf16 v[56:59], v[176:179], a[240:243], v[56:59]\n"
    "v_exp_f32 v33, v33\n"
    "v_mfma_f32_16x16x32_bf16 v[52:55], v[176:179], a[216:219], v[52:55]\n"
    "ds_read_b128 v[172:175], v201 offset:27648\n"
    "v_add_f32 v113, v113, v64\n"
    "v_mfma_f32_16x16x32_bf16 v[52:55], v[180:183], a[220:223], v[52:55]\n"
    "v_exp_f32 v34, v34\n"
    "v_mfma_f32_16x16x32_bf16 v[48:51], v[180:183], a[196:199], v[48:51]\n"
    "buffer_load_dwordx4 v200, s[40:43], s61 offen offset:2048 sc1 lds\n"
    "v_mfma_f32_16x16x32_bf16 v[48:51], v[176:179], a[192:195], v[48:51]\n"
    "s_waitcnt lgkmcnt(2)\n"
    "v_exp_f32 v35, v35\n"
    "v_mfma_f32_16x16x32_bf16 a[0:3], v[160:163], v[128:131], a[0:3]\n"
    "ds_read_b128 v[176:179], v201 offset:28672\n"
    "v_add_f32 v114, v114, v65\n"
    "v_mfma_f32_16x16x32_bf16 a[0:3], v[164:167], v[144:147], a[0:3]\n"
    "v_exp_f32 v36, v36\n"
    "v_mfma_f32_16x16x32_bf16 a[4:7], v[160:163], v[132:135], a[4:7]\n"
    "buffer_load_dwordx4 v200, s[40:43], s61 offen offset:3072 sc1 lds\n"
    "v_mfma_f32_16x16x32_bf16 a[4:7], v[164:167], v[148:151], a[4:7]\n"
    "s_add_u32 s65, s61, 0x1000\n"
    "v_exp_f32 v37, v37\n"
    "v_mfma_f32_16x16x32_bf16 a[8:11], v[160:163], v[136:139], a[8:11]\n"
    "s_add_u32 m0, s71, 0xd000\n"
    "ds_read_b128 v[180:183], v201 offset:29696\n"
    "v_add_f32 v113, v113, v66\n"
    "v_mfma_f32_16x16x32_bf16 a[8:11], v[164:167], v[152:155], a[8:11]\n"
    "v_exp_f32 v38, v38\n"
    "v_mfma_f32_16x16x32_bf16 a[12:15], v[160:163], v[140:143], a[12:15]\n"
    "buffer_load_dwordx4 v200, s[40:43], s65 offen sc1 lds\n"
    "v_mfma_f32_16x16x32_bf16 a[12:15], v[164:167], v[156:159], a[12:15]\n"
    "s_waitcnt lgkmcnt(2)\n"
    "v_exp_f32 v39, v39\n"
    "v_mfma_f32_16x16x32_bf16 a[28:31], v[168:171], v[140:143], a[28:31]\n"
    "ds_read_b128 v[160:163], v201 offset:30720\n"
    "v_add_f32 v114, v114, v67\n"
    "v_mfma_f32_16x16x32_bf16 a[28:31], v[172:175], v[156:159], a[28:31]\n"
    "v_exp_f32 v40, v40\n"
    "v_mfma_f32_16x16x32_bf16 a[24:27], v[172:175], v[152:155], a[24:27]\n"
    "buffer_load_dwordx4 v200, s[40:43], s65 offen offset:1024 sc1 lds\n"
    "v_mfma_f32_16x16x32_bf16 a[24:27], v[168:171], v[136:139], a[24:27]\n"
    "v_exp_f32 v41, v41\n"
    "v_mfma_f32_16x16x32_bf16 a[20:23], v[168:171], v[132:135], a[20:23]\n"
    "ds_read_b128 v[164:167], v201 offset:31744\n"
    "v_add_f32 v117, v117, v68\n"
    "v_mfma_f32_16x16x32_bf16 a[20:23], v[172:175], v[148:151], a[20:23]\n"
    "v_exp_f32 v42, v42\n"
    "v_mfma_f32_16x16x32_bf16 a[16:19], v[172:175], v[144:147], a[16:19]\n"
    "v_exp_f32 v43, v43\n"
    "v_mfma_f32_16x16x32_bf16 a[16:19], v[168:171], v[128:131], a[16:19]\n"
    "s_waitcnt lgkmcnt(2)\n"
    "v_exp_f32 v44, v44\n"
    "v_mfma_f32_16x16x32_bf16 a[32:35], v[176:179], v[128:131], a[32:35]\n"
    "ds_read_b128 v[168:171], v201 offset:32768\n"
    "v_add_f32 v118, v118, v69\n"
    "v_mfma_f32_16x16x32_bf16 a[32:35], v[180:183], v[144:147], a[32:35]\n"
    "buffer_load_dwordx2 v[218:219], v213, s[44:47], 0 offen sc1\n"
    "v_mfma_f32_16x16x32_bf16 a[36:39], v[180:183], v[148:151], a[36:39]\n"
    "v_exp_f32 v45, v45\n"
    "v_mfma_f32_16x16x32_bf16 a[36:39], v[176:179], v[132:135], a[36:39]\n"
    "v_exp_f32 v46, v46\n"
    "v_mfma_f32_16x16x32_bf16 a[40:43], v[176:179], v[136:139], a[40:43]\n"
    "ds_read_b128 v[172:175], v201 offset:33792\n"
    "v_add_f32 v117, v117, v70\n"
    "v_mfma_f32_16x16x32_bf16 a[40:43], v[180:183], v[152:155], a[40:43]\n"
    "v_exp_f32 v47, v47\n"
    "v_mfma_f32_16x16x32_bf16 a[44:47], v[180:183], v[156:159], a[44:47]\n"
    "v_exp_f32 v48, v48\n"
    "v_mfma_f32_16x16x32_bf16 a[44:47], v[176:179], v[140:143], a[44:47]\n"
    "s_waitcnt lgkmcnt(2)\n"
    "v_exp_f32 v49, v49\n"
    "v_mfma_f32_16x16x32_bf16 a[60:63], v[160:163], v[140:143], a[60:63]\n"
    "ds_read_b128 v[176:179], v201 offset:34816\n"
    "v_add_f32 v113, v113, v32\n"
    "v_mfma_f32_16x16x32_bf16 a[60:63], v[164:167], v[156:159], a[60:63]\n"
    "v_exp_f32 v50, v50\n"
    "v_mfma_f32_16x16x32_bf16 a[56:59], v[164:167], v[152:155], a[56:59]\n"
    "v_exp_f32 v51, v51\n"
    "v_mfma_f32_16x16x32_bf16 a[56:59], v[160:163], v[136:139], a[56:59]\n"
    "v_exp_f32 v52, v52\n"
    "v_mfma_f32_16x16x32_bf16 a[52:55], v[160:163], v[132:135], a[52:55]\n"
    "ds_read_b128 v[180:183], v201 offset:35840\n"
    "v_add_f32 v118, v118, v71\n"
    "v_mfma_f32_16x16x32_bf16 a[52:55], v[164:167], v[148:151], a[52:55]\n"
    "v_add_f32 v114, v114, v33\n"
    "v_add_f32 v121, v121, v72\n"
    "v_mfma_f32_16x16x32_bf16 a[48:51], v[164:167], v[144:147], a[48:51]\n"
    "v_add_f32 v113, v113, v34\n"
    "v_add_f32 v122, v122, v73\n"
    "v_mfma_f32_16x16x32_bf16 a[48:51], v[160:163], v[128:131], a[48:51]\n"
    "s_waitcnt lgkmcnt(2)\n"
    "v_exp_f32 v53, v53\n"
    "v_mfma_f32_16x16x32_bf16 a[64:67], v[168:171], v[128:131], a[64:67]\n"
    "ds_read_b128 v[160:163], v201 offset:36864\n"
    "v_add_f32 v114, v114, v35\n"
    "v_mfma_f32_16x16x32_bf16 a[64:67], v[172:175], v[144:147], a[64:67]\n"
    "v_add_f32 v121, v121, v74\n"
    "v_add_f32 v117, v117, v36\n"
    "v_mfma_f32_16x16x32_bf16 a[68:71], v[172:175], v[148:151], a[68:71]\n"
    "v_exp_f32 v54, v54\n"
    "v_mfma_f32_16x16x32_bf16 a[68:71], v[168:171], v[132:135], a[68:71]\n"
    "s_add_u32 m0, s51, s95\n"
    "v_add_f32 v122, v122, v75\n"
    "v_add_f32 v118, v118, v37\n"
    "v_mfma_f32_16x16x32_bf16 a[72:75], v[168:171], v[136:139], a[72:75]\n"
    "ds_read_b128 v[164:167], v201 offset:37888\n"
    "v_add_f32 v125, v125, v76\n"
    "v_mfma_f32_16x16x32_bf16 a[72:75], v[172:175], v[152:155], a[72:75]\n"
    "buffer_load_dwordx4 v202, s[32:35], s64 offen lds\n"
    "v_mfma_f32_16x16x32_bf16 a[76:79], v[172:175], v[156:159], a[76:79]\n"
    "v_add_f32 v117, v117, v38\n"
    "v_add_f32 v126, v126, v77\n"
    "v_mfma_f32_16x16x32_bf16 a[76:79], v[168:171], v[140:143], a[76:79]\n"
    "s_waitcnt lgkmcnt(2)\n"
    "v_exp_f32 v55, v55\n"
    "v_mfma_f32_16x16x32_bf16 a[92:95], v[176:179], v[140:143], a[92:95]\n"
    "ds_read_b128 v[168:171], v201 offset:38912\n"
    "v_add_f32 v118, v118, v39\n"
    "v_mfma_f32_16x16x32_bf16 a[92:95], v[180:183], v[156:159], a[92:95]\n"
    "v_add_f32 v125, v125, v78\n"
    "v_add_f32 v121, v121, v40\n"
    "v_mfma_f32_16x16x32_bf16 a[88:91], v[180:183], v[152:155], a[88:91]\n"
    "buffer_load_dwordx4 v203, s[32:35], s64 offen offset:1024 lds\n"
    "v_mfma_f32_16x16x32_bf16 a[88:91], v[176:179], v[136:139], a[88:91]\n"
    "v_exp_f32 v56, v56\n"
    "v_mfma_f32_16x16x32_bf16 a[84:87], v[176:179], v[132:135], a[84:87]\n"
    "ds_read_b128 v[172:175], v201 offset:39936\n"
    "v_add_f32 v126, v126, v79\n"
    "v_mfma_f32_16x16x32_bf16 a[84:87], v[180:183], v[148:151], a[84:87]\n"
    "v_add_f32 v122, v122, v41\n"
    "v_add_f32 v113, v113, v80\n"
    "v_mfma_f32_16x16x32_bf16 a[80:83], v[180:183], v[144:147], a[80:83]\n"
    "v_add_f32 v121, v121, v42\n"
    "v_add_f32 v114, v114, v81\n"
    "v_mfma_f32_16x16x32_bf16 a[80:83], v[176:179], v[128:131], a[80:83]\n"
    "s_waitcnt lgkmcnt(2)\n"
    "buffer_load_dwordx4 v204, s[32:35], s64 offen offset:2048 lds\n"
    "v_mfma_f32_16x16x32_bf16 a[96:99], v[160:163], v[128:131], a[96:99]\n"
    "ds_read_b128 v[176:179], v201 offset:40960\n"
    "v_add_f32 v122, v122, v43\n"
    "v_mfma_f32_16x16x32_bf16 a[96:99], v[164:167], v[144:147], a[96:99]\n"
    "v_exp_f32 v57, v57\n"
    "v_mfma_f32_16x16x32_bf16 a[100:103], v[164:167], v[148:151], a[100:103]\n"
    "v_add_f32 v113, v113, v82\n"
    "v_add_f32 v125, v125, v44\n"
    "v_mfma_f32_16x16x32_bf16 a[100:103], v[160:163], v[132:135], a[100:103]\n"
    "v_exp_f32 v58, v58\n"
    "v_mfma_f32_16x16x32_bf16 a[104:107], v[160:163], v[136:139], a[104:107]\n"
    "s_mov_b32 s95, s94\n"
    "ds_read_b128 v[180:183], v201 offset:41984\n"
    "v_add_f32 v114, v114, v83\n"
    "v_mfma_f32_16x16x32_bf16 a[104:107], v[164:167], v[152:155], a[104:107]\n"
    "s_sub_u32 s94, s94, 0x3000\n"
    "v_add_f32 v126, v126, v45\n"
    "v_add_f32 v117, v117, v84\n"
    "v_mfma_f32_16x16x32_bf16 a[108:111], v[164:167], v[156:159], a[108:111]\n"
    "s_min_u32 s94, s94, 0x6000\n"
    "v_add_f32 v125, v125, v46\n"
    "v_add_f32 v118, v118, v85\n"
    "v_mfma_f32_16x16x32_bf16 a[108:111], v[160:163], v[140:143], a[108:111]\n"
    "s_waitcnt lgkmcnt(2)\n"
    "v_exp_f32 v59, v59\n"
    "v_mfma_f32_16x16x32_bf16 a[124:127], v[168:171], v[140:143], a[124:127]\n"
    "s_sub_u32 s93, s94, s95\n"
    "ds_read_b128 v[160:163], v201 offset:43008\n"
    "v_add_f32 v126, v126, v47\n"
    "v_mfma_f32_16x16x32_bf16 a[124:127], v[172:175], v[156:159], a[124:127]\n"
    "v_add_f32 v117, v117, v86\n"
    "v_add_f32 v113, v113, v48\n"
    "v_mfma_f32_16x16x32_bf16 a[120:123], v[172:175], v[152:155], a[120:123]\n"
    "v_exp_f32 v60, v60\n"
    "v_mfma_f32_16x16x32_bf16 a[120:123], v[168:171], v[136:139], a[120:123]\n"
    "v_add_u32 v205, s93, v205\n"
    "v_add_f32 v118, v118, v87\n"
    "v_mfma_f32_16x16x32_bf16 a[116:119], v[168:171], v[132:135], a[116:119]\n"
    "ds_read_b128 v[164:167], v201 offset:44032\n"
    "v_add_f32 v114, v114, v49\n"
    "v_mfma_f32_16x16x32_bf16 a[116:119], v[172:175], v[148:151], a[116:119]\n"
    "v_add_u32 v206, s93, v206\n"
    "v_add_f32 v121, v121, v88\n"
    "v_mfma_f32_16x16x32_bf16 a[112:115], v[172:175], v[144:147], a[112:115]\n"
    "v_add_f32 v113, v113, v50\n"
    "v_add_u32 v220, 1, v220\n"
    "v_mfma_f32_16x16x32_bf16 a[112:115], v[168:171], v[128:131], a[112:115]\n"
    "s_waitcnt lgkmcnt(2)\n"
    "v_exp_f32 v61, v61\n"
    "v_mfma_f32_16x16x32_bf16 a[128:131], v[176:179], v[128:131], a[128:131]\n"
    "s_add_u32 s64, s64, 0xc000\n"
    "ds_read_b128 v[168:171], v201 offset:45056\n"
    "v_add_u32 v207, s93, v207\n"
    "v_mfma_f32_16x16x32_bf16 a[128:131], v[180:183], v[144:147], a[128:131]\n"
    "s_add_u32 s62, s62, 0x6000\n"
    "v_add_f32 v122, v122, v89\n"
    "v_add_f32 v114, v114, v51\n"
    "v_mfma_f32_16x16x32_bf16 a[132:135], v[180:183], v[148:151], a[132:135]\n"
    "s_add_u32 s63, s63, 0x6000\n"
    "v_add_f32 v121, v121, v90\n"
    "v_add_f32 v117, v117, v52\n"
    "v_mfma_f32_16x16x32_bf16 a[132:135], v[176:179], v[132:135], a[132:135]\n"
    "s_add_u32 s60, s60, 0x6000\n"
    "v_exp_f32 v62, v62\n"
    "v_mfma_f32_16x16x32_bf16 a[136:139], v[176:179], v[136:139], a[136:139]\n"
    "s_add_u32 s61, s61, 0x6000\n"
    "ds_read_b128 v[172:175], v201 offset:46080\n"
    "v_add_f32 v122, v122, v91\n"
    "v_mfma_f32_16x16x32_bf16 a[136:139], v[180:183], v[152:155], a[136:139]\n"
    "s_add_u32 s66, s66, s74\n"
    "v_add_f32 v118, v118, v53\n"
    "v_add_f32 v125, v125, v92\n"
    "v_mfma_f32_16x16x32_bf16 a[140:143], v[180:183], v[156:159], a[140:143]\n"
    "s_min_u32 s66, s66, 33\n"
    "v_add_f32 v117, v117, v54\n"
    "v_add_f32 v126, v126, v93\n"
    "v_mfma_f32_16x16x32_bf16 a[140:143], v[176:179], v[140:143], a[140:143]\n"
    "s_waitcnt lgkmcnt(2)\n"
    "v_exp_f32 v63, v63\n"
    "v_mfma_f32_16x16x32_bf16 a[156:159], v[160:163], v[140:143], a[156:159]\n"
    "ds_read_b128 v[176:179], v201 offset:47104\n"
    "v_add_f32 v118, v118, v55\n"
    "v_mfma_f32_16x16x32_bf16 a[156:159], v[164:167], v[156:159], a[156:159]\n"
    "s_waitcnt vmcnt(3)\n"
    "ds_read_b128 v[180:183], v201 offset:48128\n"
    "v_add_f32 v125, v125, v94\n"
    "v_mfma_f32_16x16x32_bf16 a[152:155], v[164:167], v[152:155], a[152:155]\n"
    "v_cmp_ne_u32 s[80:81], s67, v219\n"
    "v_add_f32 v121, v121, v56\n"
    "v_mfma_f32_16x16x32_bf16 a[152:155], v[160:163], v[136:139], a[152:155]\n"
    "v_cmp_gt_u32 vcc, s66, v218\n"
    "v_add_f32 v126, v126, v95\n"
    "v_mfma_f32_16x16x32_bf16 a[148:151], v[160:163], v[132:135], a[148:151]\n"
    "v_add_f32 v122, v122, v57\n"
    "v_mfma_f32_16x16x32_bf16 a[148:151], v[164:167], v[148:151], a[148:151]\n"
    "s_or_b64 vcc, vcc, s[80:81]\n"
    "s_cbranch_vccnz LSPRE3\n"
    "LSPB3:\n"
    "v_mfma_f32_16x16x32_bf16 a[144:147], v[164:167], v[144:147], a[144:147]\n"
    "v_mfma_f32_16x16x32_bf16 a[144:147], v[160:163], v[128:131], a[144:147]\n"
    "s_waitcnt lgkmcnt(0)\n"
    "v_mfma_f32_16x16x32_bf16 a[160:163], v[168:171], v[128:131], a[160:163]\n"
    "s_barrier\n"
    "LMID:\n"
    "v_mfma_f32_16x16x32_bf16 a[160:163], v[172:175], v[144:147], a[160:163]\n"
    "buffer_store_dwordx2 v[220:221], v214, s[44:47], 0 offen sc1\n"
    "v_mfma_f32_16x16x32_bf16 a[164:167], v[172:175], v[148:151], a[164:167]\n"
    "ds_read_b128 v[160:163], v200 offset:24576\n"
    "ds_read_u16_d16_hi v223, v209 offset:6336\n"
    "v_mfma_f32_16x16x32_bf16 a[164:167], v[168:171], v[132:135], a[164:167]\n"
    "ds_read_u16_d16_hi v222, v209 offset:192\n"
    "v_add_f32 v121, v121, v58\n"
    "v_mfma_f32_16x16x32_bf16 a[168:171], v[168:171], v[136:139], a[168:171]\n"
    "ds_read_b128 v[164:167], v200 offset:25600\n"
    "ds_read_b128 v[232:235], v205 offset:24576\n"
    "v_mfma_f32_16x16x32_bf16 a[168:171], v[172:175], v[152:155], a[168:171]\n"
    "v_add_f32 v122, v122, v59\n"
    "ds_read_b128 v[236:239], v206 offset:24576\n"
    "v_mfma_f32_16x16x32_bf16 a[172:175], v[172:175], v[156:159], a[172:175]\n"
    "v_add_f32 v125, v125, v60\n"
    "v_add_f32 v126, v126, v61\n"
    "v_mfma_f32_16x16x32_bf16 a[172:175], v[168:171], v[140:143], a[172:175]\n"
    "v_add_f32 v125, v125, v62\n"
    "v_add_f32 v126, v126, v63\n"
    "v_mfma_f32_16x16x32_bf16 a[188:191], v[176:179], v[140:143], a[188:191]\n"
    "ds_read_b128 v[168:171], v200 offset:26624\n"
    "ds_read_u16_d16_hi v225, v210 offset:528\n"
    "v_mfma_f32_16x16x32_bf16 a[188:191], v[180:183], v[156:159], a[188:191]\n"
    "ds_read_u16_d16_hi v224, v210 offset:16\n"
    "ds_read2st64_b32 v[240:241], v207 offset0:96 offset1:99\n"
    "v_mfma_f32_16x16x32_bf16 a[184:187], v[176:179], v[136:139], a[184:187]\n"
    "ds_read2st64_b32 v[242:243], v207 offset0:102 offset1:105\n"
    "ds_read2st64_b32 v[244:245], v207 offset0:108 offset1:111\n"
    "v_mfma_f32_16x16x32_bf16 a[184:187], v[180:183], v[152:155], a[184:187]\n"
    "ds_read2st64_b32 v[246:247], v207 offset0:114 offset1:117\n"
    "v_cvt_pk_bf16_f32 v140, v44, v45\n"
    "v_mfma_f32_16x16x32_bf16 a[180:183], v[176:179], v[132:135], a[180:183]\n"
    "s_add_u32 s59, s59, 1\n"
    "ds_read_b128 v[172:175], v200 offset:27648\n"
    "s_waitcnt lgkmcnt(8)\n"
    "v_mfma_f32_16x16x32_bf16 a[180:183], v[180:183], v[148:151], a[180:183]\n"
    "v_mul_f32 v226, s75, v223\n"
    "v_fma_f32 v227, -v222, v226, v217\n"
    "v_mfma_f32_16x16x32_bf16 a[176:179], v[176:179], v[128:131], a[176:179]\n"
    "s_cmp_lt_u32 s59, 15\n"
    "v_cvt_f32_i32 v232, v232\n"
    "v_cvt_f32_i32 v233, v233\n"
    "v_mfma_f32_16x16x32_bf16 a[176:179], v[180:183], v[144:147], a[176:179]\n"
    "s_cbranch_scc1 LLOOP\n"
    "LTAIL:\n"
    "s_waitcnt lgkmcnt(0)\n"
    "s_nop 1\n"
    "v_mfma_f32_16x16x32_bf16 v[64:67], v[160:163], a[192:195], v[96:99]\n"
    "ds_read_b128 v[176:179], v200 offset:28672\n"
    "v_cvt_pk_bf16_f32 v128, v32, v33\n"
    "v_mfma_f32_16x16x32_bf16 v[64:67], v[164:167], a[196:199], v[64:67]\n"
    "s_add_u32 m0, s71, 0\n"
    "v_cvt_pk_bf16_f32 v129, v34, v35\n"
    "v_cvt_pk_bf16_f32 v132, v36, v37\n"
    "v_mfma_f32_16x16x32_bf16 v[68:71], v[164:167], a[220:223], v[100:103]\n"
    "buffer_load_dwordx4 v200, s[36:39], s60 offen sc1 lds\n"
    "v_mfma_f32_16x16x32_bf16 v[68:71], v[160:163], a[216:219], v[68:71]\n"
    "v_cvt_pk_bf16_f32 v133, v38, v39\n"
    "v_cvt_pk_bf16_f32 v136, v40, v41\n"
    "v_mfma_f32_16x16x32_bf16 v[72:75], v[160:163], a[240:243], v[104:107]\n"
    "ds_read_b128 v[180:183], v200 offset:29696\n"
    "v_cvt_pk_bf16_f32 v137, v42, v43\n"
    "v_mfma_f32_16x16x32_bf16 v[72:75], v[164:167], a[244:247], v[72:75]\n"
    "v_cvt_pk_bf16_f32 v141, v46, v47\n"
    "v_cvt_pk_bf16_f32 v130, v48, v49\n"
    "v_mfma_f32_16x16x32_bf16 v[76:79], v[164:167], v[12:15], v[108:111]\n"
    "buffer_load_dwordx4 v200, s[36:39], s60 offen offset:1024 sc1 lds\n"
    "v_mfma_f32_16x16x32_bf16 v[76:79], v[160:163], v[8:11], v[76:79]\n"
    "v_cvt_pk_bf16_f32 v131, v50, v51\n"
    "v_cvt_pk_bf16_f32 v134, v52, v53\n"
    "v_mfma_f32_16x16x32_bf16 v[76:79], v[168:171], v[16:19], v[76:79]\n"
    "ds_read_b128 v[160:163], v200 offset:34816\n"
    "v_cvt_pk_bf16_f32 v135, v54, v55\n"
    "v_mfma_f32_16x16x32_bf16 v[76:79], v[172:175], v[20:23], v[76:79]\n"
    "v_cvt_pk_bf16_f32 v138, v56, v57\n"
    "v_cvt_pk_bf16_f32 v139, v58, v59\n"
    "v_mfma_f32_16x16x32_bf16 v[72:75], v[172:175], a[252:255], v[72:75]\n"
    "buffer_load_dwordx4 v200, s[36:39], s60 offen offset:2048 sc1 lds\n"
    "v_mfma_f32_16x16x32_bf16 v[72:75], v[168:171], a[248:251], v[72:75]\n"
    "v_cvt_pk_bf16_f32 v142, v60, v61\n"
    "v_cvt_pk_bf16_f32 v143, v62, v63\n"
    "v_mfma_f32_16x16x32_bf16 v[68:71], v[168:171], a[224:227], v[68:71]\n"
    "ds_read_b128 v[164:167], v200 offset:35840\n"
    "v_add_f32 v113, v113, v114\n"
    "v_mfma_f32_16x16x32_bf16 v[68:71], v[172:175], a[228:231], v[68:71]\n"
    "v_add_f32 v117, v117, v118\n"
    "v_add_f32 v121, v121, v122\n"
    "v_mfma_f32_16x16x32_bf16 v[64:67], v[172:175], a[204:207], v[64:67]\n"
    "buffer_load_dwordx4 v200, s[36:39], s60 offen offset:3072 sc1 lds\n"
    "v_mfma_f32_16x16x32_bf16 v[64:67], v[168:171], a[200:203], v[64:67]\n"
    "s_waitcnt lgkmcnt(2)\n"
    "v_add_f32 v125, v125, v126\n"
    "v_mov_b32 v115, v113\n"
    "v_mfma_f32_16x16x32_bf16 v[64:67], v[176:179], a[208:211], v[64:67]\n"
    "s_add_u32 s65, s60, 0x1000\n"
    "ds_read_b128 v[168:171], v200 offset:32768\n"
    "v_mov_b32 v119, v117\n"
    "v_mfma_f32_16x16x32_bf16 v[64:67], v[180:183], a[212:215], v[64:67]\n"
    "s_add_u32 m0, s71, 0x1000\n"
    "v_mov_b32 v123, v121\n"
    "v_mov_b32 v127, v125\n"
    "v_mfma_f32_16x16x32_bf16 v[68:71], v[180:183], a[236:239], v[68:71]\n"
    "buffer_load_dwordx4 v200, s[36:39], s65 offen sc1 lds\n"
    "v_mfma_f32_16x16x32_bf16 v[68:71], v[176:179], a[232:235], v[68:71]\n"
    "v_permlane32_swap_b32 v113, v115\n"
    "v_permlane32_swap_b32 v117, v119\n"
    "v_mfma_f32_16x16x32_bf16 v[72:75], v[176:179], v[0:3], v[72:75]\n"
    "ds_read_b128 v[172:175], v200 offset:33792\n"
    "v_permlane32_swap_b32 v121, v123\n"
    "v_mfma_f32_16x16x32_bf16 v[72:75], v[180:183], v[4:7], v[72:75]\n"
    "v_permlane32_swap_b32 v125, v127\n"
    "v_add_f32 v113, v113, v115\n"
    "v_mfma_f32_16x16x32_bf16 v[76:79], v[180:183], v[28:31], v[76:79]\n"
    "buffer_load_dwordx4 v200, s[36:39], s65 offen offset:1024 sc1 lds\n"
    "v_mfma_f32_16x16x32_bf16 v[76:79], v[176:179], v[24:27], v[76:79]\n"
    "s_waitcnt lgkmcnt(2)\n"
    "v_add_f32 v117, v117, v119\n"
    "v_add_f32 v121, v121, v123\n"
    "v_mfma_f32_16x16x32_bf16 v[92:95], v[160:163], v[24:27], v[108:111]\n"
    "ds_read_b128 v[176:179], v200 offset:30720\n"
    "v_add_f32 v125, v125, v127\n"
    "v_mfma_f32_16x16x32_bf16 v[92:95], v[164:167], v[28:31], v[92:95]\n"
    "s_add_u32 m0, s71, 0x12000\n"
    "v_exp_f32 v64, v64\n"
    "v_mfma_f32_16x16x32_bf16 v[88:91], v[164:167], v[4:7], v[104:107]\n"
    "buffer_load_dwordx4 v200, s[40:43], s61 offen sc1 lds\n"
    "v_mfma_f32_16x16x32_bf16 v[88:91], v[160:163], v[0:3], v[88:91]\n"
    "v_exp_f32 v65, v65\n"
    "v_mfma_f32_16x16x32_bf16 v[84:87], v[160:163], a[232:235], v[100:103]\n"
    "ds_read_b128 v[180:183], v200 offset:31744\n"
    "v_cvt_pk_bf16_f32 v144, v64, v65\n"
    "v_mfma_f32_16x16x32_bf16 v[84:87], v[164:167], a[236:239], v[84:87]\n"
    "v_exp_f32 v66, v66\n"
    "v_mfma_f32_16x16x32_bf16 v[80:83], v[164:167], a[212:215], v[96:99]\n"
    "buffer_load_dwordx4 v200, s[40:43], s61 offen offset:1024 sc1 lds\n"
    "v_mfma_f32_16x16x32_bf16 v[80:83], v[160:163], a[208:211], v[80:83]\n"
    "s_waitcnt lgkmcnt(2)\n"
    "v_exp_f32 v67, v67\n"
    "v_mfma_f32_16x16x32_bf16 v[80:83], v[168:171], a[200:203], v[80:83]\n"
    "ds_read_b128 v[160:163], v200 offset:36864\n"
    "v_cvt_pk_bf16_f32 v145, v66, v67\n"
    "v_mfma_f32_16x16x32_bf16 v[80:83], v[172:175], a[204:207], v[80:83]\n"
    "v_exp_f32 v68, v68\n"
    "v_mfma_f32_16x16x32_bf16 v[84:87], v[172:175], a[228:231], v[84:87]\n"
    "buffer_load_dwordx4 v200, s[40:43], s61 offen offset:2048 sc1 lds\n"
    "v_mfma_f32_16x16x32_bf16 v[84:87], v[168:171], a[224:227], v[84:87]\n"
    "v_exp_f32 v69, v69\n"
    "v_mfma_f32_16x16x32_bf16 v[88:91], v[168:171], a[248:251], v[88:91]\n"
    "ds_read_b128 v[164:167], v200 offset:37888\n"
    "v_cvt_pk_bf16_f32 v148, v68, v69\n"
    "v_mfma_f32_16x16x32_bf16 v[88:91], v[172:175], a[252:255], v[88:91]\n"
    "v_exp_f32 v70, v70\n"
    "v_mfma_f32_16x16x32_bf16 v[92:95], v[172:175], v[20:23], v[92:95]\n"
    "buffer_load_dwordx4 v200, s[40:43], s61 offen offset:3072 sc1 lds\n"
    "v_mfma_f32_16x16x32_bf16 v[92:95], v[168:171], v[16:19], v[92:95]\n"
    "s_waitcnt lgkmcnt(2)\n"
    "v_exp_f32 v71, v71\n"
    "v_mfma_f32_16x16x32_bf16 v[92:95], v[176:179], v[8:11], v[92:95]\n"
    "s_add_u32 s65, s61, 0x1000\n"
    "ds_read_b128 v[168:171], v200 offset:38912\n"
    "v_cvt_pk_bf16_f32 v149, v70, v71\n"
    "v_mfma_f32_16x16x32_bf16 v[92:95], v[180:183], v[12:15], v[92:95]\n"
    "s_add_u32 m0, s71, 0x13000\n"
    "v_exp_f32 v72, v72\n"
    "v_mfma_f32_16x16x32_bf16 v[88:91], v[180:183], a[244:247], v[88:91]\n"
    "buffer_load_dwordx4 v200, s[40:43], s65 offen sc1 lds\n"
    "v_mfma_f32_16x16x32_bf16 v[88:91], v[176:179], a[240:243], v[88:91]\n"
    "v_exp_f32 v73, v73\n"
    "v_mfma_f32_16x16x32_bf16 v[84:87], v[176:179], a[216:219], v[84:87]\n"
    "ds_read_b128 v[172:175], v200 offset:39936\n"
    "v_cvt_pk_bf16_f32 v152, v72, v73\n"
    "v_mfma_f32_16x16x32_bf16 v[84:87], v[180:183], a[220:223], v[84:87]\n"
    "v_exp_f32 v74, v74\n"
    "v_mfma_f32_16x16x32_bf16 v[80:83], v[180:183], a[196:199], v[80:83]\n"
    "buffer_load_dwordx4 v200, s[40:43], s65 offen offset:1024 sc1 lds\n"
    "v_mfma_f32_16x16x32_bf16 v[80:83], v[176:179], a[192:195], v[80:83]\n"
    "s_waitcnt lgkmcnt(2)\n"
    "v_exp_f32 v75, v75\n"
    "v_mfma_f32_16x16x32_bf16 v[32:35], v[160:163], a[192:195], v[96:99]\n"
    "ds_read_b128 v[176:179], v200 offset:40960\n"
    "v_cvt_pk_bf16_f32 v153, v74, v75\n"
    "v_mfma_f32_16x16x32_bf16 v[32:35], v[164:167], a[196:199], v[32:35]\n"
    "v_exp_f32 v76, v76\n"
    "v_mfma_f32_16x16x32_bf16 v[36:39], v[164:167], a[220:223], v[100:103]\n"
    "v_exp_f32 v77, v77\n"
    "v_mfma_f32_16x16x32_bf16 v[36:39], v[160:163], a[216:219], v[36:39]\n"
    "v_exp_f32 v78, v78\n"
    "v_mfma_f32_16x16x32_bf16 v[40:43], v[160:163], a[240:243], v[104:107]\n"
    "ds_read_b128 v[180:183], v200 offset:41984\n"
    "v_cvt_pk_bf16_f32 v156, v76, v77\n"
    "v_mfma_f32_16x16x32_bf16 v[40:43], v[164:167], a[244:247], v[40:43]\n"
    "v_exp_f32 v79, v79\n"
    "v_mfma_f32_16x16x32_bf16 v[44:47], v[164:167], v[12:15], v[108:111]\n"
    "v_exp_f32 v80, v80\n"
    "v_mfma_f32_16x16x32_bf16 v[44:47], v[160:163], v[8:11], v[44:47]\n"
    "s_waitcnt lgkmcnt(2)\n"
    "v_exp_f32 v81, v81\n"
    "v_mfma_f32_16x16x32_bf16 v[44:47], v[168:171], v[16:19], v[44:47]\n"
    "ds_read_b128 v[160:163], v200 offset:47104\n"
    "v_cvt_pk_bf16_f32 v157, v78, v79\n"
    "v_mfma_f32_16x16x32_bf16 v[44:47], v[172:175], v[20:23], v[44:47]\n"
    "v_exp_f32 v82, v82\n"
    "v_mfma_f32_16x16x32_bf16 v[40:43], v[172:175], a[252:255], v[40:43]\n"
    "v_exp_f32 v83, v83\n"
    "v_mfma_f32_16x16x32_bf16 v[40:43], v[168:171], a[248:251], v[40:43]\n"
    "v_exp_f32 v84, v84\n"
    "v_mfma_f32_16x16x32_bf16 v[36:39], v[168:171], a[224:227], v[36:39]\n"
    "ds_read_b128 v[164:167], v200 offset:48128\n"
    "v_cvt_pk_bf16_f32 v146, v80, v81\n"
    "v_mfma_f32_16x16x32_bf16 v[36:39], v[172:175], a[228:231], v[36:39]\n"
    "v_exp_f32 v85, v85\n"
    "v_mfma_f32_16x16x32_bf16 v[32:35], v[172:175], a[204:207], v[32:35]\n"
    "v_exp_f32 v86, v86\n"
    "v_mfma_f32_16x16x32_bf16 v[32:35], v[168:171], a[200:203], v[32:35]\n"
    "s_waitcnt lgkmcnt(2)\n"
    "v_exp_f32 v87, v87\n"
    "v_mfma_f32_16x16x32_bf16 v[32:35], v[176:179], a[208:211], v[32:35]\n"
    "ds_read_b128 v[168:171], v200 offset:45056\n"
    "v_cvt_pk_bf16_f32 v147, v82, v83\n"
    "v_mfma_f32_16x16x32_bf16 v[32:35], v[180:183], a[212:215], v[32:35]\n"
    "v_exp_f32 v88, v88\n"
    "v_mfma_f32_16x16x32_bf16 v[36:39], v[180:183], a[236:239], v[36:39]\n"
    "v_exp_f32 v89, v89\n"
    "v_mfma_f32_16x16x32_bf16 v[36:39], v[176:179], a[232:235], v[36:39]\n"
    "v_exp_f32 v90, v90\n"
    "v_mfma_f32_16x16x32_bf16 v[40:43], v[176:179], v[0:3], v[40:43]\n"
    "ds_read_b128 v[172:175], v200 offset:46080\n"
    "v_cvt_pk_bf16_f32 v150, v84, v85\n"
    "v_mfma_f32_16x16x32_bf16 v[40:43], v[180:183], v[4:7], v[40:43]\n"
    "v_exp_f32 v91, v91\n"
    "v_mfma_f32_16x16x32_bf16 v[44:47], v[180:183], v[28:31], v[44:47]\n"
    "v_exp_f32 v92, v92\n"
    "v_mfma_f32_16x16x32_bf16 v[44:47], v[176:179], v[24:27], v[44:47]\n"
    "s_waitcnt lgkmcnt(2)\n"
    "v_exp_f32 v93, v93\n"
    "v_mfma_f32_16x16x32_bf16 v[60:63], v[160:163], v[24:27], v[108:111]\n"
    "ds_read_b128 v[176:179], v200 offset:43008\n"
    "v_cvt_pk_bf16_f32 v151, v86, v87\n"
    "v_mfma_f32_16x16x32_bf16 v[60:63], v[164:167], v[28:31], v[60:63]\n"
    "v_exp_f32 v94, v94\n"
    "v_mfma_f32_16x16x32_bf16 v[56:59], v[164:167], v[4:7], v[104:107]\n"
    "v_exp_f32 v95, v95\n"
    "v_mfma_f32_16x16x32_bf16 v[56:59], v[160:163], v[0:3], v[56:59]\n"
    "v_exp_f32 v32, v32\n"
    "v_mfma_f32_16x16x32_bf16 v[52:55], v[160:163], a[232:235], v[100:103]\n"
    "ds_read_b128 v[180:183], v200 offset:44032\n"
    "v_cvt_pk_bf16_f32 v154, v88, v89\n"
    "v_mfma_f32_16x16x32_bf16 v[52:55], v[164:167], a[236:239], v[52:55]\n"
    "v_exp_f32 v33, v33\n"
    "v_mfma_f32_16x16x32_bf16 v[48:51], v[164:167], a[212:215], v[96:99]\n"
    "s_waitcnt vmcnt(6)\n"
    "v_exp_f32 v34, v34\n"
    "v_mfma_f32_16x16x32_bf16 v[48:51], v[160:163], a[208:211], v[48:51]\n"
    "s_waitcnt lgkmcnt(2)\n"
    "v_exp_f32 v35, v35\n"
    "v_mfma_f32_16x16x32_bf16 v[48:51], v[168:171], a[200:203], v[48:51]\n"
    "s_barrier\n"
    "v_exp_f32 v36, v36\n"
    "v_mfma_f32_16x16x32_bf16 v[48:51], v[172:175], a[204:207], v[48:51]\n"
    "ds_read_b128 v[160:163], v200\n"
    "v_cvt_pk_bf16_f32 v155, v90, v91\n"
    "v_mfma_f32_16x16x32_bf16 v[52:55], v[172:175], a[228:231], v[52:55]\n"
    "v_exp_f32 v37, v37\n"
    "v_mfma_f32_16x16x32_bf16 v[52:55], v[168:171], a[224:227], v[52:55]\n"
    "v_exp_f32 v38, v38\n"
    "v_mfma_f32_16x16x32_bf16 v[56:59], v[168:171], a[248:251], v[56:59]\n"
    "ds_read_b128 v[164:167], v200 offset:1024\n"
    "v_cvt_pk_bf16_f32 v158, v92, v93\n"
    "v_mfma_f32_16x16x32_bf16 v[56:59], v[172:175], a[252:255], v[56:59]\n"
    "buffer_load_dwordx2 v[218:219], v213, s[44:47], 0 offen sc1\n"
    "v_mfma_f32_16x16x32_bf16 v[60:63], v[172:175], v[20:23], v[60:63]\n"
    "v_exp_f32 v39, v39\n"
    "v_mfma_f32_16x16x32_bf16 v[60:63], v[168:171], v[16:19], v[60:63]\n"
    "s_waitcnt lgkmcnt(2)\n"
    "v_exp_f32 v40, v40\n"
    "v_mfma_f32_16x16x32_bf16 v[60:63], v[176:179], v[8:11], v[60:63]\n"
    "ds_read_b128 v[168:171], v200 offset:2048\n"
    "v_cvt_pk_bf16_f32 v159, v94, v95\n"
    "v_mfma_f32_16x16x32_bf16 v[60:63], v[180:183], v[12:15], v[60:63]\n"
    "v_exp_f32 v41, v41\n"
    "v_mfma_f32_16x16x32_bf16 v[56:59], v[180:183], a[244:247], v[56:59]\n"
    "v_exp_f32 v42, v42\n"
    "v_mfma_f32_16x16x32_bf16 v[56:59], v[176:179], a[240:243], v[56:59]\n"
    "v_exp_f32 v43, v43\n"
    "v_mfma_f32_16x16x32_bf16 v[52:55], v[176:179], a[216:219], v[52:55]\n"
    "ds_read_b128 v[172:175], v200 offset:3072\n"
    "v_cvt_pk_bf16_f32 v32, v32, v33\n"
    "v_mfma_f32_16x16x32_bf16 v[52:55], v[180:183], a[220:223], v[52:55]\n"
    "v_exp_f32 v44, v44\n"
    "v_mfma_f32_16x16x32_bf16 v[48:51], v[180:183], a[196:199], v[48:51]\n"
    "v_exp_f32 v45, v45\n"
    "v_mfma_f32_16x16x32_bf16 v[48:51], v[176:179], a[192:195], v[48:51]\n"
    "s_waitcnt lgkmcnt(2)\n"
    "v_exp_f32 v46, v46\n"
    "v_mfma_f32_16x16x32_bf16 v[64:67], v[160:163], a[192:195], v[96:99]\n"
    "ds_read_b128 v[176:179], v200 offset:4096\n"
    "v_cvt_pk_bf16_f32 v33, v34, v35\n"
    "v_mfma_f32_16x16x32_bf16 v[64:67], v[164:167], a[196:199], v[64:67]\n"
    "v_exp_f32 v47, v47\n"
    "v_mfma_f32_16x16x32_bf16 v[68:71], v[164:167], a[220:223], v[100:103]\n"
    "v_cvt_pk_bf16_f32 v36, v36, v37\n"
    "v_cvt_pk_bf16_f32 v37, v38, v39\n"
    "v_mfma_f32_16x16x32_bf16 v[68:71], v[160:163], a[216:219], v[68:71]\n"
    "v_cvt_pk_bf16_f32 v40, v40, v41\n"
    "v_cvt_pk_bf16_f32 v41, v42, v43\n"
    "v_mfma_f32_16x16x32_bf16 v[72:75], v[160:163], a[240:243], v[104:107]\n"
    "ds_read_b128 v[180:183], v200 offset:5120\n"
    "v_cvt_pk_bf16_f32 v44, v44, v45\n"
    "v_mfma_f32_16x16x32_bf16 v[72:75], v[164:167], a[244:247], v[72:75]\n"
    "v_cvt_pk_bf16_f32 v45, v46, v47\n"
    "v_mov_b32 v115, v113\n"
    "v_mfma_f32_16x16x32_bf16 v[76:79], v[164:167], v[12:15], v[108:111]\n"
    "v_exp_f32 v48, v48\n"
    "v_mfma_f32_16x16x32_bf16 v[76:79], v[160:163], v[8:11], v[76:79]\n"
    "s_waitcnt lgkmcnt(2)\n"
    "v_exp_f32 v49, v49\n"
    "v_mfma_f32_16x16x32_bf16 v[76:79], v[168:171], v[16:19], v[76:79]\n"
    "ds_read_b128 v[160:163], v200 offset:10240\n"
    "v_cvt_pk_bf16_f32 v34, v48, v49\n"
    "v_mfma_f32_16x16x32_bf16 v[76:79], v[172:175], v[20:23], v[76:79]\n"
    "v_exp_f32 v50, v50\n"
    "v_mfma_f32_16x16x32_bf16 v[72:75], v[172:175], a[252:255], v[72:75]\n"
    "v_exp_f32 v51, v51\n"
    "v_mfma_f32_16x16x32_bf16 v[72:75], v[168:171], a[248:251], v[72:75]\n"
    "v_exp_f32 v52, v52\n"
    "v_mfma_f32_16x16x32_bf16 v[68:71], v[168:171], a[224:227], v[68:71]\n"
    "ds_read_b128 v[164:167], v200 offset:11264\n"
    "v_cvt_pk_bf16_f32 v35, v50, v51\n"
    "v_mfma_f32_16x16x32_bf16 v[68:71], v[172:175], a[228:231], v[68:71]\n"
    "v_exp_f32 v53, v53\n"
    "v_mfma_f32_16x16x32_bf16 v[64:67], v[172:175], a[204:207], v[64:67]\n"
    "v_exp_f32 v54, v54\n"
    "v_mfma_f32_16x16x32_bf16 v[64:67], v[168:171], a[200:203], v[64:67]\n"
    "s_waitcnt lgkmcnt(2)\n"
    "v_exp_f32 v55, v55\n"
    "v_mfma_f32_16x16x32_bf16 v[64:67], v[176:179], a[208:211], v[64:67]\n"
    "s_add_u32 s66, s66, s74\n"
    "ds_read_b128 v[168:171], v200 offset:8192\n"
    "v_cvt_pk_bf16_f32 v38, v52, v53\n"
    "v_mfma_f32_16x16x32_bf16 v[64:67], v[180:183], a[212:215], v[64:67]\n"
    "s_add_u32 s60, s72, 0xbd000\n"
    "v_exp_f32 v56, v56\n"
    "v_mfma_f32_16x16x32_bf16 v[68:71], v[180:183], a[236:239], v[68:71]\n"
    "s_add_u32 s61, s71, 0xba000\n"
    "v_exp_f32 v57, v57\n"
    "v_mfma_f32_16x16x32_bf16 v[68:71], v[176:179], a[232:235], v[68:71]\n"
    "v_exp_f32 v58, v58\n"
    "v_mfma_f32_16x16x32_bf16 v[72:75], v[176:179], v[0:3], v[72:75]\n"
    "ds_read_b128 v[172:175], v200 offset:9216\n"
    "v_cvt_pk_bf16_f32 v39, v54, v55\n"
    "v_mfma_f32_16x16x32_bf16 v[72:75], v[180:183], v[4:7], v[72:75]\n"
    "v_exp_f32 v59, v59\n"
    "v_mfma_f32_16x16x32_bf16 v[76:79], v[180:183], v[28:31], v[76:79]\n"
    "s_waitcnt vmcnt(0)\n"
    "v_cmp_ne_u32 s[80:81], s67, v219\n"
    "v_cvt_pk_bf16_f32 v42, v56, v57\n"
    "v_mfma_f32_16x16x32_bf16 v[76:79], v[176:179], v[24:27], v[76:79]\n"
    "s_waitcnt lgkmcnt(2)\n"
    "v_cmp_gt_u32 vcc, s66, v218\n"
    "v_cvt_pk_bf16_f32 v43, v58, v59\n"
    "v_mfma_f32_16x16x32_bf16 v[92:95], v[160:163], v[24:27], v[108:111]\n"
    "ds_read_b128 v[176:179], v200 offset:6144\n"
    "v_mov_b32 v119, v117\n"
    "v_mfma_f32_16x16x32_bf16 v[92:95], v[164:167], v[28:31], v[92:95]\n"
    "s_or_b64 vcc, vcc, s[80:81]\n"
    "v_exp_f32 v64, v64\n"
    "v_mfma_f32_16x16x32_bf16 v[88:91], v[164:167], v[4:7], v[104:107]\n"
    "s_cbranch_vccnz LSPRE5\n"
    "LSPB5:\n"
    "v_exp_f32 v65, v65\n"
    "v_mfma_f32_16x16x32_bf16 v[88:91], v[160:163], v[0:3], v[88:91]\n"
    "v_exp_f32 v66, v66\n"
    "v_mfma_f32_16x16x32_bf16 v[84:87], v[160:163], a[232:235], v[100:103]\n"
    "s_barrier\n"
    "ds_read_b128 v[180:183], v200 offset:7168\n"
    "v_cvt_pk_bf16_f32 v64, v64, v65\n"
    "v_mfma_f32_16x16x32_bf16 v[84:87], v[164:167], a[236:239], v[84:87]\n"
    "s_add_u32 m0, s72, 0x6000\n"
    "v_exp_f32 v67, v67\n"
    "v_mfma_f32_16x16x32_bf16 v[80:83], v[164:167], a[212:215], v[96:99]\n"
    "buffer_load_dwordx4 v200, s[36:39], s60 offen sc1 lds\n"
    "v_mfma_f32_16x16x32_bf16 v[80:83], v[160:163], a[208:211], v[80:83]\n"
    "s_waitcnt lgkmcnt(2)\n"
    "v_exp_f32 v68, v68\n"
    "v_mfma_f32_16x16x32_bf16 v[80:83], v[168:171], a[200:203], v[80:83]\n"
    "ds_read_b128 v[160:163], v200 offset:12288\n"
    "v_cvt_pk_bf16_f32 v65, v66, v67\n"
    "v_mfma_f32_16x16x32_bf16 v[80:83], v[172:175], a[204:207], v[80:83]\n"
    "buffer_load_dwordx4 v200, s[36:39], s60 offen offset:1024 sc1 lds\n"
    "v_mfma_f32_16x16x32_bf16 v[84:87], v[172:175], a[228:231], v[84:87]\n"
    "v_exp_f32 v69, v69\n"
    "v_mfma_f32_16x16x32_bf16 v[84:87], v[168:171], a[224:227], v[84:87]\n"
    "buffer_load_dwordx4 v200, s[36:39], s60 offen offset:2048 sc1 lds\n"
    "v_mfma_f32_16x16x32_bf16 v[88:91], v[168:171], a[248:251], v[88:91]\n"
    "ds_read_b128 v[164:167], v200 offset:13312\n"
    "v_cvt_pk_bf16_f32 v68, v68, v69\n"
    "v_mfma_f32_16x16x32_bf16 v[88:91], v[172:175], a[252:255], v[88:91]\n"
    "v_exp_f32 v60, v60\n"
    "v_mfma_f32_16x16x32_bf16 v[92:95], v[172:175], v[20:23], v[92:95]\n"
    "s_add_u32 m0, s71, 0x18000\n"
    "v_exp_f32 v70, v70\n"
    "v_mfma_f32_16x16x32_bf16 v[92:95], v[168:171], v[16:19], v[92:95]\n"
    "s_waitcnt lgkmcnt(2)\n"
    "buffer_load_dwordx4 v200, s[40:43], s61 offen sc1 lds\n"
    "v_mfma_f32_16x16x32_bf16 v[92:95], v[176:179], v[8:11], v[92:95]\n"
    "ds_read_b128 v[168:171], v200 offset:14336\n"
    "v_mov_b32 v123, v121\n"
    "v_mfma_f32_16x16x32_bf16 v[92:95], v[180:183], v[12:15], v[92:95]\n"
    "v_exp_f32 v61, v61\n"
    "v_mfma_f32_16x16x32_bf16 v[88:91], v[180:183], a[244:247], v[88:91]\n"
    "v_exp_f32 v71, v71\n"
    "v_mfma_f32_16x16x32_bf16 v[88:91], v[176:179], a[240:243], v[88:91]\n"
    "v_exp_f32 v62, v62\n"
    "v_mfma_f32_16x16x32_bf16 v[84:87], v[176:179], a[216:219], v[84:87]\n"
    "ds_read_b128 v[172:175], v200 offset:15360\n"
    "v_cvt_pk_bf16_f32 v46, v60, v61\n"
    "v_mfma_f32_16x16x32_bf16 v[84:87], v[180:183], a[220:223], v[84:87]\n"
    "v_exp_f32 v72, v72\n"
    "v_mfma_f32_16x16x32_bf16 v[80:83], v[180:183], a[196:199], v[80:83]\n"
    "v_exp_f32 v63, v63\n"
    "v_mfma_f32_16x16x32_bf16 v[80:83], v[176:179], a[192:195], v[80:83]\n"
    "s_waitcnt lgkmcnt(2)\n"
    "buffer_load_dwordx4 v200, s[40:43], s61 offen offset:1024 sc1 lds\n"
    "v_mfma_f32_16x16x32_bf16 v[232:235], v[160:163], a[192:195], v[96:99]\n"
    "ds_read_b128 v[176:179], v200 offset:16384\n"
    "v_cvt_pk_bf16_f32 v47, v62, v63\n"
    "v_mfma_f32_16x16x32_bf16 v[232:235], v[164:167], a[196:199], v[232:235]\n"
    "v_exp_f32 v73, v73\n"
    "v_mfma_f32_16x16x32_bf16 v[236:239], v[164:167], a[220:223], v[100:103]\n"
    "v_exp_f32 v74, v74\n"
    "v_mfma_f32_16x16x32_bf16 v[236:239], v[160:163], a[216:219], v[236:239]\n"
    "v_exp_f32 v75, v75\n"
    "v_mfma_f32_16x16x32_bf16 v[240:243], v[160:163], a[240:243], v[104:107]\n"
    "ds_read_b128 v[180:183], v200 offset:17408\n"
    "v_cvt_pk_bf16_f32 v69, v70, v71\n"
    "v_mfma_f32_16x16x32_bf16 v[240:243], v[164:167], a[244:247], v[240:243]\n"
    "v_exp_f32 v76, v76\n"
    "v_mfma_f32_16x16x32_bf16 v[244:247], v[164:167], v[12:15], v[108:111]\n"
    "v_exp_f32 v77, v77\n"
    "v_mfma_f32_16x16x32_bf16 v[244:247], v[160:163], v[8:11], v[244:247]\n"
    "s_waitcnt lgkmcnt(2)\n"
    "buffer_load_dwordx4 v200, s[40:43], s61 offen offset:2048 sc1 lds\n"
    "v_mfma_f32_16x16x32_bf16 v[244:247], v[168:171], v[16:19], v[244:247]\n"
    "ds_read_b128 v[160:163], v200 offset:22528\n"
    "v_cvt_pk_bf16_f32 v72, v72, v73\n"
    "v_mfma_f32_16x16x32_bf16 v[244:247], v[172:175], v[20:23], v[244:247]\n"
    "v_exp_f32 v78, v78\n"
    "v_mfma_f32_16x16x32_bf16 v[240:243], v[172:175], a[252:255], v[240:243]\n"
    "v_exp_f32 v79, v79\n"
    "v_mfma_f32_16x16x32_bf16 v[240:243], v[168:171], a[248:251], v[240:243]\n"
    "v_exp_f32 v80, v80\n"
    "v_mfma_f32_16x16x32_bf16 v[236:239], v[168:171], a[224:227], v[236:239]\n"
    "ds_read_b128 v[164:167], v200 offset:23552\n"
    "v_cvt_pk_bf16_f32 v73, v74, v75\n"
    "v_mfma_f32_16x16x32_bf16 v[236:239], v[172:175], a[228:231], v[236:239]\n"
    "v_exp_f32 v81, v81\n"
    "v_mfma_f32_16x16x32_bf16 v[232:235], v[172:175], a[204:207], v[232:235]\n"
    "v_exp_f32 v82, v82\n"
    "v_mfma_f32_16x16x32_bf16 v[232:235], v[168:171], a[200:203], v[232:235]\n"
    "s_waitcnt lgkmcnt(2)\n"
    "buffer_load_dwordx4 v200, s[40:43], s61 offen offset:3072 sc1 lds\n"
    "v_mfma_f32_16x16x32_bf16 v[232:235], v[176:179], a[208:211], v[232:235]\n"
    "ds_read_b128 v[168:171], v200 offset:20480\n"
    "v_cvt_pk_bf16_f32 v76, v76, v77\n"
    "v_mfma_f32_16x16x32_bf16 v[232:235], v[180:183], a[212:215], v[232:235]\n"
    "v_exp_f32 v83, v83\n"
    "v_mfma_f32_16x16x32_bf16 v[236:239], v[180:183], a[236:239], v[236:239]\n"
    "v_exp_f32 v84, v84\n"
    "v_mfma_f32_16x16x32_bf16 v[236:239], v[176:179], a[232:235], v[236:239]\n"
    "v_exp_f32 v85, v85\n"
    "v_mfma_f32_16x16x32_bf16 v[240:243], v[176:179], v[0:3], v[240:243]\n"
    "ds_read_b128 v[172:175], v200 offset:21504\n"
    "v_cvt_pk_bf16_f32 v77, v78, v79\n"
    "v_mfma_f32_16x16x32_bf16 v[240:243], v[180:183], v[4:7], v[240:243]\n"
    "s_add_u32 s65, s61, 0x1000\n"
    "v_exp_f32 v86, v86\n"
    "v_mfma_f32_16x16x32_bf16 v[244:247], v[180:183], v[28:31], v[244:247]\n"
    "s_add_u32 m0, s71, 0x19000\n"
    "v_exp_f32 v87, v87\n"
    "v_mfma_f32_16x16x32_bf16 v[244:247], v[176:179], v[24:27], v[244:247]\n"
    "s_waitcnt lgkmcnt(2)\n"
    "buffer_load_dwordx4 v200, s[40:43], s65 offen sc1 lds\n"
    "v_mfma_f32_16x16x32_bf16 v[60:63], v[160:163], v[24:27], v[108:111]\n"
    "ds_read_b128 v[176:179], v200 offset:18432\n"
    "v_cvt_pk_bf16_f32 v66, v80, v81\n"
    "v_mfma_f32_16x16x32_bf16 v[60:63], v[164:167], v[28:31], v[60:63]\n"
    "v_exp_f32 v88, v88\n"
    "v_mfma_f32_16x16x32_bf16 v[56:59], v[164:167], v[4:7], v[104:107]\n"
    "v_exp_f32 v89, v89\n"
    "v_mfma_f32_16x16x32_bf16 v[56:59], v[160:163], v[0:3], v[56:59]\n"
    "v_exp_f32 v90, v90\n"
    "v_mfma_f32_16x16x32_bf16 v[52:55], v[160:163], a[232:235], v[100:103]\n"
    "ds_read_b128 v[180:183], v200 offset:19456\n"
    "v_cvt_pk_bf16_f32 v67, v82, v83\n"
    "v_mfma_f32_16x16x32_bf16 v[52:55], v[164:167], a[236:239], v[52:55]\n"
    "v_exp_f32 v91, v91\n"
    "v_mfma_f32_16x16x32_bf16 v[48:51], v[164:167], a[212:215], v[96:99]\n"
    "s_waitcnt vmcnt(5)\n"
    "v_exp_f32 v92, v92\n"
    "v_mfma_f32_16x16x32_bf16 v[48:51], v[160:163], a[208:211], v[48:51]\n"
    "s_waitcnt lgkmcnt(2)\n"
    "buffer_load_dwordx4 v200, s[40:43], s65 offen offset:1024 sc1 lds\n"
    "v_mfma_f32_16x16x32_bf16 v[48:51], v[168:171], a[200:203], v[48:51]\n"
    "s_barrier\n"
    "v_exp_f32 v93, v93\n"
    "v_mfma_f32_16x16x32_bf16 v[48:51], v[172:175], a[204:207], v[48:51]\n"
    "ds_read_b128 v[160:163], v200 offset:24576\n"
    "v_cvt_pk_bf16_f32 v70, v84, v85\n"
    "v_mfma_f32_16x16x32_bf16 v[52:55], v[172:175], a[228:231], v[52:55]\n"
    "v_exp_f32 v94, v94\n"
    "v_mfma_f32_16x16x32_bf16 v[52:55], v[168:171], a[224:227], v[52:55]\n"
    "v_exp_f32 v95, v95\n"
    "v_mfma_f32_16x16x32_bf16 v[56:59], v[168:171], a[248:251], v[56:59]\n"
    "ds_read_b128 v[164:167], v200 offset:25600\n"
    "v_cvt_pk_bf16_f32 v71, v86, v87\n"
    "v_mfma_f32_16x16x32_bf16 v[56:59], v[172:175], a[252:255], v[56:59]\n"
    "v_exp_f32 v232, v232\n"
    "v_mfma_f32_16x16x32_bf16 v[60:63], v[172:175], v[20:23], v[60:63]\n"
    "v_exp_f32 v233, v233\n"
    "v_mfma_f32_16x16x32_bf16 v[60:63], v[168:171], v[16:19], v[60:63]\n"
    "s_waitcnt lgkmcnt(2)\n"
    "v_exp_f32 v234, v234\n"
    "v_mfma_f32_16x16x32_bf16 v[60:63], v[176:179], v[8:11], v[60:63]\n"
    "s_lshl_b32 s76, s56, 3\n"
    "ds_read_b128 v[168:171], v200 offset:26624\n"
    "v_cvt_pk_bf16_f32 v74, v88, v89\n"
    "v_mfma_f32_16x16x32_bf16 v[60:63], v[180:183], v[12:15], v[60:63]\n"
    "s_add_u32 s76, s76, s57\n"
    "v_exp_f32 v235, v235\n"
    "v_mfma_f32_16x16x32_bf16 v[56:59], v[180:183], a[244:247], v[56:59]\n"
    "s_mul_i32 s76, s76, 0x18000\n"
    "v_exp_f32 v236, v236\n"
    "v_mfma_f32_16x16x32_bf16 v[56:59], v[176:179], a[240:243], v[56:59]\n"
    "s_add_u32 s76, s76, s88\n"
    "v_exp_f32 v237, v237\n"
    "v_mfma_f32_16x16x32_bf16 v[52:55], v[176:179], a[216:219], v[52:55]\n"
    "s_add_u32 s28, s24, s76\n"
    "ds_read_b128 v[172:175], v200 offset:27648\n"
    "v_cvt_pk_bf16_f32 v75, v90, v91\n"
    "v_mfma_f32_16x16x32_bf16 v[52:55], v[180:183], a[220:223], v[52:55]\n"
    "s_addc_u32 s29, s25, 0\n"
    "v_exp_f32 v238, v238\n"
    "v_mfma_f32_16x16x32_bf16 v[48:51], v[180:183], a[196:199], v[48:51]\n"
    "s_and_b32 s29, s29, 0xffff\n"
    "v_exp_f32 v239, v239\n"
    "v_mfma_f32_16x16x32_bf16 v[48:51], v[176:179], a[192:195], v[48:51]\n"
    "s_waitcnt lgkmcnt(2)\n"
    "v_exp_f32 v240, v240\n"
    "v_mfma_f32_16x16x32_bf16 v[80:83], v[160:163], a[192:195], v[96:99]\n"
    "s_mov_b32 s30, 0x6000\n"
    "ds_read_b128 v[176:179], v200 offset:28672\n"
    "v_cvt_pk_bf16_f32 v78, v92, v93\n"
    "v_mfma_f32_16x16x32_bf16 v[80:83], v[164:167], a[196:199], v[80:83]\n"
    "s_mov_b32 s31, 0x20000\n"
    "v_cvt_pk_bf16_f32 v79, v94, v95\n"
    "v_mov_b32 v127, v125\n"
    "v_mfma_f32_16x16x32_bf16 v[84:87], v[164:167], a[220:223], v[100:103]\n"
    "s_xor_b32 s94, s70, 0x80000000\n"
    "v_exp_f32 v241, v241\n"
    "v_mfma_f32_16x16x32_bf16 v[84:87], v[160:163], a[216:219], v[84:87]\n"
    "s_xor_b32 s95, s70, 0x80000000\n"
    "v_exp_f32 v242, v242\n"
    "v_mfma_f32_16x16x32_bf16 v[88:91], v[160:163], a[240:243], v[104:107]\n"
    "s_mov_b32 s62, 0x1800\n"
    "ds_read_b128 v[180:183], v200 offset:29696\n"
    "v_permlane16_swap_b32 v113, v115\n"
    "v_mfma_f32_16x16x32_bf16 v[88:91], v[164:167], a[244:247], v[88:91]\n"
    "s_mov_b32 s63, 0x3000\n"
    "v_exp_f32 v243, v243\n"
    "v_mfma_f32_16x16x32_bf16 v[92:95], v[164:167], v[12:15], v[108:111]\n"
    "s_mov_b32 s64, 0x4800\n"
    "v_exp_f32 v244, v244\n"
    "v_mfma_f32_16x16x32_bf16 v[92:95], v[160:163], v[8:11], v[92:95]\n"
    "s_waitcnt lgkmcnt(2)\n"
    "v_exp_f32 v245, v245\n"
    "v_mfma_f32_16x16x32_bf16 v[92:95], v[168:171], v[16:19], v[92:95]\n"
    "ds_read_b128 v[160:163], v200 offset:34816\n"
    "v_permlane16_swap_b32 v117, v119\n"
    "v_mfma_f32_16x16x32_bf16 v[92:95], v[172:175], v[20:23], v[92:95]\n"
    "v_exp_f32 v246, v246\n"
    "v_mfma_f32_16x16x32_bf16 v[88:91], v[172:175], a[252:255], v[88:91]\n"
    "v_exp_f32 v247, v247\n"
    "v_mfma_f32_16x16x32_bf16 v[88:91], v[168:171], a[248:251], v[88:91]\n"
    "v_exp_f32 v48, v48\n"
    "v_mfma_f32_16x16x32_bf16 v[84:87], v[168:171], a[224:227], v[84:87]\n"
    "ds_read_b128 v[164:167], v200 offset:35840\n"
    "v_permlane16_swap_b32 v121, v123\n"
    "v_mfma_f32_16x16x32_bf16 v[84:87], v[172:175], a[228:231], v[84:87]\n"
    "v_exp_f32 v49, v49\n"
    "v_mfma_f32_16x16x32_bf16 v[80:83], v[172:175], a[204:207], v[80:83]\n"
    "v_exp_f32 v50, v50\n"
    "v_mfma_f32_16x16x32_bf16 v[80:83], v[168:171], a[200:203], v[80:83]\n"
    "s_waitcnt lgkmcnt(2)\n"
    "v_exp_f32 v51, v51\n"
    "v_mfma_f32_16x16x32_bf16 v[80:83], v[176:179], a[208:211], v[80:83]\n"
    "ds_read_b128 v[168:171], v200 offset:32768\n"
    "v_permlane16_swap_b32 v125, v127\n"
    "v_mfma_f32_16x16x32_bf16 v[80:83], v[180:183], a[212:215], v[80:83]\n"
    "v_exp_f32 v52, v52\n"
    "v_mfma_f32_16x16x32_bf16 v[84:87], v[180:183], a[236:239], v[84:87]\n"
    "v_exp_f32 v53, v53\n"
    "v_mfma_f32_16x16x32_bf16 v[84:87], v[176:179], a[232:235], v[84:87]\n"
    "v_exp_f32 v54, v54\n"
    "v_mfma_f32_16x16x32_bf16 v[88:91], v[176:179], v[0:3], v[88:91]\n"
    "ds_read_b128 v[172:175], v200 offset:33792\n"
    "v_add_f32 v113, v113, v115\n"
    "v_mfma_f32_16x16x32_bf16 v[88:91], v[180:183], v[4:7], v[88:91]\n"
    "v_add_f32 v117, v117, v119\n"
    "v_add_f32 v121, v121, v123\n"
    "v_mfma_f32_16x16x32_bf16 v[92:95], v[180:183], v[28:31], v[92:95]\n"
    "v_add_f32 v125, v125, v127\n"
    "v_add_f32 v112, v112, v113\n"
    "v_mfma_f32_16x16x32_bf16 v[92:95], v[176:179], v[24:27], v[92:95]\n"
    "s_waitcnt lgkmcnt(2)\n"
    "v_add_f32 v116, v116, v117\n"
    "v_add_f32 v120, v120, v121\n"
    "v_mfma_f32_16x16x32_bf16 v[252:255], v[160:163], v[24:27], v[108:111]\n"
    "ds_read_b128 v[176:179], v200 offset:30720\n"
    "v_add_f32 v124, v124, v125\n"
    "v_mfma_f32_16x16x32_bf16 v[252:255], v[164:167], v[28:31], v[252:255]\n"
    "v_and_b32 v198, 15, v231\n"
    "v_mul_u32_u24 v205, 0x180, v198\n"
    "v_mfma_f32_16x16x32_bf16 v[248:251], v[164:167], v[4:7], v[104:107]\n"
    "v_exp_f32 v55, v55\n"
    "v_mfma_f32_16x16x32_bf16 v[248:251], v[160:163], v[0:3], v[248:251]\n"
    "v_lshrrev_b32 v199, 4, v231\n"
    "v_lshl_add_u32 v205, v199, 4, v205\n"
    "v_mfma_f32_16x16x32_bf16 v[188:191], v[160:163], a[232:235], v[100:103]\n"
    "ds_read_b128 v[180:183], v200 offset:31744\n"
    "v_add_u32 v207, 0x18000, v200\n"
    "v_mfma_f32_16x16x32_bf16 v[188:191], v[164:167], a[236:239], v[188:191]\n"
    "v_exp_f32 v56, v56\n"
    "v_mfma_f32_16x16x32_bf16 v[184:187], v[164:167], a[212:215], v[96:99]\n"
    "v_exp_f32 v57, v57\n"
    "v_mfma_f32_16x16x32_bf16 v[184:187], v[160:163], a[208:211], v[184:187]\n"
    "s_waitcnt lgkmcnt(2)\n"
    "v_exp_f32 v58, v58\n"
    "v_mfma_f32_16x16x32_bf16 v[184:187], v[168:171], a[200:203], v[184:187]\n"
    "ds_read_b128 v[160:163], v201\n"
    "v_cvt_pk_bf16_f32 v232, v232, v233\n"
    "v_mfma_f32_16x16x32_bf16 v[184:187], v[172:175], a[204:207], v[184:187]\n"
    "v_exp_f32 v59, v59\n"
    "v_mfma_f32_16x16x32_bf16 v[188:191], v[172:175], a[228:231], v[188:191]\n"
    "v_exp_f32 v60, v60\n"
    "v_mfma_f32_16x16x32_bf16 v[188:191], v[168:171], a[224:227], v[188:191]\n"
    "v_exp_f32 v61, v61\n"
    "v_mfma_f32_16x16x32_bf16 v[248:251], v[168:171], a[248:251], v[248:251]\n"
    "ds_read_b128 v[164:167], v201 offset:1024\n"
    "v_cvt_pk_bf16_f32 v233, v234, v235\n"
    "v_mfma_f32_16x16x32_bf16 v[248:251], v[172:175], a[252:255], v[248:251]\n"
    "v_exp_f32 v62, v62\n"
    "v_mfma_f32_16x16x32_bf16 v[252:255], v[172:175], v[20:23], v[252:255]\n"
    "v_exp_f32 v63, v63\n"
    "v_mfma_f32_16x16x32_bf16 v[252:255], v[168:171], v[16:19], v[252:255]\n"
    "s_waitcnt lgkmcnt(2)\n"
    "v_exp_f32 v80, v80\n"
    "v_mfma_f32_16x16x32_bf16 v[252:255], v[176:179], v[8:11], v[252:255]\n"
    "ds_read_b128 v[168:171], v201 offset:24576\n"
    "v_cvt_pk_bf16_f32 v236, v236, v237\n"
    "v_mfma_f32_16x16x32_bf16 v[252:255], v[180:183], v[12:15], v[252:255]\n"
    "v_exp_f32 v81, v81\n"
    "v_mfma_f32_16x16x32_bf16 v[248:251], v[180:183], a[244:247], v[248:251]\n"
    "v_exp_f32 v82, v82\n"
    "v_mfma_f32_16x16x32_bf16 v[248:251], v[176:179], a[240:243], v[248:251]\n"
    "v_exp_f32 v83, v83\n"
    "v_mfma_f32_16x16x32_bf16 v[188:191], v[176:179], a[216:219], v[188:191]\n"
    "ds_read_b128 v[172:175], v201 offset:25600\n"
    "v_cvt_pk_bf16_f32 v237, v238, v239\n"
    "v_mfma_f32_16x16x32_bf16 v[188:191], v[180:183], a[220:223], v[188:191]\n"
    "v_exp_f32 v84, v84\n"
    "v_mfma_f32_16x16x32_bf16 v[184:187], v[180:183], a[196:199], v[184:187]\n"
    "v_exp_f32 v85, v85\n"
    "v_mfma_f32_16x16x32_bf16 v[184:187], v[176:179], a[192:195], v[184:187]\n"
    "s_waitcnt lgkmcnt(2)\n"
    "v_exp_f32 v86, v86\n"
    "v_mfma_f32_16x16x32_bf16 a[0:3], v[160:163], v[128:131], a[0:3]\n"
    "ds_read_b128 v[176:179], v201 offset:2048\n"
    "v_cvt_pk_bf16_f32 v240, v240, v241\n"
    "v_mfma_f32_16x16x32_bf16 a[0:3], v[164:167], v[144:147], a[0:3]\n"
    "v_exp_f32 v87, v87\n"
    "v_mfma_f32_16x16x32_bf16 a[4:7], v[160:163], v[132:135], a[4:7]\n"
    "v_exp_f32 v88, v88\n"
    "v_mfma_f32_16x16x32_bf16 a[4:7], v[164:167], v[148:151], a[4:7]\n"
    "v_cvt_pk_bf16_f32 v241, v242, v243\n"
    "v_cvt_pk_bf16_f32 v244, v244, v245\n"
    "v_mfma_f32_16x16x32_bf16 a[8:11], v[160:163], v[136:139], a[8:11]\n"
    "ds_read_b128 v[180:183], v201 offset:3072\n"
    "v_cvt_pk_bf16_f32 v245, v246, v247\n"
    "v_mfma_f32_16x16x32_bf16 a[8:11], v[164:167], v[152:155], a[8:11]\n"
    "v_cvt_pk_bf16_f32 v234, v48, v49\n"
    "v_cvt_pk_bf16_f32 v235, v50, v51\n"
    "v_mfma_f32_16x16x32_bf16 a[12:15], v[160:163], v[140:143], a[12:15]\n"
    "v_exp_f32 v89, v89\n"
    "v_mfma_f32_16x16x32_bf16 a[12:15], v[164:167], v[156:159], a[12:15]\n"
    "v_cvt_pk_bf16_f32 v238, v52, v53\n"
    "v_cvt_pk_bf16_f32 v239, v54, v55\n"
    "v_mfma_f32_16x16x32_bf16 v[112:115], v[192:195], v[144:147], v[112:115]\n"
    "v_cvt_pk_bf16_f32 v242, v56, v57\n"
    "v_cvt_pk_bf16_f32 v243, v58, v59\n"
    "v_mfma_f32_16x16x32_bf16 v[112:115], v[192:195], v[32:35], v[112:115]\n"
    "v_cvt_pk_bf16_f32 v246, v60, v61\n"
    "v_cvt_pk_bf16_f32 v247, v62, v63\n"
    "v_mfma_f32_16x16x32_bf16 v[112:115], v[192:195], v[64:67], v[112:115]\n"
    "v_exp_f32 v90, v90\n"
    "v_mfma_f32_16x16x32_bf16 v[116:119], v[192:195], v[148:151], v[116:119]\n"
    "v_exp_f32 v91, v91\n"
    "v_mfma_f32_16x16x32_bf16 v[116:119], v[192:195], v[36:39], v[116:119]\n"
    "v_exp_f32 v92, v92\n"
    "v_mfma_f32_16x16x32_bf16 v[116:119], v[192:195], v[68:71], v[116:119]\n"
    "s_waitcnt lgkmcnt(2)\n"
    "v_exp_f32 v93, v93\n"
    "v_mfma_f32_16x16x32_bf16 a[0:3], v[168:171], v[32:35], a[0:3]\n"
    "ds_read_b128 v[160:163], v201 offset:26624\n"
    "v_cvt_pk_bf16_f32 v80, v80, v81\n"
    "v_mfma_f32_16x16x32_bf16 a[0:3], v[172:175], v[64:67], a[0:3]\n"
    "v_exp_f32 v94, v94\n"
    "v_mfma_f32_16x16x32_bf16 a[4:7], v[168:171], v[36:39], a[4:7]\n"
    "v_exp_f32 v95, v95\n"
    "v_mfma_f32_16x16x32_bf16 a[4:7], v[172:175], v[68:71], a[4:7]\n"
    "v_exp_f32 v184, v184\n"
    "v_mfma_f32_16x16x32_bf16 a[8:11], v[168:171], v[40:43], a[8:11]\n"
    "ds_read_b128 v[164:167], v201 offset:27648\n"
    "v_cvt_pk_bf16_f32 v81, v82, v83\n"
    "v_mfma_f32_16x16x32_bf16 a[8:11], v[172:175], v[72:75], a[8:11]\n"
    "v_exp_f32 v185, v185\n"
    "v_mfma_f32_16x16x32_bf16 a[12:15], v[168:171], v[44:47], a[12:15]\n"
    "v_exp_f32 v186, v186\n"
    "v_mfma_f32_16x16x32_bf16 a[12:15], v[172:175], v[76:79], a[12:15]\n"
    "v_exp_f32 v187, v187\n"
    "v_mfma_f32_16x16x32_bf16 v[120:123], v[192:195], v[152:155], v[120:123]\n"
    "v_exp_f32 v188, v188\n"
    "v_mfma_f32_16x16x32_bf16 v[120:123], v[192:195], v[40:43], v[120:123]\n"
    "v_exp_f32 v189, v189\n"
    "v_mfma_f32_16x16x32_bf16 v[120:123], v[192:195], v[72:75], v[120:123]\n"
    "v_exp_f32 v190, v190\n"
    "v_mfma_f32_16x16x32_bf16 v[124:127], v[192:195], v[156:159], v[124:127]\n"
    "v_exp_f32 v191, v191\n"
    "v_mfma_f32_16x16x32_bf16 v[124:127], v[192:195], v[44:47], v[124:127]\n"
    "s_waitcnt vmcnt(0)\n"
    "v_exp_f32 v248, v248\n"
    "v_mfma_f32_16x16x32_bf16 v[124:127], v[192:195], v[76:79], v[124:127]\n"
    "s_waitcnt lgkmcnt(2)\n"
    "v_exp_f32 v249, v249\n"
    "v_mfma_f32_16x16x32_bf16 a[16:19], v[176:179], v[128:131], a[16:19]\n"
    "s_barrier\n"
    "v_exp_f32 v250, v250\n"
    "v_mfma_f32_16x16x32_bf16 a[16:19], v[180:183], v[144:147], a[16:19]\n"
    "ds_read_b128 v[168:171], v207\n"
    "v_cvt_pk_bf16_f32 v84, v84, v85\n"
    "v_mfma_f32_16x16x32_bf16 a[20:23], v[176:179], v[132:135], a[20:23]\n"
    "v_exp_f32 v251, v251\n"
    "v_mfma_f32_16x16x32_bf16 a[20:23], v[180:183], v[148:151], a[20:23]\n"
    "v_exp_f32 v252, v252\n"
    "v_mfma_f32_16x16x32_bf16 a[24:27], v[176:179], v[136:139], a[24:27]\n"
    "ds_read_b128 v[172:175], v207 offset:1024\n"
    "v_cvt_pk_bf16_f32 v85, v86, v87\n"
    "v_mfma_f32_16x16x32_bf16 a[24:27], v[180:183], v[152:155], a[24:27]\n"
    "v_exp_f32 v253, v253\n"
    "v_mfma_f32_16x16x32_bf16 a[28:31], v[176:179], v[140:143], a[28:31]\n"
    "v_cvt_pk_bf16_f32 v88, v88, v89\n"
    "v_cvt_pk_bf16_f32 v89, v90, v91\n"
    "v_mfma_f32_16x16x32_bf16 a[28:31], v[180:183], v[156:159], a[28:31]\n"
    "s_waitcnt lgkmcnt(2)\n"
    "v_exp_f32 v254, v254\n"
    "v_mfma_f32_16x16x32_bf16 a[16:19], v[160:163], v[32:35], a[16:19]\n"
    "ds_read_b128 v[176:179], v207 offset:2048\n"
    "v_cvt_pk_bf16_f32 v92, v92, v93\n"
    "v_mfma_f32_16x16x32_bf16 a[16:19], v[164:167], v[64:67], a[16:19]\n"
    "ds_read_b128 v[180:183], v207 offset:3072\n"
    "v_cvt_pk_bf16_f32 v93, v94, v95\n"
    "v_mfma_f32_16x16x32_bf16 a[20:23], v[160:163], v[36:39], a[20:23]\n"
    "v_cvt_pk_bf16_f32 v82, v184, v185\n"
    "v_cvt_pk_bf16_f32 v83, v186, v187\n"
    "v_mfma_f32_16x16x32_bf16 a[20:23], v[164:167], v[68:71], a[20:23]\n"
    "v_exp_f32 v255, v255\n"
    "v_mfma_f32_16x16x32_bf16 a[24:27], v[160:163], v[40:43], a[24:27]\n"
    "v_cvt_pk_bf16_f32 v86, v188, v189\n"
    "v_cvt_pk_bf16_f32 v87, v190, v191\n"
    "v_mfma_f32_16x16x32_bf16 a[24:27], v[164:167], v[72:75], a[24:27]\n"
    "v_cvt_pk_bf16_f32 v90, v248, v249\n"
    "v_cvt_pk_bf16_f32 v91, v250, v251\n"
    "v_mfma_f32_16x16x32_bf16 a[28:31], v[160:163], v[44:47], a[28:31]\n"
    "v_cvt_pk_bf16_f32 v94, v252, v253\n"
    "v_cvt_pk_bf16_f32 v95, v254, v255\n"
    "v_mfma_f32_16x16x32_bf16 a[28:31], v[164:167], v[76:79], a[28:31]\n"
    "v_mfma_f32_16x16x32_bf16 v[112:115], v[192:195], v[232:235], v[112:115]\n"
    "ds_read_b128 v[160:163], v201 offset:4096\n"
    "v_mfma_f32_16x16x32_bf16 v[112:115], v[192:195], v[80:83], v[112:115]\n"
    "s_waitcnt lgkmcnt(3)\n"
    "v_mfma_f32_16x16x32_bf16 a[0:3], v[168:171], v[232:235], a[0:3]\n"
    "v_mfma_f32_16x16x32_bf16 a[0:3], v[172:175], v[80:83], a[0:3]\n"
    "s_waitcnt lgkmcnt(1)\n"
    "v_mfma_f32_16x16x32_bf16 a[16:19], v[176:179], v[232:235], a[16:19]\n"
    "ds_read_b128 v[164:167], v201 offset:5120\n"
    "v_mfma_f32_16x16x32_bf16 a[16:19], v[180:183], v[80:83], a[16:19]\n"
    "v_mfma_f32_16x16x32_bf16 v[116:119], v[192:195], v[236:239], v[116:119]\n"
    "v_mul_f32 v222, s68, v112\n"
    "v_rcp_f32 v222, v222\n"
    "v_mfma_f32_16x16x32_bf16 v[116:119], v[192:195], v[84:87], v[116:119]\n"
    "v_accvgpr_read_b32 v96, a0\n"
    "v_accvgpr_read_b32 v97, a1\n"
    "v_accvgpr_read_b32 v98, a2\n"
    "v_mfma_f32_16x16x32_bf16 a[4:7], v[168:171], v[236:239], a[4:7]\n"
    "v_accvgpr_read_b32 v99, a3\n"
    "v_fma_f32 v96, v96, v222, s94\n"
    "v_fma_f32 v97, v97, v222, s94\n"
    "v_mfma_f32_16x16x32_bf16 a[4:7], v[172:175], v[84:87], a[4:7]\n"
    "v_fma_f32 v98, v98, v222, s94\n"
    "v_fma_f32 v99, v99, v222, s94\n"
    "v_cvt_pk_bf16_f32 v48, v96, v97\n"
    "v_mfma_f32_16x16x32_bf16 a[20:23], v[176:179], v[236:239], a[20:23]\n"
    "v_cvt_pk_bf16_f32 v49, v98, v99\n"
    "v_accvgpr_read_b32 v100, a16\n"
    "v_accvgpr_read_b32 v101, a17\n"
    "v_mfma_f32_16x16x32_bf16 a[20:23], v[180:183], v[84:87], a[20:23]\n"
    "v_accvgpr_read_b32 v102, a18\n"
    "v_accvgpr_read_b32 v103, a19\n"
    "v_fma_f32 v100, v100, v222, s94\n"
    "v_mfma_f32_16x16x32_bf16 v[120:123], v[192:195], v[240:243], v[120:123]\n"
    "v_fma_f32 v101, v101, v222, s94\n"
    "v_fma_f32 v102, v102, v222, s94\n"
    "v_fma_f32 v103, v103, v222, s94\n"
    "v_mfma_f32_16x16x32_bf16 v[120:123], v[192:195], v[88:91], v[120:123]\n"
    "v_cvt_pk_bf16_f32 v50, v100, v101\n"
    "v_cvt_pk_bf16_f32 v51, v102, v103\n"
    "v_mfma_f32_16x16x32_bf16 a[8:11], v[168:171], v[240:243], a[8:11]\n"
    "buffer_store_dwordx4 v[48:51], v205, s[28:31], 0 offen sc0 sc1\n"
    "v_mul_f32 v224, s68, v116\n"
    "v_rcp_f32 v224, v224\n"
    "v_accvgpr_read_b32 v104, a4\n"
    "v_mfma_f32_16x16x32_bf16 a[8:11], v[172:175], v[88:91], a[8:11]\n"
    "v_accvgpr_read_b32 v105, a5\n"
    "v_accvgpr_read_b32 v106, a6\n"
    "v_accvgpr_read_b32 v107, a7\n"
    "v_mfma_f32_16x16x32_bf16 a[24:27], v[176:179], v[240:243], a[24:27]\n"
    "v_fma_f32 v104, v104, v224, s94\n"
    "v_fma_f32 v105, v105, v224, s94\n"
    "v_fma_f32 v106, v106, v224, s94\n"
    "v_mfma_f32_16x16x32_bf16 a[24:27], v[180:183], v[88:91], a[24:27]\n"
    "v_fma_f32 v107, v107, v224, s94\n"
    "v_cvt_pk_bf16_f32 v52, v104, v105\n"
    "v_cvt_pk_bf16_f32 v53, v106, v107\n"
    "v_mfma_f32_16x16x32_bf16 v[124:127], v[192:195], v[244:247], v[124:127]\n"
    "v_accvgpr_read_b32 v108, a20\n"
    "v_accvgpr_read_b32 v109, a21\n"
    "v_accvgpr_read_b32 v110, a22\n"
    "v_mfma_f32_16x16x32_bf16 v[124:127], v[192:195], v[92:95], v[124:127]\n"
    "v_accvgpr_read_b32 v111, a23\n"
    "v_fma_f32 v108, v108, v224, s94\n"
    "v_fma_f32 v109, v109, v224, s94\n"
    "v_mfma_f32_16x16x32_bf16 a[12:15], v[168:171], v[244:247], a[12:15]\n"
    "v_fma_f32 v110, v110, v224, s94\n"
    "v_fma_f32 v111, v111, v224, s94\n"
    "v_mfma_f32_16x16x32_bf16 a[12:15], v[172:175], v[92:95], a[12:15]\n"
    "v_cvt_pk_bf16_f32 v54, v108, v109\n"
    "v_cvt_pk_bf16_f32 v55, v110, v111\n"
    "v_mfma_f32_16x16x32_bf16 a[28:31], v[176:179], v[244:247], a[28:31]\n"
    "ds_read_b128 v[168:171], v201 offset:28672\n"
    "v_mfma_f32_16x16x32_bf16 a[28:31], v[180:183], v[92:95], a[28:31]\n"
    "s_waitcnt lgkmcnt(1)\n"
    "ds_read_b128 v[172:175], v201 offset:29696\n"
    "v_mfma_f32_16x16x32_bf16 a[32:35], v[160:163], v[128:131], a[32:35]\n"
    "ds_read_b128 v[176:179], v207 offset:4096\n"
    "v_mfma_f32_16x16x32_bf16 a[32:35], v[164:167], v[144:147], a[32:35]\n"
    "buffer_store_dwordx4 v[52:55], v205, s[28:31], s62 offen sc0 sc1\n"
    "v_mul_f32 v226, s68, v120\n"
    "v_rcp_f32 v226, v226\n"
    "v_mfma_f32_16x16x32_bf16 a[36:39], v[160:163], v[132:135], a[36:39]\n"
    "v_accvgpr_read_b32 v96, a8\n"
    "v_accvgpr_read_b32 v97, a9\n"
    "v_mfma_f32_16x16x32_bf16 a[36:39], v[164:167], v[148:151], a[36:39]\n"
    "v_accvgpr_read_b32 v98, a10\n"
    "v_accvgpr_read_b32 v99, a11\n"
    "v_mfma_f32_16x16x32_bf16 a[40:43], v[160:163], v[136:139], a[40:43]\n"
    "ds_read_b128 v[180:183], v207 offset:5120\n"
    "v_fma_f32 v96, v96, v226, s94\n"
    "v_mfma_f32_16x16x32_bf16 a[40:43], v[164:167], v[152:155], a[40:43]\n"
    "v_fma_f32 v97, v97, v226, s94\n"
    "v_fma_f32 v98, v98, v226, s94\n"
    "v_mfma_f32_16x16x32_bf16 a[44:47], v[160:163], v[140:143], a[44:47]\n"
    "v_fma_f32 v99, v99, v226, s94\n"
    "v_cvt_pk_bf16_f32 v56, v96, v97\n"
    "v_mfma_f32_16x16x32_bf16 a[44:47], v[164:167], v[156:159], a[44:47]\n"
    "s_waitcnt lgkmcnt(2)\n"
    "v_cvt_pk_bf16_f32 v57, v98, v99\n"
    "v_accvgpr_read_b32 v100, a24\n"
    "v_mfma_f32_16x16x32_bf16 a[44:47], v[168:171], v[44:47], a[44:47]\n"
    "ds_read_b128 v[160:163], v201 offset:6144\n"
    "v_accvgpr_read_b32 v101, a25\n"
    "v_mfma_f32_16x16x32_bf16 a[44:47], v[172:175], v[76:79], a[44:47]\n"
    "v_accvgpr_read_b32 v102, a26\n"
    "v_accvgpr_read_b32 v103, a27\n"
    "v_mfma_f32_16x16x32_bf16 a[40:43], v[168:171], v[40:43], a[40:43]\n"
    "v_fma_f32 v100, v100, v226, s94\n"
    "v_fma_f32 v101, v101, v226, s94\n"
    "v_mfma_f32_16x16x32_bf16 a[40:43], v[172:175], v[72:75], a[40:43]\n"
    "v_fma_f32 v102, v102, v226, s94\n"
    "v_fma_f32 v103, v103, v226, s94\n"
    "v_mfma_f32_16x16x32_bf16 a[36:39], v[168:171], v[36:39], a[36:39]\n"
    "ds_read_b128 v[164:167], v201 offset:7168\n"
    "v_cvt_pk_bf16_f32 v58, v100, v101\n"
    "v_mfma_f32_16x16x32_bf16 a[36:39], v[172:175], v[68:71], a[36:39]\n"
    "v_cvt_pk_bf16_f32 v59, v102, v103\n"
    "v_mfma_f32_16x16x32_bf16 a[32:35], v[168:171], v[32:35], a[32:35]\n"
    "buffer_store_dwordx4 v[56:59], v205, s[28:31], s63 offen sc0 sc1\n"
    "v_mul_f32 v228, s68, v124\n"
    "v_rcp_f32 v228, v228\n"
    "v_mfma_f32_16x16x32_bf16 a[32:35], v[172:175], v[64:67], a[32:35]\n"
    "s_waitcnt lgkmcnt(2)\n"
    "v_accvgpr_read_b32 v104, a12\n"
    "v_accvgpr_read_b32 v105, a13\n"
    "v_mfma_f32_16x16x32_bf16 a[32:35], v[176:179], v[232:235], a[32:35]\n"
    "ds_read_b128 v[168:171], v201 offset:30720\n"
    "v_accvgpr_read_b32 v106, a14\n"
    "v_mfma_f32_16x16x32_bf16 a[32:35], v[180:183], v[80:83], a[32:35]\n"
    "v_accvgpr_read_b32 v107, a15\n"
    "v_fma_f32 v104, v104, v228, s94\n"
    "v_mfma_f32_16x16x32_bf16 a[36:39], v[176:179], v[236:239], a[36:39]\n"
    "v_fma_f32 v105, v105, v228, s94\n"
    "v_fma_f32 v106, v106, v228, s94\n"
    "v_mfma_f32_16x16x32_bf16 a[36:39], v[180:183], v[84:87], a[36:39]\n"
    "v_fma_f32 v107, v107, v228, s94\n"
    "v_cvt_pk_bf16_f32 v60, v104, v105\n"
    "v_mfma_f32_16x16x32_bf16 a[40:43], v[176:179], v[240:243], a[40:43]\n"
    "ds_read_b128 v[172:175], v201 offset:31744\n"
    "v_cvt_pk_bf16_f32 v61, v106, v107\n"
    "v_mfma_f32_16x16x32_bf16 a[40:43], v[180:183], v[88:91], a[40:43]\n"
    "v_accvgpr_read_b32 v108, a28\n"
    "v_accvgpr_read_b32 v109, a29\n"
    "v_mfma_f32_16x16x32_bf16 a[44:47], v[176:179], v[244:247], a[44:47]\n"
    "v_accvgpr_read_b32 v110, a30\n"
    "v_accvgpr_read_b32 v111, a31\n"
    "v_mfma_f32_16x16x32_bf16 a[44:47], v[180:183], v[92:95], a[44:47]\n"
    "s_waitcnt lgkmcnt(2)\n"
    "v_fma_f32 v108, v108, v228, s94\n"
    "v_fma_f32 v109, v109, v228, s94\n"
    "v_mfma_f32_16x16x32_bf16 a[48:51], v[160:163], v[128:131], a[48:51]\n"
    "ds_read_b128 v[176:179], v207 offset:6144\n"
    "v_fma_f32 v110, v110, v228, s94\n"
    "v_mfma_f32_16x16x32_bf16 a[48:51], v[164:167], v[144:147], a[48:51]\n"
    "v_fma_f32 v111, v111, v228, s94\n"
    "v_cvt_pk_bf16_f32 v62, v108, v109\n"
    "v_mfma_f32_16x16x32_bf16 a[52:55], v[160:163], v[132:135], a[52:55]\n"
    "v_cvt_pk_bf16_f32 v63, v110, v111\n"
    "v_mfma_f32_16x16x32_bf16 a[52:55], v[164:167], v[148:151], a[52:55]\n"
    "buffer_store_dwordx4 v[60:63], v205, s[28:31], s64 offen sc0 sc1\n"
    "v_accvgpr_read_b32 v96, a32\n"
    "v_accvgpr_read_b32 v97, a33\n"
    "v_accvgpr_read_b32 v98, a34\n"
    "v_mfma_f32_16x16x32_bf16 a[56:59], v[160:163], v[136:139], a[56:59]\n"
    "ds_read_b128 v[180:183], v207 offset:7168\n"
    "v_accvgpr_read_b32 v99, a35\n"
    "v_mfma_f32_16x16x32_bf16 a[56:59], v[164:167], v[152:155], a[56:59]\n"
    "v_fma_f32 v96, v96, v222, s94\n"
    "v_fma_f32 v97, v97, v222, s94\n"
    "v_mfma_f32_16x16x32_bf16 a[60:63], v[160:163], v[140:143], a[60:63]\n"
    "v_fma_f32 v98, v98, v222, s94\n"
    "v_fma_f32 v99, v99, v222, s94\n"
    "v_mfma_f32_16x16x32_bf16 a[60:63], v[164:167], v[156:159], a[60:63]\n"
    "s_waitcnt lgkmcnt(2)\n"
    "v_cvt_pk_bf16_f32 v184, v96, v97\n"
    "v_cvt_pk_bf16_f32 v185, v98, v99\n"
    "v_mfma_f32_16x16x32_bf16 a[60:63], v[168:171], v[44:47], a[60:63]\n"
    "ds_read_b128 v[160:163], v201 offset:8192\n"
    "v_accvgpr_read_b32 v100, a36\n"
    "v_mfma_f32_16x16x32_bf16 a[60:63], v[172:175], v[76:79], a[60:63]\n"
    "v_accvgpr_read_b32 v101, a37\n"
    "v_accvgpr_read_b32 v102, a38\n"
    "v_mfma_f32_16x16x32_bf16 a[56:59], v[168:171], v[40:43], a[56:59]\n"
    "v_accvgpr_read_b32 v103, a39\n"
    "v_fma_f32 v100, v100, v224, s94\n"
    "v_mfma_f32_16x16x32_bf16 a[56:59], v[172:175], v[72:75], a[56:59]\n"
    "v_fma_f32 v101, v101, v224, s94\n"
    "v_fma_f32 v102, v102, v224, s94\n"
    "v_mfma_f32_16x16x32_bf16 a[52:55], v[168:171], v[36:39], a[52:55]\n"
    "ds_read_b128 v[164:167], v201 offset:9216\n"
    "v_fma_f32 v103, v103, v224, s94\n"
    "v_mfma_f32_16x16x32_bf16 a[52:55], v[172:175], v[68:71], a[52:55]\n"
    "v_cvt_pk_bf16_f32 v188, v100, v101\n"
    "v_cvt_pk_bf16_f32 v189, v102, v103\n"
    "v_mfma_f32_16x16x32_bf16 a[48:51], v[168:171], v[32:35], a[48:51]\n"
    "v_accvgpr_read_b32 v104, a40\n"
    "v_accvgpr_read_b32 v105, a41\n"
    "v_mfma_f32_16x16x32_bf16 a[48:51], v[172:175], v[64:67], a[48:51]\n"
    "s_waitcnt lgkmcnt(2)\n"
    "v_accvgpr_read_b32 v106, a42\n"
    "v_accvgpr_read_b32 v107, a43\n"
    "v_mfma_f32_16x16x32_bf16 a[48:51], v[176:179], v[232:235], a[48:51]\n"
    "ds_read_b128 v[168:171], v201 offset:32768\n"
    "v_fma_f32 v104, v104, v226, s94\n"
    "v_mfma_f32_16x16x32_bf16 a[48:51], v[180:183], v[80:83], a[48:51]\n"
    "v_fma_f32 v105, v105, v226, s94\n"
    "v_fma_f32 v106, v106, v226, s94\n"
    "v_mfma_f32_16x16x32_bf16 a[52:55], v[176:179], v[236:239], a[52:55]\n"
    "v_fma_f32 v107, v107, v226, s94\n"
    "v_cvt_pk_bf16_f32 v248, v104, v105\n"
    "v_mfma_f32_16x16x32_bf16 a[52:55], v[180:183], v[84:87], a[52:55]\n"
    "v_cvt_pk_bf16_f32 v249, v106, v107\n"
    "v_accvgpr_read_b32 v108, a44\n"
    "v_mfma_f32_16x16x32_bf16 a[56:59], v[176:179], v[240:243], a[56:59]\n"
    "ds_read_b128 v[172:175], v201 offset:33792\n"
    "v_accvgpr_read_b32 v109, a45\n"
    "v_mfma_f32_16x16x32_bf16 a[56:59], v[180:183], v[88:91], a[56:59]\n"
    "v_accvgpr_read_b32 v110, a46\n"
    "v_accvgpr_read_b32 v111, a47\n"
    "v_mfma_f32_16x16x32_bf16 a[60:63], v[176:179], v[244:247], a[60:63]\n"
    "v_fma_f32 v108, v108, v228, s94\n"
    "v_fma_f32 v109, v109, v228, s94\n"
    "v_mfma_f32_16x16x32_bf16 a[60:63], v[180:183], v[92:95], a[60:63]\n"
    "s_waitcnt lgkmcnt(2)\n"
    "v_fma_f32 v110, v110, v228, s94\n"
    "v_fma_f32 v111, v111, v228, s94\n"
    "v_mfma_f32_16x16x32_bf16 a[64:67], v[160:163], v[128:131], a[64:67]\n"
    "ds_read_b128 v[176:179], v207 offset:8192\n"
    "v_cvt_pk_bf16_f32 v252, v108, v109\n"
    "v_mfma_f32_16x16x32_bf16 a[64:67], v[164:167], v[144:147], a[64:67]\n"
    "v_cvt_pk_bf16_f32 v253, v110, v111\n"
    "v_accvgpr_read_b32 v96, a48\n"
    "v_mfma_f32_16x16x32_bf16 a[68:71], v[160:163], v[132:135], a[68:71]\n"
    "v_accvgpr_read_b32 v97, a49\n"
    "v_accvgpr_read_b32 v98, a50\n"
    "v_mfma_f32_16x16x32_bf16 a[68:71], v[164:167], v[148:151], a[68:71]\n"
    "v_accvgpr_read_b32 v99, a51\n"
    "v_fma_f32 v96, v96, v222, s94\n"
    "v_mfma_f32_16x16x32_bf16 a[72:75], v[160:163], v[136:139], a[72:75]\n"
    "ds_read_b128 v[180:183], v207 offset:9216\n"
    "v_fma_f32 v97, v97, v222, s94\n"
    "v_mfma_f32_16x16x32_bf16 a[72:75], v[164:167], v[152:155], a[72:75]\n"
    "v_fma_f32 v98, v98, v222, s94\n"
    "v_fma_f32 v99, v99, v222, s94\n"
    "v_mfma_f32_16x16x32_bf16 a[76:79], v[160:163], v[140:143], a[76:79]\n"
    "v_cvt_pk_bf16_f32 v186, v96, v97\n"
    "v_cvt_pk_bf16_f32 v187, v98, v99\n"
    "v_mfma_f32_16x16x32_bf16 a[76:79], v[164:167], v[156:159], a[76:79]\n"
    "s_waitcnt lgkmcnt(2)\n"
    "buffer_store_dwordx4 v[184:187], v205, s[28:31], 0 offen offset:64 sc0 sc1\n"
    "v_accvgpr_read_b32 v100, a52\n"
    "v_accvgpr_read_b32 v101, a53\n"
    "v_accvgpr_read_b32 v102, a54\n"
    "v_mfma_f32_16x16x32_bf16 a[76:79], v[168:171], v[44:47], a[76:79]\n"
    "ds_read_b128 v[160:163], v201 offset:10240\n"
    "v_accvgpr_read_b32 v103, a55\n"
    "v_mfma_f32_16x16x32_bf16 a[76:79], v[172:175], v[76:79], a[76:79]\n"
    "v_fma_f32 v100, v100, v224, s94\n"
    "v_fma_f32 v101, v101, v224, s94\n"
    "v_mfma_f32_16x16x32_bf16 a[72:75], v[168:171], v[40:43], a[72:75]\n"
    "v_fma_f32 v102, v102, v224, s94\n"
    "v_fma_f32 v103, v103, v224, s94\n"
    "v_mfma_f32_16x16x32_bf16 a[72:75], v[172:175], v[72:75], a[72:75]\n"
    "v_cvt_pk_bf16_f32 v190, v100, v101\n"
    "v_cvt_pk_bf16_f32 v191, v102, v103\n"
    "v_mfma_f32_16x16x32_bf16 a[68:71], v[168:171], v[36:39], a[68:71]\n"
    "ds_read_b128 v[164:167], v201 offset:11264\n"
    "v_mfma_f32_16x16x32_bf16 a[68:71], v[172:175], v[68:71], a[68:71]\n"
    "buffer_store_dwordx4 v[188:191], v205, s[28:31], s62 offen offset:64 sc0 sc1\n"
    "v_accvgpr_read_b32 v104, a56\n"
    "v_accvgpr_read_b32 v105, a57\n"
    "v_accvgpr_read_b32 v106, a58\n"
    "v_mfma_f32_16x16x32_bf16 a[64:67], v[168:171], v[32:35], a[64:67]\n"
    "v_accvgpr_read_b32 v107, a59\n"
    "v_fma_f32 v104, v104, v226, s94\n"
    "v_mfma_f32_16x16x32_bf16 a[64:67], v[172:175], v[64:67], a[64:67]\n"
    "s_waitcnt lgkmcnt(2)\n"
    "v_fma_f32 v105, v105, v226, s94\n"
    "v_fma_f32 v106, v106, v226, s94\n"
    "v_mfma_f32_16x16x32_bf16 a[64:67], v[176:179], v[232:235], a[64:67]\n"
    "ds_read_b128 v[168:171], v201 offset:34816\n"
    "v_fma_f32 v107, v107, v226, s94\n"
    "v_mfma_f32_16x16x32_bf16 a[64:67], v[180:183], v[80:83], a[64:67]\n"
    "v_cvt_pk_bf16_f32 v250, v104, v105\n"
    "v_cvt_pk_bf16_f32 v251, v106, v107\n"
    "v_mfma_f32_16x16x32_bf16 a[68:71], v[176:179], v[236:239], a[68:71]\n"
    "buffer_store_dwordx4 v[248:251], v205, s[28:31], s63 offen offset:64 sc0 sc1\n"
    "v_accvgpr_read_b32 v108, a60\n"
    "v_accvgpr_read_b32 v109, a61\n"
    "v_accvgpr_read_b32 v110, a62\n"
    "v_mfma_f32_16x16x32_bf16 a[68:71], v[180:183], v[84:87], a[68:71]\n"
    "v_accvgpr_read_b32 v111, a63\n"
    "v_fma_f32 v108, v108, v228, s94\n"
    "v_mfma_f32_16x16x32_bf16 a[72:75], v[176:179], v[240:243], a[72:75]\n"
    "ds_read_b128 v[172:175], v201 offset:35840\n"
    "v_fma_f32 v109, v109, v228, s94\n"
    "v_mfma_f32_16x16x32_bf16 a[72:75], v[180:183], v[88:91], a[72:75]\n"
    "v_fma_f32 v110, v110, v228, s94\n"
    "v_fma_f32 v111, v111, v228, s94\n"
    "v_mfma_f32_16x16x32_bf16 a[76:79], v[176:179], v[244:247], a[76:79]\n"
    "v_cvt_pk_bf16_f32 v254, v108, v109\n"
    "v_cvt_pk_bf16_f32 v255, v110, v111\n"
    "v_mfma_f32_16x16x32_bf16 a[76:79], v[180:183], v[92:95], a[76:79]\n"
    "s_waitcnt lgkmcnt(2)\n"
    "buffer_store_dwordx4 v[252:255], v205, s[28:31], s64 offen offset:64 sc0 sc1\n"
    "v_accvgpr_read_b32 v96, a64\n"
    "v_accvgpr_read_b32 v97, a65\n"
    "v_accvgpr_read_b32 v98, a66\n"
    "v_mfma_f32_16x16x32_bf16 a[80:83], v[160:163], v[128:131], a[80:83]\n"
    "ds_read_b128 v[176:179], v207 offset:10240\n"
    "v_accvgpr_read_b32 v99, a67\n"
    "v_mfma_f32_16x16x32_bf16 a[80:83], v[164:167], v[144:147], a[80:83]\n"
    "v_fma_f32 v96, v96, v222, s94\n"
    "v_fma_f32 v97, v97, v222, s94\n"
    "v_mfma_f32_16x16x32_bf16 a[84:87], v[160:163], v[132:135], a[84:87]\n"
    "v_fma_f32 v98, v98, v222, s94\n"
    "v_fma_f32 v99, v99, v222, s94\n"
    "v_mfma_f32_16x16x32_bf16 a[84:87], v[164:167], v[148:151], a[84:87]\n"
    "v_cvt_pk_bf16_f32 v48, v96, v97\n"
    "v_cvt_pk_bf16_f32 v49, v98, v99\n"
    "v_mfma_f32_16x16x32_bf16 a[88:91], v[160:163], v[136:139], a[88:91]\n"
    "ds_read_b128 v[180:183], v207 offset:11264\n"
    "v_accvgpr_read_b32 v100, a68\n"
    "v_mfma_f32_16x16x32_bf16 a[88:91], v[164:167], v[152:155], a[88:91]\n"
    "v_accvgpr_read_b32 v101, a69\n"
    "v_accvgpr_read_b32 v102, a70\n"
    "v_mfma_f32_16x16x32_bf16 a[92:95], v[160:163], v[140:143], a[92:95]\n"
    "v_accvgpr_read_b32 v103, a71\n"
    "v_fma_f32 v100, v100, v224, s94\n"
    "v_mfma_f32_16x16x32_bf16 a[92:95], v[164:167], v[156:159], a[92:95]\n"
    "s_waitcnt lgkmcnt(2)\n"
    "v_fma_f32 v101, v101, v224, s94\n"
    "v_fma_f32 v102, v102, v224, s94\n"
    "v_mfma_f32_16x16x32_bf16 a[92:95], v[168:171], v[44:47], a[92:95]\n"
    "ds_read_b128 v[160:163], v201 offset:12288\n"
    "v_fma_f32 v103, v103, v224, s94\n"
    "v_mfma_f32_16x16x32_bf16 a[92:95], v[172:175], v[76:79], a[92:95]\n"
    "v_cvt_pk_bf16_f32 v52, v100, v101\n"
    "v_cvt_pk_bf16_f32 v53, v102, v103\n"
    "v_mfma_f32_16x16x32_bf16 a[88:91], v[168:171], v[40:43], a[88:91]\n"
    "v_accvgpr_read_b32 v104, a72\n"
    "v_accvgpr_read_b32 v105, a73\n"
    "v_mfma_f32_16x16x32_bf16 a[88:91], v[172:175], v[72:75], a[88:91]\n"
    "v_accvgpr_read_b32 v106, a74\n"
    "v_accvgpr_read_b32 v107, a75\n"
    "v_mfma_f32_16x16x32_bf16 a[84:87], v[168:171], v[36:39], a[84:87]\n"
    "ds_read_b128 v[164:167], v201 offset:13312\n"
    "v_fma_f32 v104, v104, v226, s94\n"
    "v_mfma_f32_16x16x32_bf16 a[84:87], v[172:175], v[68:71], a[84:87]\n"
    "v_fma_f32 v105, v105, v226, s94\n"
    "v_fma_f32 v106, v106, v226, s94\n"
    "v_mfma_f32_16x16x32_bf16 a[80:83], v[168:171], v[32:35], a[80:83]\n"
    "v_fma_f32 v107, v107, v226, s94\n"
    "v_cvt_pk_bf16_f32 v56, v104, v105\n"
    "v_mfma_f32_16x16x32_bf16 a[80:83], v[172:175], v[64:67], a[80:83]\n"
    "s_waitcnt lgkmcnt(2)\n"
    "v_cvt_pk_bf16_f32 v57, v106, v107\n"
    "v_accvgpr_read_b32 v108, a76\n"
    "v_mfma_f32_16x16x32_bf16 a[80:83], v[176:179], v[232:235], a[80:83]\n"
    "ds_read_b128 v[168:171], v201 offset:36864\n"
    "v_accvgpr_read_b32 v109, a77\n"
    "v_mfma_f32_16x16x32_bf16 a[80:83], v[180:183], v[80:83], a[80:83]\n"
    "v_accvgpr_read_b32 v110, a78\n"
    "v_accvgpr_read_b32 v111, a79\n"
    "v_mfma_f32_16x16x32_bf16 a[84:87], v[176:179], v[236:239], a[84:87]\n"
    "v_fma_f32 v108, v108, v228, s94\n"
    "v_fma_f32 v109, v109, v228, s94\n"
    "v_mfma_f32_16x16x32_bf16 a[84:87], v[180:183], v[84:87], a[84:87]\n"
    "v_fma_f32 v110, v110, v228, s94\n"
    "v_fma_f32 v111, v111, v228, s94\n"
    "v_mfma_f32_16x16x32_bf16 a[88:91], v[176:179], v[240:243], a[88:91]\n"
    "ds_read_b128 v[172:175], v201 offset:37888\n"
    "v_cvt_pk_bf16_f32 v60, v108, v109\n"
    "v_mfma_f32_16x16x32_bf16 a[88:91], v[180:183], v[88:91], a[88:91]\n"
    "v_cvt_pk_bf16_f32 v61, v110, v111\n"
    "v_mfma_f32_16x16x32_bf16 a[92:95], v[176:179], v[244:247], a[92:95]\n"
    "v_mfma_f32_16x16x32_bf16 a[92:95], v[180:183], v[92:95], a[92:95]\n"
    "s_waitcnt lgkmcnt(2)\n"
    "v_accvgpr_read_b32 v96, a80\n"
    "v_accvgpr_read_b32 v97, a81\n"
    "v_mfma_f32_16x16x32_bf16 a[96:99], v[160:163], v[128:131], a[96:99]\n"
    "ds_read_b128 v[176:179], v207 offset:12288\n"
    "v_accvgpr_read_b32 v98, a82\n"
    "v_mfma_f32_16x16x32_bf16 a[96:99], v[164:167], v[144:147], a[96:99]\n"
    "v_accvgpr_read_b32 v99, a83\n"
    "v_fma_f32 v96, v96, v222, s94\n"
    "v_mfma_f32_16x16x32_bf16 a[100:103], v[160:163], v[132:135], a[100:103]\n"
    "v_fma_f32 v97, v97, v222, s94\n"
    "v_fma_f32 v98, v98, v222, s94\n"
    "v_mfma_f32_16x16x32_bf16 a[100:103], v[164:167], v[148:151], a[100:103]\n"
    "v_fma_f32 v99, v99, v222, s94\n"
    "v_cvt_pk_bf16_f32 v50, v96, v97\n"
    "v_mfma_f32_16x16x32_bf16 a[104:107], v[160:163], v[136:139], a[104:107]\n"
    "ds_read_b128 v[180:183], v207 offset:13312\n"
    "v_cvt_pk_bf16_f32 v51, v98, v99\n"
    "v_mfma_f32_16x16x32_bf16 a[104:107], v[164:167], v[152:155], a[104:107]\n"
    "buffer_store_dwordx4 v[48:51], v205, s[28:31], 0 offen offset:128 sc0 sc1\n"
    "v_accvgpr_read_b32 v100, a84\n"
    "v_accvgpr_read_b32 v101, a85\n"
    "v_accvgpr_read_b32 v102, a86\n"
    "v_mfma_f32_16x16x32_bf16 a[108:111], v[160:163], v[140:143], a[108:111]\n"
    "v_accvgpr_read_b32 v103, a87\n"
    "v_fma_f32 v100, v100, v224, s94\n"
    "v_mfma_f32_16x16x32_bf16 a[108:111], v[164:167], v[156:159], a[108:111]\n"
    "s_waitcnt lgkmcnt(2)\n"
    "s_cmp_eq_u32 s57, 0\n"
    "s_cselect_b32 s77, s74, 0\n"
    "v_mov_b32 v196, 0x40000000\n"
    "v_mov_b32 v197, 0x800\n"
    "v_cmp_gt_u32 vcc, s77, v231\n"
    "v_mov_b32 v198, 0\n"
    "v_mov_b32 v198, 0\n"
    "v_cndmask_b32 v197, v196, v197, vcc\n"
    "buffer_store_dword v221, v197, s[44:47], 0 offen sc1\n"
    "v_mfma_f32_16x16x32_bf16 a[108:111], v[168:171], v[44:47], a[108:111]\n"
    "ds_read_b128 v[160:163], v201 offset:14336\n"
    "v_fma_f32 v101, v101, v224, s94\n"
    "v_mfma_f32_16x16x32_bf16 a[108:111], v[172:175], v[76:79], a[108:111]\n"
    "v_fma_f32 v102, v102, v224, s94\n"
    "v_fma_f32 v103, v103, v224, s94\n"
    "v_mfma_f32_16x16x32_bf16 a[104:107], v[168:171], v[40:43], a[104:107]\n"
    "v_cvt_pk_bf16_f32 v54, v100, v101\n"
    "v_cvt_pk_bf16_f32 v55, v102, v103\n"
    "v_mfma_f32_16x16x32_bf16 a[104:107], v[172:175], v[72:75], a[104:107]\n"
    "buffer_store_dwordx4 v[52:55], v205, s[28:31], s62 offen offset:128 sc0 sc1\n"
    "v_accvgpr_read_b32 v104, a88\n"
    "v_accvgpr_read_b32 v105, a89\n"
    "v_accvgpr_read_b32 v106, a90\n"
    "v_mfma_f32_16x16x32_bf16 a[100:103], v[168:171], v[36:39], a[100:103]\n"
    "ds_read_b128 v[164:167], v201 offset:15360\n"
    "v_accvgpr_read_b32 v107, a91\n"
    "v_mfma_f32_16x16x32_bf16 a[100:103], v[172:175], v[68:71], a[100:103]\n"
    "v_fma_f32 v104, v104, v226, s94\n"
    "v_fma_f32 v105, v105, v226, s94\n"
    "v_mfma_f32_16x16x32_bf16 a[96:99], v[168:171], v[32:35], a[96:99]\n"
    "v_fma_f32 v106, v106, v226, s94\n"
    "v_fma_f32 v107, v107, v226, s94\n"
    "v_mfma_f32_16x16x32_bf16 a[96:99], v[172:175], v[64:67], a[96:99]\n"
    "s_waitcnt lgkmcnt(2)\n"
    "v_cvt_pk_bf16_f32 v58, v104, v105\n"
    "v_cvt_pk_bf16_f32 v59, v106, v107\n"
    "v_mfma_f32_16x16x32_bf16 a[96:99], v[176:179], v[232:235], a[96:99]\n"
    "ds_read_b128 v[168:171], v201 offset:38912\n"
    "v_mfma_f32_16x16x32_bf16 a[96:99], v[180:183], v[80:83], a[96:99]\n"
    "buffer_store_dwordx4 v[56:59], v205, s[28:31], s63 offen offset:128 sc0 sc1\n"
    "v_accvgpr_read_b32 v108, a92\n"
    "v_accvgpr_read_b32 v109, a93\n"
    "v_accvgpr_read_b32 v110, a94\n"
    "v_mfma_f32_16x16x32_bf16 a[100:103], v[176:179], v[236:239], a[100:103]\n"
    "v_accvgpr_read_b32 v111, a95\n"
    "v_fma_f32 v108, v108, v228, s94\n"
    "v_mfma_f32_16x16x32_bf16 a[100:103], v[180:183], v[84:87], a[100:103]\n"
    "v_fma_f32 v109, v109, v228, s94\n"
    "v_fma_f32 v110, v110, v228, s94\n"
    "v_mfma_f32_16x16x32_bf16 a[104:107], v[176:179], v[240:243], a[104:107]\n"
    "ds_read_b128 v[172:175], v201 offset:39936\n"
    "v_fma_f32 v111, v111, v228, s94\n"
    "v_mfma_f32_16x16x32_bf16 a[104:107], v[180:183], v[88:91], a[104:107]\n"
    "v_cvt_pk_bf16_f32 v62, v108, v109\n"
    "v_cvt_pk_bf16_f32 v63, v110, v111\n"
    "v_mfma_f32_16x16x32_bf16 a[108:111], v[176:179], v[244:247], a[108:111]\n"
    "buffer_store_dwordx4 v[60:63], v205, s[28:31], s64 offen offset:128 sc0 sc1\n"
    "v_mfma_f32_16x16x32_bf16 a[108:111], v[180:183], v[92:95], a[108:111]\n"
    "s_waitcnt lgkmcnt(2)\n"
    "v_accvgpr_read_b32 v96, a96\n"
    "v_accvgpr_read_b32 v97, a97\n"
    "v_mfma_f32_16x16x32_bf16 a[112:115], v[160:163], v[128:131], a[112:115]\n"
    "ds_read_b128 v[176:179], v207 offset:14336\n"
    "v_accvgpr_read_b32 v98, a98\n"
    "v_mfma_f32_16x16x32_bf16 a[112:115], v[164:167], v[144:147], a[112:115]\n"
    "v_accvgpr_read_b32 v99, a99\n"
    "v_fma_f32 v96, v96, v222, s94\n"
    "v_mfma_f32_16x16x32_bf16 a[116:119], v[160:163], v[132:135], a[116:119]\n"
    "v_fma_f32 v97, v97, v222, s94\n"
    "v_fma_f32 v98, v98, v222, s94\n"
    "v_mfma_f32_16x16x32_bf16 a[116:119], v[164:167], v[148:151], a[116:119]\n"
    "v_fma_f32 v99, v99, v222, s94\n"
    "v_cvt_pk_bf16_f32 v184, v96, v97\n"
    "v_mfma_f32_16x16x32_bf16 a[120:123], v[160:163], v[136:139], a[120:123]\n"
    "ds_read_b128 v[180:183], v207 offset:15360\n"
    "v_cvt_pk_bf16_f32 v185, v98, v99\n"
    "v_mfma_f32_16x16x32_bf16 a[120:123], v[164:167], v[152:155], a[120:123]\n"
    "v_accvgpr_read_b32 v100, a100\n"
    "v_accvgpr_read_b32 v101, a101\n"
    "v_mfma_f32_16x16x32_bf16 a[124:127], v[160:163], v[140:143], a[124:127]\n"
    "v_accvgpr_read_b32 v102, a102\n"
    "v_accvgpr_read_b32 v103, a103\n"
    "v_mfma_f32_16x16x32_bf16 a[124:127], v[164:167], v[156:159], a[124:127]\n"
    "s_waitcnt lgkmcnt(2)\n"
    "v_fma_f32 v100, v100, v224, s94\n"
    "v_fma_f32 v101, v101, v224, s94\n"
    "v_mfma_f32_16x16x32_bf16 a[124:127], v[168:171], v[44:47], a[124:127]\n"
    "ds_read_b128 v[160:163], v201 offset:16384\n"
    "v_fma_f32 v102, v102, v224, s94\n"
    "v_mfma_f32_16x16x32_bf16 a[124:127], v[172:175], v[76:79], a[124:127]\n"
    "v_fma_f32 v103, v103, v224, s94\n"
    "v_cvt_pk_bf16_f32 v188, v100, v101\n"
    "v_mfma_f32_16x16x32_bf16 a[120:123], v[168:171], v[40:43], a[120:123]\n"
    "v_cvt_pk_bf16_f32 v189, v102, v103\n"
    "v_accvgpr_read_b32 v104, a104\n"
    "v_mfma_f32_16x16x32_bf16 a[120:123], v[172:175], v[72:75], a[120:123]\n"
    "v_accvgpr_read_b32 v105, a105\n"
    "v_accvgpr_read_b32 v106, a106\n"
    "v_mfma_f32_16x16x32_bf16 a[116:119], v[168:171], v[36:39], a[116:119]\n"
    "ds_read_b128 v[164:167], v201 offset:17408\n"
    "v_accvgpr_read_b32 v107, a107\n"
    "v_mfma_f32_16x16x32_bf16 a[116:119], v[172:175], v[68:71], a[116:119]\n"
    "v_fma_f32 v104, v104, v226, s94\n"
    "v_fma_f32 v105, v105, v226, s94\n"
    "v_mfma_f32_16x16x32_bf16 a[112:115], v[168:171], v[32:35], a[112:115]\n"
    "v_fma_f32 v106, v106, v226, s94\n"
    "v_fma_f32 v107, v107, v226, s94\n"
    "v_mfma_f32_16x16x32_bf16 a[112:115], v[172:175], v[64:67], a[112:115]\n"
    "s_waitcnt lgkmcnt(2)\n"
    "v_cvt_pk_bf16_f32 v248, v104, v105\n"
    "v_cvt_pk_bf16_f32 v249, v106, v107\n"
    "v_mfma_f32_16x16x32_bf16 a[112:115], v[176:179], v[232:235], a[112:115]\n"
    "ds_read_b128 v[168:171], v201 offset:40960\n"
    "v_accvgpr_read_b32 v108, a108\n"
    "v_mfma_f32_16x16x32_bf16 a[112:115], v[180:183], v[80:83], a[112:115]\n"
    "v_accvgpr_read_b32 v109, a109\n"
    "v_accvgpr_read_b32 v110, a110\n"
    "v_mfma_f32_16x16x32_bf16 a[116:119], v[176:179], v[236:239], a[116:119]\n"
    "v_accvgpr_read_b32 v111, a111\n"
    "v_fma_f32 v108, v108, v228, s94\n"
    "v_mfma_f32_16x16x32_bf16 a[116:119], v[180:183], v[84:87], a[116:119]\n"
    "v_fma_f32 v109, v109, v228, s94\n"
    "v_fma_f32 v110, v110, v228, s94\n"
    "v_mfma_f32_16x16x32_bf16 a[120:123], v[176:179], v[240:243], a[120:123]\n"
    "ds_read_b128 v[172:175], v201 offset:41984\n"
    "v_fma_f32 v111, v111, v228, s94\n"
    "v_mfma_f32_16x16x32_bf16 a[120:123], v[180:183], v[88:91], a[120:123]\n"
    "v_cvt_pk_bf16_f32 v252, v108, v109\n"
    "v_cvt_pk_bf16_f32 v253, v110, v111\n"
    "v_mfma_f32_16x16x32_bf16 a[124:127], v[176:179], v[244:247], a[124:127]\n"
    "v_mfma_f32_16x16x32_bf16 a[124:127], v[180:183], v[92:95], a[124:127]\n"
    "s_waitcnt lgkmcnt(2)\n"
    "v_accvgpr_read_b32 v96, a112\n"
    "v_accvgpr_read_b32 v97, a113\n"
    "v_mfma_f32_16x16x32_bf16 a[128:131], v[160:163], v[128:131], a[128:131]\n"
    "ds_read_b128 v[176:179], v207 offset:16384\n"
    "v_accvgpr_read_b32 v98, a114\n"
    "v_mfma_f32_16x16x32_bf16 a[128:131], v[164:167], v[144:147], a[128:131]\n"
    "v_accvgpr_read_b32 v99, a115\n"
    "v_fma_f32 v96, v96, v222, s94\n"
    "v_mfma_f32_16x16x32_bf16 a[132:135], v[160:163], v[132:135], a[132:135]\n"
    "v_fma_f32 v97, v97, v222, s94\n"
    "v_fma_f32 v98, v98, v222, s94\n"
    "v_mfma_f32_16x16x32_bf16 a[132:135], v[164:167], v[148:151], a[132:135]\n"
    "v_fma_f32 v99, v99, v222, s94\n"
    "v_cvt_pk_bf16_f32 v186, v96, v97\n"
    "v_mfma_f32_16x16x32_bf16 a[136:139], v[160:163], v[136:139], a[136:139]\n"
    "ds_read_b128 v[180:183], v207 offset:17408\n"
    "v_cvt_pk_bf16_f32 v187, v98, v99\n"
    "v_mfma_f32_16x16x32_bf16 a[136:139], v[164:167], v[152:155], a[136:139]\n"
    "buffer_store_dwordx4 v[184:187], v205, s[28:31], 0 offen offset:192 sc0 sc1\n"
    "v_accvgpr_read_b32 v100, a116\n"
    "v_accvgpr_read_b32 v101, a117\n"
    "v_accvgpr_read_b32 v102, a118\n"
    "v_mfma_f32_16x16x32_bf16 a[140:143], v[160:163], v[140:143], a[140:143]\n"
    "v_accvgpr_read_b32 v103, a119\n"
    "v_fma_f32 v100, v100, v224, s94\n"
    "v_mfma_f32_16x16x32_bf16 a[140:143], v[164:167], v[156:159], a[140:143]\n"
    "s_waitcnt lgkmcnt(2)\n"
    "v_fma_f32 v101, v101, v224, s94\n"
    "v_fma_f32 v102, v102, v224, s94\n"
    "v_mfma_f32_16x16x32_bf16 a[140:143], v[168:171], v[44:47], a[140:143]\n"
    "ds_read_b128 v[160:163], v201 offset:18432\n"
    "v_fma_f32 v103, v103, v224, s94\n"
    "v_mfma_f32_16x16x32_bf16 a[140:143], v[172:175], v[76:79], a[140:143]\n"
    "v_cvt_pk_bf16_f32 v190, v100, v101\n"
    "v_cvt_pk_bf16_f32 v191, v102, v103\n"
    "v_mfma_f32_16x16x32_bf16 a[136:139], v[168:171], v[40:43], a[136:139]\n"
    "buffer_store_dwordx4 v[188:191], v205, s[28:31], s62 offen offset:192 sc0 sc1\n"
    "v_accvgpr_read_b32 v104, a120\n"
    "v_accvgpr_read_b32 v105, a121\n"
    "v_accvgpr_read_b32 v106, a122\n"
    "v_mfma_f32_16x16x32_bf16 a[136:139], v[172:175], v[72:75], a[136:139]\n"
    "v_accvgpr_read_b32 v107, a123\n"
    "v_fma_f32 v104, v104, v226, s94\n"
    "v_mfma_f32_16x16x32_bf16 a[132:135], v[168:171], v[36:39], a[132:135]\n"
    "ds_read_b128 v[164:167], v201 offset:19456\n"
    "v_fma_f32 v105, v105, v226, s94\n"
    "v_mfma_f32_16x16x32_bf16 a[132:135], v[172:175], v[68:71], a[132:135]\n"
    "v_fma_f32 v106, v106, v226, s94\n"
    "v_fma_f32 v107, v107, v226, s94\n"
    "v_mfma_f32_16x16x32_bf16 a[128:131], v[168:171], v[32:35], a[128:131]\n"
    "v_cvt_pk_bf16_f32 v250, v104, v105\n"
    "v_cvt_pk_bf16_f32 v251, v106, v107\n"
    "v_mfma_f32_16x16x32_bf16 a[128:131], v[172:175], v[64:67], a[128:131]\n"
    "s_waitcnt lgkmcnt(2)\n"
    "buffer_store_dwordx4 v[248:251], v205, s[28:31], s63 offen offset:192 sc0 sc1\n"
    "v_accvgpr_read_b32 v108, a124\n"
    "v_accvgpr_read_b32 v109, a125\n"
    "v_accvgpr_read_b32 v110, a126\n"
    "v_mfma_f32_16x16x32_bf16 a[128:131], v[176:179], v[232:235], a[128:131]\n"
    "ds_read_b128 v[168:171], v201 offset:43008\n"
    "v_accvgpr_read_b32 v111, a127\n"
    "v_mfma_f32_16x16x32_bf16 a[128:131], v[180:183], v[80:83], a[128:131]\n"
    "v_fma_f32 v108, v108, v228, s94\n"
    "v_fma_f32 v109, v109, v228, s94\n"
    "v_mfma_f32_16x16x32_bf16 a[132:135], v[176:179], v[236:239], a[132:135]\n"
    "v_fma_f32 v110, v110, v228, s94\n"
    "v_fma_f32 v111, v111, v228, s94\n"
    "v_mfma_f32_16x16x32_bf16 a[132:135], v[180:183], v[84:87], a[132:135]\n"
    "v_cvt_pk_bf16_f32 v254, v108, v109\n"
    "v_cvt_pk_bf16_f32 v255, v110, v111\n"
    "v_mfma_f32_16x16x32_bf16 a[136:139], v[176:179], v[240:243], a[136:139]\n"
    "ds_read_b128 v[172:175], v201 offset:44032\n"
    "v_mfma_f32_16x16x32_bf16 a[136:139], v[180:183], v[88:91], a[136:139]\n"
    "buffer_store_dwordx4 v[252:255], v205, s[28:31], s64 offen offset:192 sc0 sc1\n"
    "v_mfma_f32_16x16x32_bf16 a[140:143], v[176:179], v[244:247], a[140:143]\n"
    "v_mfma_f32_16x16x32_bf16 a[140:143], v[180:183], v[92:95], a[140:143]\n"
    "s_waitcnt lgkmcnt(2)\n"
    "v_accvgpr_read_b32 v96, a128\n"
    "v_accvgpr_read_b32 v97, a129\n"
    "v_mfma_f32_16x16x32_bf16 a[144:147], v[160:163], v[128:131], a[144:147]\n"
    "ds_read_b128 v[176:179], v207 offset:18432\n"
    "v_accvgpr_read_b32 v98, a130\n"
    "v_mfma_f32_16x16x32_bf16 a[144:147], v[164:167], v[144:147], a[144:147]\n"
    "v_accvgpr_read_b32 v99, a131\n"
    "v_fma_f32 v96, v96, v222, s94\n"
    "v_mfma_f32_16x16x32_bf16 a[148:151], v[160:163], v[132:135], a[148:151]\n"
    "v_fma_f32 v97, v97, v222, s94\n"
    "v_fma_f32 v98, v98, v222, s94\n"
    "v_mfma_f32_16x16x32_bf16 a[148:151], v[164:167], v[148:151], a[148:151]\n"
    "v_fma_f32 v99, v99, v222, s94\n"
    "v_cvt_pk_bf16_f32 v48, v96, v97\n"
    "v_mfma_f32_16x16x32_bf16 a[152:155], v[160:163], v[136:139], a[152:155]\n"
    "ds_read_b128 v[180:183], v207 offset:19456\n"
    "v_cvt_pk_bf16_f32 v49, v98, v99\n"
    "v_mfma_f32_16x16x32_bf16 a[152:155], v[164:167], v[152:155], a[152:155]\n"
    "v_accvgpr_read_b32 v100, a132\n"
    "v_accvgpr_read_b32 v101, a133\n"
    "v_mfma_f32_16x16x32_bf16 a[156:159], v[160:163], v[140:143], a[156:159]\n"
    "v_accvgpr_read_b32 v102, a134\n"
    "v_accvgpr_read_b32 v103, a135\n"
    "v_mfma_f32_16x16x32_bf16 a[156:159], v[164:167], v[156:159], a[156:159]\n"
    "s_waitcnt lgkmcnt(2)\n"
    "v_fma_f32 v100, v100, v224, s94\n"
    "v_fma_f32 v101, v101, v224, s94\n"
    "v_mfma_f32_16x16x32_bf16 a[156:159], v[168:171], v[44:47], a[156:159]\n"
    "ds_read_b128 v[160:163], v201 offset:20480\n"
    "v_fma_f32 v102, v102, v224, s94\n"
    "v_mfma_f32_16x16x32_bf16 a[156:159], v[172:175], v[76:79], a[156:159]\n"
    "v_fma_f32 v103, v103, v224, s94\n"
    "v_cvt_pk_bf16_f32 v52, v100, v101\n"
    "v_mfma_f32_16x16x32_bf16 a[152:155], v[168:171], v[40:43], a[152:155]\n"
    "v_cvt_pk_bf16_f32 v53, v102, v103\n"
    "v_accvgpr_read_b32 v104, a136\n"
    "v_mfma_f32_16x16x32_bf16 a[152:155], v[172:175], v[72:75], a[152:155]\n"
    "v_accvgpr_read_b32 v105, a137\n"
    "v_accvgpr_read_b32 v106, a138\n"
    "v_mfma_f32_16x16x32_bf16 a[148:151], v[168:171], v[36:39], a[148:151]\n"
    "ds_read_b128 v[164:167], v201 offset:21504\n"
    "v_accvgpr_read_b32 v107, a139\n"
    "v_mfma_f32_16x16x32_bf16 a[148:151], v[172:175], v[68:71], a[148:151]\n"
    "v_fma_f32 v104, v104, v226, s94\n"
    "v_fma_f32 v105, v105, v226, s94\n"
    "v_mfma_f32_16x16x32_bf16 a[144:147], v[168:171], v[32:35], a[144:147]\n"
    "v_fma_f32 v106, v106, v226, s94\n"
    "v_fma_f32 v107, v107, v226, s94\n"
    "v_mfma_f32_16x16x32_bf16 a[144:147], v[172:175], v[64:67], a[144:147]\n"
    "s_waitcnt lgkmcnt(2)\n"
    "v_cvt_pk_bf16_f32 v56, v104, v105\n"
    "v_cvt_pk_bf16_f32 v57, v106, v107\n"
    "v_mfma_f32_16x16x32_bf16 a[144:147], v[176:179], v[232:235], a[144:147]\n"
    "ds_read_b128 v[168:171], v201 offset:45056\n"
    "v_accvgpr_read_b32 v108, a140\n"
    "v_mfma_f32_16x16x32_bf16 a[144:147], v[180:183], v[80:83], a[144:147]\n"
    "v_accvgpr_read_b32 v109, a141\n"
    "v_accvgpr_read_b32 v110, a142\n"
    "v_mfma_f32_16x16x32_bf16 a[148:151], v[176:179], v[236:239], a[148:151]\n"
    "v_accvgpr_read_b32 v111, a143\n"
    "v_fma_f32 v108, v108, v228, s94\n"
    "v_mfma_f32_16x16x32_bf16 a[148:151], v[180:183], v[84:87], a[148:151]\n"
    "v_fma_f32 v109, v109, v228, s94\n"
    "v_fma_f32 v110, v110, v228, s94\n"
    "v_mfma_f32_16x16x32_bf16 a[152:155], v[176:179], v[240:243], a[152:155]\n"
    "ds_read_b128 v[172:175], v201 offset:46080\n"
    "v_fma_f32 v111, v111, v228, s94\n"
    "v_mfma_f32_16x16x32_bf16 a[152:155], v[180:183], v[88:91], a[152:155]\n"
    "v_cvt_pk_bf16_f32 v60, v108, v109\n"
    "v_cvt_pk_bf16_f32 v61, v110, v111\n"
    "v_mfma_f32_16x16x32_bf16 a[156:159], v[176:179], v[244:247], a[156:159]\n"
    "v_mfma_f32_16x16x32_bf16 a[156:159], v[180:183], v[92:95], a[156:159]\n"
    "s_waitcnt lgkmcnt(2)\n"
    "v_accvgpr_read_b32 v96, a144\n"
    "v_accvgpr_read_b32 v97, a145\n"
    "v_mfma_f32_16x16x32_bf16 a[160:163], v[160:163], v[128:131], a[160:163]\n"
    "ds_read_b128 v[176:179], v207 offset:20480\n"
    "v_accvgpr_read_b32 v98, a146\n"
    "v_mfma_f32_16x16x32_bf16 a[160:163], v[164:167], v[144:147], a[160:163]\n"
    "v_accvgpr_read_b32 v99, a147\n"
    "v_fma_f32 v96, v96, v222, s94\n"
    "v_mfma_f32_16x16x32_bf16 a[164:167], v[160:163], v[132:135], a[164:167]\n"
    "v_fma_f32 v97, v97, v222, s94\n"
    "v_fma_f32 v98, v98, v222, s94\n"
    "v_mfma_f32_16x16x32_bf16 a[164:167], v[164:167], v[148:151], a[164:167]\n"
    "v_fma_f32 v99, v99, v222, s94\n"
    "v_cvt_pk_bf16_f32 v50, v96, v97\n"
    "v_mfma_f32_16x16x32_bf16 a[168:171], v[160:163], v[136:139], a[168:171]\n"
    "ds_read_b128 v[180:183], v207 offset:21504\n"
    "v_cvt_pk_bf16_f32 v51, v98, v99\n"
    "v_mfma_f32_16x16x32_bf16 a[168:171], v[164:167], v[152:155], a[168:171]\n"
    "buffer_store_dwordx4 v[48:51], v205, s[28:31], 0 offen offset:256 sc0 sc1\n"
    "v_accvgpr_read_b32 v100, a148\n"
    "v_accvgpr_read_b32 v101, a149\n"
    "v_accvgpr_read_b32 v102, a150\n"
    "v_mfma_f32_16x16x32_bf16 a[172:175], v[160:163], v[140:143], a[172:175]\n"
    "v_accvgpr_read_b32 v103, a151\n"
    "v_fma_f32 v100, v100, v224, s94\n"
    "v_mfma_f32_16x16x32_bf16 a[172:175], v[164:167], v[156:159], a[172:175]\n"
    "s_waitcnt lgkmcnt(2)\n"
    "v_fma_f32 v101, v101, v224, s94\n"
    "v_fma_f32 v102, v102, v224, s94\n"
    "v_mfma_f32_16x16x32_bf16 a[172:175], v[168:171], v[44:47], a[172:175]\n"
    "ds_read_b128 v[160:163], v201 offset:22528\n"
    "v_fma_f32 v103, v103, v224, s94\n"
    "v_mfma_f32_16x16x32_bf16 a[172:175], v[172:175], v[76:79], a[172:175]\n"
    "v_cvt_pk_bf16_f32 v54, v100, v101\n"
    "v_cvt_pk_bf16_f32 v55, v102, v103\n"
    "v_mfma_f32_16x16x32_bf16 a[168:171], v[168:171], v[40:43], a[168:171]\n"
    "buffer_store_dwordx4 v[52:55], v205, s[28:31], s62 offen offset:256 sc0 sc1\n"
    "v_accvgpr_read_b32 v104, a152\n"
    "v_accvgpr_read_b32 v105, a153\n"
    "v_accvgpr_read_b32 v106, a154\n"
    "v_mfma_f32_16x16x32_bf16 a[168:171], v[172:175], v[72:75], a[168:171]\n"
    "v_accvgpr_read_b32 v107, a155\n"
    "v_fma_f32 v104, v104, v226, s94\n"
    "v_mfma_f32_16x16x32_bf16 a[164:167], v[168:171], v[36:39], a[164:167]\n"
    "ds_read_b128 v[164:167], v201 offset:23552\n"
    "v_fma_f32 v105, v105, v226, s94\n"
    "v_mfma_f32_16x16x32_bf16 a[164:167], v[172:175], v[68:71], a[164:167]\n"
    "v_fma_f32 v106, v106, v226, s94\n"
    "v_fma_f32 v107, v107, v226, s94\n"
    "v_mfma_f32_16x16x32_bf16 a[160:163], v[168:171], v[32:35], a[160:163]\n"
    "v_cvt_pk_bf16_f32 v58, v104, v105\n"
    "v_cvt_pk_bf16_f32 v59, v106, v107\n"
    "v_mfma_f32_16x16x32_bf16 a[160:163], v[172:175], v[64:67], a[160:163]\n"
    "s_waitcnt lgkmcnt(2)\n"
    "buffer_store_dwordx4 v[56:59], v205, s[28:31], s63 offen offset:256 sc0 sc1\n"
    "v_accvgpr_read_b32 v108, a156\n"
    "v_accvgpr_read_b32 v109, a157\n"
    "v_accvgpr_read_b32 v110, a158\n"
    "v_mfma_f32_16x16x32_bf16 a[160:163], v[176:179], v[232:235], a[160:163]\n"
    "ds_read_b128 v[168:171], v201 offset:47104\n"
    "v_accvgpr_read_b32 v111, a159\n"
    "v_mfma_f32_16x16x32_bf16 a[160:163], v[180:183], v[80:83], a[160:163]\n"
    "v_fma_f32 v108, v108, v228, s94\n"
    "v_fma_f32 v109, v109, v228, s94\n"
    "v_mfma_f32_16x16x32_bf16 a[164:167], v[176:179], v[236:239], a[164:167]\n"
    "v_fma_f32 v110, v110, v228, s94\n"
    "v_fma_f32 v111, v111, v228, s94\n"
    "v_mfma_f32_16x16x32_bf16 a[164:167], v[180:183], v[84:87], a[164:167]\n"
    "v_cvt_pk_bf16_f32 v62, v108, v109\n"
    "v_cvt_pk_bf16_f32 v63, v110, v111\n"
    "v_mfma_f32_16x16x32_bf16 a[168:171], v[176:179], v[240:243], a[168:171]\n"
    "ds_read_b128 v[172:175], v201 offset:48128\n"
    "v_mfma_f32_16x16x32_bf16 a[168:171], v[180:183], v[88:91], a[168:171]\n"
    "buffer_store_dwordx4 v[60:63], v205, s[28:31], s64 offen offset:256 sc0 sc1\n"
    "v_mfma_f32_16x16x32_bf16 a[172:175], v[176:179], v[244:247], a[172:175]\n"
    "v_mfma_f32_16x16x32_bf16 a[172:175], v[180:183], v[92:95], a[172:175]\n"
    "s_waitcnt lgkmcnt(2)\n"
    "v_accvgpr_read_b32 v96, a160\n"
    "v_accvgpr_read_b32 v97, a161\n"
    "v_mfma_f32_16x16x32_bf16 a[176:179], v[160:163], v[128:131], a[176:179]\n"
    "ds_read_b128 v[176:179], v207 offset:22528\n"
    "v_accvgpr_read_b32 v98, a162\n"
    "v_mfma_f32_16x16x32_bf16 a[176:179], v[164:167], v[144:147], a[176:179]\n"
    "v_accvgpr_read_b32 v99, a163\n"
    "v_fma_f32 v96, v96, v222, s94\n"
    "v_mfma_f32_16x16x32_bf16 a[180:183], v[160:163], v[132:135], a[180:183]\n"
    "v_fma_f32 v97, v97, v222, s94\n"
    "v_fma_f32 v98, v98, v222, s94\n"
    "v_mfma_f32_16x16x32_bf16 a[180:183], v[164:167], v[148:151], a[180:183]\n"
    "v_fma_f32 v99, v99, v222, s94\n"
    "v_cvt_pk_bf16_f32 v184, v96, v97\n"
    "v_mfma_f32_16x16x32_bf16 a[184:187], v[160:163], v[136:139], a[184:187]\n"
    "ds_read_b128 v[180:183], v207 offset:23552\n"
    "v_cvt_pk_bf16_f32 v185, v98, v99\n"
    "v_mfma_f32_16x16x32_bf16 a[184:187], v[164:167], v[152:155], a[184:187]\n"
    "v_accvgpr_read_b32 v100, a164\n"
    "v_accvgpr_read_b32 v101, a165\n"
    "v_mfma_f32_16x16x32_bf16 a[188:191], v[160:163], v[140:143], a[188:191]\n"
    "v_accvgpr_read_b32 v102, a166\n"
    "v_accvgpr_read_b32 v103, a167\n"
    "v_mfma_f32_16x16x32_bf16 a[188:191], v[164:167], v[156:159], a[188:191]\n"
    "s_waitcnt lgkmcnt(2)\n"
    "v_fma_f32 v100, v100, v224, s94\n"
    "v_fma_f32 v101, v101, v224, s94\n"
    "v_mfma_f32_16x16x32_bf16 a[188:191], v[168:171], v[44:47], a[188:191]\n"
    "v_fma_f32 v102, v102, v224, s94\n"
    "v_fma_f32 v103, v103, v224, s94\n"
    "v_mfma_f32_16x16x32_bf16 a[188:191], v[172:175], v[76:79], a[188:191]\n"
    "v_cvt_pk_bf16_f32 v188, v100, v101\n"
    "v_cvt_pk_bf16_f32 v189, v102, v103\n"
    "v_mfma_f32_16x16x32_bf16 a[184:187], v[168:171], v[40:43], a[184:187]\n"
    "v_accvgpr_read_b32 v104, a168\n"
    "v_accvgpr_read_b32 v105, a169\n"
    "v_mfma_f32_16x16x32_bf16 a[184:187], v[172:175], v[72:75], a[184:187]\n"
    "v_accvgpr_read_b32 v106, a170\n"
    "v_accvgpr_read_b32 v107, a171\n"
    "v_mfma_f32_16x16x32_bf16 a[180:183], v[168:171], v[36:39], a[180:183]\n"
    "v_fma_f32 v104, v104, v226, s94\n"
    "v_fma_f32 v105, v105, v226, s94\n"
    "v_mfma_f32_16x16x32_bf16 a[180:183], v[172:175], v[68:71], a[180:183]\n"
    "v_fma_f32 v106, v106, v226, s94\n"
    "v_fma_f32 v107, v107, v226, s94\n"
    "v_mfma_f32_16x16x32_bf16 a[176:179], v[168:171], v[32:35], a[176:179]\n"
    "v_cvt_pk_bf16_f32 v248, v104, v105\n"
    "v_cvt_pk_bf16_f32 v249, v106, v107\n"
    "v_mfma_f32_16x16x32_bf16 a[176:179], v[172:175], v[64:67], a[176:179]\n"
    "s_waitcnt lgkmcnt(0)\n"
    "v_accvgpr_read_b32 v108, a172\n"
    "v_accvgpr_read_b32 v109, a173\n"
    "v_mfma_f32_16x16x32_bf16 a[176:179], v[176:179], v[232:235], a[176:179]\n"
    "v_accvgpr_read_b32 v110, a174\n"
    "v_accvgpr_read_b32 v111, a175\n"
    "v_mfma_f32_16x16x32_bf16 a[176:179], v[180:183], v[80:83], a[176:179]\n"
    "v_fma_f32 v108, v108, v228, s94\n"
    "v_fma_f32 v109, v109, v228, s94\n"
    "v_mfma_f32_16x16x32_bf16 a[180:183], v[176:179], v[236:239], a[180:183]\n"
    "v_fma_f32 v110, v110, v228, s94\n"
    "v_fma_f32 v111, v111, v228, s94\n"
    "v_mfma_f32_16x16x32_bf16 a[180:183], v[180:183], v[84:87], a[180:183]\n"
    "v_cvt_pk_bf16_f32 v252, v108, v109\n"
    "v_cvt_pk_bf16_f32 v253, v110, v111\n"
    "v_mfma_f32_16x16x32_bf16 a[184:187], v[176:179], v[240:243], a[184:187]\n"
    "v_mfma_f32_16x16x32_bf16 a[184:187], v[180:183], v[88:91], a[184:187]\n"
    "v_mfma_f32_16x16x32_bf16 a[188:191], v[176:179], v[244:247], a[188:191]\n"
    "v_mfma_f32_16x16x32_bf16 a[188:191], v[180:183], v[92:95], a[188:191]\n"
    "v_accvgpr_read_b32 v96, a176\n"
    "v_accvgpr_read_b32 v97, a177\n"
    "v_accvgpr_read_b32 v98, a178\n"
    "v_accvgpr_read_b32 v99, a179\n"
    "v_fma_f32 v96, v96, v222, s94\n"
    "v_fma_f32 v97, v97, v222, s94\n"
    "v_fma_f32 v98, v98, v222, s94\n"
    "v_fma_f32 v99, v99, v222, s94\n"
    "v_cvt_pk_bf16_f32 v186, v96, v97\n"
    "v_cvt_pk_bf16_f32 v187, v98, v99\n"
    "buffer_store_dwordx4 v[184:187], v205, s[28:31], 0 offen offset:320 sc0 sc1\n"
    "v_accvgpr_read_b32 v100, a180\n"
    "v_accvgpr_read_b32 v101, a181\n"
    "v_accvgpr_read_b32 v102, a182\n"
    "v_accvgpr_read_b32 v103, a183\n"
    "v_fma_f32 v100, v100, v224, s94\n"
    "v_fma_f32 v101, v101, v224, s94\n"
    "v_fma_f32 v102, v102, v224, s94\n"
    "v_fma_f32 v103, v103, v224, s94\n"
    "v_cvt_pk_bf16_f32 v190, v100, v101\n"
    "v_cvt_pk_bf16_f32 v191, v102, v103\n"
    "buffer_store_dwordx4 v[188:191], v205, s[28:31], s62 offen offset:320 sc0 sc1\n"
    "v_accvgpr_read_b32 v104, a184\n"
    "v_accvgpr_read_b32 v105, a185\n"
    "v_accvgpr_read_b32 v106, a186\n"
    "v_accvgpr_read_b32 v107, a187\n"
    "v_fma_f32 v104, v104, v226, s94\n"
    "v_fma_f32 v105, v105, v226, s94\n"
    "v_fma_f32 v106, v106, v226, s94\n"
    "v_fma_f32 v107, v107, v226, s94\n"
    "v_cvt_pk_bf16_f32 v250, v104, v105\n"
    "v_cvt_pk_bf16_f32 v251, v106, v107\n"
    "buffer_store_dwordx4 v[248:251], v205, s[28:31], s63 offen offset:320 sc0 sc1\n"
    "v_accvgpr_read_b32 v108, a188\n"
    "v_accvgpr_read_b32 v109, a189\n"
    "v_accvgpr_read_b32 v110, a190\n"
    "v_accvgpr_read_b32 v111, a191\n"
    "v_fma_f32 v108, v108, v228, s94\n"
    "v_fma_f32 v109, v109, v228, s94\n"
    "v_fma_f32 v110, v110, v228, s94\n"
    "v_fma_f32 v111, v111, v228, s94\n"
    "v_cvt_pk_bf16_f32 v254, v108, v109\n"
    "v_cvt_pk_bf16_f32 v255, v110, v111\n"
    "buffer_store_dwordx4 v[252:255], v205, s[28:31], s64 offen offset:320 sc0 sc1\n"
    "v_add_f32 v196, v112, v116\n"
    "v_add_f32 v197, v120, v124\n"
    "v_add_f32 v196, v196, v197\n"
    "v_cmp_ngt_f32 vcc, 0x45800000, v196\n"
    "s_barrier\n"
    "s_cbranch_vccnz LSLOW\n"
    "LFIN:\n"
    "s_branch LEND\n"
    "LSPRE0:\n"
    "LSPIN0:\n"
    "s_sleep 1\n"
    "buffer_load_dwordx2 v[218:219], v213, s[44:47], 0 offen sc1\n"
    "s_waitcnt vmcnt(0)\n"
    "s_add_u32 s73, s73, 1\n"
    "s_cmp_gt_u32 s73, 200000\n"
    "s_cbranch_scc1 LSPB0\n"
    "v_cmp_ne_u32 s[80:81], s67, v219\n"
    "v_cmp_gt_u32 vcc, s66, v218\n"
    "s_or_b64 vcc, vcc, s[80:81]\n"
    "s_cbranch_vccnz LSPIN0\n"
    "s_branch LSPB0\n"
    "LSPRE1:\n"
    "LSPIN1:\n"
    "s_sleep 1\n"
    "buffer_load_dwordx2 v[218:219], v213, s[44:47], 0 offen sc1\n"
    "s_waitcnt vmcnt(0)\n"
    "s_add_u32 s73, s73, 1\n"
    "s_cmp_gt_u32 s73, 200000\n"
    "s_cbranch_scc1 LSPB1\n"
    "v_cmp_ne_u32 s[80:81], s67, v219\n"
    "v_cmp_gt_u32 vcc, s66, v218\n"
    "s_or_b64 vcc, vcc, s[80:81]\n"
    "s_cbranch_vccnz LSPIN1\n"
    "s_branch LSPB1\n"
    "LSPRE2:\n"
    "LSPIN2:\n"
    "s_sleep 1\n"
    "buffer_load_dwordx2 v[218:219], v213, s[44:47], 0 offen sc1\n"
    "s_waitcnt vmcnt(0)\n"
    "s_add_u32 s73, s73, 1\n"
    "s_cmp_gt_u32 s73, 200000\n"
    "s_cbranch_scc1 LSPB2\n"
    "v_cmp_ne_u32 s[80:81], s67, v219\n"
    "v_cmp_gt_u32 vcc, s66, v218\n"
    "s_or_b64 vcc, vcc, s[80:81]\n"
    "s_cbranch_vccnz LSPIN2\n"
    "s_branch LSPB2\n"
    "LSPRE3:\n"
    "LSPIN3:\n"
    "s_sleep 1\n"
    "buffer_load_dwordx2 v[218:219], v213, s[44:47], 0 offen sc1\n"
    "s_waitcnt vmcnt(0)\n"
    "s_add_u32 s73, s73, 1\n"
    "s_cmp_gt_u32 s73, 200000\n"
    "s_cbranch_scc1 LSPB3\n"
    "v_cmp_ne_u32 s[80:81], s67, v219\n"
    "v_cmp_gt_u32 vcc, s66, v218\n"
    "s_or_b64 vcc, vcc, s[80:81]\n"
    "s_cbranch_vccnz LSPIN3\n"
    "s_branch LSPB3\n"
    "LSPRE4:\n"
    "LSPIN4:\n"
    "s_sleep 1\n"
    "buffer_load_dwordx2 v[218:219], v213, s[44:47], 0 offen sc1\n"
    "s_waitcnt vmcnt(0)\n"
    "s_add_u32 s73, s73, 1\n"
    "s_cmp_gt_u32 s73, 200000\n"
    "s_cbranch_scc1 LSPB4\n"
    "v_cmp_ne_u32 s[80:81], s67, v219\n"
    "v_cmp_gt_u32 vcc, s66, v218\n"
    "s_or_b64 vcc, vcc, s[80:81]\n"
    "s_cbranch_vccnz LSPIN4\n"
    "s_branch LSPB4\n"
    "LSPRE5:\n"
    "LSPIN5:\n"
    "s_sleep 1\n"
    "buffer_load_dwordx2 v[218:219], v213, s[44:47], 0 offen sc1\n"
    "s_waitcnt vmcnt(0)\n"
    "s_add_u32 s73, s73, 1\n"
    "s_cmp_gt_u32 s73, 200000\n"
    "s_cbranch_scc1 LSPB5\n"
    "v_cmp_ne_u32 s[80:81], s67, v219\n"
    "v_cmp_gt_u32 vcc, s66, v218\n"
    "s_or_b64 vcc, vcc, s[80:81]\n"
    "s_cbranch_vccnz LSPIN5\n"
    "s_branch LSPB5\n"
    "LSLOW:\n"
    "s_waitcnt vmcnt(0)\n"
    "s_nop 15\n"
    "s_mul_i32 s62, s58, 0x9000\n"
    "v_add_u32 v205, s62, v200\n"
    "s_mov_b32 s63, 0\n"
    "v_mov_b32 v128, 0\n"
    "v_mov_b32 v129, 0\n"
    "v_mov_b32 v130, 0\n"
    "v_mov_b32 v131, 0\n"
    "v_mov_b32 v96, 0xff61b1e6\n"
    "v_mov_b32 v100, 0xff61b1e6\n"
    "v_mov_b32 v104, 0xff61b1e6\n"
    "v_mov_b32 v108, 0xff61b1e6\n"
    "v_mfma_f32_16x16x32_bf16 a[0:3], v[128:131], v[128:131], 0\n"
    "v_mfma_f32_16x16x32_bf16 a[4:7], v[128:131], v[128:131], 0\n"
    "v_mfma_f32_16x16x32_bf16 a[8:11], v[128:131], v[128:131], 0\n"
    "v_mfma_f32_16x16x32_bf16 a[12:15], v[128:131], v[128:131], 0\n"
    "v_mfma_f32_16x16x32_bf16 a[16:19], v[128:131], v[128:131], 0\n"
    "v_mfma_f32_16x16x32_bf16 a[20:23], v[128:131], v[128:131], 0\n"
    "v_mfma_f32_16x16x32_bf16 a[24:27], v[128:131], v[128:131], 0\n"
    "v_mfma_f32_16x16x32_bf16 a[28:31], v[128:131], v[128:131], 0\n"
    "v_mfma_f32_16x16x32_bf16 a[32:35], v[128:131], v[128:131], 0\n"
    "v_mfma_f32_16x16x32_bf16 a[36:39], v[128:131], v[128:131], 0\n"
    "v_mfma_f32_16x16x32_bf16 a[40:43], v[128:131], v[128:131], 0\n"
    "v_mfma_f32_16x16x32_bf16 a[44:47], v[128:131], v[128:131], 0\n"
    "v_mfma_f32_16x16x32_bf16 a[48:51], v[128:131], v[128:131], 0\n"
    "v_mfma_f32_16x16x32_bf16 a[52:55], v[128:131], v[128:131], 0\n"
    "v_mfma_f32_16x16x32_bf16 a[56:59], v[128:131], v[128:131], 0\n"
    "v_mfma_f32_16x16x32_bf16 a[60:63], v[128:131], v[128:131], 0\n"
    "v_mfma_f32_16x16x32_bf16 a[64:67], v[128:131], v[128:131], 0\n"
    "v_mfma_f32_16x16x32_bf16 a[68:71], v[128:131], v[128:131], 0\n"
    "v_mfma_f32_16x16x32_bf16 a[72:75], v[128:131], v[128:131], 0\n"
    "v_mfma_f32_16x16x32_bf16 a[76:79], v[128:131], v[128:131], 0\n"
    "v_mfma_f32_16x16x32_bf16 a[80:83], v[128:131], v[128:131], 0\n"
    "v_mfma_f32_16x16x32_bf16 a[84:87], v[128:131], v[128:131], 0\n"
    "v_mfma_f32_16x16x32_bf16 a[88:91], v[128:131], v[128:131], 0\n"
    "v_mfma_f32_16x16x32_bf16 a[92:95], v[128:131], v[128:131], 0\n"
    "v_mfma_f32_16x16x32_bf16 a[96:99], v[128:131], v[128:131], 0\n"
    "v_mfma_f32_16x16x32_bf16 a[100:103], v[128:131], v[128:131], 0\n"
    "v_mfma_f32_16x16x32_bf16 a[104:107], v[128:131], v[128:131], 0\n"
    "v_mfma_f32_16x16x32_bf16 a[108:111], v[128:131], v[128:131], 0\n"
    "v_mfma_f32_16x16x32_bf16 a[112:115], v[128:131], v[128:131], 0\n"
    "v_mfma_f32_16x16x32_bf16 a[116:119], v[128:131], v[128:131], 0\n"
    "v_mfma_f32_16x16x32_bf16 a[120:123], v[128:131], v[128:131], 0\n"
    "v_mfma_f32_16x16x32_bf16 a[124:127], v[128:131], v[128:131], 0\n"
    "v_mfma_f32_16x16x32_bf16 a[128:131], v[128:131], v[128:131], 0\n"
    "v_mfma_f32_16x16x32_bf16 a[132:135], v[128:131], v[128:131], 0\n"
    "v_mfma_f32_16x16x32_bf16 a[136:139], v[128:131], v[128:131], 0\n"
    "v_mfma_f32_16x16x32_bf16 a[140:143], v[128:131], v[128:131], 0\n"
    "v_mfma_f32_16x16x32_bf16 a[144:147], v[128:131], v[128:131], 0\n"
    "v_mfma_f32_16x16x32_bf16 a[148:151], v[128:131], v[128:131], 0\n"
    "v_mfma_f32_16x16x32_bf16 a[152:155], v[128:131], v[128:131], 0\n"
    "v_mfma_f32_16x16x32_bf16 a[156:159], v[128:131], v[128:131], 0\n"
    "v_mfma_f32_16x16x32_bf16 a[160:163], v[128:131], v[128:131], 0\n"
    "v_mfma_f32_16x16x32_bf16 a[164:167], v[128:131], v[128:131], 0\n"
    "v_mfma_f32_16x16x32_bf16 a[168:171], v[128:131], v[128:131], 0\n"
    "v_mfma_f32_16x16x32_bf16 a[172:175], v[128:131], v[128:131], 0\n"
    "v_mfma_f32_16x16x32_bf16 a[176:179], v[128:131], v[128:131], 0\n"
    "v_mfma_f32_16x16x32_bf16 a[180:183], v[128:131], v[128:131], 0\n"
    "v_mfma_f32_16x16x32_bf16 a[184:187], v[128:131], v[128:131], 0\n"
    "v_mfma_f32_16x16x32_bf16 a[188:191], v[128:131], v[128:131], 0\n"
    "v_mfma_f32_16x16x32_bf16 v[112:115], v[128:131], v[128:131], 0\n"
    "v_mfma_f32_16x16x32_bf16 v[116:119], v[128:131], v[128:131], 0\n"
    "v_mfma_f32_16x16x32_bf16 v[120:123], v[128:131], v[128:131], 0\n"
    "v_mfma_f32_16x16x32_bf16 v[124:127], v[128:131], v[128:131], 0\n"
    "s_nop 15\n"
    "LSLOOP:\n"
    "s_add_u32 s78, s63, 0\n"
    "s_add_u32 m0, s62, 0\n"
    "s_nop 0\n"
    "buffer_load_dwordx4 v200, s[36:39], s78 offen sc1 lds\n"
    "buffer_load_dwordx4 v200, s[36:39], s78 offen offset:1024 sc1 lds\n"
    "buffer_load_dwordx4 v200, s[36:39], s78 offen offset:2048 sc1 lds\n"
    "buffer_load_dwordx4 v200, s[36:39], s78 offen offset:3072 sc1 lds\n"
    "s_add_u32 s78, s63, 0x1000\n"
    "s_add_u32 m0, s62, 0x1000\n"
    "s_nop 0\n"
    "buffer_load_dwordx4 v200, s[36:39], s78 offen sc1 lds\n"
    "buffer_load_dwordx4 v200, s[36:39], s78 offen offset:1024 sc1 lds\n"
    "buffer_load_dwordx4 v200, s[36:39], s78 offen offset:2048 sc1 lds\n"
    "buffer_load_dwordx4 v200, s[36:39], s78 offen offset:3072 sc1 lds\n"
    "s_add_u32 s78, s63, 0x2000\n"
    "s_add_u32 m0, s62, 0x2000\n"
    "s_nop 0\n"
    "buffer_load_dwordx4 v200, s[36:39], s78 offen sc1 lds\n"
    "buffer_load_dwordx4 v200, s[36:39], s78 offen offset:1024 sc1 lds\n"
    "buffer_load_dwordx4 v200, s[36:39], s78 offen offset:2048 sc1 lds\n"
    "buffer_load_dwordx4 v200, s[36:39], s78 offen offset:3072 sc1 lds\n"
    "s_add_u32 s78, s63, 0\n"
    "s_add_u32 m0, s62, 0x3000\n"
    "s_nop 0\n"
    "buffer_load_dwordx4 v200, s[40:43], s78 offen sc1 lds\n"
    "buffer_load_dwordx4 v200, s[40:43], s78 offen offset:1024 sc1 lds\n"
    "buffer_load_dwordx4 v200, s[40:43], s78 offen offset:2048 sc1 lds\n"
    "buffer_load_dwordx4 v200, s[40:43], s78 offen offset:3072 sc1 lds\n"
    "s_add_u32 s78, s63, 0x1000\n"
    "s_add_u32 m0, s62, 0x4000\n"
    "s_nop 0\n"
    "buffer_load_dwordx4 v200, s[40:43], s78 offen sc1 lds\n"
    "buffer_load_dwordx4 v200, s[40:43], s78 offen offset:1024 sc1 lds\n"
    "buffer_load_dwordx4 v200, s[40:43], s78 offen offset:2048 sc1 lds\n"
    "buffer_load_dwordx4 v200, s[40:43], s78 offen offset:3072 sc1 lds\n"
    "s_add_u32 s78, s63, 0x2000\n"
    "s_add_u32 m0, s62, 0x5000\n"
    "s_nop 0\n"
    "buffer_load_dwordx4 v200, s[40:43], s78 offen sc1 lds\n"
    "buffer_load_dwordx4 v200, s[40:43], s78 offen offset:1024 sc1 lds\n"
    "buffer_load_dwordx4 v200, s[40:43], s78 offen offset:2048 sc1 lds\n"
    "buffer_load_dwordx4 v200, s[40:43], s78 offen offset:3072 sc1 lds\n"
    "s_add_u32 s78, s63, 0x3000\n"
    "s_add_u32 m0, s62, 0x6000\n"
    "s_nop 0\n"
    "buffer_load_dwordx4 v200, s[40:43], s78 offen sc1 lds\n"
    "buffer_load_dwordx4 v200, s[40:43], s78 offen offset:1024 sc1 lds\n"
    "buffer_load_dwordx4 v200, s[40:43], s78 offen offset:2048 sc1 lds\n"
    "buffer_load_dwordx4 v200, s[40:43], s78 offen offset:3072 sc1 lds\n"
    "s_add_u32 s78, s63, 0x4000\n"
    "s_add_u32 m0, s62, 0x7000\n"
    "s_nop 0\n"
    "buffer_load_dwordx4 v200, s[40:43], s78 offen sc1 lds\n"
    "buffer_load_dwordx4 v200, s[40:43], s78 offen offset:1024 sc1 lds\n"
    "buffer_load_dwordx4 v200, s[40:43], s78 offen offset:2048 sc1 lds\n"
    "buffer_load_dwordx4 v200, s[40:43], s78 offen offset:3072 sc1 lds\n"
    "s_add_u32 s78, s63, 0x5000\n"
    "s_add_u32 m0, s62, 0x8000\n"
    "s_nop 0\n"
    "buffer_load_dwordx4 v200, s[40:43], s78 offen sc1 lds\n"
    "buffer_load_dwordx4 v200, s[40:43], s78 offen offset:1024 sc1 lds\n"
    "buffer_load_dwordx4 v200, s[40:43], s78 offen offset:2048 sc1 lds\n"
    "buffer_load_dwordx4 v200, s[40:43], s78 offen offset:3072 sc1 lds\n"
    "s_waitcnt vmcnt(0)\n"
    "ds_read_b128 v[160:163], v205\n"
    "ds_read_b128 v[164:167], v205 offset:1024\n"
    "s_waitcnt lgkmcnt(1)\n"
    "v_mfma_f32_16x16x32_bf16 v[32:35], v[160:163], a[192:195], 0\n"
    "s_waitcnt lgkmcnt(0)\n"
    "v_mfma_f32_16x16x32_bf16 v[32:35], v[164:167], a[196:199], v[32:35]\n"
    "v_mfma_f32_16x16x32_bf16 v[36:39], v[164:167], a[220:223], 0\n"
    "v_mfma_f32_16x16x32_bf16 v[36:39], v[160:163], a[216:219], v[36:39]\n"
    "v_mfma_f32_16x16x32_bf16 v[40:43], v[160:163], a[240:243], 0\n"
    "v_mfma_f32_16x16x32_bf16 v[40:43], v[164:167], a[244:247], v[40:43]\n"
    "v_mfma_f32_16x16x32_bf16 v[44:47], v[164:167], v[12:15], 0\n"
    "v_mfma_f32_16x16x32_bf16 v[44:47], v[160:163], v[8:11], v[44:47]\n"
    "ds_read_b128 v[168:171], v205 offset:2048\n"
    "ds_read_b128 v[172:175], v205 offset:3072\n"
    "s_waitcnt lgkmcnt(1)\n"
    "v_mfma_f32_16x16x32_bf16 v[44:47], v[168:171], v[16:19], v[44:47]\n"
    "s_waitcnt lgkmcnt(0)\n"
    "v_mfma_f32_16x16x32_bf16 v[44:47], v[172:175], v[20:23], v[44:47]\n"
    "v_mfma_f32_16x16x32_bf16 v[40:43], v[172:175], a[252:255], v[40:43]\n"
    "v_mfma_f32_16x16x32_bf16 v[40:43], v[168:171], a[248:251], v[40:43]\n"
    "v_mfma_f32_16x16x32_bf16 v[36:39], v[168:171], a[224:227], v[36:39]\n"
    "v_mfma_f32_16x16x32_bf16 v[36:39], v[172:175], a[228:231], v[36:39]\n"
    "v_mfma_f32_16x16x32_bf16 v[32:35], v[172:175], a[204:207], v[32:35]\n"
    "v_mfma_f32_16x16x32_bf16 v[32:35], v[168:171], a[200:203], v[32:35]\n"
    "ds_read_b128 v[176:179], v205 offset:4096\n"
    "ds_read_b128 v[180:183], v205 offset:5120\n"
    "s_waitcnt lgkmcnt(1)\n"
    "v_mfma_f32_16x16x32_bf16 v[32:35], v[176:179], a[208:211], v[32:35]\n"
    "s_waitcnt lgkmcnt(0)\n"
    "v_mfma_f32_16x16x32_bf16 v[32:35], v[180:183], a[212:215], v[32:35]\n"
    "v_mfma_f32_16x16x32_bf16 v[36:39], v[180:183], a[236:239], v[36:39]\n"
    "v_mfma_f32_16x16x32_bf16 v[36:39], v[176:179], a[232:235], v[36:39]\n"
    "v_mfma_f32_16x16x32_bf16 v[40:43], v[176:179], v[0:3], v[40:43]\n"
    "v_mfma_f32_16x16x32_bf16 v[40:43], v[180:183], v[4:7], v[40:43]\n"
    "v_mfma_f32_16x16x32_bf16 v[44:47], v[180:183], v[28:31], v[44:47]\n"
    "v_mfma_f32_16x16x32_bf16 v[44:47], v[176:179], v[24:27], v[44:47]\n"
    "ds_read_b128 v[160:163], v205 offset:10240\n"
    "ds_read_b128 v[164:167], v205 offset:11264\n"
    "s_waitcnt lgkmcnt(1)\n"
    "v_mfma_f32_16x16x32_bf16 v[60:63], v[160:163], v[24:27], 0\n"
    "s_waitcnt lgkmcnt(0)\n"
    "v_mfma_f32_16x16x32_bf16 v[60:63], v[164:167], v[28:31], v[60:63]\n"
    "v_mfma_f32_16x16x32_bf16 v[56:59], v[164:167], v[4:7], 0\n"
    "v_mfma_f32_16x16x32_bf16 v[56:59], v[160:163], v[0:3], v[56:59]\n"
    "v_mfma_f32_16x16x32_bf16 v[52:55], v[160:163], a[232:235], 0\n"
    "v_mfma_f32_16x16x32_bf16 v[52:55], v[164:167], a[236:239], v[52:55]\n"
    "v_mfma_f32_16x16x32_bf16 v[48:51], v[164:167], a[212:215], 0\n"
    "v_mfma_f32_16x16x32_bf16 v[48:51], v[160:163], a[208:211], v[48:51]\n"
    "ds_read_b128 v[168:171], v205 offset:8192\n"
    "ds_read_b128 v[172:175], v205 offset:9216\n"
    "s_waitcnt lgkmcnt(1)\n"
    "v_mfma_f32_16x16x32_bf16 v[48:51], v[168:171], a[200:203], v[48:51]\n"
    "s_waitcnt lgkmcnt(0)\n"
    "v_mfma_f32_16x16x32_bf16 v[48:51], v[172:175], a[204:207], v[48:51]\n"
    "v_mfma_f32_16x16x32_bf16 v[52:55], v[172:175], a[228:231], v[52:55]\n"
    "v_mfma_f32_16x16x32_bf16 v[52:55], v[168:171], a[224:227], v[52:55]\n"
    "v_mfma_f32_16x16x32_bf16 v[56:59], v[168:171], a[248:251], v[56:59]\n"
    "v_mfma_f32_16x16x32_bf16 v[56:59], v[172:175], a[252:255], v[56:59]\n"
    "v_mfma_f32_16x16x32_bf16 v[60:63], v[172:175], v[20:23], v[60:63]\n"
    "v_mfma_f32_16x16x32_bf16 v[60:63], v[168:171], v[16:19], v[60:63]\n"
    "ds_read_b128 v[176:179], v205 offset:6144\n"
    "ds_read_b128 v[180:183], v205 offset:7168\n"
    "s_waitcnt lgkmcnt(1)\n"
    "v_mfma_f32_16x16x32_bf16 v[60:63], v[176:179], v[8:11], v[60:63]\n"
    "s_waitcnt lgkmcnt(0)\n"
    "v_mfma_f32_16x16x32_bf16 v[60:63], v[180:183], v[12:15], v[60:63]\n"
    "v_mfma_f32_16x16x32_bf16 v[56:59], v[180:183], a[244:247], v[56:59]\n"
    "v_mfma_f32_16x16x32_bf16 v[56:59], v[176:179], a[240:243], v[56:59]\n"
    "v_mfma_f32_16x16x32_bf16 v[52:55], v[176:179], a[216:219], v[52:55]\n"
    "v_mfma_f32_16x16x32_bf16 v[52:55], v[180:183], a[220:223], v[52:55]\n"
    "v_mfma_f32_16x16x32_bf16 v[48:51], v[180:183], a[196:199], v[48:51]\n"
    "v_mfma_f32_16x16x32_bf16 v[48:51], v[176:179], a[192:195], v[48:51]\n"
    "s_add_u32 s78, s63, 0x3000\n"
    "s_add_u32 m0, s62, 0\n"
    "s_nop 0\n"
    "buffer_load_dwordx4 v200, s[36:39], s78 offen sc1 lds\n"
    "buffer_load_dwordx4 v200, s[36:39], s78 offen offset:1024 sc1 lds\n"
    "buffer_load_dwordx4 v200, s[36:39], s78 offen offset:2048 sc1 lds\n"
    "buffer_load_dwordx4 v200, s[36:39], s78 offen offset:3072 sc1 lds\n"
    "s_add_u32 s78, s63, 0x4000\n"
    "s_add_u32 m0, s62, 0x1000\n"
    "s_nop 0\n"
    "buffer_load_dwordx4 v200, s[36:39], s78 offen sc1 lds\n"
    "buffer_load_dwordx4 v200, s[36:39], s78 offen offset:1024 sc1 lds\n"
    "buffer_load_dwordx4 v200, s[36:39], s78 offen offset:2048 sc1 lds\n"
    "buffer_load_dwordx4 v200, s[36:39], s78 offen offset:3072 sc1 lds\n"
    "s_add_u32 s78, s63, 0x5000\n"
    "s_add_u32 m0, s62, 0x2000\n"
    "s_nop 0\n"
    "buffer_load_dwordx4 v200, s[36:39], s78 offen sc1 lds\n"
    "buffer_load_dwordx4 v200, s[36:39], s78 offen offset:1024 sc1 lds\n"
    "buffer_load_dwordx4 v200, s[36:39], s78 offen offset:2048 sc1 lds\n"
    "buffer_load_dwordx4 v200, s[36:39], s78 offen offset:3072 sc1 lds\n"
    "s_waitcnt vmcnt(0)\n"
    "ds_read_b128 v[160:163], v205\n"
    "ds_read_b128 v[164:167], v205 offset:1024\n"
    "s_waitcnt lgkmcnt(1)\n"
    "v_mfma_f32_16x16x32_bf16 v[64:67], v[160:163], a[192:195], 0\n"
    "s_waitcnt lgkmcnt(0)\n"
    "v_mfma_f32_16x16x32_bf16 v[64:67], v[164:167], a[196:199], v[64:67]\n"
    "v_mfma_f32_16x16x32_bf16 v[68:71], v[164:167], a[220:223], 0\n"
    "v_mfma_f32_16x16x32_bf16 v[68:71], v[160:163], a[216:219], v[68:71]\n"
    "v_mfma_f32_16x16x32_bf16 v[72:75], v[160:163], a[240:243], 0\n"
    "v_mfma_f32_16x16x32_bf16 v[72:75], v[164:167], a[244:247], v[72:75]\n"
    "v_mfma_f32_16x16x32_bf16 v[76:79], v[164:167], v[12:15], 0\n"
    "v_mfma_f32_16x16x32_bf16 v[76:79], v[160:163], v[8:11], v[76:79]\n"
    "ds_read_b128 v[168:171], v205 offset:2048\n"
    "ds_read_b128 v[172:175], v205 offset:3072\n"
    "s_waitcnt lgkmcnt(1)\n"
    "v_mfma_f32_16x16x32_bf16 v[76:79], v[168:171], v[16:19], v[76:79]\n"
    "s_waitcnt lgkmcnt(0)\n"
    "v_mfma_f32_16x16x32_bf16 v[76:79], v[172:175], v[20:23], v[76:79]\n"
    "v_mfma_f32_16x16x32_bf16 v[72:75], v[172:175], a[252:255], v[72:75]\n"
    "v_mfma_f32_16x16x32_bf16 v[72:75], v[168:171], a[248:251], v[72:75]\n"
    "v_mfma_f32_16x16x32_bf16 v[68:71], v[168:171], a[224:227], v[68:71]\n"
    "v_mfma_f32_16x16x32_bf16 v[68:71], v[172:175], a[228:231], v[68:71]\n"
    "v_mfma_f32_16x16x32_bf16 v[64:67], v[172:175], a[204:207], v[64:67]\n"
    "v_mfma_f32_16x16x32_bf16 v[64:67], v[168:171], a[200:203], v[64:67]\n"
    "ds_read_b128 v[176:179], v205 offset:4096\n"
    "ds_read_b128 v[180:183], v205 offset:5120\n"
    "s_waitcnt lgkmcnt(1)\n"
    "v_mfma_f32_16x16x32_bf16 v[64:67], v[176:179], a[208:211], v[64:67]\n"
    "s_waitcnt lgkmcnt(0)\n"
    "v_mfma_f32_16x16x32_bf16 v[64:67], v[180:183], a[212:215], v[64:67]\n"
    "v_mfma_f32_16x16x32_bf16 v[68:71], v[180:183], a[236:239], v[68:71]\n"
    "v_mfma_f32_16x16x32_bf16 v[68:71], v[176:179], a[232:235], v[68:71]\n"
    "v_mfma_f32_16x16x32_bf16 v[72:75], v[176:179], v[0:3], v[72:75]\n"
    "v_mfma_f32_16x16x32_bf16 v[72:75], v[180:183], v[4:7], v[72:75]\n"
    "v_mfma_f32_16x16x32_bf16 v[76:79], v[180:183], v[28:31], v[76:79]\n"
    "v_mfma_f32_16x16x32_bf16 v[76:79], v[176:179], v[24:27], v[76:79]\n"
    "ds_read_b128 v[160:163], v205 offset:10240\n"
    "ds_read_b128 v[164:167], v205 offset:11264\n"
    "s_waitcnt lgkmcnt(1)\n"
    "v_mfma_f32_16x16x32_bf16 v[92:95], v[160:163], v[24:27], 0\n"
    "s_waitcnt lgkmcnt(0)\n"
    "v_mfma_f32_16x16x32_bf16 v[92:95], v[164:167], v[28:31], v[92:95]\n"
    "v_mfma_f32_16x16x32_bf16 v[88:91], v[164:167], v[4:7], 0\n"
    "v_mfma_f32_16x16x32_bf16 v[88:91], v[160:163], v[0:3], v[88:91]\n"
    "v_mfma_f32_16x16x32_bf16 v[84:87], v[160:163], a[232:235], 0\n"
    "v_mfma_f32_16x16x32_bf16 v[84:87], v[164:167], a[236:239], v[84:87]\n"
    "v_mfma_f32_16x16x32_bf16 v[80:83], v[164:167], a[212:215], 0\n"
    "v_mfma_f32_16x16x32_bf16 v[80:83], v[160:163], a[208:211], v[80:83]\n"
    "ds_read_b128 v[168:171], v205 offset:8192\n"
    "ds_read_b128 v[172:175], v205 offset:9216\n"
    "s_waitcnt lgkmcnt(1)\n"
    "v_mfma_f32_16x16x32_bf16 v[80:83], v[168:171], a[200:203], v[80:83]\n"
    "s_waitcnt lgkmcnt(0)\n"
    "v_mfma_f32_16x16x32_bf16 v[80:83], v[172:175], a[204:207], v[80:83]\n"
    "v_mfma_f32_16x16x32_bf16 v[84:87], v[172:175], a[228:231], v[84:87]\n"
    "v_mfma_f32_16x16x32_bf16 v[84:87], v[168:171], a[224:227], v[84:87]\n"
    "v_mfma_f32_16x16x32_bf16 v[88:91], v[168:171], a[248:251], v[88:91]\n"
    "v_mfma_f32_16x16x32_bf16 v[88:91], v[172:175], a[252:255], v[88:91]\n"
    "v_mfma_f32_16x16x32_bf16 v[92:95], v[172:175], v[20:23], v[92:95]\n"
    "v_mfma_f32_16x16x32_bf16 v[92:95], v[168:171], v[16:19], v[92:95]\n"
    "ds_read_b128 v[176:179], v205 offset:6144\n"
    "ds_read_b128 v[180:183], v205 offset:7168\n"
    "s_waitcnt lgkmcnt(1)\n"
    "v_mfma_f32_16x16x32_bf16 v[92:95], v[176:179], v[8:11], v[92:95]\n"
    "s_waitcnt lgkmcnt(0)\n"
    "v_mfma_f32_16x16x32_bf16 v[92:95], v[180:183], v[12:15], v[92:95]\n"
    "v_mfma_f32_16x16x32_bf16 v[88:91], v[180:183], a[244:247], v[88:91]\n"
    "v_mfma_f32_16x16x32_bf16 v[88:91], v[176:179], a[240:243], v[88:91]\n"
    "v_mfma_f32_16x16x32_bf16 v[84:87], v[176:179], a[216:219], v[84:87]\n"
    "v_mfma_f32_16x16x32_bf16 v[84:87], v[180:183], a[220:223], v[84:87]\n"
    "v_mfma_f32_16x16x32_bf16 v[80:83], v[180:183], a[196:199], v[80:83]\n"
    "v_mfma_f32_16x16x32_bf16 v[80:83], v[176:179], a[192:195], v[80:83]\n"
    "v_max3_f32 v196, v32, v33, v34\n"
    "v_max3_f32 v196, v196, v35, v48\n"
    "v_max3_f32 v196, v196, v49, v50\n"
    "v_max3_f32 v196, v196, v51, v64\n"
    "v_max3_f32 v196, v196, v65, v66\n"
    "s_nop 2\n"
    "v_max3_f32 v196, v196, v67, v80\n"
    "v_max3_f32 v196, v196, v81, v82\n"
    "v_max_f32 v196, v196, v83\n"
    "v_mov_b32 v197, v196\n"
    "s_nop 1\n"
    "v_permlane32_swap_b32 v196, v197\n"
    "v_max_f32 v196, v196, v197\n"
    "v_mov_b32 v197, v196\n"
    "s_nop 1\n"
    "v_permlane16_swap_b32 v196, v197\n"
    "v_max_f32 v196, v196, v197\n"
    "v_max_f32 v196, v196, v96\n"
    "v_sub_f32 v197, v96, v196\n"
    "v_mov_b32 v96, v196\n"
    "v_exp_f32 v197, v197\n"
    "v_sub_f32 v32, v32, v196\n"
    "v_sub_f32 v33, v33, v196\n"
    "v_sub_f32 v34, v34, v196\n"
    "v_sub_f32 v35, v35, v196\n"
    "v_sub_f32 v48, v48, v196\n"
    "v_sub_f32 v49, v49, v196\n"
    "v_sub_f32 v50, v50, v196\n"
    "v_sub_f32 v51, v51, v196\n"
    "v_sub_f32 v64, v64, v196\n"
    "v_sub_f32 v65, v65, v196\n"
    "v_sub_f32 v66, v66, v196\n"
    "v_sub_f32 v67, v67, v196\n"
    "v_sub_f32 v80, v80, v196\n"
    "v_sub_f32 v81, v81, v196\n"
    "v_sub_f32 v82, v82, v196\n"
    "v_sub_f32 v83, v83, v196\n"
    "v_mul_f32 v112, v112, v197\n"
    "v_mul_f32 v113, v113, v197\n"
    "v_mul_f32 v114, v114, v197\n"
    "v_mul_f32 v115, v115, v197\n"
    "v_accvgpr_read_b32 v198, a0\n"
    "v_accvgpr_read_b32 v199, a1\n"
    "v_mul_f32 v198, v198, v197\n"
    "v_mul_f32 v199, v199, v197\n"
    "v_accvgpr_write_b32 a0, v198\n"
    "v_accvgpr_write_b32 a1, v199\n"
    "v_accvgpr_read_b32 v198, a2\n"
    "v_accvgpr_read_b32 v199, a3\n"
    "v_mul_f32 v198, v198, v197\n"
    "v_mul_f32 v199, v199, v197\n"
    "v_accvgpr_write_b32 a2, v198\n"
    "v_accvgpr_write_b32 a3, v199\n"
    "v_accvgpr_read_b32 v198, a16\n"
    "v_accvgpr_read_b32 v199, a17\n"
    "v_mul_f32 v198, v198, v197\n"
    "v_mul_f32 v199, v199, v197\n"
    "v_accvgpr_write_b32 a16, v198\n"
    "v_accvgpr_write_b32 a17, v199\n"
    "v_accvgpr_read_b32 v198, a18\n"
    "v_accvgpr_read_b32 v199, a19\n"
    "v_mul_f32 v198, v198, v197\n"
    "v_mul_f32 v199, v199, v197\n"
    "v_accvgpr_write_b32 a18, v198\n"
    "v_accvgpr_write_b32 a19, v199\n"
    "v_accvgpr_read_b32 v198, a32\n"
    "v_accvgpr_read_b32 v199, a33\n"
    "v_mul_f32 v198, v198, v197\n"
    "v_mul_f32 v199, v199, v197\n"
    "v_accvgpr_write_b32 a32, v198\n"
    "v_accvgpr_write_b32 a33, v199\n"
    "v_accvgpr_read_b32 v198, a34\n"
    "v_accvgpr_read_b32 v199, a35\n"
    "v_mul_f32 v198, v198, v197\n"
    "v_mul_f32 v199, v199, v197\n"
    "v_accvgpr_write_b32 a34, v198\n"
    "v_accvgpr_write_b32 a35, v199\n"
    "v_accvgpr_read_b32 v198, a48\n"
    "v_accvgpr_read_b32 v199, a49\n"
    "v_mul_f32 v198, v198, v197\n"
    "v_mul_f32 v199, v199, v197\n"
    "v_accvgpr_write_b32 a48, v198\n"
    "v_accvgpr_write_b32 a49, v199\n"
    "v_accvgpr_read_b32 v198, a50\n"
    "v_accvgpr_read_b32 v199, a51\n"
    "v_mul_f32 v198, v198, v197\n"
    "v_mul_f32 v199, v199, v197\n"
    "v_accvgpr_write_b32 a50, v198\n"
    "v_accvgpr_write_b32 a51, v199\n"
    "v_accvgpr_read_b32 v198, a64\n"
    "v_accvgpr_read_b32 v199, a65\n"
    "v_mul_f32 v198, v198, v197\n"
    "v_mul_f32 v199, v199, v197\n"
    "v_accvgpr_write_b32 a64, v198\n"
    "v_accvgpr_write_b32 a65, v199\n"
    "v_accvgpr_read_b32 v198, a66\n"
    "v_accvgpr_read_b32 v199, a67\n"
    "v_mul_f32 v198, v198, v197\n"
    "v_mul_f32 v199, v199, v197\n"
    "v_accvgpr_write_b32 a66, v198\n"
    "v_accvgpr_write_b32 a67, v199\n"
    "v_accvgpr_read_b32 v198, a80\n"
    "v_accvgpr_read_b32 v199, a81\n"
    "v_mul_f32 v198, v198, v197\n"
    "v_mul_f32 v199, v199, v197\n"
    "v_accvgpr_write_b32 a80, v198\n"
    "v_accvgpr_write_b32 a81, v199\n"
    "v_accvgpr_read_b32 v198, a82\n"
    "v_accvgpr_read_b32 v199, a83\n"
    "v_mul_f32 v198, v198, v197\n"
    "v_mul_f32 v199, v199, v197\n"
    "v_accvgpr_write_b32 a82, v198\n"
    "v_accvgpr_write_b32 a83, v199\n"
    "v_accvgpr_read_b32 v198, a96\n"
    "v_accvgpr_read_b32 v199, a97\n"
    "v_mul_f32 v198, v198, v197\n"
    "v_mul_f32 v199, v199, v197\n"
    "v_accvgpr_write_b32 a96, v198\n"
    "v_accvgpr_write_b32 a97, v199\n"
    "v_accvgpr_read_b32 v198, a98\n"
    "v_accvgpr_read_b32 v199, a99\n"
    "v_mul_f32 v198, v198, v197\n"
    "v_mul_f32 v199, v199, v197\n"
    "v_accvgpr_write_b32 a98, v198\n"
    "v_accvgpr_write_b32 a99, v199\n"
    "v_accvgpr_read_b32 v198, a112\n"
    "v_accvgpr_read_b32 v199, a113\n"
    "v_mul_f32 v198, v198, v197\n"
    "v_mul_f32 v199, v199, v197\n"
    "v_accvgpr_write_b32 a112, v198\n"
    "v_accvgpr_write_b32 a113, v199\n"
    "v_accvgpr_read_b32 v198, a114\n"
    "v_accvgpr_read_b32 v199, a115\n"
    "v_mul_f32 v198, v198, v197\n"
    "v_mul_f32 v199, v199, v197\n"
    "v_accvgpr_write_b32 a114, v198\n"
    "v_accvgpr_write_b32 a115, v199\n"
    "v_accvgpr_read_b32 v198, a128\n"
    "v_accvgpr_read_b32 v199, a129\n"
    "v_mul_f32 v198, v198, v197\n"
    "v_mul_f32 v199, v199, v197\n"
    "v_accvgpr_write_b32 a128, v198\n"
    "v_accvgpr_write_b32 a129, v199\n"
    "v_accvgpr_read_b32 v198, a130\n"
    "v_accvgpr_read_b32 v199, a131\n"
    "v_mul_f32 v198, v198, v197\n"
    "v_mul_f32 v199, v199, v197\n"
    "v_accvgpr_write_b32 a130, v198\n"
    "v_accvgpr_write_b32 a131, v199\n"
    "v_accvgpr_read_b32 v198, a144\n"
    "v_accvgpr_read_b32 v199, a145\n"
    "v_mul_f32 v198, v198, v197\n"
    "v_mul_f32 v199, v199, v197\n"
    "v_accvgpr_write_b32 a144, v198\n"
    "v_accvgpr_write_b32 a145, v199\n"
    "v_accvgpr_read_b32 v198, a146\n"
    "v_accvgpr_read_b32 v199, a147\n"
    "v_mul_f32 v198, v198, v197\n"
    "v_mul_f32 v199, v199, v197\n"
    "v_accvgpr_write_b32 a146, v198\n"
    "v_accvgpr_write_b32 a147, v199\n"
    "v_accvgpr_read_b32 v198, a160\n"
    "v_accvgpr_read_b32 v199, a161\n"
    "v_mul_f32 v198, v198, v197\n"
    "v_mul_f32 v199, v199, v197\n"
    "v_accvgpr_write_b32 a160, v198\n"
    "v_accvgpr_write_b32 a161, v199\n"
    "v_accvgpr_read_b32 v198, a162\n"
    "v_accvgpr_read_b32 v199, a163\n"
    "v_mul_f32 v198, v198, v197\n"
    "v_mul_f32 v199, v199, v197\n"
    "v_accvgpr_write_b32 a162, v198\n"
    "v_accvgpr_write_b32 a163, v199\n"
    "v_accvgpr_read_b32 v198, a176\n"
    "v_accvgpr_read_b32 v199, a177\n"
    "v_mul_f32 v198, v198, v197\n"
    "v_mul_f32 v199, v199, v197\n"
    "v_accvgpr_write_b32 a176, v198\n"
    "v_accvgpr_write_b32 a177, v199\n"
    "v_accvgpr_read_b32 v198, a178\n"
    "v_accvgpr_read_b32 v199, a179\n"
    "v_mul_f32 v198, v198, v197\n"
    "v_mul_f32 v199, v199, v197\n"
    "v_accvgpr_write_b32 a178, v198\n"
    "v_accvgpr_write_b32 a179, v199\n"
    "v_max3_f32 v196, v36, v37, v38\n"
    "v_max3_f32 v196, v196, v39, v52\n"
    "v_max3_f32 v196, v196, v53, v54\n"
    "v_max3_f32 v196, v196, v55, v68\n"
    "v_max3_f32 v196, v196, v69, v70\n"
    "v_max3_f32 v196, v196, v71, v84\n"
    "v_max3_f32 v196, v196, v85, v86\n"
    "v_max_f32 v196, v196, v87\n"
    "v_mov_b32 v197, v196\n"
    "s_nop 1\n"
    "v_permlane32_swap_b32 v196, v197\n"
    "v_max_f32 v196, v196, v197\n"
    "v_mov_b32 v197, v196\n"
    "s_nop 1\n"
    "v_permlane16_swap_b32 v196, v197\n"
    "v_max_f32 v196, v196, v197\n"
    "v_max_f32 v196, v196, v100\n"
    "v_sub_f32 v197, v100, v196\n"
    "v_mov_b32 v100, v196\n"
    "v_exp_f32 v197, v197\n"
    "v_sub_f32 v36, v36, v196\n"
    "v_sub_f32 v37, v37, v196\n"
    "v_sub_f32 v38, v38, v196\n"
    "v_sub_f32 v39, v39, v196\n"
    "v_sub_f32 v52, v52, v196\n"
    "v_sub_f32 v53, v53, v196\n"
    "v_sub_f32 v54, v54, v196\n"
    "v_sub_f32 v55, v55, v196\n"
    "v_sub_f32 v68, v68, v196\n"
    "v_sub_f32 v69, v69, v196\n"
    "v_sub_f32 v70, v70, v196\n"
    "v_sub_f32 v71, v71, v196\n"
    "v_sub_f32 v84, v84, v196\n"
    "v_sub_f32 v85, v85, v196\n"
    "v_sub_f32 v86, v86, v196\n"
    "v_sub_f32 v87, v87, v196\n"
    "v_mul_f32 v116, v116, v197\n"
    "v_mul_f32 v117, v117, v197\n"
    "v_mul_f32 v118, v118, v197\n"
    "v_mul_f32 v119, v119, v197\n"
    "v_accvgpr_read_b32 v198, a4\n"
    "v_accvgpr_read_b32 v199, a5\n"
    "v_mul_f32 v198, v198, v197\n"
    "v_mul_f32 v199, v199, v197\n"
    "v_accvgpr_write_b32 a4, v198\n"
    "v_accvgpr_write_b32 a5, v199\n"
    "v_accvgpr_read_b32 v198, a6\n"
    "v_accvgpr_read_b32 v199, a7\n"
    "v_mul_f32 v198, v198, v197\n"
    "v_mul_f32 v199, v199, v197\n"
    "v_accvgpr_write_b32 a6, v198\n"
    "v_accvgpr_write_b32 a7, v199\n"
    "v_accvgpr_read_b32 v198, a20\n"
    "v_accvgpr_read_b32 v199, a21\n"
    "v_mul_f32 v198, v198, v197\n"
    "v_mul_f32 v199, v199, v197\n"
    "v_accvgpr_write_b32 a20, v198\n"
    "v_accvgpr_write_b32 a21, v199\n"
    "v_accvgpr_read_b32 v198, a22\n"
    "v_accvgpr_read_b32 v199, a23\n"
    "v_mul_f32 v198, v198, v197\n"
    "v_mul_f32 v199, v199, v197\n"
    "v_accvgpr_write_b32 a22, v198\n"
    "v_accvgpr_write_b32 a23, v199\n"
    "v_accvgpr_read_b32 v198, a36\n"
    "v_accvgpr_read_b32 v199, a37\n"
    "v_mul_f32 v198, v198, v197\n"
    "v_mul_f32 v199, v199, v197\n"
    "v_accvgpr_write_b32 a36, v198\n"
    "v_accvgpr_write_b32 a37, v199\n"
    "v_accvgpr_read_b32 v198, a38\n"
    "v_accvgpr_read_b32 v199, a39\n"
    "v_mul_f32 v198, v198, v197\n"
    "v_mul_f32 v199, v199, v197\n"
    "v_accvgpr_write_b32 a38, v198\n"
    "v_accvgpr_write_b32 a39, v199\n"
    "v_accvgpr_read_b32 v198, a52\n"
    "v_accvgpr_read_b32 v199, a53\n"
    "v_mul_f32 v198, v198, v197\n"
    "v_mul_f32 v199, v199, v197\n"
    "v_accvgpr_write_b32 a52, v198\n"
    "v_accvgpr_write_b32 a53, v199\n"
    "v_accvgpr_read_b32 v198, a54\n"
    "v_accvgpr_read_b32 v199, a55\n"
    "v_mul_f32 v198, v198, v197\n"
    "v_mul_f32 v199, v199, v197\n"
    "v_accvgpr_write_b32 a54, v198\n"
    "v_accvgpr_write_b32 a55, v199\n"
    "v_accvgpr_read_b32 v198, a68\n"
    "v_accvgpr_read_b32 v199, a69\n"
    "v_mul_f32 v198, v198, v197\n"
    "v_mul_f32 v199, v199, v197\n"
    "v_accvgpr_write_b32 a68, v198\n"
    "v_accvgpr_write_b32 a69, v199\n"
    "v_accvgpr_read_b32 v198, a70\n"
    "v_accvgpr_read_b32 v199, a71\n"
    "v_mul_f32 v198, v198, v197\n"
    "v_mul_f32 v199, v199, v197\n"
    "v_accvgpr_write_b32 a70, v198\n"
    "v_accvgpr_write_b32 a71, v199\n"
    "v_accvgpr_read_b32 v198, a84\n"
    "v_accvgpr_read_b32 v199, a85\n"
    "v_mul_f32 v198, v198, v197\n"
    "v_mul_f32 v199, v199, v197\n"
    "v_accvgpr_write_b32 a84, v198\n"
    "v_accvgpr_write_b32 a85, v199\n"
    "v_accvgpr_read_b32 v198, a86\n"
    "v_accvgpr_read_b32 v199, a87\n"
    "v_mul_f32 v198, v198, v197\n"
    "v_mul_f32 v199, v199, v197\n"
    "v_accvgpr_write_b32 a86, v198\n"
    "v_accvgpr_write_b32 a87, v199\n"
    "v_accvgpr_read_b32 v198, a100\n"
    "v_accvgpr_read_b32 v199, a101\n"
    "v_mul_f32 v198, v198, v197\n"
    "v_mul_f32 v199, v199, v197\n"
    "v_accvgpr_write_b32 a100, v198\n"
    "v_accvgpr_write_b32 a101, v199\n"
    "v_accvgpr_read_b32 v198, a102\n"
    "v_accvgpr_read_b32 v199, a103\n"
    "v_mul_f32 v198, v198, v197\n"
    "v_mul_f32 v199, v199, v197\n"
    "v_accvgpr_write_b32 a102, v198\n"
    "v_accvgpr_write_b32 a103, v199\n"
    "v_accvgpr_read_b32 v198, a116\n"
    "v_accvgpr_read_b32 v199, a117\n"
    "v_mul_f32 v198, v198, v197\n"
    "v_mul_f32 v199, v199, v197\n"
    "v_accvgpr_write_b32 a116, v198\n"
    "v_accvgpr_write_b32 a117, v199\n"
    "v_accvgpr_read_b32 v198, a118\n"
    "v_accvgpr_read_b32 v199, a119\n"
    "v_mul_f32 v198, v198, v197\n"
    "v_mul_f32 v199, v199, v197\n"
    "v_accvgpr_write_b32 a118, v198\n"
    "v_accvgpr_write_b32 a119, v199\n"
    "v_accvgpr_read_b32 v198, a132\n"
    "v_accvgpr_read_b32 v199, a133\n"
    "v_mul_f32 v198, v198, v197\n"
    "v_mul_f32 v199, v199, v197\n"
    "v_accvgpr_write_b32 a132, v198\n"
    "v_accvgpr_write_b32 a133, v199\n"
    "v_accvgpr_read_b32 v198, a134\n"
    "v_accvgpr_read_b32 v199, a135\n"
    "v_mul_f32 v198, v198, v197\n"
    "v_mul_f32 v199, v199, v197\n"
    "v_accvgpr_write_b32 a134, v198\n"
    "v_accvgpr_write_b32 a135, v199\n"
    "v_accvgpr_read_b32 v198, a148\n"
    "v_accvgpr_read_b32 v199, a149\n"
    "v_mul_f32 v198, v198, v197\n"
    "v_mul_f32 v199, v199, v197\n"
    "v_accvgpr_write_b32 a148, v198\n"
    "v_accvgpr_write_b32 a149, v199\n"
    "v_accvgpr_read_b32 v198, a150\n"
    "v_accvgpr_read_b32 v199, a151\n"
    "v_mul_f32 v198, v198, v197\n"
    "v_mul_f32 v199, v199, v197\n"
    "v_accvgpr_write_b32 a150, v198\n"
    "v_accvgpr_write_b32 a151, v199\n"
    "v_accvgpr_read_b32 v198, a164\n"
    "v_accvgpr_read_b32 v199, a165\n"
    "v_mul_f32 v198, v198, v197\n"
    "v_mul_f32 v199, v199, v197\n"
    "v_accvgpr_write_b32 a164, v198\n"
    "v_accvgpr_write_b32 a165, v199\n"
    "v_accvgpr_read_b32 v198, a166\n"
    "v_accvgpr_read_b32 v199, a167\n"
    "v_mul_f32 v198, v198, v197\n"
    "v_mul_f32 v199, v199, v197\n"
    "v_accvgpr_write_b32 a166, v198\n"
    "v_accvgpr_write_b32 a167, v199\n"
    "v_accvgpr_read_b32 v198, a180\n"
    "v_accvgpr_read_b32 v199, a181\n"
    "v_mul_f32 v198, v198, v197\n"
    "v_mul_f32 v199, v199, v197\n"
    "v_accvgpr_write_b32 a180, v198\n"
    "v_accvgpr_write_b32 a181, v199\n"
    "v_accvgpr_read_b32 v198, a182\n"
    "v_accvgpr_read_b32 v199, a183\n"
    "v_mul_f32 v198, v198, v197\n"
    "v_mul_f32 v199, v199, v197\n"
    "v_accvgpr_write_b32 a182, v198\n"
    "v_accvgpr_write_b32 a183, v199\n"
    "v_max3_f32 v196, v40, v41, v42\n"
    "v_max3_f32 v196, v196, v43, v56\n"
    "v_max3_f32 v196, v196, v57, v58\n"
    "v_max3_f32 v196, v196, v59, v72\n"
    "v_max3_f32 v196, v196, v73, v74\n"
    "v_max3_f32 v196, v196, v75, v88\n"
    "v_max3_f32 v196, v196, v89, v90\n"
    "v_max_f32 v196, v196, v91\n"
    "v_mov_b32 v197, v196\n"
    "s_nop 1\n"
    "v_permlane32_swap_b32 v196, v197\n"
    "v_max_f32 v196, v196, v197\n"
    "v_mov_b32 v197, v196\n"
    "s_nop 1\n"
    "v_permlane16_swap_b32 v196, v197\n"
    "v_max_f32 v196, v196, v197\n"
    "v_max_f32 v196, v196, v104\n"
    "v_sub_f32 v197, v104, v196\n"
    "v_mov_b32 v104, v196\n"
    "v_exp_f32 v197, v197\n"
    "v_sub_f32 v40, v40, v196\n"
    "v_sub_f32 v41, v41, v196\n"
    "v_sub_f32 v42, v42, v196\n"
    "v_sub_f32 v43, v43, v196\n"
    "v_sub_f32 v56, v56, v196\n"
    "v_sub_f32 v57, v57, v196\n"
    "v_sub_f32 v58, v58, v196\n"
    "v_sub_f32 v59, v59, v196\n"
    "v_sub_f32 v72, v72, v196\n"
    "v_sub_f32 v73, v73, v196\n"
    "v_sub_f32 v74, v74, v196\n"
    "v_sub_f32 v75, v75, v196\n"
    "v_sub_f32 v88, v88, v196\n"
    "v_sub_f32 v89, v89, v196\n"
    "v_sub_f32 v90, v90, v196\n"
    "v_sub_f32 v91, v91, v196\n"
    "v_mul_f32 v120, v120, v197\n"
    "v_mul_f32 v121, v121, v197\n"
    "v_mul_f32 v122, v122, v197\n"
    "v_mul_f32 v123, v123, v197\n"
    "v_accvgpr_read_b32 v198, a8\n"
    "v_accvgpr_read_b32 v199, a9\n"
    "v_mul_f32 v198, v198, v197\n"
    "v_mul_f32 v199, v199, v197\n"
    "v_accvgpr_write_b32 a8, v198\n"
    "v_accvgpr_write_b32 a9, v199\n"
    "v_accvgpr_read_b32 v198, a10\n"
    "v_accvgpr_read_b32 v199, a11\n"
    "v_mul_f32 v198, v198, v197\n"
    "v_mul_f32 v199, v199, v197\n"
    "v_accvgpr_write_b32 a10, v198\n"
    "v_accvgpr_write_b32 a11, v199\n"
    "v_accvgpr_read_b32 v198, a24\n"
    "v_accvgpr_read_b32 v199, a25\n"
    "v_mul_f32 v198, v198, v197\n"
    "v_mul_f32 v199, v199, v197\n"
    "v_accvgpr_write_b32 a24, v198\n"
    "v_accvgpr_write_b32 a25, v199\n"
    "v_accvgpr_read_b32 v198, a26\n"
    "v_accvgpr_read_b32 v199, a27\n"
    "v_mul_f32 v198, v198, v197\n"
    "v_mul_f32 v199, v199, v197\n"
    "v_accvgpr_write_b32 a26, v198\n"
    "v_accvgpr_write_b32 a27, v199\n"
    "v_accvgpr_read_b32 v198, a40\n"
    "v_accvgpr_read_b32 v199, a41\n"
    "v_mul_f32 v198, v198, v197\n"
    "v_mul_f32 v199, v199, v197\n"
    "v_accvgpr_write_b32 a40, v198\n"
    "v_accvgpr_write_b32 a41, v199\n"
    "v_accvgpr_read_b32 v198, a42\n"
    "v_accvgpr_read_b32 v199, a43\n"
    "v_mul_f32 v198, v198, v197\n"
    "v_mul_f32 v199, v199, v197\n"
    "v_accvgpr_write_b32 a42, v198\n"
    "v_accvgpr_write_b32 a43, v199\n"
    "v_accvgpr_read_b32 v198, a56\n"
    "v_accvgpr_read_b32 v199, a57\n"
    "v_mul_f32 v198, v198, v197\n"
    "v_mul_f32 v199, v199, v197\n"
    "v_accvgpr_write_b32 a56, v198\n"
    "v_accvgpr_write_b32 a57, v199\n"
    "v_accvgpr_read_b32 v198, a58\n"
    "v_accvgpr_read_b32 v199, a59\n"
    "v_mul_f32 v198, v198, v197\n"
    "v_mul_f32 v199, v199, v197\n"
    "v_accvgpr_write_b32 a58, v198\n"
    "v_accvgpr_write_b32 a59, v199\n"
    "v_accvgpr_read_b32 v198, a72\n"
    "v_accvgpr_read_b32 v199, a73\n"
    "v_mul_f32 v198, v198, v197\n"
    "v_mul_f32 v199, v199, v197\n"
    "v_accvgpr_write_b32 a72, v198\n"
    "v_accvgpr_write_b32 a73, v199\n"
    "v_accvgpr_read_b32 v198, a74\n"
    "v_accvgpr_read_b32 v199, a75\n"
    "v_mul_f32 v198, v198, v197\n"
    "v_mul_f32 v199, v199, v197\n"
    "v_accvgpr_write_b32 a74, v198\n"
    "v_accvgpr_write_b32 a75, v199\n"
    "v_accvgpr_read_b32 v198, a88\n"
    "v_accvgpr_read_b32 v199, a89\n"
    "v_mul_f32 v198, v198, v197\n"
    "v_mul_f32 v199, v199, v197\n"
    "v_accvgpr_write_b32 a88, v198\n"
    "v_accvgpr_write_b32 a89, v199\n"
    "v_accvgpr_read_b32 v198, a90\n"
    "v_accvgpr_read_b32 v199, a91\n"
    "v_mul_f32 v198, v198, v197\n"
    "v_mul_f32 v199, v199, v197\n"
    "v_accvgpr_write_b32 a90, v198\n"
    "v_accvgpr_write_b32 a91, v199\n"
    "v_accvgpr_read_b32 v198, a104\n"
    "v_accvgpr_read_b32 v199, a105\n"
    "v_mul_f32 v198, v198, v197\n"
    "v_mul_f32 v199, v199, v197\n"
    "v_accvgpr_write_b32 a104, v198\n"
    "v_accvgpr_write_b32 a105, v199\n"
    "v_accvgpr_read_b32 v198, a106\n"
    "v_accvgpr_read_b32 v199, a107\n"
    "v_mul_f32 v198, v198, v197\n"
    "v_mul_f32 v199, v199, v197\n"
    "v_accvgpr_write_b32 a106, v198\n"
    "v_accvgpr_write_b32 a107, v199\n"
    "v_accvgpr_read_b32 v198, a120\n"
    "v_accvgpr_read_b32 v199, a121\n"
    "v_mul_f32 v198, v198, v197\n"
    "v_mul_f32 v199, v199, v197\n"
    "v_accvgpr_write_b32 a120, v198\n"
    "v_accvgpr_write_b32 a121, v199\n"
    "v_accvgpr_read_b32 v198, a122\n"
    "v_accvgpr_read_b32 v199, a123\n"
    "v_mul_f32 v198, v198, v197\n"
    "v_mul_f32 v199, v199, v197\n"
    "v_accvgpr_write_b32 a122, v198\n"
    "v_accvgpr_write_b32 a123, v199\n"
    "v_accvgpr_read_b32 v198, a136\n"
    "v_accvgpr_read_b32 v199, a137\n"
    "v_mul_f32 v198, v198, v197\n"
    "v_mul_f32 v199, v199, v197\n"
    "v_accvgpr_write_b32 a136, v198\n"
    "v_accvgpr_write_b32 a137, v199\n"
    "v_accvgpr_read_b32 v198, a138\n"
    "v_accvgpr_read_b32 v199, a139\n"
    "v_mul_f32 v198, v198, v197\n"
    "v_mul_f32 v199, v199, v197\n"
    "v_accvgpr_write_b32 a138, v198\n"
    "v_accvgpr_write_b32 a139, v199\n"
    "v_accvgpr_read_b32 v198, a152\n"
    "v_accvgpr_read_b32 v199, a153\n"
    "v_mul_f32 v198, v198, v197\n"
    "v_mul_f32 v199, v199, v197\n"
    "v_accvgpr_write_b32 a152, v198\n"
    "v_accvgpr_write_b32 a153, v199\n"
    "v_accvgpr_read_b32 v198, a154\n"
    "v_accvgpr_read_b32 v199, a155\n"
    "v_mul_f32 v198, v198, v197\n"
    "v_mul_f32 v199, v199, v197\n"
    "v_accvgpr_write_b32 a154, v198\n"
    "v_accvgpr_write_b32 a155, v199\n"
    "v_accvgpr_read_b32 v198, a168\n"
    "v_accvgpr_read_b32 v199, a169\n"
    "v_mul_f32 v198, v198, v197\n"
    "v_mul_f32 v199, v199, v197\n"
    "v_accvgpr_write_b32 a168, v198\n"
    "v_accvgpr_write_b32 a169, v199\n"
    "v_accvgpr_read_b32 v198, a170\n"
    "v_accvgpr_read_b32 v199, a171\n"
    "v_mul_f32 v198, v198, v197\n"
    "v_mul_f32 v199, v199, v197\n"
    "v_accvgpr_write_b32 a170, v198\n"
    "v_accvgpr_write_b32 a171, v199\n"
    "v_accvgpr_read_b32 v198, a184\n"
    "v_accvgpr_read_b32 v199, a185\n"
    "v_mul_f32 v198, v198, v197\n"
    "v_mul_f32 v199, v199, v197\n"
    "v_accvgpr_write_b32 a184, v198\n"
    "v_accvgpr_write_b32 a185, v199\n"
    "v_accvgpr_read_b32 v198, a186\n"
    "v_accvgpr_read_b32 v199, a187\n"
    "v_mul_f32 v198, v198, v197\n"
    "v_mul_f32 v199, v199, v197\n"
    "v_accvgpr_write_b32 a186, v198\n"
    "v_accvgpr_write_b32 a187, v199\n"
    "v_max3_f32 v196, v44, v45, v46\n"
    "v_max3_f32 v196, v196, v47, v60\n"
    "v_max3_f32 v196, v196, v61, v62\n"
    "v_max3_f32 v196, v196, v63, v76\n"
    "v_max3_f32 v196, v196, v77, v78\n"
    "v_max3_f32 v196, v196, v79, v92\n"
    "v_max3_f32 v196, v196, v93, v94\n"
    "v_max_f32 v196, v196, v95\n"
    "v_mov_b32 v197, v196\n"
    "s_nop 1\n"
    "v_permlane32_swap_b32 v196, v197\n"
    "v_max_f32 v196, v196, v197\n"
    "v_mov_b32 v197, v196\n"
    "s_nop 1\n"
    "v_permlane16_swap_b32 v196, v197\n"
    "v_max_f32 v196, v196, v197\n"
    "v_max_f32 v196, v196, v108\n"
    "v_sub_f32 v197, v108, v196\n"
    "v_mov_b32 v108, v196\n"
    "v_exp_f32 v197, v197\n"
    "v_sub_f32 v44, v44, v196\n"
    "v_sub_f32 v45, v45, v196\n"
    "v_sub_f32 v46, v46, v196\n"
    "v_sub_f32 v47, v47, v196\n"
    "v_sub_f32 v60, v60, v196\n"
    "v_sub_f32 v61, v61, v196\n"
    "v_sub_f32 v62, v62, v196\n"
    "v_sub_f32 v63, v63, v196\n"
    "v_sub_f32 v76, v76, v196\n"
    "v_sub_f32 v77, v77, v196\n"
    "v_sub_f32 v78, v78, v196\n"
    "v_sub_f32 v79, v79, v196\n"
    "v_sub_f32 v92, v92, v196\n"
    "v_sub_f32 v93, v93, v196\n"
    "v_sub_f32 v94, v94, v196\n"
    "v_sub_f32 v95, v95, v196\n"
    "v_mul_f32 v124, v124, v197\n"
    "v_mul_f32 v125, v125, v197\n"
    "v_mul_f32 v126, v126, v197\n"
    "v_mul_f32 v127, v127, v197\n"
    "v_accvgpr_read_b32 v198, a12\n"
    "v_accvgpr_read_b32 v199, a13\n"
    "v_mul_f32 v198, v198, v197\n"
    "v_mul_f32 v199, v199, v197\n"
    "v_accvgpr_write_b32 a12, v198\n"
    "v_accvgpr_write_b32 a13, v199\n"
    "v_accvgpr_read_b32 v198, a14\n"
    "v_accvgpr_read_b32 v199, a15\n"
    "v_mul_f32 v198, v198, v197\n"
    "v_mul_f32 v199, v199, v197\n"
    "v_accvgpr_write_b32 a14, v198\n"
    "v_accvgpr_write_b32 a15, v199\n"
    "v_accvgpr_read_b32 v198, a28\n"
    "v_accvgpr_read_b32 v199, a29\n"
    "v_mul_f32 v198, v198, v197\n"
    "v_mul_f32 v199, v199, v197\n"
    "v_accvgpr_write_b32 a28, v198\n"
    "v_accvgpr_write_b32 a29, v199\n"
    "v_accvgpr_read_b32 v198, a30\n"
    "v_accvgpr_read_b32 v199, a31\n"
    "v_mul_f32 v198, v198, v197\n"
    "v_mul_f32 v199, v199, v197\n"
    "v_accvgpr_write_b32 a30, v198\n"
    "v_accvgpr_write_b32 a31, v199\n"
    "v_accvgpr_read_b32 v198, a44\n"
    "v_accvgpr_read_b32 v199, a45\n"
    "v_mul_f32 v198, v198, v197\n"
    "v_mul_f32 v199, v199, v197\n"
    "v_accvgpr_write_b32 a44, v198\n"
    "v_accvgpr_write_b32 a45, v199\n"
    "v_accvgpr_read_b32 v198, a46\n"
    "v_accvgpr_read_b32 v199, a47\n"
    "v_mul_f32 v198, v198, v197\n"
    "v_mul_f32 v199, v199, v197\n"
    "v_accvgpr_write_b32 a46, v198\n"
    "v_accvgpr_write_b32 a47, v199\n"
    "v_accvgpr_read_b32 v198, a60\n"
    "v_accvgpr_read_b32 v199, a61\n"
    "v_mul_f32 v198, v198, v197\n"
    "v_mul_f32 v199, v199, v197\n"
    "v_accvgpr_write_b32 a60, v198\n"
    "v_accvgpr_write_b32 a61, v199\n"
    "v_accvgpr_read_b32 v198, a62\n"
    "v_accvgpr_read_b32 v199, a63\n"
    "v_mul_f32 v198, v198, v197\n"
    "v_mul_f32 v199, v199, v197\n"
    "v_accvgpr_write_b32 a62, v198\n"
    "v_accvgpr_write_b32 a63, v199\n"
    "v_accvgpr_read_b32 v198, a76\n"
    "v_accvgpr_read_b32 v199, a77\n"
    "v_mul_f32 v198, v198, v197\n"
    "v_mul_f32 v199, v199, v197\n"
    "v_accvgpr_write_b32 a76, v198\n"
    "v_accvgpr_write_b32 a77, v199\n"
    "v_accvgpr_read_b32 v198, a78\n"
    "v_accvgpr_read_b32 v199, a79\n"
    "v_mul_f32 v198, v198, v197\n"
    "v_mul_f32 v199, v199, v197\n"
    "v_accvgpr_write_b32 a78, v198\n"
    "v_accvgpr_write_b32 a79, v199\n"
    "v_accvgpr_read_b32 v198, a92\n"
    "v_accvgpr_read_b32 v199, a93\n"
    "v_mul_f32 v198, v198, v197\n"
    "v_mul_f32 v199, v199, v197\n"
    "v_accvgpr_write_b32 a92, v198\n"
    "v_accvgpr_write_b32 a93, v199\n"
    "v_accvgpr_read_b32 v198, a94\n"
    "v_accvgpr_read_b32 v199, a95\n"
    "v_mul_f32 v198, v198, v197\n"
    "v_mul_f32 v199, v199, v197\n"
    "v_accvgpr_write_b32 a94, v198\n"
    "v_accvgpr_write_b32 a95, v199\n"
    "v_accvgpr_read_b32 v198, a108\n"
    "v_accvgpr_read_b32 v199, a109\n"
    "v_mul_f32 v198, v198, v197\n"
    "v_mul_f32 v199, v199, v197\n"
    "v_accvgpr_write_b32 a108, v198\n"
    "v_accvgpr_write_b32 a109, v199\n"
    "v_accvgpr_read_b32 v198, a110\n"
    "v_accvgpr_read_b32 v199, a111\n"
    "v_mul_f32 v198, v198, v197\n"
    "v_mul_f32 v199, v199, v197\n"
    "v_accvgpr_write_b32 a110, v198\n"
    "v_accvgpr_write_b32 a111, v199\n"
    "v_accvgpr_read_b32 v198, a124\n"
    "v_accvgpr_read_b32 v199, a125\n"
    "v_mul_f32 v198, v198, v197\n"
    "v_mul_f32 v199, v199, v197\n"
    "v_accvgpr_write_b32 a124, v198\n"
    "v_accvgpr_write_b32 a125, v199\n"
    "v_accvgpr_read_b32 v198, a126\n"
    "v_accvgpr_read_b32 v199, a127\n"
    "v_mul_f32 v198, v198, v197\n"
    "v_mul_f32 v199, v199, v197\n"
    "v_accvgpr_write_b32 a126, v198\n"
    "v_accvgpr_write_b32 a127, v199\n"
    "v_accvgpr_read_b32 v198, a140\n"
    "v_accvgpr_read_b32 v199, a141\n"
    "v_mul_f32 v198, v198, v197\n"
    "v_mul_f32 v199, v199, v197\n"
    "v_accvgpr_write_b32 a140, v198\n"
    "v_accvgpr_write_b32 a141, v199\n"
    "v_accvgpr_read_b32 v198, a142\n"
    "v_accvgpr_read_b32 v199, a143\n"
    "v_mul_f32 v198, v198, v197\n"
    "v_mul_f32 v199, v199, v197\n"
    "v_accvgpr_write_b32 a142, v198\n"
    "v_accvgpr_write_b32 a143, v199\n"
    "v_accvgpr_read_b32 v198, a156\n"
    "v_accvgpr_read_b32 v199, a157\n"
    "v_mul_f32 v198, v198, v197\n"
    "v_mul_f32 v199, v199, v197\n"
    "v_accvgpr_write_b32 a156, v198\n"
    "v_accvgpr_write_b32 a157, v199\n"
    "v_accvgpr_read_b32 v198, a158\n"
    "v_accvgpr_read_b32 v199, a159\n"
    "v_mul_f32 v198, v198, v197\n"
    "v_mul_f32 v199, v199, v197\n"
    "v_accvgpr_write_b32 a158, v198\n"
    "v_accvgpr_write_b32 a159, v199\n"
    "v_accvgpr_read_b32 v198, a172\n"
    "v_accvgpr_read_b32 v199, a173\n"
    "v_mul_f32 v198, v198, v197\n"
    "v_mul_f32 v199, v199, v197\n"
    "v_accvgpr_write_b32 a172, v198\n"
    "v_accvgpr_write_b32 a173, v199\n"
    "v_accvgpr_read_b32 v198, a174\n"
    "v_accvgpr_read_b32 v199, a175\n"
    "v_mul_f32 v198, v198, v197\n"
    "v_mul_f32 v199, v199, v197\n"
    "v_accvgpr_write_b32 a174, v198\n"
    "v_accvgpr_write_b32 a175, v199\n"
    "v_accvgpr_read_b32 v198, a188\n"
    "v_accvgpr_read_b32 v199, a189\n"
    "v_mul_f32 v198, v198, v197\n"
    "v_mul_f32 v199, v199, v197\n"
    "v_accvgpr_write_b32 a188, v198\n"
    "v_accvgpr_write_b32 a189, v199\n"
    "v_accvgpr_read_b32 v198, a190\n"
    "v_accvgpr_read_b32 v199, a191\n"
    "v_mul_f32 v198, v198, v197\n"
    "v_mul_f32 v199, v199, v197\n"
    "v_accvgpr_write_b32 a190, v198\n"
    "v_accvgpr_write_b32 a191, v199\n"
    "v_exp_f32 v32, v32\n"
    "v_exp_f32 v33, v33\n"
    "v_exp_f32 v34, v34\n"
    "v_cvt_pk_bf16_f32 v128, v32, v33\n"
    "v_exp_f32 v35, v35\n"
    "v_exp_f32 v36, v36\n"
    "v_cvt_pk_bf16_f32 v129, v34, v35\n"
    "v_exp_f32 v37, v37\n"
    "v_exp_f32 v38, v38\n"
    "v_cvt_pk_bf16_f32 v132, v36, v37\n"
    "v_exp_f32 v39, v39\n"
    "v_exp_f32 v40, v40\n"
    "v_cvt_pk_bf16_f32 v133, v38, v39\n"
    "v_exp_f32 v41, v41\n"
    "v_exp_f32 v42, v42\n"
    "v_cvt_pk_bf16_f32 v136, v40, v41\n"
    "v_exp_f32 v43, v43\n"
    "v_exp_f32 v44, v44\n"
    "v_cvt_pk_bf16_f32 v137, v42, v43\n"
    "v_exp_f32 v45, v45\n"
    "v_exp_f32 v46, v46\n"
    "v_cvt_pk_bf16_f32 v140, v44, v45\n"
    "v_exp_f32 v47, v47\n"
    "v_exp_f32 v48, v48\n"
    "v_cvt_pk_bf16_f32 v141, v46, v47\n"
    "v_exp_f32 v49, v49\n"
    "v_exp_f32 v50, v50\n"
    "v_cvt_pk_bf16_f32 v130, v48, v49\n"
    "v_exp_f32 v51, v51\n"
    "v_exp_f32 v52, v52\n"
    "v_cvt_pk_bf16_f32 v131, v50, v51\n"
    "v_exp_f32 v53, v53\n"
    "v_exp_f32 v54, v54\n"
    "v_cvt_pk_bf16_f32 v134, v52, v53\n"
    "v_exp_f32 v55, v55\n"
    "v_exp_f32 v56, v56\n"
    "v_cvt_pk_bf16_f32 v135, v54, v55\n"
    "v_exp_f32 v57, v57\n"
    "v_exp_f32 v58, v58\n"
    "v_cvt_pk_bf16_f32 v138, v56, v57\n"
    "v_exp_f32 v59, v59\n"
    "v_exp_f32 v60, v60\n"
    "v_cvt_pk_bf16_f32 v139, v58, v59\n"
    "v_exp_f32 v61, v61\n"
    "v_exp_f32 v62, v62\n"
    "v_cvt_pk_bf16_f32 v142, v60, v61\n"
    "v_exp_f32 v63, v63\n"
    "s_nop 0\n"
    "v_cvt_pk_bf16_f32 v143, v62, v63\n"
    "v_exp_f32 v64, v64\n"
    "v_exp_f32 v65, v65\n"
    "v_exp_f32 v66, v66\n"
    "v_cvt_pk_bf16_f32 v144, v64, v65\n"
    "v_exp_f32 v67, v67\n"
    "v_exp_f32 v68, v68\n"
    "v_cvt_pk_bf16_f32 v145, v66, v67\n"
    "v_exp_f32 v69, v69\n"
    "v_exp_f32 v70, v70\n"
    "v_cvt_pk_bf16_f32 v148, v68, v69\n"
    "v_exp_f32 v71, v71\n"
    "v_exp_f32 v72, v72\n"
    "v_cvt_pk_bf16_f32 v149, v70, v71\n"
    "v_exp_f32 v73, v73\n"
    "v_exp_f32 v74, v74\n"
    "v_cvt_pk_bf16_f32 v152, v72, v73\n"
    "v_exp_f32 v75, v75\n"
    "v_exp_f32 v76, v76\n"
    "v_cvt_pk_bf16_f32 v153, v74, v75\n"
    "v_exp_f32 v77, v77\n"
    "v_exp_f32 v78, v78\n"
    "v_cvt_pk_bf16_f32 v156, v76, v77\n"
    "v_exp_f32 v79, v79\n"
    "v_exp_f32 v80, v80\n"
    "v_cvt_pk_bf16_f32 v157, v78, v79\n"
    "v_exp_f32 v81, v81\n"
    "v_exp_f32 v82, v82\n"
    "v_cvt_pk_bf16_f32 v146, v80, v81\n"
    "v_exp_f32 v83, v83\n"
    "v_exp_f32 v84, v84\n"
    "v_cvt_pk_bf16_f32 v147, v82, v83\n"
    "v_exp_f32 v85, v85\n"
    "v_exp_f32 v86, v86\n"
    "v_cvt_pk_bf16_f32 v150, v84, v85\n"
    "v_exp_f32 v87, v87\n"
    "v_exp_f32 v88, v88\n"
    "v_cvt_pk_bf16_f32 v151, v86, v87\n"
    "v_exp_f32 v89, v89\n"
    "v_exp_f32 v90, v90\n"
    "v_cvt_pk_bf16_f32 v154, v88, v89\n"
    "v_exp_f32 v91, v91\n"
    "v_exp_f32 v92, v92\n"
    "v_cvt_pk_bf16_f32 v155, v90, v91\n"
    "v_exp_f32 v93, v93\n"
    "v_exp_f32 v94, v94\n"
    "v_cvt_pk_bf16_f32 v158, v92, v93\n"
    "v_exp_f32 v95, v95\n"
    "s_nop 0\n"
    "v_cvt_pk_bf16_f32 v159, v94, v95\n"
    "ds_read_b128 v[160:163], v205 offset:12288\n"
    "ds_read_b128 v[164:167], v205 offset:13312\n"
    "s_waitcnt lgkmcnt(1)\n"
    "v_mfma_f32_16x16x32_bf16 a[0:3], v[160:163], v[128:131], a[0:3]\n"
    "s_waitcnt lgkmcnt(0)\n"
    "v_mfma_f32_16x16x32_bf16 a[0:3], v[164:167], v[144:147], a[0:3]\n"
    "v_mfma_f32_16x16x32_bf16 a[4:7], v[160:163], v[132:135], a[4:7]\n"
    "v_mfma_f32_16x16x32_bf16 a[4:7], v[164:167], v[148:151], a[4:7]\n"
    "v_mfma_f32_16x16x32_bf16 a[8:11], v[160:163], v[136:139], a[8:11]\n"
    "v_mfma_f32_16x16x32_bf16 a[8:11], v[164:167], v[152:155], a[8:11]\n"
    "v_mfma_f32_16x16x32_bf16 a[12:15], v[160:163], v[140:143], a[12:15]\n"
    "v_mfma_f32_16x16x32_bf16 a[12:15], v[164:167], v[156:159], a[12:15]\n"
    "ds_read_b128 v[168:171], v205 offset:14336\n"
    "ds_read_b128 v[172:175], v205 offset:15360\n"
    "s_waitcnt lgkmcnt(1)\n"
    "v_mfma_f32_16x16x32_bf16 a[28:31], v[168:171], v[140:143], a[28:31]\n"
    "s_waitcnt lgkmcnt(0)\n"
    "v_mfma_f32_16x16x32_bf16 a[28:31], v[172:175], v[156:159], a[28:31]\n"
    "v_mfma_f32_16x16x32_bf16 a[24:27], v[172:175], v[152:155], a[24:27]\n"
    "v_mfma_f32_16x16x32_bf16 a[24:27], v[168:171], v[136:139], a[24:27]\n"
    "v_mfma_f32_16x16x32_bf16 a[20:23], v[168:171], v[132:135], a[20:23]\n"
    "v_mfma_f32_16x16x32_bf16 a[20:23], v[172:175], v[148:151], a[20:23]\n"
    "v_mfma_f32_16x16x32_bf16 a[16:19], v[172:175], v[144:147], a[16:19]\n"
    "v_mfma_f32_16x16x32_bf16 a[16:19], v[168:171], v[128:131], a[16:19]\n"
    "ds_read_b128 v[176:179], v205 offset:16384\n"
    "ds_read_b128 v[180:183], v205 offset:17408\n"
    "s_waitcnt lgkmcnt(1)\n"
    "v_mfma_f32_16x16x32_bf16 a[32:35], v[176:179], v[128:131], a[32:35]\n"
    "s_waitcnt lgkmcnt(0)\n"
    "v_mfma_f32_16x16x32_bf16 a[32:35], v[180:183], v[144:147], a[32:35]\n"
    "v_mfma_f32_16x16x32_bf16 a[36:39], v[180:183], v[148:151], a[36:39]\n"
    "v_mfma_f32_16x16x32_bf16 a[36:39], v[176:179], v[132:135], a[36:39]\n"
    "v_mfma_f32_16x16x32_bf16 a[40:43], v[176:179], v[136:139], a[40:43]\n"
    "v_mfma_f32_16x16x32_bf16 a[40:43], v[180:183], v[152:155], a[40:43]\n"
    "v_mfma_f32_16x16x32_bf16 a[44:47], v[180:183], v[156:159], a[44:47]\n"
    "v_mfma_f32_16x16x32_bf16 a[44:47], v[176:179], v[140:143], a[44:47]\n"
    "ds_read_b128 v[160:163], v205 offset:18432\n"
    "ds_read_b128 v[164:167], v205 offset:19456\n"
    "s_waitcnt lgkmcnt(1)\n"
    "v_mfma_f32_16x16x32_bf16 a[60:63], v[160:163], v[140:143], a[60:63]\n"
    "s_waitcnt lgkmcnt(0)\n"
    "v_mfma_f32_16x16x32_bf16 a[60:63], v[164:167], v[156:159], a[60:63]\n"
    "v_mfma_f32_16x16x32_bf16 a[56:59], v[164:167], v[152:155], a[56:59]\n"
    "v_mfma_f32_16x16x32_bf16 a[56:59], v[160:163], v[136:139], a[56:59]\n"
    "v_mfma_f32_16x16x32_bf16 a[52:55], v[160:163], v[132:135], a[52:55]\n"
    "v_mfma_f32_16x16x32_bf16 a[52:55], v[164:167], v[148:151], a[52:55]\n"
    "v_mfma_f32_16x16x32_bf16 a[48:51], v[164:167], v[144:147], a[48:51]\n"
    "v_mfma_f32_16x16x32_bf16 a[48:51], v[160:163], v[128:131], a[48:51]\n"
    "ds_read_b128 v[168:171], v205 offset:20480\n"
    "ds_read_b128 v[172:175], v205 offset:21504\n"
    "s_waitcnt lgkmcnt(1)\n"
    "v_mfma_f32_16x16x32_bf16 a[64:67], v[168:171], v[128:131], a[64:67]\n"
    "s_waitcnt lgkmcnt(0)\n"
    "v_mfma_f32_16x16x32_bf16 a[64:67], v[172:175], v[144:147], a[64:67]\n"
    "v_mfma_f32_16x16x32_bf16 a[68:71], v[172:175], v[148:151], a[68:71]\n"
    "v_mfma_f32_16x16x32_bf16 a[68:71], v[168:171], v[132:135], a[68:71]\n"
    "v_mfma_f32_16x16x32_bf16 a[72:75], v[168:171], v[136:139], a[72:75]\n"
    "v_mfma_f32_16x16x32_bf16 a[72:75], v[172:175], v[152:155], a[72:75]\n"
    "v_mfma_f32_16x16x32_bf16 a[76:79], v[172:175], v[156:159], a[76:79]\n"
    "v_mfma_f32_16x16x32_bf16 a[76:79], v[168:171], v[140:143], a[76:79]\n"
    "ds_read_b128 v[176:179], v205 offset:22528\n"
    "ds_read_b128 v[180:183], v205 offset:23552\n"
    "s_waitcnt lgkmcnt(1)\n"
    "v_mfma_f32_16x16x32_bf16 a[92:95], v[176:179], v[140:143], a[92:95]\n"
    "s_waitcnt lgkmcnt(0)\n"
    "v_mfma_f32_16x16x32_bf16 a[92:95], v[180:183], v[156:159], a[92:95]\n"
    "v_mfma_f32_16x16x32_bf16 a[88:91], v[180:183], v[152:155], a[88:91]\n"
    "v_mfma_f32_16x16x32_bf16 a[88:91], v[176:179], v[136:139], a[88:91]\n"
    "v_mfma_f32_16x16x32_bf16 a[84:87], v[176:179], v[132:135], a[84:87]\n"
    "v_mfma_f32_16x16x32_bf16 a[84:87], v[180:183], v[148:151], a[84:87]\n"
    "v_mfma_f32_16x16x32_bf16 a[80:83], v[180:183], v[144:147], a[80:83]\n"
    "v_mfma_f32_16x16x32_bf16 a[80:83], v[176:179], v[128:131], a[80:83]\n"
    "ds_read_b128 v[160:163], v205 offset:24576\n"
    "ds_read_b128 v[164:167], v205 offset:25600\n"
    "s_waitcnt lgkmcnt(1)\n"
    "v_mfma_f32_16x16x32_bf16 a[96:99], v[160:163], v[128:131], a[96:99]\n"
    "s_waitcnt lgkmcnt(0)\n"
    "v_mfma_f32_16x16x32_bf16 a[96:99], v[164:167], v[144:147], a[96:99]\n"
    "v_mfma_f32_16x16x32_bf16 a[100:103], v[164:167], v[148:151], a[100:103]\n"
    "v_mfma_f32_16x16x32_bf16 a[100:103], v[160:163], v[132:135], a[100:103]\n"
    "v_mfma_f32_16x16x32_bf16 a[104:107], v[160:163], v[136:139], a[104:107]\n"
    "v_mfma_f32_16x16x32_bf16 a[104:107], v[164:167], v[152:155], a[104:107]\n"
    "v_mfma_f32_16x16x32_bf16 a[108:111], v[164:167], v[156:159], a[108:111]\n"
    "v_mfma_f32_16x16x32_bf16 a[108:111], v[160:163], v[140:143], a[108:111]\n"
    "ds_read_b128 v[168:171], v205 offset:26624\n"
    "ds_read_b128 v[172:175], v205 offset:27648\n"
    "s_waitcnt lgkmcnt(1)\n"
    "v_mfma_f32_16x16x32_bf16 a[124:127], v[168:171], v[140:143], a[124:127]\n"
    "s_waitcnt lgkmcnt(0)\n"
    "v_mfma_f32_16x16x32_bf16 a[124:127], v[172:175], v[156:159], a[124:127]\n"
    "v_mfma_f32_16x16x32_bf16 a[120:123], v[172:175], v[152:155], a[120:123]\n"
    "v_mfma_f32_16x16x32_bf16 a[120:123], v[168:171], v[136:139], a[120:123]\n"
    "v_mfma_f32_16x16x32_bf16 a[116:119], v[168:171], v[132:135], a[116:119]\n"
    "v_mfma_f32_16x16x32_bf16 a[116:119], v[172:175], v[148:151], a[116:119]\n"
    "v_mfma_f32_16x16x32_bf16 a[112:115], v[172:175], v[144:147], a[112:115]\n"
    "v_mfma_f32_16x16x32_bf16 a[112:115], v[168:171], v[128:131], a[112:115]\n"
    "ds_read_b128 v[176:179], v205 offset:28672\n"
    "ds_read_b128 v[180:183], v205 offset:29696\n"
    "s_waitcnt lgkmcnt(1)\n"
    "v_mfma_f32_16x16x32_bf16 a[128:131], v[176:179], v[128:131], a[128:131]\n"
    "s_waitcnt lgkmcnt(0)\n"
    "v_mfma_f32_16x16x32_bf16 a[128:131], v[180:183], v[144:147], a[128:131]\n"
    "v_mfma_f32_16x16x32_bf16 a[132:135], v[180:183], v[148:151], a[132:135]\n"
    "v_mfma_f32_16x16x32_bf16 a[132:135], v[176:179], v[132:135], a[132:135]\n"
    "v_mfma_f32_16x16x32_bf16 a[136:139], v[176:179], v[136:139], a[136:139]\n"
    "v_mfma_f32_16x16x32_bf16 a[136:139], v[180:183], v[152:155], a[136:139]\n"
    "v_mfma_f32_16x16x32_bf16 a[140:143], v[180:183], v[156:159], a[140:143]\n"
    "v_mfma_f32_16x16x32_bf16 a[140:143], v[176:179], v[140:143], a[140:143]\n"
    "ds_read_b128 v[160:163], v205 offset:30720\n"
    "ds_read_b128 v[164:167], v205 offset:31744\n"
    "s_waitcnt lgkmcnt(1)\n"
    "v_mfma_f32_16x16x32_bf16 a[156:159], v[160:163], v[140:143], a[156:159]\n"
    "s_waitcnt lgkmcnt(0)\n"
    "v_mfma_f32_16x16x32_bf16 a[156:159], v[164:167], v[156:159], a[156:159]\n"
    "v_mfma_f32_16x16x32_bf16 a[152:155], v[164:167], v[152:155], a[152:155]\n"
    "v_mfma_f32_16x16x32_bf16 a[152:155], v[160:163], v[136:139], a[152:155]\n"
    "v_mfma_f32_16x16x32_bf16 a[148:151], v[160:163], v[132:135], a[148:151]\n"
    "v_mfma_f32_16x16x32_bf16 a[148:151], v[164:167], v[148:151], a[148:151]\n"
    "v_mfma_f32_16x16x32_bf16 a[144:147], v[164:167], v[144:147], a[144:147]\n"
    "v_mfma_f32_16x16x32_bf16 a[144:147], v[160:163], v[128:131], a[144:147]\n"
    "ds_read_b128 v[168:171], v205 offset:32768\n"
    "ds_read_b128 v[172:175], v205 offset:33792\n"
    "s_waitcnt lgkmcnt(1)\n"
    "v_mfma_f32_16x16x32_bf16 a[160:163], v[168:171], v[128:131], a[160:163]\n"
    "s_waitcnt lgkmcnt(0)\n"
    "v_mfma_f32_16x16x32_bf16 a[160:163], v[172:175], v[144:147], a[160:163]\n"
    "v_mfma_f32_16x16x32_bf16 a[164:167], v[172:175], v[148:151], a[164:167]\n"
    "v_mfma_f32_16x16x32_bf16 a[164:167], v[168:171], v[132:135], a[164:167]\n"
    "v_mfma_f32_16x16x32_bf16 a[168:171], v[168:171], v[136:139], a[168:171]\n"
    "v_mfma_f32_16x16x32_bf16 a[168:171], v[172:175], v[152:155], a[168:171]\n"
    "v_mfma_f32_16x16x32_bf16 a[172:175], v[172:175], v[156:159], a[172:175]\n"
    "v_mfma_f32_16x16x32_bf16 a[172:175], v[168:171], v[140:143], a[172:175]\n"
    "ds_read_b128 v[176:179], v205 offset:34816\n"
    "ds_read_b128 v[180:183], v205 offset:35840\n"
    "s_waitcnt lgkmcnt(1)\n"
    "v_mfma_f32_16x16x32_bf16 a[188:191], v[176:179], v[140:143], a[188:191]\n"
    "s_waitcnt lgkmcnt(0)\n"
    "v_mfma_f32_16x16x32_bf16 a[188:191], v[180:183], v[156:159], a[188:191]\n"
    "v_mfma_f32_16x16x32_bf16 a[184:187], v[176:179], v[136:139], a[184:187]\n"
    "v_mfma_f32_16x16x32_bf16 a[184:187], v[180:183], v[152:155], a[184:187]\n"
    "v_mfma_f32_16x16x32_bf16 a[180:183], v[176:179], v[132:135], a[180:183]\n"
    "v_mfma_f32_16x16x32_bf16 a[180:183], v[180:183], v[148:151], a[180:183]\n"
    "v_mfma_f32_16x16x32_bf16 a[176:179], v[176:179], v[128:131], a[176:179]\n"
    "v_mfma_f32_16x16x32_bf16 a[176:179], v[180:183], v[144:147], a[176:179]\n"
    "v_mfma_f32_16x16x32_bf16 v[112:115], v[192:195], v[128:131], v[112:115]\n"
    "v_mfma_f32_16x16x32_bf16 v[112:115], v[192:195], v[144:147], v[112:115]\n"
    "v_mfma_f32_16x16x32_bf16 v[116:119], v[192:195], v[132:135], v[116:119]\n"
    "v_mfma_f32_16x16x32_bf16 v[116:119], v[192:195], v[148:151], v[116:119]\n"
    "v_mfma_f32_16x16x32_bf16 v[120:123], v[192:195], v[136:139], v[120:123]\n"
    "v_mfma_f32_16x16x32_bf16 v[120:123], v[192:195], v[152:155], v[120:123]\n"
    "v_mfma_f32_16x16x32_bf16 v[124:127], v[192:195], v[140:143], v[124:127]\n"
    "v_mfma_f32_16x16x32_bf16 v[124:127], v[192:195], v[156:159], v[124:127]\n"
    "s_nop 15\n"
    "s_add_u32 s63, s63, 0x6000\n"
    "s_cmp_lt_u32 s63, 0xc0000\n"
    "s_cbranch_scc1 LSLOOP\n"
    "s_branch LEPI\n"
    "LEPI:\n"
    "s_lshl_b32 s76, s56, 3\n"
    "s_add_u32 s76, s76, s57\n"
    "s_mul_i32 s76, s76, 0x18000\n"
    "s_add_u32 s76, s76, s88\n"
    "s_add_u32 s28, s24, s76\n"
    "s_addc_u32 s29, s25, 0\n"
    "s_and_b32 s29, s29, 0xffff\n"
    "s_mov_b32 s30, 0x6000\n"
    "s_mov_b32 s31, 0x20000\n"
    "s_xor_b32 s94, s70, 0x80000000\n"
    "s_xor_b32 s95, s70, 0x80000000\n"
    "s_mov_b32 s62, 0x1800\n"
    "s_mov_b32 s63, 0x3000\n"
    "s_mov_b32 s64, 0x4800\n"
    "v_and_b32 v198, 15, v231\n"
    "v_mul_u32_u24 v205, 0x180, v198\n"
    "v_lshrrev_b32 v199, 4, v231\n"
    "v_lshl_add_u32 v205, v199, 4, v205\n"
    "v_mul_f32 v222, s68, v112\n"
    "v_rcp_f32 v222, v222\n"
    "v_mul_f32 v224, s68, v116\n"
    "v_rcp_f32 v224, v224\n"
    "v_mul_f32 v226, s68, v120\n"
    "v_rcp_f32 v226, v226\n"
    "v_mul_f32 v228, s68, v124\n"
    "v_rcp_f32 v228, v228\n"
    "v_mov_b32 v223, v222\n"
    "v_mov_b32 v225, v224\n"
    "v_mov_b32 v227, v226\n"
    "v_mov_b32 v229, v228\n"
    "v_accvgpr_read_b32 v96, a0\n"
    "v_accvgpr_read_b32 v97, a1\n"
    "v_accvgpr_read_b32 v98, a2\n"
    "v_accvgpr_read_b32 v99, a3\n"
    "v_pk_fma_f32 v[96:97], v[96:97], v[222:223], s[94:95]\n"
    "v_pk_fma_f32 v[98:99], v[98:99], v[222:223], s[94:95]\n"
    "v_cvt_pk_bf16_f32 v32, v96, v97\n"
    "v_cvt_pk_bf16_f32 v33, v98, v99\n"
    "v_accvgpr_read_b32 v100, a16\n"
    "v_accvgpr_read_b32 v101, a17\n"
    "v_accvgpr_read_b32 v102, a18\n"
    "v_accvgpr_read_b32 v103, a19\n"
    "v_pk_fma_f32 v[100:101], v[100:101], v[222:223], s[94:95]\n"
    "v_pk_fma_f32 v[102:103], v[102:103], v[222:223], s[94:95]\n"
    "v_cvt_pk_bf16_f32 v34, v100, v101\n"
    "v_cvt_pk_bf16_f32 v35, v102, v103\n"
    "buffer_store_dwordx4 v[32:35], v205, s[28:31], 0 offen sc0 sc1\n"
    "v_accvgpr_read_b32 v104, a32\n"
    "v_accvgpr_read_b32 v105, a33\n"
    "v_accvgpr_read_b32 v106, a34\n"
    "v_accvgpr_read_b32 v107, a35\n"
    "v_pk_fma_f32 v[104:105], v[104:105], v[222:223], s[94:95]\n"
    "v_pk_fma_f32 v[106:107], v[106:107], v[222:223], s[94:95]\n"
    "v_cvt_pk_bf16_f32 v36, v104, v105\n"
    "v_cvt_pk_bf16_f32 v37, v106, v107\n"
    "v_accvgpr_read_b32 v108, a48\n"
    "v_accvgpr_read_b32 v109, a49\n"
    "v_accvgpr_read_b32 v110, a50\n"
    "v_accvgpr_read_b32 v111, a51\n"
    "v_pk_fma_f32 v[108:109], v[108:109], v[222:223], s[94:95]\n"
    "v_pk_fma_f32 v[110:111], v[110:111], v[222:223], s[94:95]\n"
    "v_cvt_pk_bf16_f32 v38, v108, v109\n"
    "v_cvt_pk_bf16_f32 v39, v110, v111\n"
    "buffer_store_dwordx4 v[36:39], v205, s[28:31], 0 offen offset:64 sc0 sc1\n"
    "v_accvgpr_read_b32 v96, a64\n"
    "v_accvgpr_read_b32 v97, a65\n"
    "v_accvgpr_read_b32 v98, a66\n"
    "v_accvgpr_read_b32 v99, a67\n"
    "v_pk_fma_f32 v[96:97], v[96:97], v[222:223], s[94:95]\n"
    "v_pk_fma_f32 v[98:99], v[98:99], v[222:223], s[94:95]\n"
    "v_cvt_pk_bf16_f32 v40, v96, v97\n"
    "v_cvt_pk_bf16_f32 v41, v98, v99\n"
    "v_accvgpr_read_b32 v100, a80\n"
    "v_accvgpr_read_b32 v101, a81\n"
    "v_accvgpr_read_b32 v102, a82\n"
    "v_accvgpr_read_b32 v103, a83\n"
    "v_pk_fma_f32 v[100:101], v[100:101], v[222:223], s[94:95]\n"
    "v_pk_fma_f32 v[102:103], v[102:103], v[222:223], s[94:95]\n"
    "v_cvt_pk_bf16_f32 v42, v100, v101\n"
    "v_cvt_pk_bf16_f32 v43, v102, v103\n"
    "buffer_store_dwordx4 v[40:43], v205, s[28:31], 0 offen offset:128 sc0 sc1\n"
    "v_accvgpr_read_b32 v104, a96\n"
    "v_accvgpr_read_b32 v105, a97\n"
    "v_accvgpr_read_b32 v106, a98\n"
    "v_accvgpr_read_b32 v107, a99\n"
    "v_pk_fma_f32 v[104:105], v[104:105], v[222:223], s[94:95]\n"
    "v_pk_fma_f32 v[106:107], v[106:107], v[222:223], s[94:95]\n"
    "v_cvt_pk_bf16_f32 v44, v104, v105\n"
    "v_cvt_pk_bf16_f32 v45, v106, v107\n"
    "v_accvgpr_read_b32 v108, a112\n"
    "v_accvgpr_read_b32 v109, a113\n"
    "v_accvgpr_read_b32 v110, a114\n"
    "v_accvgpr_read_b32 v111, a115\n"
    "v_pk_fma_f32 v[108:109], v[108:109], v[222:223], s[94:95]\n"
    "v_pk_fma_f32 v[110:111], v[110:111], v[222:223], s[94:95]\n"
    "v_cvt_pk_bf16_f32 v46, v108, v109\n"
    "v_cvt_pk_bf16_f32 v47, v110, v111\n"
    "buffer_store_dwordx4 v[44:47], v205, s[28:31], 0 offen offset:192 sc0 sc1\n"
    "v_accvgpr_read_b32 v96, a128\n"
    "v_accvgpr_read_b32 v97, a129\n"
    "v_accvgpr_read_b32 v98, a130\n"
    "v_accvgpr_read_b32 v99, a131\n"
    "v_pk_fma_f32 v[96:97], v[96:97], v[222:223], s[94:95]\n"
    "v_pk_fma_f32 v[98:99], v[98:99], v[222:223], s[94:95]\n"
    "v_cvt_pk_bf16_f32 v48, v96, v97\n"
    "v_cvt_pk_bf16_f32 v49, v98, v99\n"
    "v_accvgpr_read_b32 v100, a144\n"
    "v_accvgpr_read_b32 v101, a145\n"
    "v_accvgpr_read_b32 v102, a146\n"
    "v_accvgpr_read_b32 v103, a147\n"
    "v_pk_fma_f32 v[100:101], v[100:101], v[222:223], s[94:95]\n"
    "v_pk_fma_f32 v[102:103], v[102:103], v[222:223], s[94:95]\n"
    "v_cvt_pk_bf16_f32 v50, v100, v101\n"
    "v_cvt_pk_bf16_f32 v51, v102, v103\n"
    "buffer_store_dwordx4 v[48:51], v205, s[28:31], 0 offen offset:256 sc0 sc1\n"
    "v_accvgpr_read_b32 v104, a160\n"
    "v_accvgpr_read_b32 v105, a161\n"
    "v_accvgpr_read_b32 v106, a162\n"
    "v_accvgpr_read_b32 v107, a163\n"
    "v_pk_fma_f32 v[104:105], v[104:105], v[222:223], s[94:95]\n"
    "v_pk_fma_f32 v[106:107], v[106:107], v[222:223], s[94:95]\n"
    "v_cvt_pk_bf16_f32 v52, v104, v105\n"
    "v_cvt_pk_bf16_f32 v53, v106, v107\n"
    "v_accvgpr_read_b32 v108, a176\n"
    "v_accvgpr_read_b32 v109, a177\n"
    "v_accvgpr_read_b32 v110, a178\n"
    "v_accvgpr_read_b32 v111, a179\n"
    "v_pk_fma_f32 v[108:109], v[108:109], v[222:223], s[94:95]\n"
    "v_pk_fma_f32 v[110:111], v[110:111], v[222:223], s[94:95]\n"
    "v_cvt_pk_bf16_f32 v54, v108, v109\n"
    "v_cvt_pk_bf16_f32 v55, v110, v111\n"
    "buffer_store_dwordx4 v[52:55], v205, s[28:31], 0 offen offset:320 sc0 sc1\n"
    "v_accvgpr_read_b32 v96, a4\n"
    "v_accvgpr_read_b32 v97, a5\n"
    "v_accvgpr_read_b32 v98, a6\n"
    "v_accvgpr_read_b32 v99, a7\n"
    "v_pk_fma_f32 v[96:97], v[96:97], v[224:225], s[94:95]\n"
    "v_pk_fma_f32 v[98:99], v[98:99], v[224:225], s[94:95]\n"
    "v_cvt_pk_bf16_f32 v56, v96, v97\n"
    "v_cvt_pk_bf16_f32 v57, v98, v99\n"
    "v_accvgpr_read_b32 v100, a20\n"
    "v_accvgpr_read_b32 v101, a21\n"
    "v_accvgpr_read_b32 v102, a22\n"
    "v_accvgpr_read_b32 v103, a23\n"
    "v_pk_fma_f32 v[100:101], v[100:101], v[224:225], s[94:95]\n"
    "v_pk_fma_f32 v[102:103], v[102:103], v[224:225], s[94:95]\n"
    "v_cvt_pk_bf16_f32 v58, v100, v101\n"
    "v_cvt_pk_bf16_f32 v59, v102, v103\n"
    "buffer_store_dwordx4 v[56:59], v205, s[28:31], s62 offen sc0 sc1\n"
    "v_accvgpr_read_b32 v104, a36\n"
    "v_accvgpr_read_b32 v105, a37\n"
    "v_accvgpr_read_b32 v106, a38\n"
    "v_accvgpr_read_b32 v107, a39\n"
    "v_pk_fma_f32 v[104:105], v[104:105], v[224:225], s[94:95]\n"
    "v_pk_fma_f32 v[106:107], v[106:107], v[224:225], s[94:95]\n"
    "v_cvt_pk_bf16_f32 v60, v104, v105\n"
    "v_cvt_pk_bf16_f32 v61, v106, v107\n"
    "v_accvgpr_read_b32 v108, a52\n"
    "v_accvgpr_read_b32 v109, a53\n"
    "v_accvgpr_read_b32 v110, a54\n"
    "v_accvgpr_read_b32 v111, a55\n"
    "v_pk_fma_f32 v[108:109], v[108:109], v[224:225], s[94:95]\n"
    "v_pk_fma_f32 v[110:111], v[110:111], v[224:225], s[94:95]\n"
    "v_cvt_pk_bf16_f32 v62, v108, v109\n"
    "v_cvt_pk_bf16_f32 v63, v110, v111\n"
    "buffer_store_dwordx4 v[60:63], v205, s[28:31], s62 offen offset:64 sc0 sc1\n"
    "v_accvgpr_read_b32 v96, a68\n"
    "v_accvgpr_read_b32 v97, a69\n"
    "v_accvgpr_read_b32 v98, a70\n"
    "v_accvgpr_read_b32 v99, a71\n"
    "v_pk_fma_f32 v[96:97], v[96:97], v[224:225], s[94:95]\n"
    "v_pk_fma_f32 v[98:99], v[98:99], v[224:225], s[94:95]\n"
    "v_cvt_pk_bf16_f32 v32, v96, v97\n"
    "v_cvt_pk_bf16_f32 v33, v98, v99\n"
    "v_accvgpr_read_b32 v100, a84\n"
    "v_accvgpr_read_b32 v101, a85\n"
    "v_accvgpr_read_b32 v102, a86\n"
    "v_accvgpr_read_b32 v103, a87\n"
    "v_pk_fma_f32 v[100:101], v[100:101], v[224:225], s[94:95]\n"
    "v_pk_fma_f32 v[102:103], v[102:103], v[224:225], s[94:95]\n"
    "v_cvt_pk_bf16_f32 v34, v100, v101\n"
    "v_cvt_pk_bf16_f32 v35, v102, v103\n"
    "buffer_store_dwordx4 v[32:35], v205, s[28:31], s62 offen offset:128 sc0 sc1\n"
    "v_accvgpr_read_b32 v104, a100\n"
    "v_accvgpr_read_b32 v105, a101\n"
    "v_accvgpr_read_b32 v106, a102\n"
    "v_accvgpr_read_b32 v107, a103\n"
    "v_pk_fma_f32 v[104:105], v[104:105], v[224:225], s[94:95]\n"
    "v_pk_fma_f32 v[106:107], v[106:107], v[224:225], s[94:95]\n"
    "v_cvt_pk_bf16_f32 v36, v104, v105\n"
    "v_cvt_pk_bf16_f32 v37, v106, v107\n"
    "v_accvgpr_read_b32 v108, a116\n"
    "v_accvgpr_read_b32 v109, a117\n"
    "v_accvgpr_read_b32 v110, a118\n"
    "v_accvgpr_read_b32 v111, a119\n"
    "v_pk_fma_f32 v[108:109], v[108:109], v[224:225], s[94:95]\n"
    "v_pk_fma_f32 v[110:111], v[110:111], v[224:225], s[94:95]\n"
    "v_cvt_pk_bf16_f32 v38, v108, v109\n"
    "v_cvt_pk_bf16_f32 v39, v110, v111\n"
    "buffer_store_dwordx4 v[36:39], v205, s[28:31], s62 offen offset:192 sc0 sc1\n"
    "v_accvgpr_read_b32 v96, a132\n"
    "v_accvgpr_read_b32 v97, a133\n"
    "v_accvgpr_read_b32 v98, a134\n"
    "v_accvgpr_read_b32 v99, a135\n"
    "v_pk_fma_f32 v[96:97], v[96:97], v[224:225], s[94:95]\n"
    "v_pk_fma_f32 v[98:99], v[98:99], v[224:225], s[94:95]\n"
    "v_cvt_pk_bf16_f32 v40, v96, v97\n"
    "v_cvt_pk_bf16_f32 v41, v98, v99\n"
    "v_accvgpr_read_b32 v100, a148\n"
    "v_accvgpr_read_b32 v101, a149\n"
    "v_accvgpr_read_b32 v102, a150\n"
    "v_accvgpr_read_b32 v103, a151\n"
    "v_pk_fma_f32 v[100:101], v[100:101], v[224:225], s[94:95]\n"
    "v_pk_fma_f32 v[102:103], v[102:103], v[224:225], s[94:95]\n"
    "v_cvt_pk_bf16_f32 v42, v100, v101\n"
    "v_cvt_pk_bf16_f32 v43, v102, v103\n"
    "buffer_store_dwordx4 v[40:43], v205, s[28:31], s62 offen offset:256 sc0 sc1\n"
    "v_accvgpr_read_b32 v104, a164\n"
    "v_accvgpr_read_b32 v105, a165\n"
    "v_accvgpr_read_b32 v106, a166\n"
    "v_accvgpr_read_b32 v107, a167\n"
    "v_pk_fma_f32 v[104:105], v[104:105], v[224:225], s[94:95]\n"
    "v_pk_fma_f32 v[106:107], v[106:107], v[224:225], s[94:95]\n"
    "v_cvt_pk_bf16_f32 v44, v104, v105\n"
    "v_cvt_pk_bf16_f32 v45, v106, v107\n"
    "v_accvgpr_read_b32 v108, a180\n"
    "v_accvgpr_read_b32 v109, a181\n"
    "v_accvgpr_read_b32 v110, a182\n"
    "v_accvgpr_read_b32 v111, a183\n"
    "v_pk_fma_f32 v[108:109], v[108:109], v[224:225], s[94:95]\n"
    "v_pk_fma_f32 v[110:111], v[110:111], v[224:225], s[94:95]\n"
    "v_cvt_pk_bf16_f32 v46, v108, v109\n"
    "v_cvt_pk_bf16_f32 v47, v110, v111\n"
    "buffer_store_dwordx4 v[44:47], v205, s[28:31], s62 offen offset:320 sc0 sc1\n"
    "v_accvgpr_read_b32 v96, a8\n"
    "v_accvgpr_read_b32 v97, a9\n"
    "v_accvgpr_read_b32 v98, a10\n"
    "v_accvgpr_read_b32 v99, a11\n"
    "v_pk_fma_f32 v[96:97], v[96:97], v[226:227], s[94:95]\n"
    "v_pk_fma_f32 v[98:99], v[98:99], v[226:227], s[94:95]\n"
    "v_cvt_pk_bf16_f32 v48, v96, v97\n"
    "v_cvt_pk_bf16_f32 v49, v98, v99\n"
    "v_accvgpr_read_b32 v100, a24\n"
    "v_accvgpr_read_b32 v101, a25\n"
    "v_accvgpr_read_b32 v102, a26\n"
    "v_accvgpr_read_b32 v103, a27\n"
    "v_pk_fma_f32 v[100:101], v[100:101], v[226:227], s[94:95]\n"
    "v_pk_fma_f32 v[102:103], v[102:103], v[226:227], s[94:95]\n"
    "v_cvt_pk_bf16_f32 v50, v100, v101\n"
    "v_cvt_pk_bf16_f32 v51, v102, v103\n"
    "buffer_store_dwordx4 v[48:51], v205, s[28:31], s63 offen sc0 sc1\n"
    "v_accvgpr_read_b32 v104, a40\n"
    "v_accvgpr_read_b32 v105, a41\n"
    "v_accvgpr_read_b32 v106, a42\n"
    "v_accvgpr_read_b32 v107, a43\n"
    "v_pk_fma_f32 v[104:105], v[104:105], v[226:227], s[94:95]\n"
    "v_pk_fma_f32 v[106:107], v[106:107], v[226:227], s[94:95]\n"
    "v_cvt_pk_bf16_f32 v52, v104, v105\n"
    "v_cvt_pk_bf16_f32 v53, v106, v107\n"
    "v_accvgpr_read_b32 v108, a56\n"
    "v_accvgpr_read_b32 v109, a57\n"
    "v_accvgpr_read_b32 v110, a58\n"
    "v_accvgpr_read_b32 v111, a59\n"
    "v_pk_fma_f32 v[108:109], v[108:109], v[226:227], s[94:95]\n"
    "v_pk_fma_f32 v[110:111], v[110:111], v[226:227], s[94:95]\n"
    "v_cvt_pk_bf16_f32 v54, v108, v109\n"
    "v_cvt_pk_bf16_f32 v55, v110, v111\n"
    "buffer_store_dwordx4 v[52:55], v205, s[28:31], s63 offen offset:64 sc0 sc1\n"
    "v_accvgpr_read_b32 v96, a72\n"
    "v_accvgpr_read_b32 v97, a73\n"
    "v_accvgpr_read_b32 v98, a74\n"
    "v_accvgpr_read_b32 v99, a75\n"
    "v_pk_fma_f32 v[96:97], v[96:97], v[226:227], s[94:95]\n"
    "v_pk_fma_f32 v[98:99], v[98:99], v[226:227], s[94:95]\n"
    "v_cvt_pk_bf16_f32 v56, v96, v97\n"
    "v_cvt_pk_bf16_f32 v57, v98, v99\n"
    "v_accvgpr_read_b32 v100, a88\n"
    "v_accvgpr_read_b32 v101, a89\n"
    "v_accvgpr_read_b32 v102, a90\n"
    "v_accvgpr_read_b32 v103, a91\n"
    "v_pk_fma_f32 v[100:101], v[100:101], v[226:227], s[94:95]\n"
    "v_pk_fma_f32 v[102:103], v[102:103], v[226:227], s[94:95]\n"
    "v_cvt_pk_bf16_f32 v58, v100, v101\n"
    "v_cvt_pk_bf16_f32 v59, v102, v103\n"
    "buffer_store_dwordx4 v[56:59], v205, s[28:31], s63 offen offset:128 sc0 sc1\n"
    "v_accvgpr_read_b32 v104, a104\n"
    "v_accvgpr_read_b32 v105, a105\n"
    "v_accvgpr_read_b32 v106, a106\n"
    "v_accvgpr_read_b32 v107, a107\n"
    "v_pk_fma_f32 v[104:105], v[104:105], v[226:227], s[94:95]\n"
    "v_pk_fma_f32 v[106:107], v[106:107], v[226:227], s[94:95]\n"
    "v_cvt_pk_bf16_f32 v60, v104, v105\n"
    "v_cvt_pk_bf16_f32 v61, v106, v107\n"
    "v_accvgpr_read_b32 v108, a120\n"
    "v_accvgpr_read_b32 v109, a121\n"
    "v_accvgpr_read_b32 v110, a122\n"
    "v_accvgpr_read_b32 v111, a123\n"
    "v_pk_fma_f32 v[108:109], v[108:109], v[226:227], s[94:95]\n"
    "v_pk_fma_f32 v[110:111], v[110:111], v[226:227], s[94:95]\n"
    "v_cvt_pk_bf16_f32 v62, v108, v109\n"
    "v_cvt_pk_bf16_f32 v63, v110, v111\n"
    "buffer_store_dwordx4 v[60:63], v205, s[28:31], s63 offen offset:192 sc0 sc1\n"
    "v_accvgpr_read_b32 v96, a136\n"
    "v_accvgpr_read_b32 v97, a137\n"
    "v_accvgpr_read_b32 v98, a138\n"
    "v_accvgpr_read_b32 v99, a139\n"
    "v_pk_fma_f32 v[96:97], v[96:97], v[226:227], s[94:95]\n"
    "v_pk_fma_f32 v[98:99], v[98:99], v[226:227], s[94:95]\n"
    "v_cvt_pk_bf16_f32 v32, v96, v97\n"
    "v_cvt_pk_bf16_f32 v33, v98, v99\n"
    "v_accvgpr_read_b32 v100, a152\n"
    "v_accvgpr_read_b32 v101, a153\n"
    "v_accvgpr_read_b32 v102, a154\n"
    "v_accvgpr_read_b32 v103, a155\n"
    "v_pk_fma_f32 v[100:101], v[100:101], v[226:227], s[94:95]\n"
    "v_pk_fma_f32 v[102:103], v[102:103], v[226:227], s[94:95]\n"
    "v_cvt_pk_bf16_f32 v34, v100, v101\n"
    "v_cvt_pk_bf16_f32 v35, v102, v103\n"
    "buffer_store_dwordx4 v[32:35], v205, s[28:31], s63 offen offset:256 sc0 sc1\n"
    "v_accvgpr_read_b32 v104, a168\n"
    "v_accvgpr_read_b32 v105, a169\n"
    "v_accvgpr_read_b32 v106, a170\n"
    "v_accvgpr_read_b32 v107, a171\n"
    "v_pk_fma_f32 v[104:105], v[104:105], v[226:227], s[94:95]\n"
    "v_pk_fma_f32 v[106:107], v[106:107], v[226:227], s[94:95]\n"
    "v_cvt_pk_bf16_f32 v36, v104, v105\n"
    "v_cvt_pk_bf16_f32 v37, v106, v107\n"
    "v_accvgpr_read_b32 v108, a184\n"
    "v_accvgpr_read_b32 v109, a185\n"
    "v_accvgpr_read_b32 v110, a186\n"
    "v_accvgpr_read_b32 v111, a187\n"
    "v_pk_fma_f32 v[108:109], v[108:109], v[226:227], s[94:95]\n"
    "v_pk_fma_f32 v[110:111], v[110:111], v[226:227], s[94:95]\n"
    "v_cvt_pk_bf16_f32 v38, v108, v109\n"
    "v_cvt_pk_bf16_f32 v39, v110, v111\n"
    "buffer_store_dwordx4 v[36:39], v205, s[28:31], s63 offen offset:320 sc0 sc1\n"
    "v_accvgpr_read_b32 v96, a12\n"
    "v_accvgpr_read_b32 v97, a13\n"
    "v_accvgpr_read_b32 v98, a14\n"
    "v_accvgpr_read_b32 v99, a15\n"
    "v_pk_fma_f32 v[96:97], v[96:97], v[228:229], s[94:95]\n"
    "v_pk_fma_f32 v[98:99], v[98:99], v[228:229], s[94:95]\n"
    "v_cvt_pk_bf16_f32 v40, v96, v97\n"
    "v_cvt_pk_bf16_f32 v41, v98, v99\n"
    "v_accvgpr_read_b32 v100, a28\n"
    "v_accvgpr_read_b32 v101, a29\n"
    "v_accvgpr_read_b32 v102, a30\n"
    "v_accvgpr_read_b32 v103, a31\n"
    "v_pk_fma_f32 v[100:101], v[100:101], v[228:229], s[94:95]\n"
    "v_pk_fma_f32 v[102:103], v[102:103], v[228:229], s[94:95]\n"
    "v_cvt_pk_bf16_f32 v42, v100, v101\n"
    "v_cvt_pk_bf16_f32 v43, v102, v103\n"
    "buffer_store_dwordx4 v[40:43], v205, s[28:31], s64 offen sc0 sc1\n"
    "v_accvgpr_read_b32 v104, a44\n"
    "v_accvgpr_read_b32 v105, a45\n"
    "v_accvgpr_read_b32 v106, a46\n"
    "v_accvgpr_read_b32 v107, a47\n"
    "v_pk_fma_f32 v[104:105], v[104:105], v[228:229], s[94:95]\n"
    "v_pk_fma_f32 v[106:107], v[106:107], v[228:229], s[94:95]\n"
    "v_cvt_pk_bf16_f32 v44, v104, v105\n"
    "v_cvt_pk_bf16_f32 v45, v106, v107\n"
    "v_accvgpr_read_b32 v108, a60\n"
    "v_accvgpr_read_b32 v109, a61\n"
    "v_accvgpr_read_b32 v110, a62\n"
    "v_accvgpr_read_b32 v111, a63\n"
    "v_pk_fma_f32 v[108:109], v[108:109], v[228:229], s[94:95]\n"
    "v_pk_fma_f32 v[110:111], v[110:111], v[228:229], s[94:95]\n"
    "v_cvt_pk_bf16_f32 v46, v108, v109\n"
    "v_cvt_pk_bf16_f32 v47, v110, v111\n"
    "buffer_store_dwordx4 v[44:47], v205, s[28:31], s64 offen offset:64 sc0 sc1\n"
    "v_accvgpr_read_b32 v96, a76\n"
    "v_accvgpr_read_b32 v97, a77\n"
    "v_accvgpr_read_b32 v98, a78\n"
    "v_accvgpr_read_b32 v99, a79\n"
    "v_pk_fma_f32 v[96:97], v[96:97], v[228:229], s[94:95]\n"
    "v_pk_fma_f32 v[98:99], v[98:99], v[228:229], s[94:95]\n"
    "v_cvt_pk_bf16_f32 v48, v96, v97\n"
    "v_cvt_pk_bf16_f32 v49, v98, v99\n"
    "v_accvgpr_read_b32 v100, a92\n"
    "v_accvgpr_read_b32 v101, a93\n"
    "v_accvgpr_read_b32 v102, a94\n"
    "v_accvgpr_read_b32 v103, a95\n"
    "v_pk_fma_f32 v[100:101], v[100:101], v[228:229], s[94:95]\n"
    "v_pk_fma_f32 v[102:103], v[102:103], v[228:229], s[94:95]\n"
    "v_cvt_pk_bf16_f32 v50, v100, v101\n"
    "v_cvt_pk_bf16_f32 v51, v102, v103\n"
    "buffer_store_dwordx4 v[48:51], v205, s[28:31], s64 offen offset:128 sc0 sc1\n"
    "v_accvgpr_read_b32 v104, a108\n"
    "v_accvgpr_read_b32 v105, a109\n"
    "v_accvgpr_read_b32 v106, a110\n"
    "v_accvgpr_read_b32 v107, a111\n"
    "v_pk_fma_f32 v[104:105], v[104:105], v[228:229], s[94:95]\n"
    "v_pk_fma_f32 v[106:107], v[106:107], v[228:229], s[94:95]\n"
    "v_cvt_pk_bf16_f32 v52, v104, v105\n"
    "v_cvt_pk_bf16_f32 v53, v106, v107\n"
    "v_accvgpr_read_b32 v108, a124\n"
    "v_accvgpr_read_b32 v109, a125\n"
    "v_accvgpr_read_b32 v110, a126\n"
    "v_accvgpr_read_b32 v111, a127\n"
    "v_pk_fma_f32 v[108:109], v[108:109], v[228:229], s[94:95]\n"
    "v_pk_fma_f32 v[110:111], v[110:111], v[228:229], s[94:95]\n"
    "v_cvt_pk_bf16_f32 v54, v108, v109\n"
    "v_cvt_pk_bf16_f32 v55, v110, v111\n"
    "buffer_store_dwordx4 v[52:55], v205, s[28:31], s64 offen offset:192 sc0 sc1\n"
    "v_accvgpr_read_b32 v96, a140\n"
    "v_accvgpr_read_b32 v97, a141\n"
    "v_accvgpr_read_b32 v98, a142\n"
    "v_accvgpr_read_b32 v99, a143\n"
    "v_pk_fma_f32 v[96:97], v[96:97], v[228:229], s[94:95]\n"
    "v_pk_fma_f32 v[98:99], v[98:99], v[228:229], s[94:95]\n"
    "v_cvt_pk_bf16_f32 v56, v96, v97\n"
    "v_cvt_pk_bf16_f32 v57, v98, v99\n"
    "v_accvgpr_read_b32 v100, a156\n"
    "v_accvgpr_read_b32 v101, a157\n"
    "v_accvgpr_read_b32 v102, a158\n"
    "v_accvgpr_read_b32 v103, a159\n"
    "v_pk_fma_f32 v[100:101], v[100:101], v[228:229], s[94:95]\n"
    "v_pk_fma_f32 v[102:103], v[102:103], v[228:229], s[94:95]\n"
    "v_cvt_pk_bf16_f32 v58, v100, v101\n"
    "v_cvt_pk_bf16_f32 v59, v102, v103\n"
    "buffer_store_dwordx4 v[56:59], v205, s[28:31], s64 offen offset:256 sc0 sc1\n"
    "v_accvgpr_read_b32 v104, a172\n"
    "v_accvgpr_read_b32 v105, a173\n"
    "v_accvgpr_read_b32 v106, a174\n"
    "v_accvgpr_read_b32 v107, a175\n"
    "v_pk_fma_f32 v[104:105], v[104:105], v[228:229], s[94:95]\n"
    "v_pk_fma_f32 v[106:107], v[106:107], v[228:229], s[94:95]\n"
    "v_cvt_pk_bf16_f32 v60, v104, v105\n"
    "v_cvt_pk_bf16_f32 v61, v106, v107\n"
    "v_accvgpr_read_b32 v108, a188\n"
    "v_accvgpr_read_b32 v109, a189\n"
    "v_accvgpr_read_b32 v110, a190\n"
    "v_accvgpr_read_b32 v111, a191\n"
    "v_pk_fma_f32 v[108:109], v[108:109], v[228:229], s[94:95]\n"
    "v_pk_fma_f32 v[110:111], v[110:111], v[228:229], s[94:95]\n"
    "v_cvt_pk_bf16_f32 v62, v108, v109\n"
    "v_cvt_pk_bf16_f32 v63, v110, v111\n"
    "buffer_store_dwordx4 v[60:63], v205, s[28:31], s64 offen offset:320 sc0 sc1\n"
    "s_branch LFIN\n"
    "LEND:\n"
    :
    : "s"(__builtin_amdgcn_kernarg_segment_ptr()), "s"(blockIdx.x), "s"(wid)
    : "v0", "v1", "v2", "v3", "v4", "v5", "v6", "v7", "v8", "v9", "v10", "v11", "v12", "v13", "v14", "v15", "v16", "v17", "v18", "v19", "v20", "v21", "v22", "v23", "v24", "v25", "v26", "v27", "v28", "v29", "v30", "v31", "v32", "v33", "v34", "v35", "v36", "v37", "v38", "v39", "v40", "v41", "v42", "v43", "v44", "v45", "v46", "v47", "v48", "v49", "v50", "v51", "v52", "v53", "v54", "v55", "v56", "v57", "v58", "v59", "v60", "v61", "v62", "v63", "v64", "v65", "v66", "v67", "v68", "v69", "v70", "v71", "v72", "v73", "v74", "v75", "v76", "v77", "v78", "v79", "v80", "v81", "v82", "v83", "v84", "v85", "v86", "v87", "v88", "v89", "v90", "v91", "v92", "v93", "v94", "v95", "v96", "v97", "v98", "v99", "v100", "v101", "v102", "v103", "v104", "v105", "v106", "v107", "v108", "v109", "v110", "v111", "v112", "v113", "v114", "v115", "v116", "v117", "v118", "v119", "v120", "v121", "v122", "v123", "v124", "v125", "v126", "v127", "v128", "v129", "v130", "v131", "v132", "v133", "v134", "v135", "v136", "v137", "v138", "v139", "v140", "v141", "v142", "v143", "v144", "v145", "v146", "v147", "v148", "v149", "v150", "v151", "v152", "v153", "v154", "v155", "v156", "v157", "v158", "v159", "v160", "v161", "v162", "v163", "v164", "v165", "v166", "v167", "v168", "v169", "v170", "v171", "v172", "v173", "v174", "v175", "v176", "v177", "v178", "v179", "v180", "v181", "v182", "v183", "v184", "v185", "v186", "v187", "v188", "v189", "v190", "v191", "v192", "v193", "v194", "v195", "v196", "v197", "v198", "v199", "v200", "v201", "v202", "v203", "v204", "v205", "v206", "v207", "v208", "v209", "v210", "v211", "v212", "v213", "v214", "v215", "v216", "v217", "v218", "v219", "v220", "v221", "v222", "v223", "v224", "v225", "v226", "v227", "v228", "v229", "v230", "v231", "v232", "v233", "v234", "v235", "v236", "v237", "v238", "v239", "v240", "v241", "v242", "v243", "v244", "v245", "v246", "v247", "v248", "v249", "v250", "v251", "v252", "v253", "v254", "v255", "a0", "a1", "a2", "a3", "a4", "a5", "a6", "a7", "a8", "a9", "a10", "a11", "a12", "a13", "a14", "a15", "a16", "a17", "a18", "a19", "a20", "a21", "a22", "a23", "a24", "a25", "a26", "a27", "a28", "a29", "a30", "a31", "a32", "a33", "a34", "a35", "a36", "a37", "a38", "a39", "a40", "a41", "a42", "a43", "a44", "a45", "a46", "a47", "a48", "a49", "a50", "a51", "a52", "a53", "a54", "a55", "a56", "a57", "a58", "a59", "a60", "a61", "a62", "a63", "a64", "a65", "a66", "a67", "a68", "a69", "a70", "a71", "a72", "a73", "a74", "a75", "a76", "a77", "a78", "a79", "a80", "a81", "a82", "a83", "a84", "a85", "a86", "a87", "a88", "a89", "a90", "a91", "a92", "a93", "a94", "a95", "a96", "a97", "a98", "a99", "a100", "a101", "a102", "a103", "a104", "a105", "a106", "a107", "a108", "a109", "a110", "a111", "a112", "a113", "a114", "a115", "a116", "a117", "a118", "a119", "a120", "a121", "a122", "a123", "a124", "a125", "a126", "a127", "a128", "a129", "a130", "a131", "a132", "a133", "a134", "a135", "a136", "a137", "a138", "a139", "a140", "a141", "a142", "a143", "a144", "a145", "a146", "a147", "a148", "a149", "a150", "a151", "a152", "a153", "a154", "a155", "a156", "a157", "a158", "a159", "a160", "a161", "a162", "a163", "a164", "a165", "a166", "a167", "a168", "a169", "a170", "a171", "a172", "a173", "a174", "a175", "a176", "a177", "a178", "a179", "a180", "a181", "a182", "a183", "a184", "a185", "a186", "a187", "a188", "a189", "a190", "a191", "a192", "a193", "a194", "a195", "a196", "a197", "a198", "a199", "a200", "a201", "a202", "a203", "a204", "a205", "a206", "a207", "a208", "a209", "a210", "a211", "a212", "a213", "a214", "a215", "a216", "a217", "a218", "a219", "a220", "a221", "a222", "a223", "a224", "a225", "a226", "a227", "a228", "a229", "a230", "a231", "a232", "a233", "a234", "a235", "a236", "a237", "a238", "a239", "a240", "a241", "a242", "a243", "a244", "a245", "a246", "a247", "a248", "a249", "a250", "a251", "a252", "a253", "a254", "a255", "s8", "s9", "s10", "s11", "s12", "s13", "s14", "s15", "s16", "s17", "s18", "s19", "s20", "s21", "s22", "s23", "s24", "s25", "s26", "s27", "s28", "s29", "s30", "s31", "s32", "s33", "s34", "s35", "s36", "s37", "s38", "s39", "s40", "s41", "s42", "s43", "s44", "s45", "s46", "s47", "s48", "s49", "s50", "s51", "s52", "s53", "s54", "s55", "s56", "s57", "s58", "s59", "s60", "s61", "s62", "s63", "s64", "s65", "s66", "s67", "s68", "s69", "s70", "s71", "s72", "s73", "s74", "s75", "s76", "s77", "s78", "s79", "s80", "s81", "s82", "s83", "s84", "s85", "s86", "s87", "s88", "s89", "s90", "s91", "s92", "s93", "s94", "s95", "s96", "s97", "s98", "s99", "s100", "s101", "vcc", "scc", "memory");
}

extern "C" void kernel_launch(void* const* d_in, const int* in_sizes, int n_in, void* d_out, int out_size, void* d_ws, size_t ws_size,
                              hipStream_t stream) {
  (void)hipFuncSetAttribute((const void*)attn_fused, hipFuncAttributeMaxDynamicSharedMemorySize, 153088);
  attn_fused<<<256, 256, 153088, stream>>>(d_in[0], d_in[1], d_in[2], d_in[3], d_in[4], d_in[5], d_in[6], d_ws, d_out);
}
```

```cpp
#include <hip/hip_runtime.h>

__global__ __launch_bounds__(256, 1) void attn_fused(const void* q, const void* qk, const void* qv, const void* ks, const void* kz,
                                                     const void* vs, const void* vz, void* ws, void* out) {
  const int wid = __builtin_amdgcn_readfirstlane(threadIdx.x >> 6);
  asm volatile(
    "s_load_dwordx16 s[8:23], %0, 0x0\n"
    "s_mov_b32 s58, %2\n"
    "s_and_b32 s76, %1, 7\n"
    "s_lshr_b32 s77, %1, 3\n"
    "s_and_b32 s78, s77, 3\n"
    "s_lshl_b32 s56, s76, 2\n"
    "s_add_u32 s56, s56, s78\n"
    "s_lshr_b32 s57, s77, 2\n"
    "v_mbcnt_lo_u32_b32 v231, -1, 0\n"
    "v_mbcnt_hi_u32_b32 v231, -1, v231\n"
    "s_getpc_b64 s[44:45]\n"
    "LPC:\n"
    "s_mov_b32 s46, LSLOW-LPC\n"
    "s_mov_b32 s47, 0x20000\n"
    "s_lshr_b32 s76, %1, 3\n"
    "s_lshl_b32 s76, s76, 2\n"
    "s_add_u32 s76, s76, s58\n"
    "s_lshl_b32 s76, s76, 9\n"
    "v_lshlrev_b32 v196, 3, v231\n"
    "buffer_load_dword v251, v196, s[44:47], s76 offen\n"
    "s_mul_i32 s72, s58, 0xc00\n"
    "s_lshr_b32 s86, s57, 2\n"
    "s_mul_i32 s64, s86, 0xc000\n"
    "s_lshl_b32 s86, s86, 3\n"
    "s_and_b32 s79, s57, 3\n"
    "s_add_u32 s86, s86, s79\n"
    "s_lshl_b32 s86, s86, 2\n"
    "s_xor_b32 s76, s57, 4\n"
    "s_and_b32 s77, s76, 6\n"
    "s_mul_i32 s77, s77, 15\n"
    "s_add_u32 s76, s76, s77\n"
    "s_and_b32 s77, s58, 1\n"
    "s_mul_i32 s78, s77, 12\n"
    "s_add_u32 s78, s78, s86\n"
    "s_lshl_b32 s78, s78, 1\n"
    "s_mul_i32 s77, s77, 0xc0\n"
    "s_movk_i32 s90, 0x556\n"
    "s_lshl_b32 s60, s56, 12\n"
    "s_cmp_lt_u32 s58, 2\n"
    "s_cselect_b32 s76, s76, s78\n"
    "s_cmovk_i32 s90, 0xaab\n"
    "s_cselect_b32 s91, 24, 0\n"
    "s_cselect_b32 s92, 1, 10\n"
    "s_cselect_b32 s89, 7, 0\n"
    "s_cselect_b32 s64, s64, 0\n"
    "s_add_u32 s60, s60, s76\n"
    "s_mul_i32 s60, s60, 0x180\n"
    "s_mul_i32 s76, s76, 0x180\n"
    "s_sub_u32 s34, 0x180000, s76\n"
    "s_mov_b32 s35, 0x20000\n"
    "v_add_u32 v144, s77, v231\n"
    "v_add_u32 v145, 64, v144\n"
    "v_add_u32 v146, 64, v145\n"
    "v_mul_u32_u24 v197, s90, v144\n"
    "v_lshrrev_b32 v197, 16, v197\n"
    "v_mul_u32_u24 v198, s92, v197\n"
    "v_lshrrev_b32 v198, 1, v198\n"
    "v_and_b32 v198, s89, v198\n"
    "v_xor_b32 v198, v198, v144\n"
    "v_mul_u32_u24 v197, s91, v197\n"
    "v_add_lshl_u32 v202, v197, v198, 4\n"
    "v_mul_u32_u24 v197, s90, v145\n"
    "v_lshrrev_b32 v197, 16, v197\n"
    "v_mul_u32_u24 v198, s92, v197\n"
    "v_lshrrev_b32 v198, 1, v198\n"
    "v_and_b32 v198, s89, v198\n"
    "v_xor_b32 v198, v198, v144\n"
    "v_mul_u32_u24 v197, s91, v197\n"
    "v_add_lshl_u32 v203, v197, v198, 4\n"
    "v_mul_u32_u24 v197, s90, v146\n"
    "v_lshrrev_b32 v197, 16, v197\n"
    "v_mul_u32_u24 v198, s92, v197\n"
    "v_lshrrev_b32 v198, 1, v198\n"
    "v_and_b32 v198, s89, v198\n"
    "v_xor_b32 v198, v198, v144\n"
    "v_mul_u32_u24 v197, s91, v197\n"
    "v_add_lshl_u32 v204, v197, v198, 4\n"
    "s_waitcnt lgkmcnt(0)\n"
    "s_cmp_lt_u32 s58, 2\n"
    "s_cselect_b64 s[80:81], s[10:11], s[12:13]\n"
    "s_cselect_b64 s[82:83], s[16:17], s[14:15]\n"
    "s_add_u32 s32, s80, s60\n"
    "s_addc_u32 s33, s81, 0\n"
    "s_cmp_eq_u32 s64, 0\n"
    "s_cbranch_scc1 LNOR\n"
    "s_add_u32 m0, s72, 0x12000\n"
    "s_nop 0\n"
    "buffer_load_dwordx4 v202, s[32:35], 0 offen nt lds\n"
    "buffer_load_dwordx4 v203, s[32:35], 0 offen offset:1024 nt lds\n"
    "buffer_load_dwordx4 v204, s[32:35], 0 offen offset:2048 nt lds\n"
    "LNOR:\n"
    "s_add_u32 m0, s72, 0x15000\n"
    "s_nop 0\n"
    "buffer_load_dwordx4 v202, s[32:35], s64 offen nt lds\n"
    "buffer_load_dwordx4 v203, s[32:35], s64 offen offset:1024 nt lds\n"
    "buffer_load_dwordx4 v204, s[32:35], s64 offen offset:2048 nt lds\n"
    "LR0:\n"
    "s_load_dwordx2 s[24:25], %0, 0x40\n"
    "s_mul_i32 s71, s58, 0x1800\n"
    "s_lshr_b32 s87, s57, 2\n"
    "s_mul_i32 s87, s87, 0x6000\n"
    "s_mul_i32 s61, s56, 0x3000\n"
    "s_lshl_b32 s62, s56, 12\n"
    "s_lshl_b32 s59, s56, 12\n"
    "s_mov_b32 s50, 0x3000\n"
    "s_mov_b32 s51, 0x20000\n"
    "s_and_b32 s77, s57, 1\n"
    "s_mul_i32 s77, s77, 0xc0\n"
    "v_mul_u32_u24 v197, 0x1556, v144\n"
    "v_lshrrev_b32 v197, 16, v197\n"
    "v_mul_u32_u24 v196, 12, v197\n"
    "v_sub_u32 v196, v144, v196\n"
    "v_mul_u32_u24 v248, 0x180, v197\n"
    "v_lshl_add_u32 v248, v196, 4, v248\n"
    "v_add_u32 v248, s77, v248\n"
    "s_add_u32 s48, s82, s61\n"
    "s_addc_u32 s49, s83, 0\n"
    "s_add_u32 m0, s72, 0x21000\n"
    "s_nop 0\n"
    "buffer_load_dwordx4 v248, s[48:51], 0 offen lds\n"
    "s_add_u32 s40, s20, s62\n"
    "s_addc_u32 s41, s21, 0\n"
    "s_add_u32 s52, s18, s62\n"
    "s_addc_u32 s53, s19, 0\n"
    "s_mov_b32 s42, 0x1000\n"
    "s_mov_b32 s43, 0x20000\n"
    "s_mov_b32 s54, 0x1000\n"
    "s_mov_b32 s55, 0x20000\n"
    "v_lshlrev_b32 v196, 1, v231\n"
    "buffer_load_ushort v44, v196, s[40:43], 0 offen\n"
    "buffer_load_ushort v45, v196, s[52:55], 0 offen\n"
    "s_lshl_b32 s77, s58, 6\n"
    "v_add_u32 v41, s77, v231\n"
    "v_bfe_u32 v197, v231, 2, 1\n"
    "v_mul_u32_u24 v197, 12, v197\n"
    "v_and_b32 v198, 7, v231\n"
    "v_add3_u32 v197, v197, v198, s86\n"
    "v_lshrrev_b32 v40, 3, v41\n"
    "v_lshlrev_b32 v40, 7, v40\n"
    "v_lshl_add_u32 v40, v197, 1, v40\n"
    "buffer_load_ushort v42, v40, s[40:43], 0 offen\n"
    "buffer_load_ushort v43, v40, s[52:55], 0 offen\n"
    "s_add_u32 s44, s22, s59\n"
    "s_addc_u32 s45, s23, 0\n"
    "s_mov_b32 s46, 0x900\n"
    "s_mov_b32 s47, 0x20000\n"
    "v_mov_b32 v196, 0x40000000\n"
    "s_cmp_eq_u32 s58, 0\n"
    "s_cselect_b32 s77, 64, 0\n"
    "s_cselect_b32 s74, 1, 0\n"
    "v_and_b32 v213, 7, v231\n"
    "v_mul_u32_u24 v213, 0x100, v213\n"
    "v_cmp_gt_u32 vcc, s77, v231\n"
    "v_mov_b32 v197, 0x800\n"
    "s_mul_i32 s77, s57, 0x100\n"
    "v_cndmask_b32 v213, v196, v213, vcc\n"
    "v_cndmask_b32 v197, v196, v197, vcc\n"
    "buffer_load_dword v221, v197, s[44:47], 0 offen sc1\n"
    "v_cmp_gt_u32 vcc, s74, v231\n"
    "v_mov_b32 v214, s77\n"
    "s_mul_i32 s66, s74, 3\n"
    "v_cndmask_b32 v214, v196, v214, vcc\n"
    "s_add_u32 s78, s64, 0xc000\n"
    "s_add_u32 m0, s72, 0x1e000\n"
    "s_nop 0\n"
    "buffer_load_dwordx4 v202, s[32:35], s78 offen nt lds\n"
    "buffer_load_dwordx4 v203, s[32:35], s78 offen offset:1024 nt lds\n"
    "buffer_load_dwordx4 v204, s[32:35], s78 offen offset:2048 nt lds\n"
    "s_lshl_b32 s76, s56, 3\n"
    "s_add_u32 s76, s76, s57\n"
    "s_mul_i32 s76, s76, 0x18000\n"
    "s_add_u32 s28, s8, s76\n"
    "s_addc_u32 s29, s9, 0\n"
    "s_mov_b32 s30, 0x18000\n"
    "s_mov_b32 s31, 0x20000\n"
    "s_mul_i32 s88, s58, 0x6000\n"
    "v_mul_u32_u24 v197, 0xaab, v231\n"
    "v_lshrrev_b32 v197, 16, v197\n"
    "v_mul_u32_u24 v196, 24, v197\n"
    "v_sub_u32 v196, v231, v196\n"
    "v_mul_u32_u24 v152, 0x180, v197\n"
    "v_lshrrev_b32 v197, 1, v197\n"
    "v_xor_b32 v196, v196, v197\n"
    "v_lshl_add_u32 v152, v196, 4, v152\n"
    "v_add_u32 v198, 64, v231\n"
    "v_mul_u32_u24 v197, 0xaab, v198\n"
    "v_lshrrev_b32 v197, 16, v197\n"
    "v_mul_u32_u24 v196, 24, v197\n"
    "v_sub_u32 v196, v198, v196\n"
    "v_mul_u32_u24 v153, 0x180, v197\n"
    "v_lshrrev_b32 v197, 1, v197\n"
    "v_xor_b32 v196, v196, v197\n"
    "v_lshl_add_u32 v153, v196, 4, v153\n"
    "v_add_u32 v198, 0x80, v231\n"
    "v_mul_u32_u24 v197, 0xaab, v198\n"
    "v_lshrrev_b32 v197, 16, v197\n"
    "v_mul_u32_u24 v196, 24, v197\n"
    "v_sub_u32 v196, v198, v196\n"
    "v_mul_u32_u24 v154, 0x180, v197\n"
    "v_lshrrev_b32 v197, 1, v197\n"
    "v_xor_b32 v196, v196, v197\n"
    "v_lshl_add_u32 v154, v196, 4, v154\n"
    "s_mov_b32 s76, 0xc00\n"
    "v_xad_u32 v155, v152, 64, s76\n"
    "v_xad_u32 v156, v153, 64, s76\n"
    "v_xad_u32 v157, v154, 64, s76\n"
    "s_add_u32 s78, s88, 0\n"
    "s_add_u32 m0, s71, 0\n"
    "s_nop 0\n"
    "buffer_load_dwordx4 v152, s[28:31], s78 offen nt lds\n"
    "s_add_u32 s78, s88, 0\n"
    "s_add_u32 m0, s71, 0x400\n"
    "s_nop 0\n"
    "buffer_load_dwordx4 v153, s[28:31], s78 offen nt lds\n"
    "s_add_u32 s78, s88, 0\n"
    "s_add_u32 m0, s71, 0x800\n"
    "s_nop 0\n"
    "buffer_load_dwordx4 v154, s[28:31], s78 offen nt lds\n"
    "v_mov_b32 v196, 0x40000000\n"
    "v_and_b32 v46, 15, v231\n"
    "v_lshrrev_b32 v47, 4, v231\n"
    "s_mov_b32 s75, 0x3dd53b95\n"
    "v_min_u32 v199, 2, v47\n"
    "v_lshlrev_b32 v197, 3, v199\n"
    "s_lshl_b32 s76, s58, 1\n"
    "v_add_u32 v197, s76, v197\n"
    "v_lshrrev_b32 v198, 1, v46\n"
    "v_xor_b32 v197, v197, v198\n"
    "v_mul_u32_u24 v205, 0x180, v46\n"
    "v_lshl_add_u32 v205, v197, 4, v205\n"
    "v_add_u32 v205, 0x12000, v205\n"
    "v_xor_b32 v206, 16, v205\n"
    "v_and_b32 v197, 7, v231\n"
    "v_lshl_add_u32 v209, v199, 5, v197\n"
    "s_lshl_b32 s76, s58, 3\n"
    "v_add_lshl_u32 v209, v209, s76, 1\n"
    "v_add_u32 v217, 0x21000, v209\n"
    "s_lshr_b32 s76, s57, 2\n"
    "s_mul_i32 s76, s76, 0xc0\n"
    "s_add_u32 s76, s76, 0x20f40\n"
    "v_add_u32 v209, s76, v209\n"
    "v_lshrrev_b32 v198, 3, v231\n"
    "s_mul_i32 s76, s58, 12\n"
    "v_lshl_add_u32 v199, v198, 1, s76\n"
    "v_mul_u32_u24 v207, 5, v197\n"
    "v_and_b32 v207, 9, v207\n"
    "v_xor_b32 v199, v199, v207\n"
    "v_mul_u32_u24 v207, 0x300, v197\n"
    "v_lshl_add_u32 v207, v199, 4, v207\n"
    "v_add_u32 v207, 0x13800, v207\n"
    "v_xor_b32 v208, 16, v207\n"
    "s_mul_i32 s76, s58, 0xc0\n"
    "s_add_u32 s76, s76, 0x13800\n"
    "v_lshl_add_u32 v207, v231, 2, s76\n"
    "v_lshlrev_b32 v210, 3, v197\n"
    "v_add_u32 v210, 0x24400, v210\n"
    "s_mul_i32 s76, s58, 0x480\n"
    "s_add_u32 s76, s76, 0x24400\n"
    "v_mul_u32_u24 v215, 0x90, v197\n"
    "v_lshl_add_u32 v215, v198, 4, v215\n"
    "v_add_u32 v215, s76, v215\n"
    "v_and_b32 v197, 1, v197\n"
    "v_lshlrev_b32 v197, 3, v197\n"
    "v_sub_u32 v215, v215, v197\n"
    "v_bfe_u32 v197, v231, 2, 2\n"
    "v_mul_u32_u24 v216, 0x90, v197\n"
    "v_lshl_add_u32 v216, v47, 5, v216\n"
    "v_and_b32 v197, 1, v197\n"
    "v_lshlrev_b32 v197, 3, v197\n"
    "v_sub_u32 v216, v216, v197\n"
    "v_and_b32 v197, 3, v231\n"
    "v_lshl_add_u32 v216, v197, 3, v216\n"
    "v_add_u32 v216, s76, v216\n"
    "s_lshr_b32 s76, s57, 1\n"
    "s_xor_b32 s76, s76, 2\n"
    "s_mul_i32 s76, s76, 0x1800\n"
    "s_and_b32 s77, s57, 1\n"
    "s_mul_i32 s77, s77, 0xc00\n"
    "s_add_u32 s76, s76, s77\n"
    "s_lshl_b32 s77, s58, 8\n"
    "s_add_u32 s76, s76, s77\n"
    "v_lshlrev_b32 v197, 4, v46\n"
    "v_lshl_add_u32 v211, v47, 10, v197\n"
    "v_add_u32 v211, s76, v211\n"
    "s_lshr_b32 s76, s57, 2\n"
    "s_lshl_b32 s76, s76, 10\n"
    "s_lshl_b32 s77, s79, 8\n"
    "s_add_u32 s76, s76, s77\n"
    "s_mul_i32 s77, s58, 48\n"
    "v_add_u32 v198, s77, v231\n"
    "v_lshrrev_b32 v197, 5, v198\n"
    "v_lshl_add_u32 v212, v197, 12, s76\n"
    "v_bfe_u32 v197, v198, 2, 1\n"
    "v_lshl_add_u32 v212, v197, 11, v212\n"
    "v_bfe_u32 v197, v198, 3, 2\n"
    "v_lshl_add_u32 v212, v197, 6, v212\n"
    "v_cmp_gt_u32 vcc, 48, v231\n"
    "v_and_b32 v197, 3, v198\n"
    "v_lshl_add_u32 v212, v197, 4, v212\n"
    "v_cndmask_b32 v211, v196, v211, vcc\n"
    "v_cndmask_b32 v212, v196, v212, vcc\n"
    "v_mov_b32 v222, 0\n"
    "v_mov_b32 v223, 0\n"
    "v_mov_b32 v224, 0\n"
    "v_mov_b32 v225, 0\n"
    "v_mov_b32 v160, 0\n"
    "v_mov_b32 v161, 0\n"
    "v_mov_b32 v162, 0\n"
    "v_mov_b32 v163, 0\n"
    "v_mov_b32 v164, 0\n"
    "v_mov_b32 v165, 0\n"
    "v_mov_b32 v172, 0\n"
    "v_mov_b32 v173, 0\n"
    "v_mov_b32 v174, 0\n"
    "v_mov_b32 v175, 0\n"
    "v_lshlrev_b32 v41, 3, v41\n"
    "v_add_u32 v41, 0x24400, v41\n"
    "s_mul_i32 s76, s56, 0xc0000\n"
    "s_add_u32 s77, s76, 0x40000\n"
    "s_add_u32 s36, s22, s77\n"
    "s_addc_u32 s37, s23, 0\n"
    "s_mov_b32 s38, 0xc0000\n"
    "s_mov_b32 s39, 0x20000\n"
    "s_cmp_eq_u32 s87, 0\n"
    "s_cselect_b32 s62, 0x40000000, 0\n"
    "s_waitcnt vmcnt(10)\n"
    "v_lshlrev_b32 v44, 16, v44\n"
    "s_waitcnt vmcnt(9)\n"
    "v_lshlrev_b32 v45, 16, v45\n"
    "v_mul_f32 v197, v44, v45\n"
    "s_waitcnt vmcnt(8)\n"
    "v_lshlrev_b32 v42, 16, v42\n"
    "s_waitcnt vmcnt(7)\n"
    "v_lshlrev_b32 v43, 16, v43\n"
    "v_min_f32_dpp v197, v197, v197 row_ror:8 row_mask:0xf bank_mask:0xf\n"
    "s_mul_i32 s76, s56, 0xc0000\n"
    "s_add_u32 s77, s76, 0x1840000\n"
    "v_min_f32_dpp v197, v197, v197 row_ror:4 row_mask:0xf bank_mask:0xf\n"
    "s_add_u32 s40, s22, s77\n"
    "s_addc_u32 s41, s23, 0\n"
    "v_min_f32_dpp v197, v197, v197 row_ror:2 row_mask:0xf bank_mask:0xf\n"
    "s_mov_b32 s42, 0xc0000\n"
    "s_mov_b32 s43, 0x20000\n"
    "v_min_f32_dpp v197, v197, v197 row_ror:1 row_mask:0xf bank_mask:0xf\n"
    "v_mov_b32 v198, v197\n"
    "s_nop 1\n"
    "v_permlane32_swap_b32 v197, v198\n"
    "v_min_f32 v197, v197, v198\n"
    "v_mov_b32 v198, v197\n"
    "s_nop 1\n"
    "v_permlane16_swap_b32 v197, v198\n"
    "v_min_f32 v197, v197, v198\n"
    "v_rcp_f32 v198, v197\n"
    "v_subrev_u32 v199, 0x37800000, v197\n"
    "v_mul_f32 v197, 0x3fcccccd, v197\n"
    "v_cmp_gt_u32 vcc, 0x10000000, v199\n"
    "v_mul_f32 v198, 0x3f3425ed, v198\n"
    "v_mov_b32 v199, 0x3f800000\n"
    "v_cndmask_b32 v197, 0, v197, vcc\n"
    "v_cndmask_b32 v198, v199, v198, vcc\n"
    "v_readfirstlane_b32 s70, v197\n"
    "v_mul_f32 v197, v197, v198\n"
    "v_readfirstlane_b32 s68, v198\n"
    "v_readfirstlane_b32 s69, v197\n"
    "v_mul_f32 v44, v198, v43\n"
    "v_fma_f32 v45, -v42, v44, v197\n"
    "ds_write_b64 v41, v[44:45]\n"
    "s_waitcnt lgkmcnt(0)\n"
    "s_barrier\n"
    "ds_read_u16_d16_hi v172, v217 offset:6144\n"
    "ds_read_u16_d16_hi v173, v217\n"
    "ds_read_u16_d16_hi v174, v217 offset:6336\n"
    "ds_read_u16_d16_hi v175, v217 offset:192\n"
    "ds_read_u16_d16_hi v223, v209 offset:6144\n"
    "ds_read_u16_d16_hi v222, v209\n"
    "ds_read_b128 v[232:235], v205\n"
    "ds_read_b128 v[236:239], v206\n"
    "ds_read_u16_d16_hi v161, v209 offset:6336\n"
    "ds_read_u16_d16_hi v160, v209 offset:192\n"
    "ds_read_b128 v[64:67], v205 offset:12288\n"
    "ds_read_b128 v[68:71], v206 offset:12288\n"
    "s_waitcnt lgkmcnt(10)\n"
    "v_mul_f32 v197, v172, v173\n"
    "s_waitcnt lgkmcnt(8)\n"
    "v_mul_f32 v198, v174, v175\n"
    "v_min_f32 v197, v197, v198\n"
    "v_max_f32 v197, 0, v197\n"
    "v_mul_f32 v217, 0x3e3fe8d2, v197\n"
    "ds_read_b64 v[228:229], v210\n"
    "ds_read2st64_b32 v[240:241], v207 offset0:48 offset1:51\n"
    "ds_read2st64_b32 v[242:243], v207 offset0:54 offset1:57\n"
    "ds_read2st64_b32 v[244:245], v207 offset0:60 offset1:63\n"
    "ds_read2st64_b32 v[246:247], v207 offset0:66 offset1:69\n"
    "s_waitcnt lgkmcnt(12)\n"
    "v_mul_f32 v226, s75, v223\n"
    "s_waitcnt lgkmcnt(11)\n"
    "v_fma_f32 v227, -v222, v226, v217\n"
    "s_waitcnt lgkmcnt(10)\n"
    "v_cvt_f32_i32 v232, v232\n"
    "v_cvt_f32_i32 v233, v233\n"
    "v_cvt_f32_i32 v234, v234\n"
    "v_cvt_f32_i32 v235, v235\n"
    "s_waitcnt lgkmcnt(9)\n"
    "v_cvt_f32_i32 v236, v236\n"
    "v_cvt_f32_i32 v237, v237\n"
    "v_cvt_f32_i32 v238, v238\n"
    "v_cvt_f32_i32 v239, v239\n"
    "v_mul_f32_dpp v232, v226, v232 row_newbcast:0 row_mask:0xf bank_mask:0xf\n"
    "v_mul_f32_dpp v233, v226, v233 row_newbcast:1 row_mask:0xf bank_mask:0xf\n"
    "v_mul_f32_dpp v234, v226, v234 row_newbcast:2 row_mask:0xf bank_mask:0xf\n"
    "v_mul_f32_dpp v235, v226, v235 row_newbcast:3 row_mask:0xf bank_mask:0xf\n"
    "v_mul_f32_dpp v236, v226, v236 row_newbcast:4 row_mask:0xf bank_mask:0xf\n"
    "v_mul_f32_dpp v237, v226, v237 row_newbcast:5 row_mask:0xf bank_mask:0xf\n"
    "v_mul_f32_dpp v238, v226, v238 row_newbcast:6 row_mask:0xf bank_mask:0xf\n"
    "v_mul_f32_dpp v239, v226, v239 row_newbcast:7 row_mask:0xf bank_mask:0xf\n"
    "v_add_f32_dpp v232, v227, v232 row_newbcast:0 row_mask:0xf bank_mask:0xf\n"
    "v_add_f32_dpp v233, v227, v233 row_newbcast:1 row_mask:0xf bank_mask:0xf\n"
    "v_add_f32_dpp v234, v227, v234 row_newbcast:2 row_mask:0xf bank_mask:0xf\n"
    "v_add_f32_dpp v235, v227, v235 row_newbcast:3 row_mask:0xf bank_mask:0xf\n"
    "v_add_f32_dpp v236, v227, v236 row_newbcast:4 row_mask:0xf bank_mask:0xf\n"
    "v_add_f32_dpp v237, v227, v237 row_newbcast:5 row_mask:0xf bank_mask:0xf\n"
    "v_add_f32_dpp v238, v227, v238 row_newbcast:6 row_mask:0xf bank_mask:0xf\n"
    "v_add_f32_dpp v239, v227, v239 row_newbcast:7 row_mask:0xf bank_mask:0xf\n"
    "v_cvt_pk_bf16_f32 v232, v232, v233\n"
    "v_cvt_pk_bf16_f32 v233, v234, v235\n"
    "v_cvt_pk_bf16_f32 v234, v236, v237\n"
    "v_cvt_pk_bf16_f32 v235, v238, v239\n"
    "buffer_store_dwordx4 v[232:235], v211, s[36:39], s62 offen sc1\n"
    "s_waitcnt lgkmcnt(8)\n"
    "v_mul_f32 v166, s75, v161\n"
    "s_waitcnt lgkmcnt(7)\n"
    "v_fma_f32 v167, -v160, v166, v217\n"
    "s_waitcnt lgkmcnt(6)\n"
    "v_cvt_f32_i32 v64, v64\n"
    "v_cvt_f32_i32 v65, v65\n"
    "v_cvt_f32_i32 v66, v66\n"
    "v_cvt_f32_i32 v67, v67\n"
    "s_waitcnt lgkmcnt(5)\n"
    "v_cvt_f32_i32 v68, v68\n"
    "v_cvt_f32_i32 v69, v69\n"
    "v_cvt_f32_i32 v70, v70\n"
    "v_cvt_f32_i32 v71, v71\n"
    "v_mul_f32_dpp v64, v166, v64 row_newbcast:0 row_mask:0xf bank_mask:0xf\n"
    "v_mul_f32_dpp v65, v166, v65 row_newbcast:1 row_mask:0xf bank_mask:0xf\n"
    "v_mul_f32_dpp v66, v166, v66 row_newbcast:2 row_mask:0xf bank_mask:0xf\n"
    "v_mul_f32_dpp v67, v166, v67 row_newbcast:3 row_mask:0xf bank_mask:0xf\n"
    "v_mul_f32_dpp v68, v166, v68 row_newbcast:4 row_mask:0xf bank_mask:0xf\n"
    "v_mul_f32_dpp v69, v166, v69 row_newbcast:5 row_mask:0xf bank_mask:0xf\n"
    "v_mul_f32_dpp v70, v166, v70 row_newbcast:6 row_mask:0xf bank_mask:0xf\n"
    "v_mul_f32_dpp v71, v166, v71 row_newbcast:7 row_mask:0xf bank_mask:0xf\n"
    "v_add_f32_dpp v64, v167, v64 row_newbcast:0 row_mask:0xf bank_mask:0xf\n"
    "v_add_f32_dpp v65, v167, v65 row_newbcast:1 row_mask:0xf bank_mask:0xf\n"
    "v_add_f32_dpp v66, v167, v66 row_newbcast:2 row_mask:0xf bank_mask:0xf\n"
    "v_add_f32_dpp v67, v167, v67 row_newbcast:3 row_mask:0xf bank_mask:0xf\n"
    "v_add_f32_dpp v68, v167, v68 row_newbcast:4 row_mask:0xf bank_mask:0xf\n"
    "v_add_f32_dpp v69, v167, v69 row_newbcast:5 row_mask:0xf bank_mask:0xf\n"
    "v_add_f32_dpp v70, v167, v70 row_newbcast:6 row_mask:0xf bank_mask:0xf\n"
    "v_add_f32_dpp v71, v167, v71 row_newbcast:7 row_mask:0xf bank_mask:0xf\n"
    "v_cvt_pk_bf16_f32 v64, v64, v65\n"
    "v_cvt_pk_bf16_f32 v65, v66, v67\n"
    "v_cvt_pk_bf16_f32 v66, v68, v69\n"
    "v_cvt_pk_bf16_f32 v67, v70, v71\n"
    "s_add_u32 s78, s87, 0\n"
    "buffer_store_dwordx4 v[64:67], v211, s[36:39], s78 offen sc1\n"
    "s_waitcnt lgkmcnt(3)\n"
    "v_cvt_f32_i32 v240, v240\n"
    "v_cvt_f32_i32 v241, v241\n"
    "s_waitcnt lgkmcnt(2)\n"
    "v_cvt_f32_i32 v242, v242\n"
    "v_cvt_f32_i32 v243, v243\n"
    "s_waitcnt lgkmcnt(1)\n"
    "v_cvt_f32_i32 v244, v244\n"
    "v_cvt_f32_i32 v245, v245\n"
    "s_waitcnt lgkmcnt(0)\n"
    "v_cvt_f32_i32 v246, v246\n"
    "v_cvt_f32_i32 v247, v247\n"
    "v_mul_f32_dpp v240, v228, v240 row_newbcast:0 row_mask:0xf bank_mask:0xf\n"
    "v_mul_f32_dpp v241, v228, v241 row_newbcast:1 row_mask:0xf bank_mask:0xf\n"
    "v_mul_f32_dpp v242, v228, v242 row_newbcast:2 row_mask:0xf bank_mask:0xf\n"
    "v_mul_f32_dpp v243, v228, v243 row_newbcast:3 row_mask:0xf bank_mask:0xf\n"
    "v_mul_f32_dpp v244, v228, v244 row_newbcast:4 row_mask:0xf bank_mask:0xf\n"
    "v_mul_f32_dpp v245, v228, v245 row_newbcast:5 row_mask:0xf bank_mask:0xf\n"
    "v_mul_f32_dpp v246, v228, v246 row_newbcast:6 row_mask:0xf bank_mask:0xf\n"
    "v_mul_f32_dpp v247, v228, v247 row_newbcast:7 row_mask:0xf bank_mask:0xf\n"
    "v_add_f32_dpp v240, v229, v240 row_newbcast:0 row_mask:0xf bank_mask:0xf\n"
    "v_add_f32_dpp v241, v229, v241 row_newbcast:1 row_mask:0xf bank_mask:0xf\n"
    "v_add_f32_dpp v242, v229, v242 row_newbcast:2 row_mask:0xf bank_mask:0xf\n"
    "v_add_f32_dpp v243, v229, v243 row_newbcast:3 row_mask:0xf bank_mask:0xf\n"
    "v_add_f32_dpp v244, v229, v244 row_newbcast:4 row_mask:0xf bank_mask:0xf\n"
    "v_add_f32_dpp v245, v229, v245 row_newbcast:5 row_mask:0xf bank_mask:0xf\n"
    "v_add_f32_dpp v246, v229, v246 row_newbcast:6 row_mask:0xf bank_mask:0xf\n"
    "v_add_f32_dpp v247, v229, v247 row_newbcast:7 row_mask:0xf bank_mask:0xf\n"
    "v_cvt_pk_bf16_f32 v240, v240, v241\n"
    "v_cvt_pk_bf16_f32 v241, v242, v243\n"
    "v_cvt_pk_bf16_f32 v242, v244, v245\n"
    "v_cvt_pk_bf16_f32 v243, v246, v247\n"
    "s_waitcnt vmcnt(8)\n"
    "v_add_u32 v221, s74, v221\n"
    "s_mov_b32 s73, 0\n"
    "v_readfirstlane_b32 s67, v221\n"
    "s_mov_b32 s78, 0\n"
    "buffer_store_dwordx4 v[240:243], v212, s[40:43], s78 offen sc1\n"
    "s_add_u32 s78, s88, 0\n"
    "s_add_u32 m0, s71, 0xc00\n"
    "s_nop 0\n"
    "buffer_load_dwordx4 v155, s[28:31], s78 offen nt lds\n"
    "s_add_u32 s78, s88, 0\n"
    "s_add_u32 m0, s71, 0x1000\n"
    "s_nop 0\n"
    "buffer_load_dwordx4 v156, s[28:31], s78 offen nt lds\n"
    "s_add_u32 s78, s88, 0\n"
    "s_add_u32 m0, s71, 0x1400\n"
    "s_nop 0\n"
    "buffer_load_dwordx4 v157, s[28:31], s78 offen nt lds\n"
    "s_add_u32 s78, s88, 0x1800\n"
    "s_add_u32 m0, s71, 0x6000\n"
    "s_nop 0\n"
    "buffer_load_dwordx4 v152, s[28:31], s78 offen nt lds\n"
    "v_lshlrev_b32 v200, 4, v231\n"
    "v_add_u32 v201, 0xc000, v200\n"
    "v_mul_u32_u24 v198, 0x180, v46\n"
    "v_add_u32 v198, s71, v198\n"
    "v_lshrrev_b32 v197, 1, v46\n"
    "v_xor_b32 v197, v197, v47\n"
    "v_lshl_add_u32 v158, v197, 4, v198\n"
    "v_xor_b32 v159, 64, v158\n"
    "v_and_b32 v197, 3, v231\n"
    "v_cmp_eq_u32 vcc, 0, v197\n"
    "v_mov_b32 v198, 0x3f803f80\n"
    "s_nop 0\n"
    "v_cndmask_b32 v192, 0, v198, vcc\n"
    "v_cndmask_b32 v193, 0, v198, vcc\n"
    "v_cndmask_b32 v194, 0, v198, vcc\n"
    "v_cndmask_b32 v195, 0, v198, vcc\n"
    "s_and_b32 s77, s57, 1\n"
    "s_mul_i32 s77, s77, 0xc0\n"
    "s_sub_u32 s77, s77, 0x400\n"
    "v_mul_u32_u24 v197, 0x1556, v145\n"
    "v_lshrrev_b32 v197, 16, v197\n"
    "v_mul_u32_u24 v196, 12, v197\n"
    "v_sub_u32 v196, v145, v196\n"
    "v_mul_u32_u24 v249, 0x180, v197\n"
    "v_lshl_add_u32 v249, v196, 4, v249\n"
    "v_add_u32 v249, s77, v249\n"
    "s_and_b32 s77, s57, 1\n"
    "s_mul_i32 s77, s77, 0xc0\n"
    "s_sub_u32 s77, s77, 0x800\n"
    "v_mul_u32_u24 v197, 0x1556, v146\n"
    "v_lshrrev_b32 v197, 16, v197\n"
    "v_mul_u32_u24 v196, 12, v197\n"
    "v_sub_u32 v196, v146, v196\n"
    "v_mul_u32_u24 v250, 0x180, v197\n"
    "v_lshl_add_u32 v250, v196, 4, v250\n"
    "v_add_u32 v250, s77, v250\n"
    "s_mul_i32 s66, s74, 2\n"
    "s_waitcnt vmcnt(4)\n"
    "s_barrier\n"
    "v_mov_b32 v220, 2\n"
    "buffer_store_dwordx2 v[220:221], v214, s[44:47], 0 offen sc1\n"
    "s_add_u32 s78, s64, 0x18000\n"
    "s_add_u32 m0, s72, 0x1b000\n"
    "s_nop 0\n"
    "buffer_load_dwordx4 v202, s[32:35], s78 offen nt lds\n"
    "buffer_load_dwordx4 v203, s[32:35], s78 offen offset:1024 nt lds\n"
    "buffer_load_dwordx4 v204, s[32:35], s78 offen offset:2048 nt lds\n"
    "ds_read_u16_d16_hi v163, v209 offset:6528\n"
    "ds_read_u16_d16_hi v162, v209 offset:384\n"
    "ds_read_b128 v[72:75], v205 offset:49152\n"
    "ds_read_b128 v[76:79], v206 offset:49152\n"
    "ds_read_b64 v[170:171], v210 offset:64\n"
    "ds_read2st64_b32 v[80:81], v207 offset0:192 offset1:195\n"
    "ds_read2st64_b32 v[82:83], v207 offset0:198 offset1:201\n"
    "ds_read2st64_b32 v[84:85], v207 offset0:204 offset1:207\n"
    "ds_read2st64_b32 v[86:87], v207 offset0:210 offset1:213\n"
    "s_waitcnt lgkmcnt(8)\n"
    "v_mul_f32 v168, s75, v163\n"
    "s_waitcnt lgkmcnt(7)\n"
    "v_fma_f32 v169, -v162, v168, v217\n"
    "s_waitcnt lgkmcnt(6)\n"
    "v_cvt_f32_i32 v72, v72\n"
    "v_cvt_f32_i32 v73, v73\n"
    "v_cvt_f32_i32 v74, v74\n"
    "v_cvt_f32_i32 v75, v75\n"
    "s_waitcnt lgkmcnt(5)\n"
    "v_cvt_f32_i32 v76, v76\n"
    "v_cvt_f32_i32 v77, v77\n"
    "v_cvt_f32_i32 v78, v78\n"
    "v_cvt_f32_i32 v79, v79\n"
    "v_mul_f32_dpp v72, v168, v72 row_newbcast:0 row_mask:0xf bank_mask:0xf\n"
    "v_mul_f32_dpp v73, v168, v73 row_newbcast:1 row_mask:0xf bank_mask:0xf\n"
    "v_mul_f32_dpp v74, v168, v74 row_newbcast:2 row_mask:0xf bank_mask:0xf\n"
    "v_mul_f32_dpp v75, v168, v75 row_newbcast:3 row_mask:0xf bank_mask:0xf\n"
    "v_mul_f32_dpp v76, v168, v76 row_newbcast:4 row_mask:0xf bank_mask:0xf\n"
    "v_mul_f32_dpp v77, v168, v77 row_newbcast:5 row_mask:0xf bank_mask:0xf\n"
    "v_mul_f32_dpp v78, v168, v78 row_newbcast:6 row_mask:0xf bank_mask:0xf\n"
    "v_mul_f32_dpp v79, v168, v79 row_newbcast:7 row_mask:0xf bank_mask:0xf\n"
    "v_add_f32_dpp v72, v169, v72 row_newbcast:0 row_mask:0xf bank_mask:0xf\n"
    "v_add_f32_dpp v73, v169, v73 row_newbcast:1 row_mask:0xf bank_mask:0xf\n"
    "v_add_f32_dpp v74, v169, v74 row_newbcast:2 row_mask:0xf bank_mask:0xf\n"
    "v_add_f32_dpp v75, v169, v75 row_newbcast:3 row_mask:0xf bank_mask:0xf\n"
    "v_add_f32_dpp v76, v169, v76 row_newbcast:4 row_mask:0xf bank_mask:0xf\n"
    "v_add_f32_dpp v77, v169, v77 row_newbcast:5 row_mask:0xf bank_mask:0xf\n"
    "v_add_f32_dpp v78, v169, v78 row_newbcast:6 row_mask:0xf bank_mask:0xf\n"
    "v_add_f32_dpp v79, v169, v79 row_newbcast:7 row_mask:0xf bank_mask:0xf\n"
    "v_cvt_pk_bf16_f32 v72, v72, v73\n"
    "v_cvt_pk_bf16_f32 v73, v74, v75\n"
    "v_cvt_pk_bf16_f32 v74, v76, v77\n"
    "v_cvt_pk_bf16_f32 v75, v78, v79\n"
    "s_waitcnt lgkmcnt(3)\n"
    "v_cvt_f32_i32 v80, v80\n"
    "v_cvt_f32_i32 v81, v81\n"
    "s_waitcnt lgkmcnt(2)\n"
    "v_cvt_f32_i32 v82, v82\n"
    "v_cvt_f32_i32 v83, v83\n"
    "s_waitcnt lgkmcnt(1)\n"
    "v_cvt_f32_i32 v84, v84\n"
    "v_cvt_f32_i32 v85, v85\n"
    "s_waitcnt lgkmcnt(0)\n"
    "v_cvt_f32_i32 v86, v86\n"
    "v_cvt_f32_i32 v87, v87\n"
    "v_mul_f32_dpp v80, v170, v80 row_newbcast:0 row_mask:0xf bank_mask:0xf\n"
    "v_mul_f32_dpp v81, v170, v81 row_newbcast:1 row_mask:0xf bank_mask:0xf\n"
    "v_mul_f32_dpp v82, v170, v82 row_newbcast:2 row_mask:0xf bank_mask:0xf\n"
    "v_mul_f32_dpp v83, v170, v83 row_newbcast:3 row_mask:0xf bank_mask:0xf\n"
    "v_mul_f32_dpp v84, v170, v84 row_newbcast:4 row_mask:0xf bank_mask:0xf\n"
    "v_mul_f32_dpp v85, v170, v85 row_newbcast:5 row_mask:0xf bank_mask:0xf\n"
    "v_mul_f32_dpp v86, v170, v86 row_newbcast:6 row_mask:0xf bank_mask:0xf\n"
    "v_mul_f32_dpp v87, v170, v87 row_newbcast:7 row_mask:0xf bank_mask:0xf\n"
    "v_add_f32_dpp v80, v171, v80 row_newbcast:0 row_mask:0xf bank_mask:0xf\n"
    "v_add_f32_dpp v81, v171, v81 row_newbcast:1 row_mask:0xf bank_mask:0xf\n"
    "v_add_f32_dpp v82, v171, v82 row_newbcast:2 row_mask:0xf bank_mask:0xf\n"
    "v_add_f32_dpp v83, v171, v83 row_newbcast:3 row_mask:0xf bank_mask:0xf\n"
    "v_add_f32_dpp v84, v171, v84 row_newbcast:4 row_mask:0xf bank_mask:0xf\n"
    "v_add_f32_dpp v85, v171, v85 row_newbcast:5 row_mask:0xf bank_mask:0xf\n"
    "v_add_f32_dpp v86, v171, v86 row_newbcast:6 row_mask:0xf bank_mask:0xf\n"
    "v_add_f32_dpp v87, v171, v87 row_newbcast:7 row_mask:0xf bank_mask:0xf\n"
    "v_cvt_pk_bf16_f32 v80, v80, v81\n"
    "v_cvt_pk_bf16_f32 v81, v82, v83\n"
    "v_cvt_pk_bf16_f32 v82, v84, v85\n"
    "v_cvt_pk_bf16_f32 v83, v86, v87\n"
    "buffer_load_dwordx2 v[218:219], v213, s[44:47], 0 offen sc1\n"
    "s_add_u32 s78, s87, 0x6000\n"
    "buffer_store_dwordx4 v[72:75], v211, s[36:39], s78 offen sc1\n"
    "s_mov_b32 s78, 0x6000\n"
    "buffer_store_dwordx4 v[80:83], v212, s[40:43], s78 offen sc1\n"
    "s_add_u32 s78, s88, 0x1800\n"
    "s_add_u32 m0, s71, 0x6400\n"
    "s_nop 0\n"
    "buffer_load_dwordx4 v153, s[28:31], s78 offen nt lds\n"
    "s_add_u32 s78, s88, 0x1800\n"
    "s_add_u32 m0, s71, 0x6800\n"
    "s_nop 0\n"
    "buffer_load_dwordx4 v154, s[28:31], s78 offen nt lds\n"
    "s_add_u32 s78, s88, 0x1800\n"
    "s_add_u32 m0, s71, 0x6c00\n"
    "s_nop 0\n"
    "buffer_load_dwordx4 v155, s[28:31], s78 offen nt lds\n"
    "s_add_u32 s78, s88, 0x1800\n"
    "s_add_u32 m0, s71, 0x7000\n"
    "s_nop 0\n"
    "buffer_load_dwordx4 v156, s[28:31], s78 offen nt lds\n"
    "s_add_u32 s78, s88, 0x1800\n"
    "s_add_u32 m0, s71, 0x7400\n"
    "s_nop 0\n"
    "buffer_load_dwordx4 v157, s[28:31], s78 offen nt lds\n"
    "s_waitcnt vmcnt(13)\n"
    "ds_read_b128 a[192:195], v158\n"
    "ds_read_b128 a[196:199], v159\n"
    "ds_read_b128 a[200:203], v158 offset:128\n"
    "ds_read_b128 a[204:207], v159 offset:128\n"
    "ds_read_b128 a[208:211], v158 offset:256\n"
    "ds_read_b128 a[212:215], v159 offset:256\n"
    "s_waitcnt vmcnt(7)\n"
    "v_cmp_ne_u32 s[80:81], s67, v219\n"
    "v_cmp_gt_u32 vcc, s66, v218\n"
    "s_or_b64 vcc, vcc, s[80:81]\n"
    "s_cbranch_vccnz LSPRE0\n"
    "LSPB0:\n"
    "s_waitcnt vmcnt(5)\n"
    "s_waitcnt lgkmcnt(0)\n"
    "s_barrier\n"
    "v_mov_b32 v220, 3\n"
    "buffer_store_dwordx2 v[220:221], v214, s[44:47], 0 offen sc1\n"
    "s_add_u32 m0, s72, 0x1e000\n"
    "s_nop 0\n"
    "buffer_load_dwordx4 v200, s[36:39], s72 offen sc1 lds\n"
    "s_add_u32 m0, s72, 0x1e000\n"
    "s_nop 0\n"
    "buffer_load_dwordx4 v200, s[36:39], s72 offen offset:1024 sc1 lds\n"
    "s_add_u32 m0, s72, 0x1e000\n"
    "s_nop 0\n"
    "buffer_load_dwordx4 v200, s[36:39], s72 offen offset:2048 sc1 lds\n"
    "s_add_u32 s65, s71, 0x3000\n"
    "s_add_u32 m0, s71, 0\n"
    "s_nop 0\n"
    "buffer_load_dwordx4 v200, s[36:39], s65 offen sc1 lds\n"
    "s_add_u32 s65, s71, 0x3400\n"
    "s_add_u32 m0, s71, 0x400\n"
    "s_nop 0\n"
    "buffer_load_dwordx4 v200, s[36:39], s65 offen sc1 lds\n"
    "s_add_u32 s65, s71, 0x3800\n"
    "s_add_u32 m0, s71, 0x800\n"
    "s_nop 0\n"
    "buffer_load_dwordx4 v200, s[36:39], s65 offen sc1 lds\n"
    "s_add_u32 s65, s71, 0x3c00\n"
    "s_add_u32 m0, s71, 0xc00\n"
    "s_nop 0\n"
    "buffer_load_dwordx4 v200, s[36:39], s65 offen sc1 lds\n"
    "s_add_u32 s65, s71, 0x4000\n"
    "s_add_u32 m0, s71, 0x1000\n"
    "s_nop 0\n"
    "buffer_load_dwordx4 v200, s[36:39], s65 offen sc1 lds\n"
    "s_add_u32 s65, s71, 0x4400\n"
    "s_add_u32 m0, s71, 0x1400\n"
    "s_nop 0\n"
    "buffer_load_dwordx4 v200, s[36:39], s65 offen sc1 lds\n"
    "s_add_u32 s65, s71, 0\n"
    "s_add_u32 m0, s71, 0x12000\n"
    "s_nop 0\n"
    "buffer_load_dwordx4 v200, s[40:43], s65 offen sc1 lds\n"
    "s_add_u32 s65, s71, 0x400\n"
    "s_add_u32 m0, s71, 0x12400\n"
    "s_nop 0\n"
    "buffer_load_dwordx4 v200, s[40:43], s65 offen sc1 lds\n"
    "s_add_u32 s65, s71, 0x800\n"
    "s_add_u32 m0, s71, 0x12800\n"
    "s_nop 0\n"
    "buffer_load_dwordx4 v200, s[40:43], s65 offen sc1 lds\n"
    "s_add_u32 s65, s71, 0xc00\n"
    "s_add_u32 m0, s71, 0x12c00\n"
    "s_nop 0\n"
    "buffer_load_dwordx4 v200, s[40:43], s65 offen sc1 lds\n"
    "s_add_u32 s65, s71, 0x1000\n"
    "s_add_u32 m0, s71, 0x13000\n"
    "s_nop 0\n"
    "buffer_load_dwordx4 v200, s[40:43], s65 offen sc1 lds\n"
    "s_add_u32 s65, s71, 0x1400\n"
    "s_add_u32 m0, s71, 0x13400\n"
    "s_nop 0\n"
    "buffer_load_dwordx4 v200, s[40:43], s65 offen sc1 lds\n"
    "s_add_u32 s78, s64, 0x24000\n"
    "s_add_u32 m0, s72, 0x18000\n"
    "s_nop 0\n"
    "buffer_load_dwordx4 v202, s[32:35], s78 offen nt lds\n"
    "buffer_load_dwordx4 v203, s[32:35], s78 offen offset:1024 nt lds\n"
    "buffer_load_dwordx4 v204, s[32:35], s78 offen offset:2048 nt lds\n"
    "s_add_u32 s78, s88, 0x3000\n"
    "s_add_u32 m0, s71, 0xc000\n"
    "s_nop 0\n"
    "buffer_load_dwordx4 v152, s[28:31], s78 offen nt lds\n"
    "s_add_u32 s78, s88, 0x3000\n"
    "s_add_u32 m0, s71, 0xc400\n"
    "s_nop 0\n"
    "buffer_load_dwordx4 v153, s[28:31], s78 offen nt lds\n"
    "s_add_u32 s78, s88, 0x3000\n"
    "s_add_u32 m0, s71, 0xc800\n"
    "s_nop 0\n"
    "buffer_load_dwordx4 v154, s[28:31], s78 offen nt lds\n"
    "s_mul_i32 s66, s74, 3\n"
    "buffer_load_dwordx2 v[218:219], v213, s[44:47], 0 offen sc1\n"
    "s_waitcnt vmcnt(23)\n"
    "ds_read_b128 a[216:219], v158 offset:24576\n"
    "ds_read_b128 a[220:223], v159 offset:24576\n"
    "ds_read_b128 a[224:227], v158 offset:24704\n"
    "ds_read_b128 a[228:231], v159 offset:24704\n"
    "ds_read_b128 a[232:235], v158 offset:24832\n"
    "ds_read_b128 a[236:239], v159 offset:24832\n"
    "s_add_u32 m0, s72, 0x21000\n"
    "s_nop 0\n"
    "buffer_load_dwordx4 v249, s[48:51], 0 offen offset:1024 lds\n"
    "v_add_u32 v255, 0x1e000, v200\n"
    "s_waitcnt vmcnt(14) lgkmcnt(0)\n"
    "s_add_u32 s60, s71, 0x9000\n"
    "s_add_u32 s61, s71, 0x6000\n"
    "s_add_u32 s62, s87, 0x12000\n"
    "s_mov_b32 s63, 0x12000\n"
    "s_mov_b32 s59, 0\n"
    "s_mul_i32 s66, s74, 3\n"
    "s_mul_i32 s90, s58, 0x1c00\n"
    "s_sub_u32 s90, s90, 0x1000\n"
    "s_sub_u32 s92, s90, s71\n"
    "s_barrier\n"
    "ds_read_b128 v[160:163], v255\n"
    "ds_read_b128 v[164:167], v255 offset:1024\n"
    "ds_read_b128 v[168:171], v255 offset:2048\n"
    "ds_read_b128 v[172:175], v255 offset:3072\n"
    "ds_read_b128 v[176:179], v255 offset:4096\n"
    "ds_read_b128 v[180:183], v255 offset:5120\n"
    "s_add_u32 s76, s87, 0xc000\n"
    "s_mov_b32 s77, 0xc000\n"
    "s_waitcnt lgkmcnt(4)\n"
    "v_mfma_f32_16x16x32_bf16 v[40:43], v[160:163], a[192:195], 0\n"
    "ds_read_b128 v[184:187], v255 offset:6144\n"
    "ds_read_b64 v[228:229], v210 offset:128\n"
    "v_mfma_f32_16x16x32_bf16 v[40:43], v[164:167], a[196:199], v[40:43]\n"
    "s_add_u32 s78, s88, 0x3000\n"
    "ds_read2st64_b32 v[240:241], v207 offset0:144 offset1:147\n"
    "ds_read2st64_b32 v[242:243], v207 offset0:150 offset1:153\n"
    "v_mfma_f32_16x16x32_bf16 v[44:47], v[160:163], a[216:219], 0\n"
    "s_add_u32 m0, s71, 0xcc00\n"
    "ds_read_b128 v[188:191], v255 offset:7168\n"
    "ds_read2st64_b32 v[244:245], v207 offset0:156 offset1:159\n"
    "v_mfma_f32_16x16x32_bf16 v[44:47], v[164:167], a[220:223], v[44:47]\n"
    "s_waitcnt lgkmcnt(8)\n"
    "buffer_load_dwordx4 v155, s[28:31], s78 offen nt lds\n"
    "v_mfma_f32_16x16x32_bf16 v[40:43], v[168:171], a[200:203], v[40:43]\n"
    "ds_read_b128 v[160:163], v255 offset:8192\n"
    "ds_read2st64_b32 v[246:247], v207 offset0:162 offset1:165\n"
    "v_mfma_f32_16x16x32_bf16 v[40:43], v[172:175], a[204:207], v[40:43]\n"
    "ds_read_u16_d16_hi v223, v209 offset:6720\n"
    "ds_read_u16_d16_hi v222, v209 offset:576\n"
    "v_mfma_f32_16x16x32_bf16 v[44:47], v[168:171], a[224:227], v[44:47]\n"
    "ds_read_b128 v[164:167], v255 offset:9216\n"
    "ds_read_b128 v[232:235], v205 offset:36864\n"
    "v_mfma_f32_16x16x32_bf16 v[44:47], v[172:175], a[228:231], v[44:47]\n"
    "s_waitcnt lgkmcnt(12)\n"
    "ds_read_b128 v[236:239], v206 offset:36864\n"
    "v_mfma_f32_16x16x32_bf16 v[40:43], v[176:179], a[208:211], v[40:43]\n"
    "ds_read_b128 v[168:171], v255 offset:10240\n"
    "v_mfma_f32_16x16x32_bf16 v[40:43], v[180:183], a[212:215], v[40:43]\n"
    "v_mfma_f32_16x16x32_bf16 v[44:47], v[176:179], a[232:235], v[44:47]\n"
    "ds_read_b128 v[172:175], v255 offset:11264\n"
    "v_mfma_f32_16x16x32_bf16 v[44:47], v[180:183], a[236:239], v[44:47]\n"
    "s_waitcnt lgkmcnt(10)\n"
    "v_cvt_f32_i32 v240, v240\n"
    "v_cvt_f32_i32 v241, v241\n"
    "v_mfma_f32_16x16x32_bf16 v[56:59], v[184:187], a[192:195], 0\n"
    "ds_read_b128 v[176:179], v200\n"
    "v_cvt_f32_i32 v242, v242\n"
    "v_mfma_f32_16x16x32_bf16 v[56:59], v[188:191], a[196:199], v[56:59]\n"
    "s_add_u32 s78, s88, 0x3000\n"
    "v_cvt_f32_i32 v243, v243\n"
    "s_waitcnt lgkmcnt(10)\n"
    "v_cvt_f32_i32 v244, v244\n"
    "v_mfma_f32_16x16x32_bf16 v[60:63], v[184:187], a[216:219], 0\n"
    "s_add_u32 m0, s71, 0xd000\n"
    "ds_read_b128 v[180:183], v200 offset:1024\n"
    "v_cvt_f32_i32 v245, v245\n"
    "v_mfma_f32_16x16x32_bf16 v[60:63], v[188:191], a[220:223], v[60:63]\n"
    "s_waitcnt lgkmcnt(6)\n"
    "buffer_load_dwordx4 v156, s[28:31], s78 offen nt lds\n"
    "v_mfma_f32_16x16x32_bf16 v[56:59], v[160:163], a[200:203], v[56:59]\n"
    "ds_read_b128 v[184:187], v200 offset:2048\n"
    "v_cvt_f32_i32 v246, v246\n"
    "v_mfma_f32_16x16x32_bf16 v[56:59], v[164:167], a[204:207], v[56:59]\n"
    "v_mul_f32 v226, s75, v223\n"
    "v_cvt_f32_i32 v247, v247\n"
    "v_mfma_f32_16x16x32_bf16 v[60:63], v[160:163], a[224:227], v[60:63]\n"
    "ds_read_b128 v[188:191], v200 offset:3072\n"
    "v_fma_f32 v227, -v222, v226, v217\n"
    "v_mfma_f32_16x16x32_bf16 v[60:63], v[164:167], a[228:231], v[60:63]\n"
    "s_waitcnt lgkmcnt(4)\n"
    "v_mul_f32_dpp v240, v228, v240 row_newbcast:0 row_mask:0xf bank_mask:0xf\n"
    "v_cvt_f32_i32 v232, v232\n"
    "v_mfma_f32_16x16x32_bf16 v[56:59], v[168:171], a[208:211], v[56:59]\n"
    "ds_read_b128 v[160:163], v200 offset:4096\n"
    "v_mul_f32_dpp v241, v228, v241 row_newbcast:1 row_mask:0xf bank_mask:0xf\n"
    "v_mfma_f32_16x16x32_bf16 v[56:59], v[172:175], a[212:215], v[56:59]\n"
    "v_cvt_f32_i32 v233, v233\n"
    "v_mul_f32_dpp v242, v228, v242 row_newbcast:2 row_mask:0xf bank_mask:0xf\n"
    "v_mfma_f32_16x16x32_bf16 v[60:63], v[168:171], a[232:235], v[60:63]\n"
    "ds_read_b128 v[164:167], v200 offset:5120\n"
    "v_cvt_f32_i32 v234, v234\n"
    "v_mfma_f32_16x16x32_bf16 v[60:63], v[172:175], a[236:239], v[60:63]\n"
    "s_waitcnt lgkmcnt(4)\n"
    "v_mul_f32_dpp v243, v228, v243 row_newbcast:3 row_mask:0xf bank_mask:0xf\n"
    "v_cvt_f32_i32 v235, v235\n"
    "v_mfma_f32_16x16x32_bf16 v[64:67], v[176:179], a[192:195], 0\n"
    "ds_read_b128 v[168:171], v200 offset:6144\n"
    "v_mul_f32_dpp v244, v228, v244 row_newbcast:4 row_mask:0xf bank_mask:0xf\n"
    "v_cvt_f32_i32 v236, v236\n"
    "v_mul_f32_dpp v245, v228, v245 row_newbcast:5 row_mask:0xf bank_mask:0xf\n"
    "v_mfma_f32_16x16x32_bf16 v[64:67], v[180:183], a[196:199], v[64:67]\n"
    "s_add_u32 s78, s88, 0x3000\n"
    "v_cvt_f32_i32 v237, v237\n"
    "v_mul_f32_dpp v246, v228, v246 row_newbcast:6 row_mask:0xf bank_mask:0xf\n"
    "v_cvt_f32_i32 v238, v238\n"
    "v_mul_f32_dpp v247, v228, v247 row_newbcast:7 row_mask:0xf bank_mask:0xf\n"
    "v_mfma_f32_16x16x32_bf16 v[68:71], v[176:179], a[216:219], 0\n"
    "s_add_u32 m0, s71, 0xd400\n"
    "ds_read_b128 v[172:175], v200 offset:7168\n"
    "v_cvt_f32_i32 v239, v239\n"
    "v_add_f32_dpp v240, v229, v240 row_newbcast:0 row_mask:0xf bank_mask:0xf\n"
    "v_mul_f32_dpp v232, v226, v232 row_newbcast:0 row_mask:0xf bank_mask:0xf\n"
    "v_mfma_f32_16x16x32_bf16 v[68:71], v[180:183], a[220:223], v[68:71]\n"
    "s_waitcnt lgkmcnt(4)\n"
    "buffer_load_dwordx4 v157, s[28:31], s78 offen nt lds\n"
    "v_add_f32_dpp v241, v229, v241 row_newbcast:1 row_mask:0xf bank_mask:0xf\n"
    "v_mul_f32_dpp v233, v226, v233 row_newbcast:1 row_mask:0xf bank_mask:0xf\n"
    "v_mfma_f32_16x16x32_bf16 v[64:67], v[184:187], a[200:203], v[64:67]\n"
    "ds_read_b128 v[176:179], v200 offset:8192\n"
    "v_add_f32_dpp v242, v229, v242 row_newbcast:2 row_mask:0xf bank_mask:0xf\n"
    "v_mul_f32_dpp v234, v226, v234 row_newbcast:2 row_mask:0xf bank_mask:0xf\n"
    "v_add_f32_dpp v243, v229, v243 row_newbcast:3 row_mask:0xf bank_mask:0xf\n"
    "v_mfma_f32_16x16x32_bf16 v[64:67], v[188:191], a[204:207], v[64:67]\n"
    "v_mul_f32_dpp v235, v226, v235 row_newbcast:3 row_mask:0xf bank_mask:0xf\n"
    "v_add_f32_dpp v244, v229, v244 row_newbcast:4 row_mask:0xf bank_mask:0xf\n"
    "v_mul_f32_dpp v236, v226, v236 row_newbcast:4 row_mask:0xf bank_mask:0xf\n"
    "v_add_f32_dpp v245, v229, v245 row_newbcast:5 row_mask:0xf bank_mask:0xf\n"
    "v_mfma_f32_16x16x32_bf16 v[68:71], v[184:187], a[224:227], v[68:71]\n"
    "ds_read_b128 v[180:183], v200 offset:9216\n"
    "v_mul_f32_dpp v237, v226, v237 row_newbcast:5 row_mask:0xf bank_mask:0xf\n"
    "v_add_f32_dpp v246, v229, v246 row_newbcast:6 row_mask:0xf bank_mask:0xf\n"
    "v_mul_f32_dpp v238, v226, v238 row_newbcast:6 row_mask:0xf bank_mask:0xf\n"
    "v_add_f32_dpp v247, v229, v247 row_newbcast:7 row_mask:0xf bank_mask:0xf\n"
    "v_mul_f32_dpp v239, v226, v239 row_newbcast:7 row_mask:0xf bank_mask:0xf\n"
    "v_cvt_pk_bf16_f32 v240, v240, v241\n"
    "v_add_f32_dpp v232, v227, v232 row_newbcast:0 row_mask:0xf bank_mask:0xf\n"
    "v_cvt_pk_bf16_f32 v241, v242, v243\n"
    "v_add_f32_dpp v233, v227, v233 row_newbcast:1 row_mask:0xf bank_mask:0xf\n"
    "v_cvt_pk_bf16_f32 v242, v244, v245\n"
    "v_add_f32_dpp v234, v227, v234 row_newbcast:2 row_mask:0xf bank_mask:0xf\n"
    "v_cvt_pk_bf16_f32 v243, v246, v247\n"
    "v_add_f32_dpp v235, v227, v235 row_newbcast:3 row_mask:0xf bank_mask:0xf\n"
    "v_add_f32_dpp v236, v227, v236 row_newbcast:4 row_mask:0xf bank_mask:0xf\n"
    "v_add_f32_dpp v237, v227, v237 row_newbcast:5 row_mask:0xf bank_mask:0xf\n"
    "v_add_f32_dpp v238, v227, v238 row_newbcast:6 row_mask:0xf bank_mask:0xf\n"
    "v_add_f32_dpp v239, v227, v239 row_newbcast:7 row_mask:0xf bank_mask:0xf\n"
    "v_cvt_pk_bf16_f32 v232, v232, v233\n"
    "v_cvt_pk_bf16_f32 v233, v234, v235\n"
    "v_cvt_pk_bf16_f32 v234, v236, v237\n"
    "v_cvt_pk_bf16_f32 v235, v238, v239\n"
    "v_mfma_f32_16x16x32_bf16 v[68:71], v[188:191], a[228:231], v[68:71]\n"
    "s_waitcnt lgkmcnt(4)\n"
    "v_max3_f32 v196, v40, v41, v42\n"
    "v_max3_f32 v196, v196, v43, v56\n"
    "v_max3_f32 v196, v196, v57, v58\n"
    "v_max_f32 v196, v196, v59\n"
    "buffer_store_dwordx4 v[232:235], v211, s[36:39], s76 offen sc1\n"
    "v_mfma_f32_16x16x32_bf16 v[64:67], v[160:163], a[208:211], v[64:67]\n"
    "ds_read_b128 v[184:187], v200 offset:10240\n"
    "v_max3_f32 v198, v44, v45, v46\n"
    "v_mov_b32 v197, v196\n"
    "v_max3_f32 v198, v198, v47, v60\n"
    "v_mfma_f32_16x16x32_bf16 v[64:67], v[164:167], a[212:215], v[64:67]\n"
    "v_permlane32_swap_b32 v196, v197\n"
    "v_max3_f32 v198, v198, v61, v62\n"
    "v_max_f32 v196, v196, v197\n"
    "v_max_f32 v198, v198, v63\n"
    "v_mfma_f32_16x16x32_bf16 v[68:71], v[160:163], a[232:235], v[68:71]\n"
    "ds_read_b128 v[188:191], v200 offset:11264\n"
    "v_mov_b32 v197, v196\n"
    "v_mov_b32 v199, v198\n"
    "s_nop 0\n"
    "v_permlane16_swap_b32 v196, v197\n"
    "v_mfma_f32_16x16x32_bf16 v[68:71], v[164:167], a[236:239], v[68:71]\n"
    "s_waitcnt lgkmcnt(4)\n"
    "v_permlane32_swap_b32 v198, v199\n"
    "v_max_f32 v196, v196, v197\n"
    "v_max_f32 v198, v198, v199\n"
    "v_add_f32 v196, 0x42640000, v196\n"
    "v_mfma_f32_16x16x32_bf16 v[80:83], v[168:171], a[192:195], 0\n"
    "ds_read_b128 v[160:163], v200 offset:12288\n"
    "v_mov_b32 v199, v198\n"
    "v_sub_f32 v96, 0, v196\n"
    "s_nop 0\n"
    "v_permlane16_swap_b32 v198, v199\n"
    "v_mfma_f32_16x16x32_bf16 v[80:83], v[172:175], a[196:199], v[80:83]\n"
    "s_add_u32 s78, s88, 0x4800\n"
    "buffer_store_dwordx4 v[240:243], v212, s[40:43], s77 offen sc1\n"
    "v_sub_f32 v97, 0, v196\n"
    "v_max_f32 v198, v198, v199\n"
    "v_sub_f32 v98, 0, v196\n"
    "v_add_f32 v198, 0x42640000, v198\n"
    "v_sub_f32 v99, 0, v196\n"
    "v_mfma_f32_16x16x32_bf16 v[84:87], v[168:171], a[216:219], 0\n"
    "s_add_u32 m0, s71, 0x6000\n"
    "ds_read_b128 v[164:167], v200 offset:13312\n"
    "v_sub_f32 v100, 0, v198\n"
    "v_add_f32 v40, v40, v96\n"
    "v_sub_f32 v101, 0, v198\n"
    "v_mfma_f32_16x16x32_bf16 v[84:87], v[172:175], a[220:223], v[84:87]\n"
    "s_waitcnt lgkmcnt(4)\n"
    "buffer_load_dwordx4 v152, s[28:31], s78 offen nt lds\n"
    "v_add_f32 v41, v41, v96\n"
    "v_sub_f32 v102, 0, v198\n"
    "v_mfma_f32_16x16x32_bf16 v[80:83], v[176:179], a[200:203], v[80:83]\n"
    "ds_read_b128 v[168:171], v200 offset:14336\n"
    "v_add_f32 v42, v42, v96\n"
    "v_sub_f32 v103, 0, v198\n"
    "v_add_f32 v43, v43, v96\n"
    "v_mfma_f32_16x16x32_bf16 v[80:83], v[180:183], a[204:207], v[80:83]\n"
    "v_add_f32 v44, v44, v100\n"
    "v_add_f32 v56, v56, v96\n"
    "v_add_f32 v45, v45, v100\n"
    "v_add_f32 v57, v57, v96\n"
    "v_mfma_f32_16x16x32_bf16 v[84:87], v[176:179], a[224:227], v[84:87]\n"
    "ds_read_b128 v[172:175], v200 offset:15360\n"
    "v_add_f32 v46, v46, v100\n"
    "v_add_f32 v58, v58, v96\n"
    "v_add_f32 v47, v47, v100\n"
    "v_mfma_f32_16x16x32_bf16 v[84:87], v[180:183], a[228:231], v[84:87]\n"
    "s_waitcnt lgkmcnt(4)\n"
    "v_add_f32 v59, v59, v96\n"
    "v_add_f32 v60, v60, v100\n"
    "v_exp_f32 v40, v40\n"
    "v_mfma_f32_16x16x32_bf16 v[80:83], v[184:187], a[208:211], v[80:83]\n"
    "ds_read_b128 v[176:179], v200 offset:16384\n"
    "v_add_f32 v61, v61, v100\n"
    "v_add_f32 v62, v62, v100\n"
    "v_add_f32 v63, v63, v100\n"
    "v_mfma_f32_16x16x32_bf16 v[80:83], v[188:191], a[212:215], v[80:83]\n"
    "v_exp_f32 v41, v41\n"
    "v_exp_f32 v44, v44\n"
    "v_mfma_f32_16x16x32_bf16 v[84:87], v[184:187], a[232:235], v[84:87]\n"
    "ds_read_b128 v[180:183], v200 offset:17408\n"
    "v_exp_f32 v42, v42\n"
    "v_mfma_f32_16x16x32_bf16 v[84:87], v[188:191], a[236:239], v[84:87]\n"
    "s_waitcnt lgkmcnt(4)\n"
    "v_exp_f32 v45, v45\n"
    "v_exp_f32 v43, v43\n"
    "v_mfma_f32_16x16x32_bf16 v[32:35], v[160:163], a[192:195], v[96:99]\n"
    "ds_read_b128 v[184:187], v200 offset:18432\n"
    "v_exp_f32 v46, v46\n"
    "v_mfma_f32_16x16x32_bf16 v[32:35], v[164:167], a[196:199], v[32:35]\n"
    "s_add_u32 s78, s88, 0x4800\n"
    "v_exp_f32 v56, v56\n"
    "v_exp_f32 v47, v47\n"
    "v_mfma_f32_16x16x32_bf16 v[36:39], v[160:163], a[216:219], v[100:103]\n"
    "s_add_u32 m0, s71, 0x6400\n"
    "ds_read_b128 v[188:191], v200 offset:19456\n"
    "v_exp_f32 v57, v57\n"
    "v_mfma_f32_16x16x32_bf16 v[36:39], v[164:167], a[220:223], v[36:39]\n"
    "s_waitcnt lgkmcnt(4)\n"
    "buffer_load_dwordx4 v153, s[28:31], s78 offen nt lds\n"
    "v_exp_f32 v60, v60\n"
    "v_mfma_f32_16x16x32_bf16 v[32:35], v[168:171], a[200:203], v[32:35]\n"
    "ds_read_b128 v[160:163], v200 offset:20480\n"
    "v_exp_f32 v58, v58\n"
    "v_mfma_f32_16x16x32_bf16 v[32:35], v[172:175], a[204:207], v[32:35]\n"
    "v_exp_f32 v61, v61\n"
    "v_exp_f32 v59, v59\n"
    "v_mfma_f32_16x16x32_bf16 v[36:39], v[168:171], a[224:227], v[36:39]\n"
    "ds_read_b128 v[164:167], v200 offset:21504\n"
    "v_exp_f32 v62, v62\n"
    "v_cvt_pk_bf16_f32 v128, v40, v41\n"
    "v_mfma_f32_16x16x32_bf16 v[36:39], v[172:175], a[228:231], v[36:39]\n"
    "s_waitcnt lgkmcnt(4)\n"
    "v_cvt_pk_bf16_f32 v129, v42, v43\n"
    "v_exp_f32 v63, v63\n"
    "v_cvt_pk_bf16_f32 v130, v56, v57\n"
    "v_mfma_f32_16x16x32_bf16 v[32:35], v[176:179], a[208:211], v[32:35]\n"
    "ds_read_b128 v[168:171], v200 offset:22528\n"
    "v_cvt_pk_bf16_f32 v131, v58, v59\n"
    "v_cvt_pk_bf16_f32 v132, v44, v45\n"
    "v_add_f32 v64, v64, v96\n"
    "v_mfma_f32_16x16x32_bf16 v[32:35], v[180:183], a[212:215], v[32:35]\n"
    "v_cvt_pk_bf16_f32 v133, v46, v47\n"
    "v_add_f32 v65, v65, v96\n"
    "v_cvt_pk_bf16_f32 v134, v60, v61\n"
    "v_add_f32 v66, v66, v96\n"
    "v_mfma_f32_16x16x32_bf16 v[36:39], v[176:179], a[232:235], v[36:39]\n"
    "ds_read_b128 v[172:175], v200 offset:23552\n"
    "v_cvt_pk_bf16_f32 v135, v62, v63\n"
    "v_add_f32 v67, v67, v96\n"
    "v_add_f32 v68, v68, v100\n"
    "v_mfma_f32_16x16x32_bf16 v[36:39], v[180:183], a[236:239], v[36:39]\n"
    "s_waitcnt lgkmcnt(4)\n"
    "v_add_f32 v80, v80, v96\n"
    "v_add_f32 v69, v69, v100\n"
    "s_waitcnt vmcnt(12)\n"
    "s_barrier\n"
    "v_mfma_f32_16x16x32_bf16 v[48:51], v[184:187], a[192:195], v[96:99]\n"
    "ds_read_b128 v[176:179], v201 offset:24576\n"
    "v_add_f32 v81, v81, v96\n"
    "v_add_f32 v70, v70, v100\n"
    "v_add_f32 v82, v82, v96\n"
    "v_mfma_f32_16x16x32_bf16 v[48:51], v[188:191], a[196:199], v[48:51]\n"
    "s_add_u32 s78, s88, 0x4800\n"
    "v_add_f32 v71, v71, v100\n"
    "v_add_f32 v83, v83, v96\n"
    "v_add_f32 v84, v84, v100\n"
    "v_add_f32 v85, v85, v100\n"
    "v_mfma_f32_16x16x32_bf16 v[52:55], v[184:187], a[216:219], v[100:103]\n"
    "s_add_u32 m0, s71, 0x6800\n"
    "ds_read_b128 v[180:183], v201 offset:25600\n"
    "v_exp_f32 v64, v64\n"
    "v_add_f32 v86, v86, v100\n"
    "v_mfma_f32_16x16x32_bf16 v[52:55], v[188:191], a[220:223], v[52:55]\n"
    "s_waitcnt lgkmcnt(4)\n"
    "buffer_load_dwordx4 v154, s[28:31], s78 offen nt lds\n"
    "v_exp_f32 v65, v65\n"
    "v_mfma_f32_16x16x32_bf16 v[48:51], v[160:163], a[200:203], v[48:51]\n"
    "ds_read_b128 v[184:187], v201 offset:26624\n"
    "v_add_f32 v87, v87, v100\n"
    "v_exp_f32 v68, v68\n"
    "v_mfma_f32_16x16x32_bf16 v[48:51], v[164:167], a[204:207], v[48:51]\n"
    "v_exp_f32 v66, v66\n"
    "v_exp_f32 v69, v69\n"
    "v_mfma_f32_16x16x32_bf16 v[52:55], v[160:163], a[224:227], v[52:55]\n"
    "ds_read_b128 v[188:191], v201 offset:27648\n"
    "v_exp_f32 v67, v67\n"
    "ds_read_b64 v[228:229], v210 offset:192\n"
    "v_mfma_f32_16x16x32_bf16 v[52:55], v[164:167], a[228:231], v[52:55]\n"
    "s_waitcnt lgkmcnt(5)\n"
    "v_exp_f32 v70, v70\n"
    "v_exp_f32 v80, v80\n"
    "v_mfma_f32_16x16x32_bf16 v[48:51], v[168:171], a[208:211], v[48:51]\n"
    "ds_read_b128 v[160:163], v201 offset:28672\n"
    "v_exp_f32 v71, v71\n"
    "ds_read2st64_b32 v[240:241], v207 offset0:96 offset1:99\n"
    "v_mfma_f32_16x16x32_bf16 v[48:51], v[172:175], a[212:215], v[48:51]\n"
    "v_exp_f32 v81, v81\n"
    "v_exp_f32 v84, v84\n"
    "v_mfma_f32_16x16x32_bf16 v[52:55], v[168:171], a[232:235], v[52:55]\n"
    "ds_read_b128 v[164:167], v201 offset:29696\n"
    "v_exp_f32 v82, v82\n"
    "ds_read2st64_b32 v[242:243], v207 offset0:102 offset1:105\n"
    "v_exp_f32 v83, v83\n"
    "v_cvt_pk_bf16_f32 v144, v64, v65\n"
    "v_cvt_pk_bf16_f32 v145, v66, v67\n"
    "v_cvt_pk_bf16_f32 v146, v80, v81\n"
    "v_cvt_pk_bf16_f32 v147, v82, v83\n"
    "v_mfma_f32_16x16x32_bf16 v[52:55], v[172:175], a[236:239], v[52:55]\n"
    "s_waitcnt lgkmcnt(7)\n"
    "v_exp_f32 v85, v85\n"
    "v_exp_f32 v86, v86\n"
    "v_mfma_f32_16x16x32_bf16 a[0:3], v[176:179], v[128:131], 0\n"
    "ds_read_b128 v[168:171], v201 offset:30720\n"
    "ds_read2st64_b32 v[244:245], v207 offset0:108 offset1:111\n"
    "v_exp_f32 v87, v87\n"
    "v_cvt_pk_bf16_f32 v148, v68, v69\n"
    "v_cvt_pk_bf16_f32 v149, v70, v71\n"
    "v_cvt_pk_bf16_f32 v150, v84, v85\n"
    "v_cvt_pk_bf16_f32 v151, v86, v87\n"
    "v_mfma_f32_16x16x32_bf16 a[0:3], v[180:183], v[144:147], a[0:3]\n"
    "s_add_u32 s78, s88, 0x4800\n"
    "ds_read2st64_b32 v[246:247], v207 offset0:114 offset1:117\n"
    "ds_read_u16_d16_hi v223, v209 offset:6912\n"
    "v_mfma_f32_16x16x32_bf16 a[4:7], v[176:179], v[132:135], 0\n"
    "s_add_u32 m0, s71, 0x6c00\n"
    "ds_read_b128 v[172:175], v201 offset:31744\n"
    "ds_read_u16_d16_hi v222, v209 offset:768\n"
    "v_mfma_f32_16x16x32_bf16 a[4:7], v[180:183], v[148:151], a[4:7]\n"
    "s_waitcnt lgkmcnt(11)\n"
    "buffer_load_dwordx4 v155, s[28:31], s78 offen nt lds\n"
    "v_mfma_f32_16x16x32_bf16 a[16:19], v[184:187], v[128:131], 0\n"
    "ds_read_b128 v[176:179], v201 offset:32768\n"
    "ds_read_b128 v[232:235], v205 offset:24576\n"
    "v_mfma_f32_16x16x32_bf16 a[16:19], v[188:191], v[144:147], a[16:19]\n"
    "v_exp_f32 v32, v32\n"
    "v_mfma_f32_16x16x32_bf16 a[20:23], v[184:187], v[132:135], 0\n"
    "ds_read_b128 v[180:183], v201 offset:33792\n"
    "ds_read_b128 v[236:239], v206 offset:24576\n"
    "v_mfma_f32_16x16x32_bf16 a[20:23], v[188:191], v[148:151], a[20:23]\n"
    "s_waitcnt lgkmcnt(11)\n"
    "v_exp_f32 v33, v33\n"
    "v_mfma_f32_16x16x32_bf16 a[32:35], v[160:163], v[128:131], 0\n"
    "ds_read_b128 v[184:187], v201 offset:34816\n"
    "v_cvt_f32_i32 v240, v240\n"
    "v_mfma_f32_16x16x32_bf16 a[32:35], v[164:167], v[144:147], a[32:35]\n"
    "v_exp_f32 v34, v34\n"
    "v_mfma_f32_16x16x32_bf16 a[36:39], v[160:163], v[132:135], 0\n"
    "ds_read_b128 v[188:191], v201 offset:35840\n"
    "v_cvt_f32_i32 v241, v241\n"
    "v_mfma_f32_16x16x32_bf16 a[36:39], v[164:167], v[148:151], a[36:39]\n"
    "v_exp_f32 v35, v35\n"
    "v_mfma_f32_16x16x32_bf16 v[112:115], v[192:195], v[128:131], 0\n"
    "s_waitcnt lgkmcnt(12)\n"
    "v_cvt_f32_i32 v242, v242\n"
    "v_cvt_f32_i32 v243, v243\n"
    "v_mfma_f32_16x16x32_bf16 v[112:115], v[192:195], v[144:147], v[112:115]\n"
    "s_waitcnt lgkmcnt(7)\n"
    "s_add_u32 s78, s88, 0x4800\n"
    "v_cvt_f32_i32 v244, v244\n"
    "v_mfma_f32_16x16x32_bf16 a[48:51], v[168:171], v[128:131], 0\n"
    "s_add_u32 m0, s71, 0x7000\n"
    "ds_read_b128 v[160:163], v201 offset:36864\n"
    "v_cvt_f32_i32 v245, v245\n"
    "v_mfma_f32_16x16x32_bf16 a[48:51], v[172:175], v[144:147], a[48:51]\n"
    "buffer_load_dwordx4 v156, s[28:31], s78 offen nt lds\n"
    "v_mfma_f32_16x16x32_bf16 a[52:55], v[168:171], v[132:135], 0\n"
    "ds_read_b128 v[164:167], v201 offset:37888\n"
    "v_cvt_f32_i32 v246, v246\n"
    "v_mfma_f32_16x16x32_bf16 a[52:55], v[172:175], v[148:151], a[52:55]\n"
    "v_exp_f32 v36, v36\n"
    "v_mfma_f32_16x16x32_bf16 v[116:119], v[192:195], v[132:135], 0\n"
    "v_exp_f32 v37, v37\n"
    "v_mfma_f32_16x16x32_bf16 v[116:119], v[192:195], v[148:151], v[116:119]\n"
    "s_waitcnt lgkmcnt(5)\n"
    "v_exp_f32 v38, v38\n"
    "v_mfma_f32_16x16x32_bf16 a[64:67], v[176:179], v[128:131], 0\n"
    "ds_read_b128 v[168:171], v201 offset:38912\n"
    "v_mul_f32 v226, s75, v223\n"
    "v_mfma_f32_16x16x32_bf16 a[64:67], v[180:183], v[144:147], a[64:67]\n"
    "v_cvt_f32_i32 v247, v247\n"
    "v_fma_f32 v227, -v222, v226, v217\n"
    "v_mfma_f32_16x16x32_bf16 a[68:71], v[176:179], v[132:135], 0\n"
    "ds_read_b128 v[172:175], v201 offset:39936\n"
    "v_mul_f32_dpp v240, v228, v240 row_newbcast:0 row_mask:0xf bank_mask:0xf\n"
    "v_mfma_f32_16x16x32_bf16 a[68:71], v[180:183], v[148:151], a[68:71]\n"
    "s_waitcnt lgkmcnt(4)\n"
    "v_exp_f32 v39, v39\n"
    "v_mfma_f32_16x16x32_bf16 a[80:83], v[184:187], v[128:131], 0\n"
    "ds_read_b128 v[176:179], v201 offset:40960\n"
    "v_cvt_f32_i32 v232, v232\n"
    "v_mfma_f32_16x16x32_bf16 a[80:83], v[188:191], v[144:147], a[80:83]\n"
    "s_add_u32 s78, s88, 0x4800\n"
    "v_mul_f32_dpp v241, v228, v241 row_newbcast:1 row_mask:0xf bank_mask:0xf\n"
    "v_cvt_f32_i32 v233, v233\n"
    "v_mfma_f32_16x16x32_bf16 a[84:87], v[184:187], v[132:135], 0\n"
    "s_add_u32 m0, s71, 0x7400\n"
    "ds_read_b128 v[180:183], v201 offset:41984\n"
    "v_mul_f32_dpp v242, v228, v242 row_newbcast:2 row_mask:0xf bank_mask:0xf\n"
    "v_mfma_f32_16x16x32_bf16 a[84:87], v[188:191], v[148:151], a[84:87]\n"
    "s_waitcnt lgkmcnt(4)\n"
    "buffer_load_dwordx4 v157, s[28:31], s78 offen nt lds\n"
    "v_mfma_f32_16x16x32_bf16 a[96:99], v[160:163], v[128:131], 0\n"
    "ds_read_b128 v[184:187], v201 offset:43008\n"
    "v_cvt_f32_i32 v234, v234\n"
    "v_mfma_f32_16x16x32_bf16 a[96:99], v[164:167], v[144:147], a[96:99]\n"
    "v_exp_f32 v48, v48\n"
    "v_mfma_f32_16x16x32_bf16 a[100:103], v[160:163], v[132:135], 0\n"
    "ds_read_b128 v[188:191], v201 offset:44032\n"
    "v_mul_f32_dpp v243, v228, v243 row_newbcast:3 row_mask:0xf bank_mask:0xf\n"
    "v_mfma_f32_16x16x32_bf16 a[100:103], v[164:167], v[148:151], a[100:103]\n"
    "s_waitcnt lgkmcnt(4)\n"
    "v_cvt_f32_i32 v235, v235\n"
    "v_mul_f32_dpp v244, v228, v244 row_newbcast:4 row_mask:0xf bank_mask:0xf\n"
    "v_mfma_f32_16x16x32_bf16 a[112:115], v[168:171], v[128:131], 0\n"
    "ds_read_b128 v[160:163], v201 offset:45056\n"
    "v_cvt_f32_i32 v236, v236\n"
    "v_mfma_f32_16x16x32_bf16 a[112:115], v[172:175], v[144:147], a[112:115]\n"
    "v_exp_f32 v49, v49\n"
    "v_mfma_f32_16x16x32_bf16 a[116:119], v[168:171], v[132:135], 0\n"
    "ds_read_b128 v[164:167], v201 offset:46080\n"
    "v_mul_f32_dpp v245, v228, v245 row_newbcast:5 row_mask:0xf bank_mask:0xf\n"
    "v_mfma_f32_16x16x32_bf16 a[116:119], v[172:175], v[148:151], a[116:119]\n"
    "s_waitcnt lgkmcnt(4)\n"
    "v_cvt_f32_i32 v237, v237\n"
    "v_mul_f32_dpp v246, v228, v246 row_newbcast:6 row_mask:0xf bank_mask:0xf\n"
    "v_mfma_f32_16x16x32_bf16 a[128:131], v[176:179], v[128:131], 0\n"
    "ds_read_b128 v[168:171], v201 offset:47104\n"
    "v_cvt_f32_i32 v238, v238\n"
    "v_mfma_f32_16x16x32_bf16 a[128:131], v[180:183], v[144:147], a[128:131]\n"
    "v_exp_f32 v50, v50\n"
    "v_mfma_f32_16x16x32_bf16 a[132:135], v[176:179], v[132:135], 0\n"
    "ds_read_b128 v[172:175], v201 offset:48128\n"
    "v_mul_f32_dpp v247, v228, v247 row_newbcast:7 row_mask:0xf bank_mask:0xf\n"
    "v_mfma_f32_16x16x32_bf16 a[132:135], v[180:183], v[148:151], a[132:135]\n"
    "s_waitcnt lgkmcnt(4)\n"
    "v_cvt_f32_i32 v239, v239\n"
    "v_add_f32_dpp v240, v229, v240 row_newbcast:0 row_mask:0xf bank_mask:0xf\n"
    "v_mfma_f32_16x16x32_bf16 a[144:147], v[184:187], v[128:131], 0\n"
    "v_exp_f32 v51, v51\n"
    "v_mfma_f32_16x16x32_bf16 a[144:147], v[188:191], v[144:147], a[144:147]\n"
    "v_mul_f32_dpp v232, v226, v232 row_newbcast:0 row_mask:0xf bank_mask:0xf\n"
    "v_add_f32_dpp v241, v229, v241 row_newbcast:1 row_mask:0xf bank_mask:0xf\n"
    "v_mfma_f32_16x16x32_bf16 a[148:151], v[184:187], v[132:135], 0\n"
    "v_mul_f32_dpp v233, v226, v233 row_newbcast:1 row_mask:0xf bank_mask:0xf\n"
    "v_add_f32_dpp v242, v229, v242 row_newbcast:2 row_mask:0xf bank_mask:0xf\n"
    "v_mfma_f32_16x16x32_bf16 a[148:151], v[188:191], v[148:151], a[148:151]\n"
    "s_waitcnt lgkmcnt(2)\n"
    "v_exp_f32 v52, v52\n"
    "v_mfma_f32_16x16x32_bf16 a[160:163], v[160:163], v[128:131], 0\n"
    "v_mul_f32_dpp v234, v226, v234 row_newbcast:2 row_mask:0xf bank_mask:0xf\n"
    "v_add_f32_dpp v243, v229, v243 row_newbcast:3 row_mask:0xf bank_mask:0xf\n"
    "v_mfma_f32_16x16x32_bf16 a[160:163], v[164:167], v[144:147], a[160:163]\n"
    "v_mul_f32_dpp v235, v226, v235 row_newbcast:3 row_mask:0xf bank_mask:0xf\n"
    "v_add_f32_dpp v244, v229, v244 row_newbcast:4 row_mask:0xf bank_mask:0xf\n"
    "v_mfma_f32_16x16x32_bf16 a[164:167], v[160:163], v[132:135], 0\n"
    "v_exp_f32 v53, v53\n"
    "v_mfma_f32_16x16x32_bf16 a[164:167], v[164:167], v[148:151], a[164:167]\n"
    "s_waitcnt lgkmcnt(0)\n"
    "v_mul_f32_dpp v236, v226, v236 row_newbcast:4 row_mask:0xf bank_mask:0xf\n"
    "v_add_f32_dpp v245, v229, v245 row_newbcast:5 row_mask:0xf bank_mask:0xf\n"
    "v_mfma_f32_16x16x32_bf16 a[176:179], v[168:171], v[128:131], 0\n"
    "v_exp_f32 v54, v54\n"
    "v_mfma_f32_16x16x32_bf16 a[176:179], v[172:175], v[144:147], a[176:179]\n"
    "v_mul_f32_dpp v237, v226, v237 row_newbcast:5 row_mask:0xf bank_mask:0xf\n"
    "v_add_f32_dpp v246, v229, v246 row_newbcast:6 row_mask:0xf bank_mask:0xf\n"
    "v_mfma_f32_16x16x32_bf16 a[180:183], v[168:171], v[132:135], 0\n"
    "v_mul_f32_dpp v238, v226, v238 row_newbcast:6 row_mask:0xf bank_mask:0xf\n"
    "v_add_f32_dpp v247, v229, v247 row_newbcast:7 row_mask:0xf bank_mask:0xf\n"
    "v_exp_f32 v55, v55\n"
    "v_mul_f32_dpp v239, v226, v239 row_newbcast:7 row_mask:0xf bank_mask:0xf\n"
    "v_cvt_pk_bf16_f32 v240, v240, v241\n"
    "v_add_f32_dpp v232, v227, v232 row_newbcast:0 row_mask:0xf bank_mask:0xf\n"
    "v_cvt_pk_bf16_f32 v241, v242, v243\n"
    "v_add_f32_dpp v233, v227, v233 row_newbcast:1 row_mask:0xf bank_mask:0xf\n"
    "v_cvt_pk_bf16_f32 v242, v244, v245\n"
    "v_add_f32_dpp v234, v227, v234 row_newbcast:2 row_mask:0xf bank_mask:0xf\n"
    "v_cvt_pk_bf16_f32 v243, v246, v247\n"
    "v_add_f32_dpp v235, v227, v235 row_newbcast:3 row_mask:0xf bank_mask:0xf\n"
    "v_add_f32_dpp v236, v227, v236 row_newbcast:4 row_mask:0xf bank_mask:0xf\n"
    "v_add_f32_dpp v237, v227, v237 row_newbcast:5 row_mask:0xf bank_mask:0xf\n"
    "v_add_f32_dpp v238, v227, v238 row_newbcast:6 row_mask:0xf bank_mask:0xf\n"
    "v_add_f32_dpp v239, v227, v239 row_newbcast:7 row_mask:0xf bank_mask:0xf\n"
    "v_cvt_pk_bf16_f32 v232, v232, v233\n"
    "v_cvt_pk_bf16_f32 v233, v234, v235\n"
    "v_cvt_pk_bf16_f32 v234, v236, v237\n"
    "v_cvt_pk_bf16_f32 v235, v238, v239\n"
    "v_mfma_f32_16x16x32_bf16 a[180:183], v[172:175], v[148:151], a[180:183]\n"
    "v_add_f32 v113, v113, v32\n"
    "v_add_f32 v114, v114, v33\n"
    "v_add_f32 v113, v113, v34\n"
    "v_add_f32 v114, v114, v35\n"
    "v_add_f32 v117, v117, v36\n"
    "v_add_f32 v118, v118, v37\n"
    "v_add_f32 v117, v117, v38\n"
    "v_add_f32 v118, v118, v39\n"
    "v_add_f32 v113, v113, v48\n"
    "v_add_f32 v114, v114, v49\n"
    "v_add_f32 v113, v113, v50\n"
    "v_add_f32 v114, v114, v51\n"
    "v_add_f32 v117, v117, v52\n"
    "buffer_store_dwordx4 v[232:235], v211, s[36:39], s62 offen sc1\n"
    "buffer_store_dwordx4 v[240:243], v212, s[40:43], s63 offen sc1\n"
    "v_add_f32 v118, v118, v53\n"
    "v_add_f32 v117, v117, v54\n"
    "v_add_f32 v118, v118, v55\n"
    "s_waitcnt vmcnt(10)\n"
    "ds_read_b128 a[240:243], v158 offset:49152\n"
    "ds_read_b128 a[244:247], v159 offset:49152\n"
    "ds_read_b128 a[248:251], v158 offset:49280\n"
    "ds_read_b128 a[252:255], v159 offset:49280\n"
    "ds_read_b128 v[0:3], v158 offset:49408\n"
    "ds_read_b128 v[4:7], v159 offset:49408\n"
    "s_waitcnt vmcnt(8)\n"
    "v_cmp_ne_u32 s[80:81], s67, v219\n"
    "v_cmp_gt_u32 vcc, s66, v218\n"
    "s_or_b64 vcc, vcc, s[80:81]\n"
    "s_cbranch_vccnz LSPRE1\n"
    "LSPB1:\n"
    "v_mov_b32 v220, 4\n"
    "s_add_u32 s66, s66, s74\n"
    "s_waitcnt lgkmcnt(0)\n"
    "s_barrier\n"
    "buffer_store_dwordx2 v[220:221], v214, s[44:47], 0 offen sc1\n"
    "ds_read_b128 v[160:163], v255\n"
    "ds_read_b128 v[164:167], v255 offset:1024\n"
    "ds_read_b128 v[168:171], v255 offset:2048\n"
    "ds_read_b128 v[172:175], v255 offset:3072\n"
    "ds_read_b128 v[176:179], v255 offset:4096\n"
    "ds_read_b128 v[180:183], v255 offset:5120\n"
    "s_waitcnt lgkmcnt(4)\n"
    "v_mfma_f32_16x16x32_bf16 v[44:47], v[160:163], a[240:243], 0\n"
    "ds_read_b128 v[184:187], v255 offset:6144\n"
    "v_mfma_f32_16x16x32_bf16 v[44:47], v[164:167], a[244:247], v[44:47]\n"
    "s_waitcnt lgkmcnt(3)\n"
    "ds_read_b128 v[188:191], v255 offset:7168\n"
    "v_mfma_f32_16x16x32_bf16 v[44:47], v[168:171], a[248:251], v[44:47]\n"
    "s_add_u32 m0, s71, 0xc000\n"
    "ds_read_b128 v[160:163], v255 offset:8192\n"
    "v_mfma_f32_16x16x32_bf16 v[44:47], v[172:175], a[252:255], v[44:47]\n"
    "s_waitcnt lgkmcnt(3)\n"
    "ds_read_b128 v[164:167], v255 offset:9216\n"
    "buffer_load_dwordx4 v200, s[40:43], s61 offen sc1 lds\n"
    "v_mfma_f32_16x16x32_bf16 v[44:47], v[176:179], v[0:3], v[44:47]\n"
    "ds_read_b128 v[168:171], v255 offset:10240\n"
    "v_mfma_f32_16x16x32_bf16 v[44:47], v[180:183], v[4:7], v[44:47]\n"
    "s_waitcnt lgkmcnt(3)\n"
    "ds_read_b128 v[172:175], v255 offset:11264\n"
    "v_mfma_f32_16x16x32_bf16 v[60:63], v[184:187], a[240:243], 0\n"
    "ds_read_b128 v[176:179], v200\n"
    "v_mfma_f32_16x16x32_bf16 v[60:63], v[188:191], a[244:247], v[60:63]\n"
    "s_waitcnt lgkmcnt(3)\n"
    "ds_read_b128 v[180:183], v200 offset:1024\n"
    "v_mfma_f32_16x16x32_bf16 v[60:63], v[160:163], a[248:251], v[60:63]\n"
    "ds_read_b128 v[184:187], v200 offset:2048\n"
    "buffer_load_dwordx4 v200, s[40:43], s61 offen offset:1024 sc1 lds\n"
    "v_mfma_f32_16x16x32_bf16 v[60:63], v[164:167], a[252:255], v[60:63]\n"
    "s_waitcnt lgkmcnt(3)\n"
    "ds_read_b128 v[188:191], v200 offset:3072\n"
    "v_mfma_f32_16x16x32_bf16 v[60:63], v[168:171], v[0:3], v[60:63]\n"
    "ds_read_b128 v[160:163], v200 offset:4096\n"
    "v_mfma_f32_16x16x32_bf16 v[60:63], v[172:175], v[4:7], v[60:63]\n"
    "s_waitcnt lgkmcnt(3)\n"
    "ds_read_b128 v[164:167], v200 offset:5120\n"
    "v_mfma_f32_16x16x32_bf16 v[72:75], v[176:179], a[240:243], 0\n"
    "ds_read_b128 v[168:171], v200 offset:6144\n"
    "v_mfma_f32_16x16x32_bf16 v[72:75], v[180:183], a[244:247], v[72:75]\n"
    "s_waitcnt lgkmcnt(3)\n"
    "ds_read_b128 v[172:175], v200 offset:7168\n"
    "buffer_load_dwordx4 v200, s[40:43], s61 offen offset:2048 sc1 lds\n"
    "v_mfma_f32_16x16x32_bf16 v[72:75], v[184:187], a[248:251], v[72:75]\n"
    "ds_read_b128 v[176:179], v200 offset:8192\n"
    "v_mfma_f32_16x16x32_bf16 v[72:75], v[188:191], a[252:255], v[72:75]\n"
    "s_waitcnt lgkmcnt(3)\n"
    "ds_read_b128 v[180:183], v200 offset:9216\n"
    "v_mfma_f32_16x16x32_bf16 v[72:75], v[160:163], v[0:3], v[72:75]\n"
    "ds_read_b128 v[184:187], v200 offset:10240\n"
    "v_mfma_f32_16x16x32_bf16 v[72:75], v[164:167], v[4:7], v[72:75]\n"
    "s_waitcnt lgkmcnt(3)\n"
    "ds_read_b128 v[188:191], v200 offset:11264\n"
    "v_max3_f32 v196, v44, v45, v46\n"
    "v_max3_f32 v196, v196, v47, v60\n"
    "v_max3_f32 v196, v196, v61, v62\n"
    "v_mfma_f32_16x16x32_bf16 v[88:91], v[168:171], a[240:243], 0\n"
    "ds_read_b128 v[160:163], v200 offset:12288\n"
    "buffer_load_dwordx4 v200, s[40:43], s61 offen offset:3072 sc1 lds\n"
    "v_max_f32 v196, v196, v63\n"
    "v_mfma_f32_16x16x32_bf16 v[88:91], v[172:175], a[244:247], v[88:91]\n"
    "s_waitcnt lgkmcnt(3)\n"
    "ds_read_b128 v[164:167], v200 offset:13312\n"
    "v_mov_b32 v197, v196\n"
    "s_nop 1\n"
    "v_permlane32_swap_b32 v196, v197\n"
    "v_max_f32 v196, v196, v197\n"
    "v_mfma_f32_16x16x32_bf16 v[88:91], v[176:179], a[248:251], v[88:91]\n"
    "ds_read_b128 v[168:171], v200 offset:14336\n"
    "v_mov_b32 v197, v196\n"
    "s_nop 1\n"
    "v_permlane16_swap_b32 v196, v197\n"
    "v_max_f32 v196, v196, v197\n"
    "v_mfma_f32_16x16x32_bf16 v[88:91], v[180:183], a[252:255], v[88:91]\n"
    "s_waitcnt lgkmcnt(3)\n"
    "s_add_u32 s65, s61, 0x1000\n"
    "ds_read_b128 v[172:175], v200 offset:15360\n"
    "v_add_f32 v196, 0x42640000, v196\n"
    "v_sub_f32 v104, 0, v196\n"
    "v_mfma_f32_16x16x32_bf16 v[88:91], v[184:187], v[0:3], v[88:91]\n"
    "s_add_u32 m0, s71, 0xd000\n"
    "ds_read_b128 v[176:179], v200 offset:16384\n"
    "v_sub_f32 v105, 0, v196\n"
    "v_sub_f32 v106, 0, v196\n"
    "v_sub_f32 v107, 0, v196\n"
    "v_mfma_f32_16x16x32_bf16 v[88:91], v[188:191], v[4:7], v[88:91]\n"
    "s_waitcnt lgkmcnt(3)\n"
    "ds_read_b128 v[180:183], v200 offset:17408\n"
    "buffer_load_dwordx4 v200, s[40:43], s65 offen sc1 lds\n"
    "v_add_f32 v44, v44, v104\n"
    "v_mfma_f32_16x16x32_bf16 v[40:43], v[160:163], a[240:243], v[104:107]\n"
    "ds_read_b128 v[184:187], v200 offset:18432\n"
    "v_add_f32 v45, v45, v104\n"
    "v_add_f32 v46, v46, v104\n"
    "v_add_f32 v47, v47, v104\n"
    "v_mfma_f32_16x16x32_bf16 v[40:43], v[164:167], a[244:247], v[40:43]\n"
    "s_waitcnt lgkmcnt(3)\n"
    "ds_read_b128 v[188:191], v200 offset:19456\n"
    "v_add_f32 v60, v60, v104\n"
    "v_add_f32 v61, v61, v104\n"
    "v_add_f32 v62, v62, v104\n"
    "v_mfma_f32_16x16x32_bf16 v[40:43], v[168:171], a[248:251], v[40:43]\n"
    "ds_read_b128 v[160:163], v200 offset:20480\n"
    "v_add_f32 v63, v63, v104\n"
    "v_exp_f32 v44, v44\n"
    "v_mfma_f32_16x16x32_bf16 v[40:43], v[172:175], a[252:255], v[40:43]\n"
    "s_waitcnt lgkmcnt(3)\n"
    "ds_read_b128 v[164:167], v200 offset:21504\n"
    "v_exp_f32 v45, v45\n"
    "v_mfma_f32_16x16x32_bf16 v[40:43], v[176:179], v[0:3], v[40:43]\n"
    "ds_read_b128 v[168:171], v200 offset:22528\n"
    "buffer_load_dwordx4 v200, s[40:43], s65 offen offset:1024 sc1 lds\n"
    "v_mfma_f32_16x16x32_bf16 v[40:43], v[180:183], v[4:7], v[40:43]\n"
    "s_waitcnt lgkmcnt(3)\n"
    "ds_read_b128 v[172:175], v200 offset:23552\n"
    "v_exp_f32 v46, v46\n"
    "v_mfma_f32_16x16x32_bf16 v[56:59], v[184:187], a[240:243], v[104:107]\n"
    "ds_read_b128 v[176:179], v201 offset:24576\n"
    "v_exp_f32 v47, v47\n"
    "v_mfma_f32_16x16x32_bf16 v[56:59], v[188:191], a[244:247], v[56:59]\n"
    "s_waitcnt lgkmcnt(3)\n"
    "s_add_u32 s78, s64, 0x30000\n"
    "ds_read_b128 v[180:183], v201 offset:25600\n"
    "v_exp_f32 v60, v60\n"
    "v_mfma_f32_16x16x32_bf16 v[56:59], v[160:163], a[248:251], v[56:59]\n"
    "s_add_u32 m0, s72, 0x1b000\n"
    "ds_read_b128 v[184:187], v201 offset:26624\n"
    "v_exp_f32 v61, v61\n"
    "v_mfma_f32_16x16x32_bf16 v[56:59], v[164:167], a[252:255], v[56:59]\n"
    "s_waitcnt lgkmcnt(3)\n"
    "ds_read_b128 v[188:191], v201 offset:27648\n"
    "buffer_load_dwordx4 v202, s[32:35], s78 offen nt lds\n"
    "v_mfma_f32_16x16x32_bf16 v[56:59], v[168:171], v[0:3], v[56:59]\n"
    "ds_read_b128 v[160:163], v201 offset:28672\n"
    "v_exp_f32 v62, v62\n"
    "v_exp_f32 v63, v63\n"
    "v_cvt_pk_bf16_f32 v136, v44, v45\n"
    "v_cvt_pk_bf16_f32 v137, v46, v47\n"
    "v_cvt_pk_bf16_f32 v138, v60, v61\n"
    "v_cvt_pk_bf16_f32 v139, v62, v63\n"
    "v_add_f32 v72, v72, v104\n"
    "v_add_f32 v73, v73, v104\n"
    "v_add_f32 v74, v74, v104\n"
    "v_add_f32 v75, v75, v104\n"
    "v_add_f32 v88, v88, v104\n"
    "v_add_f32 v89, v89, v104\n"
    "v_add_f32 v90, v90, v104\n"
    "v_add_f32 v91, v91, v104\n"
    "v_exp_f32 v72, v72\n"
    "v_exp_f32 v73, v73\n"
    "v_exp_f32 v74, v74\n"
    "v_exp_f32 v75, v75\n"
    "v_exp_f32 v88, v88\n"
    "v_exp_f32 v89, v89\n"
    "v_exp_f32 v90, v90\n"
    "v_exp_f32 v91, v91\n"
    "v_cvt_pk_bf16_f32 v152, v72, v73\n"
    "v_cvt_pk_bf16_f32 v153, v74, v75\n"
    "v_cvt_pk_bf16_f32 v154, v88, v89\n"
    "v_cvt_pk_bf16_f32 v155, v90, v91\n"
    "v_mfma_f32_16x16x32_bf16 v[56:59], v[172:175], v[4:7], v[56:59]\n"
    "s_waitcnt lgkmcnt(3)\n"
    "ds_read_b128 v[164:167], v201 offset:29696\n"
    "v_exp_f32 v40, v40\n"
    "v_mfma_f32_16x16x32_bf16 a[8:11], v[176:179], v[136:139], 0\n"
    "ds_read_b128 v[168:171], v201 offset:30720\n"
    "v_mfma_f32_16x16x32_bf16 a[8:11], v[180:183], v[152:155], a[8:11]\n"
    "s_waitcnt lgkmcnt(3)\n"
    "ds_read_b128 v[172:175], v201 offset:31744\n"
    "v_mfma_f32_16x16x32_bf16 a[24:27], v[184:187], v[136:139], 0\n"
    "ds_read_b128 v[176:179], v201 offset:32768\n"
    "buffer_load_dwordx4 v203, s[32:35], s78 offen offset:1024 nt lds\n"
    "v_mfma_f32_16x16x32_bf16 a[24:27], v[188:191], v[152:155], a[24:27]\n"
    "s_waitcnt lgkmcnt(3)\n"
    "ds_read_b128 v[180:183], v201 offset:33792\n"
    "v_mfma_f32_16x16x32_bf16 a[40:43], v[160:163], v[136:139], 0\n"
    "ds_read_b128 v[184:187], v201 offset:34816\n"
    "v_mfma_f32_16x16x32_bf16 a[40:43], v[164:167], v[152:155], a[40:43]\n"
    "ds_read_b128 v[188:191], v201 offset:35840\n"
    "v_mfma_f32_16x16x32_bf16 v[120:123], v[192:195], v[136:139], 0\n"
    "v_exp_f32 v41, v41\n"
    "v_mfma_f32_16x16x32_bf16 v[120:123], v[192:195], v[152:155], v[120:123]\n"
    "s_waitcnt lgkmcnt(4)\n"
    "buffer_load_dwordx4 v204, s[32:35], s78 offen offset:2048 nt lds\n"
    "v_mfma_f32_16x16x32_bf16 a[56:59], v[168:171], v[136:139], 0\n"
    "ds_read_b128 v[160:163], v201 offset:36864\n"
    "v_mfma_f32_16x16x32_bf16 a[56:59], v[172:175], v[152:155], a[56:59]\n"
    "s_waitcnt lgkmcnt(3)\n"
    "ds_read_b128 v[164:167], v201 offset:37888\n"
    "v_mfma_f32_16x16x32_bf16 a[72:75], v[176:179], v[136:139], 0\n"
    "ds_read_b128 v[168:171], v201 offset:38912\n"
    "v_mfma_f32_16x16x32_bf16 a[72:75], v[180:183], v[152:155], a[72:75]\n"
    "s_waitcnt lgkmcnt(3)\n"
    "ds_read_b128 v[172:175], v201 offset:39936\n"
    "v_mfma_f32_16x16x32_bf16 a[88:91], v[184:187], v[136:139], 0\n"
    "ds_read_b128 v[176:179], v201 offset:40960\n"
    "v_mfma_f32_16x16x32_bf16 a[88:91], v[188:191], v[152:155], a[88:91]\n"
    "s_waitcnt lgkmcnt(3)\n"
    "ds_read_b128 v[180:183], v201 offset:41984\n"
    "v_mfma_f32_16x16x32_bf16 a[104:107], v[160:163], v[136:139], 0\n"
    "ds_read_b128 v[184:187], v201 offset:43008\n"
    "v_mfma_f32_16x16x32_bf16 a[104:107], v[164:167], v[152:155], a[104:107]\n"
    "s_waitcnt lgkmcnt(3)\n"
    "ds_read_b128 v[188:191], v201 offset:44032\n"
    "v_mfma_f32_16x16x32_bf16 a[120:123], v[168:171], v[136:139], 0\n"
    "ds_read_b128 v[160:163], v201 offset:45056\n"
    "v_mfma_f32_16x16x32_bf16 a[120:123], v[172:175], v[152:155], a[120:123]\n"
    "s_waitcnt lgkmcnt(3)\n"
    "ds_read_b128 v[164:167], v201 offset:46080\n"
    "v_mfma_f32_16x16x32_bf16 a[136:139], v[176:179], v[136:139], 0\n"
    "ds_read_b128 v[168:171], v201 offset:47104\n"
    "v_mfma_f32_16x16x32_bf16 a[136:139], v[180:183], v[152:155], a[136:139]\n"
    "s_waitcnt lgkmcnt(3)\n"
    "ds_read_b128 v[172:175], v201 offset:48128\n"
    "v_mfma_f32_16x16x32_bf16 a[152:155], v[184:187], v[136:139], 0\n"
    "v_exp_f32 v42, v42\n"
    "v_mfma_f32_16x16x32_bf16 a[152:155], v[188:191], v[152:155], a[152:155]\n"
    "s_waitcnt lgkmcnt(2)\n"
    "v_exp_f32 v43, v43\n"
    "v_mfma_f32_16x16x32_bf16 a[168:171], v[160:163], v[136:139], 0\n"
    "v_exp_f32 v56, v56\n"
    "v_mfma_f32_16x16x32_bf16 a[168:171], v[164:167], v[152:155], a[168:171]\n"
    "s_waitcnt lgkmcnt(0)\n"
    "v_exp_f32 v57, v57\n"
    "v_mfma_f32_16x16x32_bf16 a[184:187], v[168:171], v[136:139], 0\n"
    "v_exp_f32 v58, v58\n"
    "v_exp_f32 v59, v59\n"
    "v_mfma_f32_16x16x32_bf16 a[184:187], v[172:175], v[152:155], a[184:187]\n"
    "v_add_f32 v121, v121, v40\n"
    "v_add_f32 v122, v122, v41\n"
    "v_add_f32 v121, v121, v42\n"
    "v_add_f32 v122, v122, v43\n"
    "v_add_f32 v121, v121, v56\n"
    "v_add_f32 v122, v122, v57\n"
    "s_waitcnt vmcnt(12)\n"
    "ds_read_b128 v[8:11], v158 offset:24576\n"
    "ds_read_b128 v[12:15], v159 offset:24576\n"
    "ds_read_b128 v[16:19], v158 offset:24704\n"
    "ds_read_b128 v[20:23], v159 offset:24704\n"
    "ds_read_b128 v[24:27], v158 offset:24832\n"
    "ds_read_b128 v[28:31], v159 offset:24832\n"
    "s_waitcnt lgkmcnt(0)\n"
    "ds_read_b128 v[160:163], v255\n"
    "ds_read_b128 v[164:167], v255 offset:1024\n"
    "ds_read_b128 v[168:171], v255 offset:2048\n"
    "ds_read_b128 v[172:175], v255 offset:3072\n"
    "ds_read_b128 v[176:179], v255 offset:4096\n"
    "ds_read_b128 v[180:183], v255 offset:5120\n"
    "s_waitcnt lgkmcnt(4)\n"
    "v_mfma_f32_16x16x32_bf16 v[64:67], v[160:163], v[8:11], 0\n"
    "ds_read_b128 v[184:187], v255 offset:6144\n"
    "v_mfma_f32_16x16x32_bf16 v[64:67], v[164:167], v[12:15], v[64:67]\n"
    "s_waitcnt lgkmcnt(3)\n"
    "s_add_u32 m0, s71, 0x6000\n"
    "ds_read_b128 v[188:191], v255 offset:7168\n"
    "v_mfma_f32_16x16x32_bf16 v[64:67], v[168:171], v[16:19], v[64:67]\n"
    "ds_read_b128 v[160:163], v255 offset:8192\n"
    "buffer_load_dwordx4 v200, s[36:39], s60 offen sc1 lds\n"
    "v_mfma_f32_16x16x32_bf16 v[64:67], v[172:175], v[20:23], v[64:67]\n"
    "s_waitcnt lgkmcnt(3)\n"
    "ds_read_b128 v[164:167], v255 offset:9216\n"
    "v_mfma_f32_16x16x32_bf16 v[64:67], v[176:179], v[24:27], v[64:67]\n"
    "ds_read_b128 v[168:171], v255 offset:10240\n"
    "v_mfma_f32_16x16x32_bf16 v[64:67], v[180:183], v[28:31], v[64:67]\n"
    "s_waitcnt lgkmcnt(3)\n"
    "ds_read_b128 v[172:175], v255 offset:11264\n"
    "buffer_load_dwordx4 v200, s[36:39], s60 offen offset:1024 sc1 lds\n"
    "v_mfma_f32_16x16x32_bf16 v[80:83], v[184:187], v[8:11], 0\n"
    "ds_read_b128 v[176:179], v200\n"
    "v_mfma_f32_16x16x32_bf16 v[80:83], v[188:191], v[12:15], v[80:83]\n"
    "s_waitcnt lgkmcnt(3)\n"
    "ds_read_b128 v[180:183], v200 offset:1024\n"
    "v_mfma_f32_16x16x32_bf16 v[80:83], v[160:163], v[16:19], v[80:83]\n"
    "ds_read_b128 v[184:187], v200 offset:2048\n"
    "buffer_load_dwordx4 v200, s[36:39], s60 offen offset:2048 sc1 lds\n"
    "v_mfma_f32_16x16x32_bf16 v[80:83], v[164:167], v[20:23], v[80:83]\n"
    "s_waitcnt lgkmcnt(3)\n"
    "ds_read_b128 v[188:191], v200 offset:3072\n"
    "v_mfma_f32_16x16x32_bf16 v[80:83], v[168:171], v[24:27], v[80:83]\n"
    "ds_read_b128 v[160:163], v200 offset:4096\n"
    "v_mfma_f32_16x16x32_bf16 v[80:83], v[172:175], v[28:31], v[80:83]\n"
    "s_waitcnt lgkmcnt(3)\n"
    "ds_read_b128 v[164:167], v200 offset:5120\n"
    "buffer_load_dwordx4 v200, s[36:39], s60 offen offset:3072 sc1 lds\n"
    "v_mfma_f32_16x16x32_bf16 v[76:79], v[176:179], v[8:11], 0\n"
    "s_add_u32 s65, s60, 0x1000\n"
    "ds_read_b128 v[168:171], v200 offset:6144\n"
    "v_mfma_f32_16x16x32_bf16 v[76:79], v[180:183], v[12:15], v[76:79]\n"
    "s_waitcnt lgkmcnt(3)\n"
    "s_add_u32 m0, s71, 0x7000\n"
    "ds_read_b128 v[172:175], v200 offset:7168\n"
    "v_mfma_f32_16x16x32_bf16 v[76:79], v[184:187], v[16:19], v[76:79]\n"
    "ds_read_b128 v[176:179], v200 offset:8192\n"
    "buffer_load_dwordx4 v200, s[36:39], s65 offen sc1 lds\n"
    "v_mfma_f32_16x16x32_bf16 v[76:79], v[188:191], v[20:23], v[76:79]\n"
    "s_waitcnt lgkmcnt(3)\n"
    "ds_read_b128 v[180:183], v200 offset:9216\n"
    "v_mfma_f32_16x16x32_bf16 v[76:79], v[160:163], v[24:27], v[76:79]\n"
    "ds_read_b128 v[184:187], v200 offset:10240\n"
    "v_mfma_f32_16x16x32_bf16 v[76:79], v[164:167], v[28:31], v[76:79]\n"
    "s_waitcnt lgkmcnt(3)\n"
    "ds_read_b128 v[188:191], v200 offset:11264\n"
    "buffer_load_dwordx4 v200, s[36:39], s65 offen offset:1024 sc1 lds\n"
    "v_max3_f32 v196, v64, v65, v66\n"
    "v_mfma_f32_16x16x32_bf16 v[92:95], v[168:171], v[8:11], 0\n"
    "ds_read_b128 v[160:163], v200 offset:12288\n"
    "v_max3_f32 v196, v196, v67, v80\n"
    "v_max3_f32 v196, v196, v81, v82\n"
    "v_max_f32 v196, v196, v83\n"
    "v_mfma_f32_16x16x32_bf16 v[92:95], v[172:175], v[12:15], v[92:95]\n"
    "s_waitcnt lgkmcnt(3)\n"
    "ds_read_b128 v[164:167], v200 offset:13312\n"
    "v_mov_b32 v197, v196\n"
    "s_nop 1\n"
    "v_permlane32_swap_b32 v196, v197\n"
    "v_max_f32 v196, v196, v197\n"
    "v_mfma_f32_16x16x32_bf16 v[92:95], v[176:179], v[16:19], v[92:95]\n"
    "ds_read_b128 v[168:171], v200 offset:14336\n"
    "v_mov_b32 v197, v196\n"
    "s_nop 1\n"
    "v_permlane16_swap_b32 v196, v197\n"
    "v_max_f32 v196, v196, v197\n"
    "v_mfma_f32_16x16x32_bf16 v[92:95], v[180:183], v[20:23], v[92:95]\n"
    "s_waitcnt lgkmcnt(3)\n"
    "ds_read_b128 v[172:175], v200 offset:15360\n"
    "v_add_f32 v196, 0x42640000, v196\n"
    "v_sub_f32 v108, 0, v196\n"
    "v_sub_f32 v109, 0, v196\n"
    "v_mfma_f32_16x16x32_bf16 v[92:95], v[184:187], v[24:27], v[92:95]\n"
    "ds_read_b128 v[176:179], v200 offset:16384\n"
    "v_sub_f32 v110, 0, v196\n"
    "v_sub_f32 v111, 0, v196\n"
    "v_add_f32 v64, v64, v108\n"
    "v_mfma_f32_16x16x32_bf16 v[92:95], v[188:191], v[28:31], v[92:95]\n"
    "s_waitcnt lgkmcnt(3)\n"
    "ds_read_b128 v[180:183], v200 offset:17408\n"
    "v_add_f32 v65, v65, v108\n"
    "v_add_f32 v66, v66, v108\n"
    "v_add_f32 v67, v67, v108\n"
    "v_mfma_f32_16x16x32_bf16 v[44:47], v[160:163], v[8:11], v[108:111]\n"
    "ds_read_b128 v[184:187], v200 offset:18432\n"
    "v_add_f32 v80, v80, v108\n"
    "v_add_f32 v81, v81, v108\n"
    "v_add_f32 v82, v82, v108\n"
    "v_mfma_f32_16x16x32_bf16 v[44:47], v[164:167], v[12:15], v[44:47]\n"
    "s_waitcnt lgkmcnt(3)\n"
    "ds_read_b128 v[188:191], v200 offset:19456\n"
    "v_add_f32 v83, v83, v108\n"
    "v_exp_f32 v64, v64\n"
    "v_mfma_f32_16x16x32_bf16 v[44:47], v[168:171], v[16:19], v[44:47]\n"
    "ds_read_b128 v[160:163], v200 offset:20480\n"
    "buffer_load_dwordx2 v[218:219], v213, s[44:47], 0 offen sc1\n"
    "v_mfma_f32_16x16x32_bf16 v[44:47], v[172:175], v[20:23], v[44:47]\n"
    "s_waitcnt lgkmcnt(3)\n"
    "ds_read_b128 v[164:167], v200 offset:21504\n"
    "v_exp_f32 v65, v65\n"
    "v_mfma_f32_16x16x32_bf16 v[44:47], v[176:179], v[24:27], v[44:47]\n"
    "ds_read_b128 v[168:171], v200 offset:22528\n"
    "v_exp_f32 v66, v66\n"
    "v_mfma_f32_16x16x32_bf16 v[44:47], v[180:183], v[28:31], v[44:47]\n"
    "s_waitcnt lgkmcnt(3)\n"
    "ds_read_b128 v[172:175], v200 offset:23552\n"
    "v_exp_f32 v67, v67\n"
    "v_mfma_f32_16x16x32_bf16 v[60:63], v[184:187], v[8:11], v[108:111]\n"
    "ds_read_b128 v[176:179], v201 offset:24576\n"
    "v_exp_f32 v80, v80\n"
    "v_mfma_f32_16x16x32_bf16 v[60:63], v[188:191], v[12:15], v[60:63]\n"
    "s_waitcnt lgkmcnt(3)\n"
    "ds_read_b128 v[180:183], v201 offset:25600\n"
    "v_exp_f32 v81, v81\n"
    "v_mfma_f32_16x16x32_bf16 v[60:63], v[160:163], v[16:19], v[60:63]\n"
    "ds_read_b128 v[184:187], v201 offset:26624\n"
    "v_exp_f32 v82, v82\n"
    "v_mfma_f32_16x16x32_bf16 v[60:63], v[164:167], v[20:23], v[60:63]\n"
    "s_waitcnt lgkmcnt(3)\n"
    "ds_read_b128 v[188:191], v201 offset:27648\n"
    "v_exp_f32 v83, v83\n"
    "v_cvt_pk_bf16_f32 v140, v64, v65\n"
    "v_mfma_f32_16x16x32_bf16 v[60:63], v[168:171], v[24:27], v[60:63]\n"
    "ds_read_b128 v[160:163], v201 offset:28672\n"
    "v_cvt_pk_bf16_f32 v141, v66, v67\n"
    "v_cvt_pk_bf16_f32 v142, v80, v81\n"
    "v_cvt_pk_bf16_f32 v143, v82, v83\n"
    "v_add_f32 v76, v76, v108\n"
    "v_add_f32 v77, v77, v108\n"
    "v_add_f32 v78, v78, v108\n"
    "v_add_f32 v79, v79, v108\n"
    "v_add_f32 v92, v92, v108\n"
    "v_add_f32 v93, v93, v108\n"
    "v_add_f32 v94, v94, v108\n"
    "v_add_f32 v95, v95, v108\n"
    "v_exp_f32 v76, v76\n"
    "v_exp_f32 v77, v77\n"
    "v_exp_f32 v78, v78\n"
    "v_exp_f32 v79, v79\n"
    "v_exp_f32 v92, v92\n"
    "v_exp_f32 v93, v93\n"
    "v_exp_f32 v94, v94\n"
    "v_exp_f32 v95, v95\n"
    "v_cvt_pk_bf16_f32 v156, v76, v77\n"
    "v_cvt_pk_bf16_f32 v157, v78, v79\n"
    "v_cvt_pk_bf16_f32 v158, v92, v93\n"
    "v_cvt_pk_bf16_f32 v159, v94, v95\n"
    "v_mfma_f32_16x16x32_bf16 v[60:63], v[172:175], v[28:31], v[60:63]\n"
    "s_waitcnt lgkmcnt(3)\n"
    "ds_read_b128 v[164:167], v201 offset:29696\n"
    "v_exp_f32 v44, v44\n"
    "v_mfma_f32_16x16x32_bf16 a[12:15], v[176:179], v[140:143], 0\n"
    "ds_read_b128 v[168:171], v201 offset:30720\n"
    "v_mfma_f32_16x16x32_bf16 a[12:15], v[180:183], v[156:159], a[12:15]\n"
    "s_waitcnt lgkmcnt(3)\n"
    "ds_read_b128 v[172:175], v201 offset:31744\n"
    "v_mfma_f32_16x16x32_bf16 a[28:31], v[184:187], v[140:143], 0\n"
    "ds_read_b128 v[176:179], v201 offset:32768\n"
    "v_mfma_f32_16x16x32_bf16 a[28:31], v[188:191], v[156:159], a[28:31]\n"
    "s_waitcnt lgkmcnt(3)\n"
    "ds_read_b128 v[180:183], v201 offset:33792\n"
    "v_mfma_f32_16x16x32_bf16 a[44:47], v[160:163], v[140:143], 0\n"
    "ds_read_b128 v[184:187], v201 offset:34816\n"
    "v_mfma_f32_16x16x32_bf16 a[44:47], v[164:167], v[156:159], a[44:47]\n"
    "ds_read_b128 v[188:191], v201 offset:35840\n"
    "v_mfma_f32_16x16x32_bf16 v[124:127], v[192:195], v[140:143], 0\n"
    "v_exp_f32 v45, v45\n"
    "v_mfma_f32_16x16x32_bf16 v[124:127], v[192:195], v[156:159], v[124:127]\n"
    "s_waitcnt lgkmcnt(4)\n"
    "v_exp_f32 v46, v46\n"
    "v_mfma_f32_16x16x32_bf16 a[60:63], v[168:171], v[140:143], 0\n"
    "ds_read_b128 v[160:163], v201 offset:36864\n"
    "v_mfma_f32_16x16x32_bf16 a[60:63], v[172:175], v[156:159], a[60:63]\n"
    "s_waitcnt lgkmcnt(3)\n"
    "ds_read_b128 v[164:167], v201 offset:37888\n"
    "v_mfma_f32_16x16x32_bf16 a[76:79], v[176:179], v[140:143], 0\n"
    "ds_read_b128 v[168:171], v201 offset:38912\n"
    "v_mfma_f32_16x16x32_bf16 a[76:79], v[180:183], v[156:159], a[76:79]\n"
    "s_waitcnt lgkmcnt(3)\n"
    "ds_read_b128 v[172:175], v201 offset:39936\n"
    "v_mfma_f32_16x16x32_bf16 a[92:95], v[184:187], v[140:143], 0\n"
    "ds_read_b128 v[176:179], v201 offset:40960\n"
    "v_mfma_f32_16x16x32_bf16 a[92:95], v[188:191], v[156:159], a[92:95]\n"
    "s_waitcnt lgkmcnt(3)\n"
    "ds_read_b128 v[180:183], v201 offset:41984\n"
    "v_mfma_f32_16x16x32_bf16 a[108:111], v[160:163], v[140:143], 0\n"
    "ds_read_b128 v[184:187], v201 offset:43008\n"
    "v_mfma_f32_16x16x32_bf16 a[108:111], v[164:167], v[156:159], a[108:111]\n"
    "s_waitcnt lgkmcnt(3)\n"
    "ds_read_b128 v[188:191], v201 offset:44032\n"
    "v_mfma_f32_16x16x32_bf16 a[124:127], v[168:171], v[140:143], 0\n"
    "ds_read_b128 v[160:163], v201 offset:45056\n"
    "v_mfma_f32_16x16x32_bf16 a[124:127], v[172:175], v[156:159], a[124:127]\n"
    "s_waitcnt lgkmcnt(3)\n"
    "ds_read_b128 v[164:167], v201 offset:46080\n"
    "v_mfma_f32_16x16x32_bf16 a[140:143], v[176:179], v[140:143], 0\n"
    "ds_read_b128 v[168:171], v201 offset:47104\n"
    "v_mfma_f32_16x16x32_bf16 a[140:143], v[180:183], v[156:159], a[140:143]\n"
    "s_waitcnt lgkmcnt(3)\n"
    "ds_read_b128 v[172:175], v201 offset:48128\n"
    "v_mfma_f32_16x16x32_bf16 a[156:159], v[184:187], v[140:143], 0\n"
    "v_exp_f32 v47, v47\n"
    "v_mfma_f32_16x16x32_bf16 a[156:159], v[188:191], v[156:159], a[156:159]\n"
    "s_waitcnt lgkmcnt(2)\n"
    "v_exp_f32 v60, v60\n"
    "v_mfma_f32_16x16x32_bf16 a[172:175], v[160:163], v[140:143], 0\n"
    "v_exp_f32 v61, v61\n"
    "v_mfma_f32_16x16x32_bf16 a[172:175], v[164:167], v[156:159], a[172:175]\n"
    "s_waitcnt lgkmcnt(0)\n"
    "v_exp_f32 v62, v62\n"
    "v_mfma_f32_16x16x32_bf16 a[188:191], v[168:171], v[140:143], 0\n"
    "v_exp_f32 v63, v63\n"
    "v_mfma_f32_16x16x32_bf16 a[188:191], v[172:175], v[156:159], a[188:191]\n"
    "v_add_f32 v125, v125, v44\n"
    "v_add_f32 v126, v126, v45\n"
    "v_add_f32 v125, v125, v46\n"
    "v_add_f32 v126, v126, v47\n"
    "s_add_u32 s64, s64, 0x3c000\n"
    "s_add_u32 s62, s62, 0x6000\n"
    "s_add_u32 s63, s63, 0x6000\n"
    "s_add_u32 s60, s60, 0x6000\n"
    "s_add_u32 s61, s61, 0x6000\n"
    "v_add_u32 v209, 0x300, v209\n"
    "v_add_u32 v210, 0xc0, v210\n"
    "v_mov_b32 v220, 5\n"
    "s_waitcnt vmcnt(0)\n"
    "v_cmp_ne_u32 s[80:81], s67, v219\n"
    "v_cmp_gt_u32 vcc, s66, v218\n"
    "s_or_b64 vcc, vcc, s[80:81]\n"
    "s_cbranch_vccnz LSPRE2\n"
    "LSPB2:\n"
    "s_barrier\n"
    "s_cmp_eq_u32 s58, 0\n"
    "s_cbranch_scc1 LENT0\n"
    "LENT1:\n"
    "s_mov_b32 s59, 1\n"
    "s_add_u32 m0, s72, 0x21000\n"
    "s_nop 0\n"
    "buffer_load_dwordx4 v250, s[48:51], 0 offen offset:2048 lds\n"
    "s_add_u32 m0, s72, 0x18000\n"
    "s_nop 0\n"
    "buffer_load_dwordx4 v202, s[32:35], s64 offen lds\n"
    "buffer_load_dwordx4 v203, s[32:35], s64 offen offset:1024 lds\n"
    "buffer_load_dwordx4 v204, s[32:35], s64 offen offset:2048 lds\n"
    "s_add_u32 s64, s64, 0xc000\n"
    "s_mov_b32 s94, 0x3000\n"
    "s_mov_b32 s95, 0x6000\n"
    "s_add_u32 s51, s72, 0x18000\n"
    "v_add_u32 v205, 0x3000, v205\n"
    "v_add_u32 v206, 0x3000, v206\n"
    "v_add_u32 v207, 0x3000, v207\n"
    "ds_read_u16_d16_hi v223, v209 offset:6336\n"
    "ds_read_u16_d16_hi v222, v209 offset:192\n"
    "ds_read_b128 v[160:163], v200 offset:24576\n"
    "v_add_f32 v121, v121, v58\n"
    "ds_read_b128 v[232:235], v205 offset:24576\n"
    "v_add_f32 v122, v122, v59\n"
    "ds_read_b128 v[164:167], v200 offset:25600\n"
    "ds_read_b128 v[236:239], v206 offset:24576\n"
    "v_add_f32 v125, v125, v60\n"
    "v_add_f32 v126, v126, v61\n"
    "v_add_f32 v125, v125, v62\n"
    "v_add_f32 v126, v126, v63\n"
    "ds_read_b64 v[228:229], v210 offset:64\n"
    "ds_read2st64_b32 v[240:241], v207 offset0:96 offset1:99\n"
    "ds_read_b128 v[168:171], v200 offset:26624\n"
    "ds_read2st64_b32 v[242:243], v207 offset0:102 offset1:105\n"
    "ds_read2st64_b32 v[244:245], v207 offset0:108 offset1:111\n"
    "ds_read2st64_b32 v[246:247], v207 offset0:114 offset1:117\n"
    "v_cvt_pk_bf16_f32 v140, v44, v45\n"
    "v_cvt_pk_bf16_f32 v141, v46, v47\n"
    "s_waitcnt lgkmcnt(6)\n"
    "v_mul_f32 v226, s75, v223\n"
    "ds_read_b128 v[172:175], v200 offset:27648\n"
    "v_fma_f32 v227, -v222, v226, v217\n"
    "v_cvt_f32_i32 v232, v232\n"
    "v_cvt_f32_i32 v233, v233\n"
    "v_cvt_f32_i32 v234, v234\n"
    "v_cvt_f32_i32 v235, v235\n"
    "s_nop 7\n"
    ".p2align 6\n"
    "LLOOP1:\n"
    "v_mfma_f32_16x16x32_bf16 v[64:67], v[160:163], a[192:195], v[96:99]\n"
    "ds_read_b128 v[176:179], v200 offset:28672\n"
    "v_cvt_f32_i32 v236, v236\n"
    "v_mfma_f32_16x16x32_bf16 v[64:67], v[164:167], a[196:199], v[64:67]\n"
    "v_cvt_f32_i32 v237, v237\n"
    "v_cvt_f32_i32 v238, v238\n"
    "v_mfma_f32_16x16x32_bf16 v[68:71], v[164:167], a[220:223], v[100:103]\n"
    "v_cvt_f32_i32 v239, v239\n"
    "v_mul_f32_dpp v232, v226, v232 row_newbcast:0 row_mask:0xf bank_mask:0xf\n"
    "v_mfma_f32_16x16x32_bf16 v[68:71], v[160:163], a[216:219], v[68:71]\n"
    "v_mul_f32_dpp v233, v226, v233 row_newbcast:1 row_mask:0xf bank_mask:0xf\n"
    "v_mul_f32_dpp v234, v226, v234 row_newbcast:2 row_mask:0xf bank_mask:0xf\n"
    "v_mfma_f32_16x16x32_bf16 v[72:75], v[160:163], a[240:243], v[104:107]\n"
    "ds_read_b128 v[180:183], v200 offset:29696\n"
    "v_mul_f32_dpp v235, v226, v235 row_newbcast:3 row_mask:0xf bank_mask:0xf\n"
    "v_mfma_f32_16x16x32_bf16 v[72:75], v[164:167], a[244:247], v[72:75]\n"
    "v_mul_f32_dpp v236, v226, v236 row_newbcast:4 row_mask:0xf bank_mask:0xf\n"
    "v_mul_f32_dpp v237, v226, v237 row_newbcast:5 row_mask:0xf bank_mask:0xf\n"
    "v_mfma_f32_16x16x32_bf16 v[76:79], v[164:167], v[12:15], v[108:111]\n"
    "v_mul_f32_dpp v238, v226, v238 row_newbcast:6 row_mask:0xf bank_mask:0xf\n"
    "v_mul_f32_dpp v239, v226, v239 row_newbcast:7 row_mask:0xf bank_mask:0xf\n"
    "v_mfma_f32_16x16x32_bf16 v[76:79], v[160:163], v[8:11], v[76:79]\n"
    "s_waitcnt lgkmcnt(2)\n"
    "v_add_f32_dpp v232, v227, v232 row_newbcast:0 row_mask:0xf bank_mask:0xf\n"
    "v_add_f32_dpp v233, v227, v233 row_newbcast:1 row_mask:0xf bank_mask:0xf\n"
    "v_mfma_f32_16x16x32_bf16 v[76:79], v[168:171], v[16:19], v[76:79]\n"
    "ds_read_b128 v[160:163], v200 offset:34816\n"
    "v_add_f32_dpp v234, v227, v234 row_newbcast:2 row_mask:0xf bank_mask:0xf\n"
    "v_mfma_f32_16x16x32_bf16 v[76:79], v[172:175], v[20:23], v[76:79]\n"
    "v_add_f32_dpp v235, v227, v235 row_newbcast:3 row_mask:0xf bank_mask:0xf\n"
    "v_add_f32_dpp v236, v227, v236 row_newbcast:4 row_mask:0xf bank_mask:0xf\n"
    "v_mfma_f32_16x16x32_bf16 v[72:75], v[172:175], a[252:255], v[72:75]\n"
    "v_add_f32_dpp v237, v227, v237 row_newbcast:5 row_mask:0xf bank_mask:0xf\n"
    "v_add_f32_dpp v238, v227, v238 row_newbcast:6 row_mask:0xf bank_mask:0xf\n"
    "v_mfma_f32_16x16x32_bf16 v[72:75], v[168:171], a[248:251], v[72:75]\n"
    "v_add_f32_dpp v239, v227, v239 row_newbcast:7 row_mask:0xf bank_mask:0xf\n"
    "v_cvt_pk_bf16_f32 v232, v232, v233\n"
    "v_mfma_f32_16x16x32_bf16 v[68:71], v[168:171], a[224:227], v[68:71]\n"
    "ds_read_b128 v[164:167], v200 offset:35840\n"
    "v_cvt_f32_i32 v240, v240\n"
    "v_mfma_f32_16x16x32_bf16 v[68:71], v[172:175], a[228:231], v[68:71]\n"
    "v_cvt_pk_bf16_f32 v233, v234, v235\n"
    "v_cvt_f32_i32 v241, v241\n"
    "v_mfma_f32_16x16x32_bf16 v[64:67], v[172:175], a[204:207], v[64:67]\n"
    "v_cvt_pk_bf16_f32 v234, v236, v237\n"
    "v_cvt_f32_i32 v242, v242\n"
    "v_mfma_f32_16x16x32_bf16 v[64:67], v[168:171], a[200:203], v[64:67]\n"
    "s_waitcnt lgkmcnt(2)\n"
    "v_cvt_pk_bf16_f32 v235, v238, v239\n"
    "v_cvt_f32_i32 v243, v243\n"
    "v_mfma_f32_16x16x32_bf16 v[64:67], v[176:179], a[208:211], v[64:67]\n"
    "ds_read_b128 v[168:171], v200 offset:32768\n"
    "v_cvt_f32_i32 v244, v244\n"
    "v_mfma_f32_16x16x32_bf16 v[64:67], v[180:183], a[212:215], v[64:67]\n"
    "v_cvt_f32_i32 v245, v245\n"
    "v_cvt_f32_i32 v246, v246\n"
    "v_mfma_f32_16x16x32_bf16 v[68:71], v[180:183], a[236:239], v[68:71]\n"
    "buffer_store_dwordx4 v[232:235], v211, s[36:39], s62 offen sc1\n"
    "v_cvt_f32_i32 v247, v247\n"
    "v_mul_f32_dpp v240, v228, v240 row_newbcast:0 row_mask:0xf bank_mask:0xf\n"
    "v_mul_f32_dpp v241, v228, v241 row_newbcast:1 row_mask:0xf bank_mask:0xf\n"
    "v_mfma_f32_16x16x32_bf16 v[68:71], v[176:179], a[232:235], v[68:71]\n"
    "v_mul_f32_dpp v242, v228, v242 row_newbcast:2 row_mask:0xf bank_mask:0xf\n"
    "v_mul_f32_dpp v243, v228, v243 row_newbcast:3 row_mask:0xf bank_mask:0xf\n"
    "v_mfma_f32_16x16x32_bf16 v[72:75], v[176:179], v[0:3], v[72:75]\n"
    "ds_read_b128 v[172:175], v200 offset:33792\n"
    "v_mul_f32_dpp v244, v228, v244 row_newbcast:4 row_mask:0xf bank_mask:0xf\n"
    "v_mfma_f32_16x16x32_bf16 v[72:75], v[180:183], v[4:7], v[72:75]\n"
    "v_mul_f32_dpp v245, v228, v245 row_newbcast:5 row_mask:0xf bank_mask:0xf\n"
    "v_mul_f32_dpp v246, v228, v246 row_newbcast:6 row_mask:0xf bank_mask:0xf\n"
    "v_mfma_f32_16x16x32_bf16 v[76:79], v[180:183], v[28:31], v[76:79]\n"
    "v_mul_f32_dpp v247, v228, v247 row_newbcast:7 row_mask:0xf bank_mask:0xf\n"
    "v_add_f32_dpp v240, v229, v240 row_newbcast:0 row_mask:0xf bank_mask:0xf\n"
    "v_mfma_f32_16x16x32_bf16 v[76:79], v[176:179], v[24:27], v[76:79]\n"
    "s_waitcnt lgkmcnt(2)\n"
    "v_add_f32_dpp v241, v229, v241 row_newbcast:1 row_mask:0xf bank_mask:0xf\n"
    "v_add_f32_dpp v242, v229, v242 row_newbcast:2 row_mask:0xf bank_mask:0xf\n"
    "v_mfma_f32_16x16x32_bf16 v[92:95], v[160:163], v[24:27], v[108:111]\n"
    "ds_read_b128 v[176:179], v200 offset:30720\n"
    "v_add_f32_dpp v243, v229, v243 row_newbcast:3 row_mask:0xf bank_mask:0xf\n"
    "v_mfma_f32_16x16x32_bf16 v[92:95], v[164:167], v[28:31], v[92:95]\n"
    "v_add_f32_dpp v244, v229, v244 row_newbcast:4 row_mask:0xf bank_mask:0xf\n"
    "v_add_f32_dpp v245, v229, v245 row_newbcast:5 row_mask:0xf bank_mask:0xf\n"
    "v_mfma_f32_16x16x32_bf16 v[88:91], v[164:167], v[4:7], v[104:107]\n"
    "v_add_f32_dpp v246, v229, v246 row_newbcast:6 row_mask:0xf bank_mask:0xf\n"
    "v_add_f32_dpp v247, v229, v247 row_newbcast:7 row_mask:0xf bank_mask:0xf\n"
    "v_mfma_f32_16x16x32_bf16 v[88:91], v[160:163], v[0:3], v[88:91]\n"
    "v_cvt_pk_bf16_f32 v240, v240, v241\n"
    "v_cvt_pk_bf16_f32 v241, v242, v243\n"
    "v_mfma_f32_16x16x32_bf16 v[84:87], v[160:163], a[232:235], v[100:103]\n"
    "ds_read_b128 v[180:183], v200 offset:31744\n"
    "v_cvt_pk_bf16_f32 v242, v244, v245\n"
    "v_mfma_f32_16x16x32_bf16 v[84:87], v[164:167], a[236:239], v[84:87]\n"
    "v_cvt_pk_bf16_f32 v243, v246, v247\n"
    "v_cvt_pk_bf16_f32 v136, v40, v41\n"
    "v_mfma_f32_16x16x32_bf16 v[80:83], v[164:167], a[212:215], v[96:99]\n"
    "buffer_store_dwordx4 v[240:243], v212, s[40:43], s63 offen sc1\n"
    "v_cvt_pk_bf16_f32 v137, v42, v43\n"
    "v_cvt_pk_bf16_f32 v132, v36, v37\n"
    "v_cvt_pk_bf16_f32 v133, v38, v39\n"
    "v_mfma_f32_16x16x32_bf16 v[80:83], v[160:163], a[208:211], v[80:83]\n"
    "s_waitcnt lgkmcnt(2)\n"
    "v_cvt_pk_bf16_f32 v128, v32, v33\n"
    "v_cvt_pk_bf16_f32 v129, v34, v35\n"
    "v_mfma_f32_16x16x32_bf16 v[80:83], v[168:171], a[200:203], v[80:83]\n"
    "ds_read_b128 v[160:163], v200 offset:36864\n"
    "v_cvt_pk_bf16_f32 v142, v60, v61\n"
    "v_mfma_f32_16x16x32_bf16 v[80:83], v[172:175], a[204:207], v[80:83]\n"
    "v_exp_f32 v64, v64\n"
    "v_mfma_f32_16x16x32_bf16 v[84:87], v[172:175], a[228:231], v[84:87]\n"
    "v_exp_f32 v65, v65\n"
    "v_mfma_f32_16x16x32_bf16 v[84:87], v[168:171], a[224:227], v[84:87]\n"
    "v_exp_f32 v66, v66\n"
    "v_mfma_f32_16x16x32_bf16 v[88:91], v[168:171], a[248:251], v[88:91]\n"
    "ds_read_b128 v[164:167], v200 offset:37888\n"
    "v_cvt_pk_bf16_f32 v143, v62, v63\n"
    "v_mfma_f32_16x16x32_bf16 v[88:91], v[172:175], a[252:255], v[88:91]\n"
    "v_exp_f32 v67, v67\n"
    "v_mfma_f32_16x16x32_bf16 v[92:95], v[172:175], v[20:23], v[92:95]\n"
    "s_add_u32 s65, s60, s92\n"
    "v_exp_f32 v68, v68\n"
    "v_mfma_f32_16x16x32_bf16 v[92:95], v[168:171], v[16:19], v[92:95]\n"
    "s_waitcnt lgkmcnt(2)\n"
    "s_add_u32 m0, s90, 0\n"
    "v_cvt_pk_bf16_f32 v138, v56, v57\n"
    "v_mfma_f32_16x16x32_bf16 v[92:95], v[176:179], v[8:11], v[92:95]\n"
    "ds_read_b128 v[168:171], v200 offset:38912\n"
    "v_cvt_pk_bf16_f32 v139, v58, v59\n"
    "v_mfma_f32_16x16x32_bf16 v[92:95], v[180:183], v[12:15], v[92:95]\n"
    "buffer_load_dwordx4 v200, s[36:39], s65 offen sc1 lds\n"
    "v_mfma_f32_16x16x32_bf16 v[88:91], v[180:183], a[244:247], v[88:91]\n"
    "v_exp_f32 v69, v69\n"
    "v_mfma_f32_16x16x32_bf16 v[88:91], v[176:179], a[240:243], v[88:91]\n"
    "v_exp_f32 v70, v70\n"
    "v_mfma_f32_16x16x32_bf16 v[84:87], v[176:179], a[216:219], v[84:87]\n"
    "ds_read_b128 v[172:175], v200 offset:39936\n"
    "v_cvt_pk_bf16_f32 v134, v52, v53\n"
    "v_mfma_f32_16x16x32_bf16 v[84:87], v[180:183], a[220:223], v[84:87]\n"
    "buffer_load_dwordx4 v200, s[36:39], s65 offen offset:1024 sc1 lds\n"
    "v_mfma_f32_16x16x32_bf16 v[80:83], v[180:183], a[196:199], v[80:83]\n"
    "v_exp_f32 v71, v71\n"
    "v_mfma_f32_16x16x32_bf16 v[80:83], v[176:179], a[192:195], v[80:83]\n"
    "s_waitcnt lgkmcnt(2)\n"
    "v_exp_f32 v72, v72\n"
    "v_mfma_f32_16x16x32_bf16 v[32:35], v[160:163], a[192:195], v[96:99]\n"
    "ds_read_b128 v[176:179], v200 offset:40960\n"
    "v_cvt_pk_bf16_f32 v135, v54, v55\n"
    "v_mfma_f32_16x16x32_bf16 v[32:35], v[164:167], a[196:199], v[32:35]\n"
    "buffer_load_dwordx4 v200, s[36:39], s65 offen offset:2048 sc1 lds\n"
    "v_mfma_f32_16x16x32_bf16 v[36:39], v[164:167], a[220:223], v[100:103]\n"
    "v_exp_f32 v73, v73\n"
    "v_mfma_f32_16x16x32_bf16 v[36:39], v[160:163], a[216:219], v[36:39]\n"
    "v_exp_f32 v74, v74\n"
    "v_mfma_f32_16x16x32_bf16 v[40:43], v[160:163], a[240:243], v[104:107]\n"
    "ds_read_b128 v[180:183], v200 offset:41984\n"
    "v_cvt_pk_bf16_f32 v130, v48, v49\n"
    "v_mfma_f32_16x16x32_bf16 v[40:43], v[164:167], a[244:247], v[40:43]\n"
    "buffer_load_dwordx4 v200, s[36:39], s65 offen offset:3072 sc1 lds\n"
    "v_mfma_f32_16x16x32_bf16 v[44:47], v[164:167], v[12:15], v[108:111]\n"
    "s_add_u32 s65, s65, 0x1000\n"
    "v_exp_f32 v75, v75\n"
    "v_mfma_f32_16x16x32_bf16 v[44:47], v[160:163], v[8:11], v[44:47]\n"
    "s_waitcnt lgkmcnt(2)\n"
    "s_add_u32 m0, s90, 0x1000\n"
    "v_cvt_pk_bf16_f32 v131, v50, v51\n"
    "v_mfma_f32_16x16x32_bf16 v[44:47], v[168:171], v[16:19], v[44:47]\n"
    "ds_read_b128 v[160:163], v200 offset:47104\n"
    "v_cvt_pk_bf16_f32 v144, v64, v65\n"
    "v_mfma_f32_16x16x32_bf16 v[44:47], v[172:175], v[20:23], v[44:47]\n"
    "buffer_load_dwordx4 v200, s[36:39], s65 offen sc1 lds\n"
    "v_mfma_f32_16x16x32_bf16 v[40:43], v[172:175], a[252:255], v[40:43]\n"
    "v_exp_f32 v76, v76\n"
    "v_mfma_f32_16x16x32_bf16 v[40:43], v[168:171], a[248:251], v[40:43]\n"
    "v_exp_f32 v77, v77\n"
    "v_mfma_f32_16x16x32_bf16 v[36:39], v[168:171], a[224:227], v[36:39]\n"
    "ds_read_b128 v[164:167], v200 offset:48128\n"
    "v_cvt_pk_bf16_f32 v145, v66, v67\n"
    "v_mfma_f32_16x16x32_bf16 v[36:39], v[172:175], a[228:231], v[36:39]\n"
    "buffer_load_dwordx4 v200, s[36:39], s65 offen offset:1024 sc1 lds\n"
    "v_mfma_f32_16x16x32_bf16 v[32:35], v[172:175], a[204:207], v[32:35]\n"
    "v_exp_f32 v78, v78\n"
    "v_mfma_f32_16x16x32_bf16 v[32:35], v[168:171], a[200:203], v[32:35]\n"
    "s_waitcnt lgkmcnt(2)\n"
    "v_exp_f32 v79, v79\n"
    "v_mfma_f32_16x16x32_bf16 v[32:35], v[176:179], a[208:211], v[32:35]\n"
    "ds_read_b128 v[168:171], v200 offset:45056\n"
    "v_cvt_pk_bf16_f32 v148, v68, v69\n"
    "v_mfma_f32_16x16x32_bf16 v[32:35], v[180:183], a[212:215], v[32:35]\n"
    "buffer_load_dwordx4 v200, s[36:39], s65 offen offset:2048 sc1 lds\n"
    "v_mfma_f32_16x16x32_bf16 v[36:39], v[180:183], a[236:239], v[36:39]\n"
    "v_exp_f32 v80, v80\n"
    "v_mfma_f32_16x16x32_bf16 v[36:39], v[176:179], a[232:235], v[36:39]\n"
    "s_add_u32 m0, s71, 0x12000\n"
    "v_exp_f32 v81, v81\n"
    "v_mfma_f32_16x16x32_bf16 v[40:43], v[176:179], v[0:3], v[40:43]\n"
    "ds_read_b128 v[172:175], v200 offset:46080\n"
    "v_cvt_pk_bf16_f32 v149, v70, v71\n"
    "v_mfma_f32_16x16x32_bf16 v[40:43], v[180:183], v[4:7], v[40:43]\n"
    "buffer_load_dwordx4 v200, s[40:43], s61 offen sc1 lds\n"
    "v_mfma_f32_16x16x32_bf16 v[44:47], v[180:183], v[28:31], v[44:47]\n"
    "v_exp_f32 v82, v82\n"
    "v_mfma_f32_16x16x32_bf16 v[44:47], v[176:179], v[24:27], v[44:47]\n"
    "s_waitcnt lgkmcnt(2)\n"
    "v_exp_f32 v83, v83\n"
    "v_mfma_f32_16x16x32_bf16 v[60:63], v[160:163], v[24:27], v[108:111]\n"
    "ds_read_b128 v[176:179], v200 offset:43008\n"
    "v_cvt_pk_bf16_f32 v152, v72, v73\n"
    "v_mfma_f32_16x16x32_bf16 v[60:63], v[164:167], v[28:31], v[60:63]\n"
    "buffer_load_dwordx4 v200, s[40:43], s61 offen offset:1024 sc1 lds\n"
    "v_mfma_f32_16x16x32_bf16 v[56:59], v[164:167], v[4:7], v[104:107]\n"
    "v_exp_f32 v84, v84\n"
    "v_mfma_f32_16x16x32_bf16 v[56:59], v[160:163], v[0:3], v[56:59]\n"
    "v_exp_f32 v85, v85\n"
    "v_mfma_f32_16x16x32_bf16 v[52:55], v[160:163], a[232:235], v[100:103]\n"
    "ds_read_b128 v[180:183], v200 offset:44032\n"
    "v_cvt_pk_bf16_f32 v153, v74, v75\n"
    "v_mfma_f32_16x16x32_bf16 v[52:55], v[164:167], a[236:239], v[52:55]\n"
    "buffer_load_dwordx4 v200, s[40:43], s61 offen offset:2048 sc1 lds\n"
    "v_mfma_f32_16x16x32_bf16 v[48:51], v[164:167], a[212:215], v[96:99]\n"
    "v_exp_f32 v86, v86\n"
    "v_mfma_f32_16x16x32_bf16 v[48:51], v[160:163], a[208:211], v[48:51]\n"
    "s_waitcnt lgkmcnt(2)\n"
    "v_exp_f32 v87, v87\n"
    "v_mfma_f32_16x16x32_bf16 v[48:51], v[168:171], a[200:203], v[48:51]\n"
    "ds_read_b128 v[160:163], v201\n"
    "v_cvt_pk_bf16_f32 v156, v76, v77\n"
    "v_mfma_f32_16x16x32_bf16 v[48:51], v[172:175], a[204:207], v[48:51]\n"
    "buffer_load_dwordx4 v200, s[40:43], s61 offen offset:3072 sc1 lds\n"
    "v_mfma_f32_16x16x32_bf16 v[52:55], v[172:175], a[228:231], v[52:55]\n"
    "s_add_u32 s65, s61, 0x1000\n"
    "v_exp_f32 v88, v88\n"
    "v_mfma_f32_16x16x32_bf16 v[52:55], v[168:171], a[224:227], v[52:55]\n"
    "s_add_u32 m0, s71, 0x13000\n"
    "v_exp_f32 v89, v89\n"
    "v_mfma_f32_16x16x32_bf16 v[56:59], v[168:171], a[248:251], v[56:59]\n"
    "ds_read_b128 v[164:167], v201 offset:1024\n"
    "v_cvt_pk_bf16_f32 v157, v78, v79\n"
    "v_mfma_f32_16x16x32_bf16 v[56:59], v[172:175], a[252:255], v[56:59]\n"
    "buffer_load_dwordx4 v200, s[40:43], s65 offen sc1 lds\n"
    "v_mfma_f32_16x16x32_bf16 v[60:63], v[172:175], v[20:23], v[60:63]\n"
    "v_exp_f32 v90, v90\n"
    "v_mfma_f32_16x16x32_bf16 v[60:63], v[168:171], v[16:19], v[60:63]\n"
    "s_waitcnt lgkmcnt(2)\n"
    "v_exp_f32 v91, v91\n"
    "v_mfma_f32_16x16x32_bf16 v[60:63], v[176:179], v[8:11], v[60:63]\n"
    "ds_read_b128 v[168:171], v201 offset:2048\n"
    "v_cvt_pk_bf16_f32 v146, v80, v81\n"
    "v_mfma_f32_16x16x32_bf16 v[60:63], v[180:183], v[12:15], v[60:63]\n"
    "buffer_load_dwordx4 v200, s[40:43], s65 offen offset:1024 sc1 lds\n"
    "v_mfma_f32_16x16x32_bf16 v[56:59], v[180:183], a[244:247], v[56:59]\n"
    "v_cvt_pk_bf16_f32 v147, v82, v83\n"
    "v_cvt_pk_bf16_f32 v150, v84, v85\n"
    "v_mfma_f32_16x16x32_bf16 v[56:59], v[176:179], a[240:243], v[56:59]\n"
    "v_exp_f32 v92, v92\n"
    "v_mfma_f32_16x16x32_bf16 v[52:55], v[176:179], a[216:219], v[52:55]\n"
    "ds_read_b128 v[172:175], v201 offset:3072\n"
    "v_cvt_pk_bf16_f32 v151, v86, v87\n"
    "v_mfma_f32_16x16x32_bf16 v[52:55], v[180:183], a[220:223], v[52:55]\n"
    "v_exp_f32 v93, v93\n"
    "v_mfma_f32_16x16x32_bf16 v[48:51], v[180:183], a[196:199], v[48:51]\n"
    "v_exp_f32 v94, v94\n"
    "v_mfma_f32_16x16x32_bf16 v[48:51], v[176:179], a[192:195], v[48:51]\n"
    "s_waitcnt lgkmcnt(2)\n"
    "v_cvt_pk_bf16_f32 v154, v88, v89\n"
    "v_cvt_pk_bf16_f32 v155, v90, v91\n"
    "v_mfma_f32_16x16x32_bf16 a[0:3], v[160:163], v[128:131], a[0:3]\n"
    "ds_read_b128 v[176:179], v201 offset:4096\n"
    "v_cvt_pk_bf16_f32 v158, v92, v93\n"
    "v_mfma_f32_16x16x32_bf16 a[0:3], v[164:167], v[144:147], a[0:3]\n"
    "v_exp_f32 v95, v95\n"
    "v_mfma_f32_16x16x32_bf16 a[4:7], v[160:163], v[132:135], a[4:7]\n"
    "v_cvt_pk_bf16_f32 v159, v94, v95\n"
    "v_add_f32 v113, v113, v64\n"
    "v_mfma_f32_16x16x32_bf16 a[4:7], v[164:167], v[148:151], a[4:7]\n"
    "v_exp_f32 v32, v32\n"
    "v_mfma_f32_16x16x32_bf16 a[8:11], v[160:163], v[136:139], a[8:11]\n"
    "ds_read_b128 v[180:183], v201 offset:5120\n"
    "v_add_f32 v114, v114, v65\n"
    "v_mfma_f32_16x16x32_bf16 a[8:11], v[164:167], v[152:155], a[8:11]\n"
    "v_exp_f32 v33, v33\n"
    "v_mfma_f32_16x16x32_bf16 a[12:15], v[160:163], v[140:143], a[12:15]\n"
    "v_exp_f32 v34, v34\n"
    "v_mfma_f32_16x16x32_bf16 a[12:15], v[164:167], v[156:159], a[12:15]\n"
    "s_waitcnt lgkmcnt(2)\n"
    "v_exp_f32 v35, v35\n"
    "v_mfma_f32_16x16x32_bf16 a[28:31], v[168:171], v[140:143], a[28:31]\n"
    "ds_read_b128 v[160:163], v201 offset:6144\n"
    "v_add_f32 v113, v113, v66\n"
    "v_mfma_f32_16x16x32_bf16 a[28:31], v[172:175], v[156:159], a[28:31]\n"
    "v_exp_f32 v36, v36\n"
    "v_mfma_f32_16x16x32_bf16 a[24:27], v[172:175], v[152:155], a[24:27]\n"
    "v_exp_f32 v37, v37\n"
    "v_mfma_f32_16x16x32_bf16 a[24:27], v[168:171], v[136:139], a[24:27]\n"
    "v_exp_f32 v38, v38\n"
    "v_mfma_f32_16x16x32_bf16 a[20:23], v[168:171], v[132:135], a[20:23]\n"
    "ds_read_b128 v[164:167], v201 offset:7168\n"
    "v_add_f32 v114, v114, v67\n"
    "v_mfma_f32_16x16x32_bf16 a[20:23], v[172:175], v[148:151], a[20:23]\n"
    "v_exp_f32 v39, v39\n"
    "v_mfma_f32_16x16x32_bf16 a[16:19], v[172:175], v[144:147], a[16:19]\n"
    "v_exp_f32 v40, v40\n"
    "v_mfma_f32_16x16x32_bf16 a[16:19], v[168:171], v[128:131], a[16:19]\n"
    "s_waitcnt lgkmcnt(2)\n"
    "v_exp_f32 v41, v41\n"
    "v_mfma_f32_16x16x32_bf16 a[32:35], v[176:179], v[128:131], a[32:35]\n"
    "ds_read_b128 v[168:171], v201 offset:8192\n"
    "v_add_f32 v117, v117, v68\n"
    "v_mfma_f32_16x16x32_bf16 a[32:35], v[180:183], v[144:147], a[32:35]\n"
    "v_exp_f32 v42, v42\n"
    "v_mfma_f32_16x16x32_bf16 a[36:39], v[180:183], v[148:151], a[36:39]\n"
    "v_exp_f32 v43, v43\n"
    "v_mfma_f32_16x16x32_bf16 a[36:39], v[176:179], v[132:135], a[36:39]\n"
    "v_exp_f32 v44, v44\n"
    "v_mfma_f32_16x16x32_bf16 a[40:43], v[176:179], v[136:139], a[40:43]\n"
    "ds_read_b128 v[172:175], v201 offset:9216\n"
    "v_add_f32 v118, v118, v69\n"
    "v_mfma_f32_16x16x32_bf16 a[40:43], v[180:183], v[152:155], a[40:43]\n"
    "v_exp_f32 v45, v45\n"
    "v_mfma_f32_16x16x32_bf16 a[44:47], v[180:183], v[156:159], a[44:47]\n"
    "v_exp_f32 v46, v46\n"
    "v_mfma_f32_16x16x32_bf16 a[44:47], v[176:179], v[140:143], a[44:47]\n"
    "s_waitcnt lgkmcnt(2)\n"
    "v_exp_f32 v47, v47\n"
    "v_mfma_f32_16x16x32_bf16 a[60:63], v[160:163], v[140:143], a[60:63]\n"
    "ds_read_b128 v[176:179], v201 offset:10240\n"
    "v_add_f32 v117, v117, v70\n"
    "v_mfma_f32_16x16x32_bf16 a[60:63], v[164:167], v[156:159], a[60:63]\n"
    "v_exp_f32 v48, v48\n"
    "v_mfma_f32_16x16x32_bf16 a[56:59], v[164:167], v[152:155], a[56:59]\n"
    "v_exp_f32 v49, v49\n"
    "v_mfma_f32_16x16x32_bf16 a[56:59], v[160:163], v[136:139], a[56:59]\n"
    "v_exp_f32 v50, v50\n"
    "v_mfma_f32_16x16x32_bf16 a[52:55], v[160:163], v[132:135], a[52:55]\n"
    "ds_read_b128 v[180:183], v201 offset:11264\n"
    "v_add_f32 v113, v113, v32\n"
    "v_mfma_f32_16x16x32_bf16 a[52:55], v[164:167], v[148:151], a[52:55]\n"
    "v_exp_f32 v51, v51\n"
    "v_mfma_f32_16x16x32_bf16 a[48:51], v[164:167], v[144:147], a[48:51]\n"
    "v_exp_f32 v52, v52\n"
    "v_mfma_f32_16x16x32_bf16 a[48:51], v[160:163], v[128:131], a[48:51]\n"
    "s_waitcnt lgkmcnt(2)\n"
    "v_add_f32 v118, v118, v71\n"
    "v_add_f32 v114, v114, v33\n"
    "v_mfma_f32_16x16x32_bf16 a[64:67], v[168:171], v[128:131], a[64:67]\n"
    "ds_read_b128 v[160:163], v201 offset:12288\n"
    "v_add_f32 v121, v121, v72\n"
    "v_mfma_f32_16x16x32_bf16 a[64:67], v[172:175], v[144:147], a[64:67]\n"
    "v_add_f32 v113, v113, v34\n"
    "v_add_f32 v122, v122, v73\n"
    "v_mfma_f32_16x16x32_bf16 a[68:71], v[172:175], v[148:151], a[68:71]\n"
    "v_exp_f32 v53, v53\n"
    "v_mfma_f32_16x16x32_bf16 a[68:71], v[168:171], v[132:135], a[68:71]\n"
    "s_add_u32 m0, s51, s95\n"
    "v_add_f32 v114, v114, v35\n"
    "v_add_f32 v121, v121, v74\n"
    "v_mfma_f32_16x16x32_bf16 a[72:75], v[168:171], v[136:139], a[72:75]\n"
    "ds_read_b128 v[164:167], v201 offset:13312\n"
    "v_add_f32 v117, v117, v36\n"
    "v_mfma_f32_16x16x32_bf16 a[72:75], v[172:175], v[152:155], a[72:75]\n"
    "buffer_load_dwordx4 v202, s[32:35], s64 offen lds\n"
    "v_mfma_f32_16x16x32_bf16 a[76:79], v[172:175], v[156:159], a[76:79]\n"
    "v_exp_f32 v54, v54\n"
    "v_mfma_f32_16x16x32_bf16 a[76:79], v[168:171], v[140:143], a[76:79]\n"
    "s_waitcnt lgkmcnt(2)\n"
    "v_add_f32 v122, v122, v75\n"
    "v_add_f32 v118, v118, v37\n"
    "v_mfma_f32_16x16x32_bf16 a[92:95], v[176:179], v[140:143], a[92:95]\n"
    "ds_read_b128 v[168:171], v201 offset:14336\n"
    "v_add_f32 v125, v125, v76\n"
    "v_mfma_f32_16x16x32_bf16 a[92:95], v[180:183], v[156:159], a[92:95]\n"
    "buffer_load_dwordx4 v203, s[32:35], s64 offen offset:1024 lds\n"
    "v_mfma_f32_16x16x32_bf16 a[88:91], v[180:183], v[152:155], a[88:91]\n"
    "v_add_f32 v117, v117, v38\n"
    "v_add_f32 v126, v126, v77\n"
    "v_mfma_f32_16x16x32_bf16 a[88:91], v[176:179], v[136:139], a[88:91]\n"
    "v_exp_f32 v55, v55\n"
    "v_mfma_f32_16x16x32_bf16 a[84:87], v[176:179], v[132:135], a[84:87]\n"
    "ds_read_b128 v[172:175], v201 offset:15360\n"
    "v_add_f32 v118, v118, v39\n"
    "v_mfma_f32_16x16x32_bf16 a[84:87], v[180:183], v[148:151], a[84:87]\n"
    "buffer_load_dwordx4 v204, s[32:35], s64 offen offset:2048 lds\n"
    "v_mfma_f32_16x16x32_bf16 a[80:83], v[180:183], v[144:147], a[80:83]\n"
    "v_add_f32 v125, v125, v78\n"
    "v_add_f32 v121, v121, v40\n"
    "v_mfma_f32_16x16x32_bf16 a[80:83], v[176:179], v[128:131], a[80:83]\n"
    "s_waitcnt lgkmcnt(2)\n"
    "v_exp_f32 v56, v56\n"
    "v_mfma_f32_16x16x32_bf16 a[96:99], v[160:163], v[128:131], a[96:99]\n"
    "s_mov_b32 s95, s94\n"
    "ds_read_b128 v[176:179], v201 offset:16384\n"
    "v_add_f32 v126, v126, v79\n"
    "v_mfma_f32_16x16x32_bf16 a[96:99], v[164:167], v[144:147], a[96:99]\n"
    "s_sub_u32 s94, s94, 0x3000\n"
    "v_add_f32 v122, v122, v41\n"
    "v_add_f32 v113, v113, v80\n"
    "v_mfma_f32_16x16x32_bf16 a[100:103], v[164:167], v[148:151], a[100:103]\n"
    "s_min_u32 s94, s94, 0x6000\n"
    "v_add_f32 v121, v121, v42\n"
    "v_add_f32 v114, v114, v81\n"
    "v_mfma_f32_16x16x32_bf16 a[100:103], v[160:163], v[132:135], a[100:103]\n"
    "s_sub_u32 s93, s94, s95\n"
    "v_exp_f32 v57, v57\n"
    "v_mfma_f32_16x16x32_bf16 a[104:107], v[160:163], v[136:139], a[104:107]\n"
    "ds_read_b128 v[180:183], v201 offset:17408\n"
    "v_add_f32 v122, v122, v43\n"
    "v_mfma_f32_16x16x32_bf16 a[104:107], v[164:167], v[152:155], a[104:107]\n"
    "v_add_f32 v113, v113, v82\n"
    "v_add_f32 v125, v125, v44\n"
    "v_mfma_f32_16x16x32_bf16 a[108:111], v[164:167], v[156:159], a[108:111]\n"
    "v_exp_f32 v58, v58\n"
    "v_mfma_f32_16x16x32_bf16 a[108:111], v[160:163], v[140:143], a[108:111]\n"
    "s_waitcnt lgkmcnt(2)\n"
    "v_add_f32 v114, v114, v83\n"
    "v_add_f32 v126, v126, v45\n"
    "v_mfma_f32_16x16x32_bf16 a[124:127], v[168:171], v[140:143], a[124:127]\n"
    "ds_read_b128 v[160:163], v201 offset:18432\n"
    "v_add_f32 v117, v117, v84\n"
    "v_mfma_f32_16x16x32_bf16 a[124:127], v[172:175], v[156:159], a[124:127]\n"
    "v_add_f32 v125, v125, v46\n"
    "v_add_f32 v118, v118, v85\n"
    "v_mfma_f32_16x16x32_bf16 a[120:123], v[172:175], v[152:155], a[120:123]\n"
    "v_exp_f32 v59, v59\n"
    "v_mfma_f32_16x16x32_bf16 a[120:123], v[168:171], v[136:139], a[120:123]\n"
    "v_add_f32 v126, v126, v47\n"
    "v_add_f32 v117, v117, v86\n"
    "v_mfma_f32_16x16x32_bf16 a[116:119], v[168:171], v[132:135], a[116:119]\n"
    "ds_read_b128 v[164:167], v201 offset:19456\n"
    "v_add_f32 v113, v113, v48\n"
    "v_mfma_f32_16x16x32_bf16 a[116:119], v[172:175], v[148:151], a[116:119]\n"
    "v_exp_f32 v60, v60\n"
    "v_mfma_f32_16x16x32_bf16 a[112:115], v[172:175], v[144:147], a[112:115]\n"
    "v_add_u32 v205, s93, v205\n"
    "v_add_f32 v118, v118, v87\n"
    "v_mfma_f32_16x16x32_bf16 a[112:115], v[168:171], v[128:131], a[112:115]\n"
    "s_waitcnt lgkmcnt(2)\n"
    "v_add_f32 v114, v114, v49\n"
    "v_add_u32 v209, 0x180, v209\n"
    "v_mfma_f32_16x16x32_bf16 a[128:131], v[176:179], v[128:131], a[128:131]\n"
    "ds_read_b128 v[168:171], v201 offset:20480\n"
    "v_add_u32 v206, s93, v206\n"
    "v_mfma_f32_16x16x32_bf16 a[128:131], v[180:183], v[144:147], a[128:131]\n"
    "v_add_f32 v121, v121, v88\n"
    "v_add_f32 v113, v113, v50\n"
    "v_mfma_f32_16x16x32_bf16 a[132:135], v[180:183], v[148:151], a[132:135]\n"
    "s_add_u32 s64, s64, 0xc000\n"
    "v_exp_f32 v61, v61\n"
    "v_mfma_f32_16x16x32_bf16 a[132:135], v[176:179], v[132:135], a[132:135]\n"
    "s_add_u32 s62, s62, 0x6000\n"
    "v_add_u32 v210, 0x80, v210\n"
    "v_add_u32 v207, s93, v207\n"
    "v_mfma_f32_16x16x32_bf16 a[136:139], v[176:179], v[136:139], a[136:139]\n"
    "s_add_u32 s63, s63, 0x6000\n"
    "ds_read_b128 v[172:175], v201 offset:21504\n"
    "v_add_f32 v122, v122, v89\n"
    "v_mfma_f32_16x16x32_bf16 a[136:139], v[180:183], v[152:155], a[136:139]\n"
    "s_add_u32 s60, s60, 0x6000\n"
    "v_add_f32 v114, v114, v51\n"
    "v_add_f32 v121, v121, v90\n"
    "v_mfma_f32_16x16x32_bf16 a[140:143], v[180:183], v[156:159], a[140:143]\n"
    "s_add_u32 s61, s61, 0x6000\n"
    "v_add_f32 v117, v117, v52\n"
    "v_add_f32 v122, v122, v91\n"
    "v_mfma_f32_16x16x32_bf16 a[140:143], v[176:179], v[140:143], a[140:143]\n"
    "s_waitcnt lgkmcnt(2)\n"
    "v_exp_f32 v62, v62\n"
    "v_mfma_f32_16x16x32_bf16 a[156:159], v[160:163], v[140:143], a[156:159]\n"
    "ds_read_b128 v[176:179], v201 offset:22528\n"
    "v_add_f32 v118, v118, v53\n"
    "v_mfma_f32_16x16x32_bf16 a[156:159], v[164:167], v[156:159], a[156:159]\n"
    "s_waitcnt vmcnt(3)\n"
    "ds_read_b128 v[180:183], v201 offset:23552\n"
    "v_add_f32 v125, v125, v92\n"
    "v_mfma_f32_16x16x32_bf16 a[152:155], v[164:167], v[152:155], a[152:155]\n"
    "v_add_f32 v117, v117, v54\n"
    "v_add_f32 v126, v126, v93\n"
    "v_mfma_f32_16x16x32_bf16 a[152:155], v[160:163], v[136:139], a[152:155]\n"
    "v_exp_f32 v63, v63\n"
    "v_mfma_f32_16x16x32_bf16 a[148:151], v[160:163], v[132:135], a[148:151]\n"
    "v_add_f32 v118, v118, v55\n"
    "v_add_f32 v125, v125, v94\n"
    "v_mfma_f32_16x16x32_bf16 a[148:151], v[164:167], v[148:151], a[148:151]\n"
    "v_add_f32 v121, v121, v56\n"
    "v_add_f32 v126, v126, v95\n"
    "v_mfma_f32_16x16x32_bf16 a[144:147], v[164:167], v[144:147], a[144:147]\n"
    "v_add_f32 v122, v122, v57\n"
    "v_mfma_f32_16x16x32_bf16 a[144:147], v[160:163], v[128:131], a[144:147]\n"
    "s_waitcnt lgkmcnt(0)\n"
    "v_mfma_f32_16x16x32_bf16 a[160:163], v[168:171], v[128:131], a[160:163]\n"
    "s_barrier\n"
    "v_mfma_f32_16x16x32_bf16 a[160:163], v[172:175], v[144:147], a[160:163]\n"
    "ds_read_u16_d16_hi v223, v209 offset:6144\n"
    "ds_read_u16_d16_hi v222, v209\n"
    "v_mfma_f32_16x16x32_bf16 a[164:167], v[172:175], v[148:151], a[164:167]\n"
    "ds_read_b128 v[160:163], v200\n"
    "v_add_f32 v121, v121, v58\n"
    "v_mfma_f32_16x16x32_bf16 a[164:167], v[168:171], v[132:135], a[164:167]\n"
    "ds_read_b128 v[232:235], v205 offset:24576\n"
    "v_add_f32 v122, v122, v59\n"
    "v_mfma_f32_16x16x32_bf16 a[168:171], v[168:171], v[136:139], a[168:171]\n"
    "ds_read_b128 v[164:167], v200 offset:1024\n"
    "ds_read_b128 v[236:239], v206 offset:24576\n"
    "v_mfma_f32_16x16x32_bf16 a[168:171], v[172:175], v[152:155], a[168:171]\n"
    "v_add_f32 v125, v125, v60\n"
    "v_add_f32 v126, v126, v61\n"
    "v_mfma_f32_16x16x32_bf16 a[172:175], v[172:175], v[156:159], a[172:175]\n"
    "v_add_f32 v125, v125, v62\n"
    "v_add_f32 v126, v126, v63\n"
    "v_mfma_f32_16x16x32_bf16 a[172:175], v[168:171], v[140:143], a[172:175]\n"
    "ds_read_b64 v[228:229], v210\n"
    "ds_read2st64_b32 v[240:241], v207 offset0:96 offset1:99\n"
    "v_mfma_f32_16x16x32_bf16 a[188:191], v[176:179], v[140:143], a[188:191]\n"
    "ds_read_b128 v[168:171], v200 offset:2048\n"
    "ds_read2st64_b32 v[242:243], v207 offset0:102 offset1:105\n"
    "v_mfma_f32_16x16x32_bf16 a[188:191], v[180:183], v[156:159], a[188:191]\n"
    "ds_read2st64_b32 v[244:245], v207 offset0:108 offset1:111\n"
    "ds_read2st64_b32 v[246:247], v207 offset0:114 offset1:117\n"
    "v_mfma_f32_16x16x32_bf16 a[184:187], v[176:179], v[136:139], a[184:187]\n"
    "v_cvt_pk_bf16_f32 v140, v44, v45\n"
    "v_cvt_pk_bf16_f32 v141, v46, v47\n"
    "v_mfma_f32_16x16x32_bf16 a[184:187], v[180:183], v[152:155], a[184:187]\n"
    "s_waitcnt lgkmcnt(6)\n"
    "v_mul_f32 v226, s75, v223\n"
    "v_mfma_f32_16x16x32_bf16 a[180:183], v[176:179], v[132:135], a[180:183]\n"
    "ds_read_b128 v[172:175], v200 offset:3072\n"
    "v_fma_f32 v227, -v222, v226, v217\n"
    "v_mfma_f32_16x16x32_bf16 a[180:183], v[180:183], v[148:151], a[180:183]\n"
    "v_cvt_f32_i32 v232, v232\n"
    "v_cvt_f32_i32 v233, v233\n"
    "v_mfma_f32_16x16x32_bf16 a[176:179], v[176:179], v[128:131], a[176:179]\n"
    "v_cvt_f32_i32 v234, v234\n"
    "v_cvt_f32_i32 v235, v235\n"
    "v_mfma_f32_16x16x32_bf16 a[176:179], v[180:183], v[144:147], a[176:179]\n"
    "v_cvt_f32_i32 v236, v236\n"
    "v_cvt_f32_i32 v237, v237\n"
    "LM01:\n"
    "v_mfma_f32_16x16x32_bf16 v[64:67], v[160:163], a[192:195], v[96:99]\n"
    "ds_read_b128 v[176:179], v200 offset:4096\n"
    "v_cvt_f32_i32 v238, v238\n"
    "v_mfma_f32_16x16x32_bf16 v[64:67], v[164:167], a[196:199], v[64:67]\n"
    "v_cvt_f32_i32 v239, v239\n"
    "v_mul_f32_dpp v232, v226, v232 row_newbcast:0 row_mask:0xf bank_mask:0xf\n"
    "v_mfma_f32_16x16x32_bf16 v[68:71], v[164:167], a[220:223], v[100:103]\n"
    "v_mul_f32_dpp v233, v226, v233 row_newbcast:1 row_mask:0xf bank_mask:0xf\n"
    "v_mul_f32_dpp v234, v226, v234 row_newbcast:2 row_mask:0xf bank_mask:0xf\n"
    "v_mfma_f32_16x16x32_bf16 v[68:71], v[160:163], a[216:219], v[68:71]\n"
    "v_mul_f32_dpp v235, v226, v235 row_newbcast:3 row_mask:0xf bank_mask:0xf\n"
    "v_mul_f32_dpp v236, v226, v236 row_newbcast:4 row_mask:0xf bank_mask:0xf\n"
    "v_mfma_f32_16x16x32_bf16 v[72:75], v[160:163], a[240:243], v[104:107]\n"
    "ds_read_b128 v[180:183], v200 offset:5120\n"
    "v_mul_f32_dpp v237, v226, v237 row_newbcast:5 row_mask:0xf bank_mask:0xf\n"
    "v_mfma_f32_16x16x32_bf16 v[72:75], v[164:167], a[244:247], v[72:75]\n"
    "v_mul_f32_dpp v238, v226, v238 row_newbcast:6 row_mask:0xf bank_mask:0xf\n"
    "v_mul_f32_dpp v239, v226, v239 row_newbcast:7 row_mask:0xf bank_mask:0xf\n"
    "v_mfma_f32_16x16x32_bf16 v[76:79], v[164:167], v[12:15], v[108:111]\n"
    "v_add_f32_dpp v232, v227, v232 row_newbcast:0 row_mask:0xf bank_mask:0xf\n"
    "v_add_f32_dpp v233, v227, v233 row_newbcast:1 row_mask:0xf bank_mask:0xf\n"
    "v_mfma_f32_16x16x32_bf16 v[76:79], v[160:163], v[8:11], v[76:79]\n"
    "s_waitcnt lgkmcnt(2)\n"
    "v_add_f32_dpp v234, v227, v234 row_newbcast:2 row_mask:0xf bank_mask:0xf\n"
    "v_add_f32_dpp v235, v227, v235 row_newbcast:3 row_mask:0xf bank_mask:0xf\n"
    "v_mfma_f32_16x16x32_bf16 v[76:79], v[168:171], v[16:19], v[76:79]\n"
    "ds_read_b128 v[160:163], v200 offset:10240\n"
    "v_add_f32_dpp v236, v227, v236 row_newbcast:4 row_mask:0xf bank_mask:0xf\n"
    "v_mfma_f32_16x16x32_bf16 v[76:79], v[172:175], v[20:23], v[76:79]\n"
    "v_add_f32_dpp v237, v227, v237 row_newbcast:5 row_mask:0xf bank_mask:0xf\n"
    "v_add_f32_dpp v238, v227, v238 row_newbcast:6 row_mask:0xf bank_mask:0xf\n"
    "v_mfma_f32_16x16x32_bf16 v[72:75], v[172:175], a[252:255], v[72:75]\n"
    "v_add_f32_dpp v239, v227, v239 row_newbcast:7 row_mask:0xf bank_mask:0xf\n"
    "v_cvt_pk_bf16_f32 v232, v232, v233\n"
    "v_mfma_f32_16x16x32_bf16 v[72:75], v[168:171], a[248:251], v[72:75]\n"
    "v_cvt_f32_i32 v240, v240\n"
    "v_cvt_pk_bf16_f32 v233, v234, v235\n"
    "v_mfma_f32_16x16x32_bf16 v[68:71], v[168:171], a[224:227], v[68:71]\n"
    "ds_read_b128 v[164:167], v200 offset:11264\n"
    "v_cvt_f32_i32 v241, v241\n"
    "v_mfma_f32_16x16x32_bf16 v[68:71], v[172:175], a[228:231], v[68:71]\n"
    "v_cvt_pk_bf16_f32 v234, v236, v237\n"
    "v_cvt_f32_i32 v242, v242\n"
    "v_mfma_f32_16x16x32_bf16 v[64:67], v[172:175], a[204:207], v[64:67]\n"
    "v_cvt_pk_bf16_f32 v235, v238, v239\n"
    "v_cvt_f32_i32 v243, v243\n"
    "v_mfma_f32_16x16x32_bf16 v[64:67], v[168:171], a[200:203], v[64:67]\n"
    "s_waitcnt lgkmcnt(2)\n"
    "v_cvt_f32_i32 v244, v244\n"
    "v_cvt_f32_i32 v245, v245\n"
    "v_mfma_f32_16x16x32_bf16 v[64:67], v[176:179], a[208:211], v[64:67]\n"
    "ds_read_b128 v[168:171], v200 offset:8192\n"
    "v_cvt_f32_i32 v246, v246\n"
    "v_mfma_f32_16x16x32_bf16 v[64:67], v[180:183], a[212:215], v[64:67]\n"
    "buffer_store_dwordx4 v[232:235], v211, s[36:39], s62 offen sc1\n"
    "v_cvt_f32_i32 v247, v247\n"
    "v_mul_f32_dpp v240, v228, v240 row_newbcast:0 row_mask:0xf bank_mask:0xf\n"
    "v_mul_f32_dpp v241, v228, v241 row_newbcast:1 row_mask:0xf bank_mask:0xf\n"
    "v_mfma_f32_16x16x32_bf16 v[68:71], v[180:183], a[236:239], v[68:71]\n"
    "v_mul_f32_dpp v242, v228, v242 row_newbcast:2 row_mask:0xf bank_mask:0xf\n"
    "v_mul_f32_dpp v243, v228, v243 row_newbcast:3 row_mask:0xf bank_mask:0xf\n"
    "v_mfma_f32_16x16x32_bf16 v[68:71], v[176:179], a[232:235], v[68:71]\n"
    "v_mul_f32_dpp v244, v228, v244 row_newbcast:4 row_mask:0xf bank_mask:0xf\n"
    "v_mul_f32_dpp v245, v228, v245 row_newbcast:5 row_mask:0xf bank_mask:0xf\n"
    "v_mfma_f32_16x16x32_bf16 v[72:75], v[176:179], v[0:3], v[72:75]\n"
    "ds_read_b128 v[172:175], v200 offset:9216\n"
    "v_mul_f32_dpp v246, v228, v246 row_newbcast:6 row_mask:0xf bank_mask:0xf\n"
    "v_mfma_f32_16x16x32_bf16 v[72:75], v[180:183], v[4:7], v[72:75]\n"
    "v_mul_f32_dpp v247, v228, v247 row_newbcast:7 row_mask:0xf bank_mask:0xf\n"
    "v_add_f32_dpp v240, v229, v240 row_newbcast:0 row_mask:0xf bank_mask:0xf\n"
    "v_mfma_f32_16x16x32_bf16 v[76:79], v[180:183], v[28:31], v[76:79]\n"
    "v_add_f32_dpp v241, v229, v241 row_newbcast:1 row_mask:0xf bank_mask:0xf\n"
    "v_add_f32_dpp v242, v229, v242 row_newbcast:2 row_mask:0xf bank_mask:0xf\n"
    "v_mfma_f32_16x16x32_bf16 v[76:79], v[176:179], v[24:27], v[76:79]\n"
    "s_waitcnt lgkmcnt(2)\n"
    "v_add_f32_dpp v243, v229, v243 row_newbcast:3 row_mask:0xf bank_mask:0xf\n"
    "v_add_f32_dpp v244, v229, v244 row_newbcast:4 row_mask:0xf bank_mask:0xf\n"
    "v_mfma_f32_16x16x32_bf16 v[92:95], v[160:163], v[24:27], v[108:111]\n"
    "ds_read_b128 v[176:179], v200 offset:6144\n"
    "v_add_f32_dpp v245, v229, v245 row_newbcast:5 row_mask:0xf bank_mask:0xf\n"
    "v_mfma_f32_16x16x32_bf16 v[92:95], v[164:167], v[28:31], v[92:95]\n"
    "v_add_f32_dpp v246, v229, v246 row_newbcast:6 row_mask:0xf bank_mask:0xf\n"
    "v_add_f32_dpp v247, v229, v247 row_newbcast:7 row_mask:0xf bank_mask:0xf\n"
    "v_mfma_f32_16x16x32_bf16 v[88:91], v[164:167], v[4:7], v[104:107]\n"
    "v_cvt_pk_bf16_f32 v240, v240, v241\n"
    "v_cvt_pk_bf16_f32 v241, v242, v243\n"
    "v_mfma_f32_16x16x32_bf16 v[88:91], v[160:163], v[0:3], v[88:91]\n"
    "v_cvt_pk_bf16_f32 v242, v244, v245\n"
    "v_cvt_pk_bf16_f32 v243, v246, v247\n"
    "v_mfma_f32_16x16x32_bf16 v[84:87], v[160:163], a[232:235], v[100:103]\n"
    "ds_read_b128 v[180:183], v200 offset:7168\n"
    "v_cvt_pk_bf16_f32 v136, v40, v41\n"
    "v_mfma_f32_16x16x32_bf16 v[84:87], v[164:167], a[236:239], v[84:87]\n"
    "buffer_store_dwordx4 v[240:243], v212, s[40:43], s63 offen sc1\n"
    "v_cvt_pk_bf16_f32 v137, v42, v43\n"
    "v_cvt_pk_bf16_f32 v132, v36, v37\n"
    "v_cvt_pk_bf16_f32 v133, v38, v39\n"
    "v_mfma_f32_16x16x32_bf16 v[80:83], v[164:167], a[212:215], v[96:99]\n"
    "v_cvt_pk_bf16_f32 v128, v32, v33\n"
    "v_cvt_pk_bf16_f32 v129, v34, v35\n"
    "v_mfma_f32_16x16x32_bf16 v[80:83], v[160:163], a[208:211], v[80:83]\n"
    "s_waitcnt lgkmcnt(2)\n"
    "v_exp_f32 v64, v64\n"
    "v_mfma_f32_16x16x32_bf16 v[80:83], v[168:171], a[200:203], v[80:83]\n"
    "ds_read_b128 v[160:163], v200 offset:12288\n"
    "v_cvt_pk_bf16_f32 v142, v60, v61\n"
    "v_mfma_f32_16x16x32_bf16 v[80:83], v[172:175], a[204:207], v[80:83]\n"
    "v_exp_f32 v65, v65\n"
    "v_mfma_f32_16x16x32_bf16 v[84:87], v[172:175], a[228:231], v[84:87]\n"
    "v_exp_f32 v66, v66\n"
    "v_mfma_f32_16x16x32_bf16 v[84:87], v[168:171], a[224:227], v[84:87]\n"
    "v_exp_f32 v67, v67\n"
    "v_mfma_f32_16x16x32_bf16 v[88:91], v[168:171], a[248:251], v[88:91]\n"
    "ds_read_b128 v[164:167], v200 offset:13312\n"
    "v_cvt_pk_bf16_f32 v143, v62, v63\n"
    "v_mfma_f32_16x16x32_bf16 v[88:91], v[172:175], a[252:255], v[88:91]\n"
    "v_exp_f32 v68, v68\n"
    "v_mfma_f32_16x16x32_bf16 v[92:95], v[172:175], v[20:23], v[92:95]\n"
    "s_add_u32 s65, s60, s92\n"
    "v_exp_f32 v69, v69\n"
    "v_mfma_f32_16x16x32_bf16 v[92:95], v[168:171], v[16:19], v[92:95]\n"
    "s_waitcnt lgkmcnt(2)\n"
    "s_add_u32 m0, s90, 0x6000\n"
    "v_cvt_pk_bf16_f32 v138, v56, v57\n"
    "v_mfma_f32_16x16x32_bf16 v[92:95], v[176:179], v[8:11], v[92:95]\n"
    "ds_read_b128 v[168:171], v200 offset:14336\n"
    "v_cvt_pk_bf16_f32 v139, v58, v59\n"
    "v_mfma_f32_16x16x32_bf16 v[92:95], v[180:183], v[12:15], v[92:95]\n"
    "buffer_load_dwordx4 v200, s[36:39], s65 offen sc1 lds\n"
    "v_mfma_f32_16x16x32_bf16 v[88:91], v[180:183], a[244:247], v[88:91]\n"
    "v_exp_f32 v70, v70\n"
    "v_mfma_f32_16x16x32_bf16 v[88:91], v[176:179], a[240:243], v[88:91]\n"
    "v_exp_f32 v71, v71\n"
    "v_mfma_f32_16x16x32_bf16 v[84:87], v[176:179], a[216:219], v[84:87]\n"
    "ds_read_b128 v[172:175], v200 offset:15360\n"
    "v_cvt_pk_bf16_f32 v134, v52, v53\n"
    "v_mfma_f32_16x16x32_bf16 v[84:87], v[180:183], a[220:223], v[84:87]\n"
    "buffer_load_dwordx4 v200, s[36:39], s65 offen offset:1024 sc1 lds\n"
    "v_mfma_f32_16x16x32_bf16 v[80:83], v[180:183], a[196:199], v[80:83]\n"
    "v_exp_f32 v72, v72\n"
    "v_mfma_f32_16x16x32_bf16 v[80:83], v[176:179], a[192:195], v[80:83]\n"
    "s_waitcnt lgkmcnt(2)\n"
    "v_exp_f32 v73, v73\n"
    "v_mfma_f32_16x16x32_bf16 v[32:35], v[160:163], a[192:195], v[96:99]\n"
    "ds_read_b128 v[176:179], v200 offset:16384\n"
    "v_cvt_pk_bf16_f32 v135, v54, v55\n"
    "v_mfma_f32_16x16x32_bf16 v[32:35], v[164:167], a[196:199], v[32:35]\n"
    "buffer_load_dwordx4 v200, s[36:39], s65 offen offset:2048 sc1 lds\n"
    "v_mfma_f32_16x16x32_bf16 v[36:39], v[164:167], a[220:223], v[100:103]\n"
    "v_exp_f32 v74, v74\n"
    "v_mfma_f32_16x16x32_bf16 v[36:39], v[160:163], a[216:219], v[36:39]\n"
    "v_exp_f32 v75, v75\n"
    "v_mfma_f32_16x16x32_bf16 v[40:43], v[160:163], a[240:243], v[104:107]\n"
    "ds_read_b128 v[180:183], v200 offset:17408\n"
    "v_cvt_pk_bf16_f32 v130, v48, v49\n"
    "v_mfma_f32_16x16x32_bf16 v[40:43], v[164:167], a[244:247], v[40:43]\n"
    "buffer_load_dwordx4 v200, s[36:39], s65 offen offset:3072 sc1 lds\n"
    "v_mfma_f32_16x16x32_bf16 v[44:47], v[164:167], v[12:15], v[108:111]\n"
    "s_add_u32 s65, s65, 0x1000\n"
    "v_exp_f32 v76, v76\n"
    "v_mfma_f32_16x16x32_bf16 v[44:47], v[160:163], v[8:11], v[44:47]\n"
    "s_waitcnt lgkmcnt(2)\n"
    "s_add_u32 m0, s90, 0x7000\n"
    "v_cvt_pk_bf16_f32 v131, v50, v51\n"
    "v_mfma_f32_16x16x32_bf16 v[44:47], v[168:171], v[16:19], v[44:47]\n"
    "ds_read_b128 v[160:163], v200 offset:22528\n"
    "v_cvt_pk_bf16_f32 v144, v64, v65\n"
    "v_mfma_f32_16x16x32_bf16 v[44:47], v[172:175], v[20:23], v[44:47]\n"
    "buffer_load_dwordx4 v200, s[36:39], s65 offen sc1 lds\n"
    "v_mfma_f32_16x16x32_bf16 v[40:43], v[172:175], a[252:255], v[40:43]\n"
    "v_exp_f32 v77, v77\n"
    "v_mfma_f32_16x16x32_bf16 v[40:43], v[168:171], a[248:251], v[40:43]\n"
    "v_exp_f32 v78, v78\n"
    "v_mfma_f32_16x16x32_bf16 v[36:39], v[168:171], a[224:227], v[36:39]\n"
    "ds_read_b128 v[164:167], v200 offset:23552\n"
    "v_cvt_pk_bf16_f32 v145, v66, v67\n"
    "v_mfma_f32_16x16x32_bf16 v[36:39], v[172:175], a[228:231], v[36:39]\n"
    "buffer_load_dwordx4 v200, s[36:39], s65 offen offset:1024 sc1 lds\n"
    "v_mfma_f32_16x16x32_bf16 v[32:35], v[172:175], a[204:207], v[32:35]\n"
    "v_exp_f32 v79, v79\n"
    "v_mfma_f32_16x16x32_bf16 v[32:35], v[168:171], a[200:203], v[32:35]\n"
    "s_waitcnt lgkmcnt(2)\n"
    "v_exp_f32 v80, v80\n"
    "v_mfma_f32_16x16x32_bf16 v[32:35], v[176:179], a[208:211], v[32:35]\n"
    "ds_read_b128 v[168:171], v200 offset:20480\n"
    "v_cvt_pk_bf16_f32 v148, v68, v69\n"
    "v_mfma_f32_16x16x32_bf16 v[32:35], v[180:183], a[212:215], v[32:35]\n"
    "buffer_load_dwordx4 v200, s[36:39], s65 offen offset:2048 sc1 lds\n"
    "v_mfma_f32_16x16x32_bf16 v[36:39], v[180:183], a[236:239], v[36:39]\n"
    "v_exp_f32 v81, v81\n"
    "v_mfma_f32_16x16x32_bf16 v[36:39], v[176:179], a[232:235], v[36:39]\n"
    "s_add_u32 m0, s71, 0xc000\n"
    "v_exp_f32 v82, v82\n"
    "v_mfma_f32_16x16x32_bf16 v[40:43], v[176:179], v[0:3], v[40:43]\n"
    "ds_read_b128 v[172:175], v200 offset:21504\n"
    "v_cvt_pk_bf16_f32 v149, v70, v71\n"
    "v_mfma_f32_16x16x32_bf16 v[40:43], v[180:183], v[4:7], v[40:43]\n"
    "buffer_load_dwordx4 v200, s[40:43], s61 offen sc1 lds\n"
    "v_mfma_f32_16x16x32_bf16 v[44:47], v[180:183], v[28:31], v[44:47]\n"
    "v_exp_f32 v83, v83\n"
    "v_mfma_f32_16x16x32_bf16 v[44:47], v[176:179], v[24:27], v[44:47]\n"
    "s_waitcnt lgkmcnt(2)\n"
    "v_exp_f32 v84, v84\n"
    "v_mfma_f32_16x16x32_bf16 v[60:63], v[160:163], v[24:27], v[108:111]\n"
    "ds_read_b128 v[176:179], v200 offset:18432\n"
    "v_cvt_pk_bf16_f32 v152, v72, v73\n"
    "v_mfma_f32_16x16x32_bf16 v[60:63], v[164:167], v[28:31], v[60:63]\n"
    "buffer_load_dwordx4 v200, s[40:43], s61 offen offset:1024 sc1 lds\n"
    "v_mfma_f32_16x16x32_bf16 v[56:59], v[164:167], v[4:7], v[104:107]\n"
    "v_exp_f32 v85, v85\n"
    "v_mfma_f32_16x16x32_bf16 v[56:59], v[160:163], v[0:3], v[56:59]\n"
    "v_exp_f32 v86, v86\n"
    "v_mfma_f32_16x16x32_bf16 v[52:55], v[160:163], a[232:235], v[100:103]\n"
    "ds_read_b128 v[180:183], v200 offset:19456\n"
    "v_cvt_pk_bf16_f32 v153, v74, v75\n"
    "v_mfma_f32_16x16x32_bf16 v[52:55], v[164:167], a[236:239], v[52:55]\n"
    "buffer_load_dwordx4 v200, s[40:43], s61 offen offset:2048 sc1 lds\n"
    "v_mfma_f32_16x16x32_bf16 v[48:51], v[164:167], a[212:215], v[96:99]\n"
    "v_exp_f32 v87, v87\n"
    "v_mfma_f32_16x16x32_bf16 v[48:51], v[160:163], a[208:211], v[48:51]\n"
    "s_waitcnt lgkmcnt(2)\n"
    "v_exp_f32 v88, v88\n"
    "v_mfma_f32_16x16x32_bf16 v[48:51], v[168:171], a[200:203], v[48:51]\n"
    "ds_read_b128 v[160:163], v201 offset:24576\n"
    "v_cvt_pk_bf16_f32 v156, v76, v77\n"
    "v_mfma_f32_16x16x32_bf16 v[48:51], v[172:175], a[204:207], v[48:51]\n"
    "buffer_load_dwordx4 v200, s[40:43], s61 offen offset:3072 sc1 lds\n"
    "v_mfma_f32_16x16x32_bf16 v[52:55], v[172:175], a[228:231], v[52:55]\n"
    "s_add_u32 s65, s61, 0x1000\n"
    "v_exp_f32 v89, v89\n"
    "v_mfma_f32_16x16x32_bf16 v[52:55], v[168:171], a[224:227], v[52:55]\n"
    "s_add_u32 m0, s71, 0xd000\n"
    "v_exp_f32 v90, v90\n"
    "v_mfma_f32_16x16x32_bf16 v[56:59], v[168:171], a[248:251], v[56:59]\n"
    "ds_read_b128 v[164:167], v201 offset:25600\n"
    "v_cvt_pk_bf16_f32 v157, v78, v79\n"
    "v_mfma_f32_16x16x32_bf16 v[56:59], v[172:175], a[252:255], v[56:59]\n"
    "buffer_load_dwordx4 v200, s[40:43], s65 offen sc1 lds\n"
    "v_mfma_f32_16x16x32_bf16 v[60:63], v[172:175], v[20:23], v[60:63]\n"
    "v_exp_f32 v91, v91\n"
    "v_mfma_f32_16x16x32_bf16 v[60:63], v[168:171], v[16:19], v[60:63]\n"
    "s_waitcnt lgkmcnt(2)\n"
    "v_cvt_pk_bf16_f32 v146, v80, v81\n"
    "v_cvt_pk_bf16_f32 v147, v82, v83\n"
    "v_mfma_f32_16x16x32_bf16 v[60:63], v[176:179], v[8:11], v[60:63]\n"
    "ds_read_b128 v[168:171], v201 offset:26624\n"
    "v_cvt_pk_bf16_f32 v150, v84, v85\n"
    "v_mfma_f32_16x16x32_bf16 v[60:63], v[180:183], v[12:15], v[60:63]\n"
    "buffer_load_dwordx4 v200, s[40:43], s65 offen offset:1024 sc1 lds\n"
    "v_mfma_f32_16x16x32_bf16 v[56:59], v[180:183], a[244:247], v[56:59]\n"
    "v_exp_f32 v92, v92\n"
    "v_mfma_f32_16x16x32_bf16 v[56:59], v[176:179], a[240:243], v[56:59]\n"
    "v_exp_f32 v93, v93\n"
    "v_mfma_f32_16x16x32_bf16 v[52:55], v[176:179], a[216:219], v[52:55]\n"
    "ds_read_b128 v[172:175], v201 offset:27648\n"
    "v_cvt_pk_bf16_f32 v151, v86, v87\n"
    "v_mfma_f32_16x16x32_bf16 v[52:55], v[180:183], a[220:223], v[52:55]\n"
    "v_exp_f32 v94, v94\n"
    "v_mfma_f32_16x16x32_bf16 v[48:51], v[180:183], a[196:199], v[48:51]\n"
    "v_cvt_pk_bf16_f32 v154, v88, v89\n"
    "v_cvt_pk_bf16_f32 v155, v90, v91\n"
    "v_mfma_f32_16x16x32_bf16 v[48:51], v[176:179], a[192:195], v[48:51]\n"
    "s_waitcnt lgkmcnt(2)\n"
    "v_exp_f32 v95, v95\n"
    "v_mfma_f32_16x16x32_bf16 a[0:3], v[160:163], v[128:131], a[0:3]\n"
    "ds_read_b128 v[176:179], v201 offset:28672\n"
    "v_cvt_pk_bf16_f32 v158, v92, v93\n"
    "v_mfma_f32_16x16x32_bf16 a[0:3], v[164:167], v[144:147], a[0:3]\n"
    "v_cvt_pk_bf16_f32 v159, v94, v95\n"
    "v_add_f32 v113, v113, v64\n"
    "v_mfma_f32_16x16x32_bf16 a[4:7], v[160:163], v[132:135], a[4:7]\n"
    "v_exp_f32 v32, v32\n"
    "v_mfma_f32_16x16x32_bf16 a[4:7], v[164:167], v[148:151], a[4:7]\n"
    "v_exp_f32 v33, v33\n"
    "v_mfma_f32_16x16x32_bf16 a[8:11], v[160:163], v[136:139], a[8:11]\n"
    "ds_read_b128 v[180:183], v201 offset:29696\n"
    "v_add_f32 v114, v114, v65\n"
    "v_mfma_f32_16x16x32_bf16 a[8:11], v[164:167], v[152:155], a[8:11]\n"
    "v_exp_f32 v34, v34\n"
    "v_mfma_f32_16x16x32_bf16 a[12:15], v[160:163], v[140:143], a[12:15]\n"
    "v_exp_f32 v35, v35\n"
    "v_mfma_f32_16x16x32_bf16 a[12:15], v[164:167], v[156:159], a[12:15]\n"
    "s_waitcnt lgkmcnt(2)\n"
    "v_exp_f32 v36, v36\n"
    "v_mfma_f32_16x16x32_bf16 a[28:31], v[168:171], v[140:143], a[28:31]\n"
    "ds_read_b128 v[160:163], v201 offset:30720\n"
    "v_add_f32 v113, v113, v66\n"
    "v_mfma_f32_16x16x32_bf16 a[28:31], v[172:175], v[156:159], a[28:31]\n"
    "v_exp_f32 v37, v37\n"
    "v_mfma_f32_16x16x32_bf16 a[24:27], v[172:175], v[152:155], a[24:27]\n"
    "v_exp_f32 v38, v38\n"
    "v_mfma_f32_16x16x32_bf16 a[24:27], v[168:171], v[136:139], a[24:27]\n"
    "v_exp_f32 v39, v39\n"
    "v_mfma_f32_16x16x32_bf16 a[20:23], v[168:171], v[132:135], a[20:23]\n"
    "ds_read_b128 v[164:167], v201 offset:31744\n"
    "v_add_f32 v114, v114, v67\n"
    "v_mfma_f32_16x16x32_bf16 a[20:23], v[172:175], v[148:151], a[20:23]\n"
    "v_exp_f32 v40, v40\n"
    "v_mfma_f32_16x16x32_bf16 a[16:19], v[172:175], v[144:147], a[16:19]\n"
    "v_exp_f32 v41, v41\n"
    "v_mfma_f32_16x16x32_bf16 a[16:19], v[168:171], v[128:131], a[16:19]\n"
    "s_waitcnt lgkmcnt(2)\n"
    "v_exp_f32 v42, v42\n"
    "v_mfma_f32_16x16x32_bf16 a[32:35], v[176:179], v[128:131], a[32:35]\n"
    "ds_read_b128 v[168:171], v201 offset:32768\n"
    "v_add_f32 v117, v117, v68\n"
    "v_mfma_f32_16x16x32_bf16 a[32:35], v[180:183], v[144:147], a[32:35]\n"
    "v_exp_f32 v43, v43\n"
    "v_mfma_f32_16x16x32_bf16 a[36:39], v[180:183], v[148:151], a[36:39]\n"
    "v_exp_f32 v44, v44\n"
    "v_mfma_f32_16x16x32_bf16 a[36:39], v[176:179], v[132:135], a[36:39]\n"
    "v_exp_f32 v45, v45\n"
    "v_mfma_f32_16x16x32_bf16 a[40:43], v[176:179], v[136:139], a[40:43]\n"
    "ds_read_b128 v[172:175], v201 offset:33792\n"
    "v_add_f32 v118, v118, v69\n"
    "v_mfma_f32_16x16x32_bf16 a[40:43], v[180:183], v[152:155], a[40:43]\n"
    "v_exp_f32 v46, v46\n"
    "v_mfma_f32_16x16x32_bf16 a[44:47], v[180:183], v[156:159], a[44:47]\n"
    "v_exp_f32 v47, v47\n"
    "v_mfma_f32_16x16x32_bf16 a[44:47], v[176:179], v[140:143], a[44:47]\n"
    "s_waitcnt lgkmcnt(2)\n"
    "v_exp_f32 v48, v48\n"
    "v_mfma_f32_16x16x32_bf16 a[60:63], v[160:163], v[140:143], a[60:63]\n"
    "ds_read_b128 v[176:179], v201 offset:34816\n"
    "v_add_f32 v117, v117, v70\n"
    "v_mfma_f32_16x16x32_bf16 a[60:63], v[164:167], v[156:159], a[60:63]\n"
    "v_exp_f32 v49, v49\n"
    "v_mfma_f32_16x16x32_bf16 a[56:59], v[164:167], v[152:155], a[56:59]\n"
    "v_exp_f32 v50, v50\n"
    "v_mfma_f32_16x16x32_bf16 a[56:59], v[160:163], v[136:139], a[56:59]\n"
    "v_exp_f32 v51, v51\n"
    "v_mfma_f32_16x16x32_bf16 a[52:55], v[160:163], v[132:135], a[52:55]\n"
    "ds_read_b128 v[180:183], v201 offset:35840\n"
    "v_add_f32 v113, v113, v32\n"
    "v_mfma_f32_16x16x32_bf16 a[52:55], v[164:167], v[148:151], a[52:55]\n"
    "v_exp_f32 v52, v52\n"
    "v_mfma_f32_16x16x32_bf16 a[48:51], v[164:167], v[144:147], a[48:51]\n"
    "v_add_f32 v118, v118, v71\n"
    "v_add_f32 v114, v114, v33\n"
    "v_mfma_f32_16x16x32_bf16 a[48:51], v[160:163], v[128:131], a[48:51]\n"
    "s_waitcnt lgkmcnt(2)\n"
    "v_add_f32 v121, v121, v72\n"
    "v_add_f32 v113, v113, v34\n"
    "v_mfma_f32_16x16x32_bf16 a[64:67], v[168:171], v[128:131], a[64:67]\n"
    "ds_read_b128 v[160:163], v201 offset:36864\n"
    "v_add_f32 v122, v122, v73\n"
    "v_mfma_f32_16x16x32_bf16 a[64:67], v[172:175], v[144:147], a[64:67]\n"
    "v_exp_f32 v53, v53\n"
    "v_mfma_f32_16x16x32_bf16 a[68:71], v[172:175], v[148:151], a[68:71]\n"
    "v_add_f32 v114, v114, v35\n"
    "v_add_f32 v121, v121, v74\n"
    "v_mfma_f32_16x16x32_bf16 a[68:71], v[168:171], v[132:135], a[68:71]\n"
    "s_add_u32 m0, s51, s95\n"
    "v_add_f32 v117, v117, v36\n"
    "v_add_f32 v122, v122, v75\n"
    "v_mfma_f32_16x16x32_bf16 a[72:75], v[168:171], v[136:139], a[72:75]\n"
    "ds_read_b128 v[164:167], v201 offset:37888\n"
    "v_add_f32 v118, v118, v37\n"
    "v_mfma_f32_16x16x32_bf16 a[72:75], v[172:175], v[152:155], a[72:75]\n"
    "buffer_load_dwordx4 v202, s[32:35], s64 offen lds\n"
    "v_mfma_f32_16x16x32_bf16 a[76:79], v[172:175], v[156:159], a[76:79]\n"
    "v_exp_f32 v54, v54\n"
    "v_mfma_f32_16x16x32_bf16 a[76:79], v[168:171], v[140:143], a[76:79]\n"
    "s_waitcnt lgkmcnt(2)\n"
    "v_add_f32 v125, v125, v76\n"
    "v_add_f32 v117, v117, v38\n"
    "v_mfma_f32_16x16x32_bf16 a[92:95], v[176:179], v[140:143], a[92:95]\n"
    "ds_read_b128 v[168:171], v201 offset:38912\n"
    "v_add_f32 v126, v126, v77\n"
    "v_mfma_f32_16x16x32_bf16 a[92:95], v[180:183], v[156:159], a[92:95]\n"
    "buffer_load_dwordx4 v203, s[32:35], s64 offen offset:1024 lds\n"
    "v_mfma_f32_16x16x32_bf16 a[88:91], v[180:183], v[152:155], a[88:91]\n"
    "v_exp_f32 v55, v55\n"
    "v_mfma_f32_16x16x32_bf16 a[88:91], v[176:179], v[136:139], a[88:91]\n"
    "v_add_f32 v118, v118, v39\n"
    "v_add_f32 v125, v125, v78\n"
    "v_mfma_f32_16x16x32_bf16 a[84:87], v[176:179], v[132:135], a[84:87]\n"
    "ds_read_b128 v[172:175], v201 offset:39936\n"
    "v_add_f32 v121, v121, v40\n"
    "v_mfma_f32_16x16x32_bf16 a[84:87], v[180:183], v[148:151], a[84:87]\n"
    "buffer_load_dwordx4 v204, s[32:35], s64 offen offset:2048 lds\n"
    "v_mfma_f32_16x16x32_bf16 a[80:83], v[180:183], v[144:147], a[80:83]\n"
    "v_exp_f32 v56, v56\n"
    "v_mfma_f32_16x16x32_bf16 a[80:83], v[176:179], v[128:131], a[80:83]\n"
    "s_waitcnt lgkmcnt(2)\n"
    "v_add_f32 v126, v126, v79\n"
    "v_add_f32 v122, v122, v41\n"
    "v_mfma_f32_16x16x32_bf16 a[96:99], v[160:163], v[128:131], a[96:99]\n"
    "s_mov_b32 s95, s94\n"
    "ds_read_b128 v[176:179], v201 offset:40960\n"
    "v_add_f32 v113, v113, v80\n"
    "v_mfma_f32_16x16x32_bf16 a[96:99], v[164:167], v[144:147], a[96:99]\n"
    "s_sub_u32 s94, s94, 0x3000\n"
    "v_add_f32 v121, v121, v42\n"
    "v_add_f32 v114, v114, v81\n"
    "v_mfma_f32_16x16x32_bf16 a[100:103], v[164:167], v[148:151], a[100:103]\n"
    "s_min_u32 s94, s94, 0x6000\n"
    "v_exp_f32 v57, v57\n"
    "v_mfma_f32_16x16x32_bf16 a[100:103], v[160:163], v[132:135], a[100:103]\n"
    "s_sub_u32 s93, s94, s95\n"
    "v_add_f32 v122, v122, v43\n"
    "v_add_f32 v113, v113, v82\n"
    "v_mfma_f32_16x16x32_bf16 a[104:107], v[160:163], v[136:139], a[104:107]\n"
    "ds_read_b128 v[180:183], v201 offset:41984\n"
    "v_add_f32 v125, v125, v44\n"
    "v_mfma_f32_16x16x32_bf16 a[104:107], v[164:167], v[152:155], a[104:107]\n"
    "v_exp_f32 v58, v58\n"
    "v_mfma_f32_16x16x32_bf16 a[108:111], v[164:167], v[156:159], a[108:111]\n"
    "v_add_f32 v114, v114, v83\n"
    "v_add_f32 v126, v126, v45\n"
    "v_mfma_f32_16x16x32_bf16 a[108:111], v[160:163], v[140:143], a[108:111]\n"
    "s_waitcnt lgkmcnt(2)\n"
    "v_add_f32 v117, v117, v84\n"
    "v_add_f32 v125, v125, v46\n"
    "v_mfma_f32_16x16x32_bf16 a[124:127], v[168:171], v[140:143], a[124:127]\n"
    "ds_read_b128 v[160:163], v201 offset:43008\n"
    "v_add_f32 v118, v118, v85\n"
    "v_mfma_f32_16x16x32_bf16 a[124:127], v[172:175], v[156:159], a[124:127]\n"
    "v_exp_f32 v59, v59\n"
    "v_mfma_f32_16x16x32_bf16 a[120:123], v[172:175], v[152:155], a[120:123]\n"
    "v_add_f32 v126, v126, v47\n"
    "v_add_f32 v117, v117, v86\n"
    "v_mfma_f32_16x16x32_bf16 a[120:123], v[168:171], v[136:139], a[120:123]\n"
    "v_add_f32 v113, v113, v48\n"
    "v_add_u32 v205, s93, v205\n"
    "v_mfma_f32_16x16x32_bf16 a[116:119], v[168:171], v[132:135], a[116:119]\n"
    "ds_read_b128 v[164:167], v201 offset:44032\n"
    "v_add_f32 v118, v118, v87\n"
    "v_mfma_f32_16x16x32_bf16 a[116:119], v[172:175], v[148:151], a[116:119]\n"
    "v_exp_f32 v60, v60\n"
    "v_mfma_f32_16x16x32_bf16 a[112:115], v[172:175], v[144:147], a[112:115]\n"
    "v_add_f32 v114, v114, v49\n"
    "v_add_u32 v206, s93, v206\n"
    "v_mfma_f32_16x16x32_bf16 a[112:115], v[168:171], v[128:131], a[112:115]\n"
    "s_waitcnt lgkmcnt(2)\n"
    "v_add_f32 v121, v121, v88\n"
    "v_add_f32 v113, v113, v50\n"
    "v_mfma_f32_16x16x32_bf16 a[128:131], v[176:179], v[128:131], a[128:131]\n"
    "ds_read_b128 v[168:171], v201 offset:45056\n"
    "v_add_u32 v207, s93, v207\n"
    "v_mfma_f32_16x16x32_bf16 a[128:131], v[180:183], v[144:147], a[128:131]\n"
    "v_exp_f32 v61, v61\n"
    "v_mfma_f32_16x16x32_bf16 a[132:135], v[180:183], v[148:151], a[132:135]\n"
    "s_add_u32 s64, s64, 0xc000\n"
    "v_add_f32 v122, v122, v89\n"
    "v_add_f32 v114, v114, v51\n"
    "v_mfma_f32_16x16x32_bf16 a[132:135], v[176:179], v[132:135], a[132:135]\n"
    "s_add_u32 s62, s62, 0x6000\n"
    "v_add_f32 v121, v121, v90\n"
    "v_add_f32 v117, v117, v52\n"
    "v_mfma_f32_16x16x32_bf16 a[136:139], v[176:179], v[136:139], a[136:139]\n"
    "s_add_u32 s63, s63, 0x6000\n"
    "ds_read_b128 v[172:175], v201 offset:46080\n"
    "v_add_f32 v122, v122, v91\n"
    "v_mfma_f32_16x16x32_bf16 a[136:139], v[180:183], v[152:155], a[136:139]\n"
    "s_add_u32 s60, s60, 0x6000\n"
    "v_exp_f32 v62, v62\n"
    "v_mfma_f32_16x16x32_bf16 a[140:143], v[180:183], v[156:159], a[140:143]\n"
    "s_add_u32 s61, s61, 0x6000\n"
    "v_add_f32 v118, v118, v53\n"
    "v_add_f32 v125, v125, v92\n"
    "v_mfma_f32_16x16x32_bf16 a[140:143], v[176:179], v[140:143], a[140:143]\n"
    "s_waitcnt lgkmcnt(2)\n"
    "v_add_f32 v117, v117, v54\n"
    "v_add_f32 v126, v126, v93\n"
    "v_mfma_f32_16x16x32_bf16 a[156:159], v[160:163], v[140:143], a[156:159]\n"
    "ds_read_b128 v[176:179], v201 offset:47104\n"
    "v_add_f32 v118, v118, v55\n"
    "v_mfma_f32_16x16x32_bf16 a[156:159], v[164:167], v[156:159], a[156:159]\n"
    "s_waitcnt vmcnt(3)\n"
    "ds_read_b128 v[180:183], v201 offset:48128\n"
    "v_add_f32 v125, v125, v94\n"
    "v_mfma_f32_16x16x32_bf16 a[152:155], v[164:167], v[152:155], a[152:155]\n"
    "v_exp_f32 v63, v63\n"
    "v_mfma_f32_16x16x32_bf16 a[152:155], v[160:163], v[136:139], a[152:155]\n"
    "v_add_f32 v121, v121, v56\n"
    "v_add_f32 v126, v126, v95\n"
    "v_mfma_f32_16x16x32_bf16 a[148:151], v[160:163], v[132:135], a[148:151]\n"
    "v_add_f32 v122, v122, v57\n"
    "v_mfma_f32_16x16x32_bf16 a[148:151], v[164:167], v[148:151], a[148:151]\n"
    "v_mfma_f32_16x16x32_bf16 a[144:147], v[164:167], v[144:147], a[144:147]\n"
    "v_mfma_f32_16x16x32_bf16 a[144:147], v[160:163], v[128:131], a[144:147]\n"
    "s_waitcnt lgkmcnt(0)\n"
    "v_mfma_f32_16x16x32_bf16 a[160:163], v[168:171], v[128:131], a[160:163]\n"
    "s_barrier\n"
    "LMID1:\n"
    "v_mfma_f32_16x16x32_bf16 a[160:163], v[172:175], v[144:147], a[160:163]\n"
    "ds_read_u16_d16_hi v223, v209 offset:6336\n"
    "ds_read_u16_d16_hi v222, v209 offset:192\n"
    "v_mfma_f32_16x16x32_bf16 a[164:167], v[172:175], v[148:151], a[164:167]\n"
    "ds_read_b128 v[160:163], v200 offset:24576\n"
    "v_add_f32 v121, v121, v58\n"
    "v_mfma_f32_16x16x32_bf16 a[164:167], v[168:171], v[132:135], a[164:167]\n"
    "ds_read_b128 v[232:235], v205 offset:24576\n"
    "v_add_f32 v122, v122, v59\n"
    "v_mfma_f32_16x16x32_bf16 a[168:171], v[168:171], v[136:139], a[168:171]\n"
    "ds_read_b128 v[164:167], v200 offset:25600\n"
    "ds_read_b128 v[236:239], v206 offset:24576\n"
    "v_mfma_f32_16x16x32_bf16 a[168:171], v[172:175], v[152:155], a[168:171]\n"
    "v_add_f32 v125, v125, v60\n"
    "v_add_f32 v126, v126, v61\n"
    "v_mfma_f32_16x16x32_bf16 a[172:175], v[172:175], v[156:159], a[172:175]\n"
    "v_add_f32 v125, v125, v62\n"
    "v_add_f32 v126, v126, v63\n"
    "v_mfma_f32_16x16x32_bf16 a[172:175], v[168:171], v[140:143], a[172:175]\n"
    "ds_read_b64 v[228:229], v210 offset:64\n"
    "ds_read2st64_b32 v[240:241], v207 offset0:96 offset1:99\n"
    "v_mfma_f32_16x16x32_bf16 a[188:191], v[176:179], v[140:143], a[188:191]\n"
    "ds_read_b128 v[168:171], v200 offset:26624\n"
    "ds_read2st64_b32 v[242:243], v207 offset0:102 offset1:105\n"
    "v_mfma_f32_16x16x32_bf16 a[188:191], v[180:183], v[156:159], a[188:191]\n"
    "ds_read2st64_b32 v[244:245], v207 offset0:108 offset1:111\n"
    "ds_read2st64_b32 v[246:247], v207 offset0:114 offset1:117\n"
    "v_mfma_f32_16x16x32_bf16 a[184:187], v[176:179], v[136:139], a[184:187]\n"
    "v_cvt_pk_bf16_f32 v140, v44, v45\n"
    "v_cvt_pk_bf16_f32 v141, v46, v47\n"
    "v_mfma_f32_16x16x32_bf16 a[184:187], v[180:183], v[152:155], a[184:187]\n"
    "s_waitcnt lgkmcnt(6)\n"
    "v_mul_f32 v226, s75, v223\n"
    "v_mfma_f32_16x16x32_bf16 a[180:183], v[176:179], v[132:135], a[180:183]\n"
    "s_add_u32 s59, s59, 1\n"
    "ds_read_b128 v[172:175], v200 offset:27648\n"
    "v_fma_f32 v227, -v222, v226, v217\n"
    "v_mfma_f32_16x16x32_bf16 a[180:183], v[180:183], v[148:151], a[180:183]\n"
    "v_cvt_f32_i32 v232, v232\n"
    "v_cvt_f32_i32 v233, v233\n"
    "v_mfma_f32_16x16x32_bf16 a[176:179], v[176:179], v[128:131], a[176:179]\n"
    "s_cmp_lt_u32 s59, 15\n"
    "v_cvt_f32_i32 v234, v234\n"
    "v_cvt_f32_i32 v235, v235\n"
    "v_mfma_f32_16x16x32_bf16 a[176:179], v[180:183], v[144:147], a[176:179]\n"
    "s_cbranch_scc1 LLOOP1\n"
    "s_branch LTAIL\n"
    "LENT0:\n"
    "s_mov_b32 s59, 1\n"
    "s_add_u32 m0, s72, 0x21000\n"
    "s_nop 0\n"
    "buffer_load_dwordx4 v250, s[48:51], 0 offen offset:2048 lds\n"
    "s_add_u32 m0, s72, 0x18000\n"
    "s_nop 0\n"
    "buffer_load_dwordx4 v202, s[32:35], s64 offen lds\n"
    "buffer_load_dwordx4 v203, s[32:35], s64 offen offset:1024 lds\n"
    "buffer_load_dwordx4 v204, s[32:35], s64 offen offset:2048 lds\n"
    "s_add_u32 s64, s64, 0xc000\n"
    "s_mov_b32 s94, 0x3000\n"
    "s_mov_b32 s95, 0x6000\n"
    "s_add_u32 s51, s72, 0x18000\n"
    "v_add_u32 v205, 0x3000, v205\n"
    "v_add_u32 v206, 0x3000, v206\n"
    "v_add_u32 v207, 0x3000, v207\n"
    "buffer_store_dwordx2 v[220:221], v214, s[44:47], 0 offen sc1\n"
    "ds_read_b128 v[160:163], v200 offset:24576\n"
    "ds_read_u16_d16_hi v223, v209 offset:6336\n"
    "ds_read_u16_d16_hi v222, v209 offset:192\n"
    "v_add_f32 v121, v121, v58\n"
    "ds_read_b128 v[164:167], v200 offset:25600\n"
    "ds_read_b128 v[232:235], v205 offset:24576\n"
    "v_add_f32 v122, v122, v59\n"
    "ds_read_b128 v[236:239], v206 offset:24576\n"
    "v_add_f32 v125, v125, v60\n"
    "v_add_f32 v126, v126, v61\n"
    "v_add_f32 v125, v125, v62\n"
    "v_add_f32 v126, v126, v63\n"
    "ds_read_b128 v[168:171], v200 offset:26624\n"
    "ds_read_b64 v[228:229], v210 offset:64\n"
    "ds_read2st64_b32 v[240:241], v207 offset0:96 offset1:99\n"
    "ds_read2st64_b32 v[242:243], v207 offset0:102 offset1:105\n"
    "ds_read2st64_b32 v[244:245], v207 offset0:108 offset1:111\n"
    "ds_read2st64_b32 v[246:247], v207 offset0:114 offset1:117\n"
    "v_cvt_pk_bf16_f32 v140, v44, v45\n"
    "v_cvt_pk_bf16_f32 v141, v46, v47\n"
    "ds_read_b128 v[172:175], v200 offset:27648\n"
    "s_waitcnt lgkmcnt(7)\n"
    "v_mul_f32 v226, s75, v223\n"
    "v_fma_f32 v227, -v222, v226, v217\n"
    "v_cvt_f32_i32 v232, v232\n"
    "v_cvt_f32_i32 v233, v233\n"
    "s_nop 7\n"
    ".p2align 6\n"
    "LLOOP:\n"
    "v_mfma_f32_16x16x32_bf16 v[64:67], v[160:163], a[192:195], v[96:99]\n"
    "ds_read_b128 v[176:179], v200 offset:28672\n"
    "v_cvt_f32_i32 v234, v234\n"
    "v_mfma_f32_16x16x32_bf16 v[64:67], v[164:167], a[196:199], v[64:67]\n"
    "v_cvt_f32_i32 v235, v235\n"
    "v_cvt_f32_i32 v236, v236\n"
    "v_mfma_f32_16x16x32_bf16 v[68:71], v[164:167], a[220:223], v[100:103]\n"
    "v_cvt_f32_i32 v237, v237\n"
    "v_cvt_f32_i32 v238, v238\n"
    "v_mfma_f32_16x16x32_bf16 v[68:71], v[160:163], a[216:219], v[68:71]\n"
    "v_cvt_f32_i32 v239, v239\n"
    "v_mul_f32_dpp v232, v226, v232 row_newbcast:0 row_mask:0xf bank_mask:0xf\n"
    "v_mfma_f32_16x16x32_bf16 v[72:75], v[160:163], a[240:243], v[104:107]\n"
    "ds_read_b128 v[180:183], v200 offset:29696\n"
    "v_mul_f32_dpp v233, v226, v233 row_newbcast:1 row_mask:0xf bank_mask:0xf\n"
    "v_mfma_f32_16x16x32_bf16 v[72:75], v[164:167], a[244:247], v[72:75]\n"
    "v_mul_f32_dpp v234, v226, v234 row_newbcast:2 row_mask:0xf bank_mask:0xf\n"
    "v_mul_f32_dpp v235, v226, v235 row_newbcast:3 row_mask:0xf bank_mask:0xf\n"
    "v_mfma_f32_16x16x32_bf16 v[76:79], v[164:167], v[12:15], v[108:111]\n"
    "v_mul_f32_dpp v236, v226, v236 row_newbcast:4 row_mask:0xf bank_mask:0xf\n"
    "v_mul_f32_dpp v237, v226, v237 row_newbcast:5 row_mask:0xf bank_mask:0xf\n"
    "v_mfma_f32_16x16x32_bf16 v[76:79], v[160:163], v[8:11], v[76:79]\n"
    "s_waitcnt lgkmcnt(2)\n"
    "v_mul_f32_dpp v238, v226, v238 row_newbcast:6 row_mask:0xf bank_mask:0xf\n"
    "v_mul_f32_dpp v239, v226, v239 row_newbcast:7 row_mask:0xf bank_mask:0xf\n"
    "v_mfma_f32_16x16x32_bf16 v[76:79], v[168:171], v[16:19], v[76:79]\n"
    "ds_read_b128 v[160:163], v200 offset:34816\n"
    "v_add_f32_dpp v232, v227, v232 row_newbcast:0 row_mask:0xf bank_mask:0xf\n"
    "v_mfma_f32_16x16x32_bf16 v[76:79], v[172:175], v[20:23], v[76:79]\n"
    "v_add_f32_dpp v233, v227, v233 row_newbcast:1 row_mask:0xf bank_mask:0xf\n"
    "v_add_f32_dpp v234, v227, v234 row_newbcast:2 row_mask:0xf bank_mask:0xf\n"
    "v_mfma_f32_16x16x32_bf16 v[72:75], v[172:175], a[252:255], v[72:75]\n"
    "v_add_f32_dpp v235, v227, v235 row_newbcast:3 row_mask:0xf bank_mask:0xf\n"
    "v_add_f32_dpp v236, v227, v236 row_newbcast:4 row_mask:0xf bank_mask:0xf\n"
    "v_mfma_f32_16x16x32_bf16 v[72:75], v[168:171], a[248:251], v[72:75]\n"
    "v_add_f32_dpp v237, v227, v237 row_newbcast:5 row_mask:0xf bank_mask:0xf\n"
    "v_add_f32_dpp v238, v227, v238 row_newbcast:6 row_mask:0xf bank_mask:0xf\n"
    "v_mfma_f32_16x16x32_bf16 v[68:71], v[168:171], a[224:227], v[68:71]\n"
    "ds_read_b128 v[164:167], v200 offset:35840\n"
    "v_add_f32_dpp v239, v227, v239 row_newbcast:7 row_mask:0xf bank_mask:0xf\n"
    "v_mfma_f32_16x16x32_bf16 v[68:71], v[172:175], a[228:231], v[68:71]\n"
    "v_cvt_pk_bf16_f32 v232, v232, v233\n"
    "v_cvt_f32_i32 v240, v240\n"
    "v_mfma_f32_16x16x32_bf16 v[64:67], v[172:175], a[204:207], v[64:67]\n"
    "v_cvt_pk_bf16_f32 v233, v234, v235\n"
    "v_cvt_f32_i32 v241, v241\n"
    "v_mfma_f32_16x16x32_bf16 v[64:67], v[168:171], a[200:203], v[64:67]\n"
    "s_waitcnt lgkmcnt(2)\n"
    "v_cvt_pk_bf16_f32 v234, v236, v237\n"
    "v_cvt_f32_i32 v242, v242\n"
    "v_mfma_f32_16x16x32_bf16 v[64:67], v[176:179], a[208:211], v[64:67]\n"
    "ds_read_b128 v[168:171], v200 offset:32768\n"
    "v_cvt_pk_bf16_f32 v235, v238, v239\n"
    "v_mfma_f32_16x16x32_bf16 v[64:67], v[180:183], a[212:215], v[64:67]\n"
    "v_cvt_f32_i32 v243, v243\n"
    "v_cvt_f32_i32 v244, v244\n"
    "v_mfma_f32_16x16x32_bf16 v[68:71], v[180:183], a[236:239], v[68:71]\n"
    "v_cvt_f32_i32 v245, v245\n"
    "v_cvt_f32_i32 v246, v246\n"
    "v_mfma_f32_16x16x32_bf16 v[68:71], v[176:179], a[232:235], v[68:71]\n"
    "v_cvt_f32_i32 v247, v247\n"
    "v_mul_f32_dpp v240, v228, v240 row_newbcast:0 row_mask:0xf bank_mask:0xf\n"
    "v_mfma_f32_16x16x32_bf16 v[72:75], v[176:179], v[0:3], v[72:75]\n"
    "ds_read_b128 v[172:175], v200 offset:33792\n"
    "v_mul_f32_dpp v241, v228, v241 row_newbcast:1 row_mask:0xf bank_mask:0xf\n"
    "v_mfma_f32_16x16x32_bf16 v[72:75], v[180:183], v[4:7], v[72:75]\n"
    "v_mul_f32_dpp v242, v228, v242 row_newbcast:2 row_mask:0xf bank_mask:0xf\n"
    "v_mul_f32_dpp v243, v228, v243 row_newbcast:3 row_mask:0xf bank_mask:0xf\n"
    "v_mfma_f32_16x16x32_bf16 v[76:79], v[180:183], v[28:31], v[76:79]\n"
    "v_mul_f32_dpp v244, v228, v244 row_newbcast:4 row_mask:0xf bank_mask:0xf\n"
    "v_mul_f32_dpp v245, v228, v245 row_newbcast:5 row_mask:0xf bank_mask:0xf\n"
    "v_mfma_f32_16x16x32_bf16 v[76:79], v[176:179], v[24:27], v[76:79]\n"
    "s_waitcnt lgkmcnt(2)\n"
    "v_mul_f32_dpp v246, v228, v246 row_newbcast:6 row_mask:0xf bank_mask:0xf\n"
    "v_mul_f32_dpp v247, v228, v247 row_newbcast:7 row_mask:0xf bank_mask:0xf\n"
    "v_mfma_f32_16x16x32_bf16 v[92:95], v[160:163], v[24:27], v[108:111]\n"
    "ds_read_b128 v[176:179], v200 offset:30720\n"
    "v_add_f32_dpp v240, v229, v240 row_newbcast:0 row_mask:0xf bank_mask:0xf\n"
    "v_mfma_f32_16x16x32_bf16 v[92:95], v[164:167], v[28:31], v[92:95]\n"
    "buffer_store_dwordx4 v[232:235], v211, s[36:39], s62 offen sc1\n"
    "v_add_f32_dpp v241, v229, v241 row_newbcast:1 row_mask:0xf bank_mask:0xf\n"
    "v_add_f32_dpp v242, v229, v242 row_newbcast:2 row_mask:0xf bank_mask:0xf\n"
    "v_add_f32_dpp v243, v229, v243 row_newbcast:3 row_mask:0xf bank_mask:0xf\n"
    "v_mfma_f32_16x16x32_bf16 v[88:91], v[164:167], v[4:7], v[104:107]\n"
    "v_add_f32_dpp v244, v229, v244 row_newbcast:4 row_mask:0xf bank_mask:0xf\n"
    "v_add_f32_dpp v245, v229, v245 row_newbcast:5 row_mask:0xf bank_mask:0xf\n"
    "v_mfma_f32_16x16x32_bf16 v[88:91], v[160:163], v[0:3], v[88:91]\n"
    "v_add_f32_dpp v246, v229, v246 row_newbcast:6 row_mask:0xf bank_mask:0xf\n"
    "v_add_f32_dpp v247, v229, v247 row_newbcast:7 row_mask:0xf bank_mask:0xf\n"
    "v_mfma_f32_16x16x32_bf16 v[84:87], v[160:163], a[232:235], v[100:103]\n"
    "ds_read_b128 v[180:183], v200 offset:31744\n"
    "v_cvt_pk_bf16_f32 v240, v240, v241\n"
    "v_mfma_f32_16x16x32_bf16 v[84:87], v[164:167], a[236:239], v[84:87]\n"
    "v_cvt_pk_bf16_f32 v241, v242, v243\n"
    "v_cvt_pk_bf16_f32 v242, v244, v245\n"
    "v_mfma_f32_16x16x32_bf16 v[80:83], v[164:167], a[212:215], v[96:99]\n"
    "v_cvt_pk_bf16_f32 v243, v246, v247\n"
    "v_cvt_pk_bf16_f32 v136, v40, v41\n"
    "v_mfma_f32_16x16x32_bf16 v[80:83], v[160:163], a[208:211], v[80:83]\n"
    "s_waitcnt lgkmcnt(2)\n"
    "v_cvt_pk_bf16_f32 v137, v42, v43\n"
    "v_cvt_pk_bf16_f32 v132, v36, v37\n"
    "v_mfma_f32_16x16x32_bf16 v[80:83], v[168:171], a[200:203], v[80:83]\n"
    "ds_read_b128 v[160:163], v200 offset:36864\n"
    "v_cvt_pk_bf16_f32 v133, v38, v39\n"
    "v_mfma_f32_16x16x32_bf16 v[80:83], v[172:175], a[204:207], v[80:83]\n"
    "buffer_store_dwordx4 v[240:243], v212, s[40:43], s63 offen sc1\n"
    "v_cvt_pk_bf16_f32 v128, v32, v33\n"
    "v_cvt_pk_bf16_f32 v129, v34, v35\n"
    "v_cvt_pk_bf16_f32 v142, v60, v61\n"
    "v_mfma_f32_16x16x32_bf16 v[84:87], v[172:175], a[228:231], v[84:87]\n"
    "v_exp_f32 v64, v64\n"
    "v_mfma_f32_16x16x32_bf16 v[84:87], v[168:171], a[224:227], v[84:87]\n"
    "v_exp_f32 v65, v65\n"
    "v_mfma_f32_16x16x32_bf16 v[88:91], v[168:171], a[248:251], v[88:91]\n"
    "ds_read_b128 v[164:167], v200 offset:37888\n"
    "v_cvt_pk_bf16_f32 v143, v62, v63\n"
    "v_mfma_f32_16x16x32_bf16 v[88:91], v[172:175], a[252:255], v[88:91]\n"
    "v_exp_f32 v66, v66\n"
    "v_mfma_f32_16x16x32_bf16 v[92:95], v[172:175], v[20:23], v[92:95]\n"
    "v_exp_f32 v67, v67\n"
    "v_mfma_f32_16x16x32_bf16 v[92:95], v[168:171], v[16:19], v[92:95]\n"
    "s_waitcnt lgkmcnt(2)\n"
    "v_exp_f32 v68, v68\n"
    "v_mfma_f32_16x16x32_bf16 v[92:95], v[176:179], v[8:11], v[92:95]\n"
    "ds_read_b128 v[168:171], v200 offset:38912\n"
    "v_cvt_pk_bf16_f32 v138, v56, v57\n"
    "v_mfma_f32_16x16x32_bf16 v[92:95], v[180:183], v[12:15], v[92:95]\n"
    "v_exp_f32 v69, v69\n"
    "v_mfma_f32_16x16x32_bf16 v[88:91], v[180:183], a[244:247], v[88:91]\n"
    "v_exp_f32 v70, v70\n"
    "v_mfma_f32_16x16x32_bf16 v[88:91], v[176:179], a[240:243], v[88:91]\n"
    "v_exp_f32 v71, v71\n"
    "v_mfma_f32_16x16x32_bf16 v[84:87], v[176:179], a[216:219], v[84:87]\n"
    "ds_read_b128 v[172:175], v200 offset:39936\n"
    "v_cvt_pk_bf16_f32 v139, v58, v59\n"
    "v_mfma_f32_16x16x32_bf16 v[84:87], v[180:183], a[220:223], v[84:87]\n"
    "v_exp_f32 v72, v72\n"
    "v_mfma_f32_16x16x32_bf16 v[80:83], v[180:183], a[196:199], v[80:83]\n"
    "v_exp_f32 v73, v73\n"
    "v_mfma_f32_16x16x32_bf16 v[80:83], v[176:179], a[192:195], v[80:83]\n"
    "s_waitcnt lgkmcnt(2)\n"
    "v_exp_f32 v74, v74\n"
    "v_mfma_f32_16x16x32_bf16 v[32:35], v[160:163], a[192:195], v[96:99]\n"
    "ds_read_b128 v[176:179], v200 offset:40960\n"
    "v_cvt_pk_bf16_f32 v134, v52, v53\n"
    "v_mfma_f32_16x16x32_bf16 v[32:35], v[164:167], a[196:199], v[32:35]\n"
    "v_exp_f32 v75, v75\n"
    "v_mfma_f32_16x16x32_bf16 v[36:39], v[164:167], a[220:223], v[100:103]\n"
    "v_exp_f32 v76, v76\n"
    "v_mfma_f32_16x16x32_bf16 v[36:39], v[160:163], a[216:219], v[36:39]\n"
    "v_cvt_pk_bf16_f32 v135, v54, v55\n"
    "v_cvt_pk_bf16_f32 v130, v48, v49\n"
    "v_mfma_f32_16x16x32_bf16 v[40:43], v[160:163], a[240:243], v[104:107]\n"
    "ds_read_b128 v[180:183], v200 offset:41984\n"
    "v_cvt_pk_bf16_f32 v131, v50, v51\n"
    "v_mfma_f32_16x16x32_bf16 v[40:43], v[164:167], a[244:247], v[40:43]\n"
    "v_exp_f32 v77, v77\n"
    "v_mfma_f32_16x16x32_bf16 v[44:47], v[164:167], v[12:15], v[108:111]\n"
    "v_exp_f32 v78, v78\n"
    "v_mfma_f32_16x16x32_bf16 v[44:47], v[160:163], v[8:11], v[44:47]\n"
    "s_waitcnt lgkmcnt(2)\n"
    "v_exp_f32 v79, v79\n"
    "v_mfma_f32_16x16x32_bf16 v[44:47], v[168:171], v[16:19], v[44:47]\n"
    "ds_read_b128 v[160:163], v200 offset:47104\n"
    "v_cvt_pk_bf16_f32 v144, v64, v65\n"
    "v_mfma_f32_16x16x32_bf16 v[44:47], v[172:175], v[20:23], v[44:47]\n"
    "v_exp_f32 v80, v80\n"
    "v_mfma_f32_16x16x32_bf16 v[40:43], v[172:175], a[252:255], v[40:43]\n"
    "v_exp_f32 v81, v81\n"
    "v_mfma_f32_16x16x32_bf16 v[40:43], v[168:171], a[248:251], v[40:43]\n"
    "v_exp_f32 v82, v82\n"
    "v_mfma_f32_16x16x32_bf16 v[36:39], v[168:171], a[224:227], v[36:39]\n"
    "ds_read_b128 v[164:167], v200 offset:48128\n"
    "v_cvt_pk_bf16_f32 v145, v66, v67\n"
    "v_mfma_f32_16x16x32_bf16 v[36:39], v[172:175], a[228:231], v[36:39]\n"
    "v_exp_f32 v83, v83\n"
    "v_mfma_f32_16x16x32_bf16 v[32:35], v[172:175], a[204:207], v[32:35]\n"
    "v_exp_f32 v84, v84\n"
    "v_mfma_f32_16x16x32_bf16 v[32:35], v[168:171], a[200:203], v[32:35]\n"
    "s_waitcnt lgkmcnt(2)\n"
    "v_exp_f32 v85, v85\n"
    "v_mfma_f32_16x16x32_bf16 v[32:35], v[176:179], a[208:211], v[32:35]\n"
    "ds_read_b128 v[168:171], v200 offset:45056\n"
    "v_cvt_pk_bf16_f32 v148, v68, v69\n"
    "v_mfma_f32_16x16x32_bf16 v[32:35], v[180:183], a[212:215], v[32:35]\n"
    "v_exp_f32 v86, v86\n"
    "v_mfma_f32_16x16x32_bf16 v[36:39], v[180:183], a[236:239], v[36:39]\n"
    "v_exp_f32 v87, v87\n"
    "v_mfma_f32_16x16x32_bf16 v[36:39], v[176:179], a[232:235], v[36:39]\n"
    "v_exp_f32 v88, v88\n"
    "v_mfma_f32_16x16x32_bf16 v[40:43], v[176:179], v[0:3], v[40:43]\n"
    "ds_read_b128 v[172:175], v200 offset:46080\n"
    "v_cvt_pk_bf16_f32 v149, v70, v71\n"
    "v_mfma_f32_16x16x32_bf16 v[40:43], v[180:183], v[4:7], v[40:43]\n"
    "v_exp_f32 v89, v89\n"
    "v_mfma_f32_16x16x32_bf16 v[44:47], v[180:183], v[28:31], v[44:47]\n"
    "v_cvt_pk_bf16_f32 v152, v72, v73\n"
    "v_cvt_pk_bf16_f32 v153, v74, v75\n"
    "v_mfma_f32_16x16x32_bf16 v[44:47], v[176:179], v[24:27], v[44:47]\n"
    "s_waitcnt lgkmcnt(2)\n"
    "v_exp_f32 v90, v90\n"
    "v_mfma_f32_16x16x32_bf16 v[60:63], v[160:163], v[24:27], v[108:111]\n"
    "s_mov_b32 m0, 0\n"
    "ds_read_b128 v[176:179], v200 offset:43008\n"
    "v_cvt_pk_bf16_f32 v156, v76, v77\n"
    "v_mfma_f32_16x16x32_bf16 v[60:63], v[164:167], v[28:31], v[60:63]\n"
    "v_cvt_pk_bf16_f32 v157, v78, v79\n"
    "v_cvt_pk_bf16_f32 v146, v80, v81\n"
    "v_mfma_f32_16x16x32_bf16 v[56:59], v[164:167], v[4:7], v[104:107]\n"
    "buffer_load_dwordx4 v200, s[36:39], s60 offen sc1 lds\n"
    "v_mfma_f32_16x16x32_bf16 v[56:59], v[160:163], v[0:3], v[56:59]\n"
    "v_exp_f32 v91, v91\n"
    "v_mfma_f32_16x16x32_bf16 v[52:55], v[160:163], a[232:235], v[100:103]\n"
    "ds_read_b128 v[180:183], v200 offset:44032\n"
    "v_cvt_pk_bf16_f32 v147, v82, v83\n"
    "v_mfma_f32_16x16x32_bf16 v[52:55], v[164:167], a[236:239], v[52:55]\n"
    "v_exp_f32 v92, v92\n"
    "v_mfma_f32_16x16x32_bf16 v[48:51], v[164:167], a[212:215], v[96:99]\n"
    "buffer_load_dwordx4 v200, s[36:39], s60 offen offset:1024 sc1 lds\n"
    "v_mfma_f32_16x16x32_bf16 v[48:51], v[160:163], a[208:211], v[48:51]\n"
    "s_waitcnt lgkmcnt(2)\n"
    "v_cvt_pk_bf16_f32 v150, v84, v85\n"
    "v_cvt_pk_bf16_f32 v151, v86, v87\n"
    "v_mfma_f32_16x16x32_bf16 v[48:51], v[168:171], a[200:203], v[48:51]\n"
    "ds_read_b128 v[160:163], v201\n"
    "v_cvt_pk_bf16_f32 v154, v88, v89\n"
    "v_mfma_f32_16x16x32_bf16 v[48:51], v[172:175], a[204:207], v[48:51]\n"
    "v_exp_f32 v93, v93\n"
    "v_mfma_f32_16x16x32_bf16 v[52:55], v[172:175], a[228:231], v[52:55]\n"
    "buffer_load_dwordx4 v200, s[36:39], s60 offen offset:2048 sc1 lds\n"
    "v_mfma_f32_16x16x32_bf16 v[52:55], v[168:171], a[224:227], v[52:55]\n"
    "v_exp_f32 v94, v94\n"
    "v_mfma_f32_16x16x32_bf16 v[56:59], v[168:171], a[248:251], v[56:59]\n"
    "s_add_u32 m0, s71, 0x12000\n"
    "ds_read_b128 v[164:167], v201 offset:1024\n"
    "v_cvt_pk_bf16_f32 v155, v90, v91\n"
    "v_mfma_f32_16x16x32_bf16 v[56:59], v[172:175], a[252:255], v[56:59]\n"
    "v_exp_f32 v95, v95\n"
    "v_mfma_f32_16x16x32_bf16 v[60:63], v[172:175], v[20:23], v[60:63]\n"
    "buffer_load_dwordx4 v200, s[40:43], s61 offen sc1 lds\n"
    "v_mfma_f32_16x16x32_bf16 v[60:63], v[168:171], v[16:19], v[60:63]\n"
    "s_waitcnt lgkmcnt(2)\n"
    "v_cvt_pk_bf16_f32 v158, v92, v93\n"
    "v_cvt_pk_bf16_f32 v159, v94, v95\n"
    "v_mfma_f32_16x16x32_bf16 v[60:63], v[176:179], v[8:11], v[60:63]\n"
    "ds_read_b128 v[168:171], v201 offset:2048\n"
    "v_add_f32 v113, v113, v64\n"
    "v_mfma_f32_16x16x32_bf16 v[60:63], v[180:183], v[12:15], v[60:63]\n"
    "v_exp_f32 v32, v32\n"
    "v_mfma_f32_16x16x32_bf16 v[56:59], v[180:183], a[244:247], v[56:59]\n"
    "buffer_load_dwordx4 v200, s[40:43], s61 offen offset:1024 sc1 lds\n"
    "v_mfma_f32_16x16x32_bf16 v[56:59], v[176:179], a[240:243], v[56:59]\n"
    "v_exp_f32 v33, v33\n"
    "v_mfma_f32_16x16x32_bf16 v[52:55], v[176:179], a[216:219], v[52:55]\n"
    "ds_read_b128 v[172:175], v201 offset:3072\n"
    "v_add_f32 v114, v114, v65\n"
    "v_mfma_f32_16x16x32_bf16 v[52:55], v[180:183], a[220:223], v[52:55]\n"
    "v_exp_f32 v34, v34\n"
    "v_mfma_f32_16x16x32_bf16 v[48:51], v[180:183], a[196:199], v[48:51]\n"
    "buffer_load_dwordx4 v200, s[40:43], s61 offen offset:2048 sc1 lds\n"
    "v_mfma_f32_16x16x32_bf16 v[48:51], v[176:179], a[192:195], v[48:51]\n"
    "s_waitcnt lgkmcnt(2)\n"
    "v_exp_f32 v35, v35\n"
    "v_mfma_f32_16x16x32_bf16 a[0:3], v[160:163], v[128:131], a[0:3]\n"
    "ds_read_b128 v[176:179], v201 offset:4096\n"
    "v_add_f32 v113, v113, v66\n"
    "v_mfma_f32_16x16x32_bf16 a[0:3], v[164:167], v[144:147], a[0:3]\n"
    "v_exp_f32 v36, v36\n"
    "v_mfma_f32_16x16x32_bf16 a[4:7], v[160:163], v[132:135], a[4:7]\n"
    "buffer_load_dwordx4 v200, s[40:43], s61 offen offset:3072 sc1 lds\n"
    "v_mfma_f32_16x16x32_bf16 a[4:7], v[164:167], v[148:151], a[4:7]\n"
    "s_add_u32 s65, s61, 0x1000\n"
    "v_exp_f32 v37, v37\n"
    "v_mfma_f32_16x16x32_bf16 a[8:11], v[160:163], v[136:139], a[8:11]\n"
    "s_add_u32 m0, s71, 0x13000\n"
    "ds_read_b128 v[180:183], v201 offset:5120\n"
    "v_add_f32 v114, v114, v67\n"
    "v_mfma_f32_16x16x32_bf16 a[8:11], v[164:167], v[152:155], a[8:11]\n"
    "v_exp_f32 v38, v38\n"
    "v_mfma_f32_16x16x32_bf16 a[12:15], v[160:163], v[140:143], a[12:15]\n"
    "buffer_load_dwordx4 v200, s[40:43], s65 offen sc1 lds\n"
    "v_mfma_f32_16x16x32_bf16 a[12:15], v[164:167], v[156:159], a[12:15]\n"
    "s_waitcnt lgkmcnt(2)\n"
    "v_exp_f32 v39, v39\n"
    "v_mfma_f32_16x16x32_bf16 a[28:31], v[168:171], v[140:143], a[28:31]\n"
    "ds_read_b128 v[160:163], v201 offset:6144\n"
    "v_add_f32 v117, v117, v68\n"
    "v_mfma_f32_16x16x32_bf16 a[28:31], v[172:175], v[156:159], a[28:31]\n"
    "v_exp_f32 v40, v40\n"
    "v_mfma_f32_16x16x32_bf16 a[24:27], v[172:175], v[152:155], a[24:27]\n"
    "buffer_load_dwordx4 v200, s[40:43], s65 offen offset:1024 sc1 lds\n"
    "v_mfma_f32_16x16x32_bf16 a[24:27], v[168:171], v[136:139], a[24:27]\n"
    "v_exp_f32 v41, v41\n"
    "v_mfma_f32_16x16x32_bf16 a[20:23], v[168:171], v[132:135], a[20:23]\n"
    "ds_read_b128 v[164:167], v201 offset:7168\n"
    "v_add_f32 v118, v118, v69\n"
    "v_mfma_f32_16x16x32_bf16 a[20:23], v[172:175], v[148:151], a[20:23]\n"
    "v_exp_f32 v42, v42\n"
    "v_mfma_f32_16x16x32_bf16 a[16:19], v[172:175], v[144:147], a[16:19]\n"
    "v_exp_f32 v43, v43\n"
    "v_mfma_f32_16x16x32_bf16 a[16:19], v[168:171], v[128:131], a[16:19]\n"
    "s_waitcnt lgkmcnt(2)\n"
    "v_exp_f32 v44, v44\n"
    "v_mfma_f32_16x16x32_bf16 a[32:35], v[176:179], v[128:131], a[32:35]\n"
    "ds_read_b128 v[168:171], v201 offset:8192\n"
    "v_add_f32 v117, v117, v70\n"
    "v_mfma_f32_16x16x32_bf16 a[32:35], v[180:183], v[144:147], a[32:35]\n"
    "buffer_load_dwordx2 v[218:219], v213, s[44:47], 0 offen sc1\n"
    "v_mfma_f32_16x16x32_bf16 a[36:39], v[180:183], v[148:151], a[36:39]\n"
    "v_exp_f32 v45, v45\n"
    "v_mfma_f32_16x16x32_bf16 a[36:39], v[176:179], v[132:135], a[36:39]\n"
    "v_exp_f32 v46, v46\n"
    "v_mfma_f32_16x16x32_bf16 a[40:43], v[176:179], v[136:139], a[40:43]\n"
    "ds_read_b128 v[172:175], v201 offset:9216\n"
    "v_add_f32 v113, v113, v32\n"
    "v_mfma_f32_16x16x32_bf16 a[40:43], v[180:183], v[152:155], a[40:43]\n"
    "v_exp_f32 v47, v47\n"
    "v_mfma_f32_16x16x32_bf16 a[44:47], v[180:183], v[156:159], a[44:47]\n"
    "v_exp_f32 v48, v48\n"
    "v_mfma_f32_16x16x32_bf16 a[44:47], v[176:179], v[140:143], a[44:47]\n"
    "s_waitcnt lgkmcnt(2)\n"
    "v_exp_f32 v49, v49\n"
    "v_mfma_f32_16x16x32_bf16 a[60:63], v[160:163], v[140:143], a[60:63]\n"
    "ds_read_b128 v[176:179], v201 offset:10240\n"
    "v_add_f32 v118, v118, v71\n"
    "v_mfma_f32_16x16x32_bf16 a[60:63], v[164:167], v[156:159], a[60:63]\n"
    "v_exp_f32 v50, v50\n"
    "v_mfma_f32_16x16x32_bf16 a[56:59], v[164:167], v[152:155], a[56:59]\n"
    "v_exp_f32 v51, v51\n"
    "v_mfma_f32_16x16x32_bf16 a[56:59], v[160:163], v[136:139], a[56:59]\n"
    "v_exp_f32 v52, v52\n"
    "v_mfma_f32_16x16x32_bf16 a[52:55], v[160:163], v[132:135], a[52:55]\n"
    "ds_read_b128 v[180:183], v201 offset:11264\n"
    "v_add_f32 v114, v114, v33\n"
    "v_mfma_f32_16x16x32_bf16 a[52:55], v[164:167], v[148:151], a[52:55]\n"
    "v_add_f32 v121, v121, v72\n"
    "v_add_f32 v113, v113, v34\n"
    "v_mfma_f32_16x16x32_bf16 a[48:51], v[164:167], v[144:147], a[48:51]\n"
    "v_exp_f32 v53, v53\n"
    "v_mfma_f32_16x16x32_bf16 a[48:51], v[160:163], v[128:131], a[48:51]\n"
    "s_waitcnt lgkmcnt(2)\n"
    "v_add_f32 v122, v122, v73\n"
    "v_add_f32 v114, v114, v35\n"
    "v_mfma_f32_16x16x32_bf16 a[64:67], v[168:171], v[128:131], a[64:67]\n"
    "ds_read_b128 v[160:163], v201 offset:12288\n"
    "v_add_f32 v121, v121, v74\n"
    "v_mfma_f32_16x16x32_bf16 a[64:67], v[172:175], v[144:147], a[64:67]\n"
    "v_add_f32 v117, v117, v36\n"
    "v_add_f32 v122, v122, v75\n"
    "v_mfma_f32_16x16x32_bf16 a[68:71], v[172:175], v[148:151], a[68:71]\n"
    "v_exp_f32 v54, v54\n"
    "v_mfma_f32_16x16x32_bf16 a[68:71], v[168:171], v[132:135], a[68:71]\n"
    "s_add_u32 m0, s51, s95\n"
    "v_add_f32 v118, v118, v37\n"
    "v_add_f32 v125, v125, v76\n"
    "v_mfma_f32_16x16x32_bf16 a[72:75], v[168:171], v[136:139], a[72:75]\n"
    "ds_read_b128 v[164:167], v201 offset:13312\n"
    "v_add_f32 v117, v117, v38\n"
    "v_mfma_f32_16x16x32_bf16 a[72:75], v[172:175], v[152:155], a[72:75]\n"
    "buffer_load_dwordx4 v202, s[32:35], s64 offen lds\n"
    "v_mfma_f32_16x16x32_bf16 a[76:79], v[172:175], v[156:159], a[76:79]\n"
    "v_exp_f32 v55, v55\n"
    "v_mfma_f32_16x16x32_bf16 a[76:79], v[168:171], v[140:143], a[76:79]\n"
    "s_waitcnt lgkmcnt(2)\n"
    "v_add_f32 v126, v126, v77\n"
    "v_add_f32 v118, v118, v39\n"
    "v_mfma_f32_16x16x32_bf16 a[92:95], v[176:179], v[140:143], a[92:95]\n"
    "ds_read_b128 v[168:171], v201 offset:14336\n"
    "v_add_f32 v125, v125, v78\n"
    "v_mfma_f32_16x16x32_bf16 a[92:95], v[180:183], v[156:159], a[92:95]\n"
    "buffer_load_dwordx4 v203, s[32:35], s64 offen offset:1024 lds\n"
    "v_mfma_f32_16x16x32_bf16 a[88:91], v[180:183], v[152:155], a[88:91]\n"
    "v_add_f32 v121, v121, v40\n"
    "v_add_f32 v126, v126, v79\n"
    "v_mfma_f32_16x16x32_bf16 a[88:91], v[176:179], v[136:139], a[88:91]\n"
    "v_exp_f32 v56, v56\n"
    "v_mfma_f32_16x16x32_bf16 a[84:87], v[176:179], v[132:135], a[84:87]\n"
    "ds_read_b128 v[172:175], v201 offset:15360\n"
    "v_add_f32 v122, v122, v41\n"
    "v_mfma_f32_16x16x32_bf16 a[84:87], v[180:183], v[148:151], a[84:87]\n"
    "buffer_load_dwordx4 v204, s[32:35], s64 offen offset:2048 lds\n"
    "v_mfma_f32_16x16x32_bf16 a[80:83], v[180:183], v[144:147], a[80:83]\n"
    "v_add_f32 v113, v113, v80\n"
    "v_add_f32 v121, v121, v42\n"
    "v_mfma_f32_16x16x32_bf16 a[80:83], v[176:179], v[128:131], a[80:83]\n"
    "s_waitcnt lgkmcnt(2)\n"
    "v_exp_f32 v57, v57\n"
    "v_mfma_f32_16x16x32_bf16 a[96:99], v[160:163], v[128:131], a[96:99]\n"
    "s_mov_b32 s95, s94\n"
    "ds_read_b128 v[176:179], v201 offset:16384\n"
    "v_add_f32 v114, v114, v81\n"
    "v_mfma_f32_16x16x32_bf16 a[96:99], v[164:167], v[144:147], a[96:99]\n"
    "s_sub_u32 s94, s94, 0x3000\n"
    "v_add_f32 v122, v122, v43\n"
    "v_add_f32 v113, v113, v82\n"
    "v_mfma_f32_16x16x32_bf16 a[100:103], v[164:167], v[148:151], a[100:103]\n"
    "s_min_u32 s94, s94, 0x6000\n"
    "v_add_f32 v125, v125, v44\n"
    "v_add_f32 v114, v114, v83\n"
    "v_mfma_f32_16x16x32_bf16 a[100:103], v[160:163], v[132:135], a[100:103]\n"
    "s_sub_u32 s93, s94, s95\n"
    "v_exp_f32 v58, v58\n"
    "v_mfma_f32_16x16x32_bf16 a[104:107], v[160:163], v[136:139], a[104:107]\n"
    "ds_read_b128 v[180:183], v201 offset:17408\n"
    "v_add_f32 v126, v126, v45\n"
    "v_mfma_f32_16x16x32_bf16 a[104:107], v[164:167], v[152:155], a[104:107]\n"
    "v_add_f32 v117, v117, v84\n"
    "v_add_f32 v125, v125, v46\n"
    "v_mfma_f32_16x16x32_bf16 a[108:111], v[164:167], v[156:159], a[108:111]\n"
    "v_exp_f32 v59, v59\n"
    "v_mfma_f32_16x16x32_bf16 a[108:111], v[160:163], v[140:143], a[108:111]\n"
    "s_waitcnt lgkmcnt(2)\n"
    "v_add_f32 v118, v118, v85\n"
    "v_add_f32 v126, v126, v47\n"
    "v_mfma_f32_16x16x32_bf16 a[124:127], v[168:171], v[140:143], a[124:127]\n"
    "ds_read_b128 v[160:163], v201 offset:18432\n"
    "v_add_f32 v117, v117, v86\n"
    "v_mfma_f32_16x16x32_bf16 a[124:127], v[172:175], v[156:159], a[124:127]\n"
    "v_add_f32 v113, v113, v48\n"
    "v_add_u32 v220, 1, v220\n"
    "v_mfma_f32_16x16x32_bf16 a[120:123], v[172:175], v[152:155], a[120:123]\n"
    "v_exp_f32 v60, v60\n"
    "v_mfma_f32_16x16x32_bf16 a[120:123], v[168:171], v[136:139], a[120:123]\n"
    "v_add_u32 v205, s93, v205\n"
    "v_add_f32 v118, v118, v87\n"
    "v_mfma_f32_16x16x32_bf16 a[116:119], v[168:171], v[132:135], a[116:119]\n"
    "ds_read_b128 v[164:167], v201 offset:19456\n"
    "v_add_f32 v114, v114, v49\n"
    "v_mfma_f32_16x16x32_bf16 a[116:119], v[172:175], v[148:151], a[116:119]\n"
    "v_add_u32 v209, 0x180, v209\n"
    "v_add_u32 v206, s93, v206\n"
    "v_mfma_f32_16x16x32_bf16 a[112:115], v[172:175], v[144:147], a[112:115]\n"
    "v_add_f32 v121, v121, v88\n"
    "v_add_f32 v113, v113, v50\n"
    "v_mfma_f32_16x16x32_bf16 a[112:115], v[168:171], v[128:131], a[112:115]\n"
    "s_waitcnt lgkmcnt(2)\n"
    "v_exp_f32 v61, v61\n"
    "v_mfma_f32_16x16x32_bf16 a[128:131], v[176:179], v[128:131], a[128:131]\n"
    "s_add_u32 s64, s64, 0xc000\n"
    "ds_read_b128 v[168:171], v201 offset:20480\n"
    "v_add_u32 v210, 0x80, v210\n"
    "v_mfma_f32_16x16x32_bf16 a[128:131], v[180:183], v[144:147], a[128:131]\n"
    "s_add_u32 s62, s62, 0x6000\n"
    "v_add_u32 v207, s93, v207\n"
    "v_add_f32 v122, v122, v89\n"
    "v_mfma_f32_16x16x32_bf16 a[132:135], v[180:183], v[148:151], a[132:135]\n"
    "s_add_u32 s63, s63, 0x6000\n"
    "v_add_f32 v114, v114, v51\n"
    "v_add_f32 v121, v121, v90\n"
    "v_mfma_f32_16x16x32_bf16 a[132:135], v[176:179], v[132:135], a[132:135]\n"
    "s_add_u32 s60, s60, 0x6000\n"
    "v_add_f32 v117, v117, v52\n"
    "v_add_f32 v122, v122, v91\n"
    "v_mfma_f32_16x16x32_bf16 a[136:139], v[176:179], v[136:139], a[136:139]\n"
    "s_add_u32 s61, s61, 0x6000\n"
    "ds_read_b128 v[172:175], v201 offset:21504\n"
    "v_add_f32 v118, v118, v53\n"
    "v_mfma_f32_16x16x32_bf16 a[136:139], v[180:183], v[152:155], a[136:139]\n"
    "s_add_u32 s66, s66, s74\n"
    "v_exp_f32 v62, v62\n"
    "v_mfma_f32_16x16x32_bf16 a[140:143], v[180:183], v[156:159], a[140:143]\n"
    "s_min_u32 s66, s66, 33\n"
    "v_add_f32 v125, v125, v92\n"
    "v_add_f32 v117, v117, v54\n"
    "v_mfma_f32_16x16x32_bf16 a[140:143], v[176:179], v[140:143], a[140:143]\n"
    "s_waitcnt lgkmcnt(2)\n"
    "v_exp_f32 v63, v63\n"
    "v_mfma_f32_16x16x32_bf16 a[156:159], v[160:163], v[140:143], a[156:159]\n"
    "ds_read_b128 v[176:179], v201 offset:22528\n"
    "v_add_f32 v126, v126, v93\n"
    "v_mfma_f32_16x16x32_bf16 a[156:159], v[164:167], v[156:159], a[156:159]\n"
    "s_waitcnt vmcnt(3)\n"
    "ds_read_b128 v[180:183], v201 offset:23552\n"
    "v_add_f32 v118, v118, v55\n"
    "v_mfma_f32_16x16x32_bf16 a[152:155], v[164:167], v[152:155], a[152:155]\n"
    "v_cmp_ne_u32 s[80:81], s67, v219\n"
    "v_add_f32 v125, v125, v94\n"
    "v_mfma_f32_16x16x32_bf16 a[152:155], v[160:163], v[136:139], a[152:155]\n"
    "v_cmp_gt_u32 vcc, s66, v218\n"
    "v_add_f32 v121, v121, v56\n"
    "v_mfma_f32_16x16x32_bf16 a[148:151], v[160:163], v[132:135], a[148:151]\n"
    "v_add_f32 v126, v126, v95\n"
    "v_add_f32 v122, v122, v57\n"
    "v_mfma_f32_16x16x32_bf16 a[148:151], v[164:167], v[148:151], a[148:151]\n"
    "s_or_b64 vcc, vcc, s[80:81]\n"
    "s_cbranch_vccnz LSPRE4\n"
    "LSPB4:\n"
    "v_mfma_f32_16x16x32_bf16 a[144:147], v[164:167], v[144:147], a[144:147]\n"
    "v_mfma_f32_16x16x32_bf16 a[144:147], v[160:163], v[128:131], a[144:147]\n"
    "s_waitcnt lgkmcnt(0)\n"
    "v_mfma_f32_16x16x32_bf16 a[160:163], v[168:171], v[128:131], a[160:163]\n"
    "s_barrier\n"
    "v_mfma_f32_16x16x32_bf16 a[160:163], v[172:175], v[144:147], a[160:163]\n"
    "buffer_store_dwordx2 v[220:221], v214, s[44:47], 0 offen sc1\n"
    "v_mfma_f32_16x16x32_bf16 a[164:167], v[172:175], v[148:151], a[164:167]\n"
    "ds_read_b128 v[160:163], v200\n"
    "ds_read_u16_d16_hi v223, v209 offset:6144\n"
    "v_mfma_f32_16x16x32_bf16 a[164:167], v[168:171], v[132:135], a[164:167]\n"
    "ds_read_u16_d16_hi v222, v209\n"
    "v_add_f32 v121, v121, v58\n"
    "v_mfma_f32_16x16x32_bf16 a[168:171], v[168:171], v[136:139], a[168:171]\n"
    "ds_read_b128 v[164:167], v200 offset:1024\n"
    "ds_read_b128 v[232:235], v205 offset:24576\n"
    "v_mfma_f32_16x16x32_bf16 a[168:171], v[172:175], v[152:155], a[168:171]\n"
    "v_add_f32 v122, v122, v59\n"
    "ds_read_b128 v[236:239], v206 offset:24576\n"
    "v_mfma_f32_16x16x32_bf16 a[172:175], v[172:175], v[156:159], a[172:175]\n"
    "v_add_f32 v125, v125, v60\n"
    "v_add_f32 v126, v126, v61\n"
    "v_mfma_f32_16x16x32_bf16 a[172:175], v[168:171], v[140:143], a[172:175]\n"
    "v_add_f32 v125, v125, v62\n"
    "v_add_f32 v126, v126, v63\n"
    "v_mfma_f32_16x16x32_bf16 a[188:191], v[176:179], v[140:143], a[188:191]\n"
    "ds_read_b128 v[168:171], v200 offset:2048\n"
    "ds_read_b64 v[228:229], v210\n"
    "v_mfma_f32_16x16x32_bf16 a[188:191], v[180:183], v[156:159], a[188:191]\n"
    "ds_read2st64_b32 v[240:241], v207 offset0:96 offset1:99\n"
    "ds_read2st64_b32 v[242:243], v207 offset0:102 offset1:105\n"
    "v_mfma_f32_16x16x32_bf16 a[184:187], v[176:179], v[136:139], a[184:187]\n"
    "ds_read2st64_b32 v[244:245], v207 offset0:108 offset1:111\n"
    "ds_read2st64_b32 v[246:247], v207 offset0:114 offset1:117\n"
    "v_mfma_f32_16x16x32_bf16 a[184:187], v[180:183], v[152:155], a[184:187]\n"
    "v_cvt_pk_bf16_f32 v140, v44, v45\n"
    "v_cvt_pk_bf16_f32 v141, v46, v47\n"
    "v_mfma_f32_16x16x32_bf16 a[180:183], v[176:179], v[132:135], a[180:183]\n"
    "ds_read_b128 v[172:175], v200 offset:3072\n"
    "s_waitcnt lgkmcnt(7)\n"
    "v_mfma_f32_16x16x32_bf16 a[180:183], v[180:183], v[148:151], a[180:183]\n"
    "v_mul_f32 v226, s75, v223\n"
    "v_fma_f32 v227, -v222, v226, v217\n"
    "v_mfma_f32_16x16x32_bf16 a[176:179], v[176:179], v[128:131], a[176:179]\n"
    "v_cvt_f32_i32 v232, v232\n"
    "v_cvt_f32_i32 v233, v233\n"
    "v_mfma_f32_16x16x32_bf16 a[176:179], v[180:183], v[144:147], a[176:179]\n"
    "v_cvt_f32_i32 v234, v234\n"
    "v_cvt_f32_i32 v235, v235\n"
    "LM0:\n"
    "v_mfma_f32_16x16x32_bf16 v[64:67], v[160:163], a[192:195], v[96:99]\n"
    "ds_read_b128 v[176:179], v200 offset:4096\n"
    "v_cvt_f32_i32 v236, v236\n"
    "v_mfma_f32_16x16x32_bf16 v[64:67], v[164:167], a[196:199], v[64:67]\n"
    "v_cvt_f32_i32 v237, v237\n"
    "v_cvt_f32_i32 v238, v238\n"
    "v_mfma_f32_16x16x32_bf16 v[68:71], v[164:167], a[220:223], v[100:103]\n"
    "v_cvt_f32_i32 v239, v239\n"
    "v_mul_f32_dpp v232, v226, v232 row_newbcast:0 row_mask:0xf bank_mask:0xf\n"
    "v_mfma_f32_16x16x32_bf16 v[68:71], v[160:163], a[216:219], v[68:71]\n"
    "v_mul_f32_dpp v233, v226, v233 row_newbcast:1 row_mask:0xf bank_mask:0xf\n"
    "v_mul_f32_dpp v234, v226, v234 row_newbcast:2 row_mask:0xf bank_mask:0xf\n"
    "v_mfma_f32_16x16x32_bf16 v[72:75], v[160:163], a[240:243], v[104:107]\n"
    "ds_read_b128 v[180:183], v200 offset:5120\n"
    "v_mul_f32_dpp v235, v226, v235 row_newbcast:3 row_mask:0xf bank_mask:0xf\n"
    "v_mfma_f32_16x16x32_bf16 v[72:75], v[164:167], a[244:247], v[72:75]\n"
    "v_mul_f32_dpp v236, v226, v236 row_newbcast:4 row_mask:0xf bank_mask:0xf\n"
    "v_mul_f32_dpp v237, v226, v237 row_newbcast:5 row_mask:0xf bank_mask:0xf\n"
    "v_mfma_f32_16x16x32_bf16 v[76:79], v[164:167], v[12:15], v[108:111]\n"
    "v_mul_f32_dpp v238, v226, v238 row_newbcast:6 row_mask:0xf bank_mask:0xf\n"
    "v_mul_f32_dpp v239, v226, v239 row_newbcast:7 row_mask:0xf bank_mask:0xf\n"
    "v_mfma_f32_16x16x32_bf16 v[76:79], v[160:163], v[8:11], v[76:79]\n"
    "s_waitcnt lgkmcnt(2)\n"
    "v_add_f32_dpp v232, v227, v232 row_newbcast:0 row_mask:0xf bank_mask:0xf\n"
    "v_add_f32_dpp v233, v227, v233 row_newbcast:1 row_mask:0xf bank_mask:0xf\n"
    "v_mfma_f32_16x16x32_bf16 v[76:79], v[168:171], v[16:19], v[76:79]\n"
    "ds_read_b128 v[160:163], v200 offset:10240\n"
    "v_add_f32_dpp v234, v227, v234 row_newbcast:2 row_mask:0xf bank_mask:0xf\n"
    "v_mfma_f32_16x16x32_bf16 v[76:79], v[172:175], v[20:23], v[76:79]\n"
    "v_add_f32_dpp v235, v227, v235 row_newbcast:3 row_mask:0xf bank_mask:0xf\n"
    "v_add_f32_dpp v236, v227, v236 row_newbcast:4 row_mask:0xf bank_mask:0xf\n"
    "v_mfma_f32_16x16x32_bf16 v[72:75], v[172:175], a[252:255], v[72:75]\n"
    "v_add_f32_dpp v237, v227, v237 row_newbcast:5 row_mask:0xf bank_mask:0xf\n"
    "v_add_f32_dpp v238, v227, v238 row_newbcast:6 row_mask:0xf bank_mask:0xf\n"
    "v_mfma_f32_16x16x32_bf16 v[72:75], v[168:171], a[248:251], v[72:75]\n"
    "v_add_f32_dpp v239, v227, v239 row_newbcast:7 row_mask:0xf bank_mask:0xf\n"
    "v_cvt_pk_bf16_f32 v232, v232, v233\n"
    "v_mfma_f32_16x16x32_bf16 v[68:71], v[168:171], a[224:227], v[68:71]\n"
    "ds_read_b128 v[164:167], v200 offset:11264\n"
    "v_cvt_f32_i32 v240, v240\n"
    "v_mfma_f32_16x16x32_bf16 v[68:71], v[172:175], a[228:231], v[68:71]\n"
    "v_cvt_pk_bf16_f32 v233, v234, v235\n"
    "v_cvt_f32_i32 v241, v241\n"
    "v_mfma_f32_16x16x32_bf16 v[64:67], v[172:175], a[204:207], v[64:67]\n"
    "v_cvt_pk_bf16_f32 v234, v236, v237\n"
    "v_cvt_f32_i32 v242, v242\n"
    "v_mfma_f32_16x16x32_bf16 v[64:67], v[168:171], a[200:203], v[64:67]\n"
    "s_waitcnt lgkmcnt(2)\n"
    "v_cvt_pk_bf16_f32 v235, v238, v239\n"
    "v_cvt_f32_i32 v243, v243\n"
    "v_mfma_f32_16x16x32_bf16 v[64:67], v[176:179], a[208:211], v[64:67]\n"
    "ds_read_b128 v[168:171], v200 offset:8192\n"
    "v_cvt_f32_i32 v244, v244\n"
    "v_mfma_f32_16x16x32_bf16 v[64:67], v[180:183], a[212:215], v[64:67]\n"
    "v_cvt_f32_i32 v245, v245\n"
    "v_cvt_f32_i32 v246, v246\n"
    "v_mfma_f32_16x16x32_bf16 v[68:71], v[180:183], a[236:239], v[68:71]\n"
    "v_cvt_f32_i32 v247, v247\n"
    "v_mul_f32_dpp v240, v228, v240 row_newbcast:0 row_mask:0xf bank_mask:0xf\n"
    "v_mfma_f32_16x16x32_bf16 v[68:71], v[176:179], a[232:235], v[68:71]\n"
    "v_mul_f32_dpp v241, v228, v241 row_newbcast:1 row_mask:0xf bank_mask:0xf\n"
    "v_mul_f32_dpp v242, v228, v242 row_newbcast:2 row_mask:0xf bank_mask:0xf\n"
    "v_mfma_f32_16x16x32_bf16 v[72:75], v[176:179], v[0:3], v[72:75]\n"
    "ds_read_b128 v[172:175], v200 offset:9216\n"
    "v_mul_f32_dpp v243, v228, v243 row_newbcast:3 row_mask:0xf bank_mask:0xf\n"
    "v_mfma_f32_16x16x32_bf16 v[72:75], v[180:183], v[4:7], v[72:75]\n"
    "v_mul_f32_dpp v244, v228, v244 row_newbcast:4 row_mask:0xf bank_mask:0xf\n"
    "v_mul_f32_dpp v245, v228, v245 row_newbcast:5 row_mask:0xf bank_mask:0xf\n"
    "v_mfma_f32_16x16x32_bf16 v[76:79], v[180:183], v[28:31], v[76:79]\n"
    "v_mul_f32_dpp v246, v228, v246 row_newbcast:6 row_mask:0xf bank_mask:0xf\n"
    "v_mul_f32_dpp v247, v228, v247 row_newbcast:7 row_mask:0xf bank_mask:0xf\n"
    "v_mfma_f32_16x16x32_bf16 v[76:79], v[176:179], v[24:27], v[76:79]\n"
    "s_waitcnt lgkmcnt(2)\n"
    "v_add_f32_dpp v240, v229, v240 row_newbcast:0 row_mask:0xf bank_mask:0xf\n"
    "v_add_f32_dpp v241, v229, v241 row_newbcast:1 row_mask:0xf bank_mask:0xf\n"
    "v_mfma_f32_16x16x32_bf16 v[92:95], v[160:163], v[24:27], v[108:111]\n"
    "ds_read_b128 v[176:179], v200 offset:6144\n"
    "v_add_f32_dpp v242, v229, v242 row_newbcast:2 row_mask:0xf bank_mask:0xf\n"
    "v_mfma_f32_16x16x32_bf16 v[92:95], v[164:167], v[28:31], v[92:95]\n"
    "buffer_store_dwordx4 v[232:235], v211, s[36:39], s62 offen sc1\n"
    "v_add_f32_dpp v243, v229, v243 row_newbcast:3 row_mask:0xf bank_mask:0xf\n"
    "v_add_f32_dpp v244, v229, v244 row_newbcast:4 row_mask:0xf bank_mask:0xf\n"
    "v_add_f32_dpp v245, v229, v245 row_newbcast:5 row_mask:0xf bank_mask:0xf\n"
    "v_mfma_f32_16x16x32_bf16 v[88:91], v[164:167], v[4:7], v[104:107]\n"
    "v_add_f32_dpp v246, v229, v246 row_newbcast:6 row_mask:0xf bank_mask:0xf\n"
    "v_add_f32_dpp v247, v229, v247 row_newbcast:7 row_mask:0xf bank_mask:0xf\n"
    "v_mfma_f32_16x16x32_bf16 v[88:91], v[160:163], v[0:3], v[88:91]\n"
    "v_cvt_pk_bf16_f32 v240, v240, v241\n"
    "v_cvt_pk_bf16_f32 v241, v242, v243\n"
    "v_mfma_f32_16x16x32_bf16 v[84:87], v[160:163], a[232:235], v[100:103]\n"
    "ds_read_b128 v[180:183], v200 offset:7168\n"
    "v_cvt_pk_bf16_f32 v242, v244, v245\n"
    "v_mfma_f32_16x16x32_bf16 v[84:87], v[164:167], a[236:239], v[84:87]\n"
    "v_cvt_pk_bf16_f32 v243, v246, v247\n"
    "v_cvt_pk_bf16_f32 v136, v40, v41\n"
    "v_mfma_f32_16x16x32_bf16 v[80:83], v[164:167], a[212:215], v[96:99]\n"
    "v_cvt_pk_bf16_f32 v137, v42, v43\n"
    "v_cvt_pk_bf16_f32 v132, v36, v37\n"
    "v_mfma_f32_16x16x32_bf16 v[80:83], v[160:163], a[208:211], v[80:83]\n"
    "s_waitcnt lgkmcnt(2)\n"
    "v_cvt_pk_bf16_f32 v133, v38, v39\n"
    "v_cvt_pk_bf16_f32 v128, v32, v33\n"
    "v_mfma_f32_16x16x32_bf16 v[80:83], v[168:171], a[200:203], v[80:83]\n"
    "ds_read_b128 v[160:163], v200 offset:12288\n"
    "v_cvt_pk_bf16_f32 v129, v34, v35\n"
    "v_mfma_f32_16x16x32_bf16 v[80:83], v[172:175], a[204:207], v[80:83]\n"
    "buffer_store_dwordx4 v[240:243], v212, s[40:43], s63 offen sc1\n"
    "v_exp_f32 v64, v64\n"
    "v_cvt_pk_bf16_f32 v142, v60, v61\n"
    "v_mfma_f32_16x16x32_bf16 v[84:87], v[172:175], a[228:231], v[84:87]\n"
    "v_exp_f32 v65, v65\n"
    "v_mfma_f32_16x16x32_bf16 v[84:87], v[168:171], a[224:227], v[84:87]\n"
    "v_exp_f32 v66, v66\n"
    "v_mfma_f32_16x16x32_bf16 v[88:91], v[168:171], a[248:251], v[88:91]\n"
    "ds_read_b128 v[164:167], v200 offset:13312\n"
    "v_cvt_pk_bf16_f32 v143, v62, v63\n"
    "v_mfma_f32_16x16x32_bf16 v[88:91], v[172:175], a[252:255], v[88:91]\n"
    "v_exp_f32 v67, v67\n"
    "v_mfma_f32_16x16x32_bf16 v[92:95], v[172:175], v[20:23], v[92:95]\n"
    "v_exp_f32 v68, v68\n"
    "v_mfma_f32_16x16x32_bf16 v[92:95], v[168:171], v[16:19], v[92:95]\n"
    "s_waitcnt lgkmcnt(2)\n"
    "v_exp_f32 v69, v69\n"
    "v_mfma_f32_16x16x32_bf16 v[92:95], v[176:179], v[8:11], v[92:95]\n"
    "ds_read_b128 v[168:171], v200 offset:14336\n"
    "v_cvt_pk_bf16_f32 v138, v56, v57\n"
    "v_mfma_f32_16x16x32_bf16 v[92:95], v[180:183], v[12:15], v[92:95]\n"
    "v_exp_f32 v70, v70\n"
    "v_mfma_f32_16x16x32_bf16 v[88:91], v[180:183], a[244:247], v[88:91]\n"
    "v_exp_f32 v71, v71\n"
    "v_mfma_f32_16x16x32_bf16 v[88:91], v[176:179], a[240:243], v[88:91]\n"
    "v_exp_f32 v72, v72\n"
    "v_mfma_f32_16x16x32_bf16 v[84:87], v[176:179], a[216:219], v[84:87]\n"
    "ds_read_b128 v[172:175], v200 offset:15360\n"
    "v_cvt_pk_bf16_f32 v139, v58, v59\n"
    "v_mfma_f32_16x16x32_bf16 v[84:87], v[180:183], a[220:223], v[84:87]\n"
    "v_exp_f32 v73, v73\n"
    "v_mfma_f32_16x16x32_bf16 v[80:83], v[180:183], a[196:199], v[80:83]\n"
    "v_exp_f32 v74, v74\n"
    "v_mfma_f32_16x16x32_bf16 v[80:83], v[176:179], a[192:195], v[80:83]\n"
    "s_waitcnt lgkmcnt(2)\n"
    "v_exp_f32 v75, v75\n"
    "v_mfma_f32_16x16x32_bf16 v[32:35], v[160:163], a[192:195], v[96:99]\n"
    "ds_read_b128 v[176:179], v200 offset:16384\n"
    "v_cvt_pk_bf16_f32 v134, v52, v53\n"
    "v_mfma_f32_16x16x32_bf16 v[32:35], v[164:167], a[196:199], v[32:35]\n"
    "v_exp_f32 v76, v76\n"
    "v_mfma_f32_16x16x32_bf16 v[36:39], v[164:167], a[220:223], v[100:103]\n"
    "v_cvt_pk_bf16_f32 v135, v54, v55\n"
    "v_cvt_pk_bf16_f32 v130, v48, v49\n"
    "v_mfma_f32_16x16x32_bf16 v[36:39], v[160:163], a[216:219], v[36:39]\n"
    "v_exp_f32 v77, v77\n"
    "v_mfma_f32_16x16x32_bf16 v[40:43], v[160:163], a[240:243], v[104:107]\n"
    "ds_read_b128 v[180:183], v200 offset:17408\n"
    "v_cvt_pk_bf16_f32 v131, v50, v51\n"
    "v_mfma_f32_16x16x32_bf16 v[40:43], v[164:167], a[244:247], v[40:43]\n"
    "v_exp_f32 v78, v78\n"
    "v_mfma_f32_16x16x32_bf16 v[44:47], v[164:167], v[12:15], v[108:111]\n"
    "v_exp_f32 v79, v79\n"
    "v_mfma_f32_16x16x32_bf16 v[44:47], v[160:163], v[8:11], v[44:47]\n"
    "s_waitcnt lgkmcnt(2)\n"
    "v_exp_f32 v80, v80\n"
    "v_mfma_f32_16x16x32_bf16 v[44:47], v[168:171], v[16:19], v[44:47]\n"
    "ds_read_b128 v[160:163], v200 offset:22528\n"
    "v_cvt_pk_bf16_f32 v144, v64, v65\n"
    "v_mfma_f32_16x16x32_bf16 v[44:47], v[172:175], v[20:23], v[44:47]\n"
    "v_exp_f32 v81, v81\n"
    "v_mfma_f32_16x16x32_bf16 v[40:43], v[172:175], a[252:255], v[40:43]\n"
    "v_exp_f32 v82, v82\n"
    "v_mfma_f32_16x16x32_bf16 v[40:43], v[168:171], a[248:251], v[40:43]\n"
    "v_exp_f32 v83, v83\n"
    "v_mfma_f32_16x16x32_bf16 v[36:39], v[168:171], a[224:227], v[36:39]\n"
    "ds_read_b128 v[164:167], v200 offset:23552\n"
    "v_cvt_pk_bf16_f32 v145, v66, v67\n"
    "v_mfma_f32_16x16x32_bf16 v[36:39], v[172:175], a[228:231], v[36:39]\n"
    "v_exp_f32 v84, v84\n"
    "v_mfma_f32_16x16x32_bf16 v[32:35], v[172:175], a[204:207], v[32:35]\n"
    "v_exp_f32 v85, v85\n"
    "v_mfma_f32_16x16x32_bf16 v[32:35], v[168:171], a[200:203], v[32:35]\n"
    "s_waitcnt lgkmcnt(2)\n"
    "v_exp_f32 v86, v86\n"
    "v_mfma_f32_16x16x32_bf16 v[32:35], v[176:179], a[208:211], v[32:35]\n"
    "ds_read_b128 v[168:171], v200 offset:20480\n"
    "v_cvt_pk_bf16_f32 v148, v68, v69\n"
    "v_mfma_f32_16x16x32_bf16 v[32:35], v[180:183], a[212:215], v[32:35]\n"
    "v_exp_f32 v87, v87\n"
    "v_mfma_f32_16x16x32_bf16 v[36:39], v[180:183], a[236:239], v[36:39]\n"
    "v_exp_f32 v88, v88\n"
    "v_mfma_f32_16x16x32_bf16 v[36:39], v[176:179], a[232:235], v[36:39]\n"
    "v_cvt_pk_bf16_f32 v149, v70, v71\n"
    "v_cvt_pk_bf16_f32 v152, v72, v73\n"
    "v_mfma_f32_16x16x32_bf16 v[40:43], v[176:179], v[0:3], v[40:43]\n"
    "ds_read_b128 v[172:175], v200 offset:21504\n"
    "v_cvt_pk_bf16_f32 v153, v74, v75\n"
    "v_mfma_f32_16x16x32_bf16 v[40:43], v[180:183], v[4:7], v[40:43]\n"
    "v_exp_f32 v89, v89\n"
    "v_mfma_f32_16x16x32_bf16 v[44:47], v[180:183], v[28:31], v[44:47]\n"
    "v_exp_f32 v90, v90\n"
    "v_mfma_f32_16x16x32_bf16 v[44:47], v[176:179], v[24:27], v[44:47]\n"
    "s_waitcnt lgkmcnt(2)\n"
    "v_cvt_pk_bf16_f32 v156, v76, v77\n"
    "v_cvt_pk_bf16_f32 v157, v78, v79\n"
    "v_mfma_f32_16x16x32_bf16 v[60:63], v[160:163], v[24:27], v[108:111]\n"
    "s_mov_b32 m0, 0x6000\n"
    "ds_read_b128 v[176:179], v200 offset:18432\n"
    "v_cvt_pk_bf16_f32 v146, v80, v81\n"
    "v_mfma_f32_16x16x32_bf16 v[60:63], v[164:167], v[28:31], v[60:63]\n"
    "v_exp_f32 v91, v91\n"
    "v_mfma_f32_16x16x32_bf16 v[56:59], v[164:167], v[4:7], v[104:107]\n"
    "buffer_load_dwordx4 v200, s[36:39], s60 offen sc1 lds\n"
    "v_mfma_f32_16x16x32_bf16 v[56:59], v[160:163], v[0:3], v[56:59]\n"
    "v_cvt_pk_bf16_f32 v147, v82, v83\n"
    "v_cvt_pk_bf16_f32 v150, v84, v85\n"
    "v_mfma_f32_16x16x32_bf16 v[52:55], v[160:163], a[232:235], v[100:103]\n"
    "ds_read_b128 v[180:183], v200 offset:19456\n"
    "v_cvt_pk_bf16_f32 v151, v86, v87\n"
    "v_mfma_f32_16x16x32_bf16 v[52:55], v[164:167], a[236:239], v[52:55]\n"
    "v_exp_f32 v92, v92\n"
    "v_mfma_f32_16x16x32_bf16 v[48:51], v[164:167], a[212:215], v[96:99]\n"
    "buffer_load_dwordx4 v200, s[36:39], s60 offen offset:1024 sc1 lds\n"
    "v_mfma_f32_16x16x32_bf16 v[48:51], v[160:163], a[208:211], v[48:51]\n"
    "s_waitcnt lgkmcnt(2)\n"
    "v_exp_f32 v93, v93\n"
    "v_mfma_f32_16x16x32_bf16 v[48:51], v[168:171], a[200:203], v[48:51]\n"
    "ds_read_b128 v[160:163], v201 offset:24576\n"
    "v_cvt_pk_bf16_f32 v154, v88, v89\n"
    "v_mfma_f32_16x16x32_bf16 v[48:51], v[172:175], a[204:207], v[48:51]\n"
    "v_exp_f32 v94, v94\n"
    "v_mfma_f32_16x16x32_bf16 v[52:55], v[172:175], a[228:231], v[52:55]\n"
    "buffer_load_dwordx4 v200, s[36:39], s60 offen offset:2048 sc1 lds\n"
    "v_mfma_f32_16x16x32_bf16 v[52:55], v[168:171], a[224:227], v[52:55]\n"
    "v_cvt_pk_bf16_f32 v155, v90, v91\n"
    "v_cvt_pk_bf16_f32 v158, v92, v93\n"
    "v_mfma_f32_16x16x32_bf16 v[56:59], v[168:171], a[248:251], v[56:59]\n"
    "s_add_u32 m0, s71, 0xc000\n"
    "ds_read_b128 v[164:167], v201 offset:25600\n"
    "v_add_f32 v113, v113, v64\n"
    "v_mfma_f32_16x16x32_bf16 v[56:59], v[172:175], a[252:255], v[56:59]\n"
    "v_exp_f32 v95, v95\n"
    "v_mfma_f32_16x16x32_bf16 v[60:63], v[172:175], v[20:23], v[60:63]\n"
    "buffer_load_dwordx4 v200, s[40:43], s61 offen sc1 lds\n"
    "v_mfma_f32_16x16x32_bf16 v[60:63], v[168:171], v[16:19], v[60:63]\n"
    "s_waitcnt lgkmcnt(2)\n"
    "v_cvt_pk_bf16_f32 v159, v94, v95\n"
    "v_add_f32 v114, v114, v65\n"
    "v_mfma_f32_16x16x32_bf16 v[60:63], v[176:179], v[8:11], v[60:63]\n"
    "ds_read_b128 v[168:171], v201 offset:26624\n"
    "v_add_f32 v113, v113, v66\n"
    "v_mfma_f32_16x16x32_bf16 v[60:63], v[180:183], v[12:15], v[60:63]\n"
    "v_exp_f32 v32, v32\n"
    "v_mfma_f32_16x16x32_bf16 v[56:59], v[180:183], a[244:247], v[56:59]\n"
    "buffer_load_dwordx4 v200, s[40:43], s61 offen offset:1024 sc1 lds\n"
    "v_mfma_f32_16x16x32_bf16 v[56:59], v[176:179], a[240:243], v[56:59]\n"
    "v_exp_f32 v33, v33\n"
    "v_mfma_f32_16x16x32_bf16 v[52:55], v[176:179], a[216:219], v[52:55]\n"
    "ds_read_b128 v[172:175], v201 offset:27648\n"
    "v_add_f32 v114, v114, v67\n"
    "v_mfma_f32_16x16x32_bf16 v[52:55], v[180:183], a[220:223], v[52:55]\n"
    "v_exp_f32 v34, v34\n"
    "v_mfma_f32_16x16x32_bf16 v[48:51], v[180:183], a[196:199], v[48:51]\n"
    "buffer_load_dwordx4 v200, s[40:43], s61 offen offset:2048 sc1 lds\n"
    "v_mfma_f32_16x16x32_bf16 v[48:51], v[176:179], a[192:195], v[48:51]\n"
    "s_waitcnt lgkmcnt(2)\n"
    "v_exp_f32 v35, v35\n"
    "v_mfma_f32_16x16x32_bf16 a[0:3], v[160:163], v[128:131], a[0:3]\n"
    "ds_read_b128 v[176:179], v201 offset:28672\n"
    "v_add_f32 v117, v117, v68\n"
    "v_mfma_f32_16x16x32_bf16 a[0:3], v[164:167], v[144:147], a[0:3]\n"
    "v_exp_f32 v36, v36\n"
    "v_mfma_f32_16x16x32_bf16 a[4:7], v[160:163], v[132:135], a[4:7]\n"
    "buffer_load_dwordx4 v200, s[40:43], s61 offen offset:3072 sc1 lds\n"
    "v_mfma_f32_16x16x32_bf16 a[4:7], v[164:167], v[148:151], a[4:7]\n"
    "s_add_u32 s65, s61, 0x1000\n"
    "v_exp_f32 v37, v37\n"
    "v_mfma_f32_16x16x32_bf16 a[8:11], v[160:163], v[136:139], a[8:11]\n"
    "s_add_u32 m0, s71, 0xd000\n"
    "ds_read_b128 v[180:183], v201 offset:29696\n"
    "v_add_f32 v118, v118, v69\n"
    "v_mfma_f32_16x16x32_bf16 a[8:11], v[164:167], v[152:155], a[8:11]\n"
    "v_exp_f32 v38, v38\n"
    "v_mfma_f32_16x16x32_bf16 a[12:15], v[160:163], v[140:143], a[12:15]\n"
    "buffer_load_dwordx4 v200, s[40:43], s65 offen sc1 lds\n"
    "v_mfma_f32_16x16x32_bf16 a[12:15], v[164:167], v[156:159], a[12:15]\n"
    "s_waitcnt lgkmcnt(2)\n"
    "v_exp_f32 v39, v39\n"
    "v_mfma_f32_16x16x32_bf16 a[28:31], v[168:171], v[140:143], a[28:31]\n"
    "ds_read_b128 v[160:163], v201 offset:30720\n"
    "v_add_f32 v117, v117, v70\n"
    "v_mfma_f32_16x16x32_bf16 a[28:31], v[172:175], v[156:159], a[28:31]\n"
    "v_exp_f32 v40, v40\n"
    "v_mfma_f32_16x16x32_bf16 a[24:27], v[172:175], v[152:155], a[24:27]\n"
    "buffer_load_dwordx4 v200, s[40:43], s65 offen offset:1024 sc1 lds\n"
    "v_mfma_f32_16x16x32_bf16 a[24:27], v[168:171], v[136:139], a[24:27]\n"
    "v_exp_f32 v41, v41\n"
    "v_mfma_f32_16x16x32_bf16 a[20:23], v[168:171], v[132:135], a[20:23]\n"
    "ds_read_b128 v[164:167], v201 offset:31744\n"
    "v_add_f32 v113, v113, v32\n"
    "v_mfma_f32_16x16x32_bf16 a[20:23], v[172:175], v[148:151], a[20:23]\n"
    "v_exp_f32 v42, v42\n"
    "v_mfma_f32_16x16x32_bf16 a[16:19], v[172:175], v[144:147], a[16:19]\n"
    "v_exp_f32 v43, v43\n"
    "v_mfma_f32_16x16x32_bf16 a[16:19], v[168:171], v[128:131], a[16:19]\n"
    "s_waitcnt lgkmcnt(2)\n"
    "v_exp_f32 v44, v44\n"
    "v_mfma_f32_16x16x32_bf16 a[32:35], v[176:179], v[128:131], a[32:35]\n"
    "ds_read_b128 v[168:171], v201 offset:32768\n"
    "v_add_f32 v118, v118, v71\n"
    "v_mfma_f32_16x16x32_bf16 a[32:35], v[180:183], v[144:147], a[32:35]\n"
    "buffer_load_dwordx2 v[218:219], v213, s[44:47], 0 offen sc1\n"
    "v_mfma_f32_16x16x32_bf16 a[36:39], v[180:183], v[148:151], a[36:39]\n"
    "v_exp_f32 v45, v45\n"
    "v_mfma_f32_16x16x32_bf16 a[36:39], v[176:179], v[132:135], a[36:39]\n"
    "v_exp_f32 v46, v46\n"
    "v_mfma_f32_16x16x32_bf16 a[40:43], v[176:179], v[136:139], a[40:43]\n"
    "ds_read_b128 v[172:175], v201 offset:33792\n"
    "v_add_f32 v114, v114, v33\n"
    "v_mfma_f32_16x16x32_bf16 a[40:43], v[180:183], v[152:155], a[40:43]\n"
    "v_exp_f32 v47, v47\n"
    "v_mfma_f32_16x16x32_bf16 a[44:47], v[180:183], v[156:159], a[44:47]\n"
    "v_exp_f32 v48, v48\n"
    "v_mfma_f32_16x16x32_bf16 a[44:47], v[176:179], v[140:143], a[44:47]\n"
    "s_waitcnt lgkmcnt(2)\n"
    "v_exp_f32 v49, v49\n"
    "v_mfma_f32_16x16x32_bf16 a[60:63], v[160:163], v[140:143], a[60:63]\n"
    "ds_read_b128 v[176:179], v201 offset:34816\n"
    "v_add_f32 v121, v121, v72\n"
    "v_mfma_f32_16x16x32_bf16 a[60:63], v[164:167], v[156:159], a[60:63]\n"
    "v_exp_f32 v50, v50\n"
    "v_mfma_f32_16x16x32_bf16 a[56:59], v[164:167], v[152:155], a[56:59]\n"
    "v_exp_f32 v51, v51\n"
    "v_mfma_f32_16x16x32_bf16 a[56:59], v[160:163], v[136:139], a[56:59]\n"
    "v_exp_f32 v52, v52\n"
    "v_mfma_f32_16x16x32_bf16 a[52:55], v[160:163], v[132:135], a[52:55]\n"
    "ds_read_b128 v[180:183], v201 offset:35840\n"
    "v_add_f32 v113, v113, v34\n"
    "v_mfma_f32_16x16x32_bf16 a[52:55], v[164:167], v[148:151], a[52:55]\n"
    "v_exp_f32 v53, v53\n"
    "v_mfma_f32_16x16x32_bf16 a[48:51], v[164:167], v[144:147], a[48:51]\n"
    "v_add_f32 v122, v122, v73\n"
    "v_add_f32 v114, v114, v35\n"
    "v_mfma_f32_16x16x32_bf16 a[48:51], v[160:163], v[128:131], a[48:51]\n"
    "s_waitcnt lgkmcnt(2)\n"
    "v_add_f32 v121, v121, v74\n"
    "v_add_f32 v117, v117, v36\n"
    "v_mfma_f32_16x16x32_bf16 a[64:67], v[168:171], v[128:131], a[64:67]\n"
    "ds_read_b128 v[160:163], v201 offset:36864\n"
    "v_add_f32 v122, v122, v75\n"
    "v_mfma_f32_16x16x32_bf16 a[64:67], v[172:175], v[144:147], a[64:67]\n"
    "v_exp_f32 v54, v54\n"
    "v_mfma_f32_16x16x32_bf16 a[68:71], v[172:175], v[148:151], a[68:71]\n"
    "v_add_f32 v118, v118, v37\n"
    "v_add_f32 v125, v125, v76\n"
    "v_mfma_f32_16x16x32_bf16 a[68:71], v[168:171], v[132:135], a[68:71]\n"
    "s_add_u32 m0, s51, s95\n"
    "v_add_f32 v117, v117, v38\n"
    "v_add_f32 v126, v126, v77\n"
    "v_mfma_f32_16x16x32_bf16 a[72:75], v[168:171], v[136:139], a[72:75]\n"
    "ds_read_b128 v[164:167], v201 offset:37888\n"
    "v_add_f32 v118, v118, v39\n"
    "v_mfma_f32_16x16x32_bf16 a[72:75], v[172:175], v[152:155], a[72:75]\n"
    "buffer_load_dwordx4 v202, s[32:35], s64 offen lds\n"
    "v_mfma_f32_16x16x32_bf16 a[76:79], v[172:175], v[156:159], a[76:79]\n"
    "v_exp_f32 v55, v55\n"
    "v_mfma_f32_16x16x32_bf16 a[76:79], v[168:171], v[140:143], a[76:79]\n"
    "s_waitcnt lgkmcnt(2)\n"
    "v_add_f32 v125, v125, v78\n"
    "v_add_f32 v121, v121, v40\n"
    "v_mfma_f32_16x16x32_bf16 a[92:95], v[176:179], v[140:143], a[92:95]\n"
    "ds_read_b128 v[168:171], v201 offset:38912\n"
    "v_add_f32 v126, v126, v79\n"
    "v_mfma_f32_16x16x32_bf16 a[92:95], v[180:183], v[156:159], a[92:95]\n"
    "buffer_load_dwordx4 v203, s[32:35], s64 offen offset:1024 lds\n"
    "v_mfma_f32_16x16x32_bf16 a[88:91], v[180:183], v[152:155], a[88:91]\n"
    "v_exp_f32 v56, v56\n"
    "v_mfma_f32_16x16x32_bf16 a[88:91], v[176:179], v[136:139], a[88:91]\n"
    "v_add_f32 v122, v122, v41\n"
    "v_add_f32 v113, v113, v80\n"
    "v_mfma_f32_16x16x32_bf16 a[84:87], v[176:179], v[132:135], a[84:87]\n"
    "ds_read_b128 v[172:175], v201 offset:39936\n"
    "v_add_f32 v121, v121, v42\n"
    "v_mfma_f32_16x16x32_bf16 a[84:87], v[180:183], v[148:151], a[84:87]\n"
    "buffer_load_dwordx4 v204, s[32:35], s64 offen offset:2048 lds\n"
    "v_mfma_f32_16x16x32_bf16 a[80:83], v[180:183], v[144:147], a[80:83]\n"
    "v_exp_f32 v57, v57\n"
    "v_mfma_f32_16x16x32_bf16 a[80:83], v[176:179], v[128:131], a[80:83]\n"
    "s_waitcnt lgkmcnt(2)\n"
    "v_add_f32 v114, v114, v81\n"
    "v_add_f32 v122, v122, v43\n"
    "v_mfma_f32_16x16x32_bf16 a[96:99], v[160:163], v[128:131], a[96:99]\n"
    "s_mov_b32 s95, s94\n"
    "ds_read_b128 v[176:179], v201 offset:40960\n"
    "v_add_f32 v113, v113, v82\n"
    "v_mfma_f32_16x16x32_bf16 a[96:99], v[164:167], v[144:147], a[96:99]\n"
    "s_sub_u32 s94, s94, 0x3000\n"
    "v_add_f32 v125, v125, v44\n"
    "v_add_f32 v114, v114, v83\n"
    "v_mfma_f32_16x16x32_bf16 a[100:103], v[164:167], v[148:151], a[100:103]\n"
    "s_min_u32 s94, s94, 0x6000\n"
    "v_exp_f32 v58, v58\n"
    "v_mfma_f32_16x16x32_bf16 a[100:103], v[160:163], v[132:135], a[100:103]\n"
    "s_sub_u32 s93, s94, s95\n"
    "v_add_f32 v126, v126, v45\n"
    "v_add_f32 v117, v117, v84\n"
    "v_mfma_f32_16x16x32_bf16 a[104:107], v[160:163], v[136:139], a[104:107]\n"
    "ds_read_b128 v[180:183], v201 offset:41984\n"
    "v_add_f32 v125, v125, v46\n"
    "v_mfma_f32_16x16x32_bf16 a[104:107], v[164:167], v[152:155], a[104:107]\n"
    "v_exp_f32 v59, v59\n"
    "v_mfma_f32_16x16x32_bf16 a[108:111], v[164:167], v[156:159], a[108:111]\n"
    "v_add_f32 v118, v118, v85\n"
    "v_add_f32 v126, v126, v47\n"
    "v_mfma_f32_16x16x32_bf16 a[108:111], v[160:163], v[140:143], a[108:111]\n"
    "s_waitcnt lgkmcnt(2)\n"
    "v_add_f32 v117, v117, v86\n"
    "v_add_f32 v113, v113, v48\n"
    "v_mfma_f32_16x16x32_bf16 a[124:127], v[168:171], v[140:143], a[124:127]\n"
    "ds_read_b128 v[160:163], v201 offset:43008\n"
    "v_add_u32 v205, s93, v205\n"
    "v_mfma_f32_16x16x32_bf16 a[124:127], v[172:175], v[156:159], a[124:127]\n"
    "v_exp_f32 v60, v60\n"
    "v_mfma_f32_16x16x32_bf16 a[120:123], v[172:175], v[152:155], a[120:123]\n"
    "v_add_f32 v118, v118, v87\n"
    "v_add_f32 v114, v114, v49\n"
    "v_mfma_f32_16x16x32_bf16 a[120:123], v[168:171], v[136:139], a[120:123]\n"
    "v_add_u32 v206, s93, v206\n"
    "v_add_f32 v121, v121, v88\n"
    "v_mfma_f32_16x16x32_bf16 a[116:119], v[168:171], v[132:135], a[116:119]\n"
    "ds_read_b128 v[164:167], v201 offset:44032\n"
    "v_add_f32 v113, v113, v50\n"
    "v_mfma_f32_16x16x32_bf16 a[116:119], v[172:175], v[148:151], a[116:119]\n"
    "v_exp_f32 v61, v61\n"
    "v_mfma_f32_16x16x32_bf16 a[112:115], v[172:175], v[144:147], a[112:115]\n"
    "v_add_u32 v220, 1, v220\n"
    "v_add_u32 v207, s93, v207\n"
    "v_mfma_f32_16x16x32_bf16 a[112:115], v[168:171], v[128:131], a[112:115]\n"
    "s_waitcnt lgkmcnt(2)\n"
    "v_add_f32 v122, v122, v89\n"
    "v_add_f32 v114, v114, v51\n"
    "v_mfma_f32_16x16x32_bf16 a[128:131], v[176:179], v[128:131], a[128:131]\n"
    "s_add_u32 s64, s64, 0xc000\n"
    "ds_read_b128 v[168:171], v201 offset:45056\n"
    "v_add_f32 v121, v121, v90\n"
    "v_mfma_f32_16x16x32_bf16 a[128:131], v[180:183], v[144:147], a[128:131]\n"
    "s_add_u32 s62, s62, 0x6000\n"
    "v_add_f32 v117, v117, v52\n"
    "v_add_f32 v122, v122, v91\n"
    "v_mfma_f32_16x16x32_bf16 a[132:135], v[180:183], v[148:151], a[132:135]\n"
    "s_add_u32 s63, s63, 0x6000\n"
    "v_exp_f32 v62, v62\n"
    "v_mfma_f32_16x16x32_bf16 a[132:135], v[176:179], v[132:135], a[132:135]\n"
    "s_add_u32 s60, s60, 0x6000\n"
    "v_add_f32 v118, v118, v53\n"
    "v_add_f32 v125, v125, v92\n"
    "v_mfma_f32_16x16x32_bf16 a[136:139], v[176:179], v[136:139], a[136:139]\n"
    "s_add_u32 s61, s61, 0x6000\n"
    "ds_read_b128 v[172:175], v201 offset:46080\n"
    "v_add_f32 v117, v117, v54\n"
    "v_mfma_f32_16x16x32_bf16 a[136:139], v[180:183], v[152:155], a[136:139]\n"
    "s_add_u32 s66, s66, s74\n"
    "v_exp_f32 v63, v63\n"
    "v_mfma_f32_16x16x32_bf16 a[140:143], v[180:183], v[156:159], a[140:143]\n"
    "s_min_u32 s66, s66, 33\n"
    "v_add_f32 v126, v126, v93\n"
    "v_add_f32 v118, v118, v55\n"
    "v_mfma_f32_16x16x32_bf16 a[140:143], v[176:179], v[140:143], a[140:143]\n"
    "s_waitcnt lgkmcnt(2)\n"
    "v_add_f32 v125, v125, v94\n"
    "v_add_f32 v121, v121, v56\n"
    "v_mfma_f32_16x16x32_bf16 a[156:159], v[160:163], v[140:143], a[156:159]\n"
    "ds_read_b128 v[176:179], v201 offset:47104\n"
    "v_add_f32 v126, v126, v95\n"
    "v_mfma_f32_16x16x32_bf16 a[156:159], v[164:167], v[156:159], a[156:159]\n"
    "s_waitcnt vmcnt(3)\n"
    "ds_read_b128 v[180:183], v201 offset:48128\n"
    "v_add_f32 v122, v122, v57\n"
    "v_mfma_f32_16x16x32_bf16 a[152:155], v[164:167], v[152:155], a[152:155]\n"
    "v_cmp_ne_u32 s[80:81], s67, v219\n"
    "v_mfma_f32_16x16x32_bf16 a[152:155], v[160:163], v[136:139], a[152:155]\n"
    "v_cmp_gt_u32 vcc, s66, v218\n"
    "v_mfma_f32_16x16x32_bf16 a[148:151], v[160:163], v[132:135], a[148:151]\n"
    "v_mfma_f32_16x16x32_bf16 a[148:151], v[164:167], v[148:151], a[148:151]\n"
    "s_or_b64 vcc, vcc, s[80:81]\n"
    "s_cbranch_vccnz LSPRE3\n"
    "LSPB3:\n"
    "v_mfma_f32_16x16x32_bf16 a[144:147], v[164:167], v[144:147], a[144:147]\n"
    "v_mfma_f32_16x16x32_bf16 a[144:147], v[160:163], v[128:131], a[144:147]\n"
    "s_waitcnt lgkmcnt(0)\n"
    "v_mfma_f32_16x16x32_bf16 a[160:163], v[168:171], v[128:131], a[160:163]\n"
    "s_barrier\n"
    "LMID:\n"
    "v_mfma_f32_16x16x32_bf16 a[160:163], v[172:175], v[144:147], a[160:163]\n"
    "buffer_store_dwordx2 v[220:221], v214, s[44:47], 0 offen sc1\n"
    "v_mfma_f32_16x16x32_bf16 a[164:167], v[172:175], v[148:151], a[164:167]\n"
    "ds_read_b128 v[160:163], v200 offset:24576\n"
    "ds_read_u16_d16_hi v223, v209 offset:6336\n"
    "v_mfma_f32_16x16x32_bf16 a[164:167], v[168:171], v[132:135], a[164:167]\n"
    "ds_read_u16_d16_hi v222, v209 offset:192\n"
    "v_add_f32 v121, v121, v58\n"
    "v_mfma_f32_16x16x32_bf16 a[168:171], v[168:171], v[136:139], a[168:171]\n"
    "ds_read_b128 v[164:167], v200 offset:25600\n"
    "ds_read_b128 v[232:235], v205 offset:24576\n"
    "v_mfma_f32_16x16x32_bf16 a[168:171], v[172:175], v[152:155], a[168:171]\n"
    "v_add_f32 v122, v122, v59\n"
    "ds_read_b128 v[236:239], v206 offset:24576\n"
    "v_mfma_f32_16x16x32_bf16 a[172:175], v[172:175], v[156:159], a[172:175]\n"
    "v_add_f32 v125, v125, v60\n"
    "v_add_f32 v126, v126, v61\n"
    "v_mfma_f32_16x16x32_bf16 a[172:175], v[168:171], v[140:143], a[172:175]\n"
    "v_add_f32 v125, v125, v62\n"
    "v_add_f32 v126, v126, v63\n"
    "v_mfma_f32_16x16x32_bf16 a[188:191], v[176:179], v[140:143], a[188:191]\n"
    "ds_read_b128 v[168:171], v200 offset:26624\n"
    "ds_read_b64 v[228:229], v210 offset:64\n"
    "v_mfma_f32_16x16x32_bf16 a[188:191], v[180:183], v[156:159], a[188:191]\n"
    "ds_read2st64_b32 v[240:241], v207 offset0:96 offset1:99\n"
    "ds_read2st64_b32 v[242:243], v207 offset0:102 offset1:105\n"
    "v_mfma_f32_16x16x32_bf16 a[184:187], v[176:179], v[136:139], a[184:187]\n"
    "ds_read2st64_b32 v[244:245], v207 offset0:108 offset1:111\n"
    "ds_read2st64_b32 v[246:247], v207 offset0:114 offset1:117\n"
    "v_mfma_f32_16x16x32_bf16 a[184:187], v[180:183], v[152:155], a[184:187]\n"
    "v_cvt_pk_bf16_f32 v140, v44, v45\n"
    "v_cvt_pk_bf16_f32 v141, v46, v47\n"
    "v_mfma_f32_16x16x32_bf16 a[180:183], v[176:179], v[132:135], a[180:183]\n"
    "s_add_u32 s59, s59, 1\n"
    "ds_read_b128 v[172:175], v200 offset:27648\n"
    "s_waitcnt lgkmcnt(7)\n"
    "v_mfma_f32_16x16x32_bf16 a[180:183], v[180:183], v[148:151], a[180:183]\n"
    "v_mul_f32 v226, s75, v223\n"
    "v_fma_f32 v227, -v222, v226, v217\n"
    "v_mfma_f32_16x16x32_bf16 a[176:179], v[176:179], v[128:131], a[176:179]\n"
    "s_cmp_lt_u32 s59, 15\n"
    "v_cvt_f32_i32 v232, v232\n"
    "v_cvt_f32_i32 v233, v233\n"
    "v_mfma_f32_16x16x32_bf16 a[176:179], v[180:183], v[144:147], a[176:179]\n"
    "s_cbranch_scc1 LLOOP\n"
    "LTAIL:\n"
    "s_waitcnt lgkmcnt(0)\n"
    "s_nop 1\n"
    "v_mfma_f32_16x16x32_bf16 v[64:67], v[160:163], a[192:195], v[96:99]\n"
    "ds_read_b128 v[176:179], v200 offset:28672\n"
    "v_cvt_pk_bf16_f32 v128, v32, v33\n"
    "v_mfma_f32_16x16x32_bf16 v[64:67], v[164:167], a[196:199], v[64:67]\n"
    "s_add_u32 m0, s71, 0\n"
    "v_cvt_pk_bf16_f32 v129, v34, v35\n"
    "v_cvt_pk_bf16_f32 v132, v36, v37\n"
    "v_mfma_f32_16x16x32_bf16 v[68:71], v[164:167], a[220:223], v[100:103]\n"
    "buffer_load_dwordx4 v200, s[36:39], s60 offen sc1 lds\n"
    "v_mfma_f32_16x16x32_bf16 v[68:71], v[160:163], a[216:219], v[68:71]\n"
    "v_cvt_pk_bf16_f32 v133, v38, v39\n"
    "v_cvt_pk_bf16_f32 v136, v40, v41\n"
    "v_mfma_f32_16x16x32_bf16 v[72:75], v[160:163], a[240:243], v[104:107]\n"
    "ds_read_b128 v[180:183], v200 offset:29696\n"
    "v_cvt_pk_bf16_f32 v137, v42, v43\n"
    "v_mfma_f32_16x16x32_bf16 v[72:75], v[164:167], a[244:247], v[72:75]\n"
    "v_cvt_pk_bf16_f32 v130, v48, v49\n"
    "v_cvt_pk_bf16_f32 v131, v50, v51\n"
    "v_mfma_f32_16x16x32_bf16 v[76:79], v[164:167], v[12:15], v[108:111]\n"
    "buffer_load_dwordx4 v200, s[36:39], s60 offen offset:1024 sc1 lds\n"
    "v_mfma_f32_16x16x32_bf16 v[76:79], v[160:163], v[8:11], v[76:79]\n"
    "v_cvt_pk_bf16_f32 v134, v52, v53\n"
    "v_cvt_pk_bf16_f32 v135, v54, v55\n"
    "v_mfma_f32_16x16x32_bf16 v[76:79], v[168:171], v[16:19], v[76:79]\n"
    "ds_read_b128 v[160:163], v200 offset:34816\n"
    "v_cvt_pk_bf16_f32 v138, v56, v57\n"
    "v_mfma_f32_16x16x32_bf16 v[76:79], v[172:175], v[20:23], v[76:79]\n"
    "v_cvt_pk_bf16_f32 v139, v58, v59\n"
    "v_cvt_pk_bf16_f32 v142, v60, v61\n"
    "v_mfma_f32_16x16x32_bf16 v[72:75], v[172:175], a[252:255], v[72:75]\n"
    "buffer_load_dwordx4 v200, s[36:39], s60 offen offset:2048 sc1 lds\n"
    "v_mfma_f32_16x16x32_bf16 v[72:75], v[168:171], a[248:251], v[72:75]\n"
    "v_cvt_pk_bf16_f32 v143, v62, v63\n"
    "v_add_f32 v113, v113, v114\n"
    "v_mfma_f32_16x16x32_bf16 v[68:71], v[168:171], a[224:227], v[68:71]\n"
    "ds_read_b128 v[164:167], v200 offset:35840\n"
    "v_add_f32 v117, v117, v118\n"
    "v_mfma_f32_16x16x32_bf16 v[68:71], v[172:175], a[228:231], v[68:71]\n"
    "v_add_f32 v121, v121, v122\n"
    "v_add_f32 v125, v125, v126\n"
    "v_mfma_f32_16x16x32_bf16 v[64:67], v[172:175], a[204:207], v[64:67]\n"
    "buffer_load_dwordx4 v200, s[36:39], s60 offen offset:3072 sc1 lds\n"
    "v_mfma_f32_16x16x32_bf16 v[64:67], v[168:171], a[200:203], v[64:67]\n"
    "s_waitcnt lgkmcnt(2)\n"
    "v_mov_b32 v115, v113\n"
    "v_mov_b32 v119, v117\n"
    "v_mfma_f32_16x16x32_bf16 v[64:67], v[176:179], a[208:211], v[64:67]\n"
    "s_add_u32 s65, s60, 0x1000\n"
    "ds_read_b128 v[168:171], v200 offset:32768\n"
    "v_mov_b32 v123, v121\n"
    "v_mfma_f32_16x16x32_bf16 v[64:67], v[180:183], a[212:215], v[64:67]\n"
    "s_add_u32 m0, s71, 0x1000\n"
    "v_mov_b32 v127, v125\n"
    "v_permlane32_swap_b32 v113, v115\n"
    "v_mfma_f32_16x16x32_bf16 v[68:71], v[180:183], a[236:239], v[68:71]\n"
    "buffer_load_dwordx4 v200, s[36:39], s65 offen sc1 lds\n"
    "v_mfma_f32_16x16x32_bf16 v[68:71], v[176:179], a[232:235], v[68:71]\n"
    "v_permlane32_swap_b32 v117, v119\n"
    "v_permlane32_swap_b32 v121, v123\n"
    "v_mfma_f32_16x16x32_bf16 v[72:75], v[176:179], v[0:3], v[72:75]\n"
    "ds_read_b128 v[172:175], v200 offset:33792\n"
    "v_permlane32_swap_b32 v125, v127\n"
    "v_mfma_f32_16x16x32_bf16 v[72:75], v[180:183], v[4:7], v[72:75]\n"
    "v_add_f32 v113, v113, v115\n"
    "v_add_f32 v117, v117, v119\n"
    "v_mfma_f32_16x16x32_bf16 v[76:79], v[180:183], v[28:31], v[76:79]\n"
    "buffer_load_dwordx4 v200, s[36:39], s65 offen offset:1024 sc1 lds\n"
    "v_mfma_f32_16x16x32_bf16 v[76:79], v[176:179], v[24:27], v[76:79]\n"
    "s_waitcnt lgkmcnt(2)\n"
    "v_add_f32 v121, v121, v123\n"
    "v_add_f32 v125, v125, v127\n"
    "v_mfma_f32_16x16x32_bf16 v[92:95], v[160:163], v[24:27], v[108:111]\n"
    "ds_read_b128 v[176:179], v200 offset:30720\n"
    "v_mov_b32 v115, v113\n"
    "v_mfma_f32_16x16x32_bf16 v[92:95], v[164:167], v[28:31], v[92:95]\n"
    "s_add_u32 m0, s71, 0x12000\n"
    "v_exp_f32 v64, v64\n"
    "v_mfma_f32_16x16x32_bf16 v[88:91], v[164:167], v[4:7], v[104:107]\n"
    "buffer_load_dwordx4 v200, s[40:43], s61 offen sc1 lds\n"
    "v_mfma_f32_16x16x32_bf16 v[88:91], v[160:163], v[0:3], v[88:91]\n"
    "v_exp_f32 v65, v65\n"
    "v_mfma_f32_16x16x32_bf16 v[84:87], v[160:163], a[232:235], v[100:103]\n"
    "ds_read_b128 v[180:183], v200 offset:31744\n"
    "v_cvt_pk_bf16_f32 v144, v64, v65\n"
    "v_mfma_f32_16x16x32_bf16 v[84:87], v[164:167], a[236:239], v[84:87]\n"
    "v_exp_f32 v66, v66\n"
    "v_mfma_f32_16x16x32_bf16 v[80:83], v[164:167], a[212:215], v[96:99]\n"
    "buffer_load_dwordx4 v200, s[40:43], s61 offen offset:1024 sc1 lds\n"
    "v_mfma_f32_16x16x32_bf16 v[80:83], v[160:163], a[208:211], v[80:83]\n"
    "s_waitcnt lgkmcnt(2)\n"
    "v_exp_f32 v67, v67\n"
    "v_mfma_f32_16x16x32_bf16 v[80:83], v[168:171], a[200:203], v[80:83]\n"
    "ds_read_b128 v[160:163], v200 offset:36864\n"
    "v_cvt_pk_bf16_f32 v145, v66, v67\n"
    "v_mfma_f32_16x16x32_bf16 v[80:83], v[172:175], a[204:207], v[80:83]\n"
    "v_exp_f32 v68, v68\n"
    "v_mfma_f32_16x16x32_bf16 v[84:87], v[172:175], a[228:231], v[84:87]\n"
    "buffer_load_dwordx4 v200, s[40:43], s61 offen offset:2048 sc1 lds\n"
    "v_mfma_f32_16x16x32_bf16 v[84:87], v[168:171], a[224:227], v[84:87]\n"
    "v_exp_f32 v69, v69\n"
    "v_mfma_f32_16x16x32_bf16 v[88:91], v[168:171], a[248:251], v[88:91]\n"
    "ds_read_b128 v[164:167], v200 offset:37888\n"
    "v_cvt_pk_bf16_f32 v148, v68, v69\n"
    "v_mfma_f32_16x16x32_bf16 v[88:91], v[172:175], a[252:255], v[88:91]\n"
    "v_exp_f32 v70, v70\n"
    "v_mfma_f32_16x16x32_bf16 v[92:95], v[172:175], v[20:23], v[92:95]\n"
    "buffer_load_dwordx4 v200, s[40:43], s61 offen offset:3072 sc1 lds\n"
    "v_mfma_f32_16x16x32_bf16 v[92:95], v[168:171], v[16:19], v[92:95]\n"
    "s_waitcnt lgkmcnt(2)\n"
    "v_exp_f32 v71, v71\n"
    "v_mfma_f32_16x16x32_bf16 v[92:95], v[176:179], v[8:11], v[92:95]\n"
    "s_add_u32 s65, s61, 0x1000\n"
    "ds_read_b128 v[168:171], v200 offset:38912\n"
    "v_cvt_pk_bf16_f32 v149, v70, v71\n"
    "v_mfma_f32_16x16x32_bf16 v[92:95], v[180:183], v[12:15], v[92:95]\n"
    "s_add_u32 m0, s71, 0x13000\n"
    "v_exp_f32 v72, v72\n"
    "v_mfma_f32_16x16x32_bf16 v[88:91], v[180:183], a[244:247], v[88:91]\n"
    "buffer_load_dwordx4 v200, s[40:43], s65 offen sc1 lds\n"
    "v_mfma_f32_16x16x32_bf16 v[88:91], v[176:179], a[240:243], v[88:91]\n"
    "v_exp_f32 v73, v73\n"
    "v_mfma_f32_16x16x32_bf16 v[84:87], v[176:179], a[216:219], v[84:87]\n"
    "ds_read_b128 v[172:175], v200 offset:39936\n"
    "v_cvt_pk_bf16_f32 v152, v72, v73\n"
    "v_mfma_f32_16x16x32_bf16 v[84:87], v[180:183], a[220:223], v[84:87]\n"
    "v_exp_f32 v74, v74\n"
    "v_mfma_f32_16x16x32_bf16 v[80:83], v[180:183], a[196:199], v[80:83]\n"
    "buffer_load_dwordx4 v200, s[40:43], s65 offen offset:1024 sc1 lds\n"
    "v_mfma_f32_16x16x32_bf16 v[80:83], v[176:179], a[192:195], v[80:83]\n"
    "s_waitcnt lgkmcnt(2)\n"
    "v_exp_f32 v75, v75\n"
    "v_mfma_f32_16x16x32_bf16 v[32:35], v[160:163], a[192:195], v[96:99]\n"
    "ds_read_b128 v[176:179], v200 offset:40960\n"
    "v_cvt_pk_bf16_f32 v153, v74, v75\n"
    "v_mfma_f32_16x16x32_bf16 v[32:35], v[164:167], a[196:199], v[32:35]\n"
    "v_exp_f32 v76, v76\n"
    "v_mfma_f32_16x16x32_bf16 v[36:39], v[164:167], a[220:223], v[100:103]\n"
    "v_exp_f32 v77, v77\n"
    "v_mfma_f32_16x16x32_bf16 v[36:39], v[160:163], a[216:219], v[36:39]\n"
    "v_exp_f32 v78, v78\n"
    "v_mfma_f32_16x16x32_bf16 v[40:43], v[160:163], a[240:243], v[104:107]\n"
    "ds_read_b128 v[180:183], v200 offset:41984\n"
    "v_cvt_pk_bf16_f32 v156, v76, v77\n"
    "v_mfma_f32_16x16x32_bf16 v[40:43], v[164:167], a[244:247], v[40:43]\n"
    "v_exp_f32 v79, v79\n"
    "v_mfma_f32_16x16x32_bf16 v[44:47], v[164:167], v[12:15], v[108:111]\n"
    "v_exp_f32 v80, v80\n"
    "v_mfma_f32_16x16x32_bf16 v[44:47], v[160:163], v[8:11], v[44:47]\n"
    "s_waitcnt lgkmcnt(2)\n"
    "v_exp_f32 v81, v81\n"
    "v_mfma_f32_16x16x32_bf16 v[44:47], v[168:171], v[16:19], v[44:47]\n"
    "ds_read_b128 v[160:163], v200 offset:47104\n"
    "v_cvt_pk_bf16_f32 v157, v78, v79\n"
    "v_mfma_f32_16x16x32_bf16 v[44:47], v[172:175], v[20:23], v[44:47]\n"
    "v_exp_f32 v82, v82\n"
    "v_mfma_f32_16x16x32_bf16 v[40:43], v[172:175], a[252:255], v[40:43]\n"
    "v_exp_f32 v83, v83\n"
    "v_mfma_f32_16x16x32_bf16 v[40:43], v[168:171], a[248:251], v[40:43]\n"
    "v_exp_f32 v84, v84\n"
    "v_mfma_f32_16x16x32_bf16 v[36:39], v[168:171], a[224:227], v[36:39]\n"
    "ds_read_b128 v[164:167], v200 offset:48128\n"
    "v_cvt_pk_bf16_f32 v146, v80, v81\n"
    "v_mfma_f32_16x16x32_bf16 v[36:39], v[172:175], a[228:231], v[36:39]\n"
    "v_exp_f32 v85, v85\n"
    "v_mfma_f32_16x16x32_bf16 v[32:35], v[172:175], a[204:207], v[32:35]\n"
    "v_exp_f32 v86, v86\n"
    "v_mfma_f32_16x16x32_bf16 v[32:35], v[168:171], a[200:203], v[32:35]\n"
    "s_waitcnt lgkmcnt(2)\n"
    "v_exp_f32 v87, v87\n"
    "v_mfma_f32_16x16x32_bf16 v[32:35], v[176:179], a[208:211], v[32:35]\n"
    "ds_read_b128 v[168:171], v200 offset:45056\n"
    "v_cvt_pk_bf16_f32 v147, v82, v83\n"
    "v_mfma_f32_16x16x32_bf16 v[32:35], v[180:183], a[212:215], v[32:35]\n"
    "v_exp_f32 v88, v88\n"
    "v_mfma_f32_16x16x32_bf16 v[36:39], v[180:183], a[236:239], v[36:39]\n"
    "v_exp_f32 v89, v89\n"
    "v_mfma_f32_16x16x32_bf16 v[36:39], v[176:179], a[232:235], v[36:39]\n"
    "v_exp_f32 v90, v90\n"
    "v_mfma_f32_16x16x32_bf16 v[40:43], v[176:179], v[0:3], v[40:43]\n"
    "ds_read_b128 v[172:175], v200 offset:46080\n"
    "v_cvt_pk_bf16_f32 v150, v84, v85\n"
    "v_mfma_f32_16x16x32_bf16 v[40:43], v[180:183], v[4:7], v[40:43]\n"
    "v_exp_f32 v91, v91\n"
    "v_mfma_f32_16x16x32_bf16 v[44:47], v[180:183], v[28:31], v[44:47]\n"
    "v_exp_f32 v92, v92\n"
    "v_mfma_f32_16x16x32_bf16 v[44:47], v[176:179], v[24:27], v[44:47]\n"
    "s_waitcnt lgkmcnt(2)\n"
    "v_exp_f32 v93, v93\n"
    "v_mfma_f32_16x16x32_bf16 v[60:63], v[160:163], v[24:27], v[108:111]\n"
    "ds_read_b128 v[176:179], v200 offset:43008\n"
    "v_cvt_pk_bf16_f32 v151, v86, v87\n"
    "v_mfma_f32_16x16x32_bf16 v[60:63], v[164:167], v[28:31], v[60:63]\n"
    "v_exp_f32 v94, v94\n"
    "v_mfma_f32_16x16x32_bf16 v[56:59], v[164:167], v[4:7], v[104:107]\n"
    "v_exp_f32 v95, v95\n"
    "v_mfma_f32_16x16x32_bf16 v[56:59], v[160:163], v[0:3], v[56:59]\n"
    "v_exp_f32 v32, v32\n"
    "v_mfma_f32_16x16x32_bf16 v[52:55], v[160:163], a[232:235], v[100:103]\n"
    "ds_read_b128 v[180:183], v200 offset:44032\n"
    "v_cvt_pk_bf16_f32 v154, v88, v89\n"
    "v_mfma_f32_16x16x32_bf16 v[52:55], v[164:167], a[236:239], v[52:55]\n"
    "v_exp_f32 v33, v33\n"
    "v_mfma_f32_16x16x32_bf16 v[48:51], v[164:167], a[212:215], v[96:99]\n"
    "s_waitcnt vmcnt(6)\n"
    "v_exp_f32 v34, v34\n"
    "v_mfma_f32_16x16x32_bf16 v[48:51], v[160:163], a[208:211], v[48:51]\n"
    "s_waitcnt lgkmcnt(2)\n"
    "v_exp_f32 v35, v35\n"
    "v_mfma_f32_16x16x32_bf16 v[48:51], v[168:171], a[200:203], v[48:51]\n"
    "s_barrier\n"
    "v_exp_f32 v36, v36\n"
    "v_mfma_f32_16x16x32_bf16 v[48:51], v[172:175], a[204:207], v[48:51]\n"
    "ds_read_b128 v[160:163], v200\n"
    "v_cvt_pk_bf16_f32 v155, v90, v91\n"
    "v_mfma_f32_16x16x32_bf16 v[52:55], v[172:175], a[228:231], v[52:55]\n"
    "v_exp_f32 v37, v37\n"
    "v_mfma_f32_16x16x32_bf16 v[52:55], v[168:171], a[224:227], v[52:55]\n"
    "v_exp_f32 v38, v38\n"
    "v_mfma_f32_16x16x32_bf16 v[56:59], v[168:171], a[248:251], v[56:59]\n"
    "ds_read_b128 v[164:167], v200 offset:1024\n"
    "v_cvt_pk_bf16_f32 v158, v92, v93\n"
    "v_mfma_f32_16x16x32_bf16 v[56:59], v[172:175], a[252:255], v[56:59]\n"
    "buffer_load_dwordx2 v[218:219], v213, s[44:47], 0 offen sc1\n"
    "v_mfma_f32_16x16x32_bf16 v[60:63], v[172:175], v[20:23], v[60:63]\n"
    "v_exp_f32 v39, v39\n"
    "v_mfma_f32_16x16x32_bf16 v[60:63], v[168:171], v[16:19], v[60:63]\n"
    "s_waitcnt lgkmcnt(2)\n"
    "v_exp_f32 v40, v40\n"
    "v_mfma_f32_16x16x32_bf16 v[60:63], v[176:179], v[8:11], v[60:63]\n"
    "ds_read_b128 v[168:171], v200 offset:2048\n"
    "v_cvt_pk_bf16_f32 v159, v94, v95\n"
    "v_mfma_f32_16x16x32_bf16 v[60:63], v[180:183], v[12:15], v[60:63]\n"
    "v_exp_f32 v41, v41\n"
    "v_mfma_f32_16x16x32_bf16 v[56:59], v[180:183], a[244:247], v[56:59]\n"
    "v_exp_f32 v42, v42\n"
    "v_mfma_f32_16x16x32_bf16 v[56:59], v[176:179], a[240:243], v[56:59]\n"
    "v_exp_f32 v43, v43\n"
    "v_mfma_f32_16x16x32_bf16 v[52:55], v[176:179], a[216:219], v[52:55]\n"
    "ds_read_b128 v[172:175], v200 offset:3072\n"
    "v_cvt_pk_bf16_f32 v32, v32, v33\n"
    "v_mfma_f32_16x16x32_bf16 v[52:55], v[180:183], a[220:223], v[52:55]\n"
    "v_exp_f32 v44, v44\n"
    "v_mfma_f32_16x16x32_bf16 v[48:51], v[180:183], a[196:199], v[48:51]\n"
    "v_exp_f32 v45, v45\n"
    "v_mfma_f32_16x16x32_bf16 v[48:51], v[176:179], a[192:195], v[48:51]\n"
    "s_waitcnt lgkmcnt(2)\n"
    "v_exp_f32 v46, v46\n"
    "v_mfma_f32_16x16x32_bf16 v[64:67], v[160:163], a[192:195], v[96:99]\n"
    "ds_read_b128 v[176:179], v200 offset:4096\n"
    "v_cvt_pk_bf16_f32 v33, v34, v35\n"
    "v_mfma_f32_16x16x32_bf16 v[64:67], v[164:167], a[196:199], v[64:67]\n"
    "v_exp_f32 v47, v47\n"
    "v_mfma_f32_16x16x32_bf16 v[68:71], v[164:167], a[220:223], v[100:103]\n"
    "v_cvt_pk_bf16_f32 v36, v36, v37\n"
    "v_cvt_pk_bf16_f32 v37, v38, v39\n"
    "v_mfma_f32_16x16x32_bf16 v[68:71], v[160:163], a[216:219], v[68:71]\n"
    "v_cvt_pk_bf16_f32 v40, v40, v41\n"
    "v_cvt_pk_bf16_f32 v41, v42, v43\n"
    "v_mfma_f32_16x16x32_bf16 v[72:75], v[160:163], a[240:243], v[104:107]\n"
    "ds_read_b128 v[180:183], v200 offset:5120\n"
    "v_cvt_pk_bf16_f32 v44, v44, v45\n"
    "v_mfma_f32_16x16x32_bf16 v[72:75], v[164:167], a[244:247], v[72:75]\n"
    "v_cvt_pk_bf16_f32 v45, v46, v47\n"
    "v_mov_b32 v119, v117\n"
    "v_mfma_f32_16x16x32_bf16 v[76:79], v[164:167], v[12:15], v[108:111]\n"
    "v_exp_f32 v48, v48\n"
    "v_mfma_f32_16x16x32_bf16 v[76:79], v[160:163], v[8:11], v[76:79]\n"
    "s_waitcnt lgkmcnt(2)\n"
    "v_exp_f32 v49, v49\n"
    "v_mfma_f32_16x16x32_bf16 v[76:79], v[168:171], v[16:19], v[76:79]\n"
    "ds_read_b128 v[160:163], v200 offset:10240\n"
    "v_cvt_pk_bf16_f32 v34, v48, v49\n"
    "v_mfma_f32_16x16x32_bf16 v[76:79], v[172:175], v[20:23], v[76:79]\n"
    "v_exp_f32 v50, v50\n"
    "v_mfma_f32_16x16x32_bf16 v[72:75], v[172:175], a[252:255], v[72:75]\n"
    "v_exp_f32 v51, v51\n"
    "v_mfma_f32_16x16x32_bf16 v[72:75], v[168:171], a[248:251], v[72:75]\n"
    "v_exp_f32 v52, v52\n"
    "v_mfma_f32_16x16x32_bf16 v[68:71], v[168:171], a[224:227], v[68:71]\n"
    "ds_read_b128 v[164:167], v200 offset:11264\n"
    "v_cvt_pk_bf16_f32 v35, v50, v51\n"
    "v_mfma_f32_16x16x32_bf16 v[68:71], v[172:175], a[228:231], v[68:71]\n"
    "v_exp_f32 v53, v53\n"
    "v_mfma_f32_16x16x32_bf16 v[64:67], v[172:175], a[204:207], v[64:67]\n"
    "v_exp_f32 v54, v54\n"
    "v_mfma_f32_16x16x32_bf16 v[64:67], v[168:171], a[200:203], v[64:67]\n"
    "s_waitcnt lgkmcnt(2)\n"
    "v_exp_f32 v55, v55\n"
    "v_mfma_f32_16x16x32_bf16 v[64:67], v[176:179], a[208:211], v[64:67]\n"
    "s_add_u32 s66, s66, s74\n"
    "ds_read_b128 v[168:171], v200 offset:8192\n"
    "v_cvt_pk_bf16_f32 v38, v52, v53\n"
    "v_mfma_f32_16x16x32_bf16 v[64:67], v[180:183], a[212:215], v[64:67]\n"
    "s_add_u32 s60, s72, 0xbd000\n"
    "v_exp_f32 v56, v56\n"
    "v_mfma_f32_16x16x32_bf16 v[68:71], v[180:183], a[236:239], v[68:71]\n"
    "s_add_u32 s61, s71, 0xba000\n"
    "v_exp_f32 v57, v57\n"
    "v_mfma_f32_16x16x32_bf16 v[68:71], v[176:179], a[232:235], v[68:71]\n"
    "v_exp_f32 v58, v58\n"
    "v_mfma_f32_16x16x32_bf16 v[72:75], v[176:179], v[0:3], v[72:75]\n"
    "ds_read_b128 v[172:175], v200 offset:9216\n"
    "v_cvt_pk_bf16_f32 v39, v54, v55\n"
    "v_mfma_f32_16x16x32_bf16 v[72:75], v[180:183], v[4:7], v[72:75]\n"
    "v_exp_f32 v59, v59\n"
    "v_mfma_f32_16x16x32_bf16 v[76:79], v[180:183], v[28:31], v[76:79]\n"
    "s_waitcnt vmcnt(0)\n"
    "v_cmp_ne_u32 s[80:81], s67, v219\n"
    "v_cvt_pk_bf16_f32 v42, v56, v57\n"
    "v_mfma_f32_16x16x32_bf16 v[76:79], v[176:179], v[24:27], v[76:79]\n"
    "s_waitcnt lgkmcnt(2)\n"
    "v_cmp_gt_u32 vcc, s66, v218\n"
    "v_cvt_pk_bf16_f32 v43, v58, v59\n"
    "v_mfma_f32_16x16x32_bf16 v[92:95], v[160:163], v[24:27], v[108:111]\n"
    "ds_read_b128 v[176:179], v200 offset:6144\n"
    "v_mov_b32 v123, v121\n"
    "v_mfma_f32_16x16x32_bf16 v[92:95], v[164:167], v[28:31], v[92:95]\n"
    "s_or_b64 vcc, vcc, s[80:81]\n"
    "v_exp_f32 v64, v64\n"
    "v_mfma_f32_16x16x32_bf16 v[88:91], v[164:167], v[4:7], v[104:107]\n"
    "s_cbranch_vccnz LSPRE5\n"
    "LSPB5:\n"
    "v_exp_f32 v65, v65\n"
    "v_mfma_f32_16x16x32_bf16 v[88:91], v[160:163], v[0:3], v[88:91]\n"
    "v_exp_f32 v66, v66\n"
    "v_mfma_f32_16x16x32_bf16 v[84:87], v[160:163], a[232:235], v[100:103]\n"
    "s_barrier\n"
    "ds_read_b128 v[180:183], v200 offset:7168\n"
    "v_cvt_pk_bf16_f32 v64, v64, v65\n"
    "v_mfma_f32_16x16x32_bf16 v[84:87], v[164:167], a[236:239], v[84:87]\n"
    "s_add_u32 m0, s72, 0x6000\n"
    "v_exp_f32 v67, v67\n"
    "v_mfma_f32_16x16x32_bf16 v[80:83], v[164:167], a[212:215], v[96:99]\n"
    "buffer_load_dwordx4 v200, s[36:39], s60 offen sc1 lds\n"
    "v_mfma_f32_16x16x32_bf16 v[80:83], v[160:163], a[208:211], v[80:83]\n"
    "s_waitcnt lgkmcnt(2)\n"
    "v_exp_f32 v68, v68\n"
    "v_mfma_f32_16x16x32_bf16 v[80:83], v[168:171], a[200:203], v[80:83]\n"
    "ds_read_b128 v[160:163], v200 offset:12288\n"
    "v_cvt_pk_bf16_f32 v65, v66, v67\n"
    "v_mfma_f32_16x16x32_bf16 v[80:83], v[172:175], a[204:207], v[80:83]\n"
    "buffer_load_dwordx4 v200, s[36:39], s60 offen offset:1024 sc1 lds\n"
    "v_mfma_f32_16x16x32_bf16 v[84:87], v[172:175], a[228:231], v[84:87]\n"
    "v_exp_f32 v69, v69\n"
    "v_mfma_f32_16x16x32_bf16 v[84:87], v[168:171], a[224:227], v[84:87]\n"
    "buffer_load_dwordx4 v200, s[36:39], s60 offen offset:2048 sc1 lds\n"
    "v_mfma_f32_16x16x32_bf16 v[88:91], v[168:171], a[248:251], v[88:91]\n"
    "ds_read_b128 v[164:167], v200 offset:13312\n"
    "v_cvt_pk_bf16_f32 v68, v68, v69\n"
    "v_mfma_f32_16x16x32_bf16 v[88:91], v[172:175], a[252:255], v[88:91]\n"
    "v_exp_f32 v60, v60\n"
    "v_mfma_f32_16x16x32_bf16 v[92:95], v[172:175], v[20:23], v[92:95]\n"
    "s_add_u32 m0, s71, 0x18000\n"
    "v_exp_f32 v70, v70\n"
    "v_mfma_f32_16x16x32_bf16 v[92:95], v[168:171], v[16:19], v[92:95]\n"
    "s_waitcnt lgkmcnt(2)\n"
    "buffer_load_dwordx4 v200, s[40:43], s61 offen sc1 lds\n"
    "v_mfma_f32_16x16x32_bf16 v[92:95], v[176:179], v[8:11], v[92:95]\n"
    "ds_read_b128 v[168:171], v200 offset:14336\n"
    "v_mov_b32 v127, v125\n"
    "v_mfma_f32_16x16x32_bf16 v[92:95], v[180:183], v[12:15], v[92:95]\n"
    "v_exp_f32 v61, v61\n"
    "v_mfma_f32_16x16x32_bf16 v[88:91], v[180:183], a[244:247], v[88:91]\n"
    "v_exp_f32 v71, v71\n"
    "v_mfma_f32_16x16x32_bf16 v[88:91], v[176:179], a[240:243], v[88:91]\n"
    "v_exp_f32 v62, v62\n"
    "v_mfma_f32_16x16x32_bf16 v[84:87], v[176:179], a[216:219], v[84:87]\n"
    "ds_read_b128 v[172:175], v200 offset:15360\n"
    "v_cvt_pk_bf16_f32 v46, v60, v61\n"
    "v_mfma_f32_16x16x32_bf16 v[84:87], v[180:183], a[220:223], v[84:87]\n"
    "v_exp_f32 v72, v72\n"
    "v_mfma_f32_16x16x32_bf16 v[80:83], v[180:183], a[196:199], v[80:83]\n"
    "v_exp_f32 v63, v63\n"
    "v_mfma_f32_16x16x32_bf16 v[80:83], v[176:179], a[192:195], v[80:83]\n"
    "s_waitcnt lgkmcnt(2)\n"
    "buffer_load_dwordx4 v200, s[40:43], s61 offen offset:1024 sc1 lds\n"
    "v_mfma_f32_16x16x32_bf16 v[232:235], v[160:163], a[192:195], v[96:99]\n"
    "ds_read_b128 v[176:179], v200 offset:16384\n"
    "v_cvt_pk_bf16_f32 v47, v62, v63\n"
    "v_mfma_f32_16x16x32_bf16 v[232:235], v[164:167], a[196:199], v[232:235]\n"
    "v_exp_f32 v73, v73\n"
    "v_mfma_f32_16x16x32_bf16 v[236:239], v[164:167], a[220:223], v[100:103]\n"
    "v_exp_f32 v74, v74\n"
    "v_mfma_f32_16x16x32_bf16 v[236:239], v[160:163], a[216:219], v[236:239]\n"
    "v_exp_f32 v75, v75\n"
    "v_mfma_f32_16x16x32_bf16 v[240:243], v[160:163], a[240:243], v[104:107]\n"
    "ds_read_b128 v[180:183], v200 offset:17408\n"
    "v_cvt_pk_bf16_f32 v69, v70, v71\n"
    "v_mfma_f32_16x16x32_bf16 v[240:243], v[164:167], a[244:247], v[240:243]\n"
    "v_exp_f32 v76, v76\n"
    "v_mfma_f32_16x16x32_bf16 v[244:247], v[164:167], v[12:15], v[108:111]\n"
    "v_exp_f32 v77, v77\n"
    "v_mfma_f32_16x16x32_bf16 v[244:247], v[160:163], v[8:11], v[244:247]\n"
    "s_waitcnt lgkmcnt(2)\n"
    "buffer_load_dwordx4 v200, s[40:43], s61 offen offset:2048 sc1 lds\n"
    "v_mfma_f32_16x16x32_bf16 v[244:247], v[168:171], v[16:19], v[244:247]\n"
    "ds_read_b128 v[160:163], v200 offset:22528\n"
    "v_cvt_pk_bf16_f32 v72, v72, v73\n"
    "v_mfma_f32_16x16x32_bf16 v[244:247], v[172:175], v[20:23], v[244:247]\n"
    "v_exp_f32 v78, v78\n"
    "v_mfma_f32_16x16x32_bf16 v[240:243], v[172:175], a[252:255], v[240:243]\n"
    "v_exp_f32 v79, v79\n"
    "v_mfma_f32_16x16x32_bf16 v[240:243], v[168:171], a[248:251], v[240:243]\n"
    "v_exp_f32 v80, v80\n"
    "v_mfma_f32_16x16x32_bf16 v[236:239], v[168:171], a[224:227], v[236:239]\n"
    "ds_read_b128 v[164:167], v200 offset:23552\n"
    "v_cvt_pk_bf16_f32 v73, v74, v75\n"
    "v_mfma_f32_16x16x32_bf16 v[236:239], v[172:175], a[228:231], v[236:239]\n"
    "v_exp_f32 v81, v81\n"
    "v_mfma_f32_16x16x32_bf16 v[232:235], v[172:175], a[204:207], v[232:235]\n"
    "v_exp_f32 v82, v82\n"
    "v_mfma_f32_16x16x32_bf16 v[232:235], v[168:171], a[200:203], v[232:235]\n"
    "s_waitcnt lgkmcnt(2)\n"
    "buffer_load_dwordx4 v200, s[40:43], s61 offen offset:3072 sc1 lds\n"
    "v_mfma_f32_16x16x32_bf16 v[232:235], v[176:179], a[208:211], v[232:235]\n"
    "ds_read_b128 v[168:171], v200 offset:20480\n"
    "v_cvt_pk_bf16_f32 v76, v76, v77\n"
    "v_mfma_f32_16x16x32_bf16 v[232:235], v[180:183], a[212:215], v[232:235]\n"
    "v_exp_f32 v83, v83\n"
    "v_mfma_f32_16x16x32_bf16 v[236:239], v[180:183], a[236:239], v[236:239]\n"
    "v_exp_f32 v84, v84\n"
    "v_mfma_f32_16x16x32_bf16 v[236:239], v[176:179], a[232:235], v[236:239]\n"
    "v_exp_f32 v85, v85\n"
    "v_mfma_f32_16x16x32_bf16 v[240:243], v[176:179], v[0:3], v[240:243]\n"
    "ds_read_b128 v[172:175], v200 offset:21504\n"
    "v_cvt_pk_bf16_f32 v77, v78, v79\n"
    "v_mfma_f32_16x16x32_bf16 v[240:243], v[180:183], v[4:7], v[240:243]\n"
    "s_add_u32 s65, s61, 0x1000\n"
    "v_exp_f32 v86, v86\n"
    "v_mfma_f32_16x16x32_bf16 v[244:247], v[180:183], v[28:31], v[244:247]\n"
    "s_add_u32 m0, s71, 0x19000\n"
    "v_exp_f32 v87, v87\n"
    "v_mfma_f32_16x16x32_bf16 v[244:247], v[176:179], v[24:27], v[244:247]\n"
    "s_waitcnt lgkmcnt(2)\n"
    "buffer_load_dwordx4 v200, s[40:43], s65 offen sc1 lds\n"
    "v_mfma_f32_16x16x32_bf16 v[60:63], v[160:163], v[24:27], v[108:111]\n"
    "ds_read_b128 v[176:179], v200 offset:18432\n"
    "v_cvt_pk_bf16_f32 v66, v80, v81\n"
    "v_mfma_f32_16x16x32_bf16 v[60:63], v[164:167], v[28:31], v[60:63]\n"
    "v_exp_f32 v88, v88\n"
    "v_mfma_f32_16x16x32_bf16 v[56:59], v[164:167], v[4:7], v[104:107]\n"
    "v_exp_f32 v89, v89\n"
    "v_mfma_f32_16x16x32_bf16 v[56:59], v[160:163], v[0:3], v[56:59]\n"
    "v_exp_f32 v90, v90\n"
    "v_mfma_f32_16x16x32_bf16 v[52:55], v[160:163], a[232:235], v[100:103]\n"
    "ds_read_b128 v[180:183], v200 offset:19456\n"
    "v_cvt_pk_bf16_f32 v67, v82, v83\n"
    "v_mfma_f32_16x16x32_bf16 v[52:55], v[164:167], a[236:239], v[52:55]\n"
    "v_exp_f32 v91, v91\n"
    "v_mfma_f32_16x16x32_bf16 v[48:51], v[164:167], a[212:215], v[96:99]\n"
    "s_waitcnt vmcnt(5)\n"
    "v_exp_f32 v92, v92\n"
    "v_mfma_f32_16x16x32_bf16 v[48:51], v[160:163], a[208:211], v[48:51]\n"
    "s_waitcnt lgkmcnt(2)\n"
    "buffer_load_dwordx4 v200, s[40:43], s65 offen offset:1024 sc1 lds\n"
    "v_mfma_f32_16x16x32_bf16 v[48:51], v[168:171], a[200:203], v[48:51]\n"
    "s_barrier\n"
    "v_exp_f32 v93, v93\n"
    "v_mfma_f32_16x16x32_bf16 v[48:51], v[172:175], a[204:207], v[48:51]\n"
    "ds_read_b128 v[160:163], v200 offset:24576\n"
    "v_cvt_pk_bf16_f32 v70, v84, v85\n"
    "v_mfma_f32_16x16x32_bf16 v[52:55], v[172:175], a[228:231], v[52:55]\n"
    "v_exp_f32 v94, v94\n"
    "v_mfma_f32_16x16x32_bf16 v[52:55], v[168:171], a[224:227], v[52:55]\n"
    "v_exp_f32 v95, v95\n"
    "v_mfma_f32_16x16x32_bf16 v[56:59], v[168:171], a[248:251], v[56:59]\n"
    "ds_read_b128 v[164:167], v200 offset:25600\n"
    "v_cvt_pk_bf16_f32 v71, v86, v87\n"
    "v_mfma_f32_16x16x32_bf16 v[56:59], v[172:175], a[252:255], v[56:59]\n"
    "v_exp_f32 v232, v232\n"
    "v_mfma_f32_16x16x32_bf16 v[60:63], v[172:175], v[20:23], v[60:63]\n"
    "v_exp_f32 v233, v233\n"
    "v_mfma_f32_16x16x32_bf16 v[60:63], v[168:171], v[16:19], v[60:63]\n"
    "s_waitcnt lgkmcnt(2)\n"
    "v_exp_f32 v234, v234\n"
    "v_mfma_f32_16x16x32_bf16 v[60:63], v[176:179], v[8:11], v[60:63]\n"
    "s_lshl_b32 s76, s56, 3\n"
    "ds_read_b128 v[168:171], v200 offset:26624\n"
    "v_cvt_pk_bf16_f32 v74, v88, v89\n"
    "v_mfma_f32_16x16x32_bf16 v[60:63], v[180:183], v[12:15], v[60:63]\n"
    "s_add_u32 s76, s76, s57\n"
    "v_exp_f32 v235, v235\n"
    "v_mfma_f32_16x16x32_bf16 v[56:59], v[180:183], a[244:247], v[56:59]\n"
    "s_mul_i32 s76, s76, 0x18000\n"
    "v_exp_f32 v236, v236\n"
    "v_mfma_f32_16x16x32_bf16 v[56:59], v[176:179], a[240:243], v[56:59]\n"
    "s_add_u32 s76, s76, s88\n"
    "v_exp_f32 v237, v237\n"
    "v_mfma_f32_16x16x32_bf16 v[52:55], v[176:179], a[216:219], v[52:55]\n"
    "s_add_u32 s28, s24, s76\n"
    "ds_read_b128 v[172:175], v200 offset:27648\n"
    "v_cvt_pk_bf16_f32 v75, v90, v91\n"
    "v_mfma_f32_16x16x32_bf16 v[52:55], v[180:183], a[220:223], v[52:55]\n"
    "s_addc_u32 s29, s25, 0\n"
    "v_exp_f32 v238, v238\n"
    "v_mfma_f32_16x16x32_bf16 v[48:51], v[180:183], a[196:199], v[48:51]\n"
    "s_and_b32 s29, s29, 0xffff\n"
    "v_exp_f32 v239, v239\n"
    "v_mfma_f32_16x16x32_bf16 v[48:51], v[176:179], a[192:195], v[48:51]\n"
    "s_waitcnt lgkmcnt(2)\n"
    "v_exp_f32 v240, v240\n"
    "v_mfma_f32_16x16x32_bf16 v[80:83], v[160:163], a[192:195], v[96:99]\n"
    "s_mov_b32 s30, 0x6000\n"
    "ds_read_b128 v[176:179], v200 offset:28672\n"
    "v_cvt_pk_bf16_f32 v78, v92, v93\n"
    "v_mfma_f32_16x16x32_bf16 v[80:83], v[164:167], a[196:199], v[80:83]\n"
    "s_mov_b32 s31, 0x20000\n"
    "v_cvt_pk_bf16_f32 v79, v94, v95\n"
    "v_permlane16_swap_b32 v113, v115\n"
    "v_mfma_f32_16x16x32_bf16 v[84:87], v[164:167], a[220:223], v[100:103]\n"
    "s_xor_b32 s94, s70, 0x80000000\n"
    "v_exp_f32 v241, v241\n"
    "v_mfma_f32_16x16x32_bf16 v[84:87], v[160:163], a[216:219], v[84:87]\n"
    "s_xor_b32 s95, s70, 0x80000000\n"
    "v_exp_f32 v242, v242\n"
    "v_mfma_f32_16x16x32_bf16 v[88:91], v[160:163], a[240:243], v[104:107]\n"
    "s_mov_b32 s62, 0x1800\n"
    "ds_read_b128 v[180:183], v200 offset:29696\n"
    "v_permlane16_swap_b32 v117, v119\n"
    "v_mfma_f32_16x16x32_bf16 v[88:91], v[164:167], a[244:247], v[88:91]\n"
    "s_mov_b32 s63, 0x3000\n"
    "v_exp_f32 v243, v243\n"
    "v_mfma_f32_16x16x32_bf16 v[92:95], v[164:167], v[12:15], v[108:111]\n"
    "s_mov_b32 s64, 0x4800\n"
    "v_exp_f32 v244, v244\n"
    "v_mfma_f32_16x16x32_bf16 v[92:95], v[160:163], v[8:11], v[92:95]\n"
    "s_waitcnt lgkmcnt(2)\n"
    "v_exp_f32 v245, v245\n"
    "v_mfma_f32_16x16x32_bf16 v[92:95], v[168:171], v[16:19], v[92:95]\n"
    "ds_read_b128 v[160:163], v200 offset:34816\n"
    "v_permlane16_swap_b32 v121, v123\n"
    "v_mfma_f32_16x16x32_bf16 v[92:95], v[172:175], v[20:23], v[92:95]\n"
    "v_exp_f32 v246, v246\n"
    "v_mfma_f32_16x16x32_bf16 v[88:91], v[172:175], a[252:255], v[88:91]\n"
    "v_exp_f32 v247, v247\n"
    "v_mfma_f32_16x16x32_bf16 v[88:91], v[168:171], a[248:251], v[88:91]\n"
    "v_exp_f32 v48, v48\n"
    "v_mfma_f32_16x16x32_bf16 v[84:87], v[168:171], a[224:227], v[84:87]\n"
    "ds_read_b128 v[164:167], v200 offset:35840\n"
    "v_permlane16_swap_b32 v125, v127\n"
    "v_mfma_f32_16x16x32_bf16 v[84:87], v[172:175], a[228:231], v[84:87]\n"
    "v_exp_f32 v49, v49\n"
    "v_mfma_f32_16x16x32_bf16 v[80:83], v[172:175], a[204:207], v[80:83]\n"
    "v_exp_f32 v50, v50\n"
    "v_mfma_f32_16x16x32_bf16 v[80:83], v[168:171], a[200:203], v[80:83]\n"
    "s_waitcnt lgkmcnt(2)\n"
    "v_exp_f32 v51, v51\n"
    "v_mfma_f32_16x16x32_bf16 v[80:83], v[176:179], a[208:211], v[80:83]\n"
    "ds_read_b128 v[168:171], v200 offset:32768\n"
    "v_add_f32 v113, v113, v115\n"
    "v_mfma_f32_16x16x32_bf16 v[80:83], v[180:183], a[212:215], v[80:83]\n"
    "v_exp_f32 v52, v52\n"
    "v_mfma_f32_16x16x32_bf16 v[84:87], v[180:183], a[236:239], v[84:87]\n"
    "v_exp_f32 v53, v53\n"
    "v_mfma_f32_16x16x32_bf16 v[84:87], v[176:179], a[232:235], v[84:87]\n"
    "v_exp_f32 v54, v54\n"
    "v_mfma_f32_16x16x32_bf16 v[88:91], v[176:179], v[0:3], v[88:91]\n"
    "ds_read_b128 v[172:175], v200 offset:33792\n"
    "v_add_f32 v117, v117, v119\n"
    "v_mfma_f32_16x16x32_bf16 v[88:91], v[180:183], v[4:7], v[88:91]\n"
    "v_add_f32 v121, v121, v123\n"
    "v_add_f32 v125, v125, v127\n"
    "v_mfma_f32_16x16x32_bf16 v[92:95], v[180:183], v[28:31], v[92:95]\n"
    "v_exp_f32 v55, v55\n"
    "v_mfma_f32_16x16x32_bf16 v[92:95], v[176:179], v[24:27], v[92:95]\n"
    "s_waitcnt lgkmcnt(2)\n"
    "v_add_f32 v112, v112, v113\n"
    "v_add_f32 v116, v116, v117\n"
    "v_mfma_f32_16x16x32_bf16 v[252:255], v[160:163], v[24:27], v[108:111]\n"
    "ds_read_b128 v[176:179], v200 offset:30720\n"
    "v_add_f32 v120, v120, v121\n"
    "v_mfma_f32_16x16x32_bf16 v[252:255], v[164:167], v[28:31], v[252:255]\n"
    "v_add_f32 v124, v124, v125\n"
    "v_and_b32 v198, 15, v231\n"
    "v_mfma_f32_16x16x32_bf16 v[248:251], v[164:167], v[4:7], v[104:107]\n"
    "v_mul_u32_u24 v205, 0x180, v198\n"
    "v_lshrrev_b32 v199, 4, v231\n"
    "v_mfma_f32_16x16x32_bf16 v[248:251], v[160:163], v[0:3], v[248:251]\n"
    "v_lshl_add_u32 v205, v199, 4, v205\n"
    "v_add_u32 v207, 0x18000, v200\n"
    "v_mfma_f32_16x16x32_bf16 v[188:191], v[160:163], a[232:235], v[100:103]\n"
    "ds_read_b128 v[180:183], v200 offset:31744\n"
    "v_cvt_pk_bf16_f32 v232, v232, v233\n"
    "v_mfma_f32_16x16x32_bf16 v[188:191], v[164:167], a[236:239], v[188:191]\n"
    "v_exp_f32 v56, v56\n"
    "v_mfma_f32_16x16x32_bf16 v[184:187], v[164:167], a[212:215], v[96:99]\n"
    "v_exp_f32 v57, v57\n"
    "v_mfma_f32_16x16x32_bf16 v[184:187], v[160:163], a[208:211], v[184:187]\n"
    "s_waitcnt lgkmcnt(2)\n"
    "v_exp_f32 v58, v58\n"
    "v_mfma_f32_16x16x32_bf16 v[184:187], v[168:171], a[200:203], v[184:187]\n"
    "ds_read_b128 v[160:163], v201\n"
    "v_cvt_pk_bf16_f32 v233, v234, v235\n"
    "v_mfma_f32_16x16x32_bf16 v[184:187], v[172:175], a[204:207], v[184:187]\n"
    "v_exp_f32 v59, v59\n"
    "v_mfma_f32_16x16x32_bf16 v[188:191], v[172:175], a[228:231], v[188:191]\n"
    "v_exp_f32 v60, v60\n"
    "v_mfma_f32_16x16x32_bf16 v[188:191], v[168:171], a[224:227], v[188:191]\n"
    "v_exp_f32 v61, v61\n"
    "v_mfma_f32_16x16x32_bf16 v[248:251], v[168:171], a[248:251], v[248:251]\n"
    "ds_read_b128 v[164:167], v201 offset:1024\n"
    "v_cvt_pk_bf16_f32 v236, v236, v237\n"
    "v_mfma_f32_16x16x32_bf16 v[248:251], v[172:175], a[252:255], v[248:251]\n"
    "v_exp_f32 v62, v62\n"
    "v_mfma_f32_16x16x32_bf16 v[252:255], v[172:175], v[20:23], v[252:255]\n"
    "v_exp_f32 v63, v63\n"
    "v_mfma_f32_16x16x32_bf16 v[252:255], v[168:171], v[16:19], v[252:255]\n"
    "s_waitcnt lgkmcnt(2)\n"
    "v_exp_f32 v80, v80\n"
    "v_mfma_f32_16x16x32_bf16 v[252:255], v[176:179], v[8:11], v[252:255]\n"
    "ds_read_b128 v[168:171], v201 offset:24576\n"
    "v_cvt_pk_bf16_f32 v237, v238, v239\n"
    "v_mfma_f32_16x16x32_bf16 v[252:255], v[180:183], v[12:15], v[252:255]\n"
    "v_exp_f32 v81, v81\n"
    "v_mfma_f32_16x16x32_bf16 v[248:251], v[180:183], a[244:247], v[248:251]\n"
    "v_exp_f32 v82, v82\n"
    "v_mfma_f32_16x16x32_bf16 v[248:251], v[176:179], a[240:243], v[248:251]\n"
    "v_exp_f32 v83, v83\n"
    "v_mfma_f32_16x16x32_bf16 v[188:191], v[176:179], a[216:219], v[188:191]\n"
    "ds_read_b128 v[172:175], v201 offset:25600\n"
    "v_cvt_pk_bf16_f32 v240, v240, v241\n"
    "v_mfma_f32_16x16x32_bf16 v[188:191], v[180:183], a[220:223], v[188:191]\n"
    "v_exp_f32 v84, v84\n"
    "v_mfma_f32_16x16x32_bf16 v[184:187], v[180:183], a[196:199], v[184:187]\n"
    "v_exp_f32 v85, v85\n"
    "v_mfma_f32_16x16x32_bf16 v[184:187], v[176:179], a[192:195], v[184:187]\n"
    "s_waitcnt lgkmcnt(2)\n"
    "v_exp_f32 v86, v86\n"
    "v_mfma_f32_16x16x32_bf16 a[0:3], v[160:163], v[128:131], a[0:3]\n"
    "ds_read_b128 v[176:179], v201 offset:2048\n"
    "v_cvt_pk_bf16_f32 v241, v242, v243\n"
    "v_mfma_f32_16x16x32_bf16 a[0:3], v[164:167], v[144:147], a[0:3]\n"
    "v_exp_f32 v87, v87\n"
    "v_mfma_f32_16x16x32_bf16 a[4:7], v[160:163], v[132:135], a[4:7]\n"
    "v_exp_f32 v88, v88\n"
    "v_mfma_f32_16x16x32_bf16 a[4:7], v[164:167], v[148:151], a[4:7]\n"
    "v_exp_f32 v89, v89\n"
    "v_mfma_f32_16x16x32_bf16 a[8:11], v[160:163], v[136:139], a[8:11]\n"
    "ds_read_b128 v[180:183], v201 offset:3072\n"
    "v_cvt_pk_bf16_f32 v244, v244, v245\n"
    "v_mfma_f32_16x16x32_bf16 a[8:11], v[164:167], v[152:155], a[8:11]\n"
    "v_cvt_pk_bf16_f32 v245, v246, v247\n"
    "v_cvt_pk_bf16_f32 v234, v48, v49\n"
    "v_mfma_f32_16x16x32_bf16 a[12:15], v[160:163], v[140:143], a[12:15]\n"
    "v_cvt_pk_bf16_f32 v235, v50, v51\n"
    "v_cvt_pk_bf16_f32 v238, v52, v53\n"
    "v_mfma_f32_16x16x32_bf16 a[12:15], v[164:167], v[156:159], a[12:15]\n"
    "v_cvt_pk_bf16_f32 v239, v54, v55\n"
    "v_cvt_pk_bf16_f32 v242, v56, v57\n"
    "v_mfma_f32_16x16x32_bf16 v[112:115], v[192:195], v[144:147], v[112:115]\n"
    "v_exp_f32 v90, v90\n"
    "v_mfma_f32_16x16x32_bf16 v[112:115], v[192:195], v[32:35], v[112:115]\n"
    "v_cvt_pk_bf16_f32 v243, v58, v59\n"
    "v_cvt_pk_bf16_f32 v246, v60, v61\n"
    "v_mfma_f32_16x16x32_bf16 v[112:115], v[192:195], v[64:67], v[112:115]\n"
    "v_cvt_pk_bf16_f32 v247, v62, v63\n"
    "v_cvt_pk_bf16_f32 v80, v80, v81\n"
    "v_mfma_f32_16x16x32_bf16 v[116:119], v[192:195], v[148:151], v[116:119]\n"
    "v_exp_f32 v91, v91\n"
    "v_mfma_f32_16x16x32_bf16 v[116:119], v[192:195], v[36:39], v[116:119]\n"
    "v_exp_f32 v92, v92\n"
    "v_mfma_f32_16x16x32_bf16 v[116:119], v[192:195], v[68:71], v[116:119]\n"
    "s_waitcnt lgkmcnt(2)\n"
    "v_exp_f32 v93, v93\n"
    "v_mfma_f32_16x16x32_bf16 a[0:3], v[168:171], v[32:35], a[0:3]\n"
    "ds_read_b128 v[160:163], v201 offset:26624\n"
    "v_cvt_pk_bf16_f32 v81, v82, v83\n"
    "v_mfma_f32_16x16x32_bf16 a[0:3], v[172:175], v[64:67], a[0:3]\n"
    "v_exp_f32 v94, v94\n"
    "v_mfma_f32_16x16x32_bf16 a[4:7], v[168:171], v[36:39], a[4:7]\n"
    "v_exp_f32 v95, v95\n"
    "v_mfma_f32_16x16x32_bf16 a[4:7], v[172:175], v[68:71], a[4:7]\n"
    "v_exp_f32 v184, v184\n"
    "v_mfma_f32_16x16x32_bf16 a[8:11], v[168:171], v[40:43], a[8:11]\n"
    "ds_read_b128 v[164:167], v201 offset:27648\n"
    "v_cvt_pk_bf16_f32 v84, v84, v85\n"
    "v_mfma_f32_16x16x32_bf16 a[8:11], v[172:175], v[72:75], a[8:11]\n"
    "v_exp_f32 v185, v185\n"
    "v_mfma_f32_16x16x32_bf16 a[12:15], v[168:171], v[44:47], a[12:15]\n"
    "v_exp_f32 v186, v186\n"
    "v_mfma_f32_16x16x32_bf16 a[12:15], v[172:175], v[76:79], a[12:15]\n"
    "v_exp_f32 v187, v187\n"
    "v_mfma_f32_16x16x32_bf16 v[120:123], v[192:195], v[152:155], v[120:123]\n"
    "v_exp_f32 v188, v188\n"
    "v_mfma_f32_16x16x32_bf16 v[120:123], v[192:195], v[40:43], v[120:123]\n"
    "v_exp_f32 v189, v189\n"
    "v_mfma_f32_16x16x32_bf16 v[120:123], v[192:195], v[72:75], v[120:123]\n"
    "v_exp_f32 v190, v190\n"
    "v_mfma_f32_16x16x32_bf16 v[124:127], v[192:195], v[156:159], v[124:127]\n"
    "v_exp_f32 v191, v191\n"
    "v_mfma_f32_16x16x32_bf16 v[124:127], v[192:195], v[44:47], v[124:127]\n"
    "s_waitcnt vmcnt(0)\n"
    "v_exp_f32 v248, v248\n"
    "v_mfma_f32_16x16x32_bf16 v[124:127], v[192:195], v[76:79], v[124:127]\n"
    "s_waitcnt lgkmcnt(2)\n"
    "v_exp_f32 v249, v249\n"
    "v_mfma_f32_16x16x32_bf16 a[16:19], v[176:179], v[128:131], a[16:19]\n"
    "s_barrier\n"
    "v_exp_f32 v250, v250\n"
    "v_mfma_f32_16x16x32_bf16 a[16:19], v[180:183], v[144:147], a[16:19]\n"
    "ds_read_b128 v[168:171], v207\n"
    "v_cvt_pk_bf16_f32 v85, v86, v87\n"
    "v_mfma_f32_16x16x32_bf16 a[20:23], v[176:179], v[132:135], a[20:23]\n"
    "v_exp_f32 v251, v251\n"
    "v_mfma_f32_16x16x32_bf16 a[20:23], v[180:183], v[148:151], a[20:23]\n"
    "v_exp_f32 v252, v252\n"
    "v_mfma_f32_16x16x32_bf16 a[24:27], v[176:179], v[136:139], a[24:27]\n"
    "ds_read_b128 v[172:175], v207 offset:1024\n"
    "v_cvt_pk_bf16_f32 v88, v88, v89\n"
    "v_mfma_f32_16x16x32_bf16 a[24:27], v[180:183], v[152:155], a[24:27]\n"
    "v_exp_f32 v253, v253\n"
    "v_mfma_f32_16x16x32_bf16 a[28:31], v[176:179], v[140:143], a[28:31]\n"
    "v_exp_f32 v254, v254\n"
    "v_mfma_f32_16x16x32_bf16 a[28:31], v[180:183], v[156:159], a[28:31]\n"
    "s_waitcnt lgkmcnt(2)\n"
    "v_cvt_pk_bf16_f32 v89, v90, v91\n"
    "v_cvt_pk_bf16_f32 v92, v92, v93\n"
    "v_mfma_f32_16x16x32_bf16 a[16:19], v[160:163], v[32:35], a[16:19]\n"
    "ds_read_b128 v[176:179], v207 offset:2048\n"
    "v_cvt_pk_bf16_f32 v93, v94, v95\n"
    "v_mfma_f32_16x16x32_bf16 a[16:19], v[164:167], v[64:67], a[16:19]\n"
    "ds_read_b128 v[180:183], v207 offset:3072\n"
    "v_cvt_pk_bf16_f32 v82, v184, v185\n"
    "v_mfma_f32_16x16x32_bf16 a[20:23], v[160:163], v[36:39], a[20:23]\n"
    "v_cvt_pk_bf16_f32 v83, v186, v187\n"
    "v_cvt_pk_bf16_f32 v86, v188, v189\n"
    "v_mfma_f32_16x16x32_bf16 a[20:23], v[164:167], v[68:71], a[20:23]\n"
    "v_exp_f32 v255, v255\n"
    "v_mfma_f32_16x16x32_bf16 a[24:27], v[160:163], v[40:43], a[24:27]\n"
    "v_cvt_pk_bf16_f32 v87, v190, v191\n"
    "v_cvt_pk_bf16_f32 v90, v248, v249\n"
    "v_mfma_f32_16x16x32_bf16 a[24:27], v[164:167], v[72:75], a[24:27]\n"
    "v_cvt_pk_bf16_f32 v91, v250, v251\n"
    "v_cvt_pk_bf16_f32 v94, v252, v253\n"
    "v_mfma_f32_16x16x32_bf16 a[28:31], v[160:163], v[44:47], a[28:31]\n"
    "v_cvt_pk_bf16_f32 v95, v254, v255\n"
    "v_mfma_f32_16x16x32_bf16 a[28:31], v[164:167], v[76:79], a[28:31]\n"
    "v_mfma_f32_16x16x32_bf16 v[112:115], v[192:195], v[232:235], v[112:115]\n"
    "ds_read_b128 v[160:163], v201 offset:4096\n"
    "v_mfma_f32_16x16x32_bf16 v[112:115], v[192:195], v[80:83], v[112:115]\n"
    "s_waitcnt lgkmcnt(3)\n"
    "v_mfma_f32_16x16x32_bf16 a[0:3], v[168:171], v[232:235], a[0:3]\n"
    "v_mfma_f32_16x16x32_bf16 a[0:3], v[172:175], v[80:83], a[0:3]\n"
    "s_waitcnt lgkmcnt(1)\n"
    "v_mfma_f32_16x16x32_bf16 a[16:19], v[176:179], v[232:235], a[16:19]\n"
    "ds_read_b128 v[164:167], v201 offset:5120\n"
    "v_mfma_f32_16x16x32_bf16 a[16:19], v[180:183], v[80:83], a[16:19]\n"
    "v_mfma_f32_16x16x32_bf16 v[116:119], v[192:195], v[236:239], v[116:119]\n"
    "v_mul_f32 v222, s68, v112\n"
    "v_rcp_f32 v222, v222\n"
    "v_mfma_f32_16x16x32_bf16 v[116:119], v[192:195], v[84:87], v[116:119]\n"
    "v_accvgpr_read_b32 v96, a0\n"
    "v_accvgpr_read_b32 v97, a1\n"
    "v_accvgpr_read_b32 v98, a2\n"
    "v_mfma_f32_16x16x32_bf16 a[4:7], v[168:171], v[236:239], a[4:7]\n"
    "v_accvgpr_read_b32 v99, a3\n"
    "v_fma_f32 v96, v96, v222, s94\n"
    "v_fma_f32 v97, v97, v222, s94\n"
    "v_mfma_f32_16x16x32_bf16 a[4:7], v[172:175], v[84:87], a[4:7]\n"
    "v_fma_f32 v98, v98, v222, s94\n"
    "v_fma_f32 v99, v99, v222, s94\n"
    "v_cvt_pk_bf16_f32 v48, v96, v97\n"
    "v_mfma_f32_16x16x32_bf16 a[20:23], v[176:179], v[236:239], a[20:23]\n"
    "v_cvt_pk_bf16_f32 v49, v98, v99\n"
    "v_accvgpr_read_b32 v100, a16\n"
    "v_accvgpr_read_b32 v101, a17\n"
    "v_mfma_f32_16x16x32_bf16 a[20:23], v[180:183], v[84:87], a[20:23]\n"
    "v_accvgpr_read_b32 v102, a18\n"
    "v_accvgpr_read_b32 v103, a19\n"
    "v_fma_f32 v100, v100, v222, s94\n"
    "v_mfma_f32_16x16x32_bf16 v[120:123], v[192:195], v[240:243], v[120:123]\n"
    "v_fma_f32 v101, v101, v222, s94\n"
    "v_fma_f32 v102, v102, v222, s94\n"
    "v_fma_f32 v103, v103, v222, s94\n"
    "v_mfma_f32_16x16x32_bf16 v[120:123], v[192:195], v[88:91], v[120:123]\n"
    "v_cvt_pk_bf16_f32 v50, v100, v101\n"
    "v_cvt_pk_bf16_f32 v51, v102, v103\n"
    "v_mfma_f32_16x16x32_bf16 a[8:11], v[168:171], v[240:243], a[8:11]\n"
    "buffer_store_dwordx4 v[48:51], v205, s[28:31], 0 offen sc0 sc1\n"
    "v_mul_f32 v224, s68, v116\n"
    "v_rcp_f32 v224, v224\n"
    "v_accvgpr_read_b32 v104, a4\n"
    "v_mfma_f32_16x16x32_bf16 a[8:11], v[172:175], v[88:91], a[8:11]\n"
    "v_accvgpr_read_b32 v105, a5\n"
    "v_accvgpr_read_b32 v106, a6\n"
    "v_accvgpr_read_b32 v107, a7\n"
    "v_mfma_f32_16x16x32_bf16 a[24:27], v[176:179], v[240:243], a[24:27]\n"
    "v_fma_f32 v104, v104, v224, s94\n"
    "v_fma_f32 v105, v105, v224, s94\n"
    "v_fma_f32 v106, v106, v224, s94\n"
    "v_mfma_f32_16x16x32_bf16 a[24:27], v[180:183], v[88:91], a[24:27]\n"
    "v_fma_f32 v107, v107, v224, s94\n"
    "v_cvt_pk_bf16_f32 v52, v104, v105\n"
    "v_cvt_pk_bf16_f32 v53, v106, v107\n"
    "v_mfma_f32_16x16x32_bf16 v[124:127], v[192:195], v[244:247], v[124:127]\n"
    "v_accvgpr_read_b32 v108, a20\n"
    "v_accvgpr_read_b32 v109, a21\n"
    "v_accvgpr_read_b32 v110, a22\n"
    "v_mfma_f32_16x16x32_bf16 v[124:127], v[192:195], v[92:95], v[124:127]\n"
    "v_accvgpr_read_b32 v111, a23\n"
    "v_fma_f32 v108, v108, v224, s94\n"
    "v_fma_f32 v109, v109, v224, s94\n"
    "v_mfma_f32_16x16x32_bf16 a[12:15], v[168:171], v[244:247], a[12:15]\n"
    "v_fma_f32 v110, v110, v224, s94\n"
    "v_fma_f32 v111, v111, v224, s94\n"
    "v_mfma_f32_16x16x32_bf16 a[12:15], v[172:175], v[92:95], a[12:15]\n"
    "v_cvt_pk_bf16_f32 v54, v108, v109\n"
    "v_cvt_pk_bf16_f32 v55, v110, v111\n"
    "v_mfma_f32_16x16x32_bf16 a[28:31], v[176:179], v[244:247], a[28:31]\n"
    "ds_read_b128 v[168:171], v201 offset:28672\n"
    "v_mfma_f32_16x16x32_bf16 a[28:31], v[180:183], v[92:95], a[28:31]\n"
    "s_waitcnt lgkmcnt(1)\n"
    "ds_read_b128 v[172:175], v201 offset:29696\n"
    "v_mfma_f32_16x16x32_bf16 a[32:35], v[160:163], v[128:131], a[32:35]\n"
    "ds_read_b128 v[176:179], v207 offset:4096\n"
    "v_mfma_f32_16x16x32_bf16 a[32:35], v[164:167], v[144:147], a[32:35]\n"
    "buffer_store_dwordx4 v[52:55], v205, s[28:31], s62 offen sc0 sc1\n"
    "v_mul_f32 v226, s68, v120\n"
    "v_rcp_f32 v226, v226\n"
    "v_mfma_f32_16x16x32_bf16 a[36:39], v[160:163], v[132:135], a[36:39]\n"
    "v_accvgpr_read_b32 v96, a8\n"
    "v_accvgpr_read_b32 v97, a9\n"
    "v_mfma_f32_16x16x32_bf16 a[36:39], v[164:167], v[148:151], a[36:39]\n"
    "v_accvgpr_read_b32 v98, a10\n"
    "v_accvgpr_read_b32 v99, a11\n"
    "v_mfma_f32_16x16x32_bf16 a[40:43], v[160:163], v[136:139], a[40:43]\n"
    "ds_read_b128 v[180:183], v207 offset:5120\n"
    "v_fma_f32 v96, v96, v226, s94\n"
    "v_mfma_f32_16x16x32_bf16 a[40:43], v[164:167], v[152:155], a[40:43]\n"
    "v_fma_f32 v97, v97, v226, s94\n"
    "v_fma_f32 v98, v98, v226, s94\n"
    "v_mfma_f32_16x16x32_bf16 a[44:47], v[160:163], v[140:143], a[44:47]\n"
    "v_fma_f32 v99, v99, v226, s94\n"
    "v_cvt_pk_bf16_f32 v56, v96, v97\n"
    "v_mfma_f32_16x16x32_bf16 a[44:47], v[164:167], v[156:159], a[44:47]\n"
    "s_waitcnt lgkmcnt(2)\n"
    "v_cvt_pk_bf16_f32 v57, v98, v99\n"
    "v_accvgpr_read_b32 v100, a24\n"
    "v_mfma_f32_16x16x32_bf16 a[44:47], v[168:171], v[44:47], a[44:47]\n"
    "ds_read_b128 v[160:163], v201 offset:6144\n"
    "v_accvgpr_read_b32 v101, a25\n"
    "v_mfma_f32_16x16x32_bf16 a[44:47], v[172:175], v[76:79], a[44:47]\n"
    "v_accvgpr_read_b32 v102, a26\n"
    "v_accvgpr_read_b32 v103, a27\n"
    "v_mfma_f32_16x16x32_bf16 a[40:43], v[168:171], v[40:43], a[40:43]\n"
    "v_fma_f32 v100, v100, v226, s94\n"
    "v_fma_f32 v101, v101, v226, s94\n"
    "v_mfma_f32_16x16x32_bf16 a[40:43], v[172:175], v[72:75], a[40:43]\n"
    "v_fma_f32 v102, v102, v226, s94\n"
    "v_fma_f32 v103, v103, v226, s94\n"
    "v_mfma_f32_16x16x32_bf16 a[36:39], v[168:171], v[36:39], a[36:39]\n"
    "ds_read_b128 v[164:167], v201 offset:7168\n"
    "v_cvt_pk_bf16_f32 v58, v100, v101\n"
    "v_mfma_f32_16x16x32_bf16 a[36:39], v[172:175], v[68:71], a[36:39]\n"
    "v_cvt_pk_bf16_f32 v59, v102, v103\n"
    "v_mfma_f32_16x16x32_bf16 a[32:35], v[168:171], v[32:35], a[32:35]\n"
    "buffer_store_dwordx4 v[56:59], v205, s[28:31], s63 offen sc0 sc1\n"
    "v_mul_f32 v228, s68, v124\n"
    "v_rcp_f32 v228, v228\n"
    "v_mfma_f32_16x16x32_bf16 a[32:35], v[172:175], v[64:67], a[32:35]\n"
    "s_waitcnt lgkmcnt(2)\n"
    "v_accvgpr_read_b32 v104, a12\n"
    "v_accvgpr_read_b32 v105, a13\n"
    "v_mfma_f32_16x16x32_bf16 a[32:35], v[176:179], v[232:235], a[32:35]\n"
    "ds_read_b128 v[168:171], v201 offset:30720\n"
    "v_accvgpr_read_b32 v106, a14\n"
    "v_mfma_f32_16x16x32_bf16 a[32:35], v[180:183], v[80:83], a[32:35]\n"
    "v_accvgpr_read_b32 v107, a15\n"
    "v_fma_f32 v104, v104, v228, s94\n"
    "v_mfma_f32_16x16x32_bf16 a[36:39], v[176:179], v[236:239], a[36:39]\n"
    "v_fma_f32 v105, v105, v228, s94\n"
    "v_fma_f32 v106, v106, v228, s94\n"
    "v_mfma_f32_16x16x32_bf16 a[36:39], v[180:183], v[84:87], a[36:39]\n"
    "v_fma_f32 v107, v107, v228, s94\n"
    "v_cvt_pk_bf16_f32 v60, v104, v105\n"
    "v_mfma_f32_16x16x32_bf16 a[40:43], v[176:179], v[240:243], a[40:43]\n"
    "ds_read_b128 v[172:175], v201 offset:31744\n"
    "v_cvt_pk_bf16_f32 v61, v106, v107\n"
    "v_mfma_f32_16x16x32_bf16 a[40:43], v[180:183], v[88:91], a[40:43]\n"
    "v_accvgpr_read_b32 v108, a28\n"
    "v_accvgpr_read_b32 v109, a29\n"
    "v_mfma_f32_16x16x32_bf16 a[44:47], v[176:179], v[244:247], a[44:47]\n"
    "v_accvgpr_read_b32 v110, a30\n"
    "v_accvgpr_read_b32 v111, a31\n"
    "v_mfma_f32_16x16x32_bf16 a[44:47], v[180:183], v[92:95], a[44:47]\n"
    "s_waitcnt lgkmcnt(2)\n"
    "v_fma_f32 v108, v108, v228, s94\n"
    "v_fma_f32 v109, v109, v228, s94\n"
    "v_mfma_f32_16x16x32_bf16 a[48:51], v[160:163], v[128:131], a[48:51]\n"
    "ds_read_b128 v[176:179], v207 offset:6144\n"
    "v_fma_f32 v110, v110, v228, s94\n"
    "v_mfma_f32_16x16x32_bf16 a[48:51], v[164:167], v[144:147], a[48:51]\n"
    "v_fma_f32 v111, v111, v228, s94\n"
    "v_cvt_pk_bf16_f32 v62, v108, v109\n"
    "v_mfma_f32_16x16x32_bf16 a[52:55], v[160:163], v[132:135], a[52:55]\n"
    "v_cvt_pk_bf16_f32 v63, v110, v111\n"
    "v_mfma_f32_16x16x32_bf16 a[52:55], v[164:167], v[148:151], a[52:55]\n"
    "buffer_store_dwordx4 v[60:63], v205, s[28:31], s64 offen sc0 sc1\n"
    "v_accvgpr_read_b32 v96, a32\n"
    "v_accvgpr_read_b32 v97, a33\n"
    "v_accvgpr_read_b32 v98, a34\n"
    "v_mfma_f32_16x16x32_bf16 a[56:59], v[160:163], v[136:139], a[56:59]\n"
    "ds_read_b128 v[180:183], v207 offset:7168\n"
    "v_accvgpr_read_b32 v99, a35\n"
    "v_mfma_f32_16x16x32_bf16 a[56:59], v[164:167], v[152:155], a[56:59]\n"
    "v_fma_f32 v96, v96, v222, s94\n"
    "v_fma_f32 v97, v97, v222, s94\n"
    "v_mfma_f32_16x16x32_bf16 a[60:63], v[160:163], v[140:143], a[60:63]\n"
    "v_fma_f32 v98, v98, v222, s94\n"
    "v_fma_f32 v99, v99, v222, s94\n"
    "v_mfma_f32_16x16x32_bf16 a[60:63], v[164:167], v[156:159], a[60:63]\n"
    "s_waitcnt lgkmcnt(2)\n"
    "v_cvt_pk_bf16_f32 v184, v96, v97\n"
    "v_cvt_pk_bf16_f32 v185, v98, v99\n"
    "v_mfma_f32_16x16x32_bf16 a[60:63], v[168:171], v[44:47], a[60:63]\n"
    "ds_read_b128 v[160:163], v201 offset:8192\n"
    "v_accvgpr_read_b32 v100, a36\n"
    "v_mfma_f32_16x16x32_bf16 a[60:63], v[172:175], v[76:79], a[60:63]\n"
    "v_accvgpr_read_b32 v101, a37\n"
    "v_accvgpr_read_b32 v102, a38\n"
    "v_mfma_f32_16x16x32_bf16 a[56:59], v[168:171], v[40:43], a[56:59]\n"
    "v_accvgpr_read_b32 v103, a39\n"
    "v_fma_f32 v100, v100, v224, s94\n"
    "v_mfma_f32_16x16x32_bf16 a[56:59], v[172:175], v[72:75], a[56:59]\n"
    "v_fma_f32 v101, v101, v224, s94\n"
    "v_fma_f32 v102, v102, v224, s94\n"
    "v_mfma_f32_16x16x32_bf16 a[52:55], v[168:171], v[36:39], a[52:55]\n"
    "ds_read_b128 v[164:167], v201 offset:9216\n"
    "v_fma_f32 v103, v103, v224, s94\n"
    "v_mfma_f32_16x16x32_bf16 a[52:55], v[172:175], v[68:71], a[52:55]\n"
    "v_cvt_pk_bf16_f32 v188, v100, v101\n"
    "v_cvt_pk_bf16_f32 v189, v102, v103\n"
    "v_mfma_f32_16x16x32_bf16 a[48:51], v[168:171], v[32:35], a[48:51]\n"
    "v_accvgpr_read_b32 v104, a40\n"
    "v_accvgpr_read_b32 v105, a41\n"
    "v_mfma_f32_16x16x32_bf16 a[48:51], v[172:175], v[64:67], a[48:51]\n"
    "s_waitcnt lgkmcnt(2)\n"
    "v_accvgpr_read_b32 v106, a42\n"
    "v_accvgpr_read_b32 v107, a43\n"
    "v_mfma_f32_16x16x32_bf16 a[48:51], v[176:179], v[232:235], a[48:51]\n"
    "ds_read_b128 v[168:171], v201 offset:32768\n"
    "v_fma_f32 v104, v104, v226, s94\n"
    "v_mfma_f32_16x16x32_bf16 a[48:51], v[180:183], v[80:83], a[48:51]\n"
    "v_fma_f32 v105, v105, v226, s94\n"
    "v_fma_f32 v106, v106, v226, s94\n"
    "v_mfma_f32_16x16x32_bf16 a[52:55], v[176:179], v[236:239], a[52:55]\n"
    "v_fma_f32 v107, v107, v226, s94\n"
    "v_cvt_pk_bf16_f32 v248, v104, v105\n"
    "v_mfma_f32_16x16x32_bf16 a[52:55], v[180:183], v[84:87], a[52:55]\n"
    "v_cvt_pk_bf16_f32 v249, v106, v107\n"
    "v_accvgpr_read_b32 v108, a44\n"
    "v_mfma_f32_16x16x32_bf16 a[56:59], v[176:179], v[240:243], a[56:59]\n"
    "ds_read_b128 v[172:175], v201 offset:33792\n"
    "v_accvgpr_read_b32 v109, a45\n"
    "v_mfma_f32_16x16x32_bf16 a[56:59], v[180:183], v[88:91], a[56:59]\n"
    "v_accvgpr_read_b32 v110, a46\n"
    "v_accvgpr_read_b32 v111, a47\n"
    "v_mfma_f32_16x16x32_bf16 a[60:63], v[176:179], v[244:247], a[60:63]\n"
    "v_fma_f32 v108, v108, v228, s94\n"
    "v_fma_f32 v109, v109, v228, s94\n"
    "v_mfma_f32_16x16x32_bf16 a[60:63], v[180:183], v[92:95], a[60:63]\n"
    "s_waitcnt lgkmcnt(2)\n"
    "v_fma_f32 v110, v110, v228, s94\n"
    "v_fma_f32 v111, v111, v228, s94\n"
    "v_mfma_f32_16x16x32_bf16 a[64:67], v[160:163], v[128:131], a[64:67]\n"
    "ds_read_b128 v[176:179], v207 offset:8192\n"
    "v_cvt_pk_bf16_f32 v252, v108, v109\n"
    "v_mfma_f32_16x16x32_bf16 a[64:67], v[164:167], v[144:147], a[64:67]\n"
    "v_cvt_pk_bf16_f32 v253, v110, v111\n"
    "v_accvgpr_read_b32 v96, a48\n"
    "v_mfma_f32_16x16x32_bf16 a[68:71], v[160:163], v[132:135], a[68:71]\n"
    "v_accvgpr_read_b32 v97, a49\n"
    "v_accvgpr_read_b32 v98, a50\n"
    "v_mfma_f32_16x16x32_bf16 a[68:71], v[164:167], v[148:151], a[68:71]\n"
    "v_accvgpr_read_b32 v99, a51\n"
    "v_fma_f32 v96, v96, v222, s94\n"
    "v_mfma_f32_16x16x32_bf16 a[72:75], v[160:163], v[136:139], a[72:75]\n"
    "ds_read_b128 v[180:183], v207 offset:9216\n"
    "v_fma_f32 v97, v97, v222, s94\n"
    "v_mfma_f32_16x16x32_bf16 a[72:75], v[164:167], v[152:155], a[72:75]\n"
    "v_fma_f32 v98, v98, v222, s94\n"
    "v_fma_f32 v99, v99, v222, s94\n"
    "v_mfma_f32_16x16x32_bf16 a[76:79], v[160:163], v[140:143], a[76:79]\n"
    "v_cvt_pk_bf16_f32 v186, v96, v97\n"
    "v_cvt_pk_bf16_f32 v187, v98, v99\n"
    "v_mfma_f32_16x16x32_bf16 a[76:79], v[164:167], v[156:159], a[76:79]\n"
    "s_waitcnt lgkmcnt(2)\n"
    "buffer_store_dwordx4 v[184:187], v205, s[28:31], 0 offen offset:64 sc0 sc1\n"
    "v_accvgpr_read_b32 v100, a52\n"
    "v_accvgpr_read_b32 v101, a53\n"
    "v_accvgpr_read_b32 v102, a54\n"
    "v_mfma_f32_16x16x32_bf16 a[76:79], v[168:171], v[44:47], a[76:79]\n"
    "ds_read_b128 v[160:163], v201 offset:10240\n"
    "v_accvgpr_read_b32 v103, a55\n"
    "v_mfma_f32_16x16x32_bf16 a[76:79], v[172:175], v[76:79], a[76:79]\n"
    "v_fma_f32 v100, v100, v224, s94\n"
    "v_fma_f32 v101, v101, v224, s94\n"
    "v_mfma_f32_16x16x32_bf16 a[72:75], v[168:171], v[40:43], a[72:75]\n"
    "v_fma_f32 v102, v102, v224, s94\n"
    "v_fma_f32 v103, v103, v224, s94\n"
    "v_mfma_f32_16x16x32_bf16 a[72:75], v[172:175], v[72:75], a[72:75]\n"
    "v_cvt_pk_bf16_f32 v190, v100, v101\n"
    "v_cvt_pk_bf16_f32 v191, v102, v103\n"
    "v_mfma_f32_16x16x32_bf16 a[68:71], v[168:171], v[36:39], a[68:71]\n"
    "ds_read_b128 v[164:167], v201 offset:11264\n"
    "v_mfma_f32_16x16x32_bf16 a[68:71], v[172:175], v[68:71], a[68:71]\n"
    "buffer_store_dwordx4 v[188:191], v205, s[28:31], s62 offen offset:64 sc0 sc1\n"
    "v_accvgpr_read_b32 v104, a56\n"
    "v_accvgpr_read_b32 v105, a57\n"
    "v_accvgpr_read_b32 v106, a58\n"
    "v_mfma_f32_16x16x32_bf16 a[64:67], v[168:171], v[32:35], a[64:67]\n"
    "v_accvgpr_read_b32 v107, a59\n"
    "v_fma_f32 v104, v104, v226, s94\n"
    "v_mfma_f32_16x16x32_bf16 a[64:67], v[172:175], v[64:67], a[64:67]\n"
    "s_waitcnt lgkmcnt(2)\n"
    "v_fma_f32 v105, v105, v226, s94\n"
    "v_fma_f32 v106, v106, v226, s94\n"
    "v_mfma_f32_16x16x32_bf16 a[64:67], v[176:179], v[232:235], a[64:67]\n"
    "ds_read_b128 v[168:171], v201 offset:34816\n"
    "v_fma_f32 v107, v107, v226, s94\n"
    "v_mfma_f32_16x16x32_bf16 a[64:67], v[180:183], v[80:83], a[64:67]\n"
    "v_cvt_pk_bf16_f32 v250, v104, v105\n"
    "v_cvt_pk_bf16_f32 v251, v106, v107\n"
    "v_mfma_f32_16x16x32_bf16 a[68:71], v[176:179], v[236:239], a[68:71]\n"
    "buffer_store_dwordx4 v[248:251], v205, s[28:31], s63 offen offset:64 sc0 sc1\n"
    "v_accvgpr_read_b32 v108, a60\n"
    "v_accvgpr_read_b32 v109, a61\n"
    "v_accvgpr_read_b32 v110, a62\n"
    "v_mfma_f32_16x16x32_bf16 a[68:71], v[180:183], v[84:87], a[68:71]\n"
    "v_accvgpr_read_b32 v111, a63\n"
    "v_fma_f32 v108, v108, v228, s94\n"
    "v_mfma_f32_16x16x32_bf16 a[72:75], v[176:179], v[240:243], a[72:75]\n"
    "ds_read_b128 v[172:175], v201 offset:35840\n"
    "v_fma_f32 v109, v109, v228, s94\n"
    "v_mfma_f32_16x16x32_bf16 a[72:75], v[180:183], v[88:91], a[72:75]\n"
    "v_fma_f32 v110, v110, v228, s94\n"
    "v_fma_f32 v111, v111, v228, s94\n"
    "v_mfma_f32_16x16x32_bf16 a[76:79], v[176:179], v[244:247], a[76:79]\n"
    "v_cvt_pk_bf16_f32 v254, v108, v109\n"
    "v_cvt_pk_bf16_f32 v255, v110, v111\n"
    "v_mfma_f32_16x16x32_bf16 a[76:79], v[180:183], v[92:95], a[76:79]\n"
    "s_waitcnt lgkmcnt(2)\n"
    "buffer_store_dwordx4 v[252:255], v205, s[28:31], s64 offen offset:64 sc0 sc1\n"
    "v_accvgpr_read_b32 v96, a64\n"
    "v_accvgpr_read_b32 v97, a65\n"
    "v_accvgpr_read_b32 v98, a66\n"
    "v_mfma_f32_16x16x32_bf16 a[80:83], v[160:163], v[128:131], a[80:83]\n"
    "ds_read_b128 v[176:179], v207 offset:10240\n"
    "v_accvgpr_read_b32 v99, a67\n"
    "v_mfma_f32_16x16x32_bf16 a[80:83], v[164:167], v[144:147], a[80:83]\n"
    "v_fma_f32 v96, v96, v222, s94\n"
    "v_fma_f32 v97, v97, v222, s94\n"
    "v_mfma_f32_16x16x32_bf16 a[84:87], v[160:163], v[132:135], a[84:87]\n"
    "v_fma_f32 v98, v98, v222, s94\n"
    "v_fma_f32 v99, v99, v222, s94\n"
    "v_mfma_f32_16x16x32_bf16 a[84:87], v[164:167], v[148:151], a[84:87]\n"
    "v_cvt_pk_bf16_f32 v48, v96, v97\n"
    "v_cvt_pk_bf16_f32 v49, v98, v99\n"
    "v_mfma_f32_16x16x32_bf16 a[88:91], v[160:163], v[136:139], a[88:91]\n"
    "ds_read_b128 v[180:183], v207 offset:11264\n"
    "v_accvgpr_read_b32 v100, a68\n"
    "v_mfma_f32_16x16x32_bf16 a[88:91], v[164:167], v[152:155], a[88:91]\n"
    "v_accvgpr_read_b32 v101, a69\n"
    "v_accvgpr_read_b32 v102, a70\n"
    "v_mfma_f32_16x16x32_bf16 a[92:95], v[160:163], v[140:143], a[92:95]\n"
    "v_accvgpr_read_b32 v103, a71\n"
    "v_fma_f32 v100, v100, v224, s94\n"
    "v_mfma_f32_16x16x32_bf16 a[92:95], v[164:167], v[156:159], a[92:95]\n"
    "s_waitcnt lgkmcnt(2)\n"
    "v_fma_f32 v101, v101, v224, s94\n"
    "v_fma_f32 v102, v102, v224, s94\n"
    "v_mfma_f32_16x16x32_bf16 a[92:95], v[168:171], v[44:47], a[92:95]\n"
    "ds_read_b128 v[160:163], v201 offset:12288\n"
    "v_fma_f32 v103, v103, v224, s94\n"
    "v_mfma_f32_16x16x32_bf16 a[92:95], v[172:175], v[76:79], a[92:95]\n"
    "v_cvt_pk_bf16_f32 v52, v100, v101\n"
    "v_cvt_pk_bf16_f32 v53, v102, v103\n"
    "v_mfma_f32_16x16x32_bf16 a[88:91], v[168:171], v[40:43], a[88:91]\n"
    "v_accvgpr_read_b32 v104, a72\n"
    "v_accvgpr_read_b32 v105, a73\n"
    "v_mfma_f32_16x16x32_bf16 a[88:91], v[172:175], v[72:75], a[88:91]\n"
    "v_accvgpr_read_b32 v106, a74\n"
    "v_accvgpr_read_b32 v107, a75\n"
    "v_mfma_f32_16x16x32_bf16 a[84:87], v[168:171], v[36:39], a[84:87]\n"
    "ds_read_b128 v[164:167], v201 offset:13312\n"
    "v_fma_f32 v104, v104, v226, s94\n"
    "v_mfma_f32_16x16x32_bf16 a[84:87], v[172:175], v[68:71], a[84:87]\n"
    "v_fma_f32 v105, v105, v226, s94\n"
    "v_fma_f32 v106, v106, v226, s94\n"
    "v_mfma_f32_16x16x32_bf16 a[80:83], v[168:171], v[32:35], a[80:83]\n"
    "v_fma_f32 v107, v107, v226, s94\n"
    "v_cvt_pk_bf16_f32 v56, v104, v105\n"
    "v_mfma_f32_16x16x32_bf16 a[80:83], v[172:175], v[64:67], a[80:83]\n"
    "s_waitcnt lgkmcnt(2)\n"
    "v_cvt_pk_bf16_f32 v57, v106, v107\n"
    "v_accvgpr_read_b32 v108, a76\n"
    "v_mfma_f32_16x16x32_bf16 a[80:83], v[176:179], v[232:235], a[80:83]\n"
    "ds_read_b128 v[168:171], v201 offset:36864\n"
    "v_accvgpr_read_b32 v109, a77\n"
    "v_mfma_f32_16x16x32_bf16 a[80:83], v[180:183], v[80:83], a[80:83]\n"
    "v_accvgpr_read_b32 v110, a78\n"
    "v_accvgpr_read_b32 v111, a79\n"
    "v_mfma_f32_16x16x32_bf16 a[84:87], v[176:179], v[236:239], a[84:87]\n"
    "v_fma_f32 v108, v108, v228, s94\n"
    "v_fma_f32 v109, v109, v228, s94\n"
    "v_mfma_f32_16x16x32_bf16 a[84:87], v[180:183], v[84:87], a[84:87]\n"
    "v_fma_f32 v110, v110, v228, s94\n"
    "v_fma_f32 v111, v111, v228, s94\n"
    "v_mfma_f32_16x16x32_bf16 a[88:91], v[176:179], v[240:243], a[88:91]\n"
    "ds_read_b128 v[172:175], v201 offset:37888\n"
    "v_cvt_pk_bf16_f32 v60, v108, v109\n"
    "v_mfma_f32_16x16x32_bf16 a[88:91], v[180:183], v[88:91], a[88:91]\n"
    "v_cvt_pk_bf16_f32 v61, v110, v111\n"
    "v_mfma_f32_16x16x32_bf16 a[92:95], v[176:179], v[244:247], a[92:95]\n"
    "v_mfma_f32_16x16x32_bf16 a[92:95], v[180:183], v[92:95], a[92:95]\n"
    "s_waitcnt lgkmcnt(2)\n"
    "v_accvgpr_read_b32 v96, a80\n"
    "v_accvgpr_read_b32 v97, a81\n"
    "v_mfma_f32_16x16x32_bf16 a[96:99], v[160:163], v[128:131], a[96:99]\n"
    "ds_read_b128 v[176:179], v207 offset:12288\n"
    "v_accvgpr_read_b32 v98, a82\n"
    "v_mfma_f32_16x16x32_bf16 a[96:99], v[164:167], v[144:147], a[96:99]\n"
    "v_accvgpr_read_b32 v99, a83\n"
    "v_fma_f32 v96, v96, v222, s94\n"
    "v_mfma_f32_16x16x32_bf16 a[100:103], v[160:163], v[132:135], a[100:103]\n"
    "v_fma_f32 v97, v97, v222, s94\n"
    "v_fma_f32 v98, v98, v222, s94\n"
    "v_mfma_f32_16x16x32_bf16 a[100:103], v[164:167], v[148:151], a[100:103]\n"
    "v_fma_f32 v99, v99, v222, s94\n"
    "v_cvt_pk_bf16_f32 v50, v96, v97\n"
    "v_mfma_f32_16x16x32_bf16 a[104:107], v[160:163], v[136:139], a[104:107]\n"
    "ds_read_b128 v[180:183], v207 offset:13312\n"
    "v_cvt_pk_bf16_f32 v51, v98, v99\n"
    "v_mfma_f32_16x16x32_bf16 a[104:107], v[164:167], v[152:155], a[104:107]\n"
    "buffer_store_dwordx4 v[48:51], v205, s[28:31], 0 offen offset:128 sc0 sc1\n"
    "v_accvgpr_read_b32 v100, a84\n"
    "v_accvgpr_read_b32 v101, a85\n"
    "v_accvgpr_read_b32 v102, a86\n"
    "v_mfma_f32_16x16x32_bf16 a[108:111], v[160:163], v[140:143], a[108:111]\n"
    "v_accvgpr_read_b32 v103, a87\n"
    "v_fma_f32 v100, v100, v224, s94\n"
    "v_mfma_f32_16x16x32_bf16 a[108:111], v[164:167], v[156:159], a[108:111]\n"
    "s_waitcnt lgkmcnt(2)\n"
    "s_cmp_eq_u32 s57, 0\n"
    "s_cselect_b32 s77, s74, 0\n"
    "v_mov_b32 v196, 0x40000000\n"
    "v_mov_b32 v197, 0x800\n"
    "v_cmp_gt_u32 vcc, s77, v231\n"
    "v_mov_b32 v198, 0\n"
    "v_mov_b32 v198, 0\n"
    "v_cndmask_b32 v197, v196, v197, vcc\n"
    "buffer_store_dword v221, v197, s[44:47], 0 offen sc1\n"
    "v_mfma_f32_16x16x32_bf16 a[108:111], v[168:171], v[44:47], a[108:111]\n"
    "ds_read_b128 v[160:163], v201 offset:14336\n"
    "v_fma_f32 v101, v101, v224, s94\n"
    "v_mfma_f32_16x16x32_bf16 a[108:111], v[172:175], v[76:79], a[108:111]\n"
    "v_fma_f32 v102, v102, v224, s94\n"
    "v_fma_f32 v103, v103, v224, s94\n"
    "v_mfma_f32_16x16x32_bf16 a[104:107], v[168:171], v[40:43], a[104:107]\n"
    "v_cvt_pk_bf16_f32 v54, v100, v101\n"
    "v_cvt_pk_bf16_f32 v55, v102, v103\n"
    "v_mfma_f32_16x16x32_bf16 a[104:107], v[172:175], v[72:75], a[104:107]\n"
    "buffer_store_dwordx4 v[52:55], v205, s[28:31], s62 offen offset:128 sc0 sc1\n"
    "v_accvgpr_read_b32 v104, a88\n"
    "v_accvgpr_read_b32 v105, a89\n"
    "v_accvgpr_read_b32 v106, a90\n"
    "v_mfma_f32_16x16x32_bf16 a[100:103], v[168:171], v[36:39], a[100:103]\n"
    "ds_read_b128 v[164:167], v201 offset:15360\n"
    "v_accvgpr_read_b32 v107, a91\n"
    "v_mfma_f32_16x16x32_bf16 a[100:103], v[172:175], v[68:71], a[100:103]\n"
    "v_fma_f32 v104, v104, v226, s94\n"
    "v_fma_f32 v105, v105, v226, s94\n"
    "v_mfma_f32_16x16x32_bf16 a[96:99], v[168:171], v[32:35], a[96:99]\n"
    "v_fma_f32 v106, v106, v226, s94\n"
    "v_fma_f32 v107, v107, v226, s94\n"
    "v_mfma_f32_16x16x32_bf16 a[96:99], v[172:175], v[64:67], a[96:99]\n"
    "s_waitcnt lgkmcnt(2)\n"
    "v_cvt_pk_bf16_f32 v58, v104, v105\n"
    "v_cvt_pk_bf16_f32 v59, v106, v107\n"
    "v_mfma_f32_16x16x32_bf16 a[96:99], v[176:179], v[232:235], a[96:99]\n"
    "ds_read_b128 v[168:171], v201 offset:38912\n"
    "v_mfma_f32_16x16x32_bf16 a[96:99], v[180:183], v[80:83], a[96:99]\n"
    "buffer_store_dwordx4 v[56:59], v205, s[28:31], s63 offen offset:128 sc0 sc1\n"
    "v_accvgpr_read_b32 v108, a92\n"
    "v_accvgpr_read_b32 v109, a93\n"
    "v_accvgpr_read_b32 v110, a94\n"
    "v_mfma_f32_16x16x32_bf16 a[100:103], v[176:179], v[236:239], a[100:103]\n"
    "v_accvgpr_read_b32 v111, a95\n"
    "v_fma_f32 v108, v108, v228, s94\n"
    "v_mfma_f32_16x16x32_bf16 a[100:103], v[180:183], v[84:87], a[100:103]\n"
    "v_fma_f32 v109, v109, v228, s94\n"
    "v_fma_f32 v110, v110, v228, s94\n"
    "v_mfma_f32_16x16x32_bf16 a[104:107], v[176:179], v[240:243], a[104:107]\n"
    "ds_read_b128 v[172:175], v201 offset:39936\n"
    "v_fma_f32 v111, v111, v228, s94\n"
    "v_mfma_f32_16x16x32_bf16 a[104:107], v[180:183], v[88:91], a[104:107]\n"
    "v_cvt_pk_bf16_f32 v62, v108, v109\n"
    "v_cvt_pk_bf16_f32 v63, v110, v111\n"
    "v_mfma_f32_16x16x32_bf16 a[108:111], v[176:179], v[244:247], a[108:111]\n"
    "buffer_store_dwordx4 v[60:63], v205, s[28:31], s64 offen offset:128 sc0 sc1\n"
    "v_mfma_f32_16x16x32_bf16 a[108:111], v[180:183], v[92:95], a[108:111]\n"
    "s_waitcnt lgkmcnt(2)\n"
    "v_accvgpr_read_b32 v96, a96\n"
    "v_accvgpr_read_b32 v97, a97\n"
    "v_mfma_f32_16x16x32_bf16 a[112:115], v[160:163], v[128:131], a[112:115]\n"
    "ds_read_b128 v[176:179], v207 offset:14336\n"
    "v_accvgpr_read_b32 v98, a98\n"
    "v_mfma_f32_16x16x32_bf16 a[112:115], v[164:167], v[144:147], a[112:115]\n"
    "v_accvgpr_read_b32 v99, a99\n"
    "v_fma_f32 v96, v96, v222, s94\n"
    "v_mfma_f32_16x16x32_bf16 a[116:119], v[160:163], v[132:135], a[116:119]\n"
    "v_fma_f32 v97, v97, v222, s94\n"
    "v_fma_f32 v98, v98, v222, s94\n"
    "v_mfma_f32_16x16x32_bf16 a[116:119], v[164:167], v[148:151], a[116:119]\n"
    "v_fma_f32 v99, v99, v222, s94\n"
    "v_cvt_pk_bf16_f32 v184, v96, v97\n"
    "v_mfma_f32_16x16x32_bf16 a[120:123], v[160:163], v[136:139], a[120:123]\n"
    "ds_read_b128 v[180:183], v207 offset:15360\n"
    "v_cvt_pk_bf16_f32 v185, v98, v99\n"
    "v_mfma_f32_16x16x32_bf16 a[120:123], v[164:167], v[152:155], a[120:123]\n"
    "v_accvgpr_read_b32 v100, a100\n"
    "v_accvgpr_read_b32 v101, a101\n"
    "v_mfma_f32_16x16x32_bf16 a[124:127], v[160:163], v[140:143], a[124:127]\n"
    "v_accvgpr_read_b32 v102, a102\n"
    "v_accvgpr_read_b32 v103, a103\n"
    "v_mfma_f32_16x16x32_bf16 a[124:127], v[164:167], v[156:159], a[124:127]\n"
    "s_waitcnt lgkmcnt(2)\n"
    "v_fma_f32 v100, v100, v224, s94\n"
    "v_fma_f32 v101, v101, v224, s94\n"
    "v_mfma_f32_16x16x32_bf16 a[124:127], v[168:171], v[44:47], a[124:127]\n"
    "ds_read_b128 v[160:163], v201 offset:16384\n"
    "v_fma_f32 v102, v102, v224, s94\n"
    "v_mfma_f32_16x16x32_bf16 a[124:127], v[172:175], v[76:79], a[124:127]\n"
    "v_fma_f32 v103, v103, v224, s94\n"
    "v_cvt_pk_bf16_f32 v188, v100, v101\n"
    "v_mfma_f32_16x16x32_bf16 a[120:123], v[168:171], v[40:43], a[120:123]\n"
    "v_cvt_pk_bf16_f32 v189, v102, v103\n"
    "v_accvgpr_read_b32 v104, a104\n"
    "v_mfma_f32_16x16x32_bf16 a[120:123], v[172:175], v[72:75], a[120:123]\n"
    "v_accvgpr_read_b32 v105, a105\n"
    "v_accvgpr_read_b32 v106, a106\n"
    "v_mfma_f32_16x16x32_bf16 a[116:119], v[168:171], v[36:39], a[116:119]\n"
    "ds_read_b128 v[164:167], v201 offset:17408\n"
    "v_accvgpr_read_b32 v107, a107\n"
    "v_mfma_f32_16x16x32_bf16 a[116:119], v[172:175], v[68:71], a[116:119]\n"
    "v_fma_f32 v104, v104, v226, s94\n"
    "v_fma_f32 v105, v105, v226, s94\n"
    "v_mfma_f32_16x16x32_bf16 a[112:115], v[168:171], v[32:35], a[112:115]\n"
    "v_fma_f32 v106, v106, v226, s94\n"
    "v_fma_f32 v107, v107, v226, s94\n"
    "v_mfma_f32_16x16x32_bf16 a[112:115], v[172:175], v[64:67], a[112:115]\n"
    "s_waitcnt lgkmcnt(2)\n"
    "v_cvt_pk_bf16_f32 v248, v104, v105\n"
    "v_cvt_pk_bf16_f32 v249, v106, v107\n"
    "v_mfma_f32_16x16x32_bf16 a[112:115], v[176:179], v[232:235], a[112:115]\n"
    "ds_read_b128 v[168:171], v201 offset:40960\n"
    "v_accvgpr_read_b32 v108, a108\n"
    "v_mfma_f32_16x16x32_bf16 a[112:115], v[180:183], v[80:83], a[112:115]\n"
    "v_accvgpr_read_b32 v109, a109\n"
    "v_accvgpr_read_b32 v110, a110\n"
    "v_mfma_f32_16x16x32_bf16 a[116:119], v[176:179], v[236:239], a[116:119]\n"
    "v_accvgpr_read_b32 v111, a111\n"
    "v_fma_f32 v108, v108, v228, s94\n"
    "v_mfma_f32_16x16x32_bf16 a[116:119], v[180:183], v[84:87], a[116:119]\n"
    "v_fma_f32 v109, v109, v228, s94\n"
    "v_fma_f32 v110, v110, v228, s94\n"
    "v_mfma_f32_16x16x32_bf16 a[120:123], v[176:179], v[240:243], a[120:123]\n"
    "ds_read_b128 v[172:175], v201 offset:41984\n"
    "v_fma_f32 v111, v111, v228, s94\n"
    "v_mfma_f32_16x16x32_bf16 a[120:123], v[180:183], v[88:91], a[120:123]\n"
    "v_cvt_pk_bf16_f32 v252, v108, v109\n"
    "v_cvt_pk_bf16_f32 v253, v110, v111\n"
    "v_mfma_f32_16x16x32_bf16 a[124:127], v[176:179], v[244:247], a[124:127]\n"
    "v_mfma_f32_16x16x32_bf16 a[124:127], v[180:183], v[92:95], a[124:127]\n"
    "s_waitcnt lgkmcnt(2)\n"
    "v_accvgpr_read_b32 v96, a112\n"
    "v_accvgpr_read_b32 v97, a113\n"
    "v_mfma_f32_16x16x32_bf16 a[128:131], v[160:163], v[128:131], a[128:131]\n"
    "ds_read_b128 v[176:179], v207 offset:16384\n"
    "v_accvgpr_read_b32 v98, a114\n"
    "v_mfma_f32_16x16x32_bf16 a[128:131], v[164:167], v[144:147], a[128:131]\n"
    "v_accvgpr_read_b32 v99, a115\n"
    "v_fma_f32 v96, v96, v222, s94\n"
    "v_mfma_f32_16x16x32_bf16 a[132:135], v[160:163], v[132:135], a[132:135]\n"
    "v_fma_f32 v97, v97, v222, s94\n"
    "v_fma_f32 v98, v98, v222, s94\n"
    "v_mfma_f32_16x16x32_bf16 a[132:135], v[164:167], v[148:151], a[132:135]\n"
    "v_fma_f32 v99, v99, v222, s94\n"
    "v_cvt_pk_bf16_f32 v186, v96, v97\n"
    "v_mfma_f32_16x16x32_bf16 a[136:139], v[160:163], v[136:139], a[136:139]\n"
    "ds_read_b128 v[180:183], v207 offset:17408\n"
    "v_cvt_pk_bf16_f32 v187, v98, v99\n"
    "v_mfma_f32_16x16x32_bf16 a[136:139], v[164:167], v[152:155], a[136:139]\n"
    "buffer_store_dwordx4 v[184:187], v205, s[28:31], 0 offen offset:192 sc0 sc1\n"
    "v_accvgpr_read_b32 v100, a116\n"
    "v_accvgpr_read_b32 v101, a117\n"
    "v_accvgpr_read_b32 v102, a118\n"
    "v_mfma_f32_16x16x32_bf16 a[140:143], v[160:163], v[140:143], a[140:143]\n"
    "v_accvgpr_read_b32 v103, a119\n"
    "v_fma_f32 v100, v100, v224, s94\n"
    "v_mfma_f32_16x16x32_bf16 a[140:143], v[164:167], v[156:159], a[140:143]\n"
    "s_waitcnt lgkmcnt(2)\n"
    "v_fma_f32 v101, v101, v224, s94\n"
    "v_fma_f32 v102, v102, v224, s94\n"
    "v_mfma_f32_16x16x32_bf16 a[140:143], v[168:171], v[44:47], a[140:143]\n"
    "ds_read_b128 v[160:163], v201 offset:18432\n"
    "v_fma_f32 v103, v103, v224, s94\n"
    "v_mfma_f32_16x16x32_bf16 a[140:143], v[172:175], v[76:79], a[140:143]\n"
    "v_cvt_pk_bf16_f32 v190, v100, v101\n"
    "v_cvt_pk_bf16_f32 v191, v102, v103\n"
    "v_mfma_f32_16x16x32_bf16 a[136:139], v[168:171], v[40:43], a[136:139]\n"
    "buffer_store_dwordx4 v[188:191], v205, s[28:31], s62 offen offset:192 sc0 sc1\n"
    "v_accvgpr_read_b32 v104, a120\n"
    "v_accvgpr_read_b32 v105, a121\n"
    "v_accvgpr_read_b32 v106, a122\n"
    "v_mfma_f32_16x16x32_bf16 a[136:139], v[172:175], v[72:75], a[136:139]\n"
    "v_accvgpr_read_b32 v107, a123\n"
    "v_fma_f32 v104, v104, v226, s94\n"
    "v_mfma_f32_16x16x32_bf16 a[132:135], v[168:171], v[36:39], a[132:135]\n"
    "ds_read_b128 v[164:167], v201 offset:19456\n"
    "v_fma_f32 v105, v105, v226, s94\n"
    "v_mfma_f32_16x16x32_bf16 a[132:135], v[172:175], v[68:71], a[132:135]\n"
    "v_fma_f32 v106, v106, v226, s94\n"
    "v_fma_f32 v107, v107, v226, s94\n"
    "v_mfma_f32_16x16x32_bf16 a[128:131], v[168:171], v[32:35], a[128:131]\n"
    "v_cvt_pk_bf16_f32 v250, v104, v105\n"
    "v_cvt_pk_bf16_f32 v251, v106, v107\n"
    "v_mfma_f32_16x16x32_bf16 a[128:131], v[172:175], v[64:67], a[128:131]\n"
    "s_waitcnt lgkmcnt(2)\n"
    "buffer_store_dwordx4 v[248:251], v205, s[28:31], s63 offen offset:192 sc0 sc1\n"
    "v_accvgpr_read_b32 v108, a124\n"
    "v_accvgpr_read_b32 v109, a125\n"
    "v_accvgpr_read_b32 v110, a126\n"
    "v_mfma_f32_16x16x32_bf16 a[128:131], v[176:179], v[232:235], a[128:131]\n"
    "ds_read_b128 v[168:171], v201 offset:43008\n"
    "v_accvgpr_read_b32 v111, a127\n"
    "v_mfma_f32_16x16x32_bf16 a[128:131], v[180:183], v[80:83], a[128:131]\n"
    "v_fma_f32 v108, v108, v228, s94\n"
    "v_fma_f32 v109, v109, v228, s94\n"
    "v_mfma_f32_16x16x32_bf16 a[132:135], v[176:179], v[236:239], a[132:135]\n"
    "v_fma_f32 v110, v110, v228, s94\n"
    "v_fma_f32 v111, v111, v228, s94\n"
    "v_mfma_f32_16x16x32_bf16 a[132:135], v[180:183], v[84:87], a[132:135]\n"
    "v_cvt_pk_bf16_f32 v254, v108, v109\n"
    "v_cvt_pk_bf16_f32 v255, v110, v111\n"
    "v_mfma_f32_16x16x32_bf16 a[136:139], v[176:179], v[240:243], a[136:139]\n"
    "ds_read_b128 v[172:175], v201 offset:44032\n"
    "v_mfma_f32_16x16x32_bf16 a[136:139], v[180:183], v[88:91], a[136:139]\n"
    "buffer_store_dwordx4 v[252:255], v205, s[28:31], s64 offen offset:192 sc0 sc1\n"
    "v_mfma_f32_16x16x32_bf16 a[140:143], v[176:179], v[244:247], a[140:143]\n"
    "v_mfma_f32_16x16x32_bf16 a[140:143], v[180:183], v[92:95], a[140:143]\n"
    "s_waitcnt lgkmcnt(2)\n"
    "v_accvgpr_read_b32 v96, a128\n"
    "v_accvgpr_read_b32 v97, a129\n"
    "v_mfma_f32_16x16x32_bf16 a[144:147], v[160:163], v[128:131], a[144:147]\n"
    "ds_read_b128 v[176:179], v207 offset:18432\n"
    "v_accvgpr_read_b32 v98, a130\n"
    "v_mfma_f32_16x16x32_bf16 a[144:147], v[164:167], v[144:147], a[144:147]\n"
    "v_accvgpr_read_b32 v99, a131\n"
    "v_fma_f32 v96, v96, v222, s94\n"
    "v_mfma_f32_16x16x32_bf16 a[148:151], v[160:163], v[132:135], a[148:151]\n"
    "v_fma_f32 v97, v97, v222, s94\n"
    "v_fma_f32 v98, v98, v222, s94\n"
    "v_mfma_f32_16x16x32_bf16 a[148:151], v[164:167], v[148:151], a[148:151]\n"
    "v_fma_f32 v99, v99, v222, s94\n"
    "v_cvt_pk_bf16_f32 v48, v96, v97\n"
    "v_mfma_f32_16x16x32_bf16 a[152:155], v[160:163], v[136:139], a[152:155]\n"
    "ds_read_b128 v[180:183], v207 offset:19456\n"
    "v_cvt_pk_bf16_f32 v49, v98, v99\n"
    "v_mfma_f32_16x16x32_bf16 a[152:155], v[164:167], v[152:155], a[152:155]\n"
    "v_accvgpr_read_b32 v100, a132\n"
    "v_accvgpr_read_b32 v101, a133\n"
    "v_mfma_f32_16x16x32_bf16 a[156:159], v[160:163], v[140:143], a[156:159]\n"
    "v_accvgpr_read_b32 v102, a134\n"
    "v_accvgpr_read_b32 v103, a135\n"
    "v_mfma_f32_16x16x32_bf16 a[156:159], v[164:167], v[156:159], a[156:159]\n"
    "s_waitcnt lgkmcnt(2)\n"
    "v_fma_f32 v100, v100, v224, s94\n"
    "v_fma_f32 v101, v101, v224, s94\n"
    "v_mfma_f32_16x16x32_bf16 a[156:159], v[168:171], v[44:47], a[156:159]\n"
    "ds_read_b128 v[160:163], v201 offset:20480\n"
    "v_fma_f32 v102, v102, v224, s94\n"
    "v_mfma_f32_16x16x32_bf16 a[156:159], v[172:175], v[76:79], a[156:159]\n"
    "v_fma_f32 v103, v103, v224, s94\n"
    "v_cvt_pk_bf16_f32 v52, v100, v101\n"
    "v_mfma_f32_16x16x32_bf16 a[152:155], v[168:171], v[40:43], a[152:155]\n"
    "v_cvt_pk_bf16_f32 v53, v102, v103\n"
    "v_accvgpr_read_b32 v104, a136\n"
    "v_mfma_f32_16x16x32_bf16 a[152:155], v[172:175], v[72:75], a[152:155]\n"
    "v_accvgpr_read_b32 v105, a137\n"
    "v_accvgpr_read_b32 v106, a138\n"
    "v_mfma_f32_16x16x32_bf16 a[148:151], v[168:171], v[36:39], a[148:151]\n"
    "ds_read_b128 v[164:167], v201 offset:21504\n"
    "v_accvgpr_read_b32 v107, a139\n"
    "v_mfma_f32_16x16x32_bf16 a[148:151], v[172:175], v[68:71], a[148:151]\n"
    "v_fma_f32 v104, v104, v226, s94\n"
    "v_fma_f32 v105, v105, v226, s94\n"
    "v_mfma_f32_16x16x32_bf16 a[144:147], v[168:171], v[32:35], a[144:147]\n"
    "v_fma_f32 v106, v106, v226, s94\n"
    "v_fma_f32 v107, v107, v226, s94\n"
    "v_mfma_f32_16x16x32_bf16 a[144:147], v[172:175], v[64:67], a[144:147]\n"
    "s_waitcnt lgkmcnt(2)\n"
    "v_cvt_pk_bf16_f32 v56, v104, v105\n"
    "v_cvt_pk_bf16_f32 v57, v106, v107\n"
    "v_mfma_f32_16x16x32_bf16 a[144:147], v[176:179], v[232:235], a[144:147]\n"
    "ds_read_b128 v[168:171], v201 offset:45056\n"
    "v_accvgpr_read_b32 v108, a140\n"
    "v_mfma_f32_16x16x32_bf16 a[144:147], v[180:183], v[80:83], a[144:147]\n"
    "v_accvgpr_read_b32 v109, a141\n"
    "v_accvgpr_read_b32 v110, a142\n"
    "v_mfma_f32_16x16x32_bf16 a[148:151], v[176:179], v[236:239], a[148:151]\n"
    "v_accvgpr_read_b32 v111, a143\n"
    "v_fma_f32 v108, v108, v228, s94\n"
    "v_mfma_f32_16x16x32_bf16 a[148:151], v[180:183], v[84:87], a[148:151]\n"
    "v_fma_f32 v109, v109, v228, s94\n"
    "v_fma_f32 v110, v110, v228, s94\n"
    "v_mfma_f32_16x16x32_bf16 a[152:155], v[176:179], v[240:243], a[152:155]\n"
    "ds_read_b128 v[172:175], v201 offset:46080\n"
    "v_fma_f32 v111, v111, v228, s94\n"
    "v_mfma_f32_16x16x32_bf16 a[152:155], v[180:183], v[88:91], a[152:155]\n"
    "v_cvt_pk_bf16_f32 v60, v108, v109\n"
    "v_cvt_pk_bf16_f32 v61, v110, v111\n"
    "v_mfma_f32_16x16x32_bf16 a[156:159], v[176:179], v[244:247], a[156:159]\n"
    "v_mfma_f32_16x16x32_bf16 a[156:159], v[180:183], v[92:95], a[156:159]\n"
    "s_waitcnt lgkmcnt(2)\n"
    "v_accvgpr_read_b32 v96, a144\n"
    "v_accvgpr_read_b32 v97, a145\n"
    "v_mfma_f32_16x16x32_bf16 a[160:163], v[160:163], v[128:131], a[160:163]\n"
    "ds_read_b128 v[176:179], v207 offset:20480\n"
    "v_accvgpr_read_b32 v98, a146\n"
    "v_mfma_f32_16x16x32_bf16 a[160:163], v[164:167], v[144:147], a[160:163]\n"
    "v_accvgpr_read_b32 v99, a147\n"
    "v_fma_f32 v96, v96, v222, s94\n"
    "v_mfma_f32_16x16x32_bf16 a[164:167], v[160:163], v[132:135], a[164:167]\n"
    "v_fma_f32 v97, v97, v222, s94\n"
    "v_fma_f32 v98, v98, v222, s94\n"
    "v_mfma_f32_16x16x32_bf16 a[164:167], v[164:167], v[148:151], a[164:167]\n"
    "v_fma_f32 v99, v99, v222, s94\n"
    "v_cvt_pk_bf16_f32 v50, v96, v97\n"
    "v_mfma_f32_16x16x32_bf16 a[168:171], v[160:163], v[136:139], a[168:171]\n"
    "ds_read_b128 v[180:183], v207 offset:21504\n"
    "v_cvt_pk_bf16_f32 v51, v98, v99\n"
    "v_mfma_f32_16x16x32_bf16 a[168:171], v[164:167], v[152:155], a[168:171]\n"
    "buffer_store_dwordx4 v[48:51], v205, s[28:31], 0 offen offset:256 sc0 sc1\n"
    "v_accvgpr_read_b32 v100, a148\n"
    "v_accvgpr_read_b32 v101, a149\n"
    "v_accvgpr_read_b32 v102, a150\n"
    "v_mfma_f32_16x16x32_bf16 a[172:175], v[160:163], v[140:143], a[172:175]\n"
    "v_accvgpr_read_b32 v103, a151\n"
    "v_fma_f32 v100, v100, v224, s94\n"
    "v_mfma_f32_16x16x32_bf16 a[172:175], v[164:167], v[156:159], a[172:175]\n"
    "s_waitcnt lgkmcnt(2)\n"
    "v_fma_f32 v101, v101, v224, s94\n"
    "v_fma_f32 v102, v102, v224, s94\n"
    "v_mfma_f32_16x16x32_bf16 a[172:175], v[168:171], v[44:47], a[172:175]\n"
    "ds_read_b128 v[160:163], v201 offset:22528\n"
    "v_fma_f32 v103, v103, v224, s94\n"
    "v_mfma_f32_16x16x32_bf16 a[172:175], v[172:175], v[76:79], a[172:175]\n"
    "v_cvt_pk_bf16_f32 v54, v100, v101\n"
    "v_cvt_pk_bf16_f32 v55, v102, v103\n"
    "v_mfma_f32_16x16x32_bf16 a[168:171], v[168:171], v[40:43], a[168:171]\n"
    "buffer_store_dwordx4 v[52:55], v205, s[28:31], s62 offen offset:256 sc0 sc1\n"
    "v_accvgpr_read_b32 v104, a152\n"
    "v_accvgpr_read_b32 v105, a153\n"
    "v_accvgpr_read_b32 v106, a154\n"
    "v_mfma_f32_16x16x32_bf16 a[168:171], v[172:175], v[72:75], a[168:171]\n"
    "v_accvgpr_read_b32 v107, a155\n"
    "v_fma_f32 v104, v104, v226, s94\n"
    "v_mfma_f32_16x16x32_bf16 a[164:167], v[168:171], v[36:39], a[164:167]\n"
    "ds_read_b128 v[164:167], v201 offset:23552\n"
    "v_fma_f32 v105, v105, v226, s94\n"
    "v_mfma_f32_16x16x32_bf16 a[164:167], v[172:175], v[68:71], a[164:167]\n"
    "v_fma_f32 v106, v106, v226, s94\n"
    "v_fma_f32 v107, v107, v226, s94\n"
    "v_mfma_f32_16x16x32_bf16 a[160:163], v[168:171], v[32:35], a[160:163]\n"
    "v_cvt_pk_bf16_f32 v58, v104, v105\n"
    "v_cvt_pk_bf16_f32 v59, v106, v107\n"
    "v_mfma_f32_16x16x32_bf16 a[160:163], v[172:175], v[64:67], a[160:163]\n"
    "s_waitcnt lgkmcnt(2)\n"
    "buffer_store_dwordx4 v[56:59], v205, s[28:31], s63 offen offset:256 sc0 sc1\n"
    "v_accvgpr_read_b32 v108, a156\n"
    "v_accvgpr_read_b32 v109, a157\n"
    "v_accvgpr_read_b32 v110, a158\n"
    "v_mfma_f32_16x16x32_bf16 a[160:163], v[176:179], v[232:235], a[160:163]\n"
    "ds_read_b128 v[168:171], v201 offset:47104\n"
    "v_accvgpr_read_b32 v111, a159\n"
    "v_mfma_f32_16x16x32_bf16 a[160:163], v[180:183], v[80:83], a[160:163]\n"
    "v_fma_f32 v108, v108, v228, s94\n"
    "v_fma_f32 v109, v109, v228, s94\n"
    "v_mfma_f32_16x16x32_bf16 a[164:167], v[176:179], v[236:239], a[164:167]\n"
    "v_fma_f32 v110, v110, v228, s94\n"
    "v_fma_f32 v111, v111, v228, s94\n"
    "v_mfma_f32_16x16x32_bf16 a[164:167], v[180:183], v[84:87], a[164:167]\n"
    "v_cvt_pk_bf16_f32 v62, v108, v109\n"
    "v_cvt_pk_bf16_f32 v63, v110, v111\n"
    "v_mfma_f32_16x16x32_bf16 a[168:171], v[176:179], v[240:243], a[168:171]\n"
    "ds_read_b128 v[172:175], v201 offset:48128\n"
    "v_mfma_f32_16x16x32_bf16 a[168:171], v[180:183], v[88:91], a[168:171]\n"
    "buffer_store_dwordx4 v[60:63], v205, s[28:31], s64 offen offset:256 sc0 sc1\n"
    "v_mfma_f32_16x16x32_bf16 a[172:175], v[176:179], v[244:247], a[172:175]\n"
    "v_mfma_f32_16x16x32_bf16 a[172:175], v[180:183], v[92:95], a[172:175]\n"
    "s_waitcnt lgkmcnt(2)\n"
    "v_accvgpr_read_b32 v96, a160\n"
    "v_accvgpr_read_b32 v97, a161\n"
    "v_mfma_f32_16x16x32_bf16 a[176:179], v[160:163], v[128:131], a[176:179]\n"
    "ds_read_b128 v[176:179], v207 offset:22528\n"
    "v_accvgpr_read_b32 v98, a162\n"
    "v_mfma_f32_16x16x32_bf16 a[176:179], v[164:167], v[144:147], a[176:179]\n"
    "v_accvgpr_read_b32 v99, a163\n"
    "v_fma_f32 v96, v96, v222, s94\n"
    "v_mfma_f32_16x16x32_bf16 a[180:183], v[160:163], v[132:135], a[180:183]\n"
    "v_fma_f32 v97, v97, v222, s94\n"
    "v_fma_f32 v98, v98, v222, s94\n"
    "v_mfma_f32_16x16x32_bf16 a[180:183], v[164:167], v[148:151], a[180:183]\n"
    "v_fma_f32 v99, v99, v222, s94\n"
    "v_cvt_pk_bf16_f32 v184, v96, v97\n"
    "v_mfma_f32_16x16x32_bf16 a[184:187], v[160:163], v[136:139], a[184:187]\n"
    "ds_read_b128 v[180:183], v207 offset:23552\n"
    "v_cvt_pk_bf16_f32 v185, v98, v99\n"
    "v_mfma_f32_16x16x32_bf16 a[184:187], v[164:167], v[152:155], a[184:187]\n"
    "v_accvgpr_read_b32 v100, a164\n"
    "v_accvgpr_read_b32 v101, a165\n"
    "v_mfma_f32_16x16x32_bf16 a[188:191], v[160:163], v[140:143], a[188:191]\n"
    "v_accvgpr_read_b32 v102, a166\n"
    "v_accvgpr_read_b32 v103, a167\n"
    "v_mfma_f32_16x16x32_bf16 a[188:191], v[164:167], v[156:159], a[188:191]\n"
    "s_waitcnt lgkmcnt(2)\n"
    "v_fma_f32 v100, v100, v224, s94\n"
    "v_fma_f32 v101, v101, v224, s94\n"
    "v_mfma_f32_16x16x32_bf16 a[188:191], v[168:171], v[44:47], a[188:191]\n"
    "v_fma_f32 v102, v102, v224, s94\n"
    "v_fma_f32 v103, v103, v224, s94\n"
    "v_mfma_f32_16x16x32_bf16 a[188:191], v[172:175], v[76:79], a[188:191]\n"
    "v_cvt_pk_bf16_f32 v188, v100, v101\n"
    "v_cvt_pk_bf16_f32 v189, v102, v103\n"
    "v_mfma_f32_16x16x32_bf16 a[184:187], v[168:171], v[40:43], a[184:187]\n"
    "v_accvgpr_read_b32 v104, a168\n"
    "v_accvgpr_read_b32 v105, a169\n"
    "v_mfma_f32_16x16x32_bf16 a[184:187], v[172:175], v[72:75], a[184:187]\n"
    "v_accvgpr_read_b32 v106, a170\n"
    "v_accvgpr_read_b32 v107, a171\n"
    "v_mfma_f32_16x16x32_bf16 a[180:183], v[168:171], v[36:39], a[180:183]\n"
    "v_fma_f32 v104, v104, v226, s94\n"
    "v_fma_f32 v105, v105, v226, s94\n"
    "v_mfma_f32_16x16x32_bf16 a[180:183], v[172:175], v[68:71], a[180:183]\n"
    "v_fma_f32 v106, v106, v226, s94\n"
    "v_fma_f32 v107, v107, v226, s94\n"
    "v_mfma_f32_16x16x32_bf16 a[176:179], v[168:171], v[32:35], a[176:179]\n"
    "v_cvt_pk_bf16_f32 v248, v104, v105\n"
    "v_cvt_pk_bf16_f32 v249, v106, v107\n"
    "v_mfma_f32_16x16x32_bf16 a[176:179], v[172:175], v[64:67], a[176:179]\n"
    "s_waitcnt lgkmcnt(0)\n"
    "v_accvgpr_read_b32 v108, a172\n"
    "v_accvgpr_read_b32 v109, a173\n"
    "v_mfma_f32_16x16x32_bf16 a[176:179], v[176:179], v[232:235], a[176:179]\n"
    "v_accvgpr_read_b32 v110, a174\n"
    "v_accvgpr_read_b32 v111, a175\n"
    "v_mfma_f32_16x16x32_bf16 a[176:179], v[180:183], v[80:83], a[176:179]\n"
    "v_fma_f32 v108, v108, v228, s94\n"
    "v_fma_f32 v109, v109, v228, s94\n"
    "v_mfma_f32_16x16x32_bf16 a[180:183], v[176:179], v[236:239], a[180:183]\n"
    "v_fma_f32 v110, v110, v228, s94\n"
    "v_fma_f32 v111, v111, v228, s94\n"
    "v_mfma_f32_16x16x32_bf16 a[180:183], v[180:183], v[84:87], a[180:183]\n"
    "v_cvt_pk_bf16_f32 v252, v108, v109\n"
    "v_cvt_pk_bf16_f32 v253, v110, v111\n"
    "v_mfma_f32_16x16x32_bf16 a[184:187], v[176:179], v[240:243], a[184:187]\n"
    "v_mfma_f32_16x16x32_bf16 a[184:187], v[180:183], v[88:91], a[184:187]\n"
    "v_mfma_f32_16x16x32_bf16 a[188:191], v[176:179], v[244:247], a[188:191]\n"
    "v_mfma_f32_16x16x32_bf16 a[188:191], v[180:183], v[92:95], a[188:191]\n"
    "v_accvgpr_read_b32 v96, a176\n"
    "v_accvgpr_read_b32 v97, a177\n"
    "v_accvgpr_read_b32 v98, a178\n"
    "v_accvgpr_read_b32 v99, a179\n"
    "v_fma_f32 v96, v96, v222, s94\n"
    "v_fma_f32 v97, v97, v222, s94\n"
    "v_fma_f32 v98, v98, v222, s94\n"
    "v_fma_f32 v99, v99, v222, s94\n"
    "v_cvt_pk_bf16_f32 v186, v96, v97\n"
    "v_cvt_pk_bf16_f32 v187, v98, v99\n"
    "buffer_store_dwordx4 v[184:187], v205, s[28:31], 0 offen offset:320 sc0 sc1\n"
    "v_accvgpr_read_b32 v100, a180\n"
    "v_accvgpr_read_b32 v101, a181\n"
    "v_accvgpr_read_b32 v102, a182\n"
    "v_accvgpr_read_b32 v103, a183\n"
    "v_fma_f32 v100, v100, v224, s94\n"
    "v_fma_f32 v101, v101, v224, s94\n"
    "v_fma_f32 v102, v102, v224, s94\n"
    "v_fma_f32 v103, v103, v224, s94\n"
    "v_cvt_pk_bf16_f32 v190, v100, v101\n"
    "v_cvt_pk_bf16_f32 v191, v102, v103\n"
    "buffer_store_dwordx4 v[188:191], v205, s[28:31], s62 offen offset:320 sc0 sc1\n"
    "v_accvgpr_read_b32 v104, a184\n"
    "v_accvgpr_read_b32 v105, a185\n"
    "v_accvgpr_read_b32 v106, a186\n"
    "v_accvgpr_read_b32 v107, a187\n"
    "v_fma_f32 v104, v104, v226, s94\n"
    "v_fma_f32 v105, v105, v226, s94\n"
    "v_fma_f32 v106, v106, v226, s94\n"
    "v_fma_f32 v107, v107, v226, s94\n"
    "v_cvt_pk_bf16_f32 v250, v104, v105\n"
    "v_cvt_pk_bf16_f32 v251, v106, v107\n"
    "buffer_store_dwordx4 v[248:251], v205, s[28:31], s63 offen offset:320 sc0 sc1\n"
    "v_accvgpr_read_b32 v108, a188\n"
    "v_accvgpr_read_b32 v109, a189\n"
    "v_accvgpr_read_b32 v110, a190\n"
    "v_accvgpr_read_b32 v111, a191\n"
    "v_fma_f32 v108, v108, v228, s94\n"
    "v_fma_f32 v109, v109, v228, s94\n"
    "v_fma_f32 v110, v110, v228, s94\n"
    "v_fma_f32 v111, v111, v228, s94\n"
    "v_cvt_pk_bf16_f32 v254, v108, v109\n"
    "v_cvt_pk_bf16_f32 v255, v110, v111\n"
    "buffer_store_dwordx4 v[252:255], v205, s[28:31], s64 offen offset:320 sc0 sc1\n"
    "v_add_f32 v196, v112, v116\n"
    "v_add_f32 v197, v120, v124\n"
    "v_add_f32 v196, v196, v197\n"
    "v_cmp_ngt_f32 vcc, 0x45800000, v196\n"
    "s_barrier\n"
    "s_cbranch_vccnz LSLOW\n"
    "LFIN:\n"
    "s_branch LEND\n"
    "LSPRE0:\n"
    "LSPIN0:\n"
    "s_sleep 1\n"
    "buffer_load_dwordx2 v[218:219], v213, s[44:47], 0 offen sc1\n"
    "s_waitcnt vmcnt(0)\n"
    "s_add_u32 s73, s73, 1\n"
    "s_cmp_gt_u32 s73, 200000\n"
    "s_cbranch_scc1 LSPB0\n"
    "v_cmp_ne_u32 s[80:81], s67, v219\n"
    "v_cmp_gt_u32 vcc, s66, v218\n"
    "s_or_b64 vcc, vcc, s[80:81]\n"
    "s_cbranch_vccnz LSPIN0\n"
    "s_branch LSPB0\n"
    "LSPRE1:\n"
    "LSPIN1:\n"
    "s_sleep 1\n"
    "buffer_load_dwordx2 v[218:219], v213, s[44:47], 0 offen sc1\n"
    "s_waitcnt vmcnt(0)\n"
    "s_add_u32 s73, s73, 1\n"
    "s_cmp_gt_u32 s73, 200000\n"
    "s_cbranch_scc1 LSPB1\n"
    "v_cmp_ne_u32 s[80:81], s67, v219\n"
    "v_cmp_gt_u32 vcc, s66, v218\n"
    "s_or_b64 vcc, vcc, s[80:81]\n"
    "s_cbranch_vccnz LSPIN1\n"
    "s_branch LSPB1\n"
    "LSPRE2:\n"
    "LSPIN2:\n"
    "s_sleep 1\n"
    "buffer_load_dwordx2 v[218:219], v213, s[44:47], 0 offen sc1\n"
    "s_waitcnt vmcnt(0)\n"
    "s_add_u32 s73, s73, 1\n"
    "s_cmp_gt_u32 s73, 200000\n"
    "s_cbranch_scc1 LSPB2\n"
    "v_cmp_ne_u32 s[80:81], s67, v219\n"
    "v_cmp_gt_u32 vcc, s66, v218\n"
    "s_or_b64 vcc, vcc, s[80:81]\n"
    "s_cbranch_vccnz LSPIN2\n"
    "s_branch LSPB2\n"
    "LSPRE3:\n"
    "LSPIN3:\n"
    "s_sleep 1\n"
    "buffer_load_dwordx2 v[218:219], v213, s[44:47], 0 offen sc1\n"
    "s_waitcnt vmcnt(0)\n"
    "s_add_u32 s73, s73, 1\n"
    "s_cmp_gt_u32 s73, 200000\n"
    "s_cbranch_scc1 LSPB3\n"
    "v_cmp_ne_u32 s[80:81], s67, v219\n"
    "v_cmp_gt_u32 vcc, s66, v218\n"
    "s_or_b64 vcc, vcc, s[80:81]\n"
    "s_cbranch_vccnz LSPIN3\n"
    "s_branch LSPB3\n"
    "LSPRE4:\n"
    "LSPIN4:\n"
    "s_sleep 1\n"
    "buffer_load_dwordx2 v[218:219], v213, s[44:47], 0 offen sc1\n"
    "s_waitcnt vmcnt(0)\n"
    "s_add_u32 s73, s73, 1\n"
    "s_cmp_gt_u32 s73, 200000\n"
    "s_cbranch_scc1 LSPB4\n"
    "v_cmp_ne_u32 s[80:81], s67, v219\n"
    "v_cmp_gt_u32 vcc, s66, v218\n"
    "s_or_b64 vcc, vcc, s[80:81]\n"
    "s_cbranch_vccnz LSPIN4\n"
    "s_branch LSPB4\n"
    "LSPRE5:\n"
    "LSPIN5:\n"
    "s_sleep 1\n"
    "buffer_load_dwordx2 v[218:219], v213, s[44:47], 0 offen sc1\n"
    "s_waitcnt vmcnt(0)\n"
    "s_add_u32 s73, s73, 1\n"
    "s_cmp_gt_u32 s73, 200000\n"
    "s_cbranch_scc1 LSPB5\n"
    "v_cmp_ne_u32 s[80:81], s67, v219\n"
    "v_cmp_gt_u32 vcc, s66, v218\n"
    "s_or_b64 vcc, vcc, s[80:81]\n"
    "s_cbranch_vccnz LSPIN5\n"
    "s_branch LSPB5\n"
    "LSLOW:\n"
    "s_waitcnt vmcnt(0)\n"
    "s_nop 15\n"
    "s_mul_i32 s62, s58, 0x9000\n"
    "v_add_u32 v205, s62, v200\n"
    "s_mov_b32 s63, 0\n"
    "v_mov_b32 v128, 0\n"
    "v_mov_b32 v129, 0\n"
    "v_mov_b32 v130, 0\n"
    "v_mov_b32 v131, 0\n"
    "v_mov_b32 v96, 0xff61b1e6\n"
    "v_mov_b32 v100, 0xff61b1e6\n"
    "v_mov_b32 v104, 0xff61b1e6\n"
    "v_mov_b32 v108, 0xff61b1e6\n"
    "v_mfma_f32_16x16x32_bf16 a[0:3], v[128:131], v[128:131], 0\n"
    "v_mfma_f32_16x16x32_bf16 a[4:7], v[128:131], v[128:131], 0\n"
    "v_mfma_f32_16x16x32_bf16 a[8:11], v[128:131], v[128:131], 0\n"
    "v_mfma_f32_16x16x32_bf16 a[12:15], v[128:131], v[128:131], 0\n"
    "v_mfma_f32_16x16x32_bf16 a[16:19], v[128:131], v[128:131], 0\n"
    "v_mfma_f32_16x16x32_bf16 a[20:23], v[128:131], v[128:131], 0\n"
    "v_mfma_f32_16x16x32_bf16 a[24:27], v[128:131], v[128:131], 0\n"
    "v_mfma_f32_16x16x32_bf16 a[28:31], v[128:131], v[128:131], 0\n"
    "v_mfma_f32_16x16x32_bf16 a[32:35], v[128:131], v[128:131], 0\n"
    "v_mfma_f32_16x16x32_bf16 a[36:39], v[128:131], v[128:131], 0\n"
    "v_mfma_f32_16x16x32_bf16 a[40:43], v[128:131], v[128:131], 0\n"
    "v_mfma_f32_16x16x32_bf16 a[44:47], v[128:131], v[128:131], 0\n"
    "v_mfma_f32_16x16x32_bf16 a[48:51], v[128:131], v[128:131], 0\n"
    "v_mfma_f32_16x16x32_bf16 a[52:55], v[128:131], v[128:131], 0\n"
    "v_mfma_f32_16x16x32_bf16 a[56:59], v[128:131], v[128:131], 0\n"
    "v_mfma_f32_16x16x32_bf16 a[60:63], v[128:131], v[128:131], 0\n"
    "v_mfma_f32_16x16x32_bf16 a[64:67], v[128:131], v[128:131], 0\n"
    "v_mfma_f32_16x16x32_bf16 a[68:71], v[128:131], v[128:131], 0\n"
    "v_mfma_f32_16x16x32_bf16 a[72:75], v[128:131], v[128:131], 0\n"
    "v_mfma_f32_16x16x32_bf16 a[76:79], v[128:131], v[128:131], 0\n"
    "v_mfma_f32_16x16x32_bf16 a[80:83], v[128:131], v[128:131], 0\n"
    "v_mfma_f32_16x16x32_bf16 a[84:87], v[128:131], v[128:131], 0\n"
    "v_mfma_f32_16x16x32_bf16 a[88:91], v[128:131], v[128:131], 0\n"
    "v_mfma_f32_16x16x32_bf16 a[92:95], v[128:131], v[128:131], 0\n"
    "v_mfma_f32_16x16x32_bf16 a[96:99], v[128:131], v[128:131], 0\n"
    "v_mfma_f32_16x16x32_bf16 a[100:103], v[128:131], v[128:131], 0\n"
    "v_mfma_f32_16x16x32_bf16 a[104:107], v[128:131], v[128:131], 0\n"
    "v_mfma_f32_16x16x32_bf16 a[108:111], v[128:131], v[128:131], 0\n"
    "v_mfma_f32_16x16x32_bf16 a[112:115], v[128:131], v[128:131], 0\n"
    "v_mfma_f32_16x16x32_bf16 a[116:119], v[128:131], v[128:131], 0\n"
    "v_mfma_f32_16x16x32_bf16 a[120:123], v[128:131], v[128:131], 0\n"
    "v_mfma_f32_16x16x32_bf16 a[124:127], v[128:131], v[128:131], 0\n"
    "v_mfma_f32_16x16x32_bf16 a[128:131], v[128:131], v[128:131], 0\n"
    "v_mfma_f32_16x16x32_bf16 a[132:135], v[128:131], v[128:131], 0\n"
    "v_mfma_f32_16x16x32_bf16 a[136:139], v[128:131], v[128:131], 0\n"
    "v_mfma_f32_16x16x32_bf16 a[140:143], v[128:131], v[128:131], 0\n"
    "v_mfma_f32_16x16x32_bf16 a[144:147], v[128:131], v[128:131], 0\n"
    "v_mfma_f32_16x16x32_bf16 a[148:151], v[128:131], v[128:131], 0\n"
    "v_mfma_f32_16x16x32_bf16 a[152:155], v[128:131], v[128:131], 0\n"
    "v_mfma_f32_16x16x32_bf16 a[156:159], v[128:131], v[128:131], 0\n"
    "v_mfma_f32_16x16x32_bf16 a[160:163], v[128:131], v[128:131], 0\n"
    "v_mfma_f32_16x16x32_bf16 a[164:167], v[128:131], v[128:131], 0\n"
    "v_mfma_f32_16x16x32_bf16 a[168:171], v[128:131], v[128:131], 0\n"
    "v_mfma_f32_16x16x32_bf16 a[172:175], v[128:131], v[128:131], 0\n"
    "v_mfma_f32_16x16x32_bf16 a[176:179], v[128:131], v[128:131], 0\n"
    "v_mfma_f32_16x16x32_bf16 a[180:183], v[128:131], v[128:131], 0\n"
    "v_mfma_f32_16x16x32_bf16 a[184:187], v[128:131], v[128:131], 0\n"
    "v_mfma_f32_16x16x32_bf16 a[188:191], v[128:131], v[128:131], 0\n"
    "v_mfma_f32_16x16x32_bf16 v[112:115], v[128:131], v[128:131], 0\n"
    "v_mfma_f32_16x16x32_bf16 v[116:119], v[128:131], v[128:131], 0\n"
    "v_mfma_f32_16x16x32_bf16 v[120:123], v[128:131], v[128:131], 0\n"
    "v_mfma_f32_16x16x32_bf16 v[124:127], v[128:131], v[128:131], 0\n"
    "s_nop 15\n"
    "LSLOOP:\n"
    "s_add_u32 s78, s63, 0\n"
    "s_add_u32 m0, s62, 0\n"
    "s_nop 0\n"
    "buffer_load_dwordx4 v200, s[36:39], s78 offen sc1 lds\n"
    "buffer_load_dwordx4 v200, s[36:39], s78 offen offset:1024 sc1 lds\n"
    "buffer_load_dwordx4 v200, s[36:39], s78 offen offset:2048 sc1 lds\n"
    "buffer_load_dwordx4 v200, s[36:39], s78 offen offset:3072 sc1 lds\n"
    "s_add_u32 s78, s63, 0x1000\n"
    "s_add_u32 m0, s62, 0x1000\n"
    "s_nop 0\n"
    "buffer_load_dwordx4 v200, s[36:39], s78 offen sc1 lds\n"
    "buffer_load_dwordx4 v200, s[36:39], s78 offen offset:1024 sc1 lds\n"
    "buffer_load_dwordx4 v200, s[36:39], s78 offen offset:2048 sc1 lds\n"
    "buffer_load_dwordx4 v200, s[36:39], s78 offen offset:3072 sc1 lds\n"
    "s_add_u32 s78, s63, 0x2000\n"
    "s_add_u32 m0, s62, 0x2000\n"
    "s_nop 0\n"
    "buffer_load_dwordx4 v200, s[36:39], s78 offen sc1 lds\n"
    "buffer_load_dwordx4 v200, s[36:39], s78 offen offset:1024 sc1 lds\n"
    "buffer_load_dwordx4 v200, s[36:39], s78 offen offset:2048 sc1 lds\n"
    "buffer_load_dwordx4 v200, s[36:39], s78 offen offset:3072 sc1 lds\n"
    "s_add_u32 s78, s63, 0\n"
    "s_add_u32 m0, s62, 0x3000\n"
    "s_nop 0\n"
    "buffer_load_dwordx4 v200, s[40:43], s78 offen sc1 lds\n"
    "buffer_load_dwordx4 v200, s[40:43], s78 offen offset:1024 sc1 lds\n"
    "buffer_load_dwordx4 v200, s[40:43], s78 offen offset:2048 sc1 lds\n"
    "buffer_load_dwordx4 v200, s[40:43], s78 offen offset:3072 sc1 lds\n"
    "s_add_u32 s78, s63, 0x1000\n"
    "s_add_u32 m0, s62, 0x4000\n"
    "s_nop 0\n"
    "buffer_load_dwordx4 v200, s[40:43], s78 offen sc1 lds\n"
    "buffer_load_dwordx4 v200, s[40:43], s78 offen offset:1024 sc1 lds\n"
    "buffer_load_dwordx4 v200, s[40:43], s78 offen offset:2048 sc1 lds\n"
    "buffer_load_dwordx4 v200, s[40:43], s78 offen offset:3072 sc1 lds\n"
    "s_add_u32 s78, s63, 0x2000\n"
    "s_add_u32 m0, s62, 0x5000\n"
    "s_nop 0\n"
    "buffer_load_dwordx4 v200, s[40:43], s78 offen sc1 lds\n"
    "buffer_load_dwordx4 v200, s[40:43], s78 offen offset:1024 sc1 lds\n"
    "buffer_load_dwordx4 v200, s[40:43], s78 offen offset:2048 sc1 lds\n"
    "buffer_load_dwordx4 v200, s[40:43], s78 offen offset:3072 sc1 lds\n"
    "s_add_u32 s78, s63, 0x3000\n"
    "s_add_u32 m0, s62, 0x6000\n"
    "s_nop 0\n"
    "buffer_load_dwordx4 v200, s[40:43], s78 offen sc1 lds\n"
    "buffer_load_dwordx4 v200, s[40:43], s78 offen offset:1024 sc1 lds\n"
    "buffer_load_dwordx4 v200, s[40:43], s78 offen offset:2048 sc1 lds\n"
    "buffer_load_dwordx4 v200, s[40:43], s78 offen offset:3072 sc1 lds\n"
    "s_add_u32 s78, s63, 0x4000\n"
    "s_add_u32 m0, s62, 0x7000\n"
    "s_nop 0\n"
    "buffer_load_dwordx4 v200, s[40:43], s78 offen sc1 lds\n"
    "buffer_load_dwordx4 v200, s[40:43], s78 offen offset:1024 sc1 lds\n"
    "buffer_load_dwordx4 v200, s[40:43], s78 offen offset:2048 sc1 lds\n"
    "buffer_load_dwordx4 v200, s[40:43], s78 offen offset:3072 sc1 lds\n"
    "s_add_u32 s78, s63, 0x5000\n"
    "s_add_u32 m0, s62, 0x8000\n"
    "s_nop 0\n"
    "buffer_load_dwordx4 v200, s[40:43], s78 offen sc1 lds\n"
    "buffer_load_dwordx4 v200, s[40:43], s78 offen offset:1024 sc1 lds\n"
    "buffer_load_dwordx4 v200, s[40:43], s78 offen offset:2048 sc1 lds\n"
    "buffer_load_dwordx4 v200, s[40:43], s78 offen offset:3072 sc1 lds\n"
    "s_waitcnt vmcnt(0)\n"
    "ds_read_b128 v[160:163], v205\n"
    "ds_read_b128 v[164:167], v205 offset:1024\n"
    "s_waitcnt lgkmcnt(1)\n"
    "v_mfma_f32_16x16x32_bf16 v[32:35], v[160:163], a[192:195], 0\n"
    "s_waitcnt lgkmcnt(0)\n"
    "v_mfma_f32_16x16x32_bf16 v[32:35], v[164:167], a[196:199], v[32:35]\n"
    "v_mfma_f32_16x16x32_bf16 v[36:39], v[164:167], a[220:223], 0\n"
    "v_mfma_f32_16x16x32_bf16 v[36:39], v[160:163], a[216:219], v[36:39]\n"
    "v_mfma_f32_16x16x32_bf16 v[40:43], v[160:163], a[240:243], 0\n"
    "v_mfma_f32_16x16x32_bf16 v[40:43], v[164:167], a[244:247], v[40:43]\n"
    "v_mfma_f32_16x16x32_bf16 v[44:47], v[164:167], v[12:15], 0\n"
    "v_mfma_f32_16x16x32_bf16 v[44:47], v[160:163], v[8:11], v[44:47]\n"
    "ds_read_b128 v[168:171], v205 offset:2048\n"
    "ds_read_b128 v[172:175], v205 offset:3072\n"
    "s_waitcnt lgkmcnt(1)\n"
    "v_mfma_f32_16x16x32_bf16 v[44:47], v[168:171], v[16:19], v[44:47]\n"
    "s_waitcnt lgkmcnt(0)\n"
    "v_mfma_f32_16x16x32_bf16 v[44:47], v[172:175], v[20:23], v[44:47]\n"
    "v_mfma_f32_16x16x32_bf16 v[40:43], v[172:175], a[252:255], v[40:43]\n"
    "v_mfma_f32_16x16x32_bf16 v[40:43], v[168:171], a[248:251], v[40:43]\n"
    "v_mfma_f32_16x16x32_bf16 v[36:39], v[168:171], a[224:227], v[36:39]\n"
    "v_mfma_f32_16x16x32_bf16 v[36:39], v[172:175], a[228:231], v[36:39]\n"
    "v_mfma_f32_16x16x32_bf16 v[32:35], v[172:175], a[204:207], v[32:35]\n"
    "v_mfma_f32_16x16x32_bf16 v[32:35], v[168:171], a[200:203], v[32:35]\n"
    "ds_read_b128 v[176:179], v205 offset:4096\n"
    "ds_read_b128 v[180:183], v205 offset:5120\n"
    "s_waitcnt lgkmcnt(1)\n"
    "v_mfma_f32_16x16x32_bf16 v[32:35], v[176:179], a[208:211], v[32:35]\n"
    "s_waitcnt lgkmcnt(0)\n"
    "v_mfma_f32_16x16x32_bf16 v[32:35], v[180:183], a[212:215], v[32:35]\n"
    "v_mfma_f32_16x16x32_bf16 v[36:39], v[180:183], a[236:239], v[36:39]\n"
    "v_mfma_f32_16x16x32_bf16 v[36:39], v[176:179], a[232:235], v[36:39]\n"
    "v_mfma_f32_16x16x32_bf16 v[40:43], v[176:179], v[0:3], v[40:43]\n"
    "v_mfma_f32_16x16x32_bf16 v[40:43], v[180:183], v[4:7], v[40:43]\n"
    "v_mfma_f32_16x16x32_bf16 v[44:47], v[180:183], v[28:31], v[44:47]\n"
    "v_mfma_f32_16x16x32_bf16 v[44:47], v[176:179], v[24:27], v[44:47]\n"
    "ds_read_b128 v[160:163], v205 offset:10240\n"
    "ds_read_b128 v[164:167], v205 offset:11264\n"
    "s_waitcnt lgkmcnt(1)\n"
    "v_mfma_f32_16x16x32_bf16 v[60:63], v[160:163], v[24:27], 0\n"
    "s_waitcnt lgkmcnt(0)\n"
    "v_mfma_f32_16x16x32_bf16 v[60:63], v[164:167], v[28:31], v[60:63]\n"
    "v_mfma_f32_16x16x32_bf16 v[56:59], v[164:167], v[4:7], 0\n"
    "v_mfma_f32_16x16x32_bf16 v[56:59], v[160:163], v[0:3], v[56:59]\n"
    "v_mfma_f32_16x16x32_bf16 v[52:55], v[160:163], a[232:235], 0\n"
    "v_mfma_f32_16x16x32_bf16 v[52:55], v[164:167], a[236:239], v[52:55]\n"
    "v_mfma_f32_16x16x32_bf16 v[48:51], v[164:167], a[212:215], 0\n"
    "v_mfma_f32_16x16x32_bf16 v[48:51], v[160:163], a[208:211], v[48:51]\n"
    "ds_read_b128 v[168:171], v205 offset:8192\n"
    "ds_read_b128 v[172:175], v205 offset:9216\n"
    "s_waitcnt lgkmcnt(1)\n"
    "v_mfma_f32_16x16x32_bf16 v[48:51], v[168:171], a[200:203], v[48:51]\n"
    "s_waitcnt lgkmcnt(0)\n"
    "v_mfma_f32_16x16x32_bf16 v[48:51], v[172:175], a[204:207], v[48:51]\n"
    "v_mfma_f32_16x16x32_bf16 v[52:55], v[172:175], a[228:231], v[52:55]\n"
    "v_mfma_f32_16x16x32_bf16 v[52:55], v[168:171], a[224:227], v[52:55]\n"
    "v_mfma_f32_16x16x32_bf16 v[56:59], v[168:171], a[248:251], v[56:59]\n"
    "v_mfma_f32_16x16x32_bf16 v[56:59], v[172:175], a[252:255], v[56:59]\n"
    "v_mfma_f32_16x16x32_bf16 v[60:63], v[172:175], v[20:23], v[60:63]\n"
    "v_mfma_f32_16x16x32_bf16 v[60:63], v[168:171], v[16:19], v[60:63]\n"
    "ds_read_b128 v[176:179], v205 offset:6144\n"
    "ds_read_b128 v[180:183], v205 offset:7168\n"
    "s_waitcnt lgkmcnt(1)\n"
    "v_mfma_f32_16x16x32_bf16 v[60:63], v[176:179], v[8:11], v[60:63]\n"
    "s_waitcnt lgkmcnt(0)\n"
    "v_mfma_f32_16x16x32_bf16 v[60:63], v[180:183], v[12:15], v[60:63]\n"
    "v_mfma_f32_16x16x32_bf16 v[56:59], v[180:183], a[244:247], v[56:59]\n"
    "v_mfma_f32_16x16x32_bf16 v[56:59], v[176:179], a[240:243], v[56:59]\n"
    "v_mfma_f32_16x16x32_bf16 v[52:55], v[176:179], a[216:219], v[52:55]\n"
    "v_mfma_f32_16x16x32_bf16 v[52:55], v[180:183], a[220:223], v[52:55]\n"
    "v_mfma_f32_16x16x32_bf16 v[48:51], v[180:183], a[196:199], v[48:51]\n"
    "v_mfma_f32_16x16x32_bf16 v[48:51], v[176:179], a[192:195], v[48:51]\n"
    "s_add_u32 s78, s63, 0x3000\n"
    "s_add_u32 m0, s62, 0\n"
    "s_nop 0\n"
    "buffer_load_dwordx4 v200, s[36:39], s78 offen sc1 lds\n"
    "buffer_load_dwordx4 v200, s[36:39], s78 offen offset:1024 sc1 lds\n"
    "buffer_load_dwordx4 v200, s[36:39], s78 offen offset:2048 sc1 lds\n"
    "buffer_load_dwordx4 v200, s[36:39], s78 offen offset:3072 sc1 lds\n"
    "s_add_u32 s78, s63, 0x4000\n"
    "s_add_u32 m0, s62, 0x1000\n"
    "s_nop 0\n"
    "buffer_load_dwordx4 v200, s[36:39], s78 offen sc1 lds\n"
    "buffer_load_dwordx4 v200, s[36:39], s78 offen offset:1024 sc1 lds\n"
    "buffer_load_dwordx4 v200, s[36:39], s78 offen offset:2048 sc1 lds\n"
    "buffer_load_dwordx4 v200, s[36:39], s78 offen offset:3072 sc1 lds\n"
    "s_add_u32 s78, s63, 0x5000\n"
    "s_add_u32 m0, s62, 0x2000\n"
    "s_nop 0\n"
    "buffer_load_dwordx4 v200, s[36:39], s78 offen sc1 lds\n"
    "buffer_load_dwordx4 v200, s[36:39], s78 offen offset:1024 sc1 lds\n"
    "buffer_load_dwordx4 v200, s[36:39], s78 offen offset:2048 sc1 lds\n"
    "buffer_load_dwordx4 v200, s[36:39], s78 offen offset:3072 sc1 lds\n"
    "s_waitcnt vmcnt(0)\n"
    "ds_read_b128 v[160:163], v205\n"
    "ds_read_b128 v[164:167], v205 offset:1024\n"
    "s_waitcnt lgkmcnt(1)\n"
    "v_mfma_f32_16x16x32_bf16 v[64:67], v[160:163], a[192:195], 0\n"
    "s_waitcnt lgkmcnt(0)\n"
    "v_mfma_f32_16x16x32_bf16 v[64:67], v[164:167], a[196:199], v[64:67]\n"
    "v_mfma_f32_16x16x32_bf16 v[68:71], v[164:167], a[220:223], 0\n"
    "v_mfma_f32_16x16x32_bf16 v[68:71], v[160:163], a[216:219], v[68:71]\n"
    "v_mfma_f32_16x16x32_bf16 v[72:75], v[160:163], a[240:243], 0\n"
    "v_mfma_f32_16x16x32_bf16 v[72:75], v[164:167], a[244:247], v[72:75]\n"
    "v_mfma_f32_16x16x32_bf16 v[76:79], v[164:167], v[12:15], 0\n"
    "v_mfma_f32_16x16x32_bf16 v[76:79], v[160:163], v[8:11], v[76:79]\n"
    "ds_read_b128 v[168:171], v205 offset:2048\n"
    "ds_read_b128 v[172:175], v205 offset:3072\n"
    "s_waitcnt lgkmcnt(1)\n"
    "v_mfma_f32_16x16x32_bf16 v[76:79], v[168:171], v[16:19], v[76:79]\n"
    "s_waitcnt lgkmcnt(0)\n"
    "v_mfma_f32_16x16x32_bf16 v[76:79], v[172:175], v[20:23], v[76:79]\n"
    "v_mfma_f32_16x16x32_bf16 v[72:75], v[172:175], a[252:255], v[72:75]\n"
    "v_mfma_f32_16x16x32_bf16 v[72:75], v[168:171], a[248:251], v[72:75]\n"
    "v_mfma_f32_16x16x32_bf16 v[68:71], v[168:171], a[224:227], v[68:71]\n"
    "v_mfma_f32_16x16x32_bf16 v[68:71], v[172:175], a[228:231], v[68:71]\n"
    "v_mfma_f32_16x16x32_bf16 v[64:67], v[172:175], a[204:207], v[64:67]\n"
    "v_mfma_f32_16x16x32_bf16 v[64:67], v[168:171], a[200:203], v[64:67]\n"
    "ds_read_b128 v[176:179], v205 offset:4096\n"
    "ds_read_b128 v[180:183], v205 offset:5120\n"
    "s_waitcnt lgkmcnt(1)\n"
    "v_mfma_f32_16x16x32_bf16 v[64:67], v[176:179], a[208:211], v[64:67]\n"
    "s_waitcnt lgkmcnt(0)\n"
    "v_mfma_f32_16x16x32_bf16 v[64:67], v[180:183], a[212:215], v[64:67]\n"
    "v_mfma_f32_16x16x32_bf16 v[68:71], v[180:183], a[236:239], v[68:71]\n"
    "v_mfma_f32_16x16x32_bf16 v[68:71], v[176:179], a[232:235], v[68:71]\n"
    "v_mfma_f32_16x16x32_bf16 v[72:75], v[176:179], v[0:3], v[72:75]\n"
    "v_mfma_f32_16x16x32_bf16 v[72:75], v[180:183], v[4:7], v[72:75]\n"
    "v_mfma_f32_16x16x32_bf16 v[76:79], v[180:183], v[28:31], v[76:79]\n"
    "v_mfma_f32_16x16x32_bf16 v[76:79], v[176:179], v[24:27], v[76:79]\n"
    "ds_read_b128 v[160:163], v205 offset:10240\n"
    "ds_read_b128 v[164:167], v205 offset:11264\n"
    "s_waitcnt lgkmcnt(1)\n"
    "v_mfma_f32_16x16x32_bf16 v[92:95], v[160:163], v[24:27], 0\n"
    "s_waitcnt lgkmcnt(0)\n"
    "v_mfma_f32_16x16x32_bf16 v[92:95], v[164:167], v[28:31], v[92:95]\n"
    "v_mfma_f32_16x16x32_bf16 v[88:91], v[164:167], v[4:7], 0\n"
    "v_mfma_f32_16x16x32_bf16 v[88:91], v[160:163], v[0:3], v[88:91]\n"
    "v_mfma_f32_16x16x32_bf16 v[84:87], v[160:163], a[232:235], 0\n"
    "v_mfma_f32_16x16x32_bf16 v[84:87], v[164:167], a[236:239], v[84:87]\n"
    "v_mfma_f32_16x16x32_bf16 v[80:83], v[164:167], a[212:215], 0\n"
    "v_mfma_f32_16x16x32_bf16 v[80:83], v[160:163], a[208:211], v[80:83]\n"
    "ds_read_b128 v[168:171], v205 offset:8192\n"
    "ds_read_b128 v[172:175], v205 offset:9216\n"
    "s_waitcnt lgkmcnt(1)\n"
    "v_mfma_f32_16x16x32_bf16 v[80:83], v[168:171], a[200:203], v[80:83]\n"
    "s_waitcnt lgkmcnt(0)\n"
    "v_mfma_f32_16x16x32_bf16 v[80:83], v[172:175], a[204:207], v[80:83]\n"
    "v_mfma_f32_16x16x32_bf16 v[84:87], v[172:175], a[228:231], v[84:87]\n"
    "v_mfma_f32_16x16x32_bf16 v[84:87], v[168:171], a[224:227], v[84:87]\n"
    "v_mfma_f32_16x16x32_bf16 v[88:91], v[168:171], a[248:251], v[88:91]\n"
    "v_mfma_f32_16x16x32_bf16 v[88:91], v[172:175], a[252:255], v[88:91]\n"
    "v_mfma_f32_16x16x32_bf16 v[92:95], v[172:175], v[20:23], v[92:95]\n"
    "v_mfma_f32_16x16x32_bf16 v[92:95], v[168:171], v[16:19], v[92:95]\n"
    "ds_read_b128 v[176:179], v205 offset:6144\n"
    "ds_read_b128 v[180:183], v205 offset:7168\n"
    "s_waitcnt lgkmcnt(1)\n"
    "v_mfma_f32_16x16x32_bf16 v[92:95], v[176:179], v[8:11], v[92:95]\n"
    "s_waitcnt lgkmcnt(0)\n"
    "v_mfma_f32_16x16x32_bf16 v[92:95], v[180:183], v[12:15], v[92:95]\n"
    "v_mfma_f32_16x16x32_bf16 v[88:91], v[180:183], a[244:247], v[88:91]\n"
    "v_mfma_f32_16x16x32_bf16 v[88:91], v[176:179], a[240:243], v[88:91]\n"
    "v_mfma_f32_16x16x32_bf16 v[84:87], v[176:179], a[216:219], v[84:87]\n"
    "v_mfma_f32_16x16x32_bf16 v[84:87], v[180:183], a[220:223], v[84:87]\n"
    "v_mfma_f32_16x16x32_bf16 v[80:83], v[180:183], a[196:199], v[80:83]\n"
    "v_mfma_f32_16x16x32_bf16 v[80:83], v[176:179], a[192:195], v[80:83]\n"
    "v_max3_f32 v196, v32, v33, v34\n"
    "v_max3_f32 v196, v196, v35, v48\n"
    "v_max3_f32 v196, v196, v49, v50\n"
    "v_max3_f32 v196, v196, v51, v64\n"
    "v_max3_f32 v196, v196, v65, v66\n"
    "s_nop 2\n"
    "v_max3_f32 v196, v196, v67, v80\n"
    "v_max3_f32 v196, v196, v81, v82\n"
    "v_max_f32 v196, v196, v83\n"
    "v_mov_b32 v197, v196\n"
    "s_nop 1\n"
    "v_permlane32_swap_b32 v196, v197\n"
    "v_max_f32 v196, v196, v197\n"
    "v_mov_b32 v197, v196\n"
    "s_nop 1\n"
    "v_permlane16_swap_b32 v196, v197\n"
    "v_max_f32 v196, v196, v197\n"
    "v_max_f32 v196, v196, v96\n"
    "v_sub_f32 v197, v96, v196\n"
    "v_mov_b32 v96, v196\n"
    "v_exp_f32 v197, v197\n"
    "v_sub_f32 v32, v32, v196\n"
    "v_sub_f32 v33, v33, v196\n"
    "v_sub_f32 v34, v34, v196\n"
    "v_sub_f32 v35, v35, v196\n"
    "v_sub_f32 v48, v48, v196\n"
    "v_sub_f32 v49, v49, v196\n"
    "v_sub_f32 v50, v50, v196\n"
    "v_sub_f32 v51, v51, v196\n"
    "v_sub_f32 v64, v64, v196\n"
    "v_sub_f32 v65, v65, v196\n"
    "v_sub_f32 v66, v66, v196\n"
    "v_sub_f32 v67, v67, v196\n"
    "v_sub_f32 v80, v80, v196\n"
    "v_sub_f32 v81, v81, v196\n"
    "v_sub_f32 v82, v82, v196\n"
    "v_sub_f32 v83, v83, v196\n"
    "v_mul_f32 v112, v112, v197\n"
    "v_mul_f32 v113, v113, v197\n"
    "v_mul_f32 v114, v114, v197\n"
    "v_mul_f32 v115, v115, v197\n"
    "v_accvgpr_read_b32 v198, a0\n"
    "v_accvgpr_read_b32 v199, a1\n"
    "v_mul_f32 v198, v198, v197\n"
    "v_mul_f32 v199, v199, v197\n"
    "v_accvgpr_write_b32 a0, v198\n"
    "v_accvgpr_write_b32 a1, v199\n"
    "v_accvgpr_read_b32 v198, a2\n"
    "v_accvgpr_read_b32 v199, a3\n"
    "v_mul_f32 v198, v198, v197\n"
    "v_mul_f32 v199, v199, v197\n"
    "v_accvgpr_write_b32 a2, v198\n"
    "v_accvgpr_write_b32 a3, v199\n"
    "v_accvgpr_read_b32 v198, a16\n"
    "v_accvgpr_read_b32 v199, a17\n"
    "v_mul_f32 v198, v198, v197\n"
    "v_mul_f32 v199, v199, v197\n"
    "v_accvgpr_write_b32 a16, v198\n"
    "v_accvgpr_write_b32 a17, v199\n"
    "v_accvgpr_read_b32 v198, a18\n"
    "v_accvgpr_read_b32 v199, a19\n"
    "v_mul_f32 v198, v198, v197\n"
    "v_mul_f32 v199, v199, v197\n"
    "v_accvgpr_write_b32 a18, v198\n"
    "v_accvgpr_write_b32 a19, v199\n"
    "v_accvgpr_read_b32 v198, a32\n"
    "v_accvgpr_read_b32 v199, a33\n"
    "v_mul_f32 v198, v198, v197\n"
    "v_mul_f32 v199, v199, v197\n"
    "v_accvgpr_write_b32 a32, v198\n"
    "v_accvgpr_write_b32 a33, v199\n"
    "v_accvgpr_read_b32 v198, a34\n"
    "v_accvgpr_read_b32 v199, a35\n"
    "v_mul_f32 v198, v198, v197\n"
    "v_mul_f32 v199, v199, v197\n"
    "v_accvgpr_write_b32 a34, v198\n"
    "v_accvgpr_write_b32 a35, v199\n"
    "v_accvgpr_read_b32 v198, a48\n"
    "v_accvgpr_read_b32 v199, a49\n"
    "v_mul_f32 v198, v198, v197\n"
    "v_mul_f32 v199, v199, v197\n"
    "v_accvgpr_write_b32 a48, v198\n"
    "v_accvgpr_write_b32 a49, v199\n"
    "v_accvgpr_read_b32 v198, a50\n"
    "v_accvgpr_read_b32 v199, a51\n"
    "v_mul_f32 v198, v198, v197\n"
    "v_mul_f32 v199, v199, v197\n"
    "v_accvgpr_write_b32 a50, v198\n"
    "v_accvgpr_write_b32 a51, v199\n"
    "v_accvgpr_read_b32 v198, a64\n"
    "v_accvgpr_read_b32 v199, a65\n"
    "v_mul_f32 v198, v198, v197\n"
    "v_mul_f32 v199, v199, v197\n"
    "v_accvgpr_write_b32 a64, v198\n"
    "v_accvgpr_write_b32 a65, v199\n"
    "v_accvgpr_read_b32 v198, a66\n"
    "v_accvgpr_read_b32 v199, a67\n"
    "v_mul_f32 v198, v198, v197\n"
    "v_mul_f32 v199, v199, v197\n"
    "v_accvgpr_write_b32 a66, v198\n"
    "v_accvgpr_write_b32 a67, v199\n"
    "v_accvgpr_read_b32 v198, a80\n"
    "v_accvgpr_read_b32 v199, a81\n"
    "v_mul_f32 v198, v198, v197\n"
    "v_mul_f32 v199, v199, v197\n"
    "v_accvgpr_write_b32 a80, v198\n"
    "v_accvgpr_write_b32 a81, v199\n"
    "v_accvgpr_read_b32 v198, a82\n"
    "v_accvgpr_read_b32 v199, a83\n"
    "v_mul_f32 v198, v198, v197\n"
    "v_mul_f32 v199, v199, v197\n"
    "v_accvgpr_write_b32 a82, v198\n"
    "v_accvgpr_write_b32 a83, v199\n"
    "v_accvgpr_read_b32 v198, a96\n"
    "v_accvgpr_read_b32 v199, a97\n"
    "v_mul_f32 v198, v198, v197\n"
    "v_mul_f32 v199, v199, v197\n"
    "v_accvgpr_write_b32 a96, v198\n"
    "v_accvgpr_write_b32 a97, v199\n"
    "v_accvgpr_read_b32 v198, a98\n"
    "v_accvgpr_read_b32 v199, a99\n"
    "v_mul_f32 v198, v198, v197\n"
    "v_mul_f32 v199, v199, v197\n"
    "v_accvgpr_write_b32 a98, v198\n"
    "v_accvgpr_write_b32 a99, v199\n"
    "v_accvgpr_read_b32 v198, a112\n"
    "v_accvgpr_read_b32 v199, a113\n"
    "v_mul_f32 v198, v198, v197\n"
    "v_mul_f32 v199, v199, v197\n"
    "v_accvgpr_write_b32 a112, v198\n"
    "v_accvgpr_write_b32 a113, v199\n"
    "v_accvgpr_read_b32 v198, a114\n"
    "v_accvgpr_read_b32 v199, a115\n"
    "v_mul_f32 v198, v198, v197\n"
    "v_mul_f32 v199, v199, v197\n"
    "v_accvgpr_write_b32 a114, v198\n"
    "v_accvgpr_write_b32 a115, v199\n"
    "v_accvgpr_read_b32 v198, a128\n"
    "v_accvgpr_read_b32 v199, a129\n"
    "v_mul_f32 v198, v198, v197\n"
    "v_mul_f32 v199, v199, v197\n"
    "v_accvgpr_write_b32 a128, v198\n"
    "v_accvgpr_write_b32 a129, v199\n"
    "v_accvgpr_read_b32 v198, a130\n"
    "v_accvgpr_read_b32 v199, a131\n"
    "v_mul_f32 v198, v198, v197\n"
    "v_mul_f32 v199, v199, v197\n"
    "v_accvgpr_write_b32 a130, v198\n"
    "v_accvgpr_write_b32 a131, v199\n"
    "v_accvgpr_read_b32 v198, a144\n"
    "v_accvgpr_read_b32 v199, a145\n"
    "v_mul_f32 v198, v198, v197\n"
    "v_mul_f32 v199, v199, v197\n"
    "v_accvgpr_write_b32 a144, v198\n"
    "v_accvgpr_write_b32 a145, v199\n"
    "v_accvgpr_read_b32 v198, a146\n"
    "v_accvgpr_read_b32 v199, a147\n"
    "v_mul_f32 v198, v198, v197\n"
    "v_mul_f32 v199, v199, v197\n"
    "v_accvgpr_write_b32 a146, v198\n"
    "v_accvgpr_write_b32 a147, v199\n"
    "v_accvgpr_read_b32 v198, a160\n"
    "v_accvgpr_read_b32 v199, a161\n"
    "v_mul_f32 v198, v198, v197\n"
    "v_mul_f32 v199, v199, v197\n"
    "v_accvgpr_write_b32 a160, v198\n"
    "v_accvgpr_write_b32 a161, v199\n"
    "v_accvgpr_read_b32 v198, a162\n"
    "v_accvgpr_read_b32 v199, a163\n"
    "v_mul_f32 v198, v198, v197\n"
    "v_mul_f32 v199, v199, v197\n"
    "v_accvgpr_write_b32 a162, v198\n"
    "v_accvgpr_write_b32 a163, v199\n"
    "v_accvgpr_read_b32 v198, a176\n"
    "v_accvgpr_read_b32 v199, a177\n"
    "v_mul_f32 v198, v198, v197\n"
    "v_mul_f32 v199, v199, v197\n"
    "v_accvgpr_write_b32 a176, v198\n"
    "v_accvgpr_write_b32 a177, v199\n"
    "v_accvgpr_read_b32 v198, a178\n"
    "v_accvgpr_read_b32 v199, a179\n"
    "v_mul_f32 v198, v198, v197\n"
    "v_mul_f32 v199, v199, v197\n"
    "v_accvgpr_write_b32 a178, v198\n"
    "v_accvgpr_write_b32 a179, v199\n"
    "v_max3_f32 v196, v36, v37, v38\n"
    "v_max3_f32 v196, v196, v39, v52\n"
    "v_max3_f32 v196, v196, v53, v54\n"
    "v_max3_f32 v196, v196, v55, v68\n"
    "v_max3_f32 v196, v196, v69, v70\n"
    "v_max3_f32 v196, v196, v71, v84\n"
    "v_max3_f32 v196, v196, v85, v86\n"
    "v_max_f32 v196, v196, v87\n"
    "v_mov_b32 v197, v196\n"
    "s_nop 1\n"
    "v_permlane32_swap_b32 v196, v197\n"
    "v_max_f32 v196, v196, v197\n"
    "v_mov_b32 v197, v196\n"
    "s_nop 1\n"
    "v_permlane16_swap_b32 v196, v197\n"
    "v_max_f32 v196, v196, v197\n"
    "v_max_f32 v196, v196, v100\n"
    "v_sub_f32 v197, v100, v196\n"
    "v_mov_b32 v100, v196\n"
    "v_exp_f32 v197, v197\n"
    "v_sub_f32 v36, v36, v196\n"
    "v_sub_f32 v37, v37, v196\n"
    "v_sub_f32 v38, v38, v196\n"
    "v_sub_f32 v39, v39, v196\n"
    "v_sub_f32 v52, v52, v196\n"
    "v_sub_f32 v53, v53, v196\n"
    "v_sub_f32 v54, v54, v196\n"
    "v_sub_f32 v55, v55, v196\n"
    "v_sub_f32 v68, v68, v196\n"
    "v_sub_f32 v69, v69, v196\n"
    "v_sub_f32 v70, v70, v196\n"
    "v_sub_f32 v71, v71, v196\n"
    "v_sub_f32 v84, v84, v196\n"
    "v_sub_f32 v85, v85, v196\n"
    "v_sub_f32 v86, v86, v196\n"
    "v_sub_f32 v87, v87, v196\n"
    "v_mul_f32 v116, v116, v197\n"
    "v_mul_f32 v117, v117, v197\n"
    "v_mul_f32 v118, v118, v197\n"
    "v_mul_f32 v119, v119, v197\n"
    "v_accvgpr_read_b32 v198, a4\n"
    "v_accvgpr_read_b32 v199, a5\n"
    "v_mul_f32 v198, v198, v197\n"
    "v_mul_f32 v199, v199, v197\n"
    "v_accvgpr_write_b32 a4, v198\n"
    "v_accvgpr_write_b32 a5, v199\n"
    "v_accvgpr_read_b32 v198, a6\n"
    "v_accvgpr_read_b32 v199, a7\n"
    "v_mul_f32 v198, v198, v197\n"
    "v_mul_f32 v199, v199, v197\n"
    "v_accvgpr_write_b32 a6, v198\n"
    "v_accvgpr_write_b32 a7, v199\n"
    "v_accvgpr_read_b32 v198, a20\n"
    "v_accvgpr_read_b32 v199, a21\n"
    "v_mul_f32 v198, v198, v197\n"
    "v_mul_f32 v199, v199, v197\n"
    "v_accvgpr_write_b32 a20, v198\n"
    "v_accvgpr_write_b32 a21, v199\n"
    "v_accvgpr_read_b32 v198, a22\n"
    "v_accvgpr_read_b32 v199, a23\n"
    "v_mul_f32 v198, v198, v197\n"
    "v_mul_f32 v199, v199, v197\n"
    "v_accvgpr_write_b32 a22, v198\n"
    "v_accvgpr_write_b32 a23, v199\n"
    "v_accvgpr_read_b32 v198, a36\n"
    "v_accvgpr_read_b32 v199, a37\n"
    "v_mul_f32 v198, v198, v197\n"
    "v_mul_f32 v199, v199, v197\n"
    "v_accvgpr_write_b32 a36, v198\n"
    "v_accvgpr_write_b32 a37, v199\n"
    "v_accvgpr_read_b32 v198, a38\n"
    "v_accvgpr_read_b32 v199, a39\n"
    "v_mul_f32 v198, v198, v197\n"
    "v_mul_f32 v199, v199, v197\n"
    "v_accvgpr_write_b32 a38, v198\n"
    "v_accvgpr_write_b32 a39, v199\n"
    "v_accvgpr_read_b32 v198, a52\n"
    "v_accvgpr_read_b32 v199, a53\n"
    "v_mul_f32 v198, v198, v197\n"
    "v_mul_f32 v199, v199, v197\n"
    "v_accvgpr_write_b32 a52, v198\n"
    "v_accvgpr_write_b32 a53, v199\n"
    "v_accvgpr_read_b32 v198, a54\n"
    "v_accvgpr_read_b32 v199, a55\n"
    "v_mul_f32 v198, v198, v197\n"
    "v_mul_f32 v199, v199, v197\n"
    "v_accvgpr_write_b32 a54, v198\n"
    "v_accvgpr_write_b32 a55, v199\n"
    "v_accvgpr_read_b32 v198, a68\n"
    "v_accvgpr_read_b32 v199, a69\n"
    "v_mul_f32 v198, v198, v197\n"
    "v_mul_f32 v199, v199, v197\n"
    "v_accvgpr_write_b32 a68, v198\n"
    "v_accvgpr_write_b32 a69, v199\n"
    "v_accvgpr_read_b32 v198, a70\n"
    "v_accvgpr_read_b32 v199, a71\n"
    "v_mul_f32 v198, v198, v197\n"
    "v_mul_f32 v199, v199, v197\n"
    "v_accvgpr_write_b32 a70, v198\n"
    "v_accvgpr_write_b32 a71, v199\n"
    "v_accvgpr_read_b32 v198, a84\n"
    "v_accvgpr_read_b32 v199, a85\n"
    "v_mul_f32 v198, v198, v197\n"
    "v_mul_f32 v199, v199, v197\n"
    "v_accvgpr_write_b32 a84, v198\n"
    "v_accvgpr_write_b32 a85, v199\n"
    "v_accvgpr_read_b32 v198, a86\n"
    "v_accvgpr_read_b32 v199, a87\n"
    "v_mul_f32 v198, v198, v197\n"
    "v_mul_f32 v199, v199, v197\n"
    "v_accvgpr_write_b32 a86, v198\n"
    "v_accvgpr_write_b32 a87, v199\n"
    "v_accvgpr_read_b32 v198, a100\n"
    "v_accvgpr_read_b32 v199, a101\n"
    "v_mul_f32 v198, v198, v197\n"
    "v_mul_f32 v199, v199, v197\n"
    "v_accvgpr_write_b32 a100, v198\n"
    "v_accvgpr_write_b32 a101, v199\n"
    "v_accvgpr_read_b32 v198, a102\n"
    "v_accvgpr_read_b32 v199, a103\n"
    "v_mul_f32 v198, v198, v197\n"
    "v_mul_f32 v199, v199, v197\n"
    "v_accvgpr_write_b32 a102, v198\n"
    "v_accvgpr_write_b32 a103, v199\n"
    "v_accvgpr_read_b32 v198, a116\n"
    "v_accvgpr_read_b32 v199, a117\n"
    "v_mul_f32 v198, v198, v197\n"
    "v_mul_f32 v199, v199, v197\n"
    "v_accvgpr_write_b32 a116, v198\n"
    "v_accvgpr_write_b32 a117, v199\n"
    "v_accvgpr_read_b32 v198, a118\n"
    "v_accvgpr_read_b32 v199, a119\n"
    "v_mul_f32 v198, v198, v197\n"
    "v_mul_f32 v199, v199, v197\n"
    "v_accvgpr_write_b32 a118, v198\n"
    "v_accvgpr_write_b32 a119, v199\n"
    "v_accvgpr_read_b32 v198, a132\n"
    "v_accvgpr_read_b32 v199, a133\n"
    "v_mul_f32 v198, v198, v197\n"
    "v_mul_f32 v199, v199, v197\n"
    "v_accvgpr_write_b32 a132, v198\n"
    "v_accvgpr_write_b32 a133, v199\n"
    "v_accvgpr_read_b32 v198, a134\n"
    "v_accvgpr_read_b32 v199, a135\n"
    "v_mul_f32 v198, v198, v197\n"
    "v_mul_f32 v199, v199, v197\n"
    "v_accvgpr_write_b32 a134, v198\n"
    "v_accvgpr_write_b32 a135, v199\n"
    "v_accvgpr_read_b32 v198, a148\n"
    "v_accvgpr_read_b32 v199, a149\n"
    "v_mul_f32 v198, v198, v197\n"
    "v_mul_f32 v199, v199, v197\n"
    "v_accvgpr_write_b32 a148, v198\n"
    "v_accvgpr_write_b32 a149, v199\n"
    "v_accvgpr_read_b32 v198, a150\n"
    "v_accvgpr_read_b32 v199, a151\n"
    "v_mul_f32 v198, v198, v197\n"
    "v_mul_f32 v199, v199, v197\n"
    "v_accvgpr_write_b32 a150, v198\n"
    "v_accvgpr_write_b32 a151, v199\n"
    "v_accvgpr_read_b32 v198, a164\n"
    "v_accvgpr_read_b32 v199, a165\n"
    "v_mul_f32 v198, v198, v197\n"
    "v_mul_f32 v199, v199, v197\n"
    "v_accvgpr_write_b32 a164, v198\n"
    "v_accvgpr_write_b32 a165, v199\n"
    "v_accvgpr_read_b32 v198, a166\n"
    "v_accvgpr_read_b32 v199, a167\n"
    "v_mul_f32 v198, v198, v197\n"
    "v_mul_f32 v199, v199, v197\n"
    "v_accvgpr_write_b32 a166, v198\n"
    "v_accvgpr_write_b32 a167, v199\n"
    "v_accvgpr_read_b32 v198, a180\n"
    "v_accvgpr_read_b32 v199, a181\n"
    "v_mul_f32 v198, v198, v197\n"
    "v_mul_f32 v199, v199, v197\n"
    "v_accvgpr_write_b32 a180, v198\n"
    "v_accvgpr_write_b32 a181, v199\n"
    "v_accvgpr_read_b32 v198, a182\n"
    "v_accvgpr_read_b32 v199, a183\n"
    "v_mul_f32 v198, v198, v197\n"
    "v_mul_f32 v199, v199, v197\n"
    "v_accvgpr_write_b32 a182, v198\n"
    "v_accvgpr_write_b32 a183, v199\n"
    "v_max3_f32 v196, v40, v41, v42\n"
    "v_max3_f32 v196, v196, v43, v56\n"
    "v_max3_f32 v196, v196, v57, v58\n"
    "v_max3_f32 v196, v196, v59, v72\n"
    "v_max3_f32 v196, v196, v73, v74\n"
    "v_max3_f32 v196, v196, v75, v88\n"
    "v_max3_f32 v196, v196, v89, v90\n"
    "v_max_f32 v196, v196, v91\n"
    "v_mov_b32 v197, v196\n"
    "s_nop 1\n"
    "v_permlane32_swap_b32 v196, v197\n"
    "v_max_f32 v196, v196, v197\n"
    "v_mov_b32 v197, v196\n"
    "s_nop 1\n"
    "v_permlane16_swap_b32 v196, v197\n"
    "v_max_f32 v196, v196, v197\n"
    "v_max_f32 v196, v196, v104\n"
    "v_sub_f32 v197, v104, v196\n"
    "v_mov_b32 v104, v196\n"
    "v_exp_f32 v197, v197\n"
    "v_sub_f32 v40, v40, v196\n"
    "v_sub_f32 v41, v41, v196\n"
    "v_sub_f32 v42, v42, v196\n"
    "v_sub_f32 v43, v43, v196\n"
    "v_sub_f32 v56, v56, v196\n"
    "v_sub_f32 v57, v57, v196\n"
    "v_sub_f32 v58, v58, v196\n"
    "v_sub_f32 v59, v59, v196\n"
    "v_sub_f32 v72, v72, v196\n"
    "v_sub_f32 v73, v73, v196\n"
    "v_sub_f32 v74, v74, v196\n"
    "v_sub_f32 v75, v75, v196\n"
    "v_sub_f32 v88, v88, v196\n"
    "v_sub_f32 v89, v89, v196\n"
    "v_sub_f32 v90, v90, v196\n"
    "v_sub_f32 v91, v91, v196\n"
    "v_mul_f32 v120, v120, v197\n"
    "v_mul_f32 v121, v121, v197\n"
    "v_mul_f32 v122, v122, v197\n"
    "v_mul_f32 v123, v123, v197\n"
    "v_accvgpr_read_b32 v198, a8\n"
    "v_accvgpr_read_b32 v199, a9\n"
    "v_mul_f32 v198, v198, v197\n"
    "v_mul_f32 v199, v199, v197\n"
    "v_accvgpr_write_b32 a8, v198\n"
    "v_accvgpr_write_b32 a9, v199\n"
    "v_accvgpr_read_b32 v198, a10\n"
    "v_accvgpr_read_b32 v199, a11\n"
    "v_mul_f32 v198, v198, v197\n"
    "v_mul_f32 v199, v199, v197\n"
    "v_accvgpr_write_b32 a10, v198\n"
    "v_accvgpr_write_b32 a11, v199\n"
    "v_accvgpr_read_b32 v198, a24\n"
    "v_accvgpr_read_b32 v199, a25\n"
    "v_mul_f32 v198, v198, v197\n"
    "v_mul_f32 v199, v199, v197\n"
    "v_accvgpr_write_b32 a24, v198\n"
    "v_accvgpr_write_b32 a25, v199\n"
    "v_accvgpr_read_b32 v198, a26\n"
    "v_accvgpr_read_b32 v199, a27\n"
    "v_mul_f32 v198, v198, v197\n"
    "v_mul_f32 v199, v199, v197\n"
    "v_accvgpr_write_b32 a26, v198\n"
    "v_accvgpr_write_b32 a27, v199\n"
    "v_accvgpr_read_b32 v198, a40\n"
    "v_accvgpr_read_b32 v199, a41\n"
    "v_mul_f32 v198, v198, v197\n"
    "v_mul_f32 v199, v199, v197\n"
    "v_accvgpr_write_b32 a40, v198\n"
    "v_accvgpr_write_b32 a41, v199\n"
    "v_accvgpr_read_b32 v198, a42\n"
    "v_accvgpr_read_b32 v199, a43\n"
    "v_mul_f32 v198, v198, v197\n"
    "v_mul_f32 v199, v199, v197\n"
    "v_accvgpr_write_b32 a42, v198\n"
    "v_accvgpr_write_b32 a43, v199\n"
    "v_accvgpr_read_b32 v198, a56\n"
    "v_accvgpr_read_b32 v199, a57\n"
    "v_mul_f32 v198, v198, v197\n"
    "v_mul_f32 v199, v199, v197\n"
    "v_accvgpr_write_b32 a56, v198\n"
    "v_accvgpr_write_b32 a57, v199\n"
    "v_accvgpr_read_b32 v198, a58\n"
    "v_accvgpr_read_b32 v199, a59\n"
    "v_mul_f32 v198, v198, v197\n"
    "v_mul_f32 v199, v199, v197\n"
    "v_accvgpr_write_b32 a58, v198\n"
    "v_accvgpr_write_b32 a59, v199\n"
    "v_accvgpr_read_b32 v198, a72\n"
    "v_accvgpr_read_b32 v199, a73\n"
    "v_mul_f32 v198, v198, v197\n"
    "v_mul_f32 v199, v199, v197\n"
    "v_accvgpr_write_b32 a72, v198\n"
    "v_accvgpr_write_b32 a73, v199\n"
    "v_accvgpr_read_b32 v198, a74\n"
    "v_accvgpr_read_b32 v199, a75\n"
    "v_mul_f32 v198, v198, v197\n"
    "v_mul_f32 v199, v199, v197\n"
    "v_accvgpr_write_b32 a74, v198\n"
    "v_accvgpr_write_b32 a75, v199\n"
    "v_accvgpr_read_b32 v198, a88\n"
    "v_accvgpr_read_b32 v199, a89\n"
    "v_mul_f32 v198, v198, v197\n"
    "v_mul_f32 v199, v199, v197\n"
    "v_accvgpr_write_b32 a88, v198\n"
    "v_accvgpr_write_b32 a89, v199\n"
    "v_accvgpr_read_b32 v198, a90\n"
    "v_accvgpr_read_b32 v199, a91\n"
    "v_mul_f32 v198, v198, v197\n"
    "v_mul_f32 v199, v199, v197\n"
    "v_accvgpr_write_b32 a90, v198\n"
    "v_accvgpr_write_b32 a91, v199\n"
    "v_accvgpr_read_b32 v198, a104\n"
    "v_accvgpr_read_b32 v199, a105\n"
    "v_mul_f32 v198, v198, v197\n"
    "v_mul_f32 v199, v199, v197\n"
    "v_accvgpr_write_b32 a104, v198\n"
    "v_accvgpr_write_b32 a105, v199\n"
    "v_accvgpr_read_b32 v198, a106\n"
    "v_accvgpr_read_b32 v199, a107\n"
    "v_mul_f32 v198, v198, v197\n"
    "v_mul_f32 v199, v199, v197\n"
    "v_accvgpr_write_b32 a106, v198\n"
    "v_accvgpr_write_b32 a107, v199\n"
    "v_accvgpr_read_b32 v198, a120\n"
    "v_accvgpr_read_b32 v199, a121\n"
    "v_mul_f32 v198, v198, v197\n"
    "v_mul_f32 v199, v199, v197\n"
    "v_accvgpr_write_b32 a120, v198\n"
    "v_accvgpr_write_b32 a121, v199\n"
    "v_accvgpr_read_b32 v198, a122\n"
    "v_accvgpr_read_b32 v199, a123\n"
    "v_mul_f32 v198, v198, v197\n"
    "v_mul_f32 v199, v199, v197\n"
    "v_accvgpr_write_b32 a122, v198\n"
    "v_accvgpr_write_b32 a123, v199\n"
    "v_accvgpr_read_b32 v198, a136\n"
    "v_accvgpr_read_b32 v199, a137\n"
    "v_mul_f32 v198, v198, v197\n"
    "v_mul_f32 v199, v199, v197\n"
    "v_accvgpr_write_b32 a136, v198\n"
    "v_accvgpr_write_b32 a137, v199\n"
    "v_accvgpr_read_b32 v198, a138\n"
    "v_accvgpr_read_b32 v199, a139\n"
    "v_mul_f32 v198, v198, v197\n"
    "v_mul_f32 v199, v199, v197\n"
    "v_accvgpr_write_b32 a138, v198\n"
    "v_accvgpr_write_b32 a139, v199\n"
    "v_accvgpr_read_b32 v198, a152\n"
    "v_accvgpr_read_b32 v199, a153\n"
    "v_mul_f32 v198, v198, v197\n"
    "v_mul_f32 v199, v199, v197\n"
    "v_accvgpr_write_b32 a152, v198\n"
    "v_accvgpr_write_b32 a153, v199\n"
    "v_accvgpr_read_b32 v198, a154\n"
    "v_accvgpr_read_b32 v199, a155\n"
    "v_mul_f32 v198, v198, v197\n"
    "v_mul_f32 v199, v199, v197\n"
    "v_accvgpr_write_b32 a154, v198\n"
    "v_accvgpr_write_b32 a155, v199\n"
    "v_accvgpr_read_b32 v198, a168\n"
    "v_accvgpr_read_b32 v199, a169\n"
    "v_mul_f32 v198, v198, v197\n"
    "v_mul_f32 v199, v199, v197\n"
    "v_accvgpr_write_b32 a168, v198\n"
    "v_accvgpr_write_b32 a169, v199\n"
    "v_accvgpr_read_b32 v198, a170\n"
    "v_accvgpr_read_b32 v199, a171\n"
    "v_mul_f32 v198, v198, v197\n"
    "v_mul_f32 v199, v199, v197\n"
    "v_accvgpr_write_b32 a170, v198\n"
    "v_accvgpr_write_b32 a171, v199\n"
    "v_accvgpr_read_b32 v198, a184\n"
    "v_accvgpr_read_b32 v199, a185\n"
    "v_mul_f32 v198, v198, v197\n"
    "v_mul_f32 v199, v199, v197\n"
    "v_accvgpr_write_b32 a184, v198\n"
    "v_accvgpr_write_b32 a185, v199\n"
    "v_accvgpr_read_b32 v198, a186\n"
    "v_accvgpr_read_b32 v199, a187\n"
    "v_mul_f32 v198, v198, v197\n"
    "v_mul_f32 v199, v199, v197\n"
    "v_accvgpr_write_b32 a186, v198\n"
    "v_accvgpr_write_b32 a187, v199\n"
    "v_max3_f32 v196, v44, v45, v46\n"
    "v_max3_f32 v196, v196, v47, v60\n"
    "v_max3_f32 v196, v196, v61, v62\n"
    "v_max3_f32 v196, v196, v63, v76\n"
    "v_max3_f32 v196, v196, v77, v78\n"
    "v_max3_f32 v196, v196, v79, v92\n"
    "v_max3_f32 v196, v196, v93, v94\n"
    "v_max_f32 v196, v196, v95\n"
    "v_mov_b32 v197, v196\n"
    "s_nop 1\n"
    "v_permlane32_swap_b32 v196, v197\n"
    "v_max_f32 v196, v196, v197\n"
    "v_mov_b32 v197, v196\n"
    "s_nop 1\n"
    "v_permlane16_swap_b32 v196, v197\n"
    "v_max_f32 v196, v196, v197\n"
    "v_max_f32 v196, v196, v108\n"
    "v_sub_f32 v197, v108, v196\n"
    "v_mov_b32 v108, v196\n"
    "v_exp_f32 v197, v197\n"
    "v_sub_f32 v44, v44, v196\n"
    "v_sub_f32 v45, v45, v196\n"
    "v_sub_f32 v46, v46, v196\n"
    "v_sub_f32 v47, v47, v196\n"
    "v_sub_f32 v60, v60, v196\n"
    "v_sub_f32 v61, v61, v196\n"
    "v_sub_f32 v62, v62, v196\n"
    "v_sub_f32 v63, v63, v196\n"
    "v_sub_f32 v76, v76, v196\n"
    "v_sub_f32 v77, v77, v196\n"
    "v_sub_f32 v78, v78, v196\n"
    "v_sub_f32 v79, v79, v196\n"
    "v_sub_f32 v92, v92, v196\n"
    "v_sub_f32 v93, v93, v196\n"
    "v_sub_f32 v94, v94, v196\n"
    "v_sub_f32 v95, v95, v196\n"
    "v_mul_f32 v124, v124, v197\n"
    "v_mul_f32 v125, v125, v197\n"
    "v_mul_f32 v126, v126, v197\n"
    "v_mul_f32 v127, v127, v197\n"
    "v_accvgpr_read_b32 v198, a12\n"
    "v_accvgpr_read_b32 v199, a13\n"
    "v_mul_f32 v198, v198, v197\n"
    "v_mul_f32 v199, v199, v197\n"
    "v_accvgpr_write_b32 a12, v198\n"
    "v_accvgpr_write_b32 a13, v199\n"
    "v_accvgpr_read_b32 v198, a14\n"
    "v_accvgpr_read_b32 v199, a15\n"
    "v_mul_f32 v198, v198, v197\n"
    "v_mul_f32 v199, v199, v197\n"
    "v_accvgpr_write_b32 a14, v198\n"
    "v_accvgpr_write_b32 a15, v199\n"
    "v_accvgpr_read_b32 v198, a28\n"
    "v_accvgpr_read_b32 v199, a29\n"
    "v_mul_f32 v198, v198, v197\n"
    "v_mul_f32 v199, v199, v197\n"
    "v_accvgpr_write_b32 a28, v198\n"
    "v_accvgpr_write_b32 a29, v199\n"
    "v_accvgpr_read_b32 v198, a30\n"
    "v_accvgpr_read_b32 v199, a31\n"
    "v_mul_f32 v198, v198, v197\n"
    "v_mul_f32 v199, v199, v197\n"
    "v_accvgpr_write_b32 a30, v198\n"
    "v_accvgpr_write_b32 a31, v199\n"
    "v_accvgpr_read_b32 v198, a44\n"
    "v_accvgpr_read_b32 v199, a45\n"
    "v_mul_f32 v198, v198, v197\n"
    "v_mul_f32 v199, v199, v197\n"
    "v_accvgpr_write_b32 a44, v198\n"
    "v_accvgpr_write_b32 a45, v199\n"
    "v_accvgpr_read_b32 v198, a46\n"
    "v_accvgpr_read_b32 v199, a47\n"
    "v_mul_f32 v198, v198, v197\n"
    "v_mul_f32 v199, v199, v197\n"
    "v_accvgpr_write_b32 a46, v198\n"
    "v_accvgpr_write_b32 a47, v199\n"
    "v_accvgpr_read_b32 v198, a60\n"
    "v_accvgpr_read_b32 v199, a61\n"
    "v_mul_f32 v198, v198, v197\n"
    "v_mul_f32 v199, v199, v197\n"
    "v_accvgpr_write_b32 a60, v198\n"
    "v_accvgpr_write_b32 a61, v199\n"
    "v_accvgpr_read_b32 v198, a62\n"
    "v_accvgpr_read_b32 v199, a63\n"
    "v_mul_f32 v198, v198, v197\n"
    "v_mul_f32 v199, v199, v197\n"
    "v_accvgpr_write_b32 a62, v198\n"
    "v_accvgpr_write_b32 a63, v199\n"
    "v_accvgpr_read_b32 v198, a76\n"
    "v_accvgpr_read_b32 v199, a77\n"
    "v_mul_f32 v198, v198, v197\n"
    "v_mul_f32 v199, v199, v197\n"
    "v_accvgpr_write_b32 a76, v198\n"
    "v_accvgpr_write_b32 a77, v199\n"
    "v_accvgpr_read_b32 v198, a78\n"
    "v_accvgpr_read_b32 v199, a79\n"
    "v_mul_f32 v198, v198, v197\n"
    "v_mul_f32 v199, v199, v197\n"
    "v_accvgpr_write_b32 a78, v198\n"
    "v_accvgpr_write_b32 a79, v199\n"
    "v_accvgpr_read_b32 v198, a92\n"
    "v_accvgpr_read_b32 v199, a93\n"
    "v_mul_f32 v198, v198, v197\n"
    "v_mul_f32 v199, v199, v197\n"
    "v_accvgpr_write_b32 a92, v198\n"
    "v_accvgpr_write_b32 a93, v199\n"
    "v_accvgpr_read_b32 v198, a94\n"
    "v_accvgpr_read_b32 v199, a95\n"
    "v_mul_f32 v198, v198, v197\n"
    "v_mul_f32 v199, v199, v197\n"
    "v_accvgpr_write_b32 a94, v198\n"
    "v_accvgpr_write_b32 a95, v199\n"
    "v_accvgpr_read_b32 v198, a108\n"
    "v_accvgpr_read_b32 v199, a109\n"
    "v_mul_f32 v198, v198, v197\n"
    "v_mul_f32 v199, v199, v197\n"
    "v_accvgpr_write_b32 a108, v198\n"
    "v_accvgpr_write_b32 a109, v199\n"
    "v_accvgpr_read_b32 v198, a110\n"
    "v_accvgpr_read_b32 v199, a111\n"
    "v_mul_f32 v198, v198, v197\n"
    "v_mul_f32 v199, v199, v197\n"
    "v_accvgpr_write_b32 a110, v198\n"
    "v_accvgpr_write_b32 a111, v199\n"
    "v_accvgpr_read_b32 v198, a124\n"
    "v_accvgpr_read_b32 v199, a125\n"
    "v_mul_f32 v198, v198, v197\n"
    "v_mul_f32 v199, v199, v197\n"
    "v_accvgpr_write_b32 a124, v198\n"
    "v_accvgpr_write_b32 a125, v199\n"
    "v_accvgpr_read_b32 v198, a126\n"
    "v_accvgpr_read_b32 v199, a127\n"
    "v_mul_f32 v198, v198, v197\n"
    "v_mul_f32 v199, v199, v197\n"
    "v_accvgpr_write_b32 a126, v198\n"
    "v_accvgpr_write_b32 a127, v199\n"
    "v_accvgpr_read_b32 v198, a140\n"
    "v_accvgpr_read_b32 v199, a141\n"
    "v_mul_f32 v198, v198, v197\n"
    "v_mul_f32 v199, v199, v197\n"
    "v_accvgpr_write_b32 a140, v198\n"
    "v_accvgpr_write_b32 a141, v199\n"
    "v_accvgpr_read_b32 v198, a142\n"
    "v_accvgpr_read_b32 v199, a143\n"
    "v_mul_f32 v198, v198, v197\n"
    "v_mul_f32 v199, v199, v197\n"
    "v_accvgpr_write_b32 a142, v198\n"
    "v_accvgpr_write_b32 a143, v199\n"
    "v_accvgpr_read_b32 v198, a156\n"
    "v_accvgpr_read_b32 v199, a157\n"
    "v_mul_f32 v198, v198, v197\n"
    "v_mul_f32 v199, v199, v197\n"
    "v_accvgpr_write_b32 a156, v198\n"
    "v_accvgpr_write_b32 a157, v199\n"
    "v_accvgpr_read_b32 v198, a158\n"
    "v_accvgpr_read_b32 v199, a159\n"
    "v_mul_f32 v198, v198, v197\n"
    "v_mul_f32 v199, v199, v197\n"
    "v_accvgpr_write_b32 a158, v198\n"
    "v_accvgpr_write_b32 a159, v199\n"
    "v_accvgpr_read_b32 v198, a172\n"
    "v_accvgpr_read_b32 v199, a173\n"
    "v_mul_f32 v198, v198, v197\n"
    "v_mul_f32 v199, v199, v197\n"
    "v_accvgpr_write_b32 a172, v198\n"
    "v_accvgpr_write_b32 a173, v199\n"
    "v_accvgpr_read_b32 v198, a174\n"
    "v_accvgpr_read_b32 v199, a175\n"
    "v_mul_f32 v198, v198, v197\n"
    "v_mul_f32 v199, v199, v197\n"
    "v_accvgpr_write_b32 a174, v198\n"
    "v_accvgpr_write_b32 a175, v199\n"
    "v_accvgpr_read_b32 v198, a188\n"
    "v_accvgpr_read_b32 v199, a189\n"
    "v_mul_f32 v198, v198, v197\n"
    "v_mul_f32 v199, v199, v197\n"
    "v_accvgpr_write_b32 a188, v198\n"
    "v_accvgpr_write_b32 a189, v199\n"
    "v_accvgpr_read_b32 v198, a190\n"
    "v_accvgpr_read_b32 v199, a191\n"
    "v_mul_f32 v198, v198, v197\n"
    "v_mul_f32 v199, v199, v197\n"
    "v_accvgpr_write_b32 a190, v198\n"
    "v_accvgpr_write_b32 a191, v199\n"
    "v_exp_f32 v32, v32\n"
    "v_exp_f32 v33, v33\n"
    "v_exp_f32 v34, v34\n"
    "v_cvt_pk_bf16_f32 v128, v32, v33\n"
    "v_exp_f32 v35, v35\n"
    "v_exp_f32 v36, v36\n"
    "v_cvt_pk_bf16_f32 v129, v34, v35\n"
    "v_exp_f32 v37, v37\n"
    "v_exp_f32 v38, v38\n"
    "v_cvt_pk_bf16_f32 v132, v36, v37\n"
    "v_exp_f32 v39, v39\n"
    "v_exp_f32 v40, v40\n"
    "v_cvt_pk_bf16_f32 v133, v38, v39\n"
    "v_exp_f32 v41, v41\n"
    "v_exp_f32 v42, v42\n"
    "v_cvt_pk_bf16_f32 v136, v40, v41\n"
    "v_exp_f32 v43, v43\n"
    "v_exp_f32 v44, v44\n"
    "v_cvt_pk_bf16_f32 v137, v42, v43\n"
    "v_exp_f32 v45, v45\n"
    "v_exp_f32 v46, v46\n"
    "v_cvt_pk_bf16_f32 v140, v44, v45\n"
    "v_exp_f32 v47, v47\n"
    "v_exp_f32 v48, v48\n"
    "v_cvt_pk_bf16_f32 v141, v46, v47\n"
    "v_exp_f32 v49, v49\n"
    "v_exp_f32 v50, v50\n"
    "v_cvt_pk_bf16_f32 v130, v48, v49\n"
    "v_exp_f32 v51, v51\n"
    "v_exp_f32 v52, v52\n"
    "v_cvt_pk_bf16_f32 v131, v50, v51\n"
    "v_exp_f32 v53, v53\n"
    "v_exp_f32 v54, v54\n"
    "v_cvt_pk_bf16_f32 v134, v52, v53\n"
    "v_exp_f32 v55, v55\n"
    "v_exp_f32 v56, v56\n"
    "v_cvt_pk_bf16_f32 v135, v54, v55\n"
    "v_exp_f32 v57, v57\n"
    "v_exp_f32 v58, v58\n"
    "v_cvt_pk_bf16_f32 v138, v56, v57\n"
    "v_exp_f32 v59, v59\n"
    "v_exp_f32 v60, v60\n"
    "v_cvt_pk_bf16_f32 v139, v58, v59\n"
    "v_exp_f32 v61, v61\n"
    "v_exp_f32 v62, v62\n"
    "v_cvt_pk_bf16_f32 v142, v60, v61\n"
    "v_exp_f32 v63, v63\n"
    "s_nop 0\n"
    "v_cvt_pk_bf16_f32 v143, v62, v63\n"
    "v_exp_f32 v64, v64\n"
    "v_exp_f32 v65, v65\n"
    "v_exp_f32 v66, v66\n"
    "v_cvt_pk_bf16_f32 v144, v64, v65\n"
    "v_exp_f32 v67, v67\n"
    "v_exp_f32 v68, v68\n"
    "v_cvt_pk_bf16_f32 v145, v66, v67\n"
    "v_exp_f32 v69, v69\n"
    "v_exp_f32 v70, v70\n"
    "v_cvt_pk_bf16_f32 v148, v68, v69\n"
    "v_exp_f32 v71, v71\n"
    "v_exp_f32 v72, v72\n"
    "v_cvt_pk_bf16_f32 v149, v70, v71\n"
    "v_exp_f32 v73, v73\n"
    "v_exp_f32 v74, v74\n"
    "v_cvt_pk_bf16_f32 v152, v72, v73\n"
    "v_exp_f32 v75, v75\n"
    "v_exp_f32 v76, v76\n"
    "v_cvt_pk_bf16_f32 v153, v74, v75\n"
    "v_exp_f32 v77, v77\n"
    "v_exp_f32 v78, v78\n"
    "v_cvt_pk_bf16_f32 v156, v76, v77\n"
    "v_exp_f32 v79, v79\n"
    "v_exp_f32 v80, v80\n"
    "v_cvt_pk_bf16_f32 v157, v78, v79\n"
    "v_exp_f32 v81, v81\n"
    "v_exp_f32 v82, v82\n"
    "v_cvt_pk_bf16_f32 v146, v80, v81\n"
    "v_exp_f32 v83, v83\n"
    "v_exp_f32 v84, v84\n"
    "v_cvt_pk_bf16_f32 v147, v82, v83\n"
    "v_exp_f32 v85, v85\n"
    "v_exp_f32 v86, v86\n"
    "v_cvt_pk_bf16_f32 v150, v84, v85\n"
    "v_exp_f32 v87, v87\n"
    "v_exp_f32 v88, v88\n"
    "v_cvt_pk_bf16_f32 v151, v86, v87\n"
    "v_exp_f32 v89, v89\n"
    "v_exp_f32 v90, v90\n"
    "v_cvt_pk_bf16_f32 v154, v88, v89\n"
    "v_exp_f32 v91, v91\n"
    "v_exp_f32 v92, v92\n"
    "v_cvt_pk_bf16_f32 v155, v90, v91\n"
    "v_exp_f32 v93, v93\n"
    "v_exp_f32 v94, v94\n"
    "v_cvt_pk_bf16_f32 v158, v92, v93\n"
    "v_exp_f32 v95, v95\n"
    "s_nop 0\n"
    "v_cvt_pk_bf16_f32 v159, v94, v95\n"
    "ds_read_b128 v[160:163], v205 offset:12288\n"
    "ds_read_b128 v[164:167], v205 offset:13312\n"
    "s_waitcnt lgkmcnt(1)\n"
    "v_mfma_f32_16x16x32_bf16 a[0:3], v[160:163], v[128:131], a[0:3]\n"
    "s_waitcnt lgkmcnt(0)\n"
    "v_mfma_f32_16x16x32_bf16 a[0:3], v[164:167], v[144:147], a[0:3]\n"
    "v_mfma_f32_16x16x32_bf16 a[4:7], v[160:163], v[132:135], a[4:7]\n"
    "v_mfma_f32_16x16x32_bf16 a[4:7], v[164:167], v[148:151], a[4:7]\n"
    "v_mfma_f32_16x16x32_bf16 a[8:11], v[160:163], v[136:139], a[8:11]\n"
    "v_mfma_f32_16x16x32_bf16 a[8:11], v[164:167], v[152:155], a[8:11]\n"
    "v_mfma_f32_16x16x32_bf16 a[12:15], v[160:163], v[140:143], a[12:15]\n"
    "v_mfma_f32_16x16x32_bf16 a[12:15], v[164:167], v[156:159], a[12:15]\n"
    "ds_read_b128 v[168:171], v205 offset:14336\n"
    "ds_read_b128 v[172:175], v205 offset:15360\n"
    "s_waitcnt lgkmcnt(1)\n"
    "v_mfma_f32_16x16x32_bf16 a[28:31], v[168:171], v[140:143], a[28:31]\n"
    "s_waitcnt lgkmcnt(0)\n"
    "v_mfma_f32_16x16x32_bf16 a[28:31], v[172:175], v[156:159], a[28:31]\n"
    "v_mfma_f32_16x16x32_bf16 a[24:27], v[172:175], v[152:155], a[24:27]\n"
    "v_mfma_f32_16x16x32_bf16 a[24:27], v[168:171], v[136:139], a[24:27]\n"
    "v_mfma_f32_16x16x32_bf16 a[20:23], v[168:171], v[132:135], a[20:23]\n"
    "v_mfma_f32_16x16x32_bf16 a[20:23], v[172:175], v[148:151], a[20:23]\n"
    "v_mfma_f32_16x16x32_bf16 a[16:19], v[172:175], v[144:147], a[16:19]\n"
    "v_mfma_f32_16x16x32_bf16 a[16:19], v[168:171], v[128:131], a[16:19]\n"
    "ds_read_b128 v[176:179], v205 offset:16384\n"
    "ds_read_b128 v[180:183], v205 offset:17408\n"
    "s_waitcnt lgkmcnt(1)\n"
    "v_mfma_f32_16x16x32_bf16 a[32:35], v[176:179], v[128:131], a[32:35]\n"
    "s_waitcnt lgkmcnt(0)\n"
    "v_mfma_f32_16x16x32_bf16 a[32:35], v[180:183], v[144:147], a[32:35]\n"
    "v_mfma_f32_16x16x32_bf16 a[36:39], v[180:183], v[148:151], a[36:39]\n"
    "v_mfma_f32_16x16x32_bf16 a[36:39], v[176:179], v[132:135], a[36:39]\n"
    "v_mfma_f32_16x16x32_bf16 a[40:43], v[176:179], v[136:139], a[40:43]\n"
    "v_mfma_f32_16x16x32_bf16 a[40:43], v[180:183], v[152:155], a[40:43]\n"
    "v_mfma_f32_16x16x32_bf16 a[44:47], v[180:183], v[156:159], a[44:47]\n"
    "v_mfma_f32_16x16x32_bf16 a[44:47], v[176:179], v[140:143], a[44:47]\n"
    "ds_read_b128 v[160:163], v205 offset:18432\n"
    "ds_read_b128 v[164:167], v205 offset:19456\n"
    "s_waitcnt lgkmcnt(1)\n"
    "v_mfma_f32_16x16x32_bf16 a[60:63], v[160:163], v[140:143], a[60:63]\n"
    "s_waitcnt lgkmcnt(0)\n"
    "v_mfma_f32_16x16x32_bf16 a[60:63], v[164:167], v[156:159], a[60:63]\n"
    "v_mfma_f32_16x16x32_bf16 a[56:59], v[164:167], v[152:155], a[56:59]\n"
    "v_mfma_f32_16x16x32_bf16 a[56:59], v[160:163], v[136:139], a[56:59]\n"
    "v_mfma_f32_16x16x32_bf16 a[52:55], v[160:163], v[132:135], a[52:55]\n"
    "v_mfma_f32_16x16x32_bf16 a[52:55], v[164:167], v[148:151], a[52:55]\n"
    "v_mfma_f32_16x16x32_bf16 a[48:51], v[164:167], v[144:147], a[48:51]\n"
    "v_mfma_f32_16x16x32_bf16 a[48:51], v[160:163], v[128:131], a[48:51]\n"
    "ds_read_b128 v[168:171], v205 offset:20480\n"
    "ds_read_b128 v[172:175], v205 offset:21504\n"
    "s_waitcnt lgkmcnt(1)\n"
    "v_mfma_f32_16x16x32_bf16 a[64:67], v[168:171], v[128:131], a[64:67]\n"
    "s_waitcnt lgkmcnt(0)\n"
    "v_mfma_f32_16x16x32_bf16 a[64:67], v[172:175], v[144:147], a[64:67]\n"
    "v_mfma_f32_16x16x32_bf16 a[68:71], v[172:175], v[148:151], a[68:71]\n"
    "v_mfma_f32_16x16x32_bf16 a[68:71], v[168:171], v[132:135], a[68:71]\n"
    "v_mfma_f32_16x16x32_bf16 a[72:75], v[168:171], v[136:139], a[72:75]\n"
    "v_mfma_f32_16x16x32_bf16 a[72:75], v[172:175], v[152:155], a[72:75]\n"
    "v_mfma_f32_16x16x32_bf16 a[76:79], v[172:175], v[156:159], a[76:79]\n"
    "v_mfma_f32_16x16x32_bf16 a[76:79], v[168:171], v[140:143], a[76:79]\n"
    "ds_read_b128 v[176:179], v205 offset:22528\n"
    "ds_read_b128 v[180:183], v205 offset:23552\n"
    "s_waitcnt lgkmcnt(1)\n"
    "v_mfma_f32_16x16x32_bf16 a[92:95], v[176:179], v[140:143], a[92:95]\n"
    "s_waitcnt lgkmcnt(0)\n"
    "v_mfma_f32_16x16x32_bf16 a[92:95], v[180:183], v[156:159], a[92:95]\n"
    "v_mfma_f32_16x16x32_bf16 a[88:91], v[180:183], v[152:155], a[88:91]\n"
    "v_mfma_f32_16x16x32_bf16 a[88:91], v[176:179], v[136:139], a[88:91]\n"
    "v_mfma_f32_16x16x32_bf16 a[84:87], v[176:179], v[132:135], a[84:87]\n"
    "v_mfma_f32_16x16x32_bf16 a[84:87], v[180:183], v[148:151], a[84:87]\n"
    "v_mfma_f32_16x16x32_bf16 a[80:83], v[180:183], v[144:147], a[80:83]\n"
    "v_mfma_f32_16x16x32_bf16 a[80:83], v[176:179], v[128:131], a[80:83]\n"
    "ds_read_b128 v[160:163], v205 offset:24576\n"
    "ds_read_b128 v[164:167], v205 offset:25600\n"
    "s_waitcnt lgkmcnt(1)\n"
    "v_mfma_f32_16x16x32_bf16 a[96:99], v[160:163], v[128:131], a[96:99]\n"
    "s_waitcnt lgkmcnt(0)\n"
    "v_mfma_f32_16x16x32_bf16 a[96:99], v[164:167], v[144:147], a[96:99]\n"
    "v_mfma_f32_16x16x32_bf16 a[100:103], v[164:167], v[148:151], a[100:103]\n"
    "v_mfma_f32_16x16x32_bf16 a[100:103], v[160:163], v[132:135], a[100:103]\n"
    "v_mfma_f32_16x16x32_bf16 a[104:107], v[160:163], v[136:139], a[104:107]\n"
    "v_mfma_f32_16x16x32_bf16 a[104:107], v[164:167], v[152:155], a[104:107]\n"
    "v_mfma_f32_16x16x32_bf16 a[108:111], v[164:167], v[156:159], a[108:111]\n"
    "v_mfma_f32_16x16x32_bf16 a[108:111], v[160:163], v[140:143], a[108:111]\n"
    "ds_read_b128 v[168:171], v205 offset:26624\n"
    "ds_read_b128 v[172:175], v205 offset:27648\n"
    "s_waitcnt lgkmcnt(1)\n"
    "v_mfma_f32_16x16x32_bf16 a[124:127], v[168:171], v[140:143], a[124:127]\n"
    "s_waitcnt lgkmcnt(0)\n"
    "v_mfma_f32_16x16x32_bf16 a[124:127], v[172:175], v[156:159], a[124:127]\n"
    "v_mfma_f32_16x16x32_bf16 a[120:123], v[172:175], v[152:155], a[120:123]\n"
    "v_mfma_f32_16x16x32_bf16 a[120:123], v[168:171], v[136:139], a[120:123]\n"
    "v_mfma_f32_16x16x32_bf16 a[116:119], v[168:171], v[132:135], a[116:119]\n"
    "v_mfma_f32_16x16x32_bf16 a[116:119], v[172:175], v[148:151], a[116:119]\n"
    "v_mfma_f32_16x16x32_bf16 a[112:115], v[172:175], v[144:147], a[112:115]\n"
    "v_mfma_f32_16x16x32_bf16 a[112:115], v[168:171], v[128:131], a[112:115]\n"
    "ds_read_b128 v[176:179], v205 offset:28672\n"
    "ds_read_b128 v[180:183], v205 offset:29696\n"
    "s_waitcnt lgkmcnt(1)\n"
    "v_mfma_f32_16x16x32_bf16 a[128:131], v[176:179], v[128:131], a[128:131]\n"
    "s_waitcnt lgkmcnt(0)\n"
    "v_mfma_f32_16x16x32_bf16 a[128:131], v[180:183], v[144:147], a[128:131]\n"
    "v_mfma_f32_16x16x32_bf16 a[132:135], v[180:183], v[148:151], a[132:135]\n"
    "v_mfma_f32_16x16x32_bf16 a[132:135], v[176:179], v[132:135], a[132:135]\n"
    "v_mfma_f32_16x16x32_bf16 a[136:139], v[176:179], v[136:139], a[136:139]\n"
    "v_mfma_f32_16x16x32_bf16 a[136:139], v[180:183], v[152:155], a[136:139]\n"
    "v_mfma_f32_16x16x32_bf16 a[140:143], v[180:183], v[156:159], a[140:143]\n"
    "v_mfma_f32_16x16x32_bf16 a[140:143], v[176:179], v[140:143], a[140:143]\n"
    "ds_read_b128 v[160:163], v205 offset:30720\n"
    "ds_read_b128 v[164:167], v205 offset:31744\n"
    "s_waitcnt lgkmcnt(1)\n"
    "v_mfma_f32_16x16x32_bf16 a[156:159], v[160:163], v[140:143], a[156:159]\n"
    "s_waitcnt lgkmcnt(0)\n"
    "v_mfma_f32_16x16x32_bf16 a[156:159], v[164:167], v[156:159], a[156:159]\n"
    "v_mfma_f32_16x16x32_bf16 a[152:155], v[164:167], v[152:155], a[152:155]\n"
    "v_mfma_f32_16x16x32_bf16 a[152:155], v[160:163], v[136:139], a[152:155]\n"
    "v_mfma_f32_16x16x32_bf16 a[148:151], v[160:163], v[132:135], a[148:151]\n"
    "v_mfma_f32_16x16x32_bf16 a[148:151], v[164:167], v[148:151], a[148:151]\n"
    "v_mfma_f32_16x16x32_bf16 a[144:147], v[164:167], v[144:147], a[144:147]\n"
    "v_mfma_f32_16x16x32_bf16 a[144:147], v[160:163], v[128:131], a[144:147]\n"
    "ds_read_b128 v[168:171], v205 offset:32768\n"
    "ds_read_b128 v[172:175], v205 offset:33792\n"
    "s_waitcnt lgkmcnt(1)\n"
    "v_mfma_f32_16x16x32_bf16 a[160:163], v[168:171], v[128:131], a[160:163]\n"
    "s_waitcnt lgkmcnt(0)\n"
    "v_mfma_f32_16x16x32_bf16 a[160:163], v[172:175], v[144:147], a[160:163]\n"
    "v_mfma_f32_16x16x32_bf16 a[164:167], v[172:175], v[148:151], a[164:167]\n"
    "v_mfma_f32_16x16x32_bf16 a[164:167], v[168:171], v[132:135], a[164:167]\n"
    "v_mfma_f32_16x16x32_bf16 a[168:171], v[168:171], v[136:139], a[168:171]\n"
    "v_mfma_f32_16x16x32_bf16 a[168:171], v[172:175], v[152:155], a[168:171]\n"
    "v_mfma_f32_16x16x32_bf16 a[172:175], v[172:175], v[156:159], a[172:175]\n"
    "v_mfma_f32_16x16x32_bf16 a[172:175], v[168:171], v[140:143], a[172:175]\n"
    "ds_read_b128 v[176:179], v205 offset:34816\n"
    "ds_read_b128 v[180:183], v205 offset:35840\n"
    "s_waitcnt lgkmcnt(1)\n"
    "v_mfma_f32_16x16x32_bf16 a[188:191], v[176:179], v[140:143], a[188:191]\n"
    "s_waitcnt lgkmcnt(0)\n"
    "v_mfma_f32_16x16x32_bf16 a[188:191], v[180:183], v[156:159], a[188:191]\n"
    "v_mfma_f32_16x16x32_bf16 a[184:187], v[176:179], v[136:139], a[184:187]\n"
    "v_mfma_f32_16x16x32_bf16 a[184:187], v[180:183], v[152:155], a[184:187]\n"
    "v_mfma_f32_16x16x32_bf16 a[180:183], v[176:179], v[132:135], a[180:183]\n"
    "v_mfma_f32_16x16x32_bf16 a[180:183], v[180:183], v[148:151], a[180:183]\n"
    "v_mfma_f32_16x16x32_bf16 a[176:179], v[176:179], v[128:131], a[176:179]\n"
    "v_mfma_f32_16x16x32_bf16 a[176:179], v[180:183], v[144:147], a[176:179]\n"
    "v_mfma_f32_16x16x32_bf16 v[112:115], v[192:195], v[128:131], v[112:115]\n"
    "v_mfma_f32_16x16x32_bf16 v[112:115], v[192:195], v[144:147], v[112:115]\n"
    "v_mfma_f32_16x16x32_bf16 v[116:119], v[192:195], v[132:135], v[116:119]\n"
    "v_mfma_f32_16x16x32_bf16 v[116:119], v[192:195], v[148:151], v[116:119]\n"
    "v_mfma_f32_16x16x32_bf16 v[120:123], v[192:195], v[136:139], v[120:123]\n"
    "v_mfma_f32_16x16x32_bf16 v[120:123], v[192:195], v[152:155], v[120:123]\n"
    "v_mfma_f32_16x16x32_bf16 v[124:127], v[192:195], v[140:143], v[124:127]\n"
    "v_mfma_f32_16x16x32_bf16 v[124:127], v[192:195], v[156:159], v[124:127]\n"
    "s_nop 15\n"
    "s_add_u32 s63, s63, 0x6000\n"
    "s_cmp_lt_u32 s63, 0xc0000\n"
    "s_cbranch_scc1 LSLOOP\n"
    "s_branch LEPI\n"
    "LEPI:\n"
    "s_lshl_b32 s76, s56, 3\n"
    "s_add_u32 s76, s76, s57\n"
    "s_mul_i32 s76, s76, 0x18000\n"
    "s_add_u32 s76, s76, s88\n"
    "s_add_u32 s28, s24, s76\n"
    "s_addc_u32 s29, s25, 0\n"
    "s_and_b32 s29, s29, 0xffff\n"
    "s_mov_b32 s30, 0x6000\n"
    "s_mov_b32 s31, 0x20000\n"
    "s_xor_b32 s94, s70, 0x80000000\n"
    "s_xor_b32 s95, s70, 0x80000000\n"
    "s_mov_b32 s62, 0x1800\n"
    "s_mov_b32 s63, 0x3000\n"
    "s_mov_b32 s64, 0x4800\n"
    "v_and_b32 v198, 15, v231\n"
    "v_mul_u32_u24 v205, 0x180, v198\n"
    "v_lshrrev_b32 v199, 4, v231\n"
    "v_lshl_add_u32 v205, v199, 4, v205\n"
    "v_mul_f32 v222, s68, v112\n"
    "v_rcp_f32 v222, v222\n"
    "v_mul_f32 v224, s68, v116\n"
    "v_rcp_f32 v224, v224\n"
    "v_mul_f32 v226, s68, v120\n"
    "v_rcp_f32 v226, v226\n"
    "v_mul_f32 v228, s68, v124\n"
    "v_rcp_f32 v228, v228\n"
    "v_mov_b32 v223, v222\n"
    "v_mov_b32 v225, v224\n"
    "v_mov_b32 v227, v226\n"
    "v_mov_b32 v229, v228\n"
    "v_accvgpr_read_b32 v96, a0\n"
    "v_accvgpr_read_b32 v97, a1\n"
    "v_accvgpr_read_b32 v98, a2\n"
    "v_accvgpr_read_b32 v99, a3\n"
    "v_pk_fma_f32 v[96:97], v[96:97], v[222:223], s[94:95]\n"
    "v_pk_fma_f32 v[98:99], v[98:99], v[222:223], s[94:95]\n"
    "v_cvt_pk_bf16_f32 v32, v96, v97\n"
    "v_cvt_pk_bf16_f32 v33, v98, v99\n"
    "v_accvgpr_read_b32 v100, a16\n"
    "v_accvgpr_read_b32 v101, a17\n"
    "v_accvgpr_read_b32 v102, a18\n"
    "v_accvgpr_read_b32 v103, a19\n"
    "v_pk_fma_f32 v[100:101], v[100:101], v[222:223], s[94:95]\n"
    "v_pk_fma_f32 v[102:103], v[102:103], v[222:223], s[94:95]\n"
    "v_cvt_pk_bf16_f32 v34, v100, v101\n"
    "v_cvt_pk_bf16_f32 v35, v102, v103\n"
    "buffer_store_dwordx4 v[32:35], v205, s[28:31], 0 offen sc0 sc1\n"
    "v_accvgpr_read_b32 v104, a32\n"
    "v_accvgpr_read_b32 v105, a33\n"
    "v_accvgpr_read_b32 v106, a34\n"
    "v_accvgpr_read_b32 v107, a35\n"
    "v_pk_fma_f32 v[104:105], v[104:105], v[222:223], s[94:95]\n"
    "v_pk_fma_f32 v[106:107], v[106:107], v[222:223], s[94:95]\n"
    "v_cvt_pk_bf16_f32 v36, v104, v105\n"
    "v_cvt_pk_bf16_f32 v37, v106, v107\n"
    "v_accvgpr_read_b32 v108, a48\n"
    "v_accvgpr_read_b32 v109, a49\n"
    "v_accvgpr_read_b32 v110, a50\n"
    "v_accvgpr_read_b32 v111, a51\n"
    "v_pk_fma_f32 v[108:109], v[108:109], v[222:223], s[94:95]\n"
    "v_pk_fma_f32 v[110:111], v[110:111], v[222:223], s[94:95]\n"
    "v_cvt_pk_bf16_f32 v38, v108, v109\n"
    "v_cvt_pk_bf16_f32 v39, v110, v111\n"
    "buffer_store_dwordx4 v[36:39], v205, s[28:31], 0 offen offset:64 sc0 sc1\n"
    "v_accvgpr_read_b32 v96, a64\n"
    "v_accvgpr_read_b32 v97, a65\n"
    "v_accvgpr_read_b32 v98, a66\n"
    "v_accvgpr_read_b32 v99, a67\n"
    "v_pk_fma_f32 v[96:97], v[96:97], v[222:223], s[94:95]\n"
    "v_pk_fma_f32 v[98:99], v[98:99], v[222:223], s[94:95]\n"
    "v_cvt_pk_bf16_f32 v40, v96, v97\n"
    "v_cvt_pk_bf16_f32 v41, v98, v99\n"
    "v_accvgpr_read_b32 v100, a80\n"
    "v_accvgpr_read_b32 v101, a81\n"
    "v_accvgpr_read_b32 v102, a82\n"
    "v_accvgpr_read_b32 v103, a83\n"
    "v_pk_fma_f32 v[100:101], v[100:101], v[222:223], s[94:95]\n"
    "v_pk_fma_f32 v[102:103], v[102:103], v[222:223], s[94:95]\n"
    "v_cvt_pk_bf16_f32 v42, v100, v101\n"
    "v_cvt_pk_bf16_f32 v43, v102, v103\n"
    "buffer_store_dwordx4 v[40:43], v205, s[28:31], 0 offen offset:128 sc0 sc1\n"
    "v_accvgpr_read_b32 v104, a96\n"
    "v_accvgpr_read_b32 v105, a97\n"
    "v_accvgpr_read_b32 v106, a98\n"
    "v_accvgpr_read_b32 v107, a99\n"
    "v_pk_fma_f32 v[104:105], v[104:105], v[222:223], s[94:95]\n"
    "v_pk_fma_f32 v[106:107], v[106:107], v[222:223], s[94:95]\n"
    "v_cvt_pk_bf16_f32 v44, v104, v105\n"
    "v_cvt_pk_bf16_f32 v45, v106, v107\n"
    "v_accvgpr_read_b32 v108, a112\n"
    "v_accvgpr_read_b32 v109, a113\n"
    "v_accvgpr_read_b32 v110, a114\n"
    "v_accvgpr_read_b32 v111, a115\n"
    "v_pk_fma_f32 v[108:109], v[108:109], v[222:223], s[94:95]\n"
    "v_pk_fma_f32 v[110:111], v[110:111], v[222:223], s[94:95]\n"
    "v_cvt_pk_bf16_f32 v46, v108, v109\n"
    "v_cvt_pk_bf16_f32 v47, v110, v111\n"
    "buffer_store_dwordx4 v[44:47], v205, s[28:31], 0 offen offset:192 sc0 sc1\n"
    "v_accvgpr_read_b32 v96, a128\n"
    "v_accvgpr_read_b32 v97, a129\n"
    "v_accvgpr_read_b32 v98, a130\n"
    "v_accvgpr_read_b32 v99, a131\n"
    "v_pk_fma_f32 v[96:97], v[96:97], v[222:223], s[94:95]\n"
    "v_pk_fma_f32 v[98:99], v[98:99], v[222:223], s[94:95]\n"
    "v_cvt_pk_bf16_f32 v48, v96, v97\n"
    "v_cvt_pk_bf16_f32 v49, v98, v99\n"
    "v_accvgpr_read_b32 v100, a144\n"
    "v_accvgpr_read_b32 v101, a145\n"
    "v_accvgpr_read_b32 v102, a146\n"
    "v_accvgpr_read_b32 v103, a147\n"
    "v_pk_fma_f32 v[100:101], v[100:101], v[222:223], s[94:95]\n"
    "v_pk_fma_f32 v[102:103], v[102:103], v[222:223], s[94:95]\n"
    "v_cvt_pk_bf16_f32 v50, v100, v101\n"
    "v_cvt_pk_bf16_f32 v51, v102, v103\n"
    "buffer_store_dwordx4 v[48:51], v205, s[28:31], 0 offen offset:256 sc0 sc1\n"
    "v_accvgpr_read_b32 v104, a160\n"
    "v_accvgpr_read_b32 v105, a161\n"
    "v_accvgpr_read_b32 v106, a162\n"
    "v_accvgpr_read_b32 v107, a163\n"
    "v_pk_fma_f32 v[104:105], v[104:105], v[222:223], s[94:95]\n"
    "v_pk_fma_f32 v[106:107], v[106:107], v[222:223], s[94:95]\n"
    "v_cvt_pk_bf16_f32 v52, v104, v105\n"
    "v_cvt_pk_bf16_f32 v53, v106, v107\n"
    "v_accvgpr_read_b32 v108, a176\n"
    "v_accvgpr_read_b32 v109, a177\n"
    "v_accvgpr_read_b32 v110, a178\n"
    "v_accvgpr_read_b32 v111, a179\n"
    "v_pk_fma_f32 v[108:109], v[108:109], v[222:223], s[94:95]\n"
    "v_pk_fma_f32 v[110:111], v[110:111], v[222:223], s[94:95]\n"
    "v_cvt_pk_bf16_f32 v54, v108, v109\n"
    "v_cvt_pk_bf16_f32 v55, v110, v111\n"
    "buffer_store_dwordx4 v[52:55], v205, s[28:31], 0 offen offset:320 sc0 sc1\n"
    "v_accvgpr_read_b32 v96, a4\n"
    "v_accvgpr_read_b32 v97, a5\n"
    "v_accvgpr_read_b32 v98, a6\n"
    "v_accvgpr_read_b32 v99, a7\n"
    "v_pk_fma_f32 v[96:97], v[96:97], v[224:225], s[94:95]\n"
    "v_pk_fma_f32 v[98:99], v[98:99], v[224:225], s[94:95]\n"
    "v_cvt_pk_bf16_f32 v56, v96, v97\n"
    "v_cvt_pk_bf16_f32 v57, v98, v99\n"
    "v_accvgpr_read_b32 v100, a20\n"
    "v_accvgpr_read_b32 v101, a21\n"
    "v_accvgpr_read_b32 v102, a22\n"
    "v_accvgpr_read_b32 v103, a23\n"
    "v_pk_fma_f32 v[100:101], v[100:101], v[224:225], s[94:95]\n"
    "v_pk_fma_f32 v[102:103], v[102:103], v[224:225], s[94:95]\n"
    "v_cvt_pk_bf16_f32 v58, v100, v101\n"
    "v_cvt_pk_bf16_f32 v59, v102, v103\n"
    "buffer_store_dwordx4 v[56:59], v205, s[28:31], s62 offen sc0 sc1\n"
    "v_accvgpr_read_b32 v104, a36\n"
    "v_accvgpr_read_b32 v105, a37\n"
    "v_accvgpr_read_b32 v106, a38\n"
    "v_accvgpr_read_b32 v107, a39\n"
    "v_pk_fma_f32 v[104:105], v[104:105], v[224:225], s[94:95]\n"
    "v_pk_fma_f32 v[106:107], v[106:107], v[224:225], s[94:95]\n"
    "v_cvt_pk_bf16_f32 v60, v104, v105\n"
    "v_cvt_pk_bf16_f32 v61, v106, v107\n"
    "v_accvgpr_read_b32 v108, a52\n"
    "v_accvgpr_read_b32 v109, a53\n"
    "v_accvgpr_read_b32 v110, a54\n"
    "v_accvgpr_read_b32 v111, a55\n"
    "v_pk_fma_f32 v[108:109], v[108:109], v[224:225], s[94:95]\n"
    "v_pk_fma_f32 v[110:111], v[110:111], v[224:225], s[94:95]\n"
    "v_cvt_pk_bf16_f32 v62, v108, v109\n"
    "v_cvt_pk_bf16_f32 v63, v110, v111\n"
    "buffer_store_dwordx4 v[60:63], v205, s[28:31], s62 offen offset:64 sc0 sc1\n"
    "v_accvgpr_read_b32 v96, a68\n"
    "v_accvgpr_read_b32 v97, a69\n"
    "v_accvgpr_read_b32 v98, a70\n"
    "v_accvgpr_read_b32 v99, a71\n"
    "v_pk_fma_f32 v[96:97], v[96:97], v[224:225], s[94:95]\n"
    "v_pk_fma_f32 v[98:99], v[98:99], v[224:225], s[94:95]\n"
    "v_cvt_pk_bf16_f32 v32, v96, v97\n"
    "v_cvt_pk_bf16_f32 v33, v98, v99\n"
    "v_accvgpr_read_b32 v100, a84\n"
    "v_accvgpr_read_b32 v101, a85\n"
    "v_accvgpr_read_b32 v102, a86\n"
    "v_accvgpr_read_b32 v103, a87\n"
    "v_pk_fma_f32 v[100:101], v[100:101], v[224:225], s[94:95]\n"
    "v_pk_fma_f32 v[102:103], v[102:103], v[224:225], s[94:95]\n"
    "v_cvt_pk_bf16_f32 v34, v100, v101\n"
    "v_cvt_pk_bf16_f32 v35, v102, v103\n"
    "buffer_store_dwordx4 v[32:35], v205, s[28:31], s62 offen offset:128 sc0 sc1\n"
    "v_accvgpr_read_b32 v104, a100\n"
    "v_accvgpr_read_b32 v105, a101\n"
    "v_accvgpr_read_b32 v106, a102\n"
    "v_accvgpr_read_b32 v107, a103\n"
    "v_pk_fma_f32 v[104:105], v[104:105], v[224:225], s[94:95]\n"
    "v_pk_fma_f32 v[106:107], v[106:107], v[224:225], s[94:95]\n"
    "v_cvt_pk_bf16_f32 v36, v104, v105\n"
    "v_cvt_pk_bf16_f32 v37, v106, v107\n"
    "v_accvgpr_read_b32 v108, a116\n"
    "v_accvgpr_read_b32 v109, a117\n"
    "v_accvgpr_read_b32 v110, a118\n"
    "v_accvgpr_read_b32 v111, a119\n"
    "v_pk_fma_f32 v[108:109], v[108:109], v[224:225], s[94:95]\n"
    "v_pk_fma_f32 v[110:111], v[110:111], v[224:225], s[94:95]\n"
    "v_cvt_pk_bf16_f32 v38, v108, v109\n"
    "v_cvt_pk_bf16_f32 v39, v110, v111\n"
    "buffer_store_dwordx4 v[36:39], v205, s[28:31], s62 offen offset:192 sc0 sc1\n"
    "v_accvgpr_read_b32 v96, a132\n"
    "v_accvgpr_read_b32 v97, a133\n"
    "v_accvgpr_read_b32 v98, a134\n"
    "v_accvgpr_read_b32 v99, a135\n"
    "v_pk_fma_f32 v[96:97], v[96:97], v[224:225], s[94:95]\n"
    "v_pk_fma_f32 v[98:99], v[98:99], v[224:225], s[94:95]\n"
    "v_cvt_pk_bf16_f32 v40, v96, v97\n"
    "v_cvt_pk_bf16_f32 v41, v98, v99\n"
    "v_accvgpr_read_b32 v100, a148\n"
    "v_accvgpr_read_b32 v101, a149\n"
    "v_accvgpr_read_b32 v102, a150\n"
    "v_accvgpr_read_b32 v103, a151\n"
    "v_pk_fma_f32 v[100:101], v[100:101], v[224:225], s[94:95]\n"
    "v_pk_fma_f32 v[102:103], v[102:103], v[224:225], s[94:95]\n"
    "v_cvt_pk_bf16_f32 v42, v100, v101\n"
    "v_cvt_pk_bf16_f32 v43, v102, v103\n"
    "buffer_store_dwordx4 v[40:43], v205, s[28:31], s62 offen offset:256 sc0 sc1\n"
    "v_accvgpr_read_b32 v104, a164\n"
    "v_accvgpr_read_b32 v105, a165\n"
    "v_accvgpr_read_b32 v106, a166\n"
    "v_accvgpr_read_b32 v107, a167\n"
    "v_pk_fma_f32 v[104:105], v[104:105], v[224:225], s[94:95]\n"
    "v_pk_fma_f32 v[106:107], v[106:107], v[224:225], s[94:95]\n"
    "v_cvt_pk_bf16_f32 v44, v104, v105\n"
    "v_cvt_pk_bf16_f32 v45, v106, v107\n"
    "v_accvgpr_read_b32 v108, a180\n"
    "v_accvgpr_read_b32 v109, a181\n"
    "v_accvgpr_read_b32 v110, a182\n"
    "v_accvgpr_read_b32 v111, a183\n"
    "v_pk_fma_f32 v[108:109], v[108:109], v[224:225], s[94:95]\n"
    "v_pk_fma_f32 v[110:111], v[110:111], v[224:225], s[94:95]\n"
    "v_cvt_pk_bf16_f32 v46, v108, v109\n"
    "v_cvt_pk_bf16_f32 v47, v110, v111\n"
    "buffer_store_dwordx4 v[44:47], v205, s[28:31], s62 offen offset:320 sc0 sc1\n"
    "v_accvgpr_read_b32 v96, a8\n"
    "v_accvgpr_read_b32 v97, a9\n"
    "v_accvgpr_read_b32 v98, a10\n"
    "v_accvgpr_read_b32 v99, a11\n"
    "v_pk_fma_f32 v[96:97], v[96:97], v[226:227], s[94:95]\n"
    "v_pk_fma_f32 v[98:99], v[98:99], v[226:227], s[94:95]\n"
    "v_cvt_pk_bf16_f32 v48, v96, v97\n"
    "v_cvt_pk_bf16_f32 v49, v98, v99\n"
    "v_accvgpr_read_b32 v100, a24\n"
    "v_accvgpr_read_b32 v101, a25\n"
    "v_accvgpr_read_b32 v102, a26\n"
    "v_accvgpr_read_b32 v103, a27\n"
    "v_pk_fma_f32 v[100:101], v[100:101], v[226:227], s[94:95]\n"
    "v_pk_fma_f32 v[102:103], v[102:103], v[226:227], s[94:95]\n"
    "v_cvt_pk_bf16_f32 v50, v100, v101\n"
    "v_cvt_pk_bf16_f32 v51, v102, v103\n"
    "buffer_store_dwordx4 v[48:51], v205, s[28:31], s63 offen sc0 sc1\n"
    "v_accvgpr_read_b32 v104, a40\n"
    "v_accvgpr_read_b32 v105, a41\n"
    "v_accvgpr_read_b32 v106, a42\n"
    "v_accvgpr_read_b32 v107, a43\n"
    "v_pk_fma_f32 v[104:105], v[104:105], v[226:227], s[94:95]\n"
    "v_pk_fma_f32 v[106:107], v[106:107], v[226:227], s[94:95]\n"
    "v_cvt_pk_bf16_f32 v52, v104, v105\n"
    "v_cvt_pk_bf16_f32 v53, v106, v107\n"
    "v_accvgpr_read_b32 v108, a56\n"
    "v_accvgpr_read_b32 v109, a57\n"
    "v_accvgpr_read_b32 v110, a58\n"
    "v_accvgpr_read_b32 v111, a59\n"
    "v_pk_fma_f32 v[108:109], v[108:109], v[226:227], s[94:95]\n"
    "v_pk_fma_f32 v[110:111], v[110:111], v[226:227], s[94:95]\n"
    "v_cvt_pk_bf16_f32 v54, v108, v109\n"
    "v_cvt_pk_bf16_f32 v55, v110, v111\n"
    "buffer_store_dwordx4 v[52:55], v205, s[28:31], s63 offen offset:64 sc0 sc1\n"
    "v_accvgpr_read_b32 v96, a72\n"
    "v_accvgpr_read_b32 v97, a73\n"
    "v_accvgpr_read_b32 v98, a74\n"
    "v_accvgpr_read_b32 v99, a75\n"
    "v_pk_fma_f32 v[96:97], v[96:97], v[226:227], s[94:95]\n"
    "v_pk_fma_f32 v[98:99], v[98:99], v[226:227], s[94:95]\n"
    "v_cvt_pk_bf16_f32 v56, v96, v97\n"
    "v_cvt_pk_bf16_f32 v57, v98, v99\n"
    "v_accvgpr_read_b32 v100, a88\n"
    "v_accvgpr_read_b32 v101, a89\n"
    "v_accvgpr_read_b32 v102, a90\n"
    "v_accvgpr_read_b32 v103, a91\n"
    "v_pk_fma_f32 v[100:101], v[100:101], v[226:227], s[94:95]\n"
    "v_pk_fma_f32 v[102:103], v[102:103], v[226:227], s[94:95]\n"
    "v_cvt_pk_bf16_f32 v58, v100, v101\n"
    "v_cvt_pk_bf16_f32 v59, v102, v103\n"
    "buffer_store_dwordx4 v[56:59], v205, s[28:31], s63 offen offset:128 sc0 sc1\n"
    "v_accvgpr_read_b32 v104, a104\n"
    "v_accvgpr_read_b32 v105, a105\n"
    "v_accvgpr_read_b32 v106, a106\n"
    "v_accvgpr_read_b32 v107, a107\n"
    "v_pk_fma_f32 v[104:105], v[104:105], v[226:227], s[94:95]\n"
    "v_pk_fma_f32 v[106:107], v[106:107], v[226:227], s[94:95]\n"
    "v_cvt_pk_bf16_f32 v60, v104, v105\n"
    "v_cvt_pk_bf16_f32 v61, v106, v107\n"
    "v_accvgpr_read_b32 v108, a120\n"
    "v_accvgpr_read_b32 v109, a121\n"
    "v_accvgpr_read_b32 v110, a122\n"
    "v_accvgpr_read_b32 v111, a123\n"
    "v_pk_fma_f32 v[108:109], v[108:109], v[226:227], s[94:95]\n"
    "v_pk_fma_f32 v[110:111], v[110:111], v[226:227], s[94:95]\n"
    "v_cvt_pk_bf16_f32 v62, v108, v109\n"
    "v_cvt_pk_bf16_f32 v63, v110, v111\n"
    "buffer_store_dwordx4 v[60:63], v205, s[28:31], s63 offen offset:192 sc0 sc1\n"
    "v_accvgpr_read_b32 v96, a136\n"
    "v_accvgpr_read_b32 v97, a137\n"
    "v_accvgpr_read_b32 v98, a138\n"
    "v_accvgpr_read_b32 v99, a139\n"
    "v_pk_fma_f32 v[96:97], v[96:97], v[226:227], s[94:95]\n"
    "v_pk_fma_f32 v[98:99], v[98:99], v[226:227], s[94:95]\n"
    "v_cvt_pk_bf16_f32 v32, v96, v97\n"
    "v_cvt_pk_bf16_f32 v33, v98, v99\n"
    "v_accvgpr_read_b32 v100, a152\n"
    "v_accvgpr_read_b32 v101, a153\n"
    "v_accvgpr_read_b32 v102, a154\n"
    "v_accvgpr_read_b32 v103, a155\n"
    "v_pk_fma_f32 v[100:101], v[100:101], v[226:227], s[94:95]\n"
    "v_pk_fma_f32 v[102:103], v[102:103], v[226:227], s[94:95]\n"
    "v_cvt_pk_bf16_f32 v34, v100, v101\n"
    "v_cvt_pk_bf16_f32 v35, v102, v103\n"
    "buffer_store_dwordx4 v[32:35], v205, s[28:31], s63 offen offset:256 sc0 sc1\n"
    "v_accvgpr_read_b32 v104, a168\n"
    "v_accvgpr_read_b32 v105, a169\n"
    "v_accvgpr_read_b32 v106, a170\n"
    "v_accvgpr_read_b32 v107, a171\n"
    "v_pk_fma_f32 v[104:105], v[104:105], v[226:227], s[94:95]\n"
    "v_pk_fma_f32 v[106:107], v[106:107], v[226:227], s[94:95]\n"
    "v_cvt_pk_bf16_f32 v36, v104, v105\n"
    "v_cvt_pk_bf16_f32 v37, v106, v107\n"
    "v_accvgpr_read_b32 v108, a184\n"
    "v_accvgpr_read_b32 v109, a185\n"
    "v_accvgpr_read_b32 v110, a186\n"
    "v_accvgpr_read_b32 v111, a187\n"
    "v_pk_fma_f32 v[108:109], v[108:109], v[226:227], s[94:95]\n"
    "v_pk_fma_f32 v[110:111], v[110:111], v[226:227], s[94:95]\n"
    "v_cvt_pk_bf16_f32 v38, v108, v109\n"
    "v_cvt_pk_bf16_f32 v39, v110, v111\n"
    "buffer_store_dwordx4 v[36:39], v205, s[28:31], s63 offen offset:320 sc0 sc1\n"
    "v_accvgpr_read_b32 v96, a12\n"
    "v_accvgpr_read_b32 v97, a13\n"
    "v_accvgpr_read_b32 v98, a14\n"
    "v_accvgpr_read_b32 v99, a15\n"
    "v_pk_fma_f32 v[96:97], v[96:97], v[228:229], s[94:95]\n"
    "v_pk_fma_f32 v[98:99], v[98:99], v[228:229], s[94:95]\n"
    "v_cvt_pk_bf16_f32 v40, v96, v97\n"
    "v_cvt_pk_bf16_f32 v41, v98, v99\n"
    "v_accvgpr_read_b32 v100, a28\n"
    "v_accvgpr_read_b32 v101, a29\n"
    "v_accvgpr_read_b32 v102, a30\n"
    "v_accvgpr_read_b32 v103, a31\n"
    "v_pk_fma_f32 v[100:101], v[100:101], v[228:229], s[94:95]\n"
    "v_pk_fma_f32 v[102:103], v[102:103], v[228:229], s[94:95]\n"
    "v_cvt_pk_bf16_f32 v42, v100, v101\n"
    "v_cvt_pk_bf16_f32 v43, v102, v103\n"
    "buffer_store_dwordx4 v[40:43], v205, s[28:31], s64 offen sc0 sc1\n"
    "v_accvgpr_read_b32 v104, a44\n"
    "v_accvgpr_read_b32 v105, a45\n"
    "v_accvgpr_read_b32 v106, a46\n"
    "v_accvgpr_read_b32 v107, a47\n"
    "v_pk_fma_f32 v[104:105], v[104:105], v[228:229], s[94:95]\n"
    "v_pk_fma_f32 v[106:107], v[106:107], v[228:229], s[94:95]\n"
    "v_cvt_pk_bf16_f32 v44, v104, v105\n"
    "v_cvt_pk_bf16_f32 v45, v106, v107\n"
    "v_accvgpr_read_b32 v108, a60\n"
    "v_accvgpr_read_b32 v109, a61\n"
    "v_accvgpr_read_b32 v110, a62\n"
    "v_accvgpr_read_b32 v111, a63\n"
    "v_pk_fma_f32 v[108:109], v[108:109], v[228:229], s[94:95]\n"
    "v_pk_fma_f32 v[110:111], v[110:111], v[228:229], s[94:95]\n"
    "v_cvt_pk_bf16_f32 v46, v108, v109\n"
    "v_cvt_pk_bf16_f32 v47, v110, v111\n"
    "buffer_store_dwordx4 v[44:47], v205, s[28:31], s64 offen offset:64 sc0 sc1\n"
    "v_accvgpr_read_b32 v96, a76\n"
    "v_accvgpr_read_b32 v97, a77\n"
    "v_accvgpr_read_b32 v98, a78\n"
    "v_accvgpr_read_b32 v99, a79\n"
    "v_pk_fma_f32 v[96:97], v[96:97], v[228:229], s[94:95]\n"
    "v_pk_fma_f32 v[98:99], v[98:99], v[228:229], s[94:95]\n"
    "v_cvt_pk_bf16_f32 v48, v96, v97\n"
    "v_cvt_pk_bf16_f32 v49, v98, v99\n"
    "v_accvgpr_read_b32 v100, a92\n"
    "v_accvgpr_read_b32 v101, a93\n"
    "v_accvgpr_read_b32 v102, a94\n"
    "v_accvgpr_read_b32 v103, a95\n"
    "v_pk_fma_f32 v[100:101], v[100:101], v[228:229], s[94:95]\n"
    "v_pk_fma_f32 v[102:103], v[102:103], v[228:229], s[94:95]\n"
    "v_cvt_pk_bf16_f32 v50, v100, v101\n"
    "v_cvt_pk_bf16_f32 v51, v102, v103\n"
    "buffer_store_dwordx4 v[48:51], v205, s[28:31], s64 offen offset:128 sc0 sc1\n"
    "v_accvgpr_read_b32 v104, a108\n"
    "v_accvgpr_read_b32 v105, a109\n"
    "v_accvgpr_read_b32 v106, a110\n"
    "v_accvgpr_read_b32 v107, a111\n"
    "v_pk_fma_f32 v[104:105], v[104:105], v[228:229], s[94:95]\n"
    "v_pk_fma_f32 v[106:107], v[106:107], v[228:229], s[94:95]\n"
    "v_cvt_pk_bf16_f32 v52, v104, v105\n"
    "v_cvt_pk_bf16_f32 v53, v106, v107\n"
    "v_accvgpr_read_b32 v108, a124\n"
    "v_accvgpr_read_b32 v109, a125\n"
    "v_accvgpr_read_b32 v110, a126\n"
    "v_accvgpr_read_b32 v111, a127\n"
    "v_pk_fma_f32 v[108:109], v[108:109], v[228:229], s[94:95]\n"
    "v_pk_fma_f32 v[110:111], v[110:111], v[228:229], s[94:95]\n"
    "v_cvt_pk_bf16_f32 v54, v108, v109\n"
    "v_cvt_pk_bf16_f32 v55, v110, v111\n"
    "buffer_store_dwordx4 v[52:55], v205, s[28:31], s64 offen offset:192 sc0 sc1\n"
    "v_accvgpr_read_b32 v96, a140\n"
    "v_accvgpr_read_b32 v97, a141\n"
    "v_accvgpr_read_b32 v98, a142\n"
    "v_accvgpr_read_b32 v99, a143\n"
    "v_pk_fma_f32 v[96:97], v[96:97], v[228:229], s[94:95]\n"
    "v_pk_fma_f32 v[98:99], v[98:99], v[228:229], s[94:95]\n"
    "v_cvt_pk_bf16_f32 v56, v96, v97\n"
    "v_cvt_pk_bf16_f32 v57, v98, v99\n"
    "v_accvgpr_read_b32 v100, a156\n"
    "v_accvgpr_read_b32 v101, a157\n"
    "v_accvgpr_read_b32 v102, a158\n"
    "v_accvgpr_read_b32 v103, a159\n"
    "v_pk_fma_f32 v[100:101], v[100:101], v[228:229], s[94:95]\n"
    "v_pk_fma_f32 v[102:103], v[102:103], v[228:229], s[94:95]\n"
    "v_cvt_pk_bf16_f32 v58, v100, v101\n"
    "v_cvt_pk_bf16_f32 v59, v102, v103\n"
    "buffer_store_dwordx4 v[56:59], v205, s[28:31], s64 offen offset:256 sc0 sc1\n"
    "v_accvgpr_read_b32 v104, a172\n"
    "v_accvgpr_read_b32 v105, a173\n"
    "v_accvgpr_read_b32 v106, a174\n"
    "v_accvgpr_read_b32 v107, a175\n"
    "v_pk_fma_f32 v[104:105], v[104:105], v[228:229], s[94:95]\n"
    "v_pk_fma_f32 v[106:107], v[106:107], v[228:229], s[94:95]\n"
    "v_cvt_pk_bf16_f32 v60, v104, v105\n"
    "v_cvt_pk_bf16_f32 v61, v106, v107\n"
    "v_accvgpr_read_b32 v108, a188\n"
    "v_accvgpr_read_b32 v109, a189\n"
    "v_accvgpr_read_b32 v110, a190\n"
    "v_accvgpr_read_b32 v111, a191\n"
    "v_pk_fma_f32 v[108:109], v[108:109], v[228:229], s[94:95]\n"
    "v_pk_fma_f32 v[110:111], v[110:111], v[228:229], s[94:95]\n"
    "v_cvt_pk_bf16_f32 v62, v108, v109\n"
    "v_cvt_pk_bf16_f32 v63, v110, v111\n"
    "buffer_store_dwordx4 v[60:63], v205, s[28:31], s64 offen offset:320 sc0 sc1\n"
    "s_branch LFIN\n"
    "LEND:\n"
    :
    : "s"(__builtin_amdgcn_kernarg_segment_ptr()), "s"(blockIdx.x), "s"(wid)
    : "v0", "v1", "v2", "v3", "v4", "v5", "v6", "v7", "v8", "v9", "v10", "v11", "v12", "v13", "v14", "v15", "v16", "v17", "v18", "v19", "v20", "v21", "v22", "v23", "v24", "v25", "v26", "v27", "v28", "v29", "v30", "v31", "v32", "v33", "v34", "v35", "v36", "v37", "v38", "v39", "v40", "v41", "v42", "v43", "v44", "v45", "v46", "v47", "v48", "v49", "v50", "v51", "v52", "v53", "v54", "v55", "v56", "v57", "v58", "v59", "v60", "v61", "v62", "v63", "v64", "v65", "v66", "v67", "v68", "v69", "v70", "v71", "v72", "v73", "v74", "v75", "v76", "v77", "v78", "v79", "v80", "v81", "v82", "v83", "v84", "v85", "v86", "v87", "v88", "v89", "v90", "v91", "v92", "v93", "v94", "v95", "v96", "v97", "v98", "v99", "v100", "v101", "v102", "v103", "v104", "v105", "v106", "v107", "v108", "v109", "v110", "v111", "v112", "v113", "v114", "v115", "v116", "v117", "v118", "v119", "v120", "v121", "v122", "v123", "v124", "v125", "v126", "v127", "v128", "v129", "v130", "v131", "v132", "v133", "v134", "v135", "v136", "v137", "v138", "v139", "v140", "v141", "v142", "v143", "v144", "v145", "v146", "v147", "v148", "v149", "v150", "v151", "v152", "v153", "v154", "v155", "v156", "v157", "v158", "v159", "v160", "v161", "v162", "v163", "v164", "v165", "v166", "v167", "v168", "v169", "v170", "v171", "v172", "v173", "v174", "v175", "v176", "v177", "v178", "v179", "v180", "v181", "v182", "v183", "v184", "v185", "v186", "v187", "v188", "v189", "v190", "v191", "v192", "v193", "v194", "v195", "v196", "v197", "v198", "v199", "v200", "v201", "v202", "v203", "v204", "v205", "v206", "v207", "v208", "v209", "v210", "v211", "v212", "v213", "v214", "v215", "v216", "v217", "v218", "v219", "v220", "v221", "v222", "v223", "v224", "v225", "v226", "v227", "v228", "v229", "v230", "v231", "v232", "v233", "v234", "v235", "v236", "v237", "v238", "v239", "v240", "v241", "v242", "v243", "v244", "v245", "v246", "v247", "v248", "v249", "v250", "v251", "v252", "v253", "v254", "v255", "a0", "a1", "a2", "a3", "a4", "a5", "a6", "a7", "a8", "a9", "a10", "a11", "a12", "a13", "a14", "a15", "a16", "a17", "a18", "a19", "a20", "a21", "a22", "a23", "a24", "a25", "a26", "a27", "a28", "a29", "a30", "a31", "a32", "a33", "a34", "a35", "a36", "a37", "a38", "a39", "a40", "a41", "a42", "a43", "a44", "a45", "a46", "a47", "a48", "a49", "a50", "a51", "a52", "a53", "a54", "a55", "a56", "a57", "a58", "a59", "a60", "a61", "a62", "a63", "a64", "a65", "a66", "a67", "a68", "a69", "a70", "a71", "a72", "a73", "a74", "a75", "a76", "a77", "a78", "a79", "a80", "a81", "a82", "a83", "a84", "a85", "a86", "a87", "a88", "a89", "a90", "a91", "a92", "a93", "a94", "a95", "a96", "a97", "a98", "a99", "a100", "a101", "a102", "a103", "a104", "a105", "a106", "a107", "a108", "a109", "a110", "a111", "a112", "a113", "a114", "a115", "a116", "a117", "a118", "a119", "a120", "a121", "a122", "a123", "a124", "a125", "a126", "a127", "a128", "a129", "a130", "a131", "a132", "a133", "a134", "a135", "a136", "a137", "a138", "a139", "a140", "a141", "a142", "a143", "a144", "a145", "a146", "a147", "a148", "a149", "a150", "a151", "a152", "a153", "a154", "a155", "a156", "a157", "a158", "a159", "a160", "a161", "a162", "a163", "a164", "a165", "a166", "a167", "a168", "a169", "a170", "a171", "a172", "a173", "a174", "a175", "a176", "a177", "a178", "a179", "a180", "a181", "a182", "a183", "a184", "a185", "a186", "a187", "a188", "a189", "a190", "a191", "a192", "a193", "a194", "a195", "a196", "a197", "a198", "a199", "a200", "a201", "a202", "a203", "a204", "a205", "a206", "a207", "a208", "a209", "a210", "a211", "a212", "a213", "a214", "a215", "a216", "a217", "a218", "a219", "a220", "a221", "a222", "a223", "a224", "a225", "a226", "a227", "a228", "a229", "a230", "a231", "a232", "a233", "a234", "a235", "a236", "a237", "a238", "a239", "a240", "a241", "a242", "a243", "a244", "a245", "a246", "a247", "a248", "a249", "a250", "a251", "a252", "a253", "a254", "a255", "s8", "s9", "s10", "s11", "s12", "s13", "s14", "s15", "s16", "s17", "s18", "s19", "s20", "s21", "s22", "s23", "s24", "s25", "s26", "s27", "s28", "s29", "s30", "s31", "s32", "s33", "s34", "s35", "s36", "s37", "s38", "s39", "s40", "s41", "s42", "s43", "s44", "s45", "s46", "s47", "s48", "s49", "s50", "s51", "s52", "s53", "s54", "s55", "s56", "s57", "s58", "s59", "s60", "s61", "s62", "s63", "s64", "s65", "s66", "s67", "s68", "s69", "s70", "s71", "s72", "s73", "s74", "s75", "s76", "s77", "s78", "s79", "s80", "s81", "s82", "s83", "s84", "s85", "s86", "s87", "s88", "s89", "s90", "s91", "s92", "s93", "s94", "s95", "s96", "s97", "s98", "s99", "s100", "s101", "vcc", "scc", "memory");
}

extern "C" void kernel_launch(void* const* d_in, const int* in_sizes, int n_in, void* d_out, int out_size, void* d_ws, size_t ws_size,
                              hipStream_t stream) {
  (void)hipFuncSetAttribute((const void*)attn_fused, hipFuncAttributeMaxDynamicSharedMemorySize, 153088);
  attn_fused<<<256, 256, 153088, stream>>>(d_in[0], d_in[1], d_in[2], d_in[3], d_in[4], d_in[5], d_in[6], d_ws, d_out);
}
```

```cpp
#include <hip/hip_runtime.h>

__global__ __launch_bounds__(256, 1) void attn_fused(const void* q, const void* qk, const void* qv, const void* ks, const void* kz,
                                                     const void* vs, const void* vz, void* ws, void* out) {
  const int wid = __builtin_amdgcn_readfirstlane(threadIdx.x >> 6);
  asm volatile(
    "s_load_dwordx16 s[8:23], %0, 0x0\n"
    "s_mov_b32 s58, %2\n"
    "s_and_b32 s76, %1, 7\n"
    "s_lshr_b32 s77, %1, 3\n"
    "s_and_b32 s78, s77, 3\n"
    "s_lshl_b32 s56, s76, 2\n"
    "s_add_u32 s56, s56, s78\n"
    "s_lshr_b32 s57, s77, 2\n"
    "v_mbcnt_lo_u32_b32 v231, -1, 0\n"
    "v_mbcnt_hi_u32_b32 v231, -1, v231\n"
    "s_getpc_b64 s[44:45]\n"
    "LPC:\n"
    "s_mov_b32 s46, LSLOW-LPC\n"
    "s_mov_b32 s47, 0x20000\n"
    "s_lshr_b32 s76, %1, 3\n"
    "s_lshl_b32 s76, s76, 2\n"
    "s_add_u32 s76, s76, s58\n"
    "s_lshl_b32 s76, s76, 9\n"
    "v_lshlrev_b32 v196, 3, v231\n"
    "buffer_load_dword v251, v196, s[44:47], s76 offen\n"
    "s_mul_i32 s72, s58, 0xc00\n"
    "s_lshr_b32 s86, s57, 2\n"
    "s_mul_i32 s64, s86, 0xc000\n"
    "s_lshl_b32 s86, s86, 3\n"
    "s_and_b32 s79, s57, 3\n"
    "s_add_u32 s86, s86, s79\n"
    "s_lshl_b32 s86, s86, 2\n"
    "s_xor_b32 s76, s57, 4\n"
    "s_and_b32 s77, s76, 6\n"
    "s_mul_i32 s77, s77, 15\n"
    "s_add_u32 s76, s76, s77\n"
    "s_and_b32 s77, s58, 1\n"
    "s_mul_i32 s78, s77, 12\n"
    "s_add_u32 s78, s78, s86\n"
    "s_lshl_b32 s78, s78, 1\n"
    "s_mul_i32 s77, s77, 0xc0\n"
    "s_movk_i32 s90, 0x556\n"
    "s_lshl_b32 s60, s56, 12\n"
    "s_cmp_lt_u32 s58, 2\n"
    "s_cselect_b32 s76, s76, s78\n"
    "s_cmovk_i32 s90, 0xaab\n"
    "s_cselect_b32 s91, 24, 0\n"
    "s_cselect_b32 s92, 1, 10\n"
    "s_cselect_b32 s89, 7, 0\n"
    "s_cselect_b32 s64, s64, 0\n"
    "s_add_u32 s60, s60, s76\n"
    "s_mul_i32 s60, s60, 0x180\n"
    "s_mul_i32 s76, s76, 0x180\n"
    "s_sub_u32 s34, 0x180000, s76\n"
    "s_mov_b32 s35, 0x20000\n"
    "v_add_u32 v144, s77, v231\n"
    "v_add_u32 v145, 64, v144\n"
    "v_add_u32 v146, 64, v145\n"
    "v_mul_u32_u24 v197, s90, v144\n"
    "v_lshrrev_b32 v197, 16, v197\n"
    "v_mul_u32_u24 v198, s92, v197\n"
    "v_lshrrev_b32 v198, 1, v198\n"
    "v_and_b32 v198, s89, v198\n"
    "v_xor_b32 v198, v198, v144\n"
    "v_mul_u32_u24 v197, s91, v197\n"
    "v_add_lshl_u32 v202, v197, v198, 4\n"
    "v_mul_u32_u24 v197, s90, v145\n"
    "v_lshrrev_b32 v197, 16, v197\n"
    "v_mul_u32_u24 v198, s92, v197\n"
    "v_lshrrev_b32 v198, 1, v198\n"
    "v_and_b32 v198, s89, v198\n"
    "v_xor_b32 v198, v198, v144\n"
    "v_mul_u32_u24 v197, s91, v197\n"
    "v_add_lshl_u32 v203, v197, v198, 4\n"
    "v_mul_u32_u24 v197, s90, v146\n"
    "v_lshrrev_b32 v197, 16, v197\n"
    "v_mul_u32_u24 v198, s92, v197\n"
    "v_lshrrev_b32 v198, 1, v198\n"
    "v_and_b32 v198, s89, v198\n"
    "v_xor_b32 v198, v198, v144\n"
    "v_mul_u32_u24 v197, s91, v197\n"
    "v_add_lshl_u32 v204, v197, v198, 4\n"
    "s_waitcnt lgkmcnt(0)\n"
    "s_cmp_lt_u32 s58, 2\n"
    "s_cselect_b64 s[80:81], s[10:11], s[12:13]\n"
    "s_cselect_b64 s[82:83], s[16:17], s[14:15]\n"
    "s_add_u32 s32, s80, s60\n"
    "s_addc_u32 s33, s81, 0\n"
    "s_cmp_eq_u32 s64, 0\n"
    "s_cbranch_scc1 LNOR\n"
    "s_add_u32 m0, s72, 0x12000\n"
    "s_nop 0\n"
    "buffer_load_dwordx4 v202, s[32:35], 0 offen nt lds\n"
    "buffer_load_dwordx4 v203, s[32:35], 0 offen offset:1024 nt lds\n"
    "buffer_load_dwordx4 v204, s[32:35], 0 offen offset:2048 nt lds\n"
    "LNOR:\n"
    "s_add_u32 m0, s72, 0x15000\n"
    "s_nop 0\n"
    "buffer_load_dwordx4 v202, s[32:35], s64 offen nt lds\n"
    "buffer_load_dwordx4 v203, s[32:35], s64 offen offset:1024 nt lds\n"
    "buffer_load_dwordx4 v204, s[32:35], s64 offen offset:2048 nt lds\n"
    "LR0:\n"
    "s_load_dwordx2 s[24:25], %0, 0x40\n"
    "s_mul_i32 s71, s58, 0x1800\n"
    "s_lshr_b32 s87, s57, 2\n"
    "s_mul_i32 s87, s87, 0x6000\n"
    "s_mul_i32 s61, s56, 0x3000\n"
    "s_lshl_b32 s62, s56, 12\n"
    "s_lshl_b32 s59, s56, 12\n"
    "s_mov_b32 s50, 0x3000\n"
    "s_mov_b32 s51, 0x20000\n"
    "s_and_b32 s77, s57, 1\n"
    "s_mul_i32 s77, s77, 0xc0\n"
    "v_mul_u32_u24 v197, 0x1556, v144\n"
    "v_lshrrev_b32 v197, 16, v197\n"
    "v_mul_u32_u24 v196, 12, v197\n"
    "v_sub_u32 v196, v144, v196\n"
    "v_mul_u32_u24 v248, 0x180, v197\n"
    "v_lshl_add_u32 v248, v196, 4, v248\n"
    "v_add_u32 v248, s77, v248\n"
    "s_add_u32 s48, s82, s61\n"
    "s_addc_u32 s49, s83, 0\n"
    "s_add_u32 m0, s72, 0x21000\n"
    "s_nop 0\n"
    "buffer_load_dwordx4 v248, s[48:51], 0 offen lds\n"
    "s_add_u32 s40, s20, s62\n"
    "s_addc_u32 s41, s21, 0\n"
    "s_add_u32 s52, s18, s62\n"
    "s_addc_u32 s53, s19, 0\n"
    "s_mov_b32 s42, 0x1000\n"
    "s_mov_b32 s43, 0x20000\n"
    "s_mov_b32 s54, 0x1000\n"
    "s_mov_b32 s55, 0x20000\n"
    "v_lshlrev_b32 v196, 1, v231\n"
    "buffer_load_ushort v44, v196, s[40:43], 0 offen\n"
    "buffer_load_ushort v45, v196, s[52:55], 0 offen\n"
    "s_lshl_b32 s77, s58, 6\n"
    "v_add_u32 v41, s77, v231\n"
    "v_bfe_u32 v197, v231, 2, 1\n"
    "v_mul_u32_u24 v197, 12, v197\n"
    "v_and_b32 v198, 7, v231\n"
    "v_add3_u32 v197, v197, v198, s86\n"
    "v_lshrrev_b32 v40, 3, v41\n"
    "v_lshlrev_b32 v40, 7, v40\n"
    "v_lshl_add_u32 v40, v197, 1, v40\n"
    "buffer_load_ushort v42, v40, s[40:43], 0 offen\n"
    "buffer_load_ushort v43, v40, s[52:55], 0 offen\n"
    "s_add_u32 s44, s22, s59\n"
    "s_addc_u32 s45, s23, 0\n"
    "s_mov_b32 s46, 0x900\n"
    "s_mov_b32 s47, 0x20000\n"
    "v_mov_b32 v196, 0x40000000\n"
    "s_cmp_eq_u32 s58, 0\n"
    "s_cselect_b32 s77, 64, 0\n"
    "s_cselect_b32 s74, 1, 0\n"
    "v_and_b32 v213, 7, v231\n"
    "v_mul_u32_u24 v213, 0x100, v213\n"
    "v_cmp_gt_u32 vcc, s77, v231\n"
    "v_mov_b32 v197, 0x800\n"
    "s_mul_i32 s77, s57, 0x100\n"
    "v_cndmask_b32 v213, v196, v213, vcc\n"
    "v_cndmask_b32 v197, v196, v197, vcc\n"
    "buffer_load_dword v221, v197, s[44:47], 0 offen sc1\n"
    "v_cmp_gt_u32 vcc, s74, v231\n"
    "v_mov_b32 v214, s77\n"
    "s_mul_i32 s66, s74, 3\n"
    "v_cndmask_b32 v214, v196, v214, vcc\n"
    "s_add_u32 s78, s64, 0xc000\n"
    "s_add_u32 m0, s72, 0x1e000\n"
    "s_nop 0\n"
    "buffer_load_dwordx4 v202, s[32:35], s78 offen nt lds\n"
    "buffer_load_dwordx4 v203, s[32:35], s78 offen offset:1024 nt lds\n"
    "buffer_load_dwordx4 v204, s[32:35], s78 offen offset:2048 nt lds\n"
    "s_lshl_b32 s76, s56, 3\n"
    "s_add_u32 s76, s76, s57\n"
    "s_mul_i32 s76, s76, 0x18000\n"
    "s_add_u32 s28, s8, s76\n"
    "s_addc_u32 s29, s9, 0\n"
    "s_mov_b32 s30, 0x18000\n"
    "s_mov_b32 s31, 0x20000\n"
    "s_mul_i32 s88, s58, 0x6000\n"
    "v_mul_u32_u24 v197, 0xaab, v231\n"
    "v_lshrrev_b32 v197, 16, v197\n"
    "v_mul_u32_u24 v196, 24, v197\n"
    "v_sub_u32 v196, v231, v196\n"
    "v_mul_u32_u24 v152, 0x180, v197\n"
    "v_lshrrev_b32 v197, 1, v197\n"
    "v_xor_b32 v196, v196, v197\n"
    "v_lshl_add_u32 v152, v196, 4, v152\n"
    "v_add_u32 v198, 64, v231\n"
    "v_mul_u32_u24 v197, 0xaab, v198\n"
    "v_lshrrev_b32 v197, 16, v197\n"
    "v_mul_u32_u24 v196, 24, v197\n"
    "v_sub_u32 v196, v198, v196\n"
    "v_mul_u32_u24 v153, 0x180, v197\n"
    "v_lshrrev_b32 v197, 1, v197\n"
    "v_xor_b32 v196, v196, v197\n"
    "v_lshl_add_u32 v153, v196, 4, v153\n"
    "v_add_u32 v198, 0x80, v231\n"
    "v_mul_u32_u24 v197, 0xaab, v198\n"
    "v_lshrrev_b32 v197, 16, v197\n"
    "v_mul_u32_u24 v196, 24, v197\n"
    "v_sub_u32 v196, v198, v196\n"
    "v_mul_u32_u24 v154, 0x180, v197\n"
    "v_lshrrev_b32 v197, 1, v197\n"
    "v_xor_b32 v196, v196, v197\n"
    "v_lshl_add_u32 v154, v196, 4, v154\n"
    "s_mov_b32 s76, 0xc00\n"
    "v_xad_u32 v155, v152, 64, s76\n"
    "v_xad_u32 v156, v153, 64, s76\n"
    "v_xad_u32 v157, v154, 64, s76\n"
    "s_add_u32 s78, s88, 0\n"
    "s_add_u32 m0, s71, 0\n"
    "s_nop 0\n"
    "buffer_load_dwordx4 v152, s[28:31], s78 offen nt lds\n"
    "s_add_u32 s78, s88, 0\n"
    "s_add_u32 m0, s71, 0x400\n"
    "s_nop 0\n"
    "buffer_load_dwordx4 v153, s[28:31], s78 offen nt lds\n"
    "s_add_u32 s78, s88, 0\n"
    "s_add_u32 m0, s71, 0x800\n"
    "s_nop 0\n"
    "buffer_load_dwordx4 v154, s[28:31], s78 offen nt lds\n"
    "v_mov_b32 v196, 0x40000000\n"
    "v_and_b32 v46, 15, v231\n"
    "v_lshrrev_b32 v47, 4, v231\n"
    "s_mov_b32 s75, 0x3dd53b95\n"
    "v_min_u32 v199, 2, v47\n"
    "v_lshlrev_b32 v197, 3, v199\n"
    "s_lshl_b32 s76, s58, 1\n"
    "v_add_u32 v197, s76, v197\n"
    "v_lshrrev_b32 v198, 1, v46\n"
    "v_xor_b32 v197, v197, v198\n"
    "v_mul_u32_u24 v205, 0x180, v46\n"
    "v_lshl_add_u32 v205, v197, 4, v205\n"
    "v_add_u32 v205, 0x12000, v205\n"
    "v_xor_b32 v206, 16, v205\n"
    "v_and_b32 v197, 7, v231\n"
    "v_lshl_add_u32 v209, v199, 5, v197\n"
    "s_lshl_b32 s76, s58, 3\n"
    "v_add_lshl_u32 v209, v209, s76, 1\n"
    "v_add_u32 v217, 0x21000, v209\n"
    "s_lshr_b32 s76, s57, 2\n"
    "s_mul_i32 s76, s76, 0xc0\n"
    "s_add_u32 s76, s76, 0x20f40\n"
    "v_add_u32 v209, s76, v209\n"
    "v_lshrrev_b32 v198, 3, v231\n"
    "s_mul_i32 s76, s58, 12\n"
    "v_lshl_add_u32 v199, v198, 1, s76\n"
    "v_mul_u32_u24 v207, 5, v197\n"
    "v_and_b32 v207, 9, v207\n"
    "v_xor_b32 v199, v199, v207\n"
    "v_mul_u32_u24 v207, 0x300, v197\n"
    "v_lshl_add_u32 v207, v199, 4, v207\n"
    "v_add_u32 v207, 0x13800, v207\n"
    "v_xor_b32 v208, 16, v207\n"
    "s_mul_i32 s76, s58, 0xc0\n"
    "s_add_u32 s76, s76, 0x13800\n"
    "v_lshl_add_u32 v207, v231, 2, s76\n"
    "v_lshlrev_b32 v210, 3, v197\n"
    "v_add_u32 v210, 0x24400, v210\n"
    "s_mul_i32 s76, s58, 0x480\n"
    "s_add_u32 s76, s76, 0x24400\n"
    "v_mul_u32_u24 v215, 0x90, v197\n"
    "v_lshl_add_u32 v215, v198, 4, v215\n"
    "v_add_u32 v215, s76, v215\n"
    "v_and_b32 v197, 1, v197\n"
    "v_lshlrev_b32 v197, 3, v197\n"
    "v_sub_u32 v215, v215, v197\n"
    "v_bfe_u32 v197, v231, 2, 2\n"
    "v_mul_u32_u24 v216, 0x90, v197\n"
    "v_lshl_add_u32 v216, v47, 5, v216\n"
    "v_and_b32 v197, 1, v197\n"
    "v_lshlrev_b32 v197, 3, v197\n"
    "v_sub_u32 v216, v216, v197\n"
    "v_and_b32 v197, 3, v231\n"
    "v_lshl_add_u32 v216, v197, 3, v216\n"
    "v_add_u32 v216, s76, v216\n"
    "s_lshr_b32 s76, s57, 1\n"
    "s_xor_b32 s76, s76, 2\n"
    "s_mul_i32 s76, s76, 0x1800\n"
    "s_and_b32 s77, s57, 1\n"
    "s_mul_i32 s77, s77, 0xc00\n"
    "s_add_u32 s76, s76, s77\n"
    "s_lshl_b32 s77, s58, 8\n"
    "s_add_u32 s76, s76, s77\n"
    "v_lshlrev_b32 v197, 4, v46\n"
    "v_lshl_add_u32 v211, v47, 10, v197\n"
    "v_add_u32 v211, s76, v211\n"
    "s_lshr_b32 s76, s57, 2\n"
    "s_lshl_b32 s76, s76, 10\n"
    "s_lshl_b32 s77, s79, 8\n"
    "s_add_u32 s76, s76, s77\n"
    "s_mul_i32 s77, s58, 48\n"
    "v_add_u32 v198, s77, v231\n"
    "v_lshrrev_b32 v197, 5, v198\n"
    "v_lshl_add_u32 v212, v197, 12, s76\n"
    "v_bfe_u32 v197, v198, 2, 1\n"
    "v_lshl_add_u32 v212, v197, 11, v212\n"
    "v_bfe_u32 v197, v198, 3, 2\n"
    "v_lshl_add_u32 v212, v197, 6, v212\n"
    "v_cmp_gt_u32 vcc, 48, v231\n"
    "v_and_b32 v197, 3, v198\n"
    "v_lshl_add_u32 v212, v197, 4, v212\n"
    "v_cndmask_b32 v211, v196, v211, vcc\n"
    "v_cndmask_b32 v212, v196, v212, vcc\n"
    "v_mov_b32 v222, 0\n"
    "v_mov_b32 v223, 0\n"
    "v_mov_b32 v224, 0\n"
    "v_mov_b32 v225, 0\n"
    "v_mov_b32 v160, 0\n"
    "v_mov_b32 v161, 0\n"
    "v_mov_b32 v162, 0\n"
    "v_mov_b32 v163, 0\n"
    "v_mov_b32 v164, 0\n"
    "v_mov_b32 v165, 0\n"
    "v_mov_b32 v172, 0\n"
    "v_mov_b32 v173, 0\n"
    "v_mov_b32 v174, 0\n"
    "v_mov_b32 v175, 0\n"
    "v_lshlrev_b32 v41, 3, v41\n"
    "v_add_u32 v41, 0x24400, v41\n"
    "s_mul_i32 s76, s56, 0xc0000\n"
    "s_add_u32 s77, s76, 0x40000\n"
    "s_add_u32 s36, s22, s77\n"
    "s_addc_u32 s37, s23, 0\n"
    "s_mov_b32 s38, 0xc0000\n"
    "s_mov_b32 s39, 0x20000\n"
    "s_cmp_eq_u32 s87, 0\n"
    "s_cselect_b32 s62, 0x40000000, 0\n"
    "s_waitcnt vmcnt(10)\n"
    "v_lshlrev_b32 v44, 16, v44\n"
    "s_waitcnt vmcnt(9)\n"
    "v_lshlrev_b32 v45, 16, v45\n"
    "v_mul_f32 v197, v44, v45\n"
    "s_waitcnt vmcnt(8)\n"
    "v_lshlrev_b32 v42, 16, v42\n"
    "s_waitcnt vmcnt(7)\n"
    "v_lshlrev_b32 v43, 16, v43\n"
    "v_min_f32_dpp v197, v197, v197 row_ror:8 row_mask:0xf bank_mask:0xf\n"
    "s_mul_i32 s76, s56, 0xc0000\n"
    "s_add_u32 s77, s76, 0x1840000\n"
    "v_min_f32_dpp v197, v197, v197 row_ror:4 row_mask:0xf bank_mask:0xf\n"
    "s_add_u32 s40, s22, s77\n"
    "s_addc_u32 s41, s23, 0\n"
    "v_min_f32_dpp v197, v197, v197 row_ror:2 row_mask:0xf bank_mask:0xf\n"
    "s_mov_b32 s42, 0xc0000\n"
    "s_mov_b32 s43, 0x20000\n"
    "v_min_f32_dpp v197, v197, v197 row_ror:1 row_mask:0xf bank_mask:0xf\n"
    "v_mov_b32 v198, v197\n"
    "s_nop 1\n"
    "v_permlane32_swap_b32 v197, v198\n"
    "v_min_f32 v197, v197, v198\n"
    "v_mov_b32 v198, v197\n"
    "s_nop 1\n"
    "v_permlane16_swap_b32 v197, v198\n"
    "v_min_f32 v197, v197, v198\n"
    "v_rcp_f32 v198, v197\n"
    "v_subrev_u32 v199, 0x37800000, v197\n"
    "v_mul_f32 v197, 0x3fcccccd, v197\n"
    "v_cmp_gt_u32 vcc, 0x10000000, v199\n"
    "v_mul_f32 v198, 0x3f3425ed, v198\n"
    "v_mov_b32 v199, 0x3f800000\n"
    "v_cndmask_b32 v197, 0, v197, vcc\n"
    "v_cndmask_b32 v198, v199, v198, vcc\n"
    "v_readfirstlane_b32 s70, v197\n"
    "v_mul_f32 v197, v197, v198\n"
    "v_readfirstlane_b32 s68, v198\n"
    "v_readfirstlane_b32 s69, v197\n"
    "v_mul_f32 v44, v198, v43\n"
    "v_fma_f32 v45, -v42, v44, v197\n"
    "ds_write_b64 v41, v[44:45]\n"
    "s_waitcnt lgkmcnt(0)\n"
    "s_barrier\n"
    "ds_read_u16_d16_hi v172, v217 offset:6144\n"
    "ds_read_u16_d16_hi v173, v217\n"
    "ds_read_u16_d16_hi v174, v217 offset:6336\n"
    "ds_read_u16_d16_hi v175, v217 offset:192\n"
    "ds_read_u16_d16_hi v223, v209 offset:6144\n"
    "ds_read_u16_d16_hi v222, v209\n"
    "ds_read_b128 v[232:235], v205\n"
    "ds_read_b128 v[236:239], v206\n"
    "ds_read_u16_d16_hi v161, v209 offset:6336\n"
    "ds_read_u16_d16_hi v160, v209 offset:192\n"
    "ds_read_b128 v[64:67], v205 offset:12288\n"
    "ds_read_b128 v[68:71], v206 offset:12288\n"
    "s_waitcnt lgkmcnt(10)\n"
    "v_mul_f32 v197, v172, v173\n"
    "s_waitcnt lgkmcnt(8)\n"
    "v_mul_f32 v198, v174, v175\n"
    "v_min_f32 v197, v197, v198\n"
    "v_max_f32 v197, 0, v197\n"
    "v_mul_f32 v217, 0x3e3fe8d2, v197\n"
    "ds_read_b64 v[228:229], v210\n"
    "ds_read2st64_b32 v[240:241], v207 offset0:48 offset1:51\n"
    "ds_read2st64_b32 v[242:243], v207 offset0:54 offset1:57\n"
    "ds_read2st64_b32 v[244:245], v207 offset0:60 offset1:63\n"
    "ds_read2st64_b32 v[246:247], v207 offset0:66 offset1:69\n"
    "s_waitcnt lgkmcnt(12)\n"
    "v_mul_f32 v226, s75, v223\n"
    "s_waitcnt lgkmcnt(11)\n"
    "v_fma_f32 v227, -v222, v226, v217\n"
    "s_waitcnt lgkmcnt(10)\n"
    "v_cvt_f32_i32 v232, v232\n"
    "v_cvt_f32_i32 v233, v233\n"
    "v_cvt_f32_i32 v234, v234\n"
    "v_cvt_f32_i32 v235, v235\n"
    "s_waitcnt lgkmcnt(9)\n"
    "v_cvt_f32_i32 v236, v236\n"
    "v_cvt_f32_i32 v237, v237\n"
    "v_cvt_f32_i32 v238, v238\n"
    "v_cvt_f32_i32 v239, v239\n"
    "v_mul_f32_dpp v232, v226, v232 row_newbcast:0 row_mask:0xf bank_mask:0xf\n"
    "v_mul_f32_dpp v233, v226, v233 row_newbcast:1 row_mask:0xf bank_mask:0xf\n"
    "v_mul_f32_dpp v234, v226, v234 row_newbcast:2 row_mask:0xf bank_mask:0xf\n"
    "v_mul_f32_dpp v235, v226, v235 row_newbcast:3 row_mask:0xf bank_mask:0xf\n"
    "v_mul_f32_dpp v236, v226, v236 row_newbcast:4 row_mask:0xf bank_mask:0xf\n"
    "v_mul_f32_dpp v237, v226, v237 row_newbcast:5 row_mask:0xf bank_mask:0xf\n"
    "v_mul_f32_dpp v238, v226, v238 row_newbcast:6 row_mask:0xf bank_mask:0xf\n"
    "v_mul_f32_dpp v239, v226, v239 row_newbcast:7 row_mask:0xf bank_mask:0xf\n"
    "v_add_f32_dpp v232, v227, v232 row_newbcast:0 row_mask:0xf bank_mask:0xf\n"
    "v_add_f32_dpp v233, v227, v233 row_newbcast:1 row_mask:0xf bank_mask:0xf\n"
    "v_add_f32_dpp v234, v227, v234 row_newbcast:2 row_mask:0xf bank_mask:0xf\n"
    "v_add_f32_dpp v235, v227, v235 row_newbcast:3 row_mask:0xf bank_mask:0xf\n"
    "v_add_f32_dpp v236, v227, v236 row_newbcast:4 row_mask:0xf bank_mask:0xf\n"
    "v_add_f32_dpp v237, v227, v237 row_newbcast:5 row_mask:0xf bank_mask:0xf\n"
    "v_add_f32_dpp v238, v227, v238 row_newbcast:6 row_mask:0xf bank_mask:0xf\n"
    "v_add_f32_dpp v239, v227, v239 row_newbcast:7 row_mask:0xf bank_mask:0xf\n"
    "v_cvt_pk_bf16_f32 v232, v232, v233\n"
    "v_cvt_pk_bf16_f32 v233, v234, v235\n"
    "v_cvt_pk_bf16_f32 v234, v236, v237\n"
    "v_cvt_pk_bf16_f32 v235, v238, v239\n"
    "buffer_store_dwordx4 v[232:235], v211, s[36:39], s62 offen sc1\n"
    "s_waitcnt lgkmcnt(8)\n"
    "v_mul_f32 v166, s75, v161\n"
    "s_waitcnt lgkmcnt(7)\n"
    "v_fma_f32 v167, -v160, v166, v217\n"
    "s_waitcnt lgkmcnt(6)\n"
    "v_cvt_f32_i32 v64, v64\n"
    "v_cvt_f32_i32 v65, v65\n"
    "v_cvt_f32_i32 v66, v66\n"
    "v_cvt_f32_i32 v67, v67\n"
    "s_waitcnt lgkmcnt(5)\n"
    "v_cvt_f32_i32 v68, v68\n"
    "v_cvt_f32_i32 v69, v69\n"
    "v_cvt_f32_i32 v70, v70\n"
    "v_cvt_f32_i32 v71, v71\n"
    "v_mul_f32_dpp v64, v166, v64 row_newbcast:0 row_mask:0xf bank_mask:0xf\n"
    "v_mul_f32_dpp v65, v166, v65 row_newbcast:1 row_mask:0xf bank_mask:0xf\n"
    "v_mul_f32_dpp v66, v166, v66 row_newbcast:2 row_mask:0xf bank_mask:0xf\n"
    "v_mul_f32_dpp v67, v166, v67 row_newbcast:3 row_mask:0xf bank_mask:0xf\n"
    "v_mul_f32_dpp v68, v166, v68 row_newbcast:4 row_mask:0xf bank_mask:0xf\n"
    "v_mul_f32_dpp v69, v166, v69 row_newbcast:5 row_mask:0xf bank_mask:0xf\n"
    "v_mul_f32_dpp v70, v166, v70 row_newbcast:6 row_mask:0xf bank_mask:0xf\n"
    "v_mul_f32_dpp v71, v166, v71 row_newbcast:7 row_mask:0xf bank_mask:0xf\n"
    "v_add_f32_dpp v64, v167, v64 row_newbcast:0 row_mask:0xf bank_mask:0xf\n"
    "v_add_f32_dpp v65, v167, v65 row_newbcast:1 row_mask:0xf bank_mask:0xf\n"
    "v_add_f32_dpp v66, v167, v66 row_newbcast:2 row_mask:0xf bank_mask:0xf\n"
    "v_add_f32_dpp v67, v167, v67 row_newbcast:3 row_mask:0xf bank_mask:0xf\n"
    "v_add_f32_dpp v68, v167, v68 row_newbcast:4 row_mask:0xf bank_mask:0xf\n"
    "v_add_f32_dpp v69, v167, v69 row_newbcast:5 row_mask:0xf bank_mask:0xf\n"
    "v_add_f32_dpp v70, v167, v70 row_newbcast:6 row_mask:0xf bank_mask:0xf\n"
    "v_add_f32_dpp v71, v167, v71 row_newbcast:7 row_mask:0xf bank_mask:0xf\n"
    "v_cvt_pk_bf16_f32 v64, v64, v65\n"
    "v_cvt_pk_bf16_f32 v65, v66, v67\n"
    "v_cvt_pk_bf16_f32 v66, v68, v69\n"
    "v_cvt_pk_bf16_f32 v67, v70, v71\n"
    "s_add_u32 s78, s87, 0\n"
    "buffer_store_dwordx4 v[64:67], v211, s[36:39], s78 offen sc1\n"
    "s_waitcnt lgkmcnt(3)\n"
    "v_cvt_f32_i32 v240, v240\n"
    "v_cvt_f32_i32 v241, v241\n"
    "s_waitcnt lgkmcnt(2)\n"
    "v_cvt_f32_i32 v242, v242\n"
    "v_cvt_f32_i32 v243, v243\n"
    "s_waitcnt lgkmcnt(1)\n"
    "v_cvt_f32_i32 v244, v244\n"
    "v_cvt_f32_i32 v245, v245\n"
    "s_waitcnt lgkmcnt(0)\n"
    "v_cvt_f32_i32 v246, v246\n"
    "v_cvt_f32_i32 v247, v247\n"
    "v_mul_f32_dpp v240, v228, v240 row_newbcast:0 row_mask:0xf bank_mask:0xf\n"
    "v_mul_f32_dpp v241, v228, v241 row_newbcast:1 row_mask:0xf bank_mask:0xf\n"
    "v_mul_f32_dpp v242, v228, v242 row_newbcast:2 row_mask:0xf bank_mask:0xf\n"
    "v_mul_f32_dpp v243, v228, v243 row_newbcast:3 row_mask:0xf bank_mask:0xf\n"
    "v_mul_f32_dpp v244, v228, v244 row_newbcast:4 row_mask:0xf bank_mask:0xf\n"
    "v_mul_f32_dpp v245, v228, v245 row_newbcast:5 row_mask:0xf bank_mask:0xf\n"
    "v_mul_f32_dpp v246, v228, v246 row_newbcast:6 row_mask:0xf bank_mask:0xf\n"
    "v_mul_f32_dpp v247, v228, v247 row_newbcast:7 row_mask:0xf bank_mask:0xf\n"
    "v_add_f32_dpp v240, v229, v240 row_newbcast:0 row_mask:0xf bank_mask:0xf\n"
    "v_add_f32_dpp v241, v229, v241 row_newbcast:1 row_mask:0xf bank_mask:0xf\n"
    "v_add_f32_dpp v242, v229, v242 row_newbcast:2 row_mask:0xf bank_mask:0xf\n"
    "v_add_f32_dpp v243, v229, v243 row_newbcast:3 row_mask:0xf bank_mask:0xf\n"
    "v_add_f32_dpp v244, v229, v244 row_newbcast:4 row_mask:0xf bank_mask:0xf\n"
    "v_add_f32_dpp v245, v229, v245 row_newbcast:5 row_mask:0xf bank_mask:0xf\n"
    "v_add_f32_dpp v246, v229, v246 row_newbcast:6 row_mask:0xf bank_mask:0xf\n"
    "v_add_f32_dpp v247, v229, v247 row_newbcast:7 row_mask:0xf bank_mask:0xf\n"
    "v_cvt_pk_bf16_f32 v240, v240, v241\n"
    "v_cvt_pk_bf16_f32 v241, v242, v243\n"
    "v_cvt_pk_bf16_f32 v242, v244, v245\n"
    "v_cvt_pk_bf16_f32 v243, v246, v247\n"
    "s_waitcnt vmcnt(8)\n"
    "v_add_u32 v221, s74, v221\n"
    "s_mov_b32 s73, 0\n"
    "v_readfirstlane_b32 s67, v221\n"
    "s_mov_b32 s78, 0\n"
    "buffer_store_dwordx4 v[240:243], v212, s[40:43], s78 offen sc1\n"
    "s_add_u32 s78, s88, 0\n"
    "s_add_u32 m0, s71, 0xc00\n"
    "s_nop 0\n"
    "buffer_load_dwordx4 v155, s[28:31], s78 offen nt lds\n"
    "s_add_u32 s78, s88, 0\n"
    "s_add_u32 m0, s71, 0x1000\n"
    "s_nop 0\n"
    "buffer_load_dwordx4 v156, s[28:31], s78 offen nt lds\n"
    "s_add_u32 s78, s88, 0\n"
    "s_add_u32 m0, s71, 0x1400\n"
    "s_nop 0\n"
    "buffer_load_dwordx4 v157, s[28:31], s78 offen nt lds\n"
    "s_add_u32 s78, s88, 0x1800\n"
    "s_add_u32 m0, s71, 0x6000\n"
    "s_nop 0\n"
    "buffer_load_dwordx4 v152, s[28:31], s78 offen nt lds\n"
    "v_lshlrev_b32 v200, 4, v231\n"
    "v_add_u32 v201, 0xc000, v200\n"
    "v_mul_u32_u24 v198, 0x180, v46\n"
    "v_add_u32 v198, s71, v198\n"
    "v_lshrrev_b32 v197, 1, v46\n"
    "v_xor_b32 v197, v197, v47\n"
    "v_lshl_add_u32 v158, v197, 4, v198\n"
    "v_xor_b32 v159, 64, v158\n"
    "v_and_b32 v197, 3, v231\n"
    "v_cmp_eq_u32 vcc, 0, v197\n"
    "v_mov_b32 v198, 0x3f803f80\n"
    "s_nop 0\n"
    "v_cndmask_b32 v192, 0, v198, vcc\n"
    "v_cndmask_b32 v193, 0, v198, vcc\n"
    "v_cndmask_b32 v194, 0, v198, vcc\n"
    "v_cndmask_b32 v195, 0, v198, vcc\n"
    "s_and_b32 s77, s57, 1\n"
    "s_mul_i32 s77, s77, 0xc0\n"
    "s_sub_u32 s77, s77, 0x400\n"
    "v_mul_u32_u24 v197, 0x1556, v145\n"
    "v_lshrrev_b32 v197, 16, v197\n"
    "v_mul_u32_u24 v196, 12, v197\n"
    "v_sub_u32 v196, v145, v196\n"
    "v_mul_u32_u24 v249, 0x180, v197\n"
    "v_lshl_add_u32 v249, v196, 4, v249\n"
    "v_add_u32 v249, s77, v249\n"
    "s_and_b32 s77, s57, 1\n"
    "s_mul_i32 s77, s77, 0xc0\n"
    "s_sub_u32 s77, s77, 0x800\n"
    "v_mul_u32_u24 v197, 0x1556, v146\n"
    "v_lshrrev_b32 v197, 16, v197\n"
    "v_mul_u32_u24 v196, 12, v197\n"
    "v_sub_u32 v196, v146, v196\n"
    "v_mul_u32_u24 v250, 0x180, v197\n"
    "v_lshl_add_u32 v250, v196, 4, v250\n"
    "v_add_u32 v250, s77, v250\n"
    "s_mul_i32 s66, s74, 2\n"
    "s_waitcnt vmcnt(4)\n"
    "s_barrier\n"
    "v_mov_b32 v220, 2\n"
    "buffer_store_dwordx2 v[220:221], v214, s[44:47], 0 offen sc1\n"
    "s_add_u32 s78, s64, 0x18000\n"
    "s_add_u32 m0, s72, 0x1b000\n"
    "s_nop 0\n"
    "buffer_load_dwordx4 v202, s[32:35], s78 offen nt lds\n"
    "buffer_load_dwordx4 v203, s[32:35], s78 offen offset:1024 nt lds\n"
    "buffer_load_dwordx4 v204, s[32:35], s78 offen offset:2048 nt lds\n"
    "ds_read_u16_d16_hi v163, v209 offset:6528\n"
    "ds_read_u16_d16_hi v162, v209 offset:384\n"
    "ds_read_b128 v[72:75], v205 offset:49152\n"
    "ds_read_b128 v[76:79], v206 offset:49152\n"
    "ds_read_b64 v[170:171], v210 offset:64\n"
    "ds_read2st64_b32 v[80:81], v207 offset0:192 offset1:195\n"
    "ds_read2st64_b32 v[82:83], v207 offset0:198 offset1:201\n"
    "ds_read2st64_b32 v[84:85], v207 offset0:204 offset1:207\n"
    "ds_read2st64_b32 v[86:87], v207 offset0:210 offset1:213\n"
    "s_waitcnt lgkmcnt(8)\n"
    "v_mul_f32 v168, s75, v163\n"
    "s_waitcnt lgkmcnt(7)\n"
    "v_fma_f32 v169, -v162, v168, v217\n"
    "s_waitcnt lgkmcnt(6)\n"
    "v_cvt_f32_i32 v72, v72\n"
    "v_cvt_f32_i32 v73, v73\n"
    "v_cvt_f32_i32 v74, v74\n"
    "v_cvt_f32_i32 v75, v75\n"
    "s_waitcnt lgkmcnt(5)\n"
    "v_cvt_f32_i32 v76, v76\n"
    "v_cvt_f32_i32 v77, v77\n"
    "v_cvt_f32_i32 v78, v78\n"
    "v_cvt_f32_i32 v79, v79\n"
    "v_mul_f32_dpp v72, v168, v72 row_newbcast:0 row_mask:0xf bank_mask:0xf\n"
    "v_mul_f32_dpp v73, v168, v73 row_newbcast:1 row_mask:0xf bank_mask:0xf\n"
    "v_mul_f32_dpp v74, v168, v74 row_newbcast:2 row_mask:0xf bank_mask:0xf\n"
    "v_mul_f32_dpp v75, v168, v75 row_newbcast:3 row_mask:0xf bank_mask:0xf\n"
    "v_mul_f32_dpp v76, v168, v76 row_newbcast:4 row_mask:0xf bank_mask:0xf\n"
    "v_mul_f32_dpp v77, v168, v77 row_newbcast:5 row_mask:0xf bank_mask:0xf\n"
    "v_mul_f32_dpp v78, v168, v78 row_newbcast:6 row_mask:0xf bank_mask:0xf\n"
    "v_mul_f32_dpp v79, v168, v79 row_newbcast:7 row_mask:0xf bank_mask:0xf\n"
    "v_add_f32_dpp v72, v169, v72 row_newbcast:0 row_mask:0xf bank_mask:0xf\n"
    "v_add_f32_dpp v73, v169, v73 row_newbcast:1 row_mask:0xf bank_mask:0xf\n"
    "v_add_f32_dpp v74, v169, v74 row_newbcast:2 row_mask:0xf bank_mask:0xf\n"
    "v_add_f32_dpp v75, v169, v75 row_newbcast:3 row_mask:0xf bank_mask:0xf\n"
    "v_add_f32_dpp v76, v169, v76 row_newbcast:4 row_mask:0xf bank_mask:0xf\n"
    "v_add_f32_dpp v77, v169, v77 row_newbcast:5 row_mask:0xf bank_mask:0xf\n"
    "v_add_f32_dpp v78, v169, v78 row_newbcast:6 row_mask:0xf bank_mask:0xf\n"
    "v_add_f32_dpp v79, v169, v79 row_newbcast:7 row_mask:0xf bank_mask:0xf\n"
    "v_cvt_pk_bf16_f32 v72, v72, v73\n"
    "v_cvt_pk_bf16_f32 v73, v74, v75\n"
    "v_cvt_pk_bf16_f32 v74, v76, v77\n"
    "v_cvt_pk_bf16_f32 v75, v78, v79\n"
    "s_waitcnt lgkmcnt(3)\n"
    "v_cvt_f32_i32 v80, v80\n"
    "v_cvt_f32_i32 v81, v81\n"
    "s_waitcnt lgkmcnt(2)\n"
    "v_cvt_f32_i32 v82, v82\n"
    "v_cvt_f32_i32 v83, v83\n"
    "s_waitcnt lgkmcnt(1)\n"
    "v_cvt_f32_i32 v84, v84\n"
    "v_cvt_f32_i32 v85, v85\n"
    "s_waitcnt lgkmcnt(0)\n"
    "v_cvt_f32_i32 v86, v86\n"
    "v_cvt_f32_i32 v87, v87\n"
    "v_mul_f32_dpp v80, v170, v80 row_newbcast:0 row_mask:0xf bank_mask:0xf\n"
    "v_mul_f32_dpp v81, v170, v81 row_newbcast:1 row_mask:0xf bank_mask:0xf\n"
    "v_mul_f32_dpp v82, v170, v82 row_newbcast:2 row_mask:0xf bank_mask:0xf\n"
    "v_mul_f32_dpp v83, v170, v83 row_newbcast:3 row_mask:0xf bank_mask:0xf\n"
    "v_mul_f32_dpp v84, v170, v84 row_newbcast:4 row_mask:0xf bank_mask:0xf\n"
    "v_mul_f32_dpp v85, v170, v85 row_newbcast:5 row_mask:0xf bank_mask:0xf\n"
    "v_mul_f32_dpp v86, v170, v86 row_newbcast:6 row_mask:0xf bank_mask:0xf\n"
    "v_mul_f32_dpp v87, v170, v87 row_newbcast:7 row_mask:0xf bank_mask:0xf\n"
    "v_add_f32_dpp v80, v171, v80 row_newbcast:0 row_mask:0xf bank_mask:0xf\n"
    "v_add_f32_dpp v81, v171, v81 row_newbcast:1 row_mask:0xf bank_mask:0xf\n"
    "v_add_f32_dpp v82, v171, v82 row_newbcast:2 row_mask:0xf bank_mask:0xf\n"
    "v_add_f32_dpp v83, v171, v83 row_newbcast:3 row_mask:0xf bank_mask:0xf\n"
    "v_add_f32_dpp v84, v171, v84 row_newbcast:4 row_mask:0xf bank_mask:0xf\n"
    "v_add_f32_dpp v85, v171, v85 row_newbcast:5 row_mask:0xf bank_mask:0xf\n"
    "v_add_f32_dpp v86, v171, v86 row_newbcast:6 row_mask:0xf bank_mask:0xf\n"
    "v_add_f32_dpp v87, v171, v87 row_newbcast:7 row_mask:0xf bank_mask:0xf\n"
    "v_cvt_pk_bf16_f32 v80, v80, v81\n"
    "v_cvt_pk_bf16_f32 v81, v82, v83\n"
    "v_cvt_pk_bf16_f32 v82, v84, v85\n"
    "v_cvt_pk_bf16_f32 v83, v86, v87\n"
    "buffer_load_dwordx2 v[218:219], v213, s[44:47], 0 offen sc1\n"
    "s_add_u32 s78, s87, 0x6000\n"
    "buffer_store_dwordx4 v[72:75], v211, s[36:39], s78 offen sc1\n"
    "s_mov_b32 s78, 0x6000\n"
    "buffer_store_dwordx4 v[80:83], v212, s[40:43], s78 offen sc1\n"
    "s_add_u32 s78, s88, 0x1800\n"
    "s_add_u32 m0, s71, 0x6400\n"
    "s_nop 0\n"
    "buffer_load_dwordx4 v153, s[28:31], s78 offen nt lds\n"
    "s_add_u32 s78, s88, 0x1800\n"
    "s_add_u32 m0, s71, 0x6800\n"
    "s_nop 0\n"
    "buffer_load_dwordx4 v154, s[28:31], s78 offen nt lds\n"
    "s_add_u32 s78, s88, 0x1800\n"
    "s_add_u32 m0, s71, 0x6c00\n"
    "s_nop 0\n"
    "buffer_load_dwordx4 v155, s[28:31], s78 offen nt lds\n"
    "s_add_u32 s78, s88, 0x1800\n"
    "s_add_u32 m0, s71, 0x7000\n"
    "s_nop 0\n"
    "buffer_load_dwordx4 v156, s[28:31], s78 offen nt lds\n"
    "s_add_u32 s78, s88, 0x1800\n"
    "s_add_u32 m0, s71, 0x7400\n"
    "s_nop 0\n"
    "buffer_load_dwordx4 v157, s[28:31], s78 offen nt lds\n"
    "s_waitcnt vmcnt(13)\n"
    "ds_read_b128 a[192:195], v158\n"
    "ds_read_b128 a[196:199], v159\n"
    "ds_read_b128 a[200:203], v158 offset:128\n"
    "ds_read_b128 a[204:207], v159 offset:128\n"
    "ds_read_b128 a[208:211], v158 offset:256\n"
    "ds_read_b128 a[212:215], v159 offset:256\n"
    "s_waitcnt vmcnt(7)\n"
    "v_cmp_ne_u32 s[80:81], s67, v219\n"
    "v_cmp_gt_u32 vcc, s66, v218\n"
    "s_or_b64 vcc, vcc, s[80:81]\n"
    "s_cbranch_vccnz LSPRE0\n"
    "LSPB0:\n"
    "s_waitcnt vmcnt(5)\n"
    "s_waitcnt lgkmcnt(0)\n"
    "s_barrier\n"
    "v_mov_b32 v220, 3\n"
    "buffer_store_dwordx2 v[220:221], v214, s[44:47], 0 offen sc1\n"
    "s_add_u32 m0, s72, 0x1e000\n"
    "s_nop 0\n"
    "buffer_load_dwordx4 v200, s[36:39], s72 offen sc1 lds\n"
    "s_add_u32 m0, s72, 0x1e000\n"
    "s_nop 0\n"
    "buffer_load_dwordx4 v200, s[36:39], s72 offen offset:1024 sc1 lds\n"
    "s_add_u32 m0, s72, 0x1e000\n"
    "s_nop 0\n"
    "buffer_load_dwordx4 v200, s[36:39], s72 offen offset:2048 sc1 lds\n"
    "s_add_u32 s65, s71, 0x3000\n"
    "s_add_u32 m0, s71, 0\n"
    "s_nop 0\n"
    "buffer_load_dwordx4 v200, s[36:39], s65 offen sc1 lds\n"
    "s_add_u32 s65, s71, 0x3400\n"
    "s_add_u32 m0, s71, 0x400\n"
    "s_nop 0\n"
    "buffer_load_dwordx4 v200, s[36:39], s65 offen sc1 lds\n"
    "s_add_u32 s65, s71, 0x3800\n"
    "s_add_u32 m0, s71, 0x800\n"
    "s_nop 0\n"
    "buffer_load_dwordx4 v200, s[36:39], s65 offen sc1 lds\n"
    "s_add_u32 s65, s71, 0x3c00\n"
    "s_add_u32 m0, s71, 0xc00\n"
    "s_nop 0\n"
    "buffer_load_dwordx4 v200, s[36:39], s65 offen sc1 lds\n"
    "s_add_u32 s65, s71, 0x4000\n"
    "s_add_u32 m0, s71, 0x1000\n"
    "s_nop 0\n"
    "buffer_load_dwordx4 v200, s[36:39], s65 offen sc1 lds\n"
    "s_add_u32 s65, s71, 0x4400\n"
    "s_add_u32 m0, s71, 0x1400\n"
    "s_nop 0\n"
    "buffer_load_dwordx4 v200, s[36:39], s65 offen sc1 lds\n"
    "s_add_u32 s65, s71, 0\n"
    "s_add_u32 m0, s71, 0x12000\n"
    "s_nop 0\n"
    "buffer_load_dwordx4 v200, s[40:43], s65 offen sc1 lds\n"
    "s_add_u32 s65, s71, 0x400\n"
    "s_add_u32 m0, s71, 0x12400\n"
    "s_nop 0\n"
    "buffer_load_dwordx4 v200, s[40:43], s65 offen sc1 lds\n"
    "s_add_u32 s65, s71, 0x800\n"
    "s_add_u32 m0, s71, 0x12800\n"
    "s_nop 0\n"
    "buffer_load_dwordx4 v200, s[40:43], s65 offen sc1 lds\n"
    "s_add_u32 s65, s71, 0xc00\n"
    "s_add_u32 m0, s71, 0x12c00\n"
    "s_nop 0\n"
    "buffer_load_dwordx4 v200, s[40:43], s65 offen sc1 lds\n"
    "s_add_u32 s65, s71, 0x1000\n"
    "s_add_u32 m0, s71, 0x13000\n"
    "s_nop 0\n"
    "buffer_load_dwordx4 v200, s[40:43], s65 offen sc1 lds\n"
    "s_add_u32 s65, s71, 0x1400\n"
    "s_add_u32 m0, s71, 0x13400\n"
    "s_nop 0\n"
    "buffer_load_dwordx4 v200, s[40:43], s65 offen sc1 lds\n"
    "s_add_u32 s78, s64, 0x24000\n"
    "s_add_u32 m0, s72, 0x18000\n"
    "s_nop 0\n"
    "buffer_load_dwordx4 v202, s[32:35], s78 offen nt lds\n"
    "buffer_load_dwordx4 v203, s[32:35], s78 offen offset:1024 nt lds\n"
    "buffer_load_dwordx4 v204, s[32:35], s78 offen offset:2048 nt lds\n"
    "s_add_u32 s78, s88, 0x3000\n"
    "s_add_u32 m0, s71, 0xc000\n"
    "s_nop 0\n"
    "buffer_load_dwordx4 v152, s[28:31], s78 offen nt lds\n"
    "s_add_u32 s78, s88, 0x3000\n"
    "s_add_u32 m0, s71, 0xc400\n"
    "s_nop 0\n"
    "buffer_load_dwordx4 v153, s[28:31], s78 offen nt lds\n"
    "s_add_u32 s78, s88, 0x3000\n"
    "s_add_u32 m0, s71, 0xc800\n"
    "s_nop 0\n"
    "buffer_load_dwordx4 v154, s[28:31], s78 offen nt lds\n"
    "s_mul_i32 s66, s74, 3\n"
    "buffer_load_dwordx2 v[218:219], v213, s[44:47], 0 offen sc1\n"
    "s_waitcnt vmcnt(23)\n"
    "ds_read_b128 a[216:219], v158 offset:24576\n"
    "ds_read_b128 a[220:223], v159 offset:24576\n"
    "ds_read_b128 a[224:227], v158 offset:24704\n"
    "ds_read_b128 a[228:231], v159 offset:24704\n"
    "ds_read_b128 a[232:235], v158 offset:24832\n"
    "ds_read_b128 a[236:239], v159 offset:24832\n"
    "s_add_u32 m0, s72, 0x21000\n"
    "s_nop 0\n"
    "buffer_load_dwordx4 v249, s[48:51], 0 offen offset:1024 lds\n"
    "v_add_u32 v255, 0x1e000, v200\n"
    "s_waitcnt vmcnt(14) lgkmcnt(0)\n"
    "s_add_u32 s60, s71, 0x9000\n"
    "s_add_u32 s61, s71, 0x6000\n"
    "s_add_u32 s62, s87, 0x12000\n"
    "s_mov_b32 s63, 0x12000\n"
    "s_mov_b32 s59, 0\n"
    "s_mul_i32 s66, s74, 3\n"
    "s_mul_i32 s90, s58, 0x1c00\n"
    "s_sub_u32 s90, s90, 0x1000\n"
    "s_sub_u32 s92, s90, s71\n"
    "s_barrier\n"
    "ds_read_b128 v[160:163], v255\n"
    "ds_read_b128 v[164:167], v255 offset:1024\n"
    "ds_read_b128 v[168:171], v255 offset:2048\n"
    "ds_read_b128 v[172:175], v255 offset:3072\n"
    "ds_read_b128 v[176:179], v255 offset:4096\n"
    "ds_read_b128 v[180:183], v255 offset:5120\n"
    "s_add_u32 s76, s87, 0xc000\n"
    "s_mov_b32 s77, 0xc000\n"
    "s_waitcnt lgkmcnt(4)\n"
    "v_mfma_f32_16x16x32_bf16 v[40:43], v[160:163], a[192:195], 0\n"
    "ds_read_b128 v[184:187], v255 offset:6144\n"
    "ds_read_b64 v[228:229], v210 offset:128\n"
    "v_mfma_f32_16x16x32_bf16 v[40:43], v[164:167], a[196:199], v[40:43]\n"
    "s_add_u32 s78, s88, 0x3000\n"
    "ds_read2st64_b32 v[240:241], v207 offset0:144 offset1:147\n"
    "ds_read2st64_b32 v[242:243], v207 offset0:150 offset1:153\n"
    "v_mfma_f32_16x16x32_bf16 v[44:47], v[160:163], a[216:219], 0\n"
    "s_add_u32 m0, s71, 0xcc00\n"
    "ds_read_b128 v[188:191], v255 offset:7168\n"
    "ds_read2st64_b32 v[244:245], v207 offset0:156 offset1:159\n"
    "v_mfma_f32_16x16x32_bf16 v[44:47], v[164:167], a[220:223], v[44:47]\n"
    "s_waitcnt lgkmcnt(8)\n"
    "buffer_load_dwordx4 v155, s[28:31], s78 offen nt lds\n"
    "v_mfma_f32_16x16x32_bf16 v[40:43], v[168:171], a[200:203], v[40:43]\n"
    "ds_read_b128 v[160:163], v255 offset:8192\n"
    "ds_read2st64_b32 v[246:247], v207 offset0:162 offset1:165\n"
    "v_mfma_f32_16x16x32_bf16 v[40:43], v[172:175], a[204:207], v[40:43]\n"
    "ds_read_u16_d16_hi v223, v209 offset:6720\n"
    "ds_read_u16_d16_hi v222, v209 offset:576\n"
    "v_mfma_f32_16x16x32_bf16 v[44:47], v[168:171], a[224:227], v[44:47]\n"
    "ds_read_b128 v[164:167], v255 offset:9216\n"
    "ds_read_b128 v[232:235], v205 offset:36864\n"
    "v_mfma_f32_16x16x32_bf16 v[44:47], v[172:175], a[228:231], v[44:47]\n"
    "s_waitcnt lgkmcnt(12)\n"
    "ds_read_b128 v[236:239], v206 offset:36864\n"
    "v_mfma_f32_16x16x32_bf16 v[40:43], v[176:179], a[208:211], v[40:43]\n"
    "ds_read_b128 v[168:171], v255 offset:10240\n"
    "v_mfma_f32_16x16x32_bf16 v[40:43], v[180:183], a[212:215], v[40:43]\n"
    "v_mfma_f32_16x16x32_bf16 v[44:47], v[176:179], a[232:235], v[44:47]\n"
    "ds_read_b128 v[172:175], v255 offset:11264\n"
    "v_mfma_f32_16x16x32_bf16 v[44:47], v[180:183], a[236:239], v[44:47]\n"
    "s_waitcnt lgkmcnt(10)\n"
    "v_cvt_f32_i32 v240, v240\n"
    "v_cvt_f32_i32 v241, v241\n"
    "v_mfma_f32_16x16x32_bf16 v[56:59], v[184:187], a[192:195], 0\n"
    "ds_read_b128 v[176:179], v200\n"
    "v_cvt_f32_i32 v242, v242\n"
    "v_mfma_f32_16x16x32_bf16 v[56:59], v[188:191], a[196:199], v[56:59]\n"
    "s_add_u32 s78, s88, 0x3000\n"
    "v_cvt_f32_i32 v243, v243\n"
    "s_waitcnt lgkmcnt(10)\n"
    "v_cvt_f32_i32 v244, v244\n"
    "v_mfma_f32_16x16x32_bf16 v[60:63], v[184:187], a[216:219], 0\n"
    "s_add_u32 m0, s71, 0xd000\n"
    "ds_read_b128 v[180:183], v200 offset:1024\n"
    "v_cvt_f32_i32 v245, v245\n"
    "v_mfma_f32_16x16x32_bf16 v[60:63], v[188:191], a[220:223], v[60:63]\n"
    "s_waitcnt lgkmcnt(6)\n"
    "buffer_load_dwordx4 v156, s[28:31], s78 offen nt lds\n"
    "v_mfma_f32_16x16x32_bf16 v[56:59], v[160:163], a[200:203], v[56:59]\n"
    "ds_read_b128 v[184:187], v200 offset:2048\n"
    "v_cvt_f32_i32 v246, v246\n"
    "v_mfma_f32_16x16x32_bf16 v[56:59], v[164:167], a[204:207], v[56:59]\n"
    "v_mul_f32 v226, s75, v223\n"
    "v_cvt_f32_i32 v247, v247\n"
    "v_mfma_f32_16x16x32_bf16 v[60:63], v[160:163], a[224:227], v[60:63]\n"
    "ds_read_b128 v[188:191], v200 offset:3072\n"
    "v_fma_f32 v227, -v222, v226, v217\n"
    "v_mfma_f32_16x16x32_bf16 v[60:63], v[164:167], a[228:231], v[60:63]\n"
    "s_waitcnt lgkmcnt(4)\n"
    "v_mul_f32_dpp v240, v228, v240 row_newbcast:0 row_mask:0xf bank_mask:0xf\n"
    "v_cvt_f32_i32 v232, v232\n"
    "v_mfma_f32_16x16x32_bf16 v[56:59], v[168:171], a[208:211], v[56:59]\n"
    "ds_read_b128 v[160:163], v200 offset:4096\n"
    "v_mul_f32_dpp v241, v228, v241 row_newbcast:1 row_mask:0xf bank_mask:0xf\n"
    "v_mfma_f32_16x16x32_bf16 v[56:59], v[172:175], a[212:215], v[56:59]\n"
    "v_cvt_f32_i32 v233, v233\n"
    "v_mul_f32_dpp v242, v228, v242 row_newbcast:2 row_mask:0xf bank_mask:0xf\n"
    "v_mfma_f32_16x16x32_bf16 v[60:63], v[168:171], a[232:235], v[60:63]\n"
    "ds_read_b128 v[164:167], v200 offset:5120\n"
    "v_cvt_f32_i32 v234, v234\n"
    "v_mfma_f32_16x16x32_bf16 v[60:63], v[172:175], a[236:239], v[60:63]\n"
    "s_waitcnt lgkmcnt(4)\n"
    "v_mul_f32_dpp v243, v228, v243 row_newbcast:3 row_mask:0xf bank_mask:0xf\n"
    "v_cvt_f32_i32 v235, v235\n"
    "v_mfma_f32_16x16x32_bf16 v[64:67], v[176:179], a[192:195], 0\n"
    "ds_read_b128 v[168:171], v200 offset:6144\n"
    "v_mul_f32_dpp v244, v228, v244 row_newbcast:4 row_mask:0xf bank_mask:0xf\n"
    "v_cvt_f32_i32 v236, v236\n"
    "v_mul_f32_dpp v245, v228, v245 row_newbcast:5 row_mask:0xf bank_mask:0xf\n"
    "v_mfma_f32_16x16x32_bf16 v[64:67], v[180:183], a[196:199], v[64:67]\n"
    "s_add_u32 s78, s88, 0x3000\n"
    "v_cvt_f32_i32 v237, v237\n"
    "v_mul_f32_dpp v246, v228, v246 row_newbcast:6 row_mask:0xf bank_mask:0xf\n"
    "v_cvt_f32_i32 v238, v238\n"
    "v_mul_f32_dpp v247, v228, v247 row_newbcast:7 row_mask:0xf bank_mask:0xf\n"
    "v_mfma_f32_16x16x32_bf16 v[68:71], v[176:179], a[216:219], 0\n"
    "s_add_u32 m0, s71, 0xd400\n"
    "ds_read_b128 v[172:175], v200 offset:7168\n"
    "v_cvt_f32_i32 v239, v239\n"
    "v_add_f32_dpp v240, v229, v240 row_newbcast:0 row_mask:0xf bank_mask:0xf\n"
    "v_mul_f32_dpp v232, v226, v232 row_newbcast:0 row_mask:0xf bank_mask:0xf\n"
    "v_mfma_f32_16x16x32_bf16 v[68:71], v[180:183], a[220:223], v[68:71]\n"
    "s_waitcnt lgkmcnt(4)\n"
    "buffer_load_dwordx4 v157, s[28:31], s78 offen nt lds\n"
    "v_add_f32_dpp v241, v229, v241 row_newbcast:1 row_mask:0xf bank_mask:0xf\n"
    "v_mul_f32_dpp v233, v226, v233 row_newbcast:1 row_mask:0xf bank_mask:0xf\n"
    "v_mfma_f32_16x16x32_bf16 v[64:67], v[184:187], a[200:203], v[64:67]\n"
    "ds_read_b128 v[176:179], v200 offset:8192\n"
    "v_add_f32_dpp v242, v229, v242 row_newbcast:2 row_mask:0xf bank_mask:0xf\n"
    "v_mul_f32_dpp v234, v226, v234 row_newbcast:2 row_mask:0xf bank_mask:0xf\n"
    "v_add_f32_dpp v243, v229, v243 row_newbcast:3 row_mask:0xf bank_mask:0xf\n"
    "v_mfma_f32_16x16x32_bf16 v[64:67], v[188:191], a[204:207], v[64:67]\n"
    "v_mul_f32_dpp v235, v226, v235 row_newbcast:3 row_mask:0xf bank_mask:0xf\n"
    "v_add_f32_dpp v244, v229, v244 row_newbcast:4 row_mask:0xf bank_mask:0xf\n"
    "v_mul_f32_dpp v236, v226, v236 row_newbcast:4 row_mask:0xf bank_mask:0xf\n"
    "v_add_f32_dpp v245, v229, v245 row_newbcast:5 row_mask:0xf bank_mask:0xf\n"
    "v_mfma_f32_16x16x32_bf16 v[68:71], v[184:187], a[224:227], v[68:71]\n"
    "ds_read_b128 v[180:183], v200 offset:9216\n"
    "v_mul_f32_dpp v237, v226, v237 row_newbcast:5 row_mask:0xf bank_mask:0xf\n"
    "v_add_f32_dpp v246, v229, v246 row_newbcast:6 row_mask:0xf bank_mask:0xf\n"
    "v_mul_f32_dpp v238, v226, v238 row_newbcast:6 row_mask:0xf bank_mask:0xf\n"
    "v_add_f32_dpp v247, v229, v247 row_newbcast:7 row_mask:0xf bank_mask:0xf\n"
    "v_mul_f32_dpp v239, v226, v239 row_newbcast:7 row_mask:0xf bank_mask:0xf\n"
    "v_cvt_pk_bf16_f32 v240, v240, v241\n"
    "v_add_f32_dpp v232, v227, v232 row_newbcast:0 row_mask:0xf bank_mask:0xf\n"
    "v_cvt_pk_bf16_f32 v241, v242, v243\n"
    "v_add_f32_dpp v233, v227, v233 row_newbcast:1 row_mask:0xf bank_mask:0xf\n"
    "v_cvt_pk_bf16_f32 v242, v244, v245\n"
    "v_add_f32_dpp v234, v227, v234 row_newbcast:2 row_mask:0xf bank_mask:0xf\n"
    "v_cvt_pk_bf16_f32 v243, v246, v247\n"
    "v_add_f32_dpp v235, v227, v235 row_newbcast:3 row_mask:0xf bank_mask:0xf\n"
    "v_add_f32_dpp v236, v227, v236 row_newbcast:4 row_mask:0xf bank_mask:0xf\n"
    "v_add_f32_dpp v237, v227, v237 row_newbcast:5 row_mask:0xf bank_mask:0xf\n"
    "v_add_f32_dpp v238, v227, v238 row_newbcast:6 row_mask:0xf bank_mask:0xf\n"
    "v_add_f32_dpp v239, v227, v239 row_newbcast:7 row_mask:0xf bank_mask:0xf\n"
    "v_cvt_pk_bf16_f32 v232, v232, v233\n"
    "v_cvt_pk_bf16_f32 v233, v234, v235\n"
    "v_cvt_pk_bf16_f32 v234, v236, v237\n"
    "v_cvt_pk_bf16_f32 v235, v238, v239\n"
    "v_mfma_f32_16x16x32_bf16 v[68:71], v[188:191], a[228:231], v[68:71]\n"
    "s_waitcnt lgkmcnt(4)\n"
    "v_max3_f32 v196, v40, v41, v42\n"
    "v_max3_f32 v196, v196, v43, v56\n"
    "v_max3_f32 v196, v196, v57, v58\n"
    "v_max_f32 v196, v196, v59\n"
    "buffer_store_dwordx4 v[232:235], v211, s[36:39], s76 offen sc1\n"
    "v_mfma_f32_16x16x32_bf16 v[64:67], v[160:163], a[208:211], v[64:67]\n"
    "ds_read_b128 v[184:187], v200 offset:10240\n"
    "v_max3_f32 v198, v44, v45, v46\n"
    "v_mov_b32 v197, v196\n"
    "v_max3_f32 v198, v198, v47, v60\n"
    "v_mfma_f32_16x16x32_bf16 v[64:67], v[164:167], a[212:215], v[64:67]\n"
    "v_permlane32_swap_b32 v196, v197\n"
    "v_max3_f32 v198, v198, v61, v62\n"
    "v_max_f32 v196, v196, v197\n"
    "v_max_f32 v198, v198, v63\n"
    "v_mfma_f32_16x16x32_bf16 v[68:71], v[160:163], a[232:235], v[68:71]\n"
    "ds_read_b128 v[188:191], v200 offset:11264\n"
    "v_mov_b32 v197, v196\n"
    "v_mov_b32 v199, v198\n"
    "s_nop 0\n"
    "v_permlane16_swap_b32 v196, v197\n"
    "v_mfma_f32_16x16x32_bf16 v[68:71], v[164:167], a[236:239], v[68:71]\n"
    "s_waitcnt lgkmcnt(4)\n"
    "v_permlane32_swap_b32 v198, v199\n"
    "v_max_f32 v196, v196, v197\n"
    "v_max_f32 v198, v198, v199\n"
    "v_add_f32 v196, 0x42640000, v196\n"
    "v_mfma_f32_16x16x32_bf16 v[80:83], v[168:171], a[192:195], 0\n"
    "ds_read_b128 v[160:163], v200 offset:12288\n"
    "v_mov_b32 v199, v198\n"
    "v_sub_f32 v96, 0, v196\n"
    "s_nop 0\n"
    "v_permlane16_swap_b32 v198, v199\n"
    "v_mfma_f32_16x16x32_bf16 v[80:83], v[172:175], a[196:199], v[80:83]\n"
    "s_add_u32 s78, s88, 0x4800\n"
    "buffer_store_dwordx4 v[240:243], v212, s[40:43], s77 offen sc1\n"
    "v_sub_f32 v97, 0, v196\n"
    "v_max_f32 v198, v198, v199\n"
    "v_sub_f32 v98, 0, v196\n"
    "v_add_f32 v198, 0x42640000, v198\n"
    "v_sub_f32 v99, 0, v196\n"
    "v_mfma_f32_16x16x32_bf16 v[84:87], v[168:171], a[216:219], 0\n"
    "s_add_u32 m0, s71, 0x6000\n"
    "ds_read_b128 v[164:167], v200 offset:13312\n"
    "v_sub_f32 v100, 0, v198\n"
    "v_add_f32 v40, v40, v96\n"
    "v_sub_f32 v101, 0, v198\n"
    "v_mfma_f32_16x16x32_bf16 v[84:87], v[172:175], a[220:223], v[84:87]\n"
    "s_waitcnt lgkmcnt(4)\n"
    "buffer_load_dwordx4 v152, s[28:31], s78 offen nt lds\n"
    "v_add_f32 v41, v41, v96\n"
    "v_sub_f32 v102, 0, v198\n"
    "v_mfma_f32_16x16x32_bf16 v[80:83], v[176:179], a[200:203], v[80:83]\n"
    "ds_read_b128 v[168:171], v200 offset:14336\n"
    "v_add_f32 v42, v42, v96\n"
    "v_sub_f32 v103, 0, v198\n"
    "v_add_f32 v43, v43, v96\n"
    "v_mfma_f32_16x16x32_bf16 v[80:83], v[180:183], a[204:207], v[80:83]\n"
    "v_add_f32 v44, v44, v100\n"
    "v_add_f32 v56, v56, v96\n"
    "v_add_f32 v45, v45, v100\n"
    "v_add_f32 v57, v57, v96\n"
    "v_mfma_f32_16x16x32_bf16 v[84:87], v[176:179], a[224:227], v[84:87]\n"
    "ds_read_b128 v[172:175], v200 offset:15360\n"
    "v_add_f32 v46, v46, v100\n"
    "v_add_f32 v58, v58, v96\n"
    "v_add_f32 v47, v47, v100\n"
    "v_mfma_f32_16x16x32_bf16 v[84:87], v[180:183], a[228:231], v[84:87]\n"
    "s_waitcnt lgkmcnt(4)\n"
    "v_add_f32 v59, v59, v96\n"
    "v_add_f32 v60, v60, v100\n"
    "v_exp_f32 v40, v40\n"
    "v_mfma_f32_16x16x32_bf16 v[80:83], v[184:187], a[208:211], v[80:83]\n"
    "ds_read_b128 v[176:179], v200 offset:16384\n"
    "v_add_f32 v61, v61, v100\n"
    "v_add_f32 v62, v62, v100\n"
    "v_add_f32 v63, v63, v100\n"
    "v_mfma_f32_16x16x32_bf16 v[80:83], v[188:191], a[212:215], v[80:83]\n"
    "v_exp_f32 v41, v41\n"
    "v_exp_f32 v44, v44\n"
    "v_mfma_f32_16x16x32_bf16 v[84:87], v[184:187], a[232:235], v[84:87]\n"
    "ds_read_b128 v[180:183], v200 offset:17408\n"
    "v_exp_f32 v42, v42\n"
    "v_mfma_f32_16x16x32_bf16 v[84:87], v[188:191], a[236:239], v[84:87]\n"
    "s_waitcnt lgkmcnt(4)\n"
    "v_exp_f32 v45, v45\n"
    "v_exp_f32 v43, v43\n"
    "v_mfma_f32_16x16x32_bf16 v[32:35], v[160:163], a[192:195], v[96:99]\n"
    "ds_read_b128 v[184:187], v200 offset:18432\n"
    "v_exp_f32 v46, v46\n"
    "v_mfma_f32_16x16x32_bf16 v[32:35], v[164:167], a[196:199], v[32:35]\n"
    "s_add_u32 s78, s88, 0x4800\n"
    "v_exp_f32 v56, v56\n"
    "v_exp_f32 v47, v47\n"
    "v_mfma_f32_16x16x32_bf16 v[36:39], v[160:163], a[216:219], v[100:103]\n"
    "s_add_u32 m0, s71, 0x6400\n"
    "ds_read_b128 v[188:191], v200 offset:19456\n"
    "v_exp_f32 v57, v57\n"
    "v_mfma_f32_16x16x32_bf16 v[36:39], v[164:167], a[220:223], v[36:39]\n"
    "s_waitcnt lgkmcnt(4)\n"
    "buffer_load_dwordx4 v153, s[28:31], s78 offen nt lds\n"
    "v_exp_f32 v60, v60\n"
    "v_mfma_f32_16x16x32_bf16 v[32:35], v[168:171], a[200:203], v[32:35]\n"
    "ds_read_b128 v[160:163], v200 offset:20480\n"
    "v_exp_f32 v58, v58\n"
    "v_mfma_f32_16x16x32_bf16 v[32:35], v[172:175], a[204:207], v[32:35]\n"
    "v_exp_f32 v61, v61\n"
    "v_exp_f32 v59, v59\n"
    "v_mfma_f32_16x16x32_bf16 v[36:39], v[168:171], a[224:227], v[36:39]\n"
    "ds_read_b128 v[164:167], v200 offset:21504\n"
    "v_exp_f32 v62, v62\n"
    "v_cvt_pk_bf16_f32 v128, v40, v41\n"
    "v_mfma_f32_16x16x32_bf16 v[36:39], v[172:175], a[228:231], v[36:39]\n"
    "s_waitcnt lgkmcnt(4)\n"
    "v_cvt_pk_bf16_f32 v129, v42, v43\n"
    "v_exp_f32 v63, v63\n"
    "v_cvt_pk_bf16_f32 v130, v56, v57\n"
    "v_mfma_f32_16x16x32_bf16 v[32:35], v[176:179], a[208:211], v[32:35]\n"
    "ds_read_b128 v[168:171], v200 offset:22528\n"
    "v_cvt_pk_bf16_f32 v131, v58, v59\n"
    "v_cvt_pk_bf16_f32 v132, v44, v45\n"
    "v_add_f32 v64, v64, v96\n"
    "v_mfma_f32_16x16x32_bf16 v[32:35], v[180:183], a[212:215], v[32:35]\n"
    "v_cvt_pk_bf16_f32 v133, v46, v47\n"
    "v_add_f32 v65, v65, v96\n"
    "v_cvt_pk_bf16_f32 v134, v60, v61\n"
    "v_add_f32 v66, v66, v96\n"
    "v_mfma_f32_16x16x32_bf16 v[36:39], v[176:179], a[232:235], v[36:39]\n"
    "ds_read_b128 v[172:175], v200 offset:23552\n"
    "v_cvt_pk_bf16_f32 v135, v62, v63\n"
    "v_add_f32 v67, v67, v96\n"
    "v_add_f32 v68, v68, v100\n"
    "v_mfma_f32_16x16x32_bf16 v[36:39], v[180:183], a[236:239], v[36:39]\n"
    "s_waitcnt lgkmcnt(4)\n"
    "v_add_f32 v80, v80, v96\n"
    "v_add_f32 v69, v69, v100\n"
    "s_waitcnt vmcnt(12)\n"
    "s_barrier\n"
    "v_mfma_f32_16x16x32_bf16 v[48:51], v[184:187], a[192:195], v[96:99]\n"
    "ds_read_b128 v[176:179], v201 offset:24576\n"
    "v_add_f32 v81, v81, v96\n"
    "v_add_f32 v70, v70, v100\n"
    "v_add_f32 v82, v82, v96\n"
    "v_mfma_f32_16x16x32_bf16 v[48:51], v[188:191], a[196:199], v[48:51]\n"
    "s_add_u32 s78, s88, 0x4800\n"
    "v_add_f32 v71, v71, v100\n"
    "v_add_f32 v83, v83, v96\n"
    "v_add_f32 v84, v84, v100\n"
    "v_add_f32 v85, v85, v100\n"
    "v_mfma_f32_16x16x32_bf16 v[52:55], v[184:187], a[216:219], v[100:103]\n"
    "s_add_u32 m0, s71, 0x6800\n"
    "ds_read_b128 v[180:183], v201 offset:25600\n"
    "v_exp_f32 v64, v64\n"
    "v_add_f32 v86, v86, v100\n"
    "v_mfma_f32_16x16x32_bf16 v[52:55], v[188:191], a[220:223], v[52:55]\n"
    "s_waitcnt lgkmcnt(4)\n"
    "buffer_load_dwordx4 v154, s[28:31], s78 offen nt lds\n"
    "v_exp_f32 v65, v65\n"
    "v_mfma_f32_16x16x32_bf16 v[48:51], v[160:163], a[200:203], v[48:51]\n"
    "ds_read_b128 v[184:187], v201 offset:26624\n"
    "v_add_f32 v87, v87, v100\n"
    "v_exp_f32 v68, v68\n"
    "v_mfma_f32_16x16x32_bf16 v[48:51], v[164:167], a[204:207], v[48:51]\n"
    "v_exp_f32 v66, v66\n"
    "v_exp_f32 v69, v69\n"
    "v_mfma_f32_16x16x32_bf16 v[52:55], v[160:163], a[224:227], v[52:55]\n"
    "ds_read_b128 v[188:191], v201 offset:27648\n"
    "v_exp_f32 v67, v67\n"
    "ds_read_b64 v[228:229], v210 offset:192\n"
    "v_mfma_f32_16x16x32_bf16 v[52:55], v[164:167], a[228:231], v[52:55]\n"
    "s_waitcnt lgkmcnt(5)\n"
    "v_exp_f32 v70, v70\n"
    "v_exp_f32 v80, v80\n"
    "v_mfma_f32_16x16x32_bf16 v[48:51], v[168:171], a[208:211], v[48:51]\n"
    "ds_read_b128 v[160:163], v201 offset:28672\n"
    "v_exp_f32 v71, v71\n"
    "ds_read2st64_b32 v[240:241], v207 offset0:96 offset1:99\n"
    "v_mfma_f32_16x16x32_bf16 v[48:51], v[172:175], a[212:215], v[48:51]\n"
    "v_exp_f32 v81, v81\n"
    "v_exp_f32 v84, v84\n"
    "v_mfma_f32_16x16x32_bf16 v[52:55], v[168:171], a[232:235], v[52:55]\n"
    "ds_read_b128 v[164:167], v201 offset:29696\n"
    "v_exp_f32 v82, v82\n"
    "ds_read2st64_b32 v[242:243], v207 offset0:102 offset1:105\n"
    "v_exp_f32 v83, v83\n"
    "v_cvt_pk_bf16_f32 v144, v64, v65\n"
    "v_cvt_pk_bf16_f32 v145, v66, v67\n"
    "v_cvt_pk_bf16_f32 v146, v80, v81\n"
    "v_cvt_pk_bf16_f32 v147, v82, v83\n"
    "v_mfma_f32_16x16x32_bf16 v[52:55], v[172:175], a[236:239], v[52:55]\n"
    "s_waitcnt lgkmcnt(7)\n"
    "v_exp_f32 v85, v85\n"
    "v_exp_f32 v86, v86\n"
    "v_mfma_f32_16x16x32_bf16 a[0:3], v[176:179], v[128:131], 0\n"
    "ds_read_b128 v[168:171], v201 offset:30720\n"
    "ds_read2st64_b32 v[244:245], v207 offset0:108 offset1:111\n"
    "v_exp_f32 v87, v87\n"
    "v_cvt_pk_bf16_f32 v148, v68, v69\n"
    "v_cvt_pk_bf16_f32 v149, v70, v71\n"
    "v_cvt_pk_bf16_f32 v150, v84, v85\n"
    "v_cvt_pk_bf16_f32 v151, v86, v87\n"
    "v_mfma_f32_16x16x32_bf16 a[0:3], v[180:183], v[144:147], a[0:3]\n"
    "s_add_u32 s78, s88, 0x4800\n"
    "ds_read2st64_b32 v[246:247], v207 offset0:114 offset1:117\n"
    "ds_read_u16_d16_hi v223, v209 offset:6912\n"
    "v_mfma_f32_16x16x32_bf16 a[4:7], v[176:179], v[132:135], 0\n"
    "s_add_u32 m0, s71, 0x6c00\n"
    "ds_read_b128 v[172:175], v201 offset:31744\n"
    "ds_read_u16_d16_hi v222, v209 offset:768\n"
    "v_mfma_f32_16x16x32_bf16 a[4:7], v[180:183], v[148:151], a[4:7]\n"
    "s_waitcnt lgkmcnt(11)\n"
    "buffer_load_dwordx4 v155, s[28:31], s78 offen nt lds\n"
    "v_mfma_f32_16x16x32_bf16 a[16:19], v[184:187], v[128:131], 0\n"
    "ds_read_b128 v[176:179], v201 offset:32768\n"
    "ds_read_b128 v[232:235], v205 offset:24576\n"
    "v_mfma_f32_16x16x32_bf16 a[16:19], v[188:191], v[144:147], a[16:19]\n"
    "v_exp_f32 v32, v32\n"
    "v_mfma_f32_16x16x32_bf16 a[20:23], v[184:187], v[132:135], 0\n"
    "ds_read_b128 v[180:183], v201 offset:33792\n"
    "ds_read_b128 v[236:239], v206 offset:24576\n"
    "v_mfma_f32_16x16x32_bf16 a[20:23], v[188:191], v[148:151], a[20:23]\n"
    "s_waitcnt lgkmcnt(11)\n"
    "v_exp_f32 v33, v33\n"
    "v_mfma_f32_16x16x32_bf16 a[32:35], v[160:163], v[128:131], 0\n"
    "ds_read_b128 v[184:187], v201 offset:34816\n"
    "v_cvt_f32_i32 v240, v240\n"
    "v_mfma_f32_16x16x32_bf16 a[32:35], v[164:167], v[144:147], a[32:35]\n"
    "v_exp_f32 v34, v34\n"
    "v_mfma_f32_16x16x32_bf16 a[36:39], v[160:163], v[132:135], 0\n"
    "ds_read_b128 v[188:191], v201 offset:35840\n"
    "v_cvt_f32_i32 v241, v241\n"
    "v_mfma_f32_16x16x32_bf16 a[36:39], v[164:167], v[148:151], a[36:39]\n"
    "v_exp_f32 v35, v35\n"
    "v_mfma_f32_16x16x32_bf16 v[112:115], v[192:195], v[128:131], 0\n"
    "s_waitcnt lgkmcnt(12)\n"
    "v_cvt_f32_i32 v242, v242\n"
    "v_cvt_f32_i32 v243, v243\n"
    "v_mfma_f32_16x16x32_bf16 v[112:115], v[192:195], v[144:147], v[112:115]\n"
    "s_waitcnt lgkmcnt(7)\n"
    "s_add_u32 s78, s88, 0x4800\n"
    "v_cvt_f32_i32 v244, v244\n"
    "v_mfma_f32_16x16x32_bf16 a[48:51], v[168:171], v[128:131], 0\n"
    "s_add_u32 m0, s71, 0x7000\n"
    "ds_read_b128 v[160:163], v201 offset:36864\n"
    "v_cvt_f32_i32 v245, v245\n"
    "v_mfma_f32_16x16x32_bf16 a[48:51], v[172:175], v[144:147], a[48:51]\n"
    "buffer_load_dwordx4 v156, s[28:31], s78 offen nt lds\n"
    "v_mfma_f32_16x16x32_bf16 a[52:55], v[168:171], v[132:135], 0\n"
    "ds_read_b128 v[164:167], v201 offset:37888\n"
    "v_cvt_f32_i32 v246, v246\n"
    "v_mfma_f32_16x16x32_bf16 a[52:55], v[172:175], v[148:151], a[52:55]\n"
    "v_exp_f32 v36, v36\n"
    "v_mfma_f32_16x16x32_bf16 v[116:119], v[192:195], v[132:135], 0\n"
    "v_exp_f32 v37, v37\n"
    "v_mfma_f32_16x16x32_bf16 v[116:119], v[192:195], v[148:151], v[116:119]\n"
    "s_waitcnt lgkmcnt(5)\n"
    "v_exp_f32 v38, v38\n"
    "v_mfma_f32_16x16x32_bf16 a[64:67], v[176:179], v[128:131], 0\n"
    "ds_read_b128 v[168:171], v201 offset:38912\n"
    "v_mul_f32 v226, s75, v223\n"
    "v_mfma_f32_16x16x32_bf16 a[64:67], v[180:183], v[144:147], a[64:67]\n"
    "v_cvt_f32_i32 v247, v247\n"
    "v_fma_f32 v227, -v222, v226, v217\n"
    "v_mfma_f32_16x16x32_bf16 a[68:71], v[176:179], v[132:135], 0\n"
    "ds_read_b128 v[172:175], v201 offset:39936\n"
    "v_mul_f32_dpp v240, v228, v240 row_newbcast:0 row_mask:0xf bank_mask:0xf\n"
    "v_mfma_f32_16x16x32_bf16 a[68:71], v[180:183], v[148:151], a[68:71]\n"
    "s_waitcnt lgkmcnt(4)\n"
    "v_exp_f32 v39, v39\n"
    "v_mfma_f32_16x16x32_bf16 a[80:83], v[184:187], v[128:131], 0\n"
    "ds_read_b128 v[176:179], v201 offset:40960\n"
    "v_cvt_f32_i32 v232, v232\n"
    "v_mfma_f32_16x16x32_bf16 a[80:83], v[188:191], v[144:147], a[80:83]\n"
    "s_add_u32 s78, s88, 0x4800\n"
    "v_mul_f32_dpp v241, v228, v241 row_newbcast:1 row_mask:0xf bank_mask:0xf\n"
    "v_cvt_f32_i32 v233, v233\n"
    "v_mfma_f32_16x16x32_bf16 a[84:87], v[184:187], v[132:135], 0\n"
    "s_add_u32 m0, s71, 0x7400\n"
    "ds_read_b128 v[180:183], v201 offset:41984\n"
    "v_mul_f32_dpp v242, v228, v242 row_newbcast:2 row_mask:0xf bank_mask:0xf\n"
    "v_mfma_f32_16x16x32_bf16 a[84:87], v[188:191], v[148:151], a[84:87]\n"
    "s_waitcnt lgkmcnt(4)\n"
    "buffer_load_dwordx4 v157, s[28:31], s78 offen nt lds\n"
    "v_mfma_f32_16x16x32_bf16 a[96:99], v[160:163], v[128:131], 0\n"
    "ds_read_b128 v[184:187], v201 offset:43008\n"
    "v_cvt_f32_i32 v234, v234\n"
    "v_mfma_f32_16x16x32_bf16 a[96:99], v[164:167], v[144:147], a[96:99]\n"
    "v_exp_f32 v48, v48\n"
    "v_mfma_f32_16x16x32_bf16 a[100:103], v[160:163], v[132:135], 0\n"
    "ds_read_b128 v[188:191], v201 offset:44032\n"
    "v_mul_f32_dpp v243, v228, v243 row_newbcast:3 row_mask:0xf bank_mask:0xf\n"
    "v_mfma_f32_16x16x32_bf16 a[100:103], v[164:167], v[148:151], a[100:103]\n"
    "s_waitcnt lgkmcnt(4)\n"
    "v_cvt_f32_i32 v235, v235\n"
    "v_mul_f32_dpp v244, v228, v244 row_newbcast:4 row_mask:0xf bank_mask:0xf\n"
    "v_mfma_f32_16x16x32_bf16 a[112:115], v[168:171], v[128:131], 0\n"
    "ds_read_b128 v[160:163], v201 offset:45056\n"
    "v_cvt_f32_i32 v236, v236\n"
    "v_mfma_f32_16x16x32_bf16 a[112:115], v[172:175], v[144:147], a[112:115]\n"
    "v_exp_f32 v49, v49\n"
    "v_mfma_f32_16x16x32_bf16 a[116:119], v[168:171], v[132:135], 0\n"
    "ds_read_b128 v[164:167], v201 offset:46080\n"
    "v_mul_f32_dpp v245, v228, v245 row_newbcast:5 row_mask:0xf bank_mask:0xf\n"
    "v_mfma_f32_16x16x32_bf16 a[116:119], v[172:175], v[148:151], a[116:119]\n"
    "s_waitcnt lgkmcnt(4)\n"
    "v_cvt_f32_i32 v237, v237\n"
    "v_mul_f32_dpp v246, v228, v246 row_newbcast:6 row_mask:0xf bank_mask:0xf\n"
    "v_mfma_f32_16x16x32_bf16 a[128:131], v[176:179], v[128:131], 0\n"
    "ds_read_b128 v[168:171], v201 offset:47104\n"
    "v_cvt_f32_i32 v238, v238\n"
    "v_mfma_f32_16x16x32_bf16 a[128:131], v[180:183], v[144:147], a[128:131]\n"
    "v_exp_f32 v50, v50\n"
    "v_mfma_f32_16x16x32_bf16 a[132:135], v[176:179], v[132:135], 0\n"
    "ds_read_b128 v[172:175], v201 offset:48128\n"
    "v_mul_f32_dpp v247, v228, v247 row_newbcast:7 row_mask:0xf bank_mask:0xf\n"
    "v_mfma_f32_16x16x32_bf16 a[132:135], v[180:183], v[148:151], a[132:135]\n"
    "s_waitcnt lgkmcnt(4)\n"
    "v_cvt_f32_i32 v239, v239\n"
    "v_add_f32_dpp v240, v229, v240 row_newbcast:0 row_mask:0xf bank_mask:0xf\n"
    "v_mfma_f32_16x16x32_bf16 a[144:147], v[184:187], v[128:131], 0\n"
    "v_exp_f32 v51, v51\n"
    "v_mfma_f32_16x16x32_bf16 a[144:147], v[188:191], v[144:147], a[144:147]\n"
    "v_mul_f32_dpp v232, v226, v232 row_newbcast:0 row_mask:0xf bank_mask:0xf\n"
    "v_add_f32_dpp v241, v229, v241 row_newbcast:1 row_mask:0xf bank_mask:0xf\n"
    "v_mfma_f32_16x16x32_bf16 a[148:151], v[184:187], v[132:135], 0\n"
    "v_mul_f32_dpp v233, v226, v233 row_newbcast:1 row_mask:0xf bank_mask:0xf\n"
    "v_add_f32_dpp v242, v229, v242 row_newbcast:2 row_mask:0xf bank_mask:0xf\n"
    "v_mfma_f32_16x16x32_bf16 a[148:151], v[188:191], v[148:151], a[148:151]\n"
    "s_waitcnt lgkmcnt(2)\n"
    "v_exp_f32 v52, v52\n"
    "v_mfma_f32_16x16x32_bf16 a[160:163], v[160:163], v[128:131], 0\n"
    "v_mul_f32_dpp v234, v226, v234 row_newbcast:2 row_mask:0xf bank_mask:0xf\n"
    "v_add_f32_dpp v243, v229, v243 row_newbcast:3 row_mask:0xf bank_mask:0xf\n"
    "v_mfma_f32_16x16x32_bf16 a[160:163], v[164:167], v[144:147], a[160:163]\n"
    "v_mul_f32_dpp v235, v226, v235 row_newbcast:3 row_mask:0xf bank_mask:0xf\n"
    "v_add_f32_dpp v244, v229, v244 row_newbcast:4 row_mask:0xf bank_mask:0xf\n"
    "v_mfma_f32_16x16x32_bf16 a[164:167], v[160:163], v[132:135], 0\n"
    "v_exp_f32 v53, v53\n"
    "v_mfma_f32_16x16x32_bf16 a[164:167], v[164:167], v[148:151], a[164:167]\n"
    "s_waitcnt lgkmcnt(0)\n"
    "v_mul_f32_dpp v236, v226, v236 row_newbcast:4 row_mask:0xf bank_mask:0xf\n"
    "v_add_f32_dpp v245, v229, v245 row_newbcast:5 row_mask:0xf bank_mask:0xf\n"
    "v_mfma_f32_16x16x32_bf16 a[176:179], v[168:171], v[128:131], 0\n"
    "v_exp_f32 v54, v54\n"
    "v_mfma_f32_16x16x32_bf16 a[176:179], v[172:175], v[144:147], a[176:179]\n"
    "v_mul_f32_dpp v237, v226, v237 row_newbcast:5 row_mask:0xf bank_mask:0xf\n"
    "v_add_f32_dpp v246, v229, v246 row_newbcast:6 row_mask:0xf bank_mask:0xf\n"
    "v_mfma_f32_16x16x32_bf16 a[180:183], v[168:171], v[132:135], 0\n"
    "v_mul_f32_dpp v238, v226, v238 row_newbcast:6 row_mask:0xf bank_mask:0xf\n"
    "v_add_f32_dpp v247, v229, v247 row_newbcast:7 row_mask:0xf bank_mask:0xf\n"
    "v_exp_f32 v55, v55\n"
    "v_mul_f32_dpp v239, v226, v239 row_newbcast:7 row_mask:0xf bank_mask:0xf\n"
    "v_cvt_pk_bf16_f32 v240, v240, v241\n"
    "v_add_f32_dpp v232, v227, v232 row_newbcast:0 row_mask:0xf bank_mask:0xf\n"
    "v_cvt_pk_bf16_f32 v241, v242, v243\n"
    "v_add_f32_dpp v233, v227, v233 row_newbcast:1 row_mask:0xf bank_mask:0xf\n"
    "v_cvt_pk_bf16_f32 v242, v244, v245\n"
    "v_add_f32_dpp v234, v227, v234 row_newbcast:2 row_mask:0xf bank_mask:0xf\n"
    "v_cvt_pk_bf16_f32 v243, v246, v247\n"
    "v_add_f32_dpp v235, v227, v235 row_newbcast:3 row_mask:0xf bank_mask:0xf\n"
    "v_add_f32_dpp v236, v227, v236 row_newbcast:4 row_mask:0xf bank_mask:0xf\n"
    "v_add_f32_dpp v237, v227, v237 row_newbcast:5 row_mask:0xf bank_mask:0xf\n"
    "v_add_f32_dpp v238, v227, v238 row_newbcast:6 row_mask:0xf bank_mask:0xf\n"
    "v_add_f32_dpp v239, v227, v239 row_newbcast:7 row_mask:0xf bank_mask:0xf\n"
    "v_cvt_pk_bf16_f32 v232, v232, v233\n"
    "v_cvt_pk_bf16_f32 v233, v234, v235\n"
    "v_cvt_pk_bf16_f32 v234, v236, v237\n"
    "v_cvt_pk_bf16_f32 v235, v238, v239\n"
    "v_mfma_f32_16x16x32_bf16 a[180:183], v[172:175], v[148:151], a[180:183]\n"
    "v_add_f32 v113, v113, v32\n"
    "v_add_f32 v114, v114, v33\n"
    "v_add_f32 v113, v113, v34\n"
    "v_add_f32 v114, v114, v35\n"
    "v_add_f32 v117, v117, v36\n"
    "v_add_f32 v118, v118, v37\n"
    "v_add_f32 v117, v117, v38\n"
    "v_add_f32 v118, v118, v39\n"
    "v_add_f32 v113, v113, v48\n"
    "v_add_f32 v114, v114, v49\n"
    "v_add_f32 v113, v113, v50\n"
    "v_add_f32 v114, v114, v51\n"
    "v_add_f32 v117, v117, v52\n"
    "buffer_store_dwordx4 v[232:235], v211, s[36:39], s62 offen sc1\n"
    "buffer_store_dwordx4 v[240:243], v212, s[40:43], s63 offen sc1\n"
    "v_add_f32 v118, v118, v53\n"
    "v_add_f32 v117, v117, v54\n"
    "v_add_f32 v118, v118, v55\n"
    "s_waitcnt vmcnt(10)\n"
    "ds_read_b128 a[240:243], v158 offset:49152\n"
    "ds_read_b128 a[244:247], v159 offset:49152\n"
    "ds_read_b128 a[248:251], v158 offset:49280\n"
    "ds_read_b128 a[252:255], v159 offset:49280\n"
    "ds_read_b128 v[0:3], v158 offset:49408\n"
    "ds_read_b128 v[4:7], v159 offset:49408\n"
    "s_waitcnt vmcnt(8)\n"
    "v_cmp_ne_u32 s[80:81], s67, v219\n"
    "v_cmp_gt_u32 vcc, s66, v218\n"
    "s_or_b64 vcc, vcc, s[80:81]\n"
    "s_cbranch_vccnz LSPRE1\n"
    "LSPB1:\n"
    "v_mov_b32 v220, 4\n"
    "s_add_u32 s66, s66, s74\n"
    "s_waitcnt lgkmcnt(0)\n"
    "s_barrier\n"
    "buffer_store_dwordx2 v[220:221], v214, s[44:47], 0 offen sc1\n"
    "ds_read_b128 v[160:163], v255\n"
    "ds_read_b128 v[164:167], v255 offset:1024\n"
    "ds_read_b128 v[168:171], v255 offset:2048\n"
    "ds_read_b128 v[172:175], v255 offset:3072\n"
    "ds_read_b128 v[176:179], v255 offset:4096\n"
    "ds_read_b128 v[180:183], v255 offset:5120\n"
    "s_waitcnt lgkmcnt(4)\n"
    "v_mfma_f32_16x16x32_bf16 v[44:47], v[160:163], a[240:243], 0\n"
    "ds_read_b128 v[184:187], v255 offset:6144\n"
    "v_mfma_f32_16x16x32_bf16 v[44:47], v[164:167], a[244:247], v[44:47]\n"
    "s_waitcnt lgkmcnt(3)\n"
    "ds_read_b128 v[188:191], v255 offset:7168\n"
    "v_mfma_f32_16x16x32_bf16 v[44:47], v[168:171], a[248:251], v[44:47]\n"
    "s_add_u32 m0, s71, 0xc000\n"
    "ds_read_b128 v[160:163], v255 offset:8192\n"
    "v_mfma_f32_16x16x32_bf16 v[44:47], v[172:175], a[252:255], v[44:47]\n"
    "s_waitcnt lgkmcnt(3)\n"
    "ds_read_b128 v[164:167], v255 offset:9216\n"
    "buffer_load_dwordx4 v200, s[40:43], s61 offen sc1 lds\n"
    "v_mfma_f32_16x16x32_bf16 v[44:47], v[176:179], v[0:3], v[44:47]\n"
    "ds_read_b128 v[168:171], v255 offset:10240\n"
    "v_mfma_f32_16x16x32_bf16 v[44:47], v[180:183], v[4:7], v[44:47]\n"
    "s_waitcnt lgkmcnt(3)\n"
    "ds_read_b128 v[172:175], v255 offset:11264\n"
    "v_mfma_f32_16x16x32_bf16 v[60:63], v[184:187], a[240:243], 0\n"
    "ds_read_b128 v[176:179], v200\n"
    "v_mfma_f32_16x16x32_bf16 v[60:63], v[188:191], a[244:247], v[60:63]\n"
    "s_waitcnt lgkmcnt(3)\n"
    "ds_read_b128 v[180:183], v200 offset:1024\n"
    "v_mfma_f32_16x16x32_bf16 v[60:63], v[160:163], a[248:251], v[60:63]\n"
    "ds_read_b128 v[184:187], v200 offset:2048\n"
    "buffer_load_dwordx4 v200, s[40:43], s61 offen offset:1024 sc1 lds\n"
    "v_mfma_f32_16x16x32_bf16 v[60:63], v[164:167], a[252:255], v[60:63]\n"
    "s_waitcnt lgkmcnt(3)\n"
    "ds_read_b128 v[188:191], v200 offset:3072\n"
    "v_mfma_f32_16x16x32_bf16 v[60:63], v[168:171], v[0:3], v[60:63]\n"
    "ds_read_b128 v[160:163], v200 offset:4096\n"
    "v_mfma_f32_16x16x32_bf16 v[60:63], v[172:175], v[4:7], v[60:63]\n"
    "s_waitcnt lgkmcnt(3)\n"
    "ds_read_b128 v[164:167], v200 offset:5120\n"
    "v_mfma_f32_16x16x32_bf16 v[72:75], v[176:179], a[240:243], 0\n"
    "ds_read_b128 v[168:171], v200 offset:6144\n"
    "v_mfma_f32_16x16x32_bf16 v[72:75], v[180:183], a[244:247], v[72:75]\n"
    "s_waitcnt lgkmcnt(3)\n"
    "ds_read_b128 v[172:175], v200 offset:7168\n"
    "buffer_load_dwordx4 v200, s[40:43], s61 offen offset:2048 sc1 lds\n"
    "v_mfma_f32_16x16x32_bf16 v[72:75], v[184:187], a[248:251], v[72:75]\n"
    "ds_read_b128 v[176:179], v200 offset:8192\n"
    "v_mfma_f32_16x16x32_bf16 v[72:75], v[188:191], a[252:255], v[72:75]\n"
    "s_waitcnt lgkmcnt(3)\n"
    "ds_read_b128 v[180:183], v200 offset:9216\n"
    "v_mfma_f32_16x16x32_bf16 v[72:75], v[160:163], v[0:3], v[72:75]\n"
    "ds_read_b128 v[184:187], v200 offset:10240\n"
    "v_mfma_f32_16x16x32_bf16 v[72:75], v[164:167], v[4:7], v[72:75]\n"
    "s_waitcnt lgkmcnt(3)\n"
    "ds_read_b128 v[188:191], v200 offset:11264\n"
    "v_max3_f32 v196, v44, v45, v46\n"
    "v_max3_f32 v196, v196, v47, v60\n"
    "v_max3_f32 v196, v196, v61, v62\n"
    "v_mfma_f32_16x16x32_bf16 v[88:91], v[168:171], a[240:243], 0\n"
    "ds_read_b128 v[160:163], v200 offset:12288\n"
    "buffer_load_dwordx4 v200, s[40:43], s61 offen offset:3072 sc1 lds\n"
    "v_max_f32 v196, v196, v63\n"
    "v_mfma_f32_16x16x32_bf16 v[88:91], v[172:175], a[244:247], v[88:91]\n"
    "s_waitcnt lgkmcnt(3)\n"
    "ds_read_b128 v[164:167], v200 offset:13312\n"
    "v_mov_b32 v197, v196\n"
    "s_nop 1\n"
    "v_permlane32_swap_b32 v196, v197\n"
    "v_max_f32 v196, v196, v197\n"
    "v_mfma_f32_16x16x32_bf16 v[88:91], v[176:179], a[248:251], v[88:91]\n"
    "ds_read_b128 v[168:171], v200 offset:14336\n"
    "v_mov_b32 v197, v196\n"
    "s_nop 1\n"
    "v_permlane16_swap_b32 v196, v197\n"
    "v_max_f32 v196, v196, v197\n"
    "v_mfma_f32_16x16x32_bf16 v[88:91], v[180:183], a[252:255], v[88:91]\n"
    "s_waitcnt lgkmcnt(3)\n"
    "s_add_u32 s65, s61, 0x1000\n"
    "ds_read_b128 v[172:175], v200 offset:15360\n"
    "v_add_f32 v196, 0x42640000, v196\n"
    "v_sub_f32 v104, 0, v196\n"
    "v_mfma_f32_16x16x32_bf16 v[88:91], v[184:187], v[0:3], v[88:91]\n"
    "s_add_u32 m0, s71, 0xd000\n"
    "ds_read_b128 v[176:179], v200 offset:16384\n"
    "v_sub_f32 v105, 0, v196\n"
    "v_sub_f32 v106, 0, v196\n"
    "v_sub_f32 v107, 0, v196\n"
    "v_mfma_f32_16x16x32_bf16 v[88:91], v[188:191], v[4:7], v[88:91]\n"
    "s_waitcnt lgkmcnt(3)\n"
    "ds_read_b128 v[180:183], v200 offset:17408\n"
    "buffer_load_dwordx4 v200, s[40:43], s65 offen sc1 lds\n"
    "v_add_f32 v44, v44, v104\n"
    "v_mfma_f32_16x16x32_bf16 v[40:43], v[160:163], a[240:243], v[104:107]\n"
    "ds_read_b128 v[184:187], v200 offset:18432\n"
    "v_add_f32 v45, v45, v104\n"
    "v_add_f32 v46, v46, v104\n"
    "v_add_f32 v47, v47, v104\n"
    "v_mfma_f32_16x16x32_bf16 v[40:43], v[164:167], a[244:247], v[40:43]\n"
    "s_waitcnt lgkmcnt(3)\n"
    "ds_read_b128 v[188:191], v200 offset:19456\n"
    "v_add_f32 v60, v60, v104\n"
    "v_add_f32 v61, v61, v104\n"
    "v_add_f32 v62, v62, v104\n"
    "v_mfma_f32_16x16x32_bf16 v[40:43], v[168:171], a[248:251], v[40:43]\n"
    "ds_read_b128 v[160:163], v200 offset:20480\n"
    "v_add_f32 v63, v63, v104\n"
    "v_exp_f32 v44, v44\n"
    "v_mfma_f32_16x16x32_bf16 v[40:43], v[172:175], a[252:255], v[40:43]\n"
    "s_waitcnt lgkmcnt(3)\n"
    "ds_read_b128 v[164:167], v200 offset:21504\n"
    "v_exp_f32 v45, v45\n"
    "v_mfma_f32_16x16x32_bf16 v[40:43], v[176:179], v[0:3], v[40:43]\n"
    "ds_read_b128 v[168:171], v200 offset:22528\n"
    "buffer_load_dwordx4 v200, s[40:43], s65 offen offset:1024 sc1 lds\n"
    "v_mfma_f32_16x16x32_bf16 v[40:43], v[180:183], v[4:7], v[40:43]\n"
    "s_waitcnt lgkmcnt(3)\n"
    "ds_read_b128 v[172:175], v200 offset:23552\n"
    "v_exp_f32 v46, v46\n"
    "v_mfma_f32_16x16x32_bf16 v[56:59], v[184:187], a[240:243], v[104:107]\n"
    "ds_read_b128 v[176:179], v201 offset:24576\n"
    "v_exp_f32 v47, v47\n"
    "v_mfma_f32_16x16x32_bf16 v[56:59], v[188:191], a[244:247], v[56:59]\n"
    "s_waitcnt lgkmcnt(3)\n"
    "s_add_u32 s78, s64, 0x30000\n"
    "ds_read_b128 v[180:183], v201 offset:25600\n"
    "v_exp_f32 v60, v60\n"
    "v_mfma_f32_16x16x32_bf16 v[56:59], v[160:163], a[248:251], v[56:59]\n"
    "s_add_u32 m0, s72, 0x1b000\n"
    "ds_read_b128 v[184:187], v201 offset:26624\n"
    "v_exp_f32 v61, v61\n"
    "v_mfma_f32_16x16x32_bf16 v[56:59], v[164:167], a[252:255], v[56:59]\n"
    "s_waitcnt lgkmcnt(3)\n"
    "ds_read_b128 v[188:191], v201 offset:27648\n"
    "buffer_load_dwordx4 v202, s[32:35], s78 offen nt lds\n"
    "v_mfma_f32_16x16x32_bf16 v[56:59], v[168:171], v[0:3], v[56:59]\n"
    "ds_read_b128 v[160:163], v201 offset:28672\n"
    "v_exp_f32 v62, v62\n"
    "v_exp_f32 v63, v63\n"
    "v_cvt_pk_bf16_f32 v136, v44, v45\n"
    "v_cvt_pk_bf16_f32 v137, v46, v47\n"
    "v_cvt_pk_bf16_f32 v138, v60, v61\n"
    "v_cvt_pk_bf16_f32 v139, v62, v63\n"
    "v_add_f32 v72, v72, v104\n"
    "v_add_f32 v73, v73, v104\n"
    "v_add_f32 v74, v74, v104\n"
    "v_add_f32 v75, v75, v104\n"
    "v_add_f32 v88, v88, v104\n"
    "v_add_f32 v89, v89, v104\n"
    "v_add_f32 v90, v90, v104\n"
    "v_add_f32 v91, v91, v104\n"
    "v_exp_f32 v72, v72\n"
    "v_exp_f32 v73, v73\n"
    "v_exp_f32 v74, v74\n"
    "v_exp_f32 v75, v75\n"
    "v_exp_f32 v88, v88\n"
    "v_exp_f32 v89, v89\n"
    "v_exp_f32 v90, v90\n"
    "v_exp_f32 v91, v91\n"
    "v_cvt_pk_bf16_f32 v152, v72, v73\n"
    "v_cvt_pk_bf16_f32 v153, v74, v75\n"
    "v_cvt_pk_bf16_f32 v154, v88, v89\n"
    "v_cvt_pk_bf16_f32 v155, v90, v91\n"
    "v_mfma_f32_16x16x32_bf16 v[56:59], v[172:175], v[4:7], v[56:59]\n"
    "s_waitcnt lgkmcnt(3)\n"
    "ds_read_b128 v[164:167], v201 offset:29696\n"
    "v_exp_f32 v40, v40\n"
    "v_mfma_f32_16x16x32_bf16 a[8:11], v[176:179], v[136:139], 0\n"
    "ds_read_b128 v[168:171], v201 offset:30720\n"
    "v_mfma_f32_16x16x32_bf16 a[8:11], v[180:183], v[152:155], a[8:11]\n"
    "s_waitcnt lgkmcnt(3)\n"
    "ds_read_b128 v[172:175], v201 offset:31744\n"
    "v_mfma_f32_16x16x32_bf16 a[24:27], v[184:187], v[136:139], 0\n"
    "ds_read_b128 v[176:179], v201 offset:32768\n"
    "buffer_load_dwordx4 v203, s[32:35], s78 offen offset:1024 nt lds\n"
    "v_mfma_f32_16x16x32_bf16 a[24:27], v[188:191], v[152:155], a[24:27]\n"
    "s_waitcnt lgkmcnt(3)\n"
    "ds_read_b128 v[180:183], v201 offset:33792\n"
    "v_mfma_f32_16x16x32_bf16 a[40:43], v[160:163], v[136:139], 0\n"
    "ds_read_b128 v[184:187], v201 offset:34816\n"
    "v_mfma_f32_16x16x32_bf16 a[40:43], v[164:167], v[152:155], a[40:43]\n"
    "ds_read_b128 v[188:191], v201 offset:35840\n"
    "v_mfma_f32_16x16x32_bf16 v[120:123], v[192:195], v[136:139], 0\n"
    "v_exp_f32 v41, v41\n"
    "v_mfma_f32_16x16x32_bf16 v[120:123], v[192:195], v[152:155], v[120:123]\n"
    "s_waitcnt lgkmcnt(4)\n"
    "buffer_load_dwordx4 v204, s[32:35], s78 offen offset:2048 nt lds\n"
    "v_mfma_f32_16x16x32_bf16 a[56:59], v[168:171], v[136:139], 0\n"
    "ds_read_b128 v[160:163], v201 offset:36864\n"
    "v_mfma_f32_16x16x32_bf16 a[56:59], v[172:175], v[152:155], a[56:59]\n"
    "s_waitcnt lgkmcnt(3)\n"
    "ds_read_b128 v[164:167], v201 offset:37888\n"
    "v_mfma_f32_16x16x32_bf16 a[72:75], v[176:179], v[136:139], 0\n"
    "ds_read_b128 v[168:171], v201 offset:38912\n"
    "v_mfma_f32_16x16x32_bf16 a[72:75], v[180:183], v[152:155], a[72:75]\n"
    "s_waitcnt lgkmcnt(3)\n"
    "ds_read_b128 v[172:175], v201 offset:39936\n"
    "v_mfma_f32_16x16x32_bf16 a[88:91], v[184:187], v[136:139], 0\n"
    "ds_read_b128 v[176:179], v201 offset:40960\n"
    "v_mfma_f32_16x16x32_bf16 a[88:91], v[188:191], v[152:155], a[88:91]\n"
    "s_waitcnt lgkmcnt(3)\n"
    "ds_read_b128 v[180:183], v201 offset:41984\n"
    "v_mfma_f32_16x16x32_bf16 a[104:107], v[160:163], v[136:139], 0\n"
    "ds_read_b128 v[184:187], v201 offset:43008\n"
    "v_mfma_f32_16x16x32_bf16 a[104:107], v[164:167], v[152:155], a[104:107]\n"
    "s_waitcnt lgkmcnt(3)\n"
    "ds_read_b128 v[188:191], v201 offset:44032\n"
    "v_mfma_f32_16x16x32_bf16 a[120:123], v[168:171], v[136:139], 0\n"
    "ds_read_b128 v[160:163], v201 offset:45056\n"
    "v_mfma_f32_16x16x32_bf16 a[120:123], v[172:175], v[152:155], a[120:123]\n"
    "s_waitcnt lgkmcnt(3)\n"
    "ds_read_b128 v[164:167], v201 offset:46080\n"
    "v_mfma_f32_16x16x32_bf16 a[136:139], v[176:179], v[136:139], 0\n"
    "ds_read_b128 v[168:171], v201 offset:47104\n"
    "v_mfma_f32_16x16x32_bf16 a[136:139], v[180:183], v[152:155], a[136:139]\n"
    "s_waitcnt lgkmcnt(3)\n"
    "ds_read_b128 v[172:175], v201 offset:48128\n"
    "v_mfma_f32_16x16x32_bf16 a[152:155], v[184:187], v[136:139], 0\n"
    "v_exp_f32 v42, v42\n"
    "v_mfma_f32_16x16x32_bf16 a[152:155], v[188:191], v[152:155], a[152:155]\n"
    "s_waitcnt lgkmcnt(2)\n"
    "v_exp_f32 v43, v43\n"
    "v_mfma_f32_16x16x32_bf16 a[168:171], v[160:163], v[136:139], 0\n"
    "v_exp_f32 v56, v56\n"
    "v_mfma_f32_16x16x32_bf16 a[168:171], v[164:167], v[152:155], a[168:171]\n"
    "s_waitcnt lgkmcnt(0)\n"
    "v_exp_f32 v57, v57\n"
    "v_mfma_f32_16x16x32_bf16 a[184:187], v[168:171], v[136:139], 0\n"
    "v_exp_f32 v58, v58\n"
    "v_exp_f32 v59, v59\n"
    "v_mfma_f32_16x16x32_bf16 a[184:187], v[172:175], v[152:155], a[184:187]\n"
    "v_add_f32 v121, v121, v40\n"
    "v_add_f32 v122, v122, v41\n"
    "v_add_f32 v121, v121, v42\n"
    "v_add_f32 v122, v122, v43\n"
    "v_add_f32 v121, v121, v56\n"
    "v_add_f32 v122, v122, v57\n"
    "s_waitcnt vmcnt(12)\n"
    "ds_read_b128 v[8:11], v158 offset:24576\n"
    "ds_read_b128 v[12:15], v159 offset:24576\n"
    "ds_read_b128 v[16:19], v158 offset:24704\n"
    "ds_read_b128 v[20:23], v159 offset:24704\n"
    "ds_read_b128 v[24:27], v158 offset:24832\n"
    "ds_read_b128 v[28:31], v159 offset:24832\n"
    "s_waitcnt lgkmcnt(0)\n"
    "ds_read_b128 v[160:163], v255\n"
    "ds_read_b128 v[164:167], v255 offset:1024\n"
    "ds_read_b128 v[168:171], v255 offset:2048\n"
    "ds_read_b128 v[172:175], v255 offset:3072\n"
    "ds_read_b128 v[176:179], v255 offset:4096\n"
    "ds_read_b128 v[180:183], v255 offset:5120\n"
    "s_waitcnt lgkmcnt(4)\n"
    "v_mfma_f32_16x16x32_bf16 v[64:67], v[160:163], v[8:11], 0\n"
    "ds_read_b128 v[184:187], v255 offset:6144\n"
    "v_mfma_f32_16x16x32_bf16 v[64:67], v[164:167], v[12:15], v[64:67]\n"
    "s_waitcnt lgkmcnt(3)\n"
    "s_add_u32 m0, s71, 0x6000\n"
    "ds_read_b128 v[188:191], v255 offset:7168\n"
    "v_mfma_f32_16x16x32_bf16 v[64:67], v[168:171], v[16:19], v[64:67]\n"
    "ds_read_b128 v[160:163], v255 offset:8192\n"
    "buffer_load_dwordx4 v200, s[36:39], s60 offen sc1 lds\n"
    "v_mfma_f32_16x16x32_bf16 v[64:67], v[172:175], v[20:23], v[64:67]\n"
    "s_waitcnt lgkmcnt(3)\n"
    "ds_read_b128 v[164:167], v255 offset:9216\n"
    "v_mfma_f32_16x16x32_bf16 v[64:67], v[176:179], v[24:27], v[64:67]\n"
    "ds_read_b128 v[168:171], v255 offset:10240\n"
    "v_mfma_f32_16x16x32_bf16 v[64:67], v[180:183], v[28:31], v[64:67]\n"
    "s_waitcnt lgkmcnt(3)\n"
    "ds_read_b128 v[172:175], v255 offset:11264\n"
    "buffer_load_dwordx4 v200, s[36:39], s60 offen offset:1024 sc1 lds\n"
    "v_mfma_f32_16x16x32_bf16 v[80:83], v[184:187], v[8:11], 0\n"
    "ds_read_b128 v[176:179], v200\n"
    "v_mfma_f32_16x16x32_bf16 v[80:83], v[188:191], v[12:15], v[80:83]\n"
    "s_waitcnt lgkmcnt(3)\n"
    "ds_read_b128 v[180:183], v200 offset:1024\n"
    "v_mfma_f32_16x16x32_bf16 v[80:83], v[160:163], v[16:19], v[80:83]\n"
    "ds_read_b128 v[184:187], v200 offset:2048\n"
    "buffer_load_dwordx4 v200, s[36:39], s60 offen offset:2048 sc1 lds\n"
    "v_mfma_f32_16x16x32_bf16 v[80:83], v[164:167], v[20:23], v[80:83]\n"
    "s_waitcnt lgkmcnt(3)\n"
    "ds_read_b128 v[188:191], v200 offset:3072\n"
    "v_mfma_f32_16x16x32_bf16 v[80:83], v[168:171], v[24:27], v[80:83]\n"
    "ds_read_b128 v[160:163], v200 offset:4096\n"
    "v_mfma_f32_16x16x32_bf16 v[80:83], v[172:175], v[28:31], v[80:83]\n"
    "s_waitcnt lgkmcnt(3)\n"
    "ds_read_b128 v[164:167], v200 offset:5120\n"
    "buffer_load_dwordx4 v200, s[36:39], s60 offen offset:3072 sc1 lds\n"
    "v_mfma_f32_16x16x32_bf16 v[76:79], v[176:179], v[8:11], 0\n"
    "s_add_u32 s65, s60, 0x1000\n"
    "ds_read_b128 v[168:171], v200 offset:6144\n"
    "v_mfma_f32_16x16x32_bf16 v[76:79], v[180:183], v[12:15], v[76:79]\n"
    "s_waitcnt lgkmcnt(3)\n"
    "s_add_u32 m0, s71, 0x7000\n"
    "ds_read_b128 v[172:175], v200 offset:7168\n"
    "v_mfma_f32_16x16x32_bf16 v[76:79], v[184:187], v[16:19], v[76:79]\n"
    "ds_read_b128 v[176:179], v200 offset:8192\n"
    "buffer_load_dwordx4 v200, s[36:39], s65 offen sc1 lds\n"
    "v_mfma_f32_16x16x32_bf16 v[76:79], v[188:191], v[20:23], v[76:79]\n"
    "s_waitcnt lgkmcnt(3)\n"
    "ds_read_b128 v[180:183], v200 offset:9216\n"
    "v_mfma_f32_16x16x32_bf16 v[76:79], v[160:163], v[24:27], v[76:79]\n"
    "ds_read_b128 v[184:187], v200 offset:10240\n"
    "v_mfma_f32_16x16x32_bf16 v[76:79], v[164:167], v[28:31], v[76:79]\n"
    "s_waitcnt lgkmcnt(3)\n"
    "ds_read_b128 v[188:191], v200 offset:11264\n"
    "buffer_load_dwordx4 v200, s[36:39], s65 offen offset:1024 sc1 lds\n"
    "v_max3_f32 v196, v64, v65, v66\n"
    "v_mfma_f32_16x16x32_bf16 v[92:95], v[168:171], v[8:11], 0\n"
    "ds_read_b128 v[160:163], v200 offset:12288\n"
    "v_max3_f32 v196, v196, v67, v80\n"
    "v_max3_f32 v196, v196, v81, v82\n"
    "v_max_f32 v196, v196, v83\n"
    "v_mfma_f32_16x16x32_bf16 v[92:95], v[172:175], v[12:15], v[92:95]\n"
    "s_waitcnt lgkmcnt(3)\n"
    "ds_read_b128 v[164:167], v200 offset:13312\n"
    "v_mov_b32 v197, v196\n"
    "s_nop 1\n"
    "v_permlane32_swap_b32 v196, v197\n"
    "v_max_f32 v196, v196, v197\n"
    "v_mfma_f32_16x16x32_bf16 v[92:95], v[176:179], v[16:19], v[92:95]\n"
    "ds_read_b128 v[168:171], v200 offset:14336\n"
    "v_mov_b32 v197, v196\n"
    "s_nop 1\n"
    "v_permlane16_swap_b32 v196, v197\n"
    "v_max_f32 v196, v196, v197\n"
    "v_mfma_f32_16x16x32_bf16 v[92:95], v[180:183], v[20:23], v[92:95]\n"
    "s_waitcnt lgkmcnt(3)\n"
    "ds_read_b128 v[172:175], v200 offset:15360\n"
    "v_add_f32 v196, 0x42640000, v196\n"
    "v_sub_f32 v108, 0, v196\n"
    "v_sub_f32 v109, 0, v196\n"
    "v_mfma_f32_16x16x32_bf16 v[92:95], v[184:187], v[24:27], v[92:95]\n"
    "ds_read_b128 v[176:179], v200 offset:16384\n"
    "v_sub_f32 v110, 0, v196\n"
    "v_sub_f32 v111, 0, v196\n"
    "v_add_f32 v64, v64, v108\n"
    "v_mfma_f32_16x16x32_bf16 v[92:95], v[188:191], v[28:31], v[92:95]\n"
    "s_waitcnt lgkmcnt(3)\n"
    "ds_read_b128 v[180:183], v200 offset:17408\n"
    "v_add_f32 v65, v65, v108\n"
    "v_add_f32 v66, v66, v108\n"
    "v_add_f32 v67, v67, v108\n"
    "v_mfma_f32_16x16x32_bf16 v[44:47], v[160:163], v[8:11], v[108:111]\n"
    "ds_read_b128 v[184:187], v200 offset:18432\n"
    "v_add_f32 v80, v80, v108\n"
    "v_add_f32 v81, v81, v108\n"
    "v_add_f32 v82, v82, v108\n"
    "v_mfma_f32_16x16x32_bf16 v[44:47], v[164:167], v[12:15], v[44:47]\n"
    "s_waitcnt lgkmcnt(3)\n"
    "ds_read_b128 v[188:191], v200 offset:19456\n"
    "v_add_f32 v83, v83, v108\n"
    "v_exp_f32 v64, v64\n"
    "v_mfma_f32_16x16x32_bf16 v[44:47], v[168:171], v[16:19], v[44:47]\n"
    "ds_read_b128 v[160:163], v200 offset:20480\n"
    "buffer_load_dwordx2 v[218:219], v213, s[44:47], 0 offen sc1\n"
    "v_mfma_f32_16x16x32_bf16 v[44:47], v[172:175], v[20:23], v[44:47]\n"
    "s_waitcnt lgkmcnt(3)\n"
    "ds_read_b128 v[164:167], v200 offset:21504\n"
    "v_exp_f32 v65, v65\n"
    "v_mfma_f32_16x16x32_bf16 v[44:47], v[176:179], v[24:27], v[44:47]\n"
    "ds_read_b128 v[168:171], v200 offset:22528\n"
    "v_exp_f32 v66, v66\n"
    "v_mfma_f32_16x16x32_bf16 v[44:47], v[180:183], v[28:31], v[44:47]\n"
    "s_waitcnt lgkmcnt(3)\n"
    "ds_read_b128 v[172:175], v200 offset:23552\n"
    "v_exp_f32 v67, v67\n"
    "v_mfma_f32_16x16x32_bf16 v[60:63], v[184:187], v[8:11], v[108:111]\n"
    "ds_read_b128 v[176:179], v201 offset:24576\n"
    "v_exp_f32 v80, v80\n"
    "v_mfma_f32_16x16x32_bf16 v[60:63], v[188:191], v[12:15], v[60:63]\n"
    "s_waitcnt lgkmcnt(3)\n"
    "ds_read_b128 v[180:183], v201 offset:25600\n"
    "v_exp_f32 v81, v81\n"
    "v_mfma_f32_16x16x32_bf16 v[60:63], v[160:163], v[16:19], v[60:63]\n"
    "ds_read_b128 v[184:187], v201 offset:26624\n"
    "v_exp_f32 v82, v82\n"
    "v_mfma_f32_16x16x32_bf16 v[60:63], v[164:167], v[20:23], v[60:63]\n"
    "s_waitcnt lgkmcnt(3)\n"
    "ds_read_b128 v[188:191], v201 offset:27648\n"
    "v_exp_f32 v83, v83\n"
    "v_cvt_pk_bf16_f32 v140, v64, v65\n"
    "v_mfma_f32_16x16x32_bf16 v[60:63], v[168:171], v[24:27], v[60:63]\n"
    "ds_read_b128 v[160:163], v201 offset:28672\n"
    "v_cvt_pk_bf16_f32 v141, v66, v67\n"
    "v_cvt_pk_bf16_f32 v142, v80, v81\n"
    "v_cvt_pk_bf16_f32 v143, v82, v83\n"
    "v_add_f32 v76, v76, v108\n"
    "v_add_f32 v77, v77, v108\n"
    "v_add_f32 v78, v78, v108\n"
    "v_add_f32 v79, v79, v108\n"
    "v_add_f32 v92, v92, v108\n"
    "v_add_f32 v93, v93, v108\n"
    "v_add_f32 v94, v94, v108\n"
    "v_add_f32 v95, v95, v108\n"
    "v_exp_f32 v76, v76\n"
    "v_exp_f32 v77, v77\n"
    "v_exp_f32 v78, v78\n"
    "v_exp_f32 v79, v79\n"
    "v_exp_f32 v92, v92\n"
    "v_exp_f32 v93, v93\n"
    "v_exp_f32 v94, v94\n"
    "v_exp_f32 v95, v95\n"
    "v_cvt_pk_bf16_f32 v156, v76, v77\n"
    "v_cvt_pk_bf16_f32 v157, v78, v79\n"
    "v_cvt_pk_bf16_f32 v158, v92, v93\n"
    "v_cvt_pk_bf16_f32 v159, v94, v95\n"
    "v_mfma_f32_16x16x32_bf16 v[60:63], v[172:175], v[28:31], v[60:63]\n"
    "s_waitcnt lgkmcnt(3)\n"
    "ds_read_b128 v[164:167], v201 offset:29696\n"
    "v_exp_f32 v44, v44\n"
    "v_mfma_f32_16x16x32_bf16 a[12:15], v[176:179], v[140:143], 0\n"
    "ds_read_b128 v[168:171], v201 offset:30720\n"
    "v_mfma_f32_16x16x32_bf16 a[12:15], v[180:183], v[156:159], a[12:15]\n"
    "s_waitcnt lgkmcnt(3)\n"
    "ds_read_b128 v[172:175], v201 offset:31744\n"
    "v_mfma_f32_16x16x32_bf16 a[28:31], v[184:187], v[140:143], 0\n"
    "ds_read_b128 v[176:179], v201 offset:32768\n"
    "v_mfma_f32_16x16x32_bf16 a[28:31], v[188:191], v[156:159], a[28:31]\n"
    "s_waitcnt lgkmcnt(3)\n"
    "ds_read_b128 v[180:183], v201 offset:33792\n"
    "v_mfma_f32_16x16x32_bf16 a[44:47], v[160:163], v[140:143], 0\n"
    "ds_read_b128 v[184:187], v201 offset:34816\n"
    "v_mfma_f32_16x16x32_bf16 a[44:47], v[164:167], v[156:159], a[44:47]\n"
    "ds_read_b128 v[188:191], v201 offset:35840\n"
    "v_mfma_f32_16x16x32_bf16 v[124:127], v[192:195], v[140:143], 0\n"
    "v_exp_f32 v45, v45\n"
    "v_mfma_f32_16x16x32_bf16 v[124:127], v[192:195], v[156:159], v[124:127]\n"
    "s_waitcnt lgkmcnt(4)\n"
    "v_exp_f32 v46, v46\n"
    "v_mfma_f32_16x16x32_bf16 a[60:63], v[168:171], v[140:143], 0\n"
    "ds_read_b128 v[160:163], v201 offset:36864\n"
    "v_mfma_f32_16x16x32_bf16 a[60:63], v[172:175], v[156:159], a[60:63]\n"
    "s_waitcnt lgkmcnt(3)\n"
    "ds_read_b128 v[164:167], v201 offset:37888\n"
    "v_mfma_f32_16x16x32_bf16 a[76:79], v[176:179], v[140:143], 0\n"
    "ds_read_b128 v[168:171], v201 offset:38912\n"
    "v_mfma_f32_16x16x32_bf16 a[76:79], v[180:183], v[156:159], a[76:79]\n"
    "s_waitcnt lgkmcnt(3)\n"
    "ds_read_b128 v[172:175], v201 offset:39936\n"
    "v_mfma_f32_16x16x32_bf16 a[92:95], v[184:187], v[140:143], 0\n"
    "ds_read_b128 v[176:179], v201 offset:40960\n"
    "v_mfma_f32_16x16x32_bf16 a[92:95], v[188:191], v[156:159], a[92:95]\n"
    "s_waitcnt lgkmcnt(3)\n"
    "ds_read_b128 v[180:183], v201 offset:41984\n"
    "v_mfma_f32_16x16x32_bf16 a[108:111], v[160:163], v[140:143], 0\n"
    "ds_read_b128 v[184:187], v201 offset:43008\n"
    "v_mfma_f32_16x16x32_bf16 a[108:111], v[164:167], v[156:159], a[108:111]\n"
    "s_waitcnt lgkmcnt(3)\n"
    "ds_read_b128 v[188:191], v201 offset:44032\n"
    "v_mfma_f32_16x16x32_bf16 a[124:127], v[168:171], v[140:143], 0\n"
    "ds_read_b128 v[160:163], v201 offset:45056\n"
    "v_mfma_f32_16x16x32_bf16 a[124:127], v[172:175], v[156:159], a[124:127]\n"
    "s_waitcnt lgkmcnt(3)\n"
    "ds_read_b128 v[164:167], v201 offset:46080\n"
    "v_mfma_f32_16x16x32_bf16 a[140:143], v[176:179], v[140:143], 0\n"
    "ds_read_b128 v[168:171], v201 offset:47104\n"
    "v_mfma_f32_16x16x32_bf16 a[140:143], v[180:183], v[156:159], a[140:143]\n"
    "s_waitcnt lgkmcnt(3)\n"
    "ds_read_b128 v[172:175], v201 offset:48128\n"
    "v_mfma_f32_16x16x32_bf16 a[156:159], v[184:187], v[140:143], 0\n"
    "v_exp_f32 v47, v47\n"
    "v_mfma_f32_16x16x32_bf16 a[156:159], v[188:191], v[156:159], a[156:159]\n"
    "s_waitcnt lgkmcnt(2)\n"
    "v_exp_f32 v60, v60\n"
    "v_mfma_f32_16x16x32_bf16 a[172:175], v[160:163], v[140:143], 0\n"
    "v_exp_f32 v61, v61\n"
    "v_mfma_f32_16x16x32_bf16 a[172:175], v[164:167], v[156:159], a[172:175]\n"
    "s_waitcnt lgkmcnt(0)\n"
    "v_exp_f32 v62, v62\n"
    "v_mfma_f32_16x16x32_bf16 a[188:191], v[168:171], v[140:143], 0\n"
    "v_exp_f32 v63, v63\n"
    "v_mfma_f32_16x16x32_bf16 a[188:191], v[172:175], v[156:159], a[188:191]\n"
    "v_add_f32 v125, v125, v44\n"
    "v_add_f32 v126, v126, v45\n"
    "v_add_f32 v125, v125, v46\n"
    "v_add_f32 v126, v126, v47\n"
    "s_add_u32 s64, s64, 0x3c000\n"
    "s_add_u32 s62, s62, 0x6000\n"
    "s_add_u32 s63, s63, 0x6000\n"
    "s_add_u32 s60, s60, 0x6000\n"
    "s_add_u32 s61, s61, 0x6000\n"
    "v_add_u32 v209, 0x300, v209\n"
    "v_add_u32 v210, 0xc0, v210\n"
    "v_mov_b32 v220, 5\n"
    "s_waitcnt vmcnt(0)\n"
    "v_cmp_ne_u32 s[80:81], s67, v219\n"
    "v_cmp_gt_u32 vcc, s66, v218\n"
    "s_or_b64 vcc, vcc, s[80:81]\n"
    "s_cbranch_vccnz LSPRE2\n"
    "LSPB2:\n"
    "s_barrier\n"
    "s_cmp_eq_u32 s58, 0\n"
    "s_cbranch_scc1 LENT0\n"
    "LENT1:\n"
    "s_mov_b32 s59, 1\n"
    "s_add_u32 m0, s72, 0x21000\n"
    "s_nop 0\n"
    "buffer_load_dwordx4 v250, s[48:51], 0 offen offset:2048 lds\n"
    "s_add_u32 m0, s72, 0x18000\n"
    "s_nop 0\n"
    "buffer_load_dwordx4 v202, s[32:35], s64 offen lds\n"
    "buffer_load_dwordx4 v203, s[32:35], s64 offen offset:1024 lds\n"
    "buffer_load_dwordx4 v204, s[32:35], s64 offen offset:2048 lds\n"
    "s_add_u32 s64, s64, 0xc000\n"
    "s_mov_b32 s94, 0x3000\n"
    "s_mov_b32 s95, 0x6000\n"
    "s_add_u32 s51, s72, 0x18000\n"
    "v_add_u32 v205, 0x3000, v205\n"
    "v_add_u32 v206, 0x3000, v206\n"
    "v_add_u32 v207, 0x3000, v207\n"
    "ds_read_u16_d16_hi v223, v209 offset:6336\n"
    "ds_read_u16_d16_hi v222, v209 offset:192\n"
    "ds_read_b128 v[160:163], v200 offset:24576\n"
    "v_add_f32 v121, v121, v58\n"
    "ds_read_b128 v[232:235], v205 offset:24576\n"
    "v_add_f32 v122, v122, v59\n"
    "ds_read_b128 v[164:167], v200 offset:25600\n"
    "ds_read_b128 v[236:239], v206 offset:24576\n"
    "v_add_f32 v125, v125, v60\n"
    "v_add_f32 v126, v126, v61\n"
    "v_add_f32 v125, v125, v62\n"
    "v_add_f32 v126, v126, v63\n"
    "ds_read_b64 v[228:229], v210 offset:64\n"
    "ds_read2st64_b32 v[240:241], v207 offset0:96 offset1:99\n"
    "ds_read_b128 v[168:171], v200 offset:26624\n"
    "ds_read2st64_b32 v[242:243], v207 offset0:102 offset1:105\n"
    "ds_read2st64_b32 v[244:245], v207 offset0:108 offset1:111\n"
    "ds_read2st64_b32 v[246:247], v207 offset0:114 offset1:117\n"
    "v_cvt_pk_bf16_f32 v140, v44, v45\n"
    "v_cvt_pk_bf16_f32 v141, v46, v47\n"
    "s_waitcnt lgkmcnt(6)\n"
    "v_mul_f32 v226, s75, v223\n"
    "ds_read_b128 v[172:175], v200 offset:27648\n"
    "v_fma_f32 v227, -v222, v226, v217\n"
    "v_cvt_f32_i32 v232, v232\n"
    "v_cvt_f32_i32 v233, v233\n"
    "v_cvt_f32_i32 v234, v234\n"
    "v_cvt_f32_i32 v235, v235\n"
    "s_nop 7\n"
    ".p2align 6\n"
    "LLOOP1:\n"
    "v_mfma_f32_16x16x32_bf16 v[64:67], v[160:163], a[192:195], v[96:99]\n"
    "ds_read_b128 v[176:179], v200 offset:28672\n"
    "v_cvt_f32_i32 v236, v236\n"
    "v_mfma_f32_16x16x32_bf16 v[64:67], v[164:167], a[196:199], v[64:67]\n"
    "s_waitcnt lgkmcnt(1)\n"
    "v_cvt_f32_i32 v237, v237\n"
    "v_cvt_f32_i32 v238, v238\n"
    "v_mfma_f32_16x16x32_bf16 v[64:67], v[168:171], a[200:203], v[64:67]\n"
    "v_cvt_f32_i32 v239, v239\n"
    "v_mul_f32_dpp v232, v226, v232 row_newbcast:0 row_mask:0xf bank_mask:0xf\n"
    "v_mfma_f32_16x16x32_bf16 v[68:71], v[168:171], a[224:227], v[100:103]\n"
    "v_mul_f32_dpp v233, v226, v233 row_newbcast:1 row_mask:0xf bank_mask:0xf\n"
    "v_mul_f32_dpp v234, v226, v234 row_newbcast:2 row_mask:0xf bank_mask:0xf\n"
    "v_mfma_f32_16x16x32_bf16 v[68:71], v[164:167], a[220:223], v[68:71]\n"
    "ds_read_b128 v[180:183], v200 offset:29696\n"
    "v_mul_f32_dpp v235, v226, v235 row_newbcast:3 row_mask:0xf bank_mask:0xf\n"
    "v_mfma_f32_16x16x32_bf16 v[68:71], v[160:163], a[216:219], v[68:71]\n"
    "v_mul_f32_dpp v236, v226, v236 row_newbcast:4 row_mask:0xf bank_mask:0xf\n"
    "v_mul_f32_dpp v237, v226, v237 row_newbcast:5 row_mask:0xf bank_mask:0xf\n"
    "v_mfma_f32_16x16x32_bf16 v[72:75], v[160:163], a[240:243], v[104:107]\n"
    "v_mul_f32_dpp v238, v226, v238 row_newbcast:6 row_mask:0xf bank_mask:0xf\n"
    "v_mul_f32_dpp v239, v226, v239 row_newbcast:7 row_mask:0xf bank_mask:0xf\n"
    "v_mfma_f32_16x16x32_bf16 v[72:75], v[164:167], a[244:247], v[72:75]\n"
    "v_add_f32_dpp v232, v227, v232 row_newbcast:0 row_mask:0xf bank_mask:0xf\n"
    "v_add_f32_dpp v233, v227, v233 row_newbcast:1 row_mask:0xf bank_mask:0xf\n"
    "v_mfma_f32_16x16x32_bf16 v[72:75], v[168:171], a[248:251], v[72:75]\n"
    "v_add_f32_dpp v234, v227, v234 row_newbcast:2 row_mask:0xf bank_mask:0xf\n"
    "v_add_f32_dpp v235, v227, v235 row_newbcast:3 row_mask:0xf bank_mask:0xf\n"
    "v_mfma_f32_16x16x32_bf16 v[76:79], v[168:171], v[16:19], v[108:111]\n"
    "v_add_f32_dpp v236, v227, v236 row_newbcast:4 row_mask:0xf bank_mask:0xf\n"
    "v_add_f32_dpp v237, v227, v237 row_newbcast:5 row_mask:0xf bank_mask:0xf\n"
    "v_mfma_f32_16x16x32_bf16 v[76:79], v[164:167], v[12:15], v[76:79]\n"
    "v_add_f32_dpp v238, v227, v238 row_newbcast:6 row_mask:0xf bank_mask:0xf\n"
    "v_add_f32_dpp v239, v227, v239 row_newbcast:7 row_mask:0xf bank_mask:0xf\n"
    "v_mfma_f32_16x16x32_bf16 v[76:79], v[160:163], v[8:11], v[76:79]\n"
    "s_waitcnt lgkmcnt(1)\n"
    "v_cvt_pk_bf16_f32 v232, v232, v233\n"
    "v_cvt_f32_i32 v240, v240\n"
    "v_mfma_f32_16x16x32_bf16 v[76:79], v[172:175], v[20:23], v[76:79]\n"
    "ds_read_b128 v[160:163], v200 offset:33792\n"
    "v_cvt_pk_bf16_f32 v233, v234, v235\n"
    "v_mfma_f32_16x16x32_bf16 v[76:79], v[176:179], v[24:27], v[76:79]\n"
    "s_waitcnt lgkmcnt(1)\n"
    "v_cvt_f32_i32 v241, v241\n"
    "v_cvt_pk_bf16_f32 v234, v236, v237\n"
    "v_mfma_f32_16x16x32_bf16 v[76:79], v[180:183], v[28:31], v[76:79]\n"
    "v_cvt_f32_i32 v242, v242\n"
    "v_cvt_pk_bf16_f32 v235, v238, v239\n"
    "v_mfma_f32_16x16x32_bf16 v[72:75], v[180:183], v[4:7], v[72:75]\n"
    "ds_read_b128 v[164:167], v200 offset:34816\n"
    "v_cvt_f32_i32 v243, v243\n"
    "v_mfma_f32_16x16x32_bf16 v[72:75], v[176:179], v[0:3], v[72:75]\n"
    "v_cvt_f32_i32 v244, v244\n"
    "v_cvt_f32_i32 v245, v245\n"
    "v_mfma_f32_16x16x32_bf16 v[72:75], v[172:175], a[252:255], v[72:75]\n"
    "buffer_store_dwordx4 v[232:235], v211, s[36:39], s62 offen sc1\n"
    "v_cvt_f32_i32 v246, v246\n"
    "v_cvt_f32_i32 v247, v247\n"
    "v_mul_f32_dpp v240, v228, v240 row_newbcast:0 row_mask:0xf bank_mask:0xf\n"
    "v_mfma_f32_16x16x32_bf16 v[68:71], v[172:175], a[228:231], v[68:71]\n"
    "ds_read_b128 v[168:171], v200 offset:35840\n"
    "v_mul_f32_dpp v241, v228, v241 row_newbcast:1 row_mask:0xf bank_mask:0xf\n"
    "v_mfma_f32_16x16x32_bf16 v[68:71], v[176:179], a[232:235], v[68:71]\n"
    "v_mul_f32_dpp v242, v228, v242 row_newbcast:2 row_mask:0xf bank_mask:0xf\n"
    "v_mul_f32_dpp v243, v228, v243 row_newbcast:3 row_mask:0xf bank_mask:0xf\n"
    "v_mfma_f32_16x16x32_bf16 v[68:71], v[180:183], a[236:239], v[68:71]\n"
    "v_mul_f32_dpp v244, v228, v244 row_newbcast:4 row_mask:0xf bank_mask:0xf\n"
    "v_mul_f32_dpp v245, v228, v245 row_newbcast:5 row_mask:0xf bank_mask:0xf\n"
    "v_mfma_f32_16x16x32_bf16 v[64:67], v[180:183], a[212:215], v[64:67]\n"
    "v_mul_f32_dpp v246, v228, v246 row_newbcast:6 row_mask:0xf bank_mask:0xf\n"
    "v_mul_f32_dpp v247, v228, v247 row_newbcast:7 row_mask:0xf bank_mask:0xf\n"
    "v_mfma_f32_16x16x32_bf16 v[64:67], v[176:179], a[208:211], v[64:67]\n"
    "v_add_f32_dpp v240, v229, v240 row_newbcast:0 row_mask:0xf bank_mask:0xf\n"
    "v_add_f32_dpp v241, v229, v241 row_newbcast:1 row_mask:0xf bank_mask:0xf\n"
    "v_mfma_f32_16x16x32_bf16 v[64:67], v[172:175], a[204:207], v[64:67]\n"
    "s_waitcnt lgkmcnt(1)\n"
    "v_add_f32_dpp v242, v229, v242 row_newbcast:2 row_mask:0xf bank_mask:0xf\n"
    "v_add_f32_dpp v243, v229, v243 row_newbcast:3 row_mask:0xf bank_mask:0xf\n"
    "v_mfma_f32_16x16x32_bf16 v[80:83], v[160:163], a[204:207], v[96:99]\n"
    "ds_read_b128 v[172:175], v200 offset:30720\n"
    "v_add_f32_dpp v244, v229, v244 row_newbcast:4 row_mask:0xf bank_mask:0xf\n"
    "v_mfma_f32_16x16x32_bf16 v[80:83], v[164:167], a[208:211], v[80:83]\n"
    "s_waitcnt lgkmcnt(1)\n"
    "v_add_f32_dpp v245, v229, v245 row_newbcast:5 row_mask:0xf bank_mask:0xf\n"
    "v_add_f32_dpp v246, v229, v246 row_newbcast:6 row_mask:0xf bank_mask:0xf\n"
    "v_mfma_f32_16x16x32_bf16 v[80:83], v[168:171], a[212:215], v[80:83]\n"
    "v_add_f32_dpp v247, v229, v247 row_newbcast:7 row_mask:0xf bank_mask:0xf\n"
    "v_cvt_pk_bf16_f32 v240, v240, v241\n"
    "v_mfma_f32_16x16x32_bf16 v[84:87], v[168:171], a[236:239], v[100:103]\n"
    "ds_read_b128 v[176:179], v200 offset:31744\n"
    "v_cvt_pk_bf16_f32 v241, v242, v243\n"
    "v_mfma_f32_16x16x32_bf16 v[84:87], v[164:167], a[232:235], v[84:87]\n"
    "v_cvt_pk_bf16_f32 v242, v244, v245\n"
    "v_cvt_pk_bf16_f32 v243, v246, v247\n"
    "v_mfma_f32_16x16x32_bf16 v[84:87], v[160:163], a[228:231], v[84:87]\n"
    "buffer_store_dwordx4 v[240:243], v212, s[40:43], s63 offen sc1\n"
    "v_cvt_pk_bf16_f32 v136, v40, v41\n"
    "v_cvt_pk_bf16_f32 v137, v42, v43\n"
    "v_cvt_pk_bf16_f32 v132, v36, v37\n"
    "v_mfma_f32_16x16x32_bf16 v[88:91], v[160:163], a[252:255], v[104:107]\n"
    "ds_read_b128 v[180:183], v200 offset:32768\n"
    "v_cvt_pk_bf16_f32 v133, v38, v39\n"
    "v_mfma_f32_16x16x32_bf16 v[88:91], v[164:167], v[0:3], v[88:91]\n"
    "v_cvt_pk_bf16_f32 v128, v32, v33\n"
    "v_cvt_pk_bf16_f32 v129, v34, v35\n"
    "v_mfma_f32_16x16x32_bf16 v[88:91], v[168:171], v[4:7], v[88:91]\n"
    "v_exp_f32 v64, v64\n"
    "v_mfma_f32_16x16x32_bf16 v[92:95], v[168:171], v[28:31], v[108:111]\n"
    "v_exp_f32 v65, v65\n"
    "v_mfma_f32_16x16x32_bf16 v[92:95], v[164:167], v[24:27], v[92:95]\n"
    "v_exp_f32 v66, v66\n"
    "v_mfma_f32_16x16x32_bf16 v[92:95], v[160:163], v[20:23], v[92:95]\n"
    "s_waitcnt lgkmcnt(1)\n"
    "v_exp_f32 v67, v67\n"
    "v_mfma_f32_16x16x32_bf16 v[92:95], v[172:175], v[8:11], v[92:95]\n"
    "ds_read_b128 v[160:163], v200 offset:36864\n"
    "v_cvt_pk_bf16_f32 v142, v60, v61\n"
    "v_mfma_f32_16x16x32_bf16 v[92:95], v[176:179], v[12:15], v[92:95]\n"
    "s_waitcnt lgkmcnt(1)\n"
    "s_add_u32 s65, s60, s92\n"
    "v_cvt_pk_bf16_f32 v143, v62, v63\n"
    "v_mfma_f32_16x16x32_bf16 v[92:95], v[180:183], v[16:19], v[92:95]\n"
    "s_add_u32 m0, s90, 0\n"
    "v_exp_f32 v68, v68\n"
    "v_mfma_f32_16x16x32_bf16 v[88:91], v[180:183], a[248:251], v[88:91]\n"
    "ds_read_b128 v[164:167], v200 offset:37888\n"
    "v_cvt_pk_bf16_f32 v138, v56, v57\n"
    "v_mfma_f32_16x16x32_bf16 v[88:91], v[176:179], a[244:247], v[88:91]\n"
    "buffer_load_dwordx4 v200, s[36:39], s65 offen sc1 lds\n"
    "v_mfma_f32_16x16x32_bf16 v[88:91], v[172:175], a[240:243], v[88:91]\n"
    "v_exp_f32 v69, v69\n"
    "v_mfma_f32_16x16x32_bf16 v[84:87], v[172:175], a[216:219], v[84:87]\n"
    "ds_read_b128 v[168:171], v200 offset:38912\n"
    "v_cvt_pk_bf16_f32 v139, v58, v59\n"
    "v_mfma_f32_16x16x32_bf16 v[84:87], v[176:179], a[220:223], v[84:87]\n"
    "v_exp_f32 v70, v70\n"
    "v_mfma_f32_16x16x32_bf16 v[84:87], v[180:183], a[224:227], v[84:87]\n"
    "buffer_load_dwordx4 v200, s[36:39], s65 offen offset:1024 sc1 lds\n"
    "v_mfma_f32_16x16x32_bf16 v[80:83], v[180:183], a[200:203], v[80:83]\n"
    "v_exp_f32 v71, v71\n"
    "v_mfma_f32_16x16x32_bf16 v[80:83], v[176:179], a[196:199], v[80:83]\n"
    "v_exp_f32 v72, v72\n"
    "v_mfma_f32_16x16x32_bf16 v[80:83], v[172:175], a[192:195], v[80:83]\n"
    "s_waitcnt lgkmcnt(1)\n"
    "v_cvt_pk_bf16_f32 v134, v52, v53\n"
    "v_cvt_pk_bf16_f32 v135, v54, v55\n"
    "v_mfma_f32_16x16x32_bf16 v[32:35], v[160:163], a[192:195], v[96:99]\n"
    "ds_read_b128 v[172:175], v200 offset:39936\n"
    "v_cvt_pk_bf16_f32 v130, v48, v49\n"
    "v_mfma_f32_16x16x32_bf16 v[32:35], v[164:167], a[196:199], v[32:35]\n"
    "s_waitcnt lgkmcnt(1)\n"
    "buffer_load_dwordx4 v200, s[36:39], s65 offen offset:2048 sc1 lds\n"
    "v_mfma_f32_16x16x32_bf16 v[32:35], v[168:171], a[200:203], v[32:35]\n"
    "v_exp_f32 v73, v73\n"
    "v_mfma_f32_16x16x32_bf16 v[36:39], v[168:171], a[224:227], v[100:103]\n"
    "ds_read_b128 v[176:179], v200 offset:40960\n"
    "v_cvt_pk_bf16_f32 v131, v50, v51\n"
    "v_mfma_f32_16x16x32_bf16 v[36:39], v[164:167], a[220:223], v[36:39]\n"
    "v_exp_f32 v74, v74\n"
    "v_mfma_f32_16x16x32_bf16 v[36:39], v[160:163], a[216:219], v[36:39]\n"
    "buffer_load_dwordx4 v200, s[36:39], s65 offen offset:3072 sc1 lds\n"
    "v_mfma_f32_16x16x32_bf16 v[40:43], v[160:163], a[240:243], v[104:107]\n"
    "s_add_u32 s65, s65, 0x1000\n"
    "ds_read_b128 v[180:183], v200 offset:41984\n"
    "v_cvt_pk_bf16_f32 v144, v64, v65\n"
    "v_mfma_f32_16x16x32_bf16 v[40:43], v[164:167], a[244:247], v[40:43]\n"
    "s_add_u32 m0, s90, 0x1000\n"
    "v_exp_f32 v75, v75\n"
    "v_mfma_f32_16x16x32_bf16 v[40:43], v[168:171], a[248:251], v[40:43]\n"
    "v_exp_f32 v76, v76\n"
    "v_mfma_f32_16x16x32_bf16 v[44:47], v[168:171], v[16:19], v[108:111]\n"
    "buffer_load_dwordx4 v200, s[36:39], s65 offen sc1 lds\n"
    "v_mfma_f32_16x16x32_bf16 v[44:47], v[164:167], v[12:15], v[44:47]\n"
    "v_exp_f32 v77, v77\n"
    "v_mfma_f32_16x16x32_bf16 v[44:47], v[160:163], v[8:11], v[44:47]\n"
    "s_waitcnt lgkmcnt(1)\n"
    "v_exp_f32 v78, v78\n"
    "v_mfma_f32_16x16x32_bf16 v[44:47], v[172:175], v[20:23], v[44:47]\n"
    "ds_read_b128 v[160:163], v200 offset:46080\n"
    "v_cvt_pk_bf16_f32 v145, v66, v67\n"
    "v_mfma_f32_16x16x32_bf16 v[44:47], v[176:179], v[24:27], v[44:47]\n"
    "s_waitcnt lgkmcnt(1)\n"
    "buffer_load_dwordx4 v200, s[36:39], s65 offen offset:1024 sc1 lds\n"
    "v_mfma_f32_16x16x32_bf16 v[44:47], v[180:183], v[28:31], v[44:47]\n"
    "v_exp_f32 v79, v79\n"
    "v_mfma_f32_16x16x32_bf16 v[40:43], v[180:183], v[4:7], v[40:43]\n"
    "ds_read_b128 v[164:167], v200 offset:47104\n"
    "v_cvt_pk_bf16_f32 v148, v68, v69\n"
    "v_mfma_f32_16x16x32_bf16 v[40:43], v[176:179], v[0:3], v[40:43]\n"
    "v_exp_f32 v80, v80\n"
    "v_mfma_f32_16x16x32_bf16 v[40:43], v[172:175], a[252:255], v[40:43]\n"
    "buffer_load_dwordx4 v200, s[36:39], s65 offen offset:2048 sc1 lds\n"
    "v_mfma_f32_16x16x32_bf16 v[36:39], v[172:175], a[228:231], v[36:39]\n"
    "ds_read_b128 v[168:171], v200 offset:48128\n"
    "v_cvt_pk_bf16_f32 v149, v70, v71\n"
    "v_mfma_f32_16x16x32_bf16 v[36:39], v[176:179], a[232:235], v[36:39]\n"
    "s_add_u32 m0, s71, 0x12000\n"
    "v_exp_f32 v81, v81\n"
    "v_mfma_f32_16x16x32_bf16 v[36:39], v[180:183], a[236:239], v[36:39]\n"
    "v_exp_f32 v82, v82\n"
    "v_mfma_f32_16x16x32_bf16 v[32:35], v[180:183], a[212:215], v[32:35]\n"
    "buffer_load_dwordx4 v200, s[40:43], s61 offen sc1 lds\n"
    "v_mfma_f32_16x16x32_bf16 v[32:35], v[176:179], a[208:211], v[32:35]\n"
    "v_exp_f32 v83, v83\n"
    "v_mfma_f32_16x16x32_bf16 v[32:35], v[172:175], a[204:207], v[32:35]\n"
    "s_waitcnt lgkmcnt(1)\n"
    "v_exp_f32 v84, v84\n"
    "v_mfma_f32_16x16x32_bf16 v[48:51], v[160:163], a[204:207], v[96:99]\n"
    "ds_read_b128 v[172:175], v200 offset:43008\n"
    "v_cvt_pk_bf16_f32 v152, v72, v73\n"
    "v_mfma_f32_16x16x32_bf16 v[48:51], v[164:167], a[208:211], v[48:51]\n"
    "s_waitcnt lgkmcnt(1)\n"
    "buffer_load_dwordx4 v200, s[40:43], s61 offen offset:1024 sc1 lds\n"
    "v_mfma_f32_16x16x32_bf16 v[48:51], v[168:171], a[212:215], v[48:51]\n"
    "v_exp_f32 v85, v85\n"
    "v_mfma_f32_16x16x32_bf16 v[52:55], v[168:171], a[236:239], v[100:103]\n"
    "ds_read_b128 v[176:179], v200 offset:44032\n"
    "v_cvt_pk_bf16_f32 v153, v74, v75\n"
    "v_mfma_f32_16x16x32_bf16 v[52:55], v[164:167], a[232:235], v[52:55]\n"
    "v_exp_f32 v86, v86\n"
    "v_mfma_f32_16x16x32_bf16 v[52:55], v[160:163], a[228:231], v[52:55]\n"
    "buffer_load_dwordx4 v200, s[40:43], s61 offen offset:2048 sc1 lds\n"
    "v_mfma_f32_16x16x32_bf16 v[56:59], v[160:163], a[252:255], v[104:107]\n"
    "ds_read_b128 v[180:183], v200 offset:45056\n"
    "v_cvt_pk_bf16_f32 v156, v76, v77\n"
    "v_mfma_f32_16x16x32_bf16 v[56:59], v[164:167], v[0:3], v[56:59]\n"
    "v_exp_f32 v87, v87\n"
    "v_mfma_f32_16x16x32_bf16 v[56:59], v[168:171], v[4:7], v[56:59]\n"
    "v_exp_f32 v88, v88\n"
    "v_mfma_f32_16x16x32_bf16 v[60:63], v[168:171], v[28:31], v[108:111]\n"
    "buffer_load_dwordx4 v200, s[40:43], s61 offen offset:3072 sc1 lds\n"
    "v_mfma_f32_16x16x32_bf16 v[60:63], v[164:167], v[24:27], v[60:63]\n"
    "s_add_u32 s65, s61, 0x1000\n"
    "v_exp_f32 v89, v89\n"
    "v_mfma_f32_16x16x32_bf16 v[60:63], v[160:163], v[20:23], v[60:63]\n"
    "s_waitcnt lgkmcnt(1)\n"
    "s_add_u32 m0, s71, 0x13000\n"
    "v_cvt_pk_bf16_f32 v157, v78, v79\n"
    "v_mfma_f32_16x16x32_bf16 v[60:63], v[172:175], v[8:11], v[60:63]\n"
    "ds_read_b128 v[160:163], v201\n"
    "v_cvt_pk_bf16_f32 v146, v80, v81\n"
    "v_mfma_f32_16x16x32_bf16 v[60:63], v[176:179], v[12:15], v[60:63]\n"
    "s_waitcnt lgkmcnt(1)\n"
    "buffer_load_dwordx4 v200, s[40:43], s65 offen sc1 lds\n"
    "v_mfma_f32_16x16x32_bf16 v[60:63], v[180:183], v[16:19], v[60:63]\n"
    "v_exp_f32 v90, v90\n"
    "v_mfma_f32_16x16x32_bf16 v[56:59], v[180:183], a[248:251], v[56:59]\n"
    "ds_read_b128 v[164:167], v201 offset:1024\n"
    "v_cvt_pk_bf16_f32 v147, v82, v83\n"
    "v_mfma_f32_16x16x32_bf16 v[56:59], v[176:179], a[244:247], v[56:59]\n"
    "v_exp_f32 v91, v91\n"
    "v_mfma_f32_16x16x32_bf16 v[56:59], v[172:175], a[240:243], v[56:59]\n"
    "buffer_load_dwordx4 v200, s[40:43], s65 offen offset:1024 sc1 lds\n"
    "v_mfma_f32_16x16x32_bf16 v[52:55], v[172:175], a[216:219], v[52:55]\n"
    "ds_read_b128 v[168:171], v201 offset:2048\n"
    "v_cvt_pk_bf16_f32 v150, v84, v85\n"
    "v_mfma_f32_16x16x32_bf16 v[52:55], v[176:179], a[220:223], v[52:55]\n"
    "v_exp_f32 v92, v92\n"
    "v_mfma_f32_16x16x32_bf16 v[52:55], v[180:183], a[224:227], v[52:55]\n"
    "v_exp_f32 v93, v93\n"
    "v_mfma_f32_16x16x32_bf16 v[48:51], v[180:183], a[200:203], v[48:51]\n"
    "v_cvt_pk_bf16_f32 v151, v86, v87\n"
    "v_cvt_pk_bf16_f32 v154, v88, v89\n"
    "v_mfma_f32_16x16x32_bf16 v[48:51], v[176:179], a[196:199], v[48:51]\n"
    "v_exp_f32 v94, v94\n"
    "v_mfma_f32_16x16x32_bf16 v[48:51], v[172:175], a[192:195], v[48:51]\n"
    "s_waitcnt lgkmcnt(1)\n"
    "v_cvt_pk_bf16_f32 v155, v90, v91\n"
    "v_cvt_pk_bf16_f32 v158, v92, v93\n"
    "v_mfma_f32_16x16x32_bf16 a[0:3], v[160:163], v[128:131], a[0:3]\n"
    "ds_read_b128 v[172:175], v201 offset:3072\n"
    "ds_read_b128 v[176:179], v201 offset:4096\n"
    "v_mfma_f32_16x16x32_bf16 a[0:3], v[164:167], v[144:147], a[0:3]\n"
    "v_exp_f32 v95, v95\n"
    "v_mfma_f32_16x16x32_bf16 a[4:7], v[160:163], v[132:135], a[4:7]\n"
    "v_cvt_pk_bf16_f32 v159, v94, v95\n"
    "v_add_f32 v113, v113, v64\n"
    "v_mfma_f32_16x16x32_bf16 a[4:7], v[164:167], v[148:151], a[4:7]\n"
    "v_exp_f32 v32, v32\n"
    "v_mfma_f32_16x16x32_bf16 a[8:11], v[160:163], v[136:139], a[8:11]\n"
    "ds_read_b128 v[180:183], v201 offset:5120\n"
    "v_add_f32 v114, v114, v65\n"
    "v_mfma_f32_16x16x32_bf16 a[8:11], v[164:167], v[152:155], a[8:11]\n"
    "v_exp_f32 v33, v33\n"
    "v_mfma_f32_16x16x32_bf16 a[12:15], v[160:163], v[140:143], a[12:15]\n"
    "v_exp_f32 v34, v34\n"
    "v_mfma_f32_16x16x32_bf16 a[12:15], v[164:167], v[156:159], a[12:15]\n"
    "s_waitcnt lgkmcnt(2)\n"
    "v_exp_f32 v35, v35\n"
    "v_mfma_f32_16x16x32_bf16 a[28:31], v[168:171], v[140:143], a[28:31]\n"
    "ds_read_b128 v[160:163], v201 offset:6144\n"
    "v_add_f32 v113, v113, v66\n"
    "v_mfma_f32_16x16x32_bf16 a[28:31], v[172:175], v[156:159], a[28:31]\n"
    "v_exp_f32 v36, v36\n"
    "v_mfma_f32_16x16x32_bf16 a[24:27], v[172:175], v[152:155], a[24:27]\n"
    "v_exp_f32 v37, v37\n"
    "v_mfma_f32_16x16x32_bf16 a[24:27], v[168:171], v[136:139], a[24:27]\n"
    "v_exp_f32 v38, v38\n"
    "v_mfma_f32_16x16x32_bf16 a[20:23], v[168:171], v[132:135], a[20:23]\n"
    "ds_read_b128 v[164:167], v201 offset:7168\n"
    "v_add_f32 v114, v114, v67\n"
    "v_mfma_f32_16x16x32_bf16 a[20:23], v[172:175], v[148:151], a[20:23]\n"
    "v_exp_f32 v39, v39\n"
    "v_mfma_f32_16x16x32_bf16 a[16:19], v[172:175], v[144:147], a[16:19]\n"
    "v_exp_f32 v40, v40\n"
    "v_mfma_f32_16x16x32_bf16 a[16:19], v[168:171], v[128:131], a[16:19]\n"
    "s_waitcnt lgkmcnt(2)\n"
    "v_exp_f32 v41, v41\n"
    "v_mfma_f32_16x16x32_bf16 a[32:35], v[176:179], v[128:131], a[32:35]\n"
    "ds_read_b128 v[168:171], v201 offset:8192\n"
    "v_add_f32 v117, v117, v68\n"
    "v_mfma_f32_16x16x32_bf16 a[32:35], v[180:183], v[144:147], a[32:35]\n"
    "v_exp_f32 v42, v42\n"
    "v_mfma_f32_16x16x32_bf16 a[36:39], v[180:183], v[148:151], a[36:39]\n"
    "v_exp_f32 v43, v43\n"
    "v_mfma_f32_16x16x32_bf16 a[36:39], v[176:179], v[132:135], a[36:39]\n"
    "v_exp_f32 v44, v44\n"
    "v_mfma_f32_16x16x32_bf16 a[40:43], v[176:179], v[136:139], a[40:43]\n"
    "ds_read_b128 v[172:175], v201 offset:9216\n"
    "v_add_f32 v118, v118, v69\n"
    "v_mfma_f32_16x16x32_bf16 a[40:43], v[180:183], v[152:155], a[40:43]\n"
    "v_exp_f32 v45, v45\n"
    "v_mfma_f32_16x16x32_bf16 a[44:47], v[180:183], v[156:159], a[44:47]\n"
    "v_exp_f32 v46, v46\n"
    "v_mfma_f32_16x16x32_bf16 a[44:47], v[176:179], v[140:143], a[44:47]\n"
    "s_waitcnt lgkmcnt(2)\n"
    "v_exp_f32 v47, v47\n"
    "v_mfma_f32_16x16x32_bf16 a[60:63], v[160:163], v[140:143], a[60:63]\n"
    "ds_read_b128 v[176:179], v201 offset:10240\n"
    "v_add_f32 v117, v117, v70\n"
    "v_mfma_f32_16x16x32_bf16 a[60:63], v[164:167], v[156:159], a[60:63]\n"
    "v_exp_f32 v48, v48\n"
    "v_mfma_f32_16x16x32_bf16 a[56:59], v[164:167], v[152:155], a[56:59]\n"
    "v_exp_f32 v49, v49\n"
    "v_mfma_f32_16x16x32_bf16 a[56:59], v[160:163], v[136:139], a[56:59]\n"
    "v_exp_f32 v50, v50\n"
    "v_mfma_f32_16x16x32_bf16 a[52:55], v[160:163], v[132:135], a[52:55]\n"
    "ds_read_b128 v[180:183], v201 offset:11264\n"
    "v_add_f32 v113, v113, v32\n"
    "v_mfma_f32_16x16x32_bf16 a[52:55], v[164:167], v[148:151], a[52:55]\n"
    "v_exp_f32 v51, v51\n"
    "v_mfma_f32_16x16x32_bf16 a[48:51], v[164:167], v[144:147], a[48:51]\n"
    "v_exp_f32 v52, v52\n"
    "v_mfma_f32_16x16x32_bf16 a[48:51], v[160:163], v[128:131], a[48:51]\n"
    "s_waitcnt lgkmcnt(2)\n"
    "v_add_f32 v118, v118, v71\n"
    "v_add_f32 v114, v114, v33\n"
    "v_mfma_f32_16x16x32_bf16 a[64:67], v[168:171], v[128:131], a[64:67]\n"
    "ds_read_b128 v[160:163], v201 offset:12288\n"
    "v_add_f32 v121, v121, v72\n"
    "v_mfma_f32_16x16x32_bf16 a[64:67], v[172:175], v[144:147], a[64:67]\n"
    "v_add_f32 v113, v113, v34\n"
    "v_add_f32 v122, v122, v73\n"
    "v_mfma_f32_16x16x32_bf16 a[68:71], v[172:175], v[148:151], a[68:71]\n"
    "v_exp_f32 v53, v53\n"
    "v_mfma_f32_16x16x32_bf16 a[68:71], v[168:171], v[132:135], a[68:71]\n"
    "s_add_u32 m0, s51, s95\n"
    "v_add_f32 v114, v114, v35\n"
    "v_add_f32 v121, v121, v74\n"
    "v_mfma_f32_16x16x32_bf16 a[72:75], v[168:171], v[136:139], a[72:75]\n"
    "ds_read_b128 v[164:167], v201 offset:13312\n"
    "v_add_f32 v117, v117, v36\n"
    "v_mfma_f32_16x16x32_bf16 a[72:75], v[172:175], v[152:155], a[72:75]\n"
    "buffer_load_dwordx4 v202, s[32:35], s64 offen lds\n"
    "v_mfma_f32_16x16x32_bf16 a[76:79], v[172:175], v[156:159], a[76:79]\n"
    "v_exp_f32 v54, v54\n"
    "v_mfma_f32_16x16x32_bf16 a[76:79], v[168:171], v[140:143], a[76:79]\n"
    "s_waitcnt lgkmcnt(2)\n"
    "v_add_f32 v122, v122, v75\n"
    "v_add_f32 v118, v118, v37\n"
    "v_mfma_f32_16x16x32_bf16 a[92:95], v[176:179], v[140:143], a[92:95]\n"
    "ds_read_b128 v[168:171], v201 offset:14336\n"
    "v_add_f32 v125, v125, v76\n"
    "v_mfma_f32_16x16x32_bf16 a[92:95], v[180:183], v[156:159], a[92:95]\n"
    "buffer_load_dwordx4 v203, s[32:35], s64 offen offset:1024 lds\n"
    "v_mfma_f32_16x16x32_bf16 a[88:91], v[180:183], v[152:155], a[88:91]\n"
    "v_add_f32 v117, v117, v38\n"
    "v_add_f32 v126, v126, v77\n"
    "v_mfma_f32_16x16x32_bf16 a[88:91], v[176:179], v[136:139], a[88:91]\n"
    "v_exp_f32 v55, v55\n"
    "v_mfma_f32_16x16x32_bf16 a[84:87], v[176:179], v[132:135], a[84:87]\n"
    "ds_read_b128 v[172:175], v201 offset:15360\n"
    "v_add_f32 v118, v118, v39\n"
    "v_mfma_f32_16x16x32_bf16 a[84:87], v[180:183], v[148:151], a[84:87]\n"
    "buffer_load_dwordx4 v204, s[32:35], s64 offen offset:2048 lds\n"
    "v_mfma_f32_16x16x32_bf16 a[80:83], v[180:183], v[144:147], a[80:83]\n"
    "v_add_f32 v125, v125, v78\n"
    "v_add_f32 v121, v121, v40\n"
    "v_mfma_f32_16x16x32_bf16 a[80:83], v[176:179], v[128:131], a[80:83]\n"
    "s_waitcnt lgkmcnt(2)\n"
    "v_exp_f32 v56, v56\n"
    "v_mfma_f32_16x16x32_bf16 a[96:99], v[160:163], v[128:131], a[96:99]\n"
    "s_mov_b32 s95, s94\n"
    "ds_read_b128 v[176:179], v201 offset:16384\n"
    "v_add_f32 v126, v126, v79\n"
    "v_mfma_f32_16x16x32_bf16 a[96:99], v[164:167], v[144:147], a[96:99]\n"
    "s_sub_u32 s94, s94, 0x3000\n"
    "v_add_f32 v122, v122, v41\n"
    "v_add_f32 v113, v113, v80\n"
    "v_mfma_f32_16x16x32_bf16 a[100:103], v[164:167], v[148:151], a[100:103]\n"
    "s_min_u32 s94, s94, 0x6000\n"
    "v_add_f32 v121, v121, v42\n"
    "v_add_f32 v114, v114, v81\n"
    "v_mfma_f32_16x16x32_bf16 a[100:103], v[160:163], v[132:135], a[100:103]\n"
    "s_sub_u32 s93, s94, s95\n"
    "v_exp_f32 v57, v57\n"
    "v_mfma_f32_16x16x32_bf16 a[104:107], v[160:163], v[136:139], a[104:107]\n"
    "ds_read_b128 v[180:183], v201 offset:17408\n"
    "v_add_f32 v122, v122, v43\n"
    "v_mfma_f32_16x16x32_bf16 a[104:107], v[164:167], v[152:155], a[104:107]\n"
    "v_add_f32 v113, v113, v82\n"
    "v_add_f32 v125, v125, v44\n"
    "v_mfma_f32_16x16x32_bf16 a[108:111], v[164:167], v[156:159], a[108:111]\n"
    "v_exp_f32 v58, v58\n"
    "v_mfma_f32_16x16x32_bf16 a[108:111], v[160:163], v[140:143], a[108:111]\n"
    "s_waitcnt lgkmcnt(2)\n"
    "v_add_f32 v114, v114, v83\n"
    "v_add_f32 v126, v126, v45\n"
    "v_mfma_f32_16x16x32_bf16 a[124:127], v[168:171], v[140:143], a[124:127]\n"
    "ds_read_b128 v[160:163], v201 offset:18432\n"
    "v_add_f32 v117, v117, v84\n"
    "v_mfma_f32_16x16x32_bf16 a[124:127], v[172:175], v[156:159], a[124:127]\n"
    "v_add_f32 v125, v125, v46\n"
    "v_add_f32 v118, v118, v85\n"
    "v_mfma_f32_16x16x32_bf16 a[120:123], v[172:175], v[152:155], a[120:123]\n"
    "v_exp_f32 v59, v59\n"
    "v_mfma_f32_16x16x32_bf16 a[120:123], v[168:171], v[136:139], a[120:123]\n"
    "v_add_f32 v126, v126, v47\n"
    "v_add_f32 v117, v117, v86\n"
    "v_mfma_f32_16x16x32_bf16 a[116:119], v[168:171], v[132:135], a[116:119]\n"
    "ds_read_b128 v[164:167], v201 offset:19456\n"
    "v_add_f32 v113, v113, v48\n"
    "v_mfma_f32_16x16x32_bf16 a[116:119], v[172:175], v[148:151], a[116:119]\n"
    "v_exp_f32 v60, v60\n"
    "v_mfma_f32_16x16x32_bf16 a[112:115], v[172:175], v[144:147], a[112:115]\n"
    "v_add_u32 v205, s93, v205\n"
    "v_add_f32 v118, v118, v87\n"
    "v_mfma_f32_16x16x32_bf16 a[112:115], v[168:171], v[128:131], a[112:115]\n"
    "s_waitcnt lgkmcnt(2)\n"
    "v_add_f32 v114, v114, v49\n"
    "v_add_u32 v209, 0x180, v209\n"
    "v_mfma_f32_16x16x32_bf16 a[128:131], v[176:179], v[128:131], a[128:131]\n"
    "ds_read_b128 v[168:171], v201 offset:20480\n"
    "v_add_u32 v206, s93, v206\n"
    "v_mfma_f32_16x16x32_bf16 a[128:131], v[180:183], v[144:147], a[128:131]\n"
    "v_add_f32 v121, v121, v88\n"
    "v_add_f32 v113, v113, v50\n"
    "v_mfma_f32_16x16x32_bf16 a[132:135], v[180:183], v[148:151], a[132:135]\n"
    "s_add_u32 s64, s64, 0xc000\n"
    "v_exp_f32 v61, v61\n"
    "v_mfma_f32_16x16x32_bf16 a[132:135], v[176:179], v[132:135], a[132:135]\n"
    "s_add_u32 s62, s62, 0x6000\n"
    "v_add_u32 v210, 0x80, v210\n"
    "v_add_u32 v207, s93, v207\n"
    "v_mfma_f32_16x16x32_bf16 a[136:139], v[176:179], v[136:139], a[136:139]\n"
    "s_add_u32 s63, s63, 0x6000\n"
    "ds_read_b128 v[172:175], v201 offset:21504\n"
    "v_add_f32 v122, v122, v89\n"
    "v_mfma_f32_16x16x32_bf16 a[136:139], v[180:183], v[152:155], a[136:139]\n"
    "s_add_u32 s60, s60, 0x6000\n"
    "v_add_f32 v114, v114, v51\n"
    "v_add_f32 v121, v121, v90\n"
    "v_mfma_f32_16x16x32_bf16 a[140:143], v[180:183], v[156:159], a[140:143]\n"
    "s_add_u32 s61, s61, 0x6000\n"
    "v_add_f32 v117, v117, v52\n"
    "v_add_f32 v122, v122, v91\n"
    "v_mfma_f32_16x16x32_bf16 a[140:143], v[176:179], v[140:143], a[140:143]\n"
    "s_waitcnt lgkmcnt(2)\n"
    "v_exp_f32 v62, v62\n"
    "v_mfma_f32_16x16x32_bf16 a[156:159], v[160:163], v[140:143], a[156:159]\n"
    "ds_read_b128 v[176:179], v201 offset:22528\n"
    "v_add_f32 v118, v118, v53\n"
    "v_mfma_f32_16x16x32_bf16 a[156:159], v[164:167], v[156:159], a[156:159]\n"
    "s_waitcnt vmcnt(3)\n"
    "ds_read_b128 v[180:183], v201 offset:23552\n"
    "v_add_f32 v125, v125, v92\n"
    "v_mfma_f32_16x16x32_bf16 a[152:155], v[164:167], v[152:155], a[152:155]\n"
    "v_add_f32 v117, v117, v54\n"
    "v_add_f32 v126, v126, v93\n"
    "v_mfma_f32_16x16x32_bf16 a[152:155], v[160:163], v[136:139], a[152:155]\n"
    "v_exp_f32 v63, v63\n"
    "v_mfma_f32_16x16x32_bf16 a[148:151], v[160:163], v[132:135], a[148:151]\n"
    "v_add_f32 v118, v118, v55\n"
    "v_add_f32 v125, v125, v94\n"
    "v_mfma_f32_16x16x32_bf16 a[148:151], v[164:167], v[148:151], a[148:151]\n"
    "v_add_f32 v121, v121, v56\n"
    "v_add_f32 v126, v126, v95\n"
    "v_mfma_f32_16x16x32_bf16 a[144:147], v[164:167], v[144:147], a[144:147]\n"
    "v_add_f32 v122, v122, v57\n"
    "v_mfma_f32_16x16x32_bf16 a[144:147], v[160:163], v[128:131], a[144:147]\n"
    "s_waitcnt lgkmcnt(0)\n"
    "v_mfma_f32_16x16x32_bf16 a[160:163], v[168:171], v[128:131], a[160:163]\n"
    "s_barrier\n"
    "v_mfma_f32_16x16x32_bf16 a[160:163], v[172:175], v[144:147], a[160:163]\n"
    "ds_read_u16_d16_hi v223, v209 offset:6144\n"
    "ds_read_u16_d16_hi v222, v209\n"
    "v_mfma_f32_16x16x32_bf16 a[164:167], v[172:175], v[148:151], a[164:167]\n"
    "ds_read_b128 v[160:163], v200\n"
    "v_add_f32 v121, v121, v58\n"
    "v_mfma_f32_16x16x32_bf16 a[164:167], v[168:171], v[132:135], a[164:167]\n"
    "ds_read_b128 v[232:235], v205 offset:24576\n"
    "v_add_f32 v122, v122, v59\n"
    "v_mfma_f32_16x16x32_bf16 a[168:171], v[168:171], v[136:139], a[168:171]\n"
    "ds_read_b128 v[164:167], v200 offset:1024\n"
    "ds_read_b128 v[236:239], v206 offset:24576\n"
    "v_mfma_f32_16x16x32_bf16 a[168:171], v[172:175], v[152:155], a[168:171]\n"
    "v_add_f32 v125, v125, v60\n"
    "v_add_f32 v126, v126, v61\n"
    "v_mfma_f32_16x16x32_bf16 a[172:175], v[172:175], v[156:159], a[172:175]\n"
    "v_add_f32 v125, v125, v62\n"
    "v_add_f32 v126, v126, v63\n"
    "v_mfma_f32_16x16x32_bf16 a[172:175], v[168:171], v[140:143], a[172:175]\n"
    "ds_read_b64 v[228:229], v210\n"
    "ds_read2st64_b32 v[240:241], v207 offset0:96 offset1:99\n"
    "v_mfma_f32_16x16x32_bf16 a[188:191], v[176:179], v[140:143], a[188:191]\n"
    "ds_read_b128 v[168:171], v200 offset:2048\n"
    "ds_read2st64_b32 v[242:243], v207 offset0:102 offset1:105\n"
    "v_mfma_f32_16x16x32_bf16 a[188:191], v[180:183], v[156:159], a[188:191]\n"
    "ds_read2st64_b32 v[244:245], v207 offset0:108 offset1:111\n"
    "ds_read2st64_b32 v[246:247], v207 offset0:114 offset1:117\n"
    "v_mfma_f32_16x16x32_bf16 a[184:187], v[176:179], v[136:139], a[184:187]\n"
    "v_cvt_pk_bf16_f32 v140, v44, v45\n"
    "v_cvt_pk_bf16_f32 v141, v46, v47\n"
    "v_mfma_f32_16x16x32_bf16 a[184:187], v[180:183], v[152:155], a[184:187]\n"
    "s_waitcnt lgkmcnt(6)\n"
    "v_mul_f32 v226, s75, v223\n"
    "v_mfma_f32_16x16x32_bf16 a[180:183], v[176:179], v[132:135], a[180:183]\n"
    "ds_read_b128 v[172:175], v200 offset:3072\n"
    "v_fma_f32 v227, -v222, v226, v217\n"
    "v_mfma_f32_16x16x32_bf16 a[180:183], v[180:183], v[148:151], a[180:183]\n"
    "v_cvt_f32_i32 v232, v232\n"
    "v_cvt_f32_i32 v233, v233\n"
    "v_mfma_f32_16x16x32_bf16 a[176:179], v[176:179], v[128:131], a[176:179]\n"
    "v_cvt_f32_i32 v234, v234\n"
    "v_cvt_f32_i32 v235, v235\n"
    "v_mfma_f32_16x16x32_bf16 a[176:179], v[180:183], v[144:147], a[176:179]\n"
    "v_cvt_f32_i32 v236, v236\n"
    "v_cvt_f32_i32 v237, v237\n"
    "LM01:\n"
    "v_mfma_f32_16x16x32_bf16 v[64:67], v[160:163], a[192:195], v[96:99]\n"
    "ds_read_b128 v[176:179], v200 offset:4096\n"
    "v_cvt_f32_i32 v238, v238\n"
    "v_mfma_f32_16x16x32_bf16 v[64:67], v[164:167], a[196:199], v[64:67]\n"
    "s_waitcnt lgkmcnt(1)\n"
    "v_cvt_f32_i32 v239, v239\n"
    "v_mul_f32_dpp v232, v226, v232 row_newbcast:0 row_mask:0xf bank_mask:0xf\n"
    "v_mfma_f32_16x16x32_bf16 v[64:67], v[168:171], a[200:203], v[64:67]\n"
    "v_mul_f32_dpp v233, v226, v233 row_newbcast:1 row_mask:0xf bank_mask:0xf\n"
    "v_mul_f32_dpp v234, v226, v234 row_newbcast:2 row_mask:0xf bank_mask:0xf\n"
    "v_mfma_f32_16x16x32_bf16 v[68:71], v[168:171], a[224:227], v[100:103]\n"
    "v_mul_f32_dpp v235, v226, v235 row_newbcast:3 row_mask:0xf bank_mask:0xf\n"
    "v_mul_f32_dpp v236, v226, v236 row_newbcast:4 row_mask:0xf bank_mask:0xf\n"
    "v_mfma_f32_16x16x32_bf16 v[68:71], v[164:167], a[220:223], v[68:71]\n"
    "ds_read_b128 v[180:183], v200 offset:5120\n"
    "v_mul_f32_dpp v237, v226, v237 row_newbcast:5 row_mask:0xf bank_mask:0xf\n"
    "v_mfma_f32_16x16x32_bf16 v[68:71], v[160:163], a[216:219], v[68:71]\n"
    "v_mul_f32_dpp v238, v226, v238 row_newbcast:6 row_mask:0xf bank_mask:0xf\n"
    "v_mul_f32_dpp v239, v226, v239 row_newbcast:7 row_mask:0xf bank_mask:0xf\n"
    "v_mfma_f32_16x16x32_bf16 v[72:75], v[160:163], a[240:243], v[104:107]\n"
    "v_add_f32_dpp v232, v227, v232 row_newbcast:0 row_mask:0xf bank_mask:0xf\n"
    "v_add_f32_dpp v233, v227, v233 row_newbcast:1 row_mask:0xf bank_mask:0xf\n"
    "v_mfma_f32_16x16x32_bf16 v[72:75], v[164:167], a[244:247], v[72:75]\n"
    "v_add_f32_dpp v234, v227, v234 row_newbcast:2 row_mask:0xf bank_mask:0xf\n"
    "v_add_f32_dpp v235, v227, v235 row_newbcast:3 row_mask:0xf bank_mask:0xf\n"
    "v_mfma_f32_16x16x32_bf16 v[72:75], v[168:171], a[248:251], v[72:75]\n"
    "v_add_f32_dpp v236, v227, v236 row_newbcast:4 row_mask:0xf bank_mask:0xf\n"
    "v_add_f32_dpp v237, v227, v237 row_newbcast:5 row_mask:0xf bank_mask:0xf\n"
    "v_mfma_f32_16x16x32_bf16 v[76:79], v[168:171], v[16:19], v[108:111]\n"
    "v_add_f32_dpp v238, v227, v238 row_newbcast:6 row_mask:0xf bank_mask:0xf\n"
    "v_add_f32_dpp v239, v227, v239 row_newbcast:7 row_mask:0xf bank_mask:0xf\n"
    "v_mfma_f32_16x16x32_bf16 v[76:79], v[164:167], v[12:15], v[76:79]\n"
    "v_cvt_pk_bf16_f32 v232, v232, v233\n"
    "v_cvt_f32_i32 v240, v240\n"
    "v_mfma_f32_16x16x32_bf16 v[76:79], v[160:163], v[8:11], v[76:79]\n"
    "s_waitcnt lgkmcnt(1)\n"
    "v_cvt_pk_bf16_f32 v233, v234, v235\n"
    "v_cvt_f32_i32 v241, v241\n"
    "v_mfma_f32_16x16x32_bf16 v[76:79], v[172:175], v[20:23], v[76:79]\n"
    "ds_read_b128 v[160:163], v200 offset:9216\n"
    "v_cvt_pk_bf16_f32 v234, v236, v237\n"
    "v_mfma_f32_16x16x32_bf16 v[76:79], v[176:179], v[24:27], v[76:79]\n"
    "s_waitcnt lgkmcnt(1)\n"
    "v_cvt_f32_i32 v242, v242\n"
    "v_cvt_pk_bf16_f32 v235, v238, v239\n"
    "v_mfma_f32_16x16x32_bf16 v[76:79], v[180:183], v[28:31], v[76:79]\n"
    "v_cvt_f32_i32 v243, v243\n"
    "v_cvt_f32_i32 v244, v244\n"
    "v_mfma_f32_16x16x32_bf16 v[72:75], v[180:183], v[4:7], v[72:75]\n"
    "ds_read_b128 v[164:167], v200 offset:10240\n"
    "v_cvt_f32_i32 v245, v245\n"
    "v_mfma_f32_16x16x32_bf16 v[72:75], v[176:179], v[0:3], v[72:75]\n"
    "buffer_store_dwordx4 v[232:235], v211, s[36:39], s62 offen sc1\n"
    "v_cvt_f32_i32 v246, v246\n"
    "v_cvt_f32_i32 v247, v247\n"
    "v_mul_f32_dpp v240, v228, v240 row_newbcast:0 row_mask:0xf bank_mask:0xf\n"
    "v_mfma_f32_16x16x32_bf16 v[72:75], v[172:175], a[252:255], v[72:75]\n"
    "v_mul_f32_dpp v241, v228, v241 row_newbcast:1 row_mask:0xf bank_mask:0xf\n"
    "v_mul_f32_dpp v242, v228, v242 row_newbcast:2 row_mask:0xf bank_mask:0xf\n"
    "v_mfma_f32_16x16x32_bf16 v[68:71], v[172:175], a[228:231], v[68:71]\n"
    "ds_read_b128 v[168:171], v200 offset:11264\n"
    "v_mul_f32_dpp v243, v228, v243 row_newbcast:3 row_mask:0xf bank_mask:0xf\n"
    "v_mfma_f32_16x16x32_bf16 v[68:71], v[176:179], a[232:235], v[68:71]\n"
    "v_mul_f32_dpp v244, v228, v244 row_newbcast:4 row_mask:0xf bank_mask:0xf\n"
    "v_mul_f32_dpp v245, v228, v245 row_newbcast:5 row_mask:0xf bank_mask:0xf\n"
    "v_mfma_f32_16x16x32_bf16 v[68:71], v[180:183], a[236:239], v[68:71]\n"
    "v_mul_f32_dpp v246, v228, v246 row_newbcast:6 row_mask:0xf bank_mask:0xf\n"
    "v_mul_f32_dpp v247, v228, v247 row_newbcast:7 row_mask:0xf bank_mask:0xf\n"
    "v_mfma_f32_16x16x32_bf16 v[64:67], v[180:183], a[212:215], v[64:67]\n"
    "v_add_f32_dpp v240, v229, v240 row_newbcast:0 row_mask:0xf bank_mask:0xf\n"
    "v_add_f32_dpp v241, v229, v241 row_newbcast:1 row_mask:0xf bank_mask:0xf\n"
    "v_mfma_f32_16x16x32_bf16 v[64:67], v[176:179], a[208:211], v[64:67]\n"
    "v_add_f32_dpp v242, v229, v242 row_newbcast:2 row_mask:0xf bank_mask:0xf\n"
    "v_add_f32_dpp v243, v229, v243 row_newbcast:3 row_mask:0xf bank_mask:0xf\n"
    "v_mfma_f32_16x16x32_bf16 v[64:67], v[172:175], a[204:207], v[64:67]\n"
    "s_waitcnt lgkmcnt(1)\n"
    "v_add_f32_dpp v244, v229, v244 row_newbcast:4 row_mask:0xf bank_mask:0xf\n"
    "v_add_f32_dpp v245, v229, v245 row_newbcast:5 row_mask:0xf bank_mask:0xf\n"
    "v_mfma_f32_16x16x32_bf16 v[80:83], v[160:163], a[204:207], v[96:99]\n"
    "ds_read_b128 v[172:175], v200 offset:6144\n"
    "v_add_f32_dpp v246, v229, v246 row_newbcast:6 row_mask:0xf bank_mask:0xf\n"
    "v_mfma_f32_16x16x32_bf16 v[80:83], v[164:167], a[208:211], v[80:83]\n"
    "s_waitcnt lgkmcnt(1)\n"
    "v_add_f32_dpp v247, v229, v247 row_newbcast:7 row_mask:0xf bank_mask:0xf\n"
    "v_cvt_pk_bf16_f32 v240, v240, v241\n"
    "v_mfma_f32_16x16x32_bf16 v[80:83], v[168:171], a[212:215], v[80:83]\n"
    "v_cvt_pk_bf16_f32 v241, v242, v243\n"
    "v_cvt_pk_bf16_f32 v242, v244, v245\n"
    "v_mfma_f32_16x16x32_bf16 v[84:87], v[168:171], a[236:239], v[100:103]\n"
    "ds_read_b128 v[176:179], v200 offset:7168\n"
    "v_cvt_pk_bf16_f32 v243, v246, v247\n"
    "v_mfma_f32_16x16x32_bf16 v[84:87], v[164:167], a[232:235], v[84:87]\n"
    "buffer_store_dwordx4 v[240:243], v212, s[40:43], s63 offen sc1\n"
    "v_cvt_pk_bf16_f32 v136, v40, v41\n"
    "v_cvt_pk_bf16_f32 v137, v42, v43\n"
    "v_cvt_pk_bf16_f32 v132, v36, v37\n"
    "v_mfma_f32_16x16x32_bf16 v[84:87], v[160:163], a[228:231], v[84:87]\n"
    "v_cvt_pk_bf16_f32 v133, v38, v39\n"
    "v_cvt_pk_bf16_f32 v128, v32, v33\n"
    "v_mfma_f32_16x16x32_bf16 v[88:91], v[160:163], a[252:255], v[104:107]\n"
    "ds_read_b128 v[180:183], v200 offset:8192\n"
    "v_cvt_pk_bf16_f32 v129, v34, v35\n"
    "v_mfma_f32_16x16x32_bf16 v[88:91], v[164:167], v[0:3], v[88:91]\n"
    "v_exp_f32 v64, v64\n"
    "v_mfma_f32_16x16x32_bf16 v[88:91], v[168:171], v[4:7], v[88:91]\n"
    "v_exp_f32 v65, v65\n"
    "v_mfma_f32_16x16x32_bf16 v[92:95], v[168:171], v[28:31], v[108:111]\n"
    "v_exp_f32 v66, v66\n"
    "v_mfma_f32_16x16x32_bf16 v[92:95], v[164:167], v[24:27], v[92:95]\n"
    "v_exp_f32 v67, v67\n"
    "v_mfma_f32_16x16x32_bf16 v[92:95], v[160:163], v[20:23], v[92:95]\n"
    "s_waitcnt lgkmcnt(1)\n"
    "v_exp_f32 v68, v68\n"
    "v_mfma_f32_16x16x32_bf16 v[92:95], v[172:175], v[8:11], v[92:95]\n"
    "ds_read_b128 v[160:163], v200 offset:12288\n"
    "v_cvt_pk_bf16_f32 v142, v60, v61\n"
    "v_mfma_f32_16x16x32_bf16 v[92:95], v[176:179], v[12:15], v[92:95]\n"
    "s_waitcnt lgkmcnt(1)\n"
    "s_add_u32 s65, s60, s92\n"
    "v_cvt_pk_bf16_f32 v143, v62, v63\n"
    "v_mfma_f32_16x16x32_bf16 v[92:95], v[180:183], v[16:19], v[92:95]\n"
    "s_add_u32 m0, s90, 0x6000\n"
    "v_exp_f32 v69, v69\n"
    "v_mfma_f32_16x16x32_bf16 v[88:91], v[180:183], a[248:251], v[88:91]\n"
    "ds_read_b128 v[164:167], v200 offset:13312\n"
    "v_cvt_pk_bf16_f32 v138, v56, v57\n"
    "v_mfma_f32_16x16x32_bf16 v[88:91], v[176:179], a[244:247], v[88:91]\n"
    "buffer_load_dwordx4 v200, s[36:39], s65 offen sc1 lds\n"
    "v_mfma_f32_16x16x32_bf16 v[88:91], v[172:175], a[240:243], v[88:91]\n"
    "v_exp_f32 v70, v70\n"
    "v_mfma_f32_16x16x32_bf16 v[84:87], v[172:175], a[216:219], v[84:87]\n"
    "ds_read_b128 v[168:171], v200 offset:14336\n"
    "v_cvt_pk_bf16_f32 v139, v58, v59\n"
    "v_mfma_f32_16x16x32_bf16 v[84:87], v[176:179], a[220:223], v[84:87]\n"
    "v_exp_f32 v71, v71\n"
    "v_mfma_f32_16x16x32_bf16 v[84:87], v[180:183], a[224:227], v[84:87]\n"
    "buffer_load_dwordx4 v200, s[36:39], s65 offen offset:1024 sc1 lds\n"
    "v_mfma_f32_16x16x32_bf16 v[80:83], v[180:183], a[200:203], v[80:83]\n"
    "v_exp_f32 v72, v72\n"
    "v_mfma_f32_16x16x32_bf16 v[80:83], v[176:179], a[196:199], v[80:83]\n"
    "v_cvt_pk_bf16_f32 v134, v52, v53\n"
    "v_cvt_pk_bf16_f32 v135, v54, v55\n"
    "v_mfma_f32_16x16x32_bf16 v[80:83], v[172:175], a[192:195], v[80:83]\n"
    "s_waitcnt lgkmcnt(1)\n"
    "v_exp_f32 v73, v73\n"
    "v_mfma_f32_16x16x32_bf16 v[32:35], v[160:163], a[192:195], v[96:99]\n"
    "ds_read_b128 v[172:175], v200 offset:15360\n"
    "v_cvt_pk_bf16_f32 v130, v48, v49\n"
    "v_mfma_f32_16x16x32_bf16 v[32:35], v[164:167], a[196:199], v[32:35]\n"
    "s_waitcnt lgkmcnt(1)\n"
    "buffer_load_dwordx4 v200, s[36:39], s65 offen offset:2048 sc1 lds\n"
    "v_mfma_f32_16x16x32_bf16 v[32:35], v[168:171], a[200:203], v[32:35]\n"
    "v_cvt_pk_bf16_f32 v131, v50, v51\n"
    "v_cvt_pk_bf16_f32 v144, v64, v65\n"
    "v_mfma_f32_16x16x32_bf16 v[36:39], v[168:171], a[224:227], v[100:103]\n"
    "ds_read_b128 v[176:179], v200 offset:16384\n"
    "v_cvt_pk_bf16_f32 v145, v66, v67\n"
    "v_mfma_f32_16x16x32_bf16 v[36:39], v[164:167], a[220:223], v[36:39]\n"
    "v_exp_f32 v74, v74\n"
    "v_mfma_f32_16x16x32_bf16 v[36:39], v[160:163], a[216:219], v[36:39]\n"
    "buffer_load_dwordx4 v200, s[36:39], s65 offen offset:3072 sc1 lds\n"
    "v_mfma_f32_16x16x32_bf16 v[40:43], v[160:163], a[240:243], v[104:107]\n"
    "s_add_u32 s65, s65, 0x1000\n"
    "ds_read_b128 v[180:183], v200 offset:17408\n"
    "v_cvt_pk_bf16_f32 v148, v68, v69\n"
    "v_mfma_f32_16x16x32_bf16 v[40:43], v[164:167], a[244:247], v[40:43]\n"
    "s_add_u32 m0, s90, 0x7000\n"
    "v_exp_f32 v75, v75\n"
    "v_mfma_f32_16x16x32_bf16 v[40:43], v[168:171], a[248:251], v[40:43]\n"
    "v_exp_f32 v76, v76\n"
    "v_mfma_f32_16x16x32_bf16 v[44:47], v[168:171], v[16:19], v[108:111]\n"
    "buffer_load_dwordx4 v200, s[36:39], s65 offen sc1 lds\n"
    "v_mfma_f32_16x16x32_bf16 v[44:47], v[164:167], v[12:15], v[44:47]\n"
    "v_exp_f32 v77, v77\n"
    "v_mfma_f32_16x16x32_bf16 v[44:47], v[160:163], v[8:11], v[44:47]\n"
    "s_waitcnt lgkmcnt(1)\n"
    "v_exp_f32 v78, v78\n"
    "v_mfma_f32_16x16x32_bf16 v[44:47], v[172:175], v[20:23], v[44:47]\n"
    "ds_read_b128 v[160:163], v200 offset:21504\n"
    "v_cvt_pk_bf16_f32 v149, v70, v71\n"
    "v_mfma_f32_16x16x32_bf16 v[44:47], v[176:179], v[24:27], v[44:47]\n"
    "s_waitcnt lgkmcnt(1)\n"
    "buffer_load_dwordx4 v200, s[36:39], s65 offen offset:1024 sc1 lds\n"
    "v_mfma_f32_16x16x32_bf16 v[44:47], v[180:183], v[28:31], v[44:47]\n"
    "v_exp_f32 v79, v79\n"
    "v_mfma_f32_16x16x32_bf16 v[40:43], v[180:183], v[4:7], v[40:43]\n"
    "ds_read_b128 v[164:167], v200 offset:22528\n"
    "v_cvt_pk_bf16_f32 v152, v72, v73\n"
    "v_mfma_f32_16x16x32_bf16 v[40:43], v[176:179], v[0:3], v[40:43]\n"
    "v_exp_f32 v80, v80\n"
    "v_mfma_f32_16x16x32_bf16 v[40:43], v[172:175], a[252:255], v[40:43]\n"
    "buffer_load_dwordx4 v200, s[36:39], s65 offen offset:2048 sc1 lds\n"
    "v_mfma_f32_16x16x32_bf16 v[36:39], v[172:175], a[228:231], v[36:39]\n"
    "ds_read_b128 v[168:171], v200 offset:23552\n"
    "v_cvt_pk_bf16_f32 v153, v74, v75\n"
    "v_mfma_f32_16x16x32_bf16 v[36:39], v[176:179], a[232:235], v[36:39]\n"
    "s_add_u32 m0, s71, 0xc000\n"
    "v_exp_f32 v81, v81\n"
    "v_mfma_f32_16x16x32_bf16 v[36:39], v[180:183], a[236:239], v[36:39]\n"
    "v_exp_f32 v82, v82\n"
    "v_mfma_f32_16x16x32_bf16 v[32:35], v[180:183], a[212:215], v[32:35]\n"
    "buffer_load_dwordx4 v200, s[40:43], s61 offen sc1 lds\n"
    "v_mfma_f32_16x16x32_bf16 v[32:35], v[176:179], a[208:211], v[32:35]\n"
    "v_exp_f32 v83, v83\n"
    "v_mfma_f32_16x16x32_bf16 v[32:35], v[172:175], a[204:207], v[32:35]\n"
    "s_waitcnt lgkmcnt(1)\n"
    "v_exp_f32 v84, v84\n"
    "v_mfma_f32_16x16x32_bf16 v[48:51], v[160:163], a[204:207], v[96:99]\n"
    "ds_read_b128 v[172:175], v200 offset:18432\n"
    "v_cvt_pk_bf16_f32 v156, v76, v77\n"
    "v_mfma_f32_16x16x32_bf16 v[48:51], v[164:167], a[208:211], v[48:51]\n"
    "s_waitcnt lgkmcnt(1)\n"
    "buffer_load_dwordx4 v200, s[40:43], s61 offen offset:1024 sc1 lds\n"
    "v_mfma_f32_16x16x32_bf16 v[48:51], v[168:171], a[212:215], v[48:51]\n"
    "v_exp_f32 v85, v85\n"
    "v_mfma_f32_16x16x32_bf16 v[52:55], v[168:171], a[236:239], v[100:103]\n"
    "ds_read_b128 v[176:179], v200 offset:19456\n"
    "v_cvt_pk_bf16_f32 v157, v78, v79\n"
    "v_mfma_f32_16x16x32_bf16 v[52:55], v[164:167], a[232:235], v[52:55]\n"
    "v_exp_f32 v86, v86\n"
    "v_mfma_f32_16x16x32_bf16 v[52:55], v[160:163], a[228:231], v[52:55]\n"
    "buffer_load_dwordx4 v200, s[40:43], s61 offen offset:2048 sc1 lds\n"
    "v_mfma_f32_16x16x32_bf16 v[56:59], v[160:163], a[252:255], v[104:107]\n"
    "ds_read_b128 v[180:183], v200 offset:20480\n"
    "v_cvt_pk_bf16_f32 v146, v80, v81\n"
    "v_mfma_f32_16x16x32_bf16 v[56:59], v[164:167], v[0:3], v[56:59]\n"
    "v_exp_f32 v87, v87\n"
    "v_mfma_f32_16x16x32_bf16 v[56:59], v[168:171], v[4:7], v[56:59]\n"
    "v_exp_f32 v88, v88\n"
    "v_mfma_f32_16x16x32_bf16 v[60:63], v[168:171], v[28:31], v[108:111]\n"
    "buffer_load_dwordx4 v200, s[40:43], s61 offen offset:3072 sc1 lds\n"
    "v_mfma_f32_16x16x32_bf16 v[60:63], v[164:167], v[24:27], v[60:63]\n"
    "s_add_u32 s65, s61, 0x1000\n"
    "v_exp_f32 v89, v89\n"
    "v_mfma_f32_16x16x32_bf16 v[60:63], v[160:163], v[20:23], v[60:63]\n"
    "s_waitcnt lgkmcnt(1)\n"
    "s_add_u32 m0, s71, 0xd000\n"
    "v_cvt_pk_bf16_f32 v147, v82, v83\n"
    "v_mfma_f32_16x16x32_bf16 v[60:63], v[172:175], v[8:11], v[60:63]\n"
    "ds_read_b128 v[160:163], v201 offset:24576\n"
    "v_cvt_pk_bf16_f32 v150, v84, v85\n"
    "v_mfma_f32_16x16x32_bf16 v[60:63], v[176:179], v[12:15], v[60:63]\n"
    "s_waitcnt lgkmcnt(1)\n"
    "buffer_load_dwordx4 v200, s[40:43], s65 offen sc1 lds\n"
    "v_mfma_f32_16x16x32_bf16 v[60:63], v[180:183], v[16:19], v[60:63]\n"
    "v_exp_f32 v90, v90\n"
    "v_mfma_f32_16x16x32_bf16 v[56:59], v[180:183], a[248:251], v[56:59]\n"
    "ds_read_b128 v[164:167], v201 offset:25600\n"
    "v_cvt_pk_bf16_f32 v151, v86, v87\n"
    "v_mfma_f32_16x16x32_bf16 v[56:59], v[176:179], a[244:247], v[56:59]\n"
    "v_exp_f32 v91, v91\n"
    "v_mfma_f32_16x16x32_bf16 v[56:59], v[172:175], a[240:243], v[56:59]\n"
    "buffer_load_dwordx4 v200, s[40:43], s65 offen offset:1024 sc1 lds\n"
    "v_mfma_f32_16x16x32_bf16 v[52:55], v[172:175], a[216:219], v[52:55]\n"
    "ds_read_b128 v[168:171], v201 offset:26624\n"
    "v_cvt_pk_bf16_f32 v154, v88, v89\n"
    "v_mfma_f32_16x16x32_bf16 v[52:55], v[176:179], a[220:223], v[52:55]\n"
    "v_exp_f32 v92, v92\n"
    "v_mfma_f32_16x16x32_bf16 v[52:55], v[180:183], a[224:227], v[52:55]\n"
    "v_exp_f32 v93, v93\n"
    "v_mfma_f32_16x16x32_bf16 v[48:51], v[180:183], a[200:203], v[48:51]\n"
    "v_exp_f32 v94, v94\n"
    "v_mfma_f32_16x16x32_bf16 v[48:51], v[176:179], a[196:199], v[48:51]\n"
    "v_cvt_pk_bf16_f32 v155, v90, v91\n"
    "v_cvt_pk_bf16_f32 v158, v92, v93\n"
    "v_mfma_f32_16x16x32_bf16 v[48:51], v[172:175], a[192:195], v[48:51]\n"
    "s_waitcnt lgkmcnt(1)\n"
    "v_exp_f32 v95, v95\n"
    "v_mfma_f32_16x16x32_bf16 a[0:3], v[160:163], v[128:131], a[0:3]\n"
    "ds_read_b128 v[172:175], v201 offset:27648\n"
    "ds_read_b128 v[176:179], v201 offset:28672\n"
    "v_mfma_f32_16x16x32_bf16 a[0:3], v[164:167], v[144:147], a[0:3]\n"
    "v_cvt_pk_bf16_f32 v159, v94, v95\n"
    "v_add_f32 v113, v113, v64\n"
    "v_mfma_f32_16x16x32_bf16 a[4:7], v[160:163], v[132:135], a[4:7]\n"
    "v_exp_f32 v32, v32\n"
    "v_mfma_f32_16x16x32_bf16 a[4:7], v[164:167], v[148:151], a[4:7]\n"
    "v_exp_f32 v33, v33\n"
    "v_mfma_f32_16x16x32_bf16 a[8:11], v[160:163], v[136:139], a[8:11]\n"
    "ds_read_b128 v[180:183], v201 offset:29696\n"
    "v_add_f32 v114, v114, v65\n"
    "v_mfma_f32_16x16x32_bf16 a[8:11], v[164:167], v[152:155], a[8:11]\n"
    "v_exp_f32 v34, v34\n"
    "v_mfma_f32_16x16x32_bf16 a[12:15], v[160:163], v[140:143], a[12:15]\n"
    "v_exp_f32 v35, v35\n"
    "v_mfma_f32_16x16x32_bf16 a[12:15], v[164:167], v[156:159], a[12:15]\n"
    "s_waitcnt lgkmcnt(2)\n"
    "v_exp_f32 v36, v36\n"
    "v_mfma_f32_16x16x32_bf16 a[28:31], v[168:171], v[140:143], a[28:31]\n"
    "ds_read_b128 v[160:163], v201 offset:30720\n"
    "v_add_f32 v113, v113, v66\n"
    "v_mfma_f32_16x16x32_bf16 a[28:31], v[172:175], v[156:159], a[28:31]\n"
    "v_exp_f32 v37, v37\n"
    "v_mfma_f32_16x16x32_bf16 a[24:27], v[172:175], v[152:155], a[24:27]\n"
    "v_exp_f32 v38, v38\n"
    "v_mfma_f32_16x16x32_bf16 a[24:27], v[168:171], v[136:139], a[24:27]\n"
    "v_exp_f32 v39, v39\n"
    "v_mfma_f32_16x16x32_bf16 a[20:23], v[168:171], v[132:135], a[20:23]\n"
    "ds_read_b128 v[164:167], v201 offset:31744\n"
    "v_add_f32 v114, v114, v67\n"
    "v_mfma_f32_16x16x32_bf16 a[20:23], v[172:175], v[148:151], a[20:23]\n"
    "v_exp_f32 v40, v40\n"
    "v_mfma_f32_16x16x32_bf16 a[16:19], v[172:175], v[144:147], a[16:19]\n"
    "v_exp_f32 v41, v41\n"
    "v_mfma_f32_16x16x32_bf16 a[16:19], v[168:171], v[128:131], a[16:19]\n"
    "s_waitcnt lgkmcnt(2)\n"
    "v_exp_f32 v42, v42\n"
    "v_mfma_f32_16x16x32_bf16 a[32:35], v[176:179], v[128:131], a[32:35]\n"
    "ds_read_b128 v[168:171], v201 offset:32768\n"
    "v_add_f32 v117, v117, v68\n"
    "v_mfma_f32_16x16x32_bf16 a[32:35], v[180:183], v[144:147], a[32:35]\n"
    "v_exp_f32 v43, v43\n"
    "v_mfma_f32_16x16x32_bf16 a[36:39], v[180:183], v[148:151], a[36:39]\n"
    "v_exp_f32 v44, v44\n"
    "v_mfma_f32_16x16x32_bf16 a[36:39], v[176:179], v[132:135], a[36:39]\n"
    "v_exp_f32 v45, v45\n"
    "v_mfma_f32_16x16x32_bf16 a[40:43], v[176:179], v[136:139], a[40:43]\n"
    "ds_read_b128 v[172:175], v201 offset:33792\n"
    "v_add_f32 v118, v118, v69\n"
    "v_mfma_f32_16x16x32_bf16 a[40:43], v[180:183], v[152:155], a[40:43]\n"
    "v_exp_f32 v46, v46\n"
    "v_mfma_f32_16x16x32_bf16 a[44:47], v[180:183], v[156:159], a[44:47]\n"
    "v_exp_f32 v47, v47\n"
    "v_mfma_f32_16x16x32_bf16 a[44:47], v[176:179], v[140:143], a[44:47]\n"
    "s_waitcnt lgkmcnt(2)\n"
    "v_exp_f32 v48, v48\n"
    "v_mfma_f32_16x16x32_bf16 a[60:63], v[160:163], v[140:143], a[60:63]\n"
    "ds_read_b128 v[176:179], v201 offset:34816\n"
    "v_add_f32 v117, v117, v70\n"
    "v_mfma_f32_16x16x32_bf16 a[60:63], v[164:167], v[156:159], a[60:63]\n"
    "v_exp_f32 v49, v49\n"
    "v_mfma_f32_16x16x32_bf16 a[56:59], v[164:167], v[152:155], a[56:59]\n"
    "v_exp_f32 v50, v50\n"
    "v_mfma_f32_16x16x32_bf16 a[56:59], v[160:163], v[136:139], a[56:59]\n"
    "v_exp_f32 v51, v51\n"
    "v_mfma_f32_16x16x32_bf16 a[52:55], v[160:163], v[132:135], a[52:55]\n"
    "ds_read_b128 v[180:183], v201 offset:35840\n"
    "v_add_f32 v113, v113, v32\n"
    "v_mfma_f32_16x16x32_bf16 a[52:55], v[164:167], v[148:151], a[52:55]\n"
    "v_exp_f32 v52, v52\n"
    "v_mfma_f32_16x16x32_bf16 a[48:51], v[164:167], v[144:147], a[48:51]\n"
    "v_add_f32 v118, v118, v71\n"
    "v_add_f32 v114, v114, v33\n"
    "v_mfma_f32_16x16x32_bf16 a[48:51], v[160:163], v[128:131], a[48:51]\n"
    "s_waitcnt lgkmcnt(2)\n"
    "v_add_f32 v121, v121, v72\n"
    "v_add_f32 v113, v113, v34\n"
    "v_mfma_f32_16x16x32_bf16 a[64:67], v[168:171], v[128:131], a[64:67]\n"
    "ds_read_b128 v[160:163], v201 offset:36864\n"
    "v_add_f32 v122, v122, v73\n"
    "v_mfma_f32_16x16x32_bf16 a[64:67], v[172:175], v[144:147], a[64:67]\n"
    "v_exp_f32 v53, v53\n"
    "v_mfma_f32_16x16x32_bf16 a[68:71], v[172:175], v[148:151], a[68:71]\n"
    "v_add_f32 v114, v114, v35\n"
    "v_add_f32 v121, v121, v74\n"
    "v_mfma_f32_16x16x32_bf16 a[68:71], v[168:171], v[132:135], a[68:71]\n"
    "s_add_u32 m0, s51, s95\n"
    "v_add_f32 v117, v117, v36\n"
    "v_add_f32 v122, v122, v75\n"
    "v_mfma_f32_16x16x32_bf16 a[72:75], v[168:171], v[136:139], a[72:75]\n"
    "ds_read_b128 v[164:167], v201 offset:37888\n"
    "v_add_f32 v118, v118, v37\n"
    "v_mfma_f32_16x16x32_bf16 a[72:75], v[172:175], v[152:155], a[72:75]\n"
    "buffer_load_dwordx4 v202, s[32:35], s64 offen lds\n"
    "v_mfma_f32_16x16x32_bf16 a[76:79], v[172:175], v[156:159], a[76:79]\n"
    "v_exp_f32 v54, v54\n"
    "v_mfma_f32_16x16x32_bf16 a[76:79], v[168:171], v[140:143], a[76:79]\n"
    "s_waitcnt lgkmcnt(2)\n"
    "v_add_f32 v125, v125, v76\n"
    "v_add_f32 v117, v117, v38\n"
    "v_mfma_f32_16x16x32_bf16 a[92:95], v[176:179], v[140:143], a[92:95]\n"
    "ds_read_b128 v[168:171], v201 offset:38912\n"
    "v_add_f32 v126, v126, v77\n"
    "v_mfma_f32_16x16x32_bf16 a[92:95], v[180:183], v[156:159], a[92:95]\n"
    "buffer_load_dwordx4 v203, s[32:35], s64 offen offset:1024 lds\n"
    "v_mfma_f32_16x16x32_bf16 a[88:91], v[180:183], v[152:155], a[88:91]\n"
    "v_exp_f32 v55, v55\n"
    "v_mfma_f32_16x16x32_bf16 a[88:91], v[176:179], v[136:139], a[88:91]\n"
    "v_add_f32 v118, v118, v39\n"
    "v_add_f32 v125, v125, v78\n"
    "v_mfma_f32_16x16x32_bf16 a[84:87], v[176:179], v[132:135], a[84:87]\n"
    "ds_read_b128 v[172:175], v201 offset:39936\n"
    "v_add_f32 v121, v121, v40\n"
    "v_mfma_f32_16x16x32_bf16 a[84:87], v[180:183], v[148:151], a[84:87]\n"
    "buffer_load_dwordx4 v204, s[32:35], s64 offen offset:2048 lds\n"
    "v_mfma_f32_16x16x32_bf16 a[80:83], v[180:183], v[144:147], a[80:83]\n"
    "v_exp_f32 v56, v56\n"
    "v_mfma_f32_16x16x32_bf16 a[80:83], v[176:179], v[128:131], a[80:83]\n"
    "s_waitcnt lgkmcnt(2)\n"
    "v_add_f32 v126, v126, v79\n"
    "v_add_f32 v122, v122, v41\n"
    "v_mfma_f32_16x16x32_bf16 a[96:99], v[160:163], v[128:131], a[96:99]\n"
    "s_mov_b32 s95, s94\n"
    "ds_read_b128 v[176:179], v201 offset:40960\n"
    "v_add_f32 v113, v113, v80\n"
    "v_mfma_f32_16x16x32_bf16 a[96:99], v[164:167], v[144:147], a[96:99]\n"
    "s_sub_u32 s94, s94, 0x3000\n"
    "v_add_f32 v121, v121, v42\n"
    "v_add_f32 v114, v114, v81\n"
    "v_mfma_f32_16x16x32_bf16 a[100:103], v[164:167], v[148:151], a[100:103]\n"
    "s_min_u32 s94, s94, 0x6000\n"
    "v_exp_f32 v57, v57\n"
    "v_mfma_f32_16x16x32_bf16 a[100:103], v[160:163], v[132:135], a[100:103]\n"
    "s_sub_u32 s93, s94, s95\n"
    "v_add_f32 v122, v122, v43\n"
    "v_add_f32 v113, v113, v82\n"
    "v_mfma_f32_16x16x32_bf16 a[104:107], v[160:163], v[136:139], a[104:107]\n"
    "ds_read_b128 v[180:183], v201 offset:41984\n"
    "v_add_f32 v125, v125, v44\n"
    "v_mfma_f32_16x16x32_bf16 a[104:107], v[164:167], v[152:155], a[104:107]\n"
    "v_exp_f32 v58, v58\n"
    "v_mfma_f32_16x16x32_bf16 a[108:111], v[164:167], v[156:159], a[108:111]\n"
    "v_add_f32 v114, v114, v83\n"
    "v_add_f32 v126, v126, v45\n"
    "v_mfma_f32_16x16x32_bf16 a[108:111], v[160:163], v[140:143], a[108:111]\n"
    "s_waitcnt lgkmcnt(2)\n"
    "v_add_f32 v117, v117, v84\n"
    "v_add_f32 v125, v125, v46\n"
    "v_mfma_f32_16x16x32_bf16 a[124:127], v[168:171], v[140:143], a[124:127]\n"
    "ds_read_b128 v[160:163], v201 offset:43008\n"
    "v_add_f32 v118, v118, v85\n"
    "v_mfma_f32_16x16x32_bf16 a[124:127], v[172:175], v[156:159], a[124:127]\n"
    "v_exp_f32 v59, v59\n"
    "v_mfma_f32_16x16x32_bf16 a[120:123], v[172:175], v[152:155], a[120:123]\n"
    "v_add_f32 v126, v126, v47\n"
    "v_add_f32 v117, v117, v86\n"
    "v_mfma_f32_16x16x32_bf16 a[120:123], v[168:171], v[136:139], a[120:123]\n"
    "v_add_f32 v113, v113, v48\n"
    "v_add_u32 v205, s93, v205\n"
    "v_mfma_f32_16x16x32_bf16 a[116:119], v[168:171], v[132:135], a[116:119]\n"
    "ds_read_b128 v[164:167], v201 offset:44032\n"
    "v_add_f32 v118, v118, v87\n"
    "v_mfma_f32_16x16x32_bf16 a[116:119], v[172:175], v[148:151], a[116:119]\n"
    "v_exp_f32 v60, v60\n"
    "v_mfma_f32_16x16x32_bf16 a[112:115], v[172:175], v[144:147], a[112:115]\n"
    "v_add_f32 v114, v114, v49\n"
    "v_add_u32 v206, s93, v206\n"
    "v_mfma_f32_16x16x32_bf16 a[112:115], v[168:171], v[128:131], a[112:115]\n"
    "s_waitcnt lgkmcnt(2)\n"
    "v_add_f32 v121, v121, v88\n"
    "v_add_f32 v113, v113, v50\n"
    "v_mfma_f32_16x16x32_bf16 a[128:131], v[176:179], v[128:131], a[128:131]\n"
    "ds_read_b128 v[168:171], v201 offset:45056\n"
    "v_add_u32 v207, s93, v207\n"
    "v_mfma_f32_16x16x32_bf16 a[128:131], v[180:183], v[144:147], a[128:131]\n"
    "v_exp_f32 v61, v61\n"
    "v_mfma_f32_16x16x32_bf16 a[132:135], v[180:183], v[148:151], a[132:135]\n"
    "s_add_u32 s64, s64, 0xc000\n"
    "v_add_f32 v122, v122, v89\n"
    "v_add_f32 v114, v114, v51\n"
    "v_mfma_f32_16x16x32_bf16 a[132:135], v[176:179], v[132:135], a[132:135]\n"
    "s_add_u32 s62, s62, 0x6000\n"
    "v_add_f32 v121, v121, v90\n"
    "v_add_f32 v117, v117, v52\n"
    "v_mfma_f32_16x16x32_bf16 a[136:139], v[176:179], v[136:139], a[136:139]\n"
    "s_add_u32 s63, s63, 0x6000\n"
    "ds_read_b128 v[172:175], v201 offset:46080\n"
    "v_add_f32 v122, v122, v91\n"
    "v_mfma_f32_16x16x32_bf16 a[136:139], v[180:183], v[152:155], a[136:139]\n"
    "s_add_u32 s60, s60, 0x6000\n"
    "v_exp_f32 v62, v62\n"
    "v_mfma_f32_16x16x32_bf16 a[140:143], v[180:183], v[156:159], a[140:143]\n"
    "s_add_u32 s61, s61, 0x6000\n"
    "v_add_f32 v118, v118, v53\n"
    "v_add_f32 v125, v125, v92\n"
    "v_mfma_f32_16x16x32_bf16 a[140:143], v[176:179], v[140:143], a[140:143]\n"
    "s_waitcnt lgkmcnt(2)\n"
    "v_add_f32 v117, v117, v54\n"
    "v_add_f32 v126, v126, v93\n"
    "v_mfma_f32_16x16x32_bf16 a[156:159], v[160:163], v[140:143], a[156:159]\n"
    "ds_read_b128 v[176:179], v201 offset:47104\n"
    "v_add_f32 v118, v118, v55\n"
    "v_mfma_f32_16x16x32_bf16 a[156:159], v[164:167], v[156:159], a[156:159]\n"
    "s_waitcnt vmcnt(3)\n"
    "ds_read_b128 v[180:183], v201 offset:48128\n"
    "v_add_f32 v125, v125, v94\n"
    "v_mfma_f32_16x16x32_bf16 a[152:155], v[164:167], v[152:155], a[152:155]\n"
    "v_exp_f32 v63, v63\n"
    "v_mfma_f32_16x16x32_bf16 a[152:155], v[160:163], v[136:139], a[152:155]\n"
    "v_add_f32 v121, v121, v56\n"
    "v_add_f32 v126, v126, v95\n"
    "v_mfma_f32_16x16x32_bf16 a[148:151], v[160:163], v[132:135], a[148:151]\n"
    "v_add_f32 v122, v122, v57\n"
    "v_mfma_f32_16x16x32_bf16 a[148:151], v[164:167], v[148:151], a[148:151]\n"
    "v_mfma_f32_16x16x32_bf16 a[144:147], v[164:167], v[144:147], a[144:147]\n"
    "v_mfma_f32_16x16x32_bf16 a[144:147], v[160:163], v[128:131], a[144:147]\n"
    "s_waitcnt lgkmcnt(0)\n"
    "v_mfma_f32_16x16x32_bf16 a[160:163], v[168:171], v[128:131], a[160:163]\n"
    "s_barrier\n"
    "LMID1:\n"
    "v_mfma_f32_16x16x32_bf16 a[160:163], v[172:175], v[144:147], a[160:163]\n"
    "ds_read_u16_d16_hi v223, v209 offset:6336\n"
    "ds_read_u16_d16_hi v222, v209 offset:192\n"
    "v_mfma_f32_16x16x32_bf16 a[164:167], v[172:175], v[148:151], a[164:167]\n"
    "ds_read_b128 v[160:163], v200 offset:24576\n"
    "v_add_f32 v121, v121, v58\n"
    "v_mfma_f32_16x16x32_bf16 a[164:167], v[168:171], v[132:135], a[164:167]\n"
    "ds_read_b128 v[232:235], v205 offset:24576\n"
    "v_add_f32 v122, v122, v59\n"
    "v_mfma_f32_16x16x32_bf16 a[168:171], v[168:171], v[136:139], a[168:171]\n"
    "ds_read_b128 v[164:167], v200 offset:25600\n"
    "ds_read_b128 v[236:239], v206 offset:24576\n"
    "v_mfma_f32_16x16x32_bf16 a[168:171], v[172:175], v[152:155], a[168:171]\n"
    "v_add_f32 v125, v125, v60\n"
    "v_add_f32 v126, v126, v61\n"
    "v_mfma_f32_16x16x32_bf16 a[172:175], v[172:175], v[156:159], a[172:175]\n"
    "v_add_f32 v125, v125, v62\n"
    "v_add_f32 v126, v126, v63\n"
    "v_mfma_f32_16x16x32_bf16 a[172:175], v[168:171], v[140:143], a[172:175]\n"
    "ds_read_b64 v[228:229], v210 offset:64\n"
    "ds_read2st64_b32 v[240:241], v207 offset0:96 offset1:99\n"
    "v_mfma_f32_16x16x32_bf16 a[188:191], v[176:179], v[140:143], a[188:191]\n"
    "ds_read_b128 v[168:171], v200 offset:26624\n"
    "ds_read2st64_b32 v[242:243], v207 offset0:102 offset1:105\n"
    "v_mfma_f32_16x16x32_bf16 a[188:191], v[180:183], v[156:159], a[188:191]\n"
    "ds_read2st64_b32 v[244:245], v207 offset0:108 offset1:111\n"
    "ds_read2st64_b32 v[246:247], v207 offset0:114 offset1:117\n"
    "v_mfma_f32_16x16x32_bf16 a[184:187], v[176:179], v[136:139], a[184:187]\n"
    "v_cvt_pk_bf16_f32 v140, v44, v45\n"
    "v_cvt_pk_bf16_f32 v141, v46, v47\n"
    "v_mfma_f32_16x16x32_bf16 a[184:187], v[180:183], v[152:155], a[184:187]\n"
    "s_waitcnt lgkmcnt(6)\n"
    "v_mul_f32 v226, s75, v223\n"
    "v_mfma_f32_16x16x32_bf16 a[180:183], v[176:179], v[132:135], a[180:183]\n"
    "s_add_u32 s59, s59, 1\n"
    "ds_read_b128 v[172:175], v200 offset:27648\n"
    "v_fma_f32 v227, -v222, v226, v217\n"
    "v_mfma_f32_16x16x32_bf16 a[180:183], v[180:183], v[148:151], a[180:183]\n"
    "v_cvt_f32_i32 v232, v232\n"
    "v_cvt_f32_i32 v233, v233\n"
    "v_mfma_f32_16x16x32_bf16 a[176:179], v[176:179], v[128:131], a[176:179]\n"
    "s_cmp_lt_u32 s59, 15\n"
    "v_cvt_f32_i32 v234, v234\n"
    "v_cvt_f32_i32 v235, v235\n"
    "v_mfma_f32_16x16x32_bf16 a[176:179], v[180:183], v[144:147], a[176:179]\n"
    "s_cbranch_scc1 LLOOP1\n"
    "s_branch LTAIL\n"
    "LENT0:\n"
    "s_mov_b32 s59, 1\n"
    "s_add_u32 m0, s72, 0x21000\n"
    "s_nop 0\n"
    "buffer_load_dwordx4 v250, s[48:51], 0 offen offset:2048 lds\n"
    "s_add_u32 m0, s72, 0x18000\n"
    "s_nop 0\n"
    "buffer_load_dwordx4 v202, s[32:35], s64 offen lds\n"
    "buffer_load_dwordx4 v203, s[32:35], s64 offen offset:1024 lds\n"
    "buffer_load_dwordx4 v204, s[32:35], s64 offen offset:2048 lds\n"
    "s_add_u32 s64, s64, 0xc000\n"
    "s_mov_b32 s94, 0x3000\n"
    "s_mov_b32 s95, 0x6000\n"
    "s_add_u32 s51, s72, 0x18000\n"
    "v_add_u32 v205, 0x3000, v205\n"
    "v_add_u32 v206, 0x3000, v206\n"
    "v_add_u32 v207, 0x3000, v207\n"
    "buffer_store_dwordx2 v[220:221], v214, s[44:47], 0 offen sc1\n"
    "ds_read_b128 v[160:163], v200 offset:24576\n"
    "ds_read_u16_d16_hi v223, v209 offset:6336\n"
    "ds_read_u16_d16_hi v222, v209 offset:192\n"
    "v_add_f32 v121, v121, v58\n"
    "ds_read_b128 v[164:167], v200 offset:25600\n"
    "ds_read_b128 v[232:235], v205 offset:24576\n"
    "v_add_f32 v122, v122, v59\n"
    "ds_read_b128 v[236:239], v206 offset:24576\n"
    "v_add_f32 v125, v125, v60\n"
    "v_add_f32 v126, v126, v61\n"
    "v_add_f32 v125, v125, v62\n"
    "v_add_f32 v126, v126, v63\n"
    "ds_read_b128 v[168:171], v200 offset:26624\n"
    "ds_read_b64 v[228:229], v210 offset:64\n"
    "ds_read2st64_b32 v[240:241], v207 offset0:96 offset1:99\n"
    "ds_read2st64_b32 v[242:243], v207 offset0:102 offset1:105\n"
    "ds_read2st64_b32 v[244:245], v207 offset0:108 offset1:111\n"
    "ds_read2st64_b32 v[246:247], v207 offset0:114 offset1:117\n"
    "v_cvt_pk_bf16_f32 v140, v44, v45\n"
    "v_cvt_pk_bf16_f32 v141, v46, v47\n"
    "ds_read_b128 v[172:175], v200 offset:27648\n"
    "s_waitcnt lgkmcnt(7)\n"
    "v_mul_f32 v226, s75, v223\n"
    "v_fma_f32 v227, -v222, v226, v217\n"
    "v_cvt_f32_i32 v232, v232\n"
    "v_cvt_f32_i32 v233, v233\n"
    "s_nop 7\n"
    ".p2align 6\n"
    "LLOOP:\n"
    "v_mfma_f32_16x16x32_bf16 v[64:67], v[160:163], a[192:195], v[96:99]\n"
    "ds_read_b128 v[176:179], v200 offset:28672\n"
    "v_cvt_f32_i32 v234, v234\n"
    "v_mfma_f32_16x16x32_bf16 v[64:67], v[164:167], a[196:199], v[64:67]\n"
    "s_waitcnt lgkmcnt(1)\n"
    "v_cvt_f32_i32 v235, v235\n"
    "v_cvt_f32_i32 v236, v236\n"
    "v_mfma_f32_16x16x32_bf16 v[64:67], v[168:171], a[200:203], v[64:67]\n"
    "v_cvt_f32_i32 v237, v237\n"
    "v_cvt_f32_i32 v238, v238\n"
    "v_mfma_f32_16x16x32_bf16 v[68:71], v[168:171], a[224:227], v[100:103]\n"
    "v_cvt_f32_i32 v239, v239\n"
    "v_mul_f32_dpp v232, v226, v232 row_newbcast:0 row_mask:0xf bank_mask:0xf\n"
    "v_mfma_f32_16x16x32_bf16 v[68:71], v[164:167], a[220:223], v[68:71]\n"
    "ds_read_b128 v[180:183], v200 offset:29696\n"
    "v_mul_f32_dpp v233, v226, v233 row_newbcast:1 row_mask:0xf bank_mask:0xf\n"
    "v_mfma_f32_16x16x32_bf16 v[68:71], v[160:163], a[216:219], v[68:71]\n"
    "v_mul_f32_dpp v234, v226, v234 row_newbcast:2 row_mask:0xf bank_mask:0xf\n"
    "v_mul_f32_dpp v235, v226, v235 row_newbcast:3 row_mask:0xf bank_mask:0xf\n"
    "v_mfma_f32_16x16x32_bf16 v[72:75], v[160:163], a[240:243], v[104:107]\n"
    "v_mul_f32_dpp v236, v226, v236 row_newbcast:4 row_mask:0xf bank_mask:0xf\n"
    "v_mul_f32_dpp v237, v226, v237 row_newbcast:5 row_mask:0xf bank_mask:0xf\n"
    "v_mfma_f32_16x16x32_bf16 v[72:75], v[164:167], a[244:247], v[72:75]\n"
    "v_mul_f32_dpp v238, v226, v238 row_newbcast:6 row_mask:0xf bank_mask:0xf\n"
    "v_mul_f32_dpp v239, v226, v239 row_newbcast:7 row_mask:0xf bank_mask:0xf\n"
    "v_mfma_f32_16x16x32_bf16 v[72:75], v[168:171], a[248:251], v[72:75]\n"
    "v_add_f32_dpp v232, v227, v232 row_newbcast:0 row_mask:0xf bank_mask:0xf\n"
    "v_add_f32_dpp v233, v227, v233 row_newbcast:1 row_mask:0xf bank_mask:0xf\n"
    "v_mfma_f32_16x16x32_bf16 v[76:79], v[168:171], v[16:19], v[108:111]\n"
    "v_add_f32_dpp v234, v227, v234 row_newbcast:2 row_mask:0xf bank_mask:0xf\n"
    "v_add_f32_dpp v235, v227, v235 row_newbcast:3 row_mask:0xf bank_mask:0xf\n"
    "v_mfma_f32_16x16x32_bf16 v[76:79], v[164:167], v[12:15], v[76:79]\n"
    "v_add_f32_dpp v236, v227, v236 row_newbcast:4 row_mask:0xf bank_mask:0xf\n"
    "v_add_f32_dpp v237, v227, v237 row_newbcast:5 row_mask:0xf bank_mask:0xf\n"
    "v_mfma_f32_16x16x32_bf16 v[76:79], v[160:163], v[8:11], v[76:79]\n"
    "s_waitcnt lgkmcnt(1)\n"
    "v_add_f32_dpp v238, v227, v238 row_newbcast:6 row_mask:0xf bank_mask:0xf\n"
    "v_add_f32_dpp v239, v227, v239 row_newbcast:7 row_mask:0xf bank_mask:0xf\n"
    "v_mfma_f32_16x16x32_bf16 v[76:79], v[172:175], v[20:23], v[76:79]\n"
    "ds_read_b128 v[160:163], v200 offset:33792\n"
    "v_cvt_pk_bf16_f32 v232, v232, v233\n"
    "v_mfma_f32_16x16x32_bf16 v[76:79], v[176:179], v[24:27], v[76:79]\n"
    "s_waitcnt lgkmcnt(1)\n"
    "v_cvt_f32_i32 v240, v240\n"
    "v_cvt_pk_bf16_f32 v233, v234, v235\n"
    "v_mfma_f32_16x16x32_bf16 v[76:79], v[180:183], v[28:31], v[76:79]\n"
    "v_cvt_f32_i32 v241, v241\n"
    "v_cvt_pk_bf16_f32 v234, v236, v237\n"
    "v_mfma_f32_16x16x32_bf16 v[72:75], v[180:183], v[4:7], v[72:75]\n"
    "ds_read_b128 v[164:167], v200 offset:34816\n"
    "v_cvt_f32_i32 v242, v242\n"
    "v_mfma_f32_16x16x32_bf16 v[72:75], v[176:179], v[0:3], v[72:75]\n"
    "v_cvt_pk_bf16_f32 v235, v238, v239\n"
    "v_cvt_f32_i32 v243, v243\n"
    "v_mfma_f32_16x16x32_bf16 v[72:75], v[172:175], a[252:255], v[72:75]\n"
    "v_cvt_f32_i32 v244, v244\n"
    "v_cvt_f32_i32 v245, v245\n"
    "v_mfma_f32_16x16x32_bf16 v[68:71], v[172:175], a[228:231], v[68:71]\n"
    "ds_read_b128 v[168:171], v200 offset:35840\n"
    "v_cvt_f32_i32 v246, v246\n"
    "v_mfma_f32_16x16x32_bf16 v[68:71], v[176:179], a[232:235], v[68:71]\n"
    "v_cvt_f32_i32 v247, v247\n"
    "v_mul_f32_dpp v240, v228, v240 row_newbcast:0 row_mask:0xf bank_mask:0xf\n"
    "v_mfma_f32_16x16x32_bf16 v[68:71], v[180:183], a[236:239], v[68:71]\n"
    "v_mul_f32_dpp v241, v228, v241 row_newbcast:1 row_mask:0xf bank_mask:0xf\n"
    "v_mul_f32_dpp v242, v228, v242 row_newbcast:2 row_mask:0xf bank_mask:0xf\n"
    "v_mfma_f32_16x16x32_bf16 v[64:67], v[180:183], a[212:215], v[64:67]\n"
    "v_mul_f32_dpp v243, v228, v243 row_newbcast:3 row_mask:0xf bank_mask:0xf\n"
    "v_mul_f32_dpp v244, v228, v244 row_newbcast:4 row_mask:0xf bank_mask:0xf\n"
    "v_mfma_f32_16x16x32_bf16 v[64:67], v[176:179], a[208:211], v[64:67]\n"
    "buffer_store_dwordx4 v[232:235], v211, s[36:39], s62 offen sc1\n"
    "v_mul_f32_dpp v245, v228, v245 row_newbcast:5 row_mask:0xf bank_mask:0xf\n"
    "v_mul_f32_dpp v246, v228, v246 row_newbcast:6 row_mask:0xf bank_mask:0xf\n"
    "v_mul_f32_dpp v247, v228, v247 row_newbcast:7 row_mask:0xf bank_mask:0xf\n"
    "v_mfma_f32_16x16x32_bf16 v[64:67], v[172:175], a[204:207], v[64:67]\n"
    "s_waitcnt lgkmcnt(1)\n"
    "v_add_f32_dpp v240, v229, v240 row_newbcast:0 row_mask:0xf bank_mask:0xf\n"
    "v_add_f32_dpp v241, v229, v241 row_newbcast:1 row_mask:0xf bank_mask:0xf\n"
    "v_mfma_f32_16x16x32_bf16 v[80:83], v[160:163], a[204:207], v[96:99]\n"
    "ds_read_b128 v[172:175], v200 offset:30720\n"
    "v_add_f32_dpp v242, v229, v242 row_newbcast:2 row_mask:0xf bank_mask:0xf\n"
    "v_mfma_f32_16x16x32_bf16 v[80:83], v[164:167], a[208:211], v[80:83]\n"
    "s_waitcnt lgkmcnt(1)\n"
    "v_add_f32_dpp v243, v229, v243 row_newbcast:3 row_mask:0xf bank_mask:0xf\n"
    "v_add_f32_dpp v244, v229, v244 row_newbcast:4 row_mask:0xf bank_mask:0xf\n"
    "v_mfma_f32_16x16x32_bf16 v[80:83], v[168:171], a[212:215], v[80:83]\n"
    "v_add_f32_dpp v245, v229, v245 row_newbcast:5 row_mask:0xf bank_mask:0xf\n"
    "v_add_f32_dpp v246, v229, v246 row_newbcast:6 row_mask:0xf bank_mask:0xf\n"
    "v_mfma_f32_16x16x32_bf16 v[84:87], v[168:171], a[236:239], v[100:103]\n"
    "ds_read_b128 v[176:179], v200 offset:31744\n"
    "v_add_f32_dpp v247, v229, v247 row_newbcast:7 row_mask:0xf bank_mask:0xf\n"
    "v_mfma_f32_16x16x32_bf16 v[84:87], v[164:167], a[232:235], v[84:87]\n"
    "v_cvt_pk_bf16_f32 v240, v240, v241\n"
    "v_cvt_pk_bf16_f32 v241, v242, v243\n"
    "v_mfma_f32_16x16x32_bf16 v[84:87], v[160:163], a[228:231], v[84:87]\n"
    "v_cvt_pk_bf16_f32 v242, v244, v245\n"
    "v_cvt_pk_bf16_f32 v243, v246, v247\n"
    "v_mfma_f32_16x16x32_bf16 v[88:91], v[160:163], a[252:255], v[104:107]\n"
    "ds_read_b128 v[180:183], v200 offset:32768\n"
    "v_cvt_pk_bf16_f32 v136, v40, v41\n"
    "v_mfma_f32_16x16x32_bf16 v[88:91], v[164:167], v[0:3], v[88:91]\n"
    "v_cvt_pk_bf16_f32 v137, v42, v43\n"
    "v_cvt_pk_bf16_f32 v132, v36, v37\n"
    "v_mfma_f32_16x16x32_bf16 v[88:91], v[168:171], v[4:7], v[88:91]\n"
    "v_cvt_pk_bf16_f32 v133, v38, v39\n"
    "v_cvt_pk_bf16_f32 v128, v32, v33\n"
    "v_mfma_f32_16x16x32_bf16 v[92:95], v[168:171], v[28:31], v[108:111]\n"
    "v_cvt_pk_bf16_f32 v129, v34, v35\n"
    "v_cvt_pk_bf16_f32 v142, v60, v61\n"
    "v_mfma_f32_16x16x32_bf16 v[92:95], v[164:167], v[24:27], v[92:95]\n"
    "v_exp_f32 v64, v64\n"
    "v_mfma_f32_16x16x32_bf16 v[92:95], v[160:163], v[20:23], v[92:95]\n"
    "s_waitcnt lgkmcnt(1)\n"
    "v_exp_f32 v65, v65\n"
    "v_mfma_f32_16x16x32_bf16 v[92:95], v[172:175], v[8:11], v[92:95]\n"
    "ds_read_b128 v[160:163], v200 offset:36864\n"
    "v_cvt_pk_bf16_f32 v143, v62, v63\n"
    "v_mfma_f32_16x16x32_bf16 v[92:95], v[176:179], v[12:15], v[92:95]\n"
    "s_waitcnt lgkmcnt(1)\n"
    "v_exp_f32 v66, v66\n"
    "v_mfma_f32_16x16x32_bf16 v[92:95], v[180:183], v[16:19], v[92:95]\n"
    "buffer_store_dwordx4 v[240:243], v212, s[40:43], s63 offen sc1\n"
    "v_exp_f32 v67, v67\n"
    "v_cvt_pk_bf16_f32 v138, v56, v57\n"
    "v_mfma_f32_16x16x32_bf16 v[88:91], v[180:183], a[248:251], v[88:91]\n"
    "ds_read_b128 v[164:167], v200 offset:37888\n"
    "v_cvt_pk_bf16_f32 v139, v58, v59\n"
    "v_mfma_f32_16x16x32_bf16 v[88:91], v[176:179], a[244:247], v[88:91]\n"
    "v_exp_f32 v68, v68\n"
    "v_mfma_f32_16x16x32_bf16 v[88:91], v[172:175], a[240:243], v[88:91]\n"
    "v_exp_f32 v69, v69\n"
    "v_mfma_f32_16x16x32_bf16 v[84:87], v[172:175], a[216:219], v[84:87]\n"
    "ds_read_b128 v[168:171], v200 offset:38912\n"
    "v_cvt_pk_bf16_f32 v134, v52, v53\n"
    "v_mfma_f32_16x16x32_bf16 v[84:87], v[176:179], a[220:223], v[84:87]\n"
    "v_exp_f32 v70, v70\n"
    "v_mfma_f32_16x16x32_bf16 v[84:87], v[180:183], a[224:227], v[84:87]\n"
    "v_exp_f32 v71, v71\n"
    "v_mfma_f32_16x16x32_bf16 v[80:83], v[180:183], a[200:203], v[80:83]\n"
    "v_exp_f32 v72, v72\n"
    "v_mfma_f32_16x16x32_bf16 v[80:83], v[176:179], a[196:199], v[80:83]\n"
    "v_cvt_pk_bf16_f32 v135, v54, v55\n"
    "v_cvt_pk_bf16_f32 v130, v48, v49\n"
    "v_mfma_f32_16x16x32_bf16 v[80:83], v[172:175], a[192:195], v[80:83]\n"
    "s_waitcnt lgkmcnt(1)\n"
    "v_exp_f32 v73, v73\n"
    "v_mfma_f32_16x16x32_bf16 v[32:35], v[160:163], a[192:195], v[96:99]\n"
    "ds_read_b128 v[172:175], v200 offset:39936\n"
    "v_cvt_pk_bf16_f32 v131, v50, v51\n"
    "v_mfma_f32_16x16x32_bf16 v[32:35], v[164:167], a[196:199], v[32:35]\n"
    "s_waitcnt lgkmcnt(1)\n"
    "v_exp_f32 v74, v74\n"
    "v_mfma_f32_16x16x32_bf16 v[32:35], v[168:171], a[200:203], v[32:35]\n"
    "v_exp_f32 v75, v75\n"
    "v_mfma_f32_16x16x32_bf16 v[36:39], v[168:171], a[224:227], v[100:103]\n"
    "ds_read_b128 v[176:179], v200 offset:40960\n"
    "v_cvt_pk_bf16_f32 v144, v64, v65\n"
    "v_mfma_f32_16x16x32_bf16 v[36:39], v[164:167], a[220:223], v[36:39]\n"
    "v_exp_f32 v76, v76\n"
    "v_mfma_f32_16x16x32_bf16 v[36:39], v[160:163], a[216:219], v[36:39]\n"
    "v_exp_f32 v77, v77\n"
    "v_mfma_f32_16x16x32_bf16 v[40:43], v[160:163], a[240:243], v[104:107]\n"
    "ds_read_b128 v[180:183], v200 offset:41984\n"
    "v_cvt_pk_bf16_f32 v145, v66, v67\n"
    "v_mfma_f32_16x16x32_bf16 v[40:43], v[164:167], a[244:247], v[40:43]\n"
    "v_exp_f32 v78, v78\n"
    "v_mfma_f32_16x16x32_bf16 v[40:43], v[168:171], a[248:251], v[40:43]\n"
    "v_exp_f32 v79, v79\n"
    "v_mfma_f32_16x16x32_bf16 v[44:47], v[168:171], v[16:19], v[108:111]\n"
    "v_exp_f32 v80, v80\n"
    "v_mfma_f32_16x16x32_bf16 v[44:47], v[164:167], v[12:15], v[44:47]\n"
    "v_exp_f32 v81, v81\n"
    "v_mfma_f32_16x16x32_bf16 v[44:47], v[160:163], v[8:11], v[44:47]\n"
    "s_waitcnt lgkmcnt(1)\n"
    "v_exp_f32 v82, v82\n"
    "v_mfma_f32_16x16x32_bf16 v[44:47], v[172:175], v[20:23], v[44:47]\n"
    "ds_read_b128 v[160:163], v200 offset:46080\n"
    "v_cvt_pk_bf16_f32 v148, v68, v69\n"
    "v_mfma_f32_16x16x32_bf16 v[44:47], v[176:179], v[24:27], v[44:47]\n"
    "s_waitcnt lgkmcnt(1)\n"
    "v_exp_f32 v83, v83\n"
    "v_mfma_f32_16x16x32_bf16 v[44:47], v[180:183], v[28:31], v[44:47]\n"
    "v_exp_f32 v84, v84\n"
    "v_mfma_f32_16x16x32_bf16 v[40:43], v[180:183], v[4:7], v[40:43]\n"
    "ds_read_b128 v[164:167], v200 offset:47104\n"
    "v_cvt_pk_bf16_f32 v149, v70, v71\n"
    "v_mfma_f32_16x16x32_bf16 v[40:43], v[176:179], v[0:3], v[40:43]\n"
    "v_exp_f32 v85, v85\n"
    "v_mfma_f32_16x16x32_bf16 v[40:43], v[172:175], a[252:255], v[40:43]\n"
    "v_exp_f32 v86, v86\n"
    "v_mfma_f32_16x16x32_bf16 v[36:39], v[172:175], a[228:231], v[36:39]\n"
    "ds_read_b128 v[168:171], v200 offset:48128\n"
    "v_cvt_pk_bf16_f32 v152, v72, v73\n"
    "v_mfma_f32_16x16x32_bf16 v[36:39], v[176:179], a[232:235], v[36:39]\n"
    "v_exp_f32 v87, v87\n"
    "v_mfma_f32_16x16x32_bf16 v[36:39], v[180:183], a[236:239], v[36:39]\n"
    "v_exp_f32 v88, v88\n"
    "v_mfma_f32_16x16x32_bf16 v[32:35], v[180:183], a[212:215], v[32:35]\n"
    "v_exp_f32 v89, v89\n"
    "v_mfma_f32_16x16x32_bf16 v[32:35], v[176:179], a[208:211], v[32:35]\n"
    "v_cvt_pk_bf16_f32 v153, v74, v75\n"
    "v_cvt_pk_bf16_f32 v156, v76, v77\n"
    "v_mfma_f32_16x16x32_bf16 v[32:35], v[172:175], a[204:207], v[32:35]\n"
    "s_waitcnt lgkmcnt(1)\n"
    "s_mov_b32 m0, 0\n"
    "v_cvt_pk_bf16_f32 v157, v78, v79\n"
    "v_mfma_f32_16x16x32_bf16 v[48:51], v[160:163], a[204:207], v[96:99]\n"
    "ds_read_b128 v[172:175], v200 offset:43008\n"
    "v_cvt_pk_bf16_f32 v146, v80, v81\n"
    "v_mfma_f32_16x16x32_bf16 v[48:51], v[164:167], a[208:211], v[48:51]\n"
    "s_waitcnt lgkmcnt(1)\n"
    "buffer_load_dwordx4 v200, s[36:39], s60 offen sc1 lds\n"
    "v_mfma_f32_16x16x32_bf16 v[48:51], v[168:171], a[212:215], v[48:51]\n"
    "v_exp_f32 v90, v90\n"
    "v_mfma_f32_16x16x32_bf16 v[52:55], v[168:171], a[236:239], v[100:103]\n"
    "ds_read_b128 v[176:179], v200 offset:44032\n"
    "v_cvt_pk_bf16_f32 v147, v82, v83\n"
    "v_mfma_f32_16x16x32_bf16 v[52:55], v[164:167], a[232:235], v[52:55]\n"
    "v_exp_f32 v91, v91\n"
    "v_mfma_f32_16x16x32_bf16 v[52:55], v[160:163], a[228:231], v[52:55]\n"
    "buffer_load_dwordx4 v200, s[36:39], s60 offen offset:1024 sc1 lds\n"
    "v_mfma_f32_16x16x32_bf16 v[56:59], v[160:163], a[252:255], v[104:107]\n"
    "ds_read_b128 v[180:183], v200 offset:45056\n"
    "v_cvt_pk_bf16_f32 v150, v84, v85\n"
    "v_mfma_f32_16x16x32_bf16 v[56:59], v[164:167], v[0:3], v[56:59]\n"
    "v_exp_f32 v92, v92\n"
    "v_mfma_f32_16x16x32_bf16 v[56:59], v[168:171], v[4:7], v[56:59]\n"
    "v_exp_f32 v93, v93\n"
    "v_mfma_f32_16x16x32_bf16 v[60:63], v[168:171], v[28:31], v[108:111]\n"
    "buffer_load_dwordx4 v200, s[36:39], s60 offen offset:2048 sc1 lds\n"
    "v_mfma_f32_16x16x32_bf16 v[60:63], v[164:167], v[24:27], v[60:63]\n"
    "v_cvt_pk_bf16_f32 v151, v86, v87\n"
    "v_cvt_pk_bf16_f32 v154, v88, v89\n"
    "v_mfma_f32_16x16x32_bf16 v[60:63], v[160:163], v[20:23], v[60:63]\n"
    "s_waitcnt lgkmcnt(1)\n"
    "s_add_u32 m0, s71, 0x12000\n"
    "v_cvt_pk_bf16_f32 v155, v90, v91\n"
    "v_mfma_f32_16x16x32_bf16 v[60:63], v[172:175], v[8:11], v[60:63]\n"
    "ds_read_b128 v[160:163], v201\n"
    "v_cvt_pk_bf16_f32 v158, v92, v93\n"
    "v_mfma_f32_16x16x32_bf16 v[60:63], v[176:179], v[12:15], v[60:63]\n"
    "s_waitcnt lgkmcnt(1)\n"
    "buffer_load_dwordx4 v200, s[40:43], s61 offen sc1 lds\n"
    "v_mfma_f32_16x16x32_bf16 v[60:63], v[180:183], v[16:19], v[60:63]\n"
    "v_exp_f32 v94, v94\n"
    "v_mfma_f32_16x16x32_bf16 v[56:59], v[180:183], a[248:251], v[56:59]\n"
    "ds_read_b128 v[164:167], v201 offset:1024\n"
    "v_add_f32 v113, v113, v64\n"
    "v_mfma_f32_16x16x32_bf16 v[56:59], v[176:179], a[244:247], v[56:59]\n"
    "v_exp_f32 v95, v95\n"
    "v_mfma_f32_16x16x32_bf16 v[56:59], v[172:175], a[240:243], v[56:59]\n"
    "buffer_load_dwordx4 v200, s[40:43], s61 offen offset:1024 sc1 lds\n"
    "v_mfma_f32_16x16x32_bf16 v[52:55], v[172:175], a[216:219], v[52:55]\n"
    "ds_read_b128 v[168:171], v201 offset:2048\n"
    "v_cvt_pk_bf16_f32 v159, v94, v95\n"
    "v_mfma_f32_16x16x32_bf16 v[52:55], v[176:179], a[220:223], v[52:55]\n"
    "v_exp_f32 v32, v32\n"
    "v_mfma_f32_16x16x32_bf16 v[52:55], v[180:183], a[224:227], v[52:55]\n"
    "v_exp_f32 v33, v33\n"
    "v_mfma_f32_16x16x32_bf16 v[48:51], v[180:183], a[200:203], v[48:51]\n"
    "buffer_load_dwordx4 v200, s[40:43], s61 offen offset:2048 sc1 lds\n"
    "v_mfma_f32_16x16x32_bf16 v[48:51], v[176:179], a[196:199], v[48:51]\n"
    "v_exp_f32 v34, v34\n"
    "v_mfma_f32_16x16x32_bf16 v[48:51], v[172:175], a[192:195], v[48:51]\n"
    "s_waitcnt lgkmcnt(1)\n"
    "v_exp_f32 v35, v35\n"
    "v_mfma_f32_16x16x32_bf16 a[0:3], v[160:163], v[128:131], a[0:3]\n"
    "ds_read_b128 v[172:175], v201 offset:3072\n"
    "ds_read_b128 v[176:179], v201 offset:4096\n"
    "v_mfma_f32_16x16x32_bf16 a[0:3], v[164:167], v[144:147], a[0:3]\n"
    "buffer_load_dwordx4 v200, s[40:43], s61 offen offset:3072 sc1 lds\n"
    "v_mfma_f32_16x16x32_bf16 a[4:7], v[160:163], v[132:135], a[4:7]\n"
    "s_add_u32 s65, s61, 0x1000\n"
    "v_exp_f32 v36, v36\n"
    "v_mfma_f32_16x16x32_bf16 a[4:7], v[164:167], v[148:151], a[4:7]\n"
    "s_add_u32 m0, s71, 0x13000\n"
    "v_exp_f32 v37, v37\n"
    "v_mfma_f32_16x16x32_bf16 a[8:11], v[160:163], v[136:139], a[8:11]\n"
    "ds_read_b128 v[180:183], v201 offset:5120\n"
    "v_add_f32 v114, v114, v65\n"
    "v_mfma_f32_16x16x32_bf16 a[8:11], v[164:167], v[152:155], a[8:11]\n"
    "buffer_load_dwordx4 v200, s[40:43], s65 offen sc1 lds\n"
    "v_mfma_f32_16x16x32_bf16 a[12:15], v[160:163], v[140:143], a[12:15]\n"
    "v_exp_f32 v38, v38\n"
    "v_mfma_f32_16x16x32_bf16 a[12:15], v[164:167], v[156:159], a[12:15]\n"
    "s_waitcnt lgkmcnt(2)\n"
    "v_exp_f32 v39, v39\n"
    "v_mfma_f32_16x16x32_bf16 a[28:31], v[168:171], v[140:143], a[28:31]\n"
    "ds_read_b128 v[160:163], v201 offset:6144\n"
    "v_add_f32 v113, v113, v66\n"
    "v_mfma_f32_16x16x32_bf16 a[28:31], v[172:175], v[156:159], a[28:31]\n"
    "buffer_load_dwordx4 v200, s[40:43], s65 offen offset:1024 sc1 lds\n"
    "v_mfma_f32_16x16x32_bf16 a[24:27], v[172:175], v[152:155], a[24:27]\n"
    "v_exp_f32 v40, v40\n"
    "v_mfma_f32_16x16x32_bf16 a[24:27], v[168:171], v[136:139], a[24:27]\n"
    "v_exp_f32 v41, v41\n"
    "v_mfma_f32_16x16x32_bf16 a[20:23], v[168:171], v[132:135], a[20:23]\n"
    "ds_read_b128 v[164:167], v201 offset:7168\n"
    "v_add_f32 v114, v114, v67\n"
    "v_mfma_f32_16x16x32_bf16 a[20:23], v[172:175], v[148:151], a[20:23]\n"
    "v_exp_f32 v42, v42\n"
    "v_mfma_f32_16x16x32_bf16 a[16:19], v[172:175], v[144:147], a[16:19]\n"
    "v_exp_f32 v43, v43\n"
    "v_mfma_f32_16x16x32_bf16 a[16:19], v[168:171], v[128:131], a[16:19]\n"
    "s_waitcnt lgkmcnt(2)\n"
    "v_exp_f32 v44, v44\n"
    "v_mfma_f32_16x16x32_bf16 a[32:35], v[176:179], v[128:131], a[32:35]\n"
    "ds_read_b128 v[168:171], v201 offset:8192\n"
    "v_add_f32 v117, v117, v68\n"
    "v_mfma_f32_16x16x32_bf16 a[32:35], v[180:183], v[144:147], a[32:35]\n"
    "buffer_load_dwordx2 v[218:219], v213, s[44:47], 0 offen sc1\n"
    "v_mfma_f32_16x16x32_bf16 a[36:39], v[180:183], v[148:151], a[36:39]\n"
    "v_exp_f32 v45, v45\n"
    "v_mfma_f32_16x16x32_bf16 a[36:39], v[176:179], v[132:135], a[36:39]\n"
    "v_exp_f32 v46, v46\n"
    "v_mfma_f32_16x16x32_bf16 a[40:43], v[176:179], v[136:139], a[40:43]\n"
    "ds_read_b128 v[172:175], v201 offset:9216\n"
    "v_add_f32 v118, v118, v69\n"
    "v_mfma_f32_16x16x32_bf16 a[40:43], v[180:183], v[152:155], a[40:43]\n"
    "v_exp_f32 v47, v47\n"
    "v_mfma_f32_16x16x32_bf16 a[44:47], v[180:183], v[156:159], a[44:47]\n"
    "v_exp_f32 v48, v48\n"
    "v_mfma_f32_16x16x32_bf16 a[44:47], v[176:179], v[140:143], a[44:47]\n"
    "s_waitcnt lgkmcnt(2)\n"
    "v_exp_f32 v49, v49\n"
    "v_mfma_f32_16x16x32_bf16 a[60:63], v[160:163], v[140:143], a[60:63]\n"
    "ds_read_b128 v[176:179], v201 offset:10240\n"
    "v_add_f32 v117, v117, v70\n"
    "v_mfma_f32_16x16x32_bf16 a[60:63], v[164:167], v[156:159], a[60:63]\n"
    "v_exp_f32 v50, v50\n"
    "v_mfma_f32_16x16x32_bf16 a[56:59], v[164:167], v[152:155], a[56:59]\n"
    "v_exp_f32 v51, v51\n"
    "v_mfma_f32_16x16x32_bf16 a[56:59], v[160:163], v[136:139], a[56:59]\n"
    "v_add_f32 v113, v113, v32\n"
    "v_add_f32 v118, v118, v71\n"
    "v_mfma_f32_16x16x32_bf16 a[52:55], v[160:163], v[132:135], a[52:55]\n"
    "ds_read_b128 v[180:183], v201 offset:11264\n"
    "v_add_f32 v114, v114, v33\n"
    "v_mfma_f32_16x16x32_bf16 a[52:55], v[164:167], v[148:151], a[52:55]\n"
    "v_exp_f32 v52, v52\n"
    "v_mfma_f32_16x16x32_bf16 a[48:51], v[164:167], v[144:147], a[48:51]\n"
    "v_add_f32 v121, v121, v72\n"
    "v_add_f32 v113, v113, v34\n"
    "v_mfma_f32_16x16x32_bf16 a[48:51], v[160:163], v[128:131], a[48:51]\n"
    "s_waitcnt lgkmcnt(2)\n"
    "v_exp_f32 v53, v53\n"
    "v_mfma_f32_16x16x32_bf16 a[64:67], v[168:171], v[128:131], a[64:67]\n"
    "ds_read_b128 v[160:163], v201 offset:12288\n"
    "v_add_f32 v122, v122, v73\n"
    "v_mfma_f32_16x16x32_bf16 a[64:67], v[172:175], v[144:147], a[64:67]\n"
    "v_add_f32 v114, v114, v35\n"
    "v_add_f32 v121, v121, v74\n"
    "v_mfma_f32_16x16x32_bf16 a[68:71], v[172:175], v[148:151], a[68:71]\n"
    "v_add_f32 v117, v117, v36\n"
    "v_add_f32 v122, v122, v75\n"
    "v_mfma_f32_16x16x32_bf16 a[68:71], v[168:171], v[132:135], a[68:71]\n"
    "s_add_u32 m0, s51, s95\n"
    "v_exp_f32 v54, v54\n"
    "v_mfma_f32_16x16x32_bf16 a[72:75], v[168:171], v[136:139], a[72:75]\n"
    "ds_read_b128 v[164:167], v201 offset:13312\n"
    "v_add_f32 v118, v118, v37\n"
    "v_mfma_f32_16x16x32_bf16 a[72:75], v[172:175], v[152:155], a[72:75]\n"
    "buffer_load_dwordx4 v202, s[32:35], s64 offen lds\n"
    "v_mfma_f32_16x16x32_bf16 a[76:79], v[172:175], v[156:159], a[76:79]\n"
    "v_add_f32 v125, v125, v76\n"
    "v_add_f32 v117, v117, v38\n"
    "v_mfma_f32_16x16x32_bf16 a[76:79], v[168:171], v[140:143], a[76:79]\n"
    "s_waitcnt lgkmcnt(2)\n"
    "v_exp_f32 v55, v55\n"
    "v_mfma_f32_16x16x32_bf16 a[92:95], v[176:179], v[140:143], a[92:95]\n"
    "ds_read_b128 v[168:171], v201 offset:14336\n"
    "v_add_f32 v126, v126, v77\n"
    "v_mfma_f32_16x16x32_bf16 a[92:95], v[180:183], v[156:159], a[92:95]\n"
    "buffer_load_dwordx4 v203, s[32:35], s64 offen offset:1024 lds\n"
    "v_mfma_f32_16x16x32_bf16 a[88:91], v[180:183], v[152:155], a[88:91]\n"
    "v_add_f32 v118, v118, v39\n"
    "v_add_f32 v125, v125, v78\n"
    "v_mfma_f32_16x16x32_bf16 a[88:91], v[176:179], v[136:139], a[88:91]\n"
    "v_add_f32 v121, v121, v40\n"
    "v_add_f32 v126, v126, v79\n"
    "v_mfma_f32_16x16x32_bf16 a[84:87], v[176:179], v[132:135], a[84:87]\n"
    "ds_read_b128 v[172:175], v201 offset:15360\n"
    "v_add_f32 v122, v122, v41\n"
    "v_mfma_f32_16x16x32_bf16 a[84:87], v[180:183], v[148:151], a[84:87]\n"
    "buffer_load_dwordx4 v204, s[32:35], s64 offen offset:2048 lds\n"
    "v_mfma_f32_16x16x32_bf16 a[80:83], v[180:183], v[144:147], a[80:83]\n"
    "v_exp_f32 v56, v56\n"
    "v_mfma_f32_16x16x32_bf16 a[80:83], v[176:179], v[128:131], a[80:83]\n"
    "s_waitcnt lgkmcnt(2)\n"
    "v_add_f32 v113, v113, v80\n"
    "v_add_f32 v121, v121, v42\n"
    "v_mfma_f32_16x16x32_bf16 a[96:99], v[160:163], v[128:131], a[96:99]\n"
    "s_mov_b32 s95, s94\n"
    "ds_read_b128 v[176:179], v201 offset:16384\n"
    "v_add_f32 v114, v114, v81\n"
    "v_mfma_f32_16x16x32_bf16 a[96:99], v[164:167], v[144:147], a[96:99]\n"
    "s_sub_u32 s94, s94, 0x3000\n"
    "v_exp_f32 v57, v57\n"
    "v_mfma_f32_16x16x32_bf16 a[100:103], v[164:167], v[148:151], a[100:103]\n"
    "s_min_u32 s94, s94, 0x6000\n"
    "v_add_f32 v122, v122, v43\n"
    "v_add_f32 v113, v113, v82\n"
    "v_mfma_f32_16x16x32_bf16 a[100:103], v[160:163], v[132:135], a[100:103]\n"
    "s_sub_u32 s93, s94, s95\n"
    "v_add_f32 v125, v125, v44\n"
    "v_add_f32 v114, v114, v83\n"
    "v_mfma_f32_16x16x32_bf16 a[104:107], v[160:163], v[136:139], a[104:107]\n"
    "ds_read_b128 v[180:183], v201 offset:17408\n"
    "v_add_f32 v126, v126, v45\n"
    "v_mfma_f32_16x16x32_bf16 a[104:107], v[164:167], v[152:155], a[104:107]\n"
    "v_exp_f32 v58, v58\n"
    "v_mfma_f32_16x16x32_bf16 a[108:111], v[164:167], v[156:159], a[108:111]\n"
    "v_add_f32 v117, v117, v84\n"
    "v_add_f32 v125, v125, v46\n"
    "v_mfma_f32_16x16x32_bf16 a[108:111], v[160:163], v[140:143], a[108:111]\n"
    "s_waitcnt lgkmcnt(2)\n"
    "v_exp_f32 v59, v59\n"
    "v_mfma_f32_16x16x32_bf16 a[124:127], v[168:171], v[140:143], a[124:127]\n"
    "ds_read_b128 v[160:163], v201 offset:18432\n"
    "v_add_f32 v118, v118, v85\n"
    "v_mfma_f32_16x16x32_bf16 a[124:127], v[172:175], v[156:159], a[124:127]\n"
    "v_add_f32 v126, v126, v47\n"
    "v_add_f32 v117, v117, v86\n"
    "v_mfma_f32_16x16x32_bf16 a[120:123], v[172:175], v[152:155], a[120:123]\n"
    "v_add_f32 v113, v113, v48\n"
    "v_add_u32 v220, 1, v220\n"
    "v_mfma_f32_16x16x32_bf16 a[120:123], v[168:171], v[136:139], a[120:123]\n"
    "v_exp_f32 v60, v60\n"
    "v_mfma_f32_16x16x32_bf16 a[116:119], v[168:171], v[132:135], a[116:119]\n"
    "ds_read_b128 v[164:167], v201 offset:19456\n"
    "v_add_u32 v205, s93, v205\n"
    "v_mfma_f32_16x16x32_bf16 a[116:119], v[172:175], v[148:151], a[116:119]\n"
    "v_add_f32 v118, v118, v87\n"
    "v_add_f32 v114, v114, v49\n"
    "v_mfma_f32_16x16x32_bf16 a[112:115], v[172:175], v[144:147], a[112:115]\n"
    "v_add_u32 v209, 0x180, v209\n"
    "v_add_u32 v206, s93, v206\n"
    "v_mfma_f32_16x16x32_bf16 a[112:115], v[168:171], v[128:131], a[112:115]\n"
    "s_waitcnt lgkmcnt(2)\n"
    "v_add_f32 v121, v121, v88\n"
    "v_add_f32 v113, v113, v50\n"
    "v_mfma_f32_16x16x32_bf16 a[128:131], v[176:179], v[128:131], a[128:131]\n"
    "s_add_u32 s64, s64, 0xc000\n"
    "ds_read_b128 v[168:171], v201 offset:20480\n"
    "v_add_u32 v210, 0x80, v210\n"
    "v_mfma_f32_16x16x32_bf16 a[128:131], v[180:183], v[144:147], a[128:131]\n"
    "s_add_u32 s62, s62, 0x6000\n"
    "v_exp_f32 v61, v61\n"
    "v_mfma_f32_16x16x32_bf16 a[132:135], v[180:183], v[148:151], a[132:135]\n"
    "s_add_u32 s63, s63, 0x6000\n"
    "v_add_u32 v207, s93, v207\n"
    "v_add_f32 v122, v122, v89\n"
    "v_mfma_f32_16x16x32_bf16 a[132:135], v[176:179], v[132:135], a[132:135]\n"
    "s_add_u32 s60, s60, 0x6000\n"
    "v_add_f32 v114, v114, v51\n"
    "v_add_f32 v121, v121, v90\n"
    "v_mfma_f32_16x16x32_bf16 a[136:139], v[176:179], v[136:139], a[136:139]\n"
    "s_add_u32 s61, s61, 0x6000\n"
    "ds_read_b128 v[172:175], v201 offset:21504\n"
    "v_add_f32 v117, v117, v52\n"
    "v_mfma_f32_16x16x32_bf16 a[136:139], v[180:183], v[152:155], a[136:139]\n"
    "s_add_u32 s66, s66, s74\n"
    "v_exp_f32 v62, v62\n"
    "v_mfma_f32_16x16x32_bf16 a[140:143], v[180:183], v[156:159], a[140:143]\n"
    "s_min_u32 s66, s66, 33\n"
    "v_add_f32 v122, v122, v91\n"
    "v_add_f32 v118, v118, v53\n"
    "v_mfma_f32_16x16x32_bf16 a[140:143], v[176:179], v[140:143], a[140:143]\n"
    "s_waitcnt lgkmcnt(2)\n"
    "v_add_f32 v125, v125, v92\n"
    "v_add_f32 v117, v117, v54\n"
    "v_mfma_f32_16x16x32_bf16 a[156:159], v[160:163], v[140:143], a[156:159]\n"
    "ds_read_b128 v[176:179], v201 offset:22528\n"
    "v_add_f32 v126, v126, v93\n"
    "v_mfma_f32_16x16x32_bf16 a[156:159], v[164:167], v[156:159], a[156:159]\n"
    "s_waitcnt vmcnt(3)\n"
    "ds_read_b128 v[180:183], v201 offset:23552\n"
    "v_add_f32 v118, v118, v55\n"
    "v_mfma_f32_16x16x32_bf16 a[152:155], v[164:167], v[152:155], a[152:155]\n"
    "v_cmp_ne_u32 s[80:81], s67, v219\n"
    "v_add_f32 v125, v125, v94\n"
    "v_mfma_f32_16x16x32_bf16 a[152:155], v[160:163], v[136:139], a[152:155]\n"
    "v_cmp_gt_u32 vcc, s66, v218\n"
    "v_add_f32 v121, v121, v56\n"
    "v_mfma_f32_16x16x32_bf16 a[148:151], v[160:163], v[132:135], a[148:151]\n"
    "v_exp_f32 v63, v63\n"
    "v_mfma_f32_16x16x32_bf16 a[148:151], v[164:167], v[148:151], a[148:151]\n"
    "s_or_b64 vcc, vcc, s[80:81]\n"
    "s_cbranch_vccnz LSPRE4\n"
    "LSPB4:\n"
    "v_add_f32 v126, v126, v95\n"
    "v_mfma_f32_16x16x32_bf16 a[144:147], v[164:167], v[144:147], a[144:147]\n"
    "v_add_f32 v122, v122, v57\n"
    "v_mfma_f32_16x16x32_bf16 a[144:147], v[160:163], v[128:131], a[144:147]\n"
    "s_waitcnt lgkmcnt(0)\n"
    "v_mfma_f32_16x16x32_bf16 a[160:163], v[168:171], v[128:131], a[160:163]\n"
    "s_barrier\n"
    "v_mfma_f32_16x16x32_bf16 a[160:163], v[172:175], v[144:147], a[160:163]\n"
    "buffer_store_dwordx2 v[220:221], v214, s[44:47], 0 offen sc1\n"
    "v_mfma_f32_16x16x32_bf16 a[164:167], v[172:175], v[148:151], a[164:167]\n"
    "ds_read_b128 v[160:163], v200\n"
    "ds_read_u16_d16_hi v223, v209 offset:6144\n"
    "v_mfma_f32_16x16x32_bf16 a[164:167], v[168:171], v[132:135], a[164:167]\n"
    "ds_read_u16_d16_hi v222, v209\n"
    "v_add_f32 v121, v121, v58\n"
    "v_mfma_f32_16x16x32_bf16 a[168:171], v[168:171], v[136:139], a[168:171]\n"
    "ds_read_b128 v[164:167], v200 offset:1024\n"
    "ds_read_b128 v[232:235], v205 offset:24576\n"
    "v_mfma_f32_16x16x32_bf16 a[168:171], v[172:175], v[152:155], a[168:171]\n"
    "v_add_f32 v122, v122, v59\n"
    "ds_read_b128 v[236:239], v206 offset:24576\n"
    "v_mfma_f32_16x16x32_bf16 a[172:175], v[172:175], v[156:159], a[172:175]\n"
    "v_add_f32 v125, v125, v60\n"
    "v_add_f32 v126, v126, v61\n"
    "v_mfma_f32_16x16x32_bf16 a[172:175], v[168:171], v[140:143], a[172:175]\n"
    "v_add_f32 v125, v125, v62\n"
    "v_add_f32 v126, v126, v63\n"
    "v_mfma_f32_16x16x32_bf16 a[188:191], v[176:179], v[140:143], a[188:191]\n"
    "ds_read_b128 v[168:171], v200 offset:2048\n"
    "ds_read_b64 v[228:229], v210\n"
    "v_mfma_f32_16x16x32_bf16 a[188:191], v[180:183], v[156:159], a[188:191]\n"
    "ds_read2st64_b32 v[240:241], v207 offset0:96 offset1:99\n"
    "ds_read2st64_b32 v[242:243], v207 offset0:102 offset1:105\n"
    "v_mfma_f32_16x16x32_bf16 a[184:187], v[176:179], v[136:139], a[184:187]\n"
    "ds_read2st64_b32 v[244:245], v207 offset0:108 offset1:111\n"
    "ds_read2st64_b32 v[246:247], v207 offset0:114 offset1:117\n"
    "v_mfma_f32_16x16x32_bf16 a[184:187], v[180:183], v[152:155], a[184:187]\n"
    "v_cvt_pk_bf16_f32 v140, v44, v45\n"
    "v_cvt_pk_bf16_f32 v141, v46, v47\n"
    "v_mfma_f32_16x16x32_bf16 a[180:183], v[176:179], v[132:135], a[180:183]\n"
    "ds_read_b128 v[172:175], v200 offset:3072\n"
    "s_waitcnt lgkmcnt(7)\n"
    "v_mfma_f32_16x16x32_bf16 a[180:183], v[180:183], v[148:151], a[180:183]\n"
    "v_mul_f32 v226, s75, v223\n"
    "v_fma_f32 v227, -v222, v226, v217\n"
    "v_mfma_f32_16x16x32_bf16 a[176:179], v[176:179], v[128:131], a[176:179]\n"
    "v_cvt_f32_i32 v232, v232\n"
    "v_cvt_f32_i32 v233, v233\n"
    "v_mfma_f32_16x16x32_bf16 a[176:179], v[180:183], v[144:147], a[176:179]\n"
    "v_cvt_f32_i32 v234, v234\n"
    "v_cvt_f32_i32 v235, v235\n"
    "LM0:\n"
    "v_mfma_f32_16x16x32_bf16 v[64:67], v[160:163], a[192:195], v[96:99]\n"
    "ds_read_b128 v[176:179], v200 offset:4096\n"
    "v_cvt_f32_i32 v236, v236\n"
    "v_mfma_f32_16x16x32_bf16 v[64:67], v[164:167], a[196:199], v[64:67]\n"
    "s_waitcnt lgkmcnt(1)\n"
    "v_cvt_f32_i32 v237, v237\n"
    "v_cvt_f32_i32 v238, v238\n"
    "v_mfma_f32_16x16x32_bf16 v[64:67], v[168:171], a[200:203], v[64:67]\n"
    "v_cvt_f32_i32 v239, v239\n"
    "v_mul_f32_dpp v232, v226, v232 row_newbcast:0 row_mask:0xf bank_mask:0xf\n"
    "v_mfma_f32_16x16x32_bf16 v[68:71], v[168:171], a[224:227], v[100:103]\n"
    "v_mul_f32_dpp v233, v226, v233 row_newbcast:1 row_mask:0xf bank_mask:0xf\n"
    "v_mul_f32_dpp v234, v226, v234 row_newbcast:2 row_mask:0xf bank_mask:0xf\n"
    "v_mfma_f32_16x16x32_bf16 v[68:71], v[164:167], a[220:223], v[68:71]\n"
    "ds_read_b128 v[180:183], v200 offset:5120\n"
    "v_mul_f32_dpp v235, v226, v235 row_newbcast:3 row_mask:0xf bank_mask:0xf\n"
    "v_mfma_f32_16x16x32_bf16 v[68:71], v[160:163], a[216:219], v[68:71]\n"
    "v_mul_f32_dpp v236, v226, v236 row_newbcast:4 row_mask:0xf bank_mask:0xf\n"
    "v_mul_f32_dpp v237, v226, v237 row_newbcast:5 row_mask:0xf bank_mask:0xf\n"
    "v_mfma_f32_16x16x32_bf16 v[72:75], v[160:163], a[240:243], v[104:107]\n"
    "v_mul_f32_dpp v238, v226, v238 row_newbcast:6 row_mask:0xf bank_mask:0xf\n"
    "v_mul_f32_dpp v239, v226, v239 row_newbcast:7 row_mask:0xf bank_mask:0xf\n"
    "v_mfma_f32_16x16x32_bf16 v[72:75], v[164:167], a[244:247], v[72:75]\n"
    "v_add_f32_dpp v232, v227, v232 row_newbcast:0 row_mask:0xf bank_mask:0xf\n"
    "v_add_f32_dpp v233, v227, v233 row_newbcast:1 row_mask:0xf bank_mask:0xf\n"
    "v_mfma_f32_16x16x32_bf16 v[72:75], v[168:171], a[248:251], v[72:75]\n"
    "v_add_f32_dpp v234, v227, v234 row_newbcast:2 row_mask:0xf bank_mask:0xf\n"
    "v_add_f32_dpp v235, v227, v235 row_newbcast:3 row_mask:0xf bank_mask:0xf\n"
    "v_mfma_f32_16x16x32_bf16 v[76:79], v[168:171], v[16:19], v[108:111]\n"
    "v_add_f32_dpp v236, v227, v236 row_newbcast:4 row_mask:0xf bank_mask:0xf\n"
    "v_add_f32_dpp v237, v227, v237 row_newbcast:5 row_mask:0xf bank_mask:0xf\n"
    "v_mfma_f32_16x16x32_bf16 v[76:79], v[164:167], v[12:15], v[76:79]\n"
    "v_add_f32_dpp v238, v227, v238 row_newbcast:6 row_mask:0xf bank_mask:0xf\n"
    "v_add_f32_dpp v239, v227, v239 row_newbcast:7 row_mask:0xf bank_mask:0xf\n"
    "v_mfma_f32_16x16x32_bf16 v[76:79], v[160:163], v[8:11], v[76:79]\n"
    "s_waitcnt lgkmcnt(1)\n"
    "v_cvt_pk_bf16_f32 v232, v232, v233\n"
    "v_cvt_f32_i32 v240, v240\n"
    "v_mfma_f32_16x16x32_bf16 v[76:79], v[172:175], v[20:23], v[76:79]\n"
    "ds_read_b128 v[160:163], v200 offset:9216\n"
    "v_cvt_pk_bf16_f32 v233, v234, v235\n"
    "v_mfma_f32_16x16x32_bf16 v[76:79], v[176:179], v[24:27], v[76:79]\n"
    "s_waitcnt lgkmcnt(1)\n"
    "v_cvt_f32_i32 v241, v241\n"
    "v_cvt_pk_bf16_f32 v234, v236, v237\n"
    "v_mfma_f32_16x16x32_bf16 v[76:79], v[180:183], v[28:31], v[76:79]\n"
    "v_cvt_f32_i32 v242, v242\n"
    "v_cvt_pk_bf16_f32 v235, v238, v239\n"
    "v_mfma_f32_16x16x32_bf16 v[72:75], v[180:183], v[4:7], v[72:75]\n"
    "ds_read_b128 v[164:167], v200 offset:10240\n"
    "v_cvt_f32_i32 v243, v243\n"
    "v_mfma_f32_16x16x32_bf16 v[72:75], v[176:179], v[0:3], v[72:75]\n"
    "v_cvt_f32_i32 v244, v244\n"
    "v_cvt_f32_i32 v245, v245\n"
    "v_mfma_f32_16x16x32_bf16 v[72:75], v[172:175], a[252:255], v[72:75]\n"
    "v_cvt_f32_i32 v246, v246\n"
    "v_cvt_f32_i32 v247, v247\n"
    "v_mfma_f32_16x16x32_bf16 v[68:71], v[172:175], a[228:231], v[68:71]\n"
    "ds_read_b128 v[168:171], v200 offset:11264\n"
    "v_mul_f32_dpp v240, v228, v240 row_newbcast:0 row_mask:0xf bank_mask:0xf\n"
    "v_mfma_f32_16x16x32_bf16 v[68:71], v[176:179], a[232:235], v[68:71]\n"
    "v_mul_f32_dpp v241, v228, v241 row_newbcast:1 row_mask:0xf bank_mask:0xf\n"
    "v_mul_f32_dpp v242, v228, v242 row_newbcast:2 row_mask:0xf bank_mask:0xf\n"
    "v_mfma_f32_16x16x32_bf16 v[68:71], v[180:183], a[236:239], v[68:71]\n"
    "v_mul_f32_dpp v243, v228, v243 row_newbcast:3 row_mask:0xf bank_mask:0xf\n"
    "v_mul_f32_dpp v244, v228, v244 row_newbcast:4 row_mask:0xf bank_mask:0xf\n"
    "v_mfma_f32_16x16x32_bf16 v[64:67], v[180:183], a[212:215], v[64:67]\n"
    "v_mul_f32_dpp v245, v228, v245 row_newbcast:5 row_mask:0xf bank_mask:0xf\n"
    "v_mul_f32_dpp v246, v228, v246 row_newbcast:6 row_mask:0xf bank_mask:0xf\n"
    "v_mfma_f32_16x16x32_bf16 v[64:67], v[176:179], a[208:211], v[64:67]\n"
    "buffer_store_dwordx4 v[232:235], v211, s[36:39], s62 offen sc1\n"
    "v_mul_f32_dpp v247, v228, v247 row_newbcast:7 row_mask:0xf bank_mask:0xf\n"
    "v_add_f32_dpp v240, v229, v240 row_newbcast:0 row_mask:0xf bank_mask:0xf\n"
    "v_add_f32_dpp v241, v229, v241 row_newbcast:1 row_mask:0xf bank_mask:0xf\n"
    "v_mfma_f32_16x16x32_bf16 v[64:67], v[172:175], a[204:207], v[64:67]\n"
    "s_waitcnt lgkmcnt(1)\n"
    "v_add_f32_dpp v242, v229, v242 row_newbcast:2 row_mask:0xf bank_mask:0xf\n"
    "v_add_f32_dpp v243, v229, v243 row_newbcast:3 row_mask:0xf bank_mask:0xf\n"
    "v_mfma_f32_16x16x32_bf16 v[80:83], v[160:163], a[204:207], v[96:99]\n"
    "ds_read_b128 v[172:175], v200 offset:6144\n"
    "v_add_f32_dpp v244, v229, v244 row_newbcast:4 row_mask:0xf bank_mask:0xf\n"
    "v_mfma_f32_16x16x32_bf16 v[80:83], v[164:167], a[208:211], v[80:83]\n"
    "s_waitcnt lgkmcnt(1)\n"
    "v_add_f32_dpp v245, v229, v245 row_newbcast:5 row_mask:0xf bank_mask:0xf\n"
    "v_add_f32_dpp v246, v229, v246 row_newbcast:6 row_mask:0xf bank_mask:0xf\n"
    "v_mfma_f32_16x16x32_bf16 v[80:83], v[168:171], a[212:215], v[80:83]\n"
    "v_add_f32_dpp v247, v229, v247 row_newbcast:7 row_mask:0xf bank_mask:0xf\n"
    "v_cvt_pk_bf16_f32 v240, v240, v241\n"
    "v_mfma_f32_16x16x32_bf16 v[84:87], v[168:171], a[236:239], v[100:103]\n"
    "ds_read_b128 v[176:179], v200 offset:7168\n"
    "v_cvt_pk_bf16_f32 v241, v242, v243\n"
    "v_mfma_f32_16x16x32_bf16 v[84:87], v[164:167], a[232:235], v[84:87]\n"
    "v_cvt_pk_bf16_f32 v242, v244, v245\n"
    "v_cvt_pk_bf16_f32 v243, v246, v247\n"
    "v_mfma_f32_16x16x32_bf16 v[84:87], v[160:163], a[228:231], v[84:87]\n"
    "v_cvt_pk_bf16_f32 v136, v40, v41\n"
    "v_cvt_pk_bf16_f32 v137, v42, v43\n"
    "v_mfma_f32_16x16x32_bf16 v[88:91], v[160:163], a[252:255], v[104:107]\n"
    "ds_read_b128 v[180:183], v200 offset:8192\n"
    "v_cvt_pk_bf16_f32 v132, v36, v37\n"
    "v_mfma_f32_16x16x32_bf16 v[88:91], v[164:167], v[0:3], v[88:91]\n"
    "v_cvt_pk_bf16_f32 v133, v38, v39\n"
    "v_cvt_pk_bf16_f32 v128, v32, v33\n"
    "v_mfma_f32_16x16x32_bf16 v[88:91], v[168:171], v[4:7], v[88:91]\n"
    "v_cvt_pk_bf16_f32 v129, v34, v35\n"
    "v_cvt_pk_bf16_f32 v142, v60, v61\n"
    "v_mfma_f32_16x16x32_bf16 v[92:95], v[168:171], v[28:31], v[108:111]\n"
    "v_exp_f32 v64, v64\n"
    "v_mfma_f32_16x16x32_bf16 v[92:95], v[164:167], v[24:27], v[92:95]\n"
    "v_exp_f32 v65, v65\n"
    "v_mfma_f32_16x16x32_bf16 v[92:95], v[160:163], v[20:23], v[92:95]\n"
    "s_waitcnt lgkmcnt(1)\n"
    "v_exp_f32 v66, v66\n"
    "v_mfma_f32_16x16x32_bf16 v[92:95], v[172:175], v[8:11], v[92:95]\n"
    "ds_read_b128 v[160:163], v200 offset:12288\n"
    "v_cvt_pk_bf16_f32 v143, v62, v63\n"
    "v_mfma_f32_16x16x32_bf16 v[92:95], v[176:179], v[12:15], v[92:95]\n"
    "s_waitcnt lgkmcnt(1)\n"
    "v_exp_f32 v67, v67\n"
    "v_mfma_f32_16x16x32_bf16 v[92:95], v[180:183], v[16:19], v[92:95]\n"
    "buffer_store_dwordx4 v[240:243], v212, s[40:43], s63 offen sc1\n"
    "v_exp_f32 v68, v68\n"
    "v_cvt_pk_bf16_f32 v138, v56, v57\n"
    "v_mfma_f32_16x16x32_bf16 v[88:91], v[180:183], a[248:251], v[88:91]\n"
    "ds_read_b128 v[164:167], v200 offset:13312\n"
    "v_cvt_pk_bf16_f32 v139, v58, v59\n"
    "v_mfma_f32_16x16x32_bf16 v[88:91], v[176:179], a[244:247], v[88:91]\n"
    "v_exp_f32 v69, v69\n"
    "v_mfma_f32_16x16x32_bf16 v[88:91], v[172:175], a[240:243], v[88:91]\n"
    "v_exp_f32 v70, v70\n"
    "v_mfma_f32_16x16x32_bf16 v[84:87], v[172:175], a[216:219], v[84:87]\n"
    "ds_read_b128 v[168:171], v200 offset:14336\n"
    "v_cvt_pk_bf16_f32 v134, v52, v53\n"
    "v_mfma_f32_16x16x32_bf16 v[84:87], v[176:179], a[220:223], v[84:87]\n"
    "v_exp_f32 v71, v71\n"
    "v_mfma_f32_16x16x32_bf16 v[84:87], v[180:183], a[224:227], v[84:87]\n"
    "v_exp_f32 v72, v72\n"
    "v_mfma_f32_16x16x32_bf16 v[80:83], v[180:183], a[200:203], v[80:83]\n"
    "v_cvt_pk_bf16_f32 v135, v54, v55\n"
    "v_cvt_pk_bf16_f32 v130, v48, v49\n"
    "v_mfma_f32_16x16x32_bf16 v[80:83], v[176:179], a[196:199], v[80:83]\n"
    "v_exp_f32 v73, v73\n"
    "v_mfma_f32_16x16x32_bf16 v[80:83], v[172:175], a[192:195], v[80:83]\n"
    "s_waitcnt lgkmcnt(1)\n"
    "v_cvt_pk_bf16_f32 v131, v50, v51\n"
    "v_cvt_pk_bf16_f32 v144, v64, v65\n"
    "v_mfma_f32_16x16x32_bf16 v[32:35], v[160:163], a[192:195], v[96:99]\n"
    "ds_read_b128 v[172:175], v200 offset:15360\n"
    "v_cvt_pk_bf16_f32 v145, v66, v67\n"
    "v_mfma_f32_16x16x32_bf16 v[32:35], v[164:167], a[196:199], v[32:35]\n"
    "s_waitcnt lgkmcnt(1)\n"
    "v_exp_f32 v74, v74\n"
    "v_mfma_f32_16x16x32_bf16 v[32:35], v[168:171], a[200:203], v[32:35]\n"
    "v_exp_f32 v75, v75\n"
    "v_mfma_f32_16x16x32_bf16 v[36:39], v[168:171], a[224:227], v[100:103]\n"
    "ds_read_b128 v[176:179], v200 offset:16384\n"
    "v_cvt_pk_bf16_f32 v148, v68, v69\n"
    "v_mfma_f32_16x16x32_bf16 v[36:39], v[164:167], a[220:223], v[36:39]\n"
    "v_exp_f32 v76, v76\n"
    "v_mfma_f32_16x16x32_bf16 v[36:39], v[160:163], a[216:219], v[36:39]\n"
    "v_exp_f32 v77, v77\n"
    "v_mfma_f32_16x16x32_bf16 v[40:43], v[160:163], a[240:243], v[104:107]\n"
    "ds_read_b128 v[180:183], v200 offset:17408\n"
    "v_cvt_pk_bf16_f32 v149, v70, v71\n"
    "v_mfma_f32_16x16x32_bf16 v[40:43], v[164:167], a[244:247], v[40:43]\n"
    "v_exp_f32 v78, v78\n"
    "v_mfma_f32_16x16x32_bf16 v[40:43], v[168:171], a[248:251], v[40:43]\n"
    "v_exp_f32 v79, v79\n"
    "v_mfma_f32_16x16x32_bf16 v[44:47], v[168:171], v[16:19], v[108:111]\n"
    "v_exp_f32 v80, v80\n"
    "v_mfma_f32_16x16x32_bf16 v[44:47], v[164:167], v[12:15], v[44:47]\n"
    "v_exp_f32 v81, v81\n"
    "v_mfma_f32_16x16x32_bf16 v[44:47], v[160:163], v[8:11], v[44:47]\n"
    "s_waitcnt lgkmcnt(1)\n"
    "v_exp_f32 v82, v82\n"
    "v_mfma_f32_16x16x32_bf16 v[44:47], v[172:175], v[20:23], v[44:47]\n"
    "ds_read_b128 v[160:163], v200 offset:21504\n"
    "v_cvt_pk_bf16_f32 v152, v72, v73\n"
    "v_mfma_f32_16x16x32_bf16 v[44:47], v[176:179], v[24:27], v[44:47]\n"
    "s_waitcnt lgkmcnt(1)\n"
    "v_exp_f32 v83, v83\n"
    "v_mfma_f32_16x16x32_bf16 v[44:47], v[180:183], v[28:31], v[44:47]\n"
    "v_exp_f32 v84, v84\n"
    "v_mfma_f32_16x16x32_bf16 v[40:43], v[180:183], v[4:7], v[40:43]\n"
    "ds_read_b128 v[164:167], v200 offset:22528\n"
    "v_cvt_pk_bf16_f32 v153, v74, v75\n"
    "v_mfma_f32_16x16x32_bf16 v[40:43], v[176:179], v[0:3], v[40:43]\n"
    "v_exp_f32 v85, v85\n"
    "v_mfma_f32_16x16x32_bf16 v[40:43], v[172:175], a[252:255], v[40:43]\n"
    "v_exp_f32 v86, v86\n"
    "v_mfma_f32_16x16x32_bf16 v[36:39], v[172:175], a[228:231], v[36:39]\n"
    "ds_read_b128 v[168:171], v200 offset:23552\n"
    "v_cvt_pk_bf16_f32 v156, v76, v77\n"
    "v_mfma_f32_16x16x32_bf16 v[36:39], v[176:179], a[232:235], v[36:39]\n"
    "v_exp_f32 v87, v87\n"
    "v_mfma_f32_16x16x32_bf16 v[36:39], v[180:183], a[236:239], v[36:39]\n"
    "v_exp_f32 v88, v88\n"
    "v_mfma_f32_16x16x32_bf16 v[32:35], v[180:183], a[212:215], v[32:35]\n"
    "v_exp_f32 v89, v89\n"
    "v_mfma_f32_16x16x32_bf16 v[32:35], v[176:179], a[208:211], v[32:35]\n"
    "v_exp_f32 v90, v90\n"
    "v_mfma_f32_16x16x32_bf16 v[32:35], v[172:175], a[204:207], v[32:35]\n"
    "s_waitcnt lgkmcnt(1)\n"
    "s_mov_b32 m0, 0x6000\n"
    "v_cvt_pk_bf16_f32 v157, v78, v79\n"
    "v_mfma_f32_16x16x32_bf16 v[48:51], v[160:163], a[204:207], v[96:99]\n"
    "ds_read_b128 v[172:175], v200 offset:18432\n"
    "v_cvt_pk_bf16_f32 v146, v80, v81\n"
    "v_mfma_f32_16x16x32_bf16 v[48:51], v[164:167], a[208:211], v[48:51]\n"
    "s_waitcnt lgkmcnt(1)\n"
    "buffer_load_dwordx4 v200, s[36:39], s60 offen sc1 lds\n"
    "v_mfma_f32_16x16x32_bf16 v[48:51], v[168:171], a[212:215], v[48:51]\n"
    "v_exp_f32 v91, v91\n"
    "v_mfma_f32_16x16x32_bf16 v[52:55], v[168:171], a[236:239], v[100:103]\n"
    "ds_read_b128 v[176:179], v200 offset:19456\n"
    "v_cvt_pk_bf16_f32 v147, v82, v83\n"
    "v_mfma_f32_16x16x32_bf16 v[52:55], v[164:167], a[232:235], v[52:55]\n"
    "v_exp_f32 v92, v92\n"
    "v_mfma_f32_16x16x32_bf16 v[52:55], v[160:163], a[228:231], v[52:55]\n"
    "buffer_load_dwordx4 v200, s[36:39], s60 offen offset:1024 sc1 lds\n"
    "v_mfma_f32_16x16x32_bf16 v[56:59], v[160:163], a[252:255], v[104:107]\n"
    "ds_read_b128 v[180:183], v200 offset:20480\n"
    "v_cvt_pk_bf16_f32 v150, v84, v85\n"
    "v_mfma_f32_16x16x32_bf16 v[56:59], v[164:167], v[0:3], v[56:59]\n"
    "v_exp_f32 v93, v93\n"
    "v_mfma_f32_16x16x32_bf16 v[56:59], v[168:171], v[4:7], v[56:59]\n"
    "v_cvt_pk_bf16_f32 v151, v86, v87\n"
    "v_cvt_pk_bf16_f32 v154, v88, v89\n"
    "v_mfma_f32_16x16x32_bf16 v[60:63], v[168:171], v[28:31], v[108:111]\n"
    "buffer_load_dwordx4 v200, s[36:39], s60 offen offset:2048 sc1 lds\n"
    "v_mfma_f32_16x16x32_bf16 v[60:63], v[164:167], v[24:27], v[60:63]\n"
    "v_exp_f32 v94, v94\n"
    "v_mfma_f32_16x16x32_bf16 v[60:63], v[160:163], v[20:23], v[60:63]\n"
    "s_waitcnt lgkmcnt(1)\n"
    "s_add_u32 m0, s71, 0xc000\n"
    "v_cvt_pk_bf16_f32 v155, v90, v91\n"
    "v_mfma_f32_16x16x32_bf16 v[60:63], v[172:175], v[8:11], v[60:63]\n"
    "ds_read_b128 v[160:163], v201 offset:24576\n"
    "v_cvt_pk_bf16_f32 v158, v92, v93\n"
    "v_mfma_f32_16x16x32_bf16 v[60:63], v[176:179], v[12:15], v[60:63]\n"
    "s_waitcnt lgkmcnt(1)\n"
    "buffer_load_dwordx4 v200, s[40:43], s61 offen sc1 lds\n"
    "v_mfma_f32_16x16x32_bf16 v[60:63], v[180:183], v[16:19], v[60:63]\n"
    "v_exp_f32 v95, v95\n"
    "v_mfma_f32_16x16x32_bf16 v[56:59], v[180:183], a[248:251], v[56:59]\n"
    "ds_read_b128 v[164:167], v201 offset:25600\n"
    "v_cvt_pk_bf16_f32 v159, v94, v95\n"
    "v_mfma_f32_16x16x32_bf16 v[56:59], v[176:179], a[244:247], v[56:59]\n"
    "v_exp_f32 v32, v32\n"
    "v_mfma_f32_16x16x32_bf16 v[56:59], v[172:175], a[240:243], v[56:59]\n"
    "buffer_load_dwordx4 v200, s[40:43], s61 offen offset:1024 sc1 lds\n"
    "v_mfma_f32_16x16x32_bf16 v[52:55], v[172:175], a[216:219], v[52:55]\n"
    "ds_read_b128 v[168:171], v201 offset:26624\n"
    "v_add_f32 v113, v113, v64\n"
    "v_mfma_f32_16x16x32_bf16 v[52:55], v[176:179], a[220:223], v[52:55]\n"
    "v_exp_f32 v33, v33\n"
    "v_mfma_f32_16x16x32_bf16 v[52:55], v[180:183], a[224:227], v[52:55]\n"
    "v_exp_f32 v34, v34\n"
    "v_mfma_f32_16x16x32_bf16 v[48:51], v[180:183], a[200:203], v[48:51]\n"
    "buffer_load_dwordx4 v200, s[40:43], s61 offen offset:2048 sc1 lds\n"
    "v_mfma_f32_16x16x32_bf16 v[48:51], v[176:179], a[196:199], v[48:51]\n"
    "v_exp_f32 v35, v35\n"
    "v_mfma_f32_16x16x32_bf16 v[48:51], v[172:175], a[192:195], v[48:51]\n"
    "s_waitcnt lgkmcnt(1)\n"
    "v_exp_f32 v36, v36\n"
    "v_mfma_f32_16x16x32_bf16 a[0:3], v[160:163], v[128:131], a[0:3]\n"
    "ds_read_b128 v[172:175], v201 offset:27648\n"
    "ds_read_b128 v[176:179], v201 offset:28672\n"
    "v_mfma_f32_16x16x32_bf16 a[0:3], v[164:167], v[144:147], a[0:3]\n"
    "buffer_load_dwordx4 v200, s[40:43], s61 offen offset:3072 sc1 lds\n"
    "v_mfma_f32_16x16x32_bf16 a[4:7], v[160:163], v[132:135], a[4:7]\n"
    "s_add_u32 s65, s61, 0x1000\n"
    "v_exp_f32 v37, v37\n"
    "v_mfma_f32_16x16x32_bf16 a[4:7], v[164:167], v[148:151], a[4:7]\n"
    "s_add_u32 m0, s71, 0xd000\n"
    "v_exp_f32 v38, v38\n"
    "v_mfma_f32_16x16x32_bf16 a[8:11], v[160:163], v[136:139], a[8:11]\n"
    "ds_read_b128 v[180:183], v201 offset:29696\n"
    "v_add_f32 v114, v114, v65\n"
    "v_mfma_f32_16x16x32_bf16 a[8:11], v[164:167], v[152:155], a[8:11]\n"
    "buffer_load_dwordx4 v200, s[40:43], s65 offen sc1 lds\n"
    "v_mfma_f32_16x16x32_bf16 a[12:15], v[160:163], v[140:143], a[12:15]\n"
    "v_exp_f32 v39, v39\n"
    "v_mfma_f32_16x16x32_bf16 a[12:15], v[164:167], v[156:159], a[12:15]\n"
    "s_waitcnt lgkmcnt(2)\n"
    "v_exp_f32 v40, v40\n"
    "v_mfma_f32_16x16x32_bf16 a[28:31], v[168:171], v[140:143], a[28:31]\n"
    "ds_read_b128 v[160:163], v201 offset:30720\n"
    "v_add_f32 v113, v113, v66\n"
    "v_mfma_f32_16x16x32_bf16 a[28:31], v[172:175], v[156:159], a[28:31]\n"
    "buffer_load_dwordx4 v200, s[40:43], s65 offen offset:1024 sc1 lds\n"
    "v_mfma_f32_16x16x32_bf16 a[24:27], v[172:175], v[152:155], a[24:27]\n"
    "v_exp_f32 v41, v41\n"
    "v_mfma_f32_16x16x32_bf16 a[24:27], v[168:171], v[136:139], a[24:27]\n"
    "v_exp_f32 v42, v42\n"
    "v_mfma_f32_16x16x32_bf16 a[20:23], v[168:171], v[132:135], a[20:23]\n"
    "ds_read_b128 v[164:167], v201 offset:31744\n"
    "v_add_f32 v114, v114, v67\n"
    "v_mfma_f32_16x16x32_bf16 a[20:23], v[172:175], v[148:151], a[20:23]\n"
    "v_exp_f32 v43, v43\n"
    "v_mfma_f32_16x16x32_bf16 a[16:19], v[172:175], v[144:147], a[16:19]\n"
    "v_exp_f32 v44, v44\n"
    "v_mfma_f32_16x16x32_bf16 a[16:19], v[168:171], v[128:131], a[16:19]\n"
    "s_waitcnt lgkmcnt(2)\n"
    "v_exp_f32 v45, v45\n"
    "v_mfma_f32_16x16x32_bf16 a[32:35], v[176:179], v[128:131], a[32:35]\n"
    "ds_read_b128 v[168:171], v201 offset:32768\n"
    "v_add_f32 v117, v117, v68\n"
    "v_mfma_f32_16x16x32_bf16 a[32:35], v[180:183], v[144:147], a[32:35]\n"
    "buffer_load_dwordx2 v[218:219], v213, s[44:47], 0 offen sc1\n"
    "v_mfma_f32_16x16x32_bf16 a[36:39], v[180:183], v[148:151], a[36:39]\n"
    "v_exp_f32 v46, v46\n"
    "v_mfma_f32_16x16x32_bf16 a[36:39], v[176:179], v[132:135], a[36:39]\n"
    "v_exp_f32 v47, v47\n"
    "v_mfma_f32_16x16x32_bf16 a[40:43], v[176:179], v[136:139], a[40:43]\n"
    "ds_read_b128 v[172:175], v201 offset:33792\n"
    "v_add_f32 v118, v118, v69\n"
    "v_mfma_f32_16x16x32_bf16 a[40:43], v[180:183], v[152:155], a[40:43]\n"
    "v_exp_f32 v48, v48\n"
    "v_mfma_f32_16x16x32_bf16 a[44:47], v[180:183], v[156:159], a[44:47]\n"
    "v_exp_f32 v49, v49\n"
    "v_mfma_f32_16x16x32_bf16 a[44:47], v[176:179], v[140:143], a[44:47]\n"
    "s_waitcnt lgkmcnt(2)\n"
    "v_exp_f32 v50, v50\n"
    "v_mfma_f32_16x16x32_bf16 a[60:63], v[160:163], v[140:143], a[60:63]\n"
    "ds_read_b128 v[176:179], v201 offset:34816\n"
    "v_add_f32 v117, v117, v70\n"
    "v_mfma_f32_16x16x32_bf16 a[60:63], v[164:167], v[156:159], a[60:63]\n"
    "v_exp_f32 v51, v51\n"
    "v_mfma_f32_16x16x32_bf16 a[56:59], v[164:167], v[152:155], a[56:59]\n"
    "v_add_f32 v113, v113, v32\n"
    "v_add_f32 v118, v118, v71\n"
    "v_mfma_f32_16x16x32_bf16 a[56:59], v[160:163], v[136:139], a[56:59]\n"
    "v_exp_f32 v52, v52\n"
    "v_mfma_f32_16x16x32_bf16 a[52:55], v[160:163], v[132:135], a[52:55]\n"
    "ds_read_b128 v[180:183], v201 offset:35840\n"
    "v_add_f32 v114, v114, v33\n"
    "v_mfma_f32_16x16x32_bf16 a[52:55], v[164:167], v[148:151], a[52:55]\n"
    "v_add_f32 v121, v121, v72\n"
    "v_add_f32 v113, v113, v34\n"
    "v_mfma_f32_16x16x32_bf16 a[48:51], v[164:167], v[144:147], a[48:51]\n"
    "v_exp_f32 v53, v53\n"
    "v_mfma_f32_16x16x32_bf16 a[48:51], v[160:163], v[128:131], a[48:51]\n"
    "s_waitcnt lgkmcnt(2)\n"
    "v_add_f32 v122, v122, v73\n"
    "v_add_f32 v114, v114, v35\n"
    "v_mfma_f32_16x16x32_bf16 a[64:67], v[168:171], v[128:131], a[64:67]\n"
    "ds_read_b128 v[160:163], v201 offset:36864\n"
    "v_add_f32 v121, v121, v74\n"
    "v_mfma_f32_16x16x32_bf16 a[64:67], v[172:175], v[144:147], a[64:67]\n"
    "v_add_f32 v117, v117, v36\n"
    "v_add_f32 v122, v122, v75\n"
    "v_mfma_f32_16x16x32_bf16 a[68:71], v[172:175], v[148:151], a[68:71]\n"
    "v_exp_f32 v54, v54\n"
    "v_mfma_f32_16x16x32_bf16 a[68:71], v[168:171], v[132:135], a[68:71]\n"
    "s_add_u32 m0, s51, s95\n"
    "v_add_f32 v118, v118, v37\n"
    "v_add_f32 v125, v125, v76\n"
    "v_mfma_f32_16x16x32_bf16 a[72:75], v[168:171], v[136:139], a[72:75]\n"
    "ds_read_b128 v[164:167], v201 offset:37888\n"
    "v_add_f32 v117, v117, v38\n"
    "v_mfma_f32_16x16x32_bf16 a[72:75], v[172:175], v[152:155], a[72:75]\n"
    "buffer_load_dwordx4 v202, s[32:35], s64 offen lds\n"
    "v_mfma_f32_16x16x32_bf16 a[76:79], v[172:175], v[156:159], a[76:79]\n"
    "v_exp_f32 v55, v55\n"
    "v_mfma_f32_16x16x32_bf16 a[76:79], v[168:171], v[140:143], a[76:79]\n"
    "s_waitcnt lgkmcnt(2)\n"
    "v_add_f32 v126, v126, v77\n"
    "v_add_f32 v118, v118, v39\n"
    "v_mfma_f32_16x16x32_bf16 a[92:95], v[176:179], v[140:143], a[92:95]\n"
    "ds_read_b128 v[168:171], v201 offset:38912\n"
    "v_add_f32 v125, v125, v78\n"
    "v_mfma_f32_16x16x32_bf16 a[92:95], v[180:183], v[156:159], a[92:95]\n"
    "buffer_load_dwordx4 v203, s[32:35], s64 offen offset:1024 lds\n"
    "v_mfma_f32_16x16x32_bf16 a[88:91], v[180:183], v[152:155], a[88:91]\n"
    "v_add_f32 v121, v121, v40\n"
    "v_add_f32 v126, v126, v79\n"
    "v_mfma_f32_16x16x32_bf16 a[88:91], v[176:179], v[136:139], a[88:91]\n"
    "v_exp_f32 v56, v56\n"
    "v_mfma_f32_16x16x32_bf16 a[84:87], v[176:179], v[132:135], a[84:87]\n"
    "ds_read_b128 v[172:175], v201 offset:39936\n"
    "v_add_f32 v122, v122, v41\n"
    "v_mfma_f32_16x16x32_bf16 a[84:87], v[180:183], v[148:151], a[84:87]\n"
    "buffer_load_dwordx4 v204, s[32:35], s64 offen offset:2048 lds\n"
    "v_mfma_f32_16x16x32_bf16 a[80:83], v[180:183], v[144:147], a[80:83]\n"
    "v_add_f32 v113, v113, v80\n"
    "v_add_f32 v121, v121, v42\n"
    "v_mfma_f32_16x16x32_bf16 a[80:83], v[176:179], v[128:131], a[80:83]\n"
    "s_waitcnt lgkmcnt(2)\n"
    "v_exp_f32 v57, v57\n"
    "v_mfma_f32_16x16x32_bf16 a[96:99], v[160:163], v[128:131], a[96:99]\n"
    "s_mov_b32 s95, s94\n"
    "ds_read_b128 v[176:179], v201 offset:40960\n"
    "v_add_f32 v114, v114, v81\n"
    "v_mfma_f32_16x16x32_bf16 a[96:99], v[164:167], v[144:147], a[96:99]\n"
    "s_sub_u32 s94, s94, 0x3000\n"
    "v_add_f32 v122, v122, v43\n"
    "v_add_f32 v113, v113, v82\n"
    "v_mfma_f32_16x16x32_bf16 a[100:103], v[164:167], v[148:151], a[100:103]\n"
    "s_min_u32 s94, s94, 0x6000\n"
    "v_add_f32 v125, v125, v44\n"
    "v_add_f32 v114, v114, v83\n"
    "v_mfma_f32_16x16x32_bf16 a[100:103], v[160:163], v[132:135], a[100:103]\n"
    "s_sub_u32 s93, s94, s95\n"
    "v_exp_f32 v58, v58\n"
    "v_mfma_f32_16x16x32_bf16 a[104:107], v[160:163], v[136:139], a[104:107]\n"
    "ds_read_b128 v[180:183], v201 offset:41984\n"
    "v_add_f32 v126, v126, v45\n"
    "v_mfma_f32_16x16x32_bf16 a[104:107], v[164:167], v[152:155], a[104:107]\n"
    "v_add_f32 v117, v117, v84\n"
    "v_add_f32 v125, v125, v46\n"
    "v_mfma_f32_16x16x32_bf16 a[108:111], v[164:167], v[156:159], a[108:111]\n"
    "v_exp_f32 v59, v59\n"
    "v_mfma_f32_16x16x32_bf16 a[108:111], v[160:163], v[140:143], a[108:111]\n"
    "s_waitcnt lgkmcnt(2)\n"
    "v_add_f32 v118, v118, v85\n"
    "v_add_f32 v126, v126, v47\n"
    "v_mfma_f32_16x16x32_bf16 a[124:127], v[168:171], v[140:143], a[124:127]\n"
    "ds_read_b128 v[160:163], v201 offset:43008\n"
    "v_add_f32 v117, v117, v86\n"
    "v_mfma_f32_16x16x32_bf16 a[124:127], v[172:175], v[156:159], a[124:127]\n"
    "v_add_f32 v113, v113, v48\n"
    "v_add_u32 v205, s93, v205\n"
    "v_mfma_f32_16x16x32_bf16 a[120:123], v[172:175], v[152:155], a[120:123]\n"
    "v_exp_f32 v60, v60\n"
    "v_mfma_f32_16x16x32_bf16 a[120:123], v[168:171], v[136:139], a[120:123]\n"
    "v_add_f32 v118, v118, v87\n"
    "v_add_f32 v114, v114, v49\n"
    "v_mfma_f32_16x16x32_bf16 a[116:119], v[168:171], v[132:135], a[116:119]\n"
    "ds_read_b128 v[164:167], v201 offset:44032\n"
    "v_add_u32 v206, s93, v206\n"
    "v_mfma_f32_16x16x32_bf16 a[116:119], v[172:175], v[148:151], a[116:119]\n"
    "v_add_f32 v121, v121, v88\n"
    "v_add_f32 v113, v113, v50\n"
    "v_mfma_f32_16x16x32_bf16 a[112:115], v[172:175], v[144:147], a[112:115]\n"
    "v_exp_f32 v61, v61\n"
    "v_mfma_f32_16x16x32_bf16 a[112:115], v[168:171], v[128:131], a[112:115]\n"
    "s_waitcnt lgkmcnt(2)\n"
    "v_add_u32 v220, 1, v220\n"
    "v_add_u32 v207, s93, v207\n"
    "v_mfma_f32_16x16x32_bf16 a[128:131], v[176:179], v[128:131], a[128:131]\n"
    "s_add_u32 s64, s64, 0xc000\n"
    "ds_read_b128 v[168:171], v201 offset:45056\n"
    "v_add_f32 v122, v122, v89\n"
    "v_mfma_f32_16x16x32_bf16 a[128:131], v[180:183], v[144:147], a[128:131]\n"
    "s_add_u32 s62, s62, 0x6000\n"
    "v_add_f32 v114, v114, v51\n"
    "v_add_f32 v121, v121, v90\n"
    "v_mfma_f32_16x16x32_bf16 a[132:135], v[180:183], v[148:151], a[132:135]\n"
    "s_add_u32 s63, s63, 0x6000\n"
    "v_add_f32 v117, v117, v52\n"
    "v_add_f32 v122, v122, v91\n"
    "v_mfma_f32_16x16x32_bf16 a[132:135], v[176:179], v[132:135], a[132:135]\n"
    "s_add_u32 s60, s60, 0x6000\n"
    "v_exp_f32 v62, v62\n"
    "v_mfma_f32_16x16x32_bf16 a[136:139], v[176:179], v[136:139], a[136:139]\n"
    "s_add_u32 s61, s61, 0x6000\n"
    "ds_read_b128 v[172:175], v201 offset:46080\n"
    "v_add_f32 v118, v118, v53\n"
    "v_mfma_f32_16x16x32_bf16 a[136:139], v[180:183], v[152:155], a[136:139]\n"
    "s_add_u32 s66, s66, s74\n"
    "v_add_f32 v125, v125, v92\n"
    "v_add_f32 v117, v117, v54\n"
    "v_mfma_f32_16x16x32_bf16 a[140:143], v[180:183], v[156:159], a[140:143]\n"
    "s_min_u32 s66, s66, 33\n"
    "v_exp_f32 v63, v63\n"
    "v_mfma_f32_16x16x32_bf16 a[140:143], v[176:179], v[140:143], a[140:143]\n"
    "s_waitcnt lgkmcnt(2)\n"
    "v_add_f32 v126, v126, v93\n"
    "v_add_f32 v118, v118, v55\n"
    "v_mfma_f32_16x16x32_bf16 a[156:159], v[160:163], v[140:143], a[156:159]\n"
    "ds_read_b128 v[176:179], v201 offset:47104\n"
    "v_add_f32 v125, v125, v94\n"
    "v_mfma_f32_16x16x32_bf16 a[156:159], v[164:167], v[156:159], a[156:159]\n"
    "s_waitcnt vmcnt(3)\n"
    "ds_read_b128 v[180:183], v201 offset:48128\n"
    "v_add_f32 v121, v121, v56\n"
    "v_mfma_f32_16x16x32_bf16 a[152:155], v[164:167], v[152:155], a[152:155]\n"
    "v_cmp_ne_u32 s[80:81], s67, v219\n"
    "v_add_f32 v126, v126, v95\n"
    "v_mfma_f32_16x16x32_bf16 a[152:155], v[160:163], v[136:139], a[152:155]\n"
    "v_cmp_gt_u32 vcc, s66, v218\n"
    "v_add_f32 v122, v122, v57\n"
    "v_mfma_f32_16x16x32_bf16 a[148:151], v[160:163], v[132:135], a[148:151]\n"
    "v_mfma_f32_16x16x32_bf16 a[148:151], v[164:167], v[148:151], a[148:151]\n"
    "s_or_b64 vcc, vcc, s[80:81]\n"
    "s_cbranch_vccnz LSPRE3\n"
    "LSPB3:\n"
    "v_mfma_f32_16x16x32_bf16 a[144:147], v[164:167], v[144:147], a[144:147]\n"
    "v_mfma_f32_16x16x32_bf16 a[144:147], v[160:163], v[128:131], a[144:147]\n"
    "s_waitcnt lgkmcnt(0)\n"
    "v_mfma_f32_16x16x32_bf16 a[160:163], v[168:171], v[128:131], a[160:163]\n"
    "s_barrier\n"
    "LMID:\n"
    "v_mfma_f32_16x16x32_bf16 a[160:163], v[172:175], v[144:147], a[160:163]\n"
    "buffer_store_dwordx2 v[220:221], v214, s[44:47], 0 offen sc1\n"
    "v_mfma_f32_16x16x32_bf16 a[164:167], v[172:175], v[148:151], a[164:167]\n"
    "ds_read_b128 v[160:163], v200 offset:24576\n"
    "ds_read_u16_d16_hi v223, v209 offset:6336\n"
    "v_mfma_f32_16x16x32_bf16 a[164:167], v[168:171], v[132:135], a[164:167]\n"
    "ds_read_u16_d16_hi v222, v209 offset:192\n"
    "v_add_f32 v121, v121, v58\n"
    "v_mfma_f32_16x16x32_bf16 a[168:171], v[168:171], v[136:139], a[168:171]\n"
    "ds_read_b128 v[164:167], v200 offset:25600\n"
    "ds_read_b128 v[232:235], v205 offset:24576\n"
    "v_mfma_f32_16x16x32_bf16 a[168:171], v[172:175], v[152:155], a[168:171]\n"
    "v_add_f32 v122, v122, v59\n"
    "ds_read_b128 v[236:239], v206 offset:24576\n"
    "v_mfma_f32_16x16x32_bf16 a[172:175], v[172:175], v[156:159], a[172:175]\n"
    "v_add_f32 v125, v125, v60\n"
    "v_add_f32 v126, v126, v61\n"
    "v_mfma_f32_16x16x32_bf16 a[172:175], v[168:171], v[140:143], a[172:175]\n"
    "v_add_f32 v125, v125, v62\n"
    "v_add_f32 v126, v126, v63\n"
    "v_mfma_f32_16x16x32_bf16 a[188:191], v[176:179], v[140:143], a[188:191]\n"
    "ds_read_b128 v[168:171], v200 offset:26624\n"
    "ds_read_b64 v[228:229], v210 offset:64\n"
    "v_mfma_f32_16x16x32_bf16 a[188:191], v[180:183], v[156:159], a[188:191]\n"
    "ds_read2st64_b32 v[240:241], v207 offset0:96 offset1:99\n"
    "ds_read2st64_b32 v[242:243], v207 offset0:102 offset1:105\n"
    "v_mfma_f32_16x16x32_bf16 a[184:187], v[176:179], v[136:139], a[184:187]\n"
    "ds_read2st64_b32 v[244:245], v207 offset0:108 offset1:111\n"
    "ds_read2st64_b32 v[246:247], v207 offset0:114 offset1:117\n"
    "v_mfma_f32_16x16x32_bf16 a[184:187], v[180:183], v[152:155], a[184:187]\n"
    "v_cvt_pk_bf16_f32 v140, v44, v45\n"
    "v_cvt_pk_bf16_f32 v141, v46, v47\n"
    "v_mfma_f32_16x16x32_bf16 a[180:183], v[176:179], v[132:135], a[180:183]\n"
    "s_add_u32 s59, s59, 1\n"
    "ds_read_b128 v[172:175], v200 offset:27648\n"
    "s_waitcnt lgkmcnt(7)\n"
    "v_mfma_f32_16x16x32_bf16 a[180:183], v[180:183], v[148:151], a[180:183]\n"
    "v_mul_f32 v226, s75, v223\n"
    "v_fma_f32 v227, -v222, v226, v217\n"
    "v_mfma_f32_16x16x32_bf16 a[176:179], v[176:179], v[128:131], a[176:179]\n"
    "s_cmp_lt_u32 s59, 15\n"
    "v_cvt_f32_i32 v232, v232\n"
    "v_cvt_f32_i32 v233, v233\n"
    "v_mfma_f32_16x16x32_bf16 a[176:179], v[180:183], v[144:147], a[176:179]\n"
    "s_cbranch_scc1 LLOOP\n"
    "LTAIL:\n"
    "s_waitcnt lgkmcnt(0)\n"
    "s_nop 1\n"
    "v_mfma_f32_16x16x32_bf16 v[64:67], v[160:163], a[192:195], v[96:99]\n"
    "ds_read_b128 v[176:179], v200 offset:28672\n"
    "v_cvt_pk_bf16_f32 v128, v32, v33\n"
    "v_mfma_f32_16x16x32_bf16 v[64:67], v[164:167], a[196:199], v[64:67]\n"
    "s_add_u32 m0, s71, 0\n"
    "v_cvt_pk_bf16_f32 v129, v34, v35\n"
    "v_cvt_pk_bf16_f32 v132, v36, v37\n"
    "v_mfma_f32_16x16x32_bf16 v[68:71], v[164:167], a[220:223], v[100:103]\n"
    "buffer_load_dwordx4 v200, s[36:39], s60 offen sc1 lds\n"
    "v_mfma_f32_16x16x32_bf16 v[68:71], v[160:163], a[216:219], v[68:71]\n"
    "v_cvt_pk_bf16_f32 v133, v38, v39\n"
    "v_cvt_pk_bf16_f32 v136, v40, v41\n"
    "v_mfma_f32_16x16x32_bf16 v[72:75], v[160:163], a[240:243], v[104:107]\n"
    "ds_read_b128 v[180:183], v200 offset:29696\n"
    "v_cvt_pk_bf16_f32 v137, v42, v43\n"
    "v_mfma_f32_16x16x32_bf16 v[72:75], v[164:167], a[244:247], v[72:75]\n"
    "v_cvt_pk_bf16_f32 v130, v48, v49\n"
    "v_cvt_pk_bf16_f32 v131, v50, v51\n"
    "v_mfma_f32_16x16x32_bf16 v[76:79], v[164:167], v[12:15], v[108:111]\n"
    "buffer_load_dwordx4 v200, s[36:39], s60 offen offset:1024 sc1 lds\n"
    "v_mfma_f32_16x16x32_bf16 v[76:79], v[160:163], v[8:11], v[76:79]\n"
    "v_cvt_pk_bf16_f32 v134, v52, v53\n"
    "v_cvt_pk_bf16_f32 v135, v54, v55\n"
    "v_mfma_f32_16x16x32_bf16 v[76:79], v[168:171], v[16:19], v[76:79]\n"
    "ds_read_b128 v[160:163], v200 offset:34816\n"
    "v_cvt_pk_bf16_f32 v138, v56, v57\n"
    "v_mfma_f32_16x16x32_bf16 v[76:79], v[172:175], v[20:23], v[76:79]\n"
    "v_cvt_pk_bf16_f32 v139, v58, v59\n"
    "v_cvt_pk_bf16_f32 v142, v60, v61\n"
    "v_mfma_f32_16x16x32_bf16 v[72:75], v[172:175], a[252:255], v[72:75]\n"
    "buffer_load_dwordx4 v200, s[36:39], s60 offen offset:2048 sc1 lds\n"
    "v_mfma_f32_16x16x32_bf16 v[72:75], v[168:171], a[248:251], v[72:75]\n"
    "v_cvt_pk_bf16_f32 v143, v62, v63\n"
    "v_add_f32 v113, v113, v114\n"
    "v_mfma_f32_16x16x32_bf16 v[68:71], v[168:171], a[224:227], v[68:71]\n"
    "ds_read_b128 v[164:167], v200 offset:35840\n"
    "v_add_f32 v117, v117, v118\n"
    "v_mfma_f32_16x16x32_bf16 v[68:71], v[172:175], a[228:231], v[68:71]\n"
    "v_add_f32 v121, v121, v122\n"
    "v_add_f32 v125, v125, v126\n"
    "v_mfma_f32_16x16x32_bf16 v[64:67], v[172:175], a[204:207], v[64:67]\n"
    "buffer_load_dwordx4 v200, s[36:39], s60 offen offset:3072 sc1 lds\n"
    "v_mfma_f32_16x16x32_bf16 v[64:67], v[168:171], a[200:203], v[64:67]\n"
    "s_waitcnt lgkmcnt(2)\n"
    "v_mov_b32 v115, v113\n"
    "v_mov_b32 v119, v117\n"
    "v_mfma_f32_16x16x32_bf16 v[64:67], v[176:179], a[208:211], v[64:67]\n"
    "s_add_u32 s65, s60, 0x1000\n"
    "ds_read_b128 v[168:171], v200 offset:32768\n"
    "v_mov_b32 v123, v121\n"
    "v_mfma_f32_16x16x32_bf16 v[64:67], v[180:183], a[212:215], v[64:67]\n"
    "s_add_u32 m0, s71, 0x1000\n"
    "v_mov_b32 v127, v125\n"
    "v_permlane32_swap_b32 v113, v115\n"
    "v_mfma_f32_16x16x32_bf16 v[68:71], v[180:183], a[236:239], v[68:71]\n"
    "buffer_load_dwordx4 v200, s[36:39], s65 offen sc1 lds\n"
    "v_mfma_f32_16x16x32_bf16 v[68:71], v[176:179], a[232:235], v[68:71]\n"
    "v_permlane32_swap_b32 v117, v119\n"
    "v_permlane32_swap_b32 v121, v123\n"
    "v_mfma_f32_16x16x32_bf16 v[72:75], v[176:179], v[0:3], v[72:75]\n"
    "ds_read_b128 v[172:175], v200 offset:33792\n"
    "v_permlane32_swap_b32 v125, v127\n"
    "v_mfma_f32_16x16x32_bf16 v[72:75], v[180:183], v[4:7], v[72:75]\n"
    "v_add_f32 v113, v113, v115\n"
    "v_add_f32 v117, v117, v119\n"
    "v_mfma_f32_16x16x32_bf16 v[76:79], v[180:183], v[28:31], v[76:79]\n"
    "buffer_load_dwordx4 v200, s[36:39], s65 offen offset:1024 sc1 lds\n"
    "v_mfma_f32_16x16x32_bf16 v[76:79], v[176:179], v[24:27], v[76:79]\n"
    "s_waitcnt lgkmcnt(2)\n"
    "v_add_f32 v121, v121, v123\n"
    "v_add_f32 v125, v125, v127\n"
    "v_mfma_f32_16x16x32_bf16 v[92:95], v[160:163], v[24:27], v[108:111]\n"
    "ds_read_b128 v[176:179], v200 offset:30720\n"
    "v_mov_b32 v115, v113\n"
    "v_mfma_f32_16x16x32_bf16 v[92:95], v[164:167], v[28:31], v[92:95]\n"
    "s_add_u32 m0, s71, 0x12000\n"
    "v_exp_f32 v64, v64\n"
    "v_mfma_f32_16x16x32_bf16 v[88:91], v[164:167], v[4:7], v[104:107]\n"
    "buffer_load_dwordx4 v200, s[40:43], s61 offen sc1 lds\n"
    "v_mfma_f32_16x16x32_bf16 v[88:91], v[160:163], v[0:3], v[88:91]\n"
    "v_exp_f32 v65, v65\n"
    "v_mfma_f32_16x16x32_bf16 v[84:87], v[160:163], a[232:235], v[100:103]\n"
    "ds_read_b128 v[180:183], v200 offset:31744\n"
    "v_cvt_pk_bf16_f32 v144, v64, v65\n"
    "v_mfma_f32_16x16x32_bf16 v[84:87], v[164:167], a[236:239], v[84:87]\n"
    "v_exp_f32 v66, v66\n"
    "v_mfma_f32_16x16x32_bf16 v[80:83], v[164:167], a[212:215], v[96:99]\n"
    "buffer_load_dwordx4 v200, s[40:43], s61 offen offset:1024 sc1 lds\n"
    "v_mfma_f32_16x16x32_bf16 v[80:83], v[160:163], a[208:211], v[80:83]\n"
    "s_waitcnt lgkmcnt(2)\n"
    "v_exp_f32 v67, v67\n"
    "v_mfma_f32_16x16x32_bf16 v[80:83], v[168:171], a[200:203], v[80:83]\n"
    "ds_read_b128 v[160:163], v200 offset:36864\n"
    "v_cvt_pk_bf16_f32 v145, v66, v67\n"
    "v_mfma_f32_16x16x32_bf16 v[80:83], v[172:175], a[204:207], v[80:83]\n"
    "v_exp_f32 v68, v68\n"
    "v_mfma_f32_16x16x32_bf16 v[84:87], v[172:175], a[228:231], v[84:87]\n"
    "buffer_load_dwordx4 v200, s[40:43], s61 offen offset:2048 sc1 lds\n"
    "v_mfma_f32_16x16x32_bf16 v[84:87], v[168:171], a[224:227], v[84:87]\n"
    "v_exp_f32 v69, v69\n"
    "v_mfma_f32_16x16x32_bf16 v[88:91], v[168:171], a[248:251], v[88:91]\n"
    "ds_read_b128 v[164:167], v200 offset:37888\n"
    "v_cvt_pk_bf16_f32 v148, v68, v69\n"
    "v_mfma_f32_16x16x32_bf16 v[88:91], v[172:175], a[252:255], v[88:91]\n"
    "v_exp_f32 v70, v70\n"
    "v_mfma_f32_16x16x32_bf16 v[92:95], v[172:175], v[20:23], v[92:95]\n"
    "buffer_load_dwordx4 v200, s[40:43], s61 offen offset:3072 sc1 lds\n"
    "v_mfma_f32_16x16x32_bf16 v[92:95], v[168:171], v[16:19], v[92:95]\n"
    "s_waitcnt lgkmcnt(2)\n"
    "v_exp_f32 v71, v71\n"
    "v_mfma_f32_16x16x32_bf16 v[92:95], v[176:179], v[8:11], v[92:95]\n"
    "s_add_u32 s65, s61, 0x1000\n"
    "ds_read_b128 v[168:171], v200 offset:38912\n"
    "v_cvt_pk_bf16_f32 v149, v70, v71\n"
    "v_mfma_f32_16x16x32_bf16 v[92:95], v[180:183], v[12:15], v[92:95]\n"
    "s_add_u32 m0, s71, 0x13000\n"
    "v_exp_f32 v72, v72\n"
    "v_mfma_f32_16x16x32_bf16 v[88:91], v[180:183], a[244:247], v[88:91]\n"
    "buffer_load_dwordx4 v200, s[40:43], s65 offen sc1 lds\n"
    "v_mfma_f32_16x16x32_bf16 v[88:91], v[176:179], a[240:243], v[88:91]\n"
    "v_exp_f32 v73, v73\n"
    "v_mfma_f32_16x16x32_bf16 v[84:87], v[176:179], a[216:219], v[84:87]\n"
    "ds_read_b128 v[172:175], v200 offset:39936\n"
    "v_cvt_pk_bf16_f32 v152, v72, v73\n"
    "v_mfma_f32_16x16x32_bf16 v[84:87], v[180:183], a[220:223], v[84:87]\n"
    "v_exp_f32 v74, v74\n"
    "v_mfma_f32_16x16x32_bf16 v[80:83], v[180:183], a[196:199], v[80:83]\n"
    "buffer_load_dwordx4 v200, s[40:43], s65 offen offset:1024 sc1 lds\n"
    "v_mfma_f32_16x16x32_bf16 v[80:83], v[176:179], a[192:195], v[80:83]\n"
    "s_waitcnt lgkmcnt(2)\n"
    "v_exp_f32 v75, v75\n"
    "v_mfma_f32_16x16x32_bf16 v[32:35], v[160:163], a[192:195], v[96:99]\n"
    "ds_read_b128 v[176:179], v200 offset:40960\n"
    "v_cvt_pk_bf16_f32 v153, v74, v75\n"
    "v_mfma_f32_16x16x32_bf16 v[32:35], v[164:167], a[196:199], v[32:35]\n"
    "v_exp_f32 v76, v76\n"
    "v_mfma_f32_16x16x32_bf16 v[36:39], v[164:167], a[220:223], v[100:103]\n"
    "v_exp_f32 v77, v77\n"
    "v_mfma_f32_16x16x32_bf16 v[36:39], v[160:163], a[216:219], v[36:39]\n"
    "v_exp_f32 v78, v78\n"
    "v_mfma_f32_16x16x32_bf16 v[40:43], v[160:163], a[240:243], v[104:107]\n"
    "ds_read_b128 v[180:183], v200 offset:41984\n"
    "v_cvt_pk_bf16_f32 v156, v76, v77\n"
    "v_mfma_f32_16x16x32_bf16 v[40:43], v[164:167], a[244:247], v[40:43]\n"
    "v_exp_f32 v79, v79\n"
    "v_mfma_f32_16x16x32_bf16 v[44:47], v[164:167], v[12:15], v[108:111]\n"
    "v_exp_f32 v80, v80\n"
    "v_mfma_f32_16x16x32_bf16 v[44:47], v[160:163], v[8:11], v[44:47]\n"
    "s_waitcnt lgkmcnt(2)\n"
    "v_exp_f32 v81, v81\n"
    "v_mfma_f32_16x16x32_bf16 v[44:47], v[168:171], v[16:19], v[44:47]\n"
    "ds_read_b128 v[160:163], v200 offset:47104\n"
    "v_cvt_pk_bf16_f32 v157, v78, v79\n"
    "v_mfma_f32_16x16x32_bf16 v[44:47], v[172:175], v[20:23], v[44:47]\n"
    "v_exp_f32 v82, v82\n"
    "v_mfma_f32_16x16x32_bf16 v[40:43], v[172:175], a[252:255], v[40:43]\n"
    "v_exp_f32 v83, v83\n"
    "v_mfma_f32_16x16x32_bf16 v[40:43], v[168:171], a[248:251], v[40:43]\n"
    "v_exp_f32 v84, v84\n"
    "v_mfma_f32_16x16x32_bf16 v[36:39], v[168:171], a[224:227], v[36:39]\n"
    "ds_read_b128 v[164:167], v200 offset:48128\n"
    "v_cvt_pk_bf16_f32 v146, v80, v81\n"
    "v_mfma_f32_16x16x32_bf16 v[36:39], v[172:175], a[228:231], v[36:39]\n"
    "v_exp_f32 v85, v85\n"
    "v_mfma_f32_16x16x32_bf16 v[32:35], v[172:175], a[204:207], v[32:35]\n"
    "v_exp_f32 v86, v86\n"
    "v_mfma_f32_16x16x32_bf16 v[32:35], v[168:171], a[200:203], v[32:35]\n"
    "s_waitcnt lgkmcnt(2)\n"
    "v_exp_f32 v87, v87\n"
    "v_mfma_f32_16x16x32_bf16 v[32:35], v[176:179], a[208:211], v[32:35]\n"
    "ds_read_b128 v[168:171], v200 offset:45056\n"
    "v_cvt_pk_bf16_f32 v147, v82, v83\n"
    "v_mfma_f32_16x16x32_bf16 v[32:35], v[180:183], a[212:215], v[32:35]\n"
    "v_exp_f32 v88, v88\n"
    "v_mfma_f32_16x16x32_bf16 v[36:39], v[180:183], a[236:239], v[36:39]\n"
    "v_exp_f32 v89, v89\n"
    "v_mfma_f32_16x16x32_bf16 v[36:39], v[176:179], a[232:235], v[36:39]\n"
    "v_exp_f32 v90, v90\n"
    "v_mfma_f32_16x16x32_bf16 v[40:43], v[176:179], v[0:3], v[40:43]\n"
    "ds_read_b128 v[172:175], v200 offset:46080\n"
    "v_cvt_pk_bf16_f32 v150, v84, v85\n"
    "v_mfma_f32_16x16x32_bf16 v[40:43], v[180:183], v[4:7], v[40:43]\n"
    "v_exp_f32 v91, v91\n"
    "v_mfma_f32_16x16x32_bf16 v[44:47], v[180:183], v[28:31], v[44:47]\n"
    "v_exp_f32 v92, v92\n"
    "v_mfma_f32_16x16x32_bf16 v[44:47], v[176:179], v[24:27], v[44:47]\n"
    "s_waitcnt lgkmcnt(2)\n"
    "v_exp_f32 v93, v93\n"
    "v_mfma_f32_16x16x32_bf16 v[60:63], v[160:163], v[24:27], v[108:111]\n"
    "ds_read_b128 v[176:179], v200 offset:43008\n"
    "v_cvt_pk_bf16_f32 v151, v86, v87\n"
    "v_mfma_f32_16x16x32_bf16 v[60:63], v[164:167], v[28:31], v[60:63]\n"
    "v_exp_f32 v94, v94\n"
    "v_mfma_f32_16x16x32_bf16 v[56:59], v[164:167], v[4:7], v[104:107]\n"
    "v_exp_f32 v95, v95\n"
    "v_mfma_f32_16x16x32_bf16 v[56:59], v[160:163], v[0:3], v[56:59]\n"
    "v_exp_f32 v32, v32\n"
    "v_mfma_f32_16x16x32_bf16 v[52:55], v[160:163], a[232:235], v[100:103]\n"
    "ds_read_b128 v[180:183], v200 offset:44032\n"
    "v_cvt_pk_bf16_f32 v154, v88, v89\n"
    "v_mfma_f32_16x16x32_bf16 v[52:55], v[164:167], a[236:239], v[52:55]\n"
    "v_exp_f32 v33, v33\n"
    "v_mfma_f32_16x16x32_bf16 v[48:51], v[164:167], a[212:215], v[96:99]\n"
    "s_waitcnt vmcnt(6)\n"
    "v_exp_f32 v34, v34\n"
    "v_mfma_f32_16x16x32_bf16 v[48:51], v[160:163], a[208:211], v[48:51]\n"
    "s_waitcnt lgkmcnt(2)\n"
    "v_exp_f32 v35, v35\n"
    "v_mfma_f32_16x16x32_bf16 v[48:51], v[168:171], a[200:203], v[48:51]\n"
    "s_barrier\n"
    "v_exp_f32 v36, v36\n"
    "v_mfma_f32_16x16x32_bf16 v[48:51], v[172:175], a[204:207], v[48:51]\n"
    "ds_read_b128 v[160:163], v200\n"
    "v_cvt_pk_bf16_f32 v155, v90, v91\n"
    "v_mfma_f32_16x16x32_bf16 v[52:55], v[172:175], a[228:231], v[52:55]\n"
    "v_exp_f32 v37, v37\n"
    "v_mfma_f32_16x16x32_bf16 v[52:55], v[168:171], a[224:227], v[52:55]\n"
    "v_exp_f32 v38, v38\n"
    "v_mfma_f32_16x16x32_bf16 v[56:59], v[168:171], a[248:251], v[56:59]\n"
    "ds_read_b128 v[164:167], v200 offset:1024\n"
    "v_cvt_pk_bf16_f32 v158, v92, v93\n"
    "v_mfma_f32_16x16x32_bf16 v[56:59], v[172:175], a[252:255], v[56:59]\n"
    "buffer_load_dwordx2 v[218:219], v213, s[44:47], 0 offen sc1\n"
    "v_mfma_f32_16x16x32_bf16 v[60:63], v[172:175], v[20:23], v[60:63]\n"
    "v_exp_f32 v39, v39\n"
    "v_mfma_f32_16x16x32_bf16 v[60:63], v[168:171], v[16:19], v[60:63]\n"
    "s_waitcnt lgkmcnt(2)\n"
    "v_exp_f32 v40, v40\n"
    "v_mfma_f32_16x16x32_bf16 v[60:63], v[176:179], v[8:11], v[60:63]\n"
    "ds_read_b128 v[168:171], v200 offset:2048\n"
    "v_cvt_pk_bf16_f32 v159, v94, v95\n"
    "v_mfma_f32_16x16x32_bf16 v[60:63], v[180:183], v[12:15], v[60:63]\n"
    "v_exp_f32 v41, v41\n"
    "v_mfma_f32_16x16x32_bf16 v[56:59], v[180:183], a[244:247], v[56:59]\n"
    "v_exp_f32 v42, v42\n"
    "v_mfma_f32_16x16x32_bf16 v[56:59], v[176:179], a[240:243], v[56:59]\n"
    "v_exp_f32 v43, v43\n"
    "v_mfma_f32_16x16x32_bf16 v[52:55], v[176:179], a[216:219], v[52:55]\n"
    "ds_read_b128 v[172:175], v200 offset:3072\n"
    "v_cvt_pk_bf16_f32 v32, v32, v33\n"
    "v_mfma_f32_16x16x32_bf16 v[52:55], v[180:183], a[220:223], v[52:55]\n"
    "v_exp_f32 v44, v44\n"
    "v_mfma_f32_16x16x32_bf16 v[48:51], v[180:183], a[196:199], v[48:51]\n"
    "v_exp_f32 v45, v45\n"
    "v_mfma_f32_16x16x32_bf16 v[48:51], v[176:179], a[192:195], v[48:51]\n"
    "s_waitcnt lgkmcnt(2)\n"
    "v_exp_f32 v46, v46\n"
    "v_mfma_f32_16x16x32_bf16 v[64:67], v[160:163], a[192:195], v[96:99]\n"
    "ds_read_b128 v[176:179], v200 offset:4096\n"
    "v_cvt_pk_bf16_f32 v33, v34, v35\n"
    "v_mfma_f32_16x16x32_bf16 v[64:67], v[164:167], a[196:199], v[64:67]\n"
    "v_exp_f32 v47, v47\n"
    "v_mfma_f32_16x16x32_bf16 v[68:71], v[164:167], a[220:223], v[100:103]\n"
    "v_cvt_pk_bf16_f32 v36, v36, v37\n"
    "v_cvt_pk_bf16_f32 v37, v38, v39\n"
    "v_mfma_f32_16x16x32_bf16 v[68:71], v[160:163], a[216:219], v[68:71]\n"
    "v_cvt_pk_bf16_f32 v40, v40, v41\n"
    "v_cvt_pk_bf16_f32 v41, v42, v43\n"
    "v_mfma_f32_16x16x32_bf16 v[72:75], v[160:163], a[240:243], v[104:107]\n"
    "ds_read_b128 v[180:183], v200 offset:5120\n"
    "v_cvt_pk_bf16_f32 v44, v44, v45\n"
    "v_mfma_f32_16x16x32_bf16 v[72:75], v[164:167], a[244:247], v[72:75]\n"
    "v_cvt_pk_bf16_f32 v45, v46, v47\n"
    "v_mov_b32 v119, v117\n"
    "v_mfma_f32_16x16x32_bf16 v[76:79], v[164:167], v[12:15], v[108:111]\n"
    "v_exp_f32 v48, v48\n"
    "v_mfma_f32_16x16x32_bf16 v[76:79], v[160:163], v[8:11], v[76:79]\n"
    "s_waitcnt lgkmcnt(2)\n"
    "v_exp_f32 v49, v49\n"
    "v_mfma_f32_16x16x32_bf16 v[76:79], v[168:171], v[16:19], v[76:79]\n"
    "ds_read_b128 v[160:163], v200 offset:10240\n"
    "v_cvt_pk_bf16_f32 v34, v48, v49\n"
    "v_mfma_f32_16x16x32_bf16 v[76:79], v[172:175], v[20:23], v[76:79]\n"
    "v_exp_f32 v50, v50\n"
    "v_mfma_f32_16x16x32_bf16 v[72:75], v[172:175], a[252:255], v[72:75]\n"
    "v_exp_f32 v51, v51\n"
    "v_mfma_f32_16x16x32_bf16 v[72:75], v[168:171], a[248:251], v[72:75]\n"
    "v_exp_f32 v52, v52\n"
    "v_mfma_f32_16x16x32_bf16 v[68:71], v[168:171], a[224:227], v[68:71]\n"
    "ds_read_b128 v[164:167], v200 offset:11264\n"
    "v_cvt_pk_bf16_f32 v35, v50, v51\n"
    "v_mfma_f32_16x16x32_bf16 v[68:71], v[172:175], a[228:231], v[68:71]\n"
    "v_exp_f32 v53, v53\n"
    "v_mfma_f32_16x16x32_bf16 v[64:67], v[172:175], a[204:207], v[64:67]\n"
    "v_exp_f32 v54, v54\n"
    "v_mfma_f32_16x16x32_bf16 v[64:67], v[168:171], a[200:203], v[64:67]\n"
    "s_waitcnt lgkmcnt(2)\n"
    "v_exp_f32 v55, v55\n"
    "v_mfma_f32_16x16x32_bf16 v[64:67], v[176:179], a[208:211], v[64:67]\n"
    "s_add_u32 s66, s66, s74\n"
    "ds_read_b128 v[168:171], v200 offset:8192\n"
    "v_cvt_pk_bf16_f32 v38, v52, v53\n"
    "v_mfma_f32_16x16x32_bf16 v[64:67], v[180:183], a[212:215], v[64:67]\n"
    "s_add_u32 s60, s72, 0xbd000\n"
    "v_exp_f32 v56, v56\n"
    "v_mfma_f32_16x16x32_bf16 v[68:71], v[180:183], a[236:239], v[68:71]\n"
    "s_add_u32 s61, s71, 0xba000\n"
    "v_exp_f32 v57, v57\n"
    "v_mfma_f32_16x16x32_bf16 v[68:71], v[176:179], a[232:235], v[68:71]\n"
    "v_exp_f32 v58, v58\n"
    "v_mfma_f32_16x16x32_bf16 v[72:75], v[176:179], v[0:3], v[72:75]\n"
    "ds_read_b128 v[172:175], v200 offset:9216\n"
    "v_cvt_pk_bf16_f32 v39, v54, v55\n"
    "v_mfma_f32_16x16x32_bf16 v[72:75], v[180:183], v[4:7], v[72:75]\n"
    "v_exp_f32 v59, v59\n"
    "v_mfma_f32_16x16x32_bf16 v[76:79], v[180:183], v[28:31], v[76:79]\n"
    "s_waitcnt vmcnt(0)\n"
    "v_cmp_ne_u32 s[80:81], s67, v219\n"
    "v_cvt_pk_bf16_f32 v42, v56, v57\n"
    "v_mfma_f32_16x16x32_bf16 v[76:79], v[176:179], v[24:27], v[76:79]\n"
    "s_waitcnt lgkmcnt(2)\n"
    "v_cmp_gt_u32 vcc, s66, v218\n"
    "v_cvt_pk_bf16_f32 v43, v58, v59\n"
    "v_mfma_f32_16x16x32_bf16 v[92:95], v[160:163], v[24:27], v[108:111]\n"
    "ds_read_b128 v[176:179], v200 offset:6144\n"
    "v_mov_b32 v123, v121\n"
    "v_mfma_f32_16x16x32_bf16 v[92:95], v[164:167], v[28:31], v[92:95]\n"
    "s_or_b64 vcc, vcc, s[80:81]\n"
    "v_exp_f32 v64, v64\n"
    "v_mfma_f32_16x16x32_bf16 v[88:91], v[164:167], v[4:7], v[104:107]\n"
    "s_cbranch_vccnz LSPRE5\n"
    "LSPB5:\n"
    "v_exp_f32 v65, v65\n"
    "v_mfma_f32_16x16x32_bf16 v[88:91], v[160:163], v[0:3], v[88:91]\n"
    "v_exp_f32 v66, v66\n"
    "v_mfma_f32_16x16x32_bf16 v[84:87], v[160:163], a[232:235], v[100:103]\n"
    "s_barrier\n"
    "ds_read_b128 v[180:183], v200 offset:7168\n"
    "v_cvt_pk_bf16_f32 v64, v64, v65\n"
    "v_mfma_f32_16x16x32_bf16 v[84:87], v[164:167], a[236:239], v[84:87]\n"
    "s_add_u32 m0, s72, 0x6000\n"
    "v_exp_f32 v67, v67\n"
    "v_mfma_f32_16x16x32_bf16 v[80:83], v[164:167], a[212:215], v[96:99]\n"
    "buffer_load_dwordx4 v200, s[36:39], s60 offen sc1 lds\n"
    "v_mfma_f32_16x16x32_bf16 v[80:83], v[160:163], a[208:211], v[80:83]\n"
    "s_waitcnt lgkmcnt(2)\n"
    "v_exp_f32 v68, v68\n"
    "v_mfma_f32_16x16x32_bf16 v[80:83], v[168:171], a[200:203], v[80:83]\n"
    "ds_read_b128 v[160:163], v200 offset:12288\n"
    "v_cvt_pk_bf16_f32 v65, v66, v67\n"
    "v_mfma_f32_16x16x32_bf16 v[80:83], v[172:175], a[204:207], v[80:83]\n"
    "buffer_load_dwordx4 v200, s[36:39], s60 offen offset:1024 sc1 lds\n"
    "v_mfma_f32_16x16x32_bf16 v[84:87], v[172:175], a[228:231], v[84:87]\n"
    "v_exp_f32 v69, v69\n"
    "v_mfma_f32_16x16x32_bf16 v[84:87], v[168:171], a[224:227], v[84:87]\n"
    "buffer_load_dwordx4 v200, s[36:39], s60 offen offset:2048 sc1 lds\n"
    "v_mfma_f32_16x16x32_bf16 v[88:91], v[168:171], a[248:251], v[88:91]\n"
    "ds_read_b128 v[164:167], v200 offset:13312\n"
    "v_cvt_pk_bf16_f32 v68, v68, v69\n"
    "v_mfma_f32_16x16x32_bf16 v[88:91], v[172:175], a[252:255], v[88:91]\n"
    "v_exp_f32 v60, v60\n"
    "v_mfma_f32_16x16x32_bf16 v[92:95], v[172:175], v[20:23], v[92:95]\n"
    "s_add_u32 m0, s71, 0x18000\n"
    "v_exp_f32 v70, v70\n"
    "v_mfma_f32_16x16x32_bf16 v[92:95], v[168:171], v[16:19], v[92:95]\n"
    "s_waitcnt lgkmcnt(2)\n"
    "buffer_load_dwordx4 v200, s[40:43], s61 offen sc1 lds\n"
    "v_mfma_f32_16x16x32_bf16 v[92:95], v[176:179], v[8:11], v[92:95]\n"
    "ds_read_b128 v[168:171], v200 offset:14336\n"
    "v_mov_b32 v127, v125\n"
    "v_mfma_f32_16x16x32_bf16 v[92:95], v[180:183], v[12:15], v[92:95]\n"
    "v_exp_f32 v61, v61\n"
    "v_mfma_f32_16x16x32_bf16 v[88:91], v[180:183], a[244:247], v[88:91]\n"
    "v_exp_f32 v71, v71\n"
    "v_mfma_f32_16x16x32_bf16 v[88:91], v[176:179], a[240:243], v[88:91]\n"
    "v_exp_f32 v62, v62\n"
    "v_mfma_f32_16x16x32_bf16 v[84:87], v[176:179], a[216:219], v[84:87]\n"
    "ds_read_b128 v[172:175], v200 offset:15360\n"
    "v_cvt_pk_bf16_f32 v46, v60, v61\n"
    "v_mfma_f32_16x16x32_bf16 v[84:87], v[180:183], a[220:223], v[84:87]\n"
    "v_exp_f32 v72, v72\n"
    "v_mfma_f32_16x16x32_bf16 v[80:83], v[180:183], a[196:199], v[80:83]\n"
    "v_exp_f32 v63, v63\n"
    "v_mfma_f32_16x16x32_bf16 v[80:83], v[176:179], a[192:195], v[80:83]\n"
    "s_waitcnt lgkmcnt(2)\n"
    "buffer_load_dwordx4 v200, s[40:43], s61 offen offset:1024 sc1 lds\n"
    "v_mfma_f32_16x16x32_bf16 v[232:235], v[160:163], a[192:195], v[96:99]\n"
    "ds_read_b128 v[176:179], v200 offset:16384\n"
    "v_cvt_pk_bf16_f32 v47, v62, v63\n"
    "v_mfma_f32_16x16x32_bf16 v[232:235], v[164:167], a[196:199], v[232:235]\n"
    "v_exp_f32 v73, v73\n"
    "v_mfma_f32_16x16x32_bf16 v[236:239], v[164:167], a[220:223], v[100:103]\n"
    "v_exp_f32 v74, v74\n"
    "v_mfma_f32_16x16x32_bf16 v[236:239], v[160:163], a[216:219], v[236:239]\n"
    "v_exp_f32 v75, v75\n"
    "v_mfma_f32_16x16x32_bf16 v[240:243], v[160:163], a[240:243], v[104:107]\n"
    "ds_read_b128 v[180:183], v200 offset:17408\n"
    "v_cvt_pk_bf16_f32 v69, v70, v71\n"
    "v_mfma_f32_16x16x32_bf16 v[240:243], v[164:167], a[244:247], v[240:243]\n"
    "v_exp_f32 v76, v76\n"
    "v_mfma_f32_16x16x32_bf16 v[244:247], v[164:167], v[12:15], v[108:111]\n"
    "v_exp_f32 v77, v77\n"
    "v_mfma_f32_16x16x32_bf16 v[244:247], v[160:163], v[8:11], v[244:247]\n"
    "s_waitcnt lgkmcnt(2)\n"
    "buffer_load_dwordx4 v200, s[40:43], s61 offen offset:2048 sc1 lds\n"
    "v_mfma_f32_16x16x32_bf16 v[244:247], v[168:171], v[16:19], v[244:247]\n"
    "ds_read_b128 v[160:163], v200 offset:22528\n"
    "v_cvt_pk_bf16_f32 v72, v72, v73\n"
    "v_mfma_f32_16x16x32_bf16 v[244:247], v[172:175], v[20:23], v[244:247]\n"
    "v_exp_f32 v78, v78\n"
    "v_mfma_f32_16x16x32_bf16 v[240:243], v[172:175], a[252:255], v[240:243]\n"
    "v_exp_f32 v79, v79\n"
    "v_mfma_f32_16x16x32_bf16 v[240:243], v[168:171], a[248:251], v[240:243]\n"
    "v_exp_f32 v80, v80\n"
    "v_mfma_f32_16x16x32_bf16 v[236:239], v[168:171], a[224:227], v[236:239]\n"
    "ds_read_b128 v[164:167], v200 offset:23552\n"
    "v_cvt_pk_bf16_f32 v73, v74, v75\n"
    "v_mfma_f32_16x16x32_bf16 v[236:239], v[172:175], a[228:231], v[236:239]\n"
    "v_exp_f32 v81, v81\n"
    "v_mfma_f32_16x16x32_bf16 v[232:235], v[172:175], a[204:207], v[232:235]\n"
    "v_exp_f32 v82, v82\n"
    "v_mfma_f32_16x16x32_bf16 v[232:235], v[168:171], a[200:203], v[232:235]\n"
    "s_waitcnt lgkmcnt(2)\n"
    "buffer_load_dwordx4 v200, s[40:43], s61 offen offset:3072 sc1 lds\n"
    "v_mfma_f32_16x16x32_bf16 v[232:235], v[176:179], a[208:211], v[232:235]\n"
    "ds_read_b128 v[168:171], v200 offset:20480\n"
    "v_cvt_pk_bf16_f32 v76, v76, v77\n"
    "v_mfma_f32_16x16x32_bf16 v[232:235], v[180:183], a[212:215], v[232:235]\n"
    "v_exp_f32 v83, v83\n"
    "v_mfma_f32_16x16x32_bf16 v[236:239], v[180:183], a[236:239], v[236:239]\n"
    "v_exp_f32 v84, v84\n"
    "v_mfma_f32_16x16x32_bf16 v[236:239], v[176:179], a[232:235], v[236:239]\n"
    "v_exp_f32 v85, v85\n"
    "v_mfma_f32_16x16x32_bf16 v[240:243], v[176:179], v[0:3], v[240:243]\n"
    "ds_read_b128 v[172:175], v200 offset:21504\n"
    "v_cvt_pk_bf16_f32 v77, v78, v79\n"
    "v_mfma_f32_16x16x32_bf16 v[240:243], v[180:183], v[4:7], v[240:243]\n"
    "s_add_u32 s65, s61, 0x1000\n"
    "v_exp_f32 v86, v86\n"
    "v_mfma_f32_16x16x32_bf16 v[244:247], v[180:183], v[28:31], v[244:247]\n"
    "s_add_u32 m0, s71, 0x19000\n"
    "v_exp_f32 v87, v87\n"
    "v_mfma_f32_16x16x32_bf16 v[244:247], v[176:179], v[24:27], v[244:247]\n"
    "s_waitcnt lgkmcnt(2)\n"
    "buffer_load_dwordx4 v200, s[40:43], s65 offen sc1 lds\n"
    "v_mfma_f32_16x16x32_bf16 v[60:63], v[160:163], v[24:27], v[108:111]\n"
    "ds_read_b128 v[176:179], v200 offset:18432\n"
    "v_cvt_pk_bf16_f32 v66, v80, v81\n"
    "v_mfma_f32_16x16x32_bf16 v[60:63], v[164:167], v[28:31], v[60:63]\n"
    "v_exp_f32 v88, v88\n"
    "v_mfma_f32_16x16x32_bf16 v[56:59], v[164:167], v[4:7], v[104:107]\n"
    "v_exp_f32 v89, v89\n"
    "v_mfma_f32_16x16x32_bf16 v[56:59], v[160:163], v[0:3], v[56:59]\n"
    "v_exp_f32 v90, v90\n"
    "v_mfma_f32_16x16x32_bf16 v[52:55], v[160:163], a[232:235], v[100:103]\n"
    "ds_read_b128 v[180:183], v200 offset:19456\n"
    "v_cvt_pk_bf16_f32 v67, v82, v83\n"
    "v_mfma_f32_16x16x32_bf16 v[52:55], v[164:167], a[236:239], v[52:55]\n"
    "v_exp_f32 v91, v91\n"
    "v_mfma_f32_16x16x32_bf16 v[48:51], v[164:167], a[212:215], v[96:99]\n"
    "s_waitcnt vmcnt(5)\n"
    "v_exp_f32 v92, v92\n"
    "v_mfma_f32_16x16x32_bf16 v[48:51], v[160:163], a[208:211], v[48:51]\n"
    "s_waitcnt lgkmcnt(2)\n"
    "buffer_load_dwordx4 v200, s[40:43], s65 offen offset:1024 sc1 lds\n"
    "v_mfma_f32_16x16x32_bf16 v[48:51], v[168:171], a[200:203], v[48:51]\n"
    "s_barrier\n"
    "v_exp_f32 v93, v93\n"
    "v_mfma_f32_16x16x32_bf16 v[48:51], v[172:175], a[204:207], v[48:51]\n"
    "ds_read_b128 v[160:163], v200 offset:24576\n"
    "v_cvt_pk_bf16_f32 v70, v84, v85\n"
    "v_mfma_f32_16x16x32_bf16 v[52:55], v[172:175], a[228:231], v[52:55]\n"
    "v_exp_f32 v94, v94\n"
    "v_mfma_f32_16x16x32_bf16 v[52:55], v[168:171], a[224:227], v[52:55]\n"
    "v_exp_f32 v95, v95\n"
    "v_mfma_f32_16x16x32_bf16 v[56:59], v[168:171], a[248:251], v[56:59]\n"
    "ds_read_b128 v[164:167], v200 offset:25600\n"
    "v_cvt_pk_bf16_f32 v71, v86, v87\n"
    "v_mfma_f32_16x16x32_bf16 v[56:59], v[172:175], a[252:255], v[56:59]\n"
    "v_exp_f32 v232, v232\n"
    "v_mfma_f32_16x16x32_bf16 v[60:63], v[172:175], v[20:23], v[60:63]\n"
    "v_exp_f32 v233, v233\n"
    "v_mfma_f32_16x16x32_bf16 v[60:63], v[168:171], v[16:19], v[60:63]\n"
    "s_waitcnt lgkmcnt(2)\n"
    "v_exp_f32 v234, v234\n"
    "v_mfma_f32_16x16x32_bf16 v[60:63], v[176:179], v[8:11], v[60:63]\n"
    "s_lshl_b32 s76, s56, 3\n"
    "ds_read_b128 v[168:171], v200 offset:26624\n"
    "v_cvt_pk_bf16_f32 v74, v88, v89\n"
    "v_mfma_f32_16x16x32_bf16 v[60:63], v[180:183], v[12:15], v[60:63]\n"
    "s_add_u32 s76, s76, s57\n"
    "v_exp_f32 v235, v235\n"
    "v_mfma_f32_16x16x32_bf16 v[56:59], v[180:183], a[244:247], v[56:59]\n"
    "s_mul_i32 s76, s76, 0x18000\n"
    "v_exp_f32 v236, v236\n"
    "v_mfma_f32_16x16x32_bf16 v[56:59], v[176:179], a[240:243], v[56:59]\n"
    "s_add_u32 s76, s76, s88\n"
    "v_exp_f32 v237, v237\n"
    "v_mfma_f32_16x16x32_bf16 v[52:55], v[176:179], a[216:219], v[52:55]\n"
    "s_add_u32 s28, s24, s76\n"
    "ds_read_b128 v[172:175], v200 offset:27648\n"
    "v_cvt_pk_bf16_f32 v75, v90, v91\n"
    "v_mfma_f32_16x16x32_bf16 v[52:55], v[180:183], a[220:223], v[52:55]\n"
    "s_addc_u32 s29, s25, 0\n"
    "v_exp_f32 v238, v238\n"
    "v_mfma_f32_16x16x32_bf16 v[48:51], v[180:183], a[196:199], v[48:51]\n"
    "s_and_b32 s29, s29, 0xffff\n"
    "v_exp_f32 v239, v239\n"
    "v_mfma_f32_16x16x32_bf16 v[48:51], v[176:179], a[192:195], v[48:51]\n"
    "s_waitcnt lgkmcnt(2)\n"
    "v_exp_f32 v240, v240\n"
    "v_mfma_f32_16x16x32_bf16 v[80:83], v[160:163], a[192:195], v[96:99]\n"
    "s_mov_b32 s30, 0x6000\n"
    "ds_read_b128 v[176:179], v200 offset:28672\n"
    "v_cvt_pk_bf16_f32 v78, v92, v93\n"
    "v_mfma_f32_16x16x32_bf16 v[80:83], v[164:167], a[196:199], v[80:83]\n"
    "s_mov_b32 s31, 0x20000\n"
    "v_cvt_pk_bf16_f32 v79, v94, v95\n"
    "v_permlane16_swap_b32 v113, v115\n"
    "v_mfma_f32_16x16x32_bf16 v[84:87], v[164:167], a[220:223], v[100:103]\n"
    "s_xor_b32 s94, s70, 0x80000000\n"
    "v_exp_f32 v241, v241\n"
    "v_mfma_f32_16x16x32_bf16 v[84:87], v[160:163], a[216:219], v[84:87]\n"
    "s_xor_b32 s95, s70, 0x80000000\n"
    "v_exp_f32 v242, v242\n"
    "v_mfma_f32_16x16x32_bf16 v[88:91], v[160:163], a[240:243], v[104:107]\n"
    "s_mov_b32 s62, 0x1800\n"
    "ds_read_b128 v[180:183], v200 offset:29696\n"
    "v_permlane16_swap_b32 v117, v119\n"
    "v_mfma_f32_16x16x32_bf16 v[88:91], v[164:167], a[244:247], v[88:91]\n"
    "s_mov_b32 s63, 0x3000\n"
    "v_exp_f32 v243, v243\n"
    "v_mfma_f32_16x16x32_bf16 v[92:95], v[164:167], v[12:15], v[108:111]\n"
    "s_mov_b32 s64, 0x4800\n"
    "v_exp_f32 v244, v244\n"
    "v_mfma_f32_16x16x32_bf16 v[92:95], v[160:163], v[8:11], v[92:95]\n"
    "s_waitcnt lgkmcnt(2)\n"
    "v_exp_f32 v245, v245\n"
    "v_mfma_f32_16x16x32_bf16 v[92:95], v[168:171], v[16:19], v[92:95]\n"
    "ds_read_b128 v[160:163], v200 offset:34816\n"
    "v_permlane16_swap_b32 v121, v123\n"
    "v_mfma_f32_16x16x32_bf16 v[92:95], v[172:175], v[20:23], v[92:95]\n"
    "v_exp_f32 v246, v246\n"
    "v_mfma_f32_16x16x32_bf16 v[88:91], v[172:175], a[252:255], v[88:91]\n"
    "v_exp_f32 v247, v247\n"
    "v_mfma_f32_16x16x32_bf16 v[88:91], v[168:171], a[248:251], v[88:91]\n"
    "v_exp_f32 v48, v48\n"
    "v_mfma_f32_16x16x32_bf16 v[84:87], v[168:171], a[224:227], v[84:87]\n"
    "ds_read_b128 v[164:167], v200 offset:35840\n"
    "v_permlane16_swap_b32 v125, v127\n"
    "v_mfma_f32_16x16x32_bf16 v[84:87], v[172:175], a[228:231], v[84:87]\n"
    "v_exp_f32 v49, v49\n"
    "v_mfma_f32_16x16x32_bf16 v[80:83], v[172:175], a[204:207], v[80:83]\n"
    "v_exp_f32 v50, v50\n"
    "v_mfma_f32_16x16x32_bf16 v[80:83], v[168:171], a[200:203], v[80:83]\n"
    "s_waitcnt lgkmcnt(2)\n"
    "v_exp_f32 v51, v51\n"
    "v_mfma_f32_16x16x32_bf16 v[80:83], v[176:179], a[208:211], v[80:83]\n"
    "ds_read_b128 v[168:171], v200 offset:32768\n"
    "v_add_f32 v113, v113, v115\n"
    "v_mfma_f32_16x16x32_bf16 v[80:83], v[180:183], a[212:215], v[80:83]\n"
    "v_exp_f32 v52, v52\n"
    "v_mfma_f32_16x16x32_bf16 v[84:87], v[180:183], a[236:239], v[84:87]\n"
    "v_exp_f32 v53, v53\n"
    "v_mfma_f32_16x16x32_bf16 v[84:87], v[176:179], a[232:235], v[84:87]\n"
    "v_exp_f32 v54, v54\n"
    "v_mfma_f32_16x16x32_bf16 v[88:91], v[176:179], v[0:3], v[88:91]\n"
    "ds_read_b128 v[172:175], v200 offset:33792\n"
    "v_add_f32 v117, v117, v119\n"
    "v_mfma_f32_16x16x32_bf16 v[88:91], v[180:183], v[4:7], v[88:91]\n"
    "v_add_f32 v121, v121, v123\n"
    "v_add_f32 v125, v125, v127\n"
    "v_mfma_f32_16x16x32_bf16 v[92:95], v[180:183], v[28:31], v[92:95]\n"
    "v_exp_f32 v55, v55\n"
    "v_mfma_f32_16x16x32_bf16 v[92:95], v[176:179], v[24:27], v[92:95]\n"
    "s_waitcnt lgkmcnt(2)\n"
    "v_add_f32 v112, v112, v113\n"
    "v_add_f32 v116, v116, v117\n"
    "v_mfma_f32_16x16x32_bf16 v[252:255], v[160:163], v[24:27], v[108:111]\n"
    "ds_read_b128 v[176:179], v200 offset:30720\n"
    "v_add_f32 v120, v120, v121\n"
    "v_mfma_f32_16x16x32_bf16 v[252:255], v[164:167], v[28:31], v[252:255]\n"
    "v_add_f32 v124, v124, v125\n"
    "v_and_b32 v198, 15, v231\n"
    "v_mfma_f32_16x16x32_bf16 v[248:251], v[164:167], v[4:7], v[104:107]\n"
    "v_mul_u32_u24 v205, 0x180, v198\n"
    "v_lshrrev_b32 v199, 4, v231\n"
    "v_mfma_f32_16x16x32_bf16 v[248:251], v[160:163], v[0:3], v[248:251]\n"
    "v_lshl_add_u32 v205, v199, 4, v205\n"
    "v_add_u32 v207, 0x18000, v200\n"
    "v_mfma_f32_16x16x32_bf16 v[188:191], v[160:163], a[232:235], v[100:103]\n"
    "ds_read_b128 v[180:183], v200 offset:31744\n"
    "v_cvt_pk_bf16_f32 v232, v232, v233\n"
    "v_mfma_f32_16x16x32_bf16 v[188:191], v[164:167], a[236:239], v[188:191]\n"
    "v_exp_f32 v56, v56\n"
    "v_mfma_f32_16x16x32_bf16 v[184:187], v[164:167], a[212:215], v[96:99]\n"
    "v_exp_f32 v57, v57\n"
    "v_mfma_f32_16x16x32_bf16 v[184:187], v[160:163], a[208:211], v[184:187]\n"
    "s_waitcnt lgkmcnt(2)\n"
    "v_exp_f32 v58, v58\n"
    "v_mfma_f32_16x16x32_bf16 v[184:187], v[168:171], a[200:203], v[184:187]\n"
    "ds_read_b128 v[160:163], v201\n"
    "v_cvt_pk_bf16_f32 v233, v234, v235\n"
    "v_mfma_f32_16x16x32_bf16 v[184:187], v[172:175], a[204:207], v[184:187]\n"
    "v_exp_f32 v59, v59\n"
    "v_mfma_f32_16x16x32_bf16 v[188:191], v[172:175], a[228:231], v[188:191]\n"
    "v_exp_f32 v60, v60\n"
    "v_mfma_f32_16x16x32_bf16 v[188:191], v[168:171], a[224:227], v[188:191]\n"
    "v_exp_f32 v61, v61\n"
    "v_mfma_f32_16x16x32_bf16 v[248:251], v[168:171], a[248:251], v[248:251]\n"
    "ds_read_b128 v[164:167], v201 offset:1024\n"
    "v_cvt_pk_bf16_f32 v236, v236, v237\n"
    "v_mfma_f32_16x16x32_bf16 v[248:251], v[172:175], a[252:255], v[248:251]\n"
    "v_exp_f32 v62, v62\n"
    "v_mfma_f32_16x16x32_bf16 v[252:255], v[172:175], v[20:23], v[252:255]\n"
    "v_exp_f32 v63, v63\n"
    "v_mfma_f32_16x16x32_bf16 v[252:255], v[168:171], v[16:19], v[252:255]\n"
    "s_waitcnt lgkmcnt(2)\n"
    "v_exp_f32 v80, v80\n"
    "v_mfma_f32_16x16x32_bf16 v[252:255], v[176:179], v[8:11], v[252:255]\n"
    "ds_read_b128 v[168:171], v201 offset:24576\n"
    "v_cvt_pk_bf16_f32 v237, v238, v239\n"
    "v_mfma_f32_16x16x32_bf16 v[252:255], v[180:183], v[12:15], v[252:255]\n"
    "v_exp_f32 v81, v81\n"
    "v_mfma_f32_16x16x32_bf16 v[248:251], v[180:183], a[244:247], v[248:251]\n"
    "v_exp_f32 v82, v82\n"
    "v_mfma_f32_16x16x32_bf16 v[248:251], v[176:179], a[240:243], v[248:251]\n"
    "v_exp_f32 v83, v83\n"
    "v_mfma_f32_16x16x32_bf16 v[188:191], v[176:179], a[216:219], v[188:191]\n"
    "ds_read_b128 v[172:175], v201 offset:25600\n"
    "v_cvt_pk_bf16_f32 v240, v240, v241\n"
    "v_mfma_f32_16x16x32_bf16 v[188:191], v[180:183], a[220:223], v[188:191]\n"
    "v_exp_f32 v84, v84\n"
    "v_mfma_f32_16x16x32_bf16 v[184:187], v[180:183], a[196:199], v[184:187]\n"
    "v_exp_f32 v85, v85\n"
    "v_mfma_f32_16x16x32_bf16 v[184:187], v[176:179], a[192:195], v[184:187]\n"
    "s_waitcnt lgkmcnt(2)\n"
    "v_exp_f32 v86, v86\n"
    "v_mfma_f32_16x16x32_bf16 a[0:3], v[160:163], v[128:131], a[0:3]\n"
    "ds_read_b128 v[176:179], v201 offset:2048\n"
    "v_cvt_pk_bf16_f32 v241, v242, v243\n"
    "v_mfma_f32_16x16x32_bf16 a[0:3], v[164:167], v[144:147], a[0:3]\n"
    "v_exp_f32 v87, v87\n"
    "v_mfma_f32_16x16x32_bf16 a[4:7], v[160:163], v[132:135], a[4:7]\n"
    "v_exp_f32 v88, v88\n"
    "v_mfma_f32_16x16x32_bf16 a[4:7], v[164:167], v[148:151], a[4:7]\n"
    "v_exp_f32 v89, v89\n"
    "v_mfma_f32_16x16x32_bf16 a[8:11], v[160:163], v[136:139], a[8:11]\n"
    "ds_read_b128 v[180:183], v201 offset:3072\n"
    "v_cvt_pk_bf16_f32 v244, v244, v245\n"
    "v_mfma_f32_16x16x32_bf16 a[8:11], v[164:167], v[152:155], a[8:11]\n"
    "v_cvt_pk_bf16_f32 v245, v246, v247\n"
    "v_cvt_pk_bf16_f32 v234, v48, v49\n"
    "v_mfma_f32_16x16x32_bf16 a[12:15], v[160:163], v[140:143], a[12:15]\n"
    "v_cvt_pk_bf16_f32 v235, v50, v51\n"
    "v_cvt_pk_bf16_f32 v238, v52, v53\n"
    "v_mfma_f32_16x16x32_bf16 a[12:15], v[164:167], v[156:159], a[12:15]\n"
    "v_cvt_pk_bf16_f32 v239, v54, v55\n"
    "v_cvt_pk_bf16_f32 v242, v56, v57\n"
    "v_mfma_f32_16x16x32_bf16 v[112:115], v[192:195], v[144:147], v[112:115]\n"
    "v_exp_f32 v90, v90\n"
    "v_mfma_f32_16x16x32_bf16 v[112:115], v[192:195], v[32:35], v[112:115]\n"
    "v_cvt_pk_bf16_f32 v243, v58, v59\n"
    "v_cvt_pk_bf16_f32 v246, v60, v61\n"
    "v_mfma_f32_16x16x32_bf16 v[112:115], v[192:195], v[64:67], v[112:115]\n"
    "v_cvt_pk_bf16_f32 v247, v62, v63\n"
    "v_cvt_pk_bf16_f32 v80, v80, v81\n"
    "v_mfma_f32_16x16x32_bf16 v[116:119], v[192:195], v[148:151], v[116:119]\n"
    "v_exp_f32 v91, v91\n"
    "v_mfma_f32_16x16x32_bf16 v[116:119], v[192:195], v[36:39], v[116:119]\n"
    "v_exp_f32 v92, v92\n"
    "v_mfma_f32_16x16x32_bf16 v[116:119], v[192:195], v[68:71], v[116:119]\n"
    "s_waitcnt lgkmcnt(2)\n"
    "v_exp_f32 v93, v93\n"
    "v_mfma_f32_16x16x32_bf16 a[0:3], v[168:171], v[32:35], a[0:3]\n"
    "ds_read_b128 v[160:163], v201 offset:26624\n"
    "v_cvt_pk_bf16_f32 v81, v82, v83\n"
    "v_mfma_f32_16x16x32_bf16 a[0:3], v[172:175], v[64:67], a[0:3]\n"
    "v_exp_f32 v94, v94\n"
    "v_mfma_f32_16x16x32_bf16 a[4:7], v[168:171], v[36:39], a[4:7]\n"
    "v_exp_f32 v95, v95\n"
    "v_mfma_f32_16x16x32_bf16 a[4:7], v[172:175], v[68:71], a[4:7]\n"
    "v_exp_f32 v184, v184\n"
    "v_mfma_f32_16x16x32_bf16 a[8:11], v[168:171], v[40:43], a[8:11]\n"
    "ds_read_b128 v[164:167], v201 offset:27648\n"
    "v_cvt_pk_bf16_f32 v84, v84, v85\n"
    "v_mfma_f32_16x16x32_bf16 a[8:11], v[172:175], v[72:75], a[8:11]\n"
    "v_exp_f32 v185, v185\n"
    "v_mfma_f32_16x16x32_bf16 a[12:15], v[168:171], v[44:47], a[12:15]\n"
    "v_exp_f32 v186, v186\n"
    "v_mfma_f32_16x16x32_bf16 a[12:15], v[172:175], v[76:79], a[12:15]\n"
    "v_exp_f32 v187, v187\n"
    "v_mfma_f32_16x16x32_bf16 v[120:123], v[192:195], v[152:155], v[120:123]\n"
    "v_exp_f32 v188, v188\n"
    "v_mfma_f32_16x16x32_bf16 v[120:123], v[192:195], v[40:43], v[120:123]\n"
    "v_exp_f32 v189, v189\n"
    "v_mfma_f32_16x16x32_bf16 v[120:123], v[192:195], v[72:75], v[120:123]\n"
    "v_exp_f32 v190, v190\n"
    "v_mfma_f32_16x16x32_bf16 v[124:127], v[192:195], v[156:159], v[124:127]\n"
    "v_exp_f32 v191, v191\n"
    "v_mfma_f32_16x16x32_bf16 v[124:127], v[192:195], v[44:47], v[124:127]\n"
    "s_waitcnt vmcnt(0)\n"
    "v_exp_f32 v248, v248\n"
    "v_mfma_f32_16x16x32_bf16 v[124:127], v[192:195], v[76:79], v[124:127]\n"
    "s_waitcnt lgkmcnt(2)\n"
    "v_exp_f32 v249, v249\n"
    "v_mfma_f32_16x16x32_bf16 a[16:19], v[176:179], v[128:131], a[16:19]\n"
    "s_barrier\n"
    "v_exp_f32 v250, v250\n"
    "v_mfma_f32_16x16x32_bf16 a[16:19], v[180:183], v[144:147], a[16:19]\n"
    "ds_read_b128 v[168:171], v207\n"
    "v_cvt_pk_bf16_f32 v85, v86, v87\n"
    "v_mfma_f32_16x16x32_bf16 a[20:23], v[176:179], v[132:135], a[20:23]\n"
    "v_exp_f32 v251, v251\n"
    "v_mfma_f32_16x16x32_bf16 a[20:23], v[180:183], v[148:151], a[20:23]\n"
    "v_exp_f32 v252, v252\n"
    "v_mfma_f32_16x16x32_bf16 a[24:27], v[176:179], v[136:139], a[24:27]\n"
    "ds_read_b128 v[172:175], v207 offset:1024\n"
    "v_cvt_pk_bf16_f32 v88, v88, v89\n"
    "v_mfma_f32_16x16x32_bf16 a[24:27], v[180:183], v[152:155], a[24:27]\n"
    "v_exp_f32 v253, v253\n"
    "v_mfma_f32_16x16x32_bf16 a[28:31], v[176:179], v[140:143], a[28:31]\n"
    "v_exp_f32 v254, v254\n"
    "v_mfma_f32_16x16x32_bf16 a[28:31], v[180:183], v[156:159], a[28:31]\n"
    "s_waitcnt lgkmcnt(2)\n"
    "v_cvt_pk_bf16_f32 v89, v90, v91\n"
    "v_cvt_pk_bf16_f32 v92, v92, v93\n"
    "v_mfma_f32_16x16x32_bf16 a[16:19], v[160:163], v[32:35], a[16:19]\n"
    "ds_read_b128 v[176:179], v207 offset:2048\n"
    "v_cvt_pk_bf16_f32 v93, v94, v95\n"
    "v_mfma_f32_16x16x32_bf16 a[16:19], v[164:167], v[64:67], a[16:19]\n"
    "ds_read_b128 v[180:183], v207 offset:3072\n"
    "v_cvt_pk_bf16_f32 v82, v184, v185\n"
    "v_mfma_f32_16x16x32_bf16 a[20:23], v[160:163], v[36:39], a[20:23]\n"
    "v_cvt_pk_bf16_f32 v83, v186, v187\n"
    "v_cvt_pk_bf16_f32 v86, v188, v189\n"
    "v_mfma_f32_16x16x32_bf16 a[20:23], v[164:167], v[68:71], a[20:23]\n"
    "v_exp_f32 v255, v255\n"
    "v_mfma_f32_16x16x32_bf16 a[24:27], v[160:163], v[40:43], a[24:27]\n"
    "v_cvt_pk_bf16_f32 v87, v190, v191\n"
    "v_cvt_pk_bf16_f32 v90, v248, v249\n"
    "v_mfma_f32_16x16x32_bf16 a[24:27], v[164:167], v[72:75], a[24:27]\n"
    "v_cvt_pk_bf16_f32 v91, v250, v251\n"
    "v_cvt_pk_bf16_f32 v94, v252, v253\n"
    "v_mfma_f32_16x16x32_bf16 a[28:31], v[160:163], v[44:47], a[28:31]\n"
    "v_cvt_pk_bf16_f32 v95, v254, v255\n"
    "v_mfma_f32_16x16x32_bf16 a[28:31], v[164:167], v[76:79], a[28:31]\n"
    "v_mfma_f32_16x16x32_bf16 v[112:115], v[192:195], v[232:235], v[112:115]\n"
    "ds_read_b128 v[160:163], v201 offset:4096\n"
    "v_mfma_f32_16x16x32_bf16 v[112:115], v[192:195], v[80:83], v[112:115]\n"
    "s_waitcnt lgkmcnt(3)\n"
    "v_mfma_f32_16x16x32_bf16 a[0:3], v[168:171], v[232:235], a[0:3]\n"
    "v_mfma_f32_16x16x32_bf16 a[0:3], v[172:175], v[80:83], a[0:3]\n"
    "s_waitcnt lgkmcnt(1)\n"
    "v_mfma_f32_16x16x32_bf16 a[16:19], v[176:179], v[232:235], a[16:19]\n"
    "ds_read_b128 v[164:167], v201 offset:5120\n"
    "v_mfma_f32_16x16x32_bf16 a[16:19], v[180:183], v[80:83], a[16:19]\n"
    "v_mfma_f32_16x16x32_bf16 v[116:119], v[192:195], v[236:239], v[116:119]\n"
    "v_mul_f32 v222, s68, v112\n"
    "v_rcp_f32 v222, v222\n"
    "v_mfma_f32_16x16x32_bf16 v[116:119], v[192:195], v[84:87], v[116:119]\n"
    "v_accvgpr_read_b32 v96, a0\n"
    "v_accvgpr_read_b32 v97, a1\n"
    "v_accvgpr_read_b32 v98, a2\n"
    "v_mfma_f32_16x16x32_bf16 a[4:7], v[168:171], v[236:239], a[4:7]\n"
    "v_accvgpr_read_b32 v99, a3\n"
    "v_fma_f32 v96, v96, v222, s94\n"
    "v_fma_f32 v97, v97, v222, s94\n"
    "v_mfma_f32_16x16x32_bf16 a[4:7], v[172:175], v[84:87], a[4:7]\n"
    "v_fma_f32 v98, v98, v222, s94\n"
    "v_fma_f32 v99, v99, v222, s94\n"
    "v_cvt_pk_bf16_f32 v48, v96, v97\n"
    "v_mfma_f32_16x16x32_bf16 a[20:23], v[176:179], v[236:239], a[20:23]\n"
    "v_cvt_pk_bf16_f32 v49, v98, v99\n"
    "v_accvgpr_read_b32 v100, a16\n"
    "v_accvgpr_read_b32 v101, a17\n"
    "v_mfma_f32_16x16x32_bf16 a[20:23], v[180:183], v[84:87], a[20:23]\n"
    "v_accvgpr_read_b32 v102, a18\n"
    "v_accvgpr_read_b32 v103, a19\n"
    "v_fma_f32 v100, v100, v222, s94\n"
    "v_mfma_f32_16x16x32_bf16 v[120:123], v[192:195], v[240:243], v[120:123]\n"
    "v_fma_f32 v101, v101, v222, s94\n"
    "v_fma_f32 v102, v102, v222, s94\n"
    "v_fma_f32 v103, v103, v222, s94\n"
    "v_mfma_f32_16x16x32_bf16 v[120:123], v[192:195], v[88:91], v[120:123]\n"
    "v_cvt_pk_bf16_f32 v50, v100, v101\n"
    "v_cvt_pk_bf16_f32 v51, v102, v103\n"
    "v_mfma_f32_16x16x32_bf16 a[8:11], v[168:171], v[240:243], a[8:11]\n"
    "buffer_store_dwordx4 v[48:51], v205, s[28:31], 0 offen sc0 sc1\n"
    "v_mul_f32 v224, s68, v116\n"
    "v_rcp_f32 v224, v224\n"
    "v_accvgpr_read_b32 v104, a4\n"
    "v_mfma_f32_16x16x32_bf16 a[8:11], v[172:175], v[88:91], a[8:11]\n"
    "v_accvgpr_read_b32 v105, a5\n"
    "v_accvgpr_read_b32 v106, a6\n"
    "v_accvgpr_read_b32 v107, a7\n"
    "v_mfma_f32_16x16x32_bf16 a[24:27], v[176:179], v[240:243], a[24:27]\n"
    "v_fma_f32 v104, v104, v224, s94\n"
    "v_fma_f32 v105, v105, v224, s94\n"
    "v_fma_f32 v106, v106, v224, s94\n"
    "v_mfma_f32_16x16x32_bf16 a[24:27], v[180:183], v[88:91], a[24:27]\n"
    "v_fma_f32 v107, v107, v224, s94\n"
    "v_cvt_pk_bf16_f32 v52, v104, v105\n"
    "v_cvt_pk_bf16_f32 v53, v106, v107\n"
    "v_mfma_f32_16x16x32_bf16 v[124:127], v[192:195], v[244:247], v[124:127]\n"
    "v_accvgpr_read_b32 v108, a20\n"
    "v_accvgpr_read_b32 v109, a21\n"
    "v_accvgpr_read_b32 v110, a22\n"
    "v_mfma_f32_16x16x32_bf16 v[124:127], v[192:195], v[92:95], v[124:127]\n"
    "v_accvgpr_read_b32 v111, a23\n"
    "v_fma_f32 v108, v108, v224, s94\n"
    "v_fma_f32 v109, v109, v224, s94\n"
    "v_mfma_f32_16x16x32_bf16 a[12:15], v[168:171], v[244:247], a[12:15]\n"
    "v_fma_f32 v110, v110, v224, s94\n"
    "v_fma_f32 v111, v111, v224, s94\n"
    "v_mfma_f32_16x16x32_bf16 a[12:15], v[172:175], v[92:95], a[12:15]\n"
    "v_cvt_pk_bf16_f32 v54, v108, v109\n"
    "v_cvt_pk_bf16_f32 v55, v110, v111\n"
    "v_mfma_f32_16x16x32_bf16 a[28:31], v[176:179], v[244:247], a[28:31]\n"
    "ds_read_b128 v[168:171], v201 offset:28672\n"
    "v_mfma_f32_16x16x32_bf16 a[28:31], v[180:183], v[92:95], a[28:31]\n"
    "s_waitcnt lgkmcnt(1)\n"
    "ds_read_b128 v[172:175], v201 offset:29696\n"
    "v_mfma_f32_16x16x32_bf16 a[32:35], v[160:163], v[128:131], a[32:35]\n"
    "ds_read_b128 v[176:179], v207 offset:4096\n"
    "v_mfma_f32_16x16x32_bf16 a[32:35], v[164:167], v[144:147], a[32:35]\n"
    "buffer_store_dwordx4 v[52:55], v205, s[28:31], s62 offen sc0 sc1\n"
    "v_mul_f32 v226, s68, v120\n"
    "v_rcp_f32 v226, v226\n"
    "v_mfma_f32_16x16x32_bf16 a[36:39], v[160:163], v[132:135], a[36:39]\n"
    "v_accvgpr_read_b32 v96, a8\n"
    "v_accvgpr_read_b32 v97, a9\n"
    "v_mfma_f32_16x16x32_bf16 a[36:39], v[164:167], v[148:151], a[36:39]\n"
    "v_accvgpr_read_b32 v98, a10\n"
    "v_accvgpr_read_b32 v99, a11\n"
    "v_mfma_f32_16x16x32_bf16 a[40:43], v[160:163], v[136:139], a[40:43]\n"
    "ds_read_b128 v[180:183], v207 offset:5120\n"
    "v_fma_f32 v96, v96, v226, s94\n"
    "v_mfma_f32_16x16x32_bf16 a[40:43], v[164:167], v[152:155], a[40:43]\n"
    "v_fma_f32 v97, v97, v226, s94\n"
    "v_fma_f32 v98, v98, v226, s94\n"
    "v_mfma_f32_16x16x32_bf16 a[44:47], v[160:163], v[140:143], a[44:47]\n"
    "v_fma_f32 v99, v99, v226, s94\n"
    "v_cvt_pk_bf16_f32 v56, v96, v97\n"
    "v_mfma_f32_16x16x32_bf16 a[44:47], v[164:167], v[156:159], a[44:47]\n"
    "s_waitcnt lgkmcnt(2)\n"
    "v_cvt_pk_bf16_f32 v57, v98, v99\n"
    "v_accvgpr_read_b32 v100, a24\n"
    "v_mfma_f32_16x16x32_bf16 a[44:47], v[168:171], v[44:47], a[44:47]\n"
    "ds_read_b128 v[160:163], v201 offset:6144\n"
    "v_accvgpr_read_b32 v101, a25\n"
    "v_mfma_f32_16x16x32_bf16 a[44:47], v[172:175], v[76:79], a[44:47]\n"
    "v_accvgpr_read_b32 v102, a26\n"
    "v_accvgpr_read_b32 v103, a27\n"
    "v_mfma_f32_16x16x32_bf16 a[40:43], v[168:171], v[40:43], a[40:43]\n"
    "v_fma_f32 v100, v100, v226, s94\n"
    "v_fma_f32 v101, v101, v226, s94\n"
    "v_mfma_f32_16x16x32_bf16 a[40:43], v[172:175], v[72:75], a[40:43]\n"
    "v_fma_f32 v102, v102, v226, s94\n"
    "v_fma_f32 v103, v103, v226, s94\n"
    "v_mfma_f32_16x16x32_bf16 a[36:39], v[168:171], v[36:39], a[36:39]\n"
    "ds_read_b128 v[164:167], v201 offset:7168\n"
    "v_cvt_pk_bf16_f32 v58, v100, v101\n"
    "v_mfma_f32_16x16x32_bf16 a[36:39], v[172:175], v[68:71], a[36:39]\n"
    "v_cvt_pk_bf16_f32 v59, v102, v103\n"
    "v_mfma_f32_16x16x32_bf16 a[32:35], v[168:171], v[32:35], a[32:35]\n"
    "buffer_store_dwordx4 v[56:59], v205, s[28:31], s63 offen sc0 sc1\n"
    "v_mul_f32 v228, s68, v124\n"
    "v_rcp_f32 v228, v228\n"
    "v_mfma_f32_16x16x32_bf16 a[32:35], v[172:175], v[64:67], a[32:35]\n"
    "s_waitcnt lgkmcnt(2)\n"
    "v_accvgpr_read_b32 v104, a12\n"
    "v_accvgpr_read_b32 v105, a13\n"
    "v_mfma_f32_16x16x32_bf16 a[32:35], v[176:179], v[232:235], a[32:35]\n"
    "ds_read_b128 v[168:171], v201 offset:30720\n"
    "v_accvgpr_read_b32 v106, a14\n"
    "v_mfma_f32_16x16x32_bf16 a[32:35], v[180:183], v[80:83], a[32:35]\n"
    "v_accvgpr_read_b32 v107, a15\n"
    "v_fma_f32 v104, v104, v228, s94\n"
    "v_mfma_f32_16x16x32_bf16 a[36:39], v[176:179], v[236:239], a[36:39]\n"
    "v_fma_f32 v105, v105, v228, s94\n"
    "v_fma_f32 v106, v106, v228, s94\n"
    "v_mfma_f32_16x16x32_bf16 a[36:39], v[180:183], v[84:87], a[36:39]\n"
    "v_fma_f32 v107, v107, v228, s94\n"
    "v_cvt_pk_bf16_f32 v60, v104, v105\n"
    "v_mfma_f32_16x16x32_bf16 a[40:43], v[176:179], v[240:243], a[40:43]\n"
    "ds_read_b128 v[172:175], v201 offset:31744\n"
    "v_cvt_pk_bf16_f32 v61, v106, v107\n"
    "v_mfma_f32_16x16x32_bf16 a[40:43], v[180:183], v[88:91], a[40:43]\n"
    "v_accvgpr_read_b32 v108, a28\n"
    "v_accvgpr_read_b32 v109, a29\n"
    "v_mfma_f32_16x16x32_bf16 a[44:47], v[176:179], v[244:247], a[44:47]\n"
    "v_accvgpr_read_b32 v110, a30\n"
    "v_accvgpr_read_b32 v111, a31\n"
    "v_mfma_f32_16x16x32_bf16 a[44:47], v[180:183], v[92:95], a[44:47]\n"
    "s_waitcnt lgkmcnt(2)\n"
    "v_fma_f32 v108, v108, v228, s94\n"
    "v_fma_f32 v109, v109, v228, s94\n"
    "v_mfma_f32_16x16x32_bf16 a[48:51], v[160:163], v[128:131], a[48:51]\n"
    "ds_read_b128 v[176:179], v207 offset:6144\n"
    "v_fma_f32 v110, v110, v228, s94\n"
    "v_mfma_f32_16x16x32_bf16 a[48:51], v[164:167], v[144:147], a[48:51]\n"
    "v_fma_f32 v111, v111, v228, s94\n"
    "v_cvt_pk_bf16_f32 v62, v108, v109\n"
    "v_mfma_f32_16x16x32_bf16 a[52:55], v[160:163], v[132:135], a[52:55]\n"
    "v_cvt_pk_bf16_f32 v63, v110, v111\n"
    "v_mfma_f32_16x16x32_bf16 a[52:55], v[164:167], v[148:151], a[52:55]\n"
    "buffer_store_dwordx4 v[60:63], v205, s[28:31], s64 offen sc0 sc1\n"
    "v_accvgpr_read_b32 v96, a32\n"
    "v_accvgpr_read_b32 v97, a33\n"
    "v_accvgpr_read_b32 v98, a34\n"
    "v_mfma_f32_16x16x32_bf16 a[56:59], v[160:163], v[136:139], a[56:59]\n"
    "ds_read_b128 v[180:183], v207 offset:7168\n"
    "v_accvgpr_read_b32 v99, a35\n"
    "v_mfma_f32_16x16x32_bf16 a[56:59], v[164:167], v[152:155], a[56:59]\n"
    "v_fma_f32 v96, v96, v222, s94\n"
    "v_fma_f32 v97, v97, v222, s94\n"
    "v_mfma_f32_16x16x32_bf16 a[60:63], v[160:163], v[140:143], a[60:63]\n"
    "v_fma_f32 v98, v98, v222, s94\n"
    "v_fma_f32 v99, v99, v222, s94\n"
    "v_mfma_f32_16x16x32_bf16 a[60:63], v[164:167], v[156:159], a[60:63]\n"
    "s_waitcnt lgkmcnt(2)\n"
    "v_cvt_pk_bf16_f32 v184, v96, v97\n"
    "v_cvt_pk_bf16_f32 v185, v98, v99\n"
    "v_mfma_f32_16x16x32_bf16 a[60:63], v[168:171], v[44:47], a[60:63]\n"
    "ds_read_b128 v[160:163], v201 offset:8192\n"
    "v_accvgpr_read_b32 v100, a36\n"
    "v_mfma_f32_16x16x32_bf16 a[60:63], v[172:175], v[76:79], a[60:63]\n"
    "v_accvgpr_read_b32 v101, a37\n"
    "v_accvgpr_read_b32 v102, a38\n"
    "v_mfma_f32_16x16x32_bf16 a[56:59], v[168:171], v[40:43], a[56:59]\n"
    "v_accvgpr_read_b32 v103, a39\n"
    "v_fma_f32 v100, v100, v224, s94\n"
    "v_mfma_f32_16x16x32_bf16 a[56:59], v[172:175], v[72:75], a[56:59]\n"
    "v_fma_f32 v101, v101, v224, s94\n"
    "v_fma_f32 v102, v102, v224, s94\n"
    "v_mfma_f32_16x16x32_bf16 a[52:55], v[168:171], v[36:39], a[52:55]\n"
    "ds_read_b128 v[164:167], v201 offset:9216\n"
    "v_fma_f32 v103, v103, v224, s94\n"
    "v_mfma_f32_16x16x32_bf16 a[52:55], v[172:175], v[68:71], a[52:55]\n"
    "v_cvt_pk_bf16_f32 v188, v100, v101\n"
    "v_cvt_pk_bf16_f32 v189, v102, v103\n"
    "v_mfma_f32_16x16x32_bf16 a[48:51], v[168:171], v[32:35], a[48:51]\n"
    "v_accvgpr_read_b32 v104, a40\n"
    "v_accvgpr_read_b32 v105, a41\n"
    "v_mfma_f32_16x16x32_bf16 a[48:51], v[172:175], v[64:67], a[48:51]\n"
    "s_waitcnt lgkmcnt(2)\n"
    "v_accvgpr_read_b32 v106, a42\n"
    "v_accvgpr_read_b32 v107, a43\n"
    "v_mfma_f32_16x16x32_bf16 a[48:51], v[176:179], v[232:235], a[48:51]\n"
    "ds_read_b128 v[168:171], v201 offset:32768\n"
    "v_fma_f32 v104, v104, v226, s94\n"
    "v_mfma_f32_16x16x32_bf16 a[48:51], v[180:183], v[80:83], a[48:51]\n"
    "v_fma_f32 v105, v105, v226, s94\n"
    "v_fma_f32 v106, v106, v226, s94\n"
    "v_mfma_f32_16x16x32_bf16 a[52:55], v[176:179], v[236:239], a[52:55]\n"
    "v_fma_f32 v107, v107, v226, s94\n"
    "v_cvt_pk_bf16_f32 v248, v104, v105\n"
    "v_mfma_f32_16x16x32_bf16 a[52:55], v[180:183], v[84:87], a[52:55]\n"
    "v_cvt_pk_bf16_f32 v249, v106, v107\n"
    "v_accvgpr_read_b32 v108, a44\n"
    "v_mfma_f32_16x16x32_bf16 a[56:59], v[176:179], v[240:243], a[56:59]\n"
    "ds_read_b128 v[172:175], v201 offset:33792\n"
    "v_accvgpr_read_b32 v109, a45\n"
    "v_mfma_f32_16x16x32_bf16 a[56:59], v[180:183], v[88:91], a[56:59]\n"
    "v_accvgpr_read_b32 v110, a46\n"
    "v_accvgpr_read_b32 v111, a47\n"
    "v_mfma_f32_16x16x32_bf16 a[60:63], v[176:179], v[244:247], a[60:63]\n"
    "v_fma_f32 v108, v108, v228, s94\n"
    "v_fma_f32 v109, v109, v228, s94\n"
    "v_mfma_f32_16x16x32_bf16 a[60:63], v[180:183], v[92:95], a[60:63]\n"
    "s_waitcnt lgkmcnt(2)\n"
    "v_fma_f32 v110, v110, v228, s94\n"
    "v_fma_f32 v111, v111, v228, s94\n"
    "v_mfma_f32_16x16x32_bf16 a[64:67], v[160:163], v[128:131], a[64:67]\n"
    "ds_read_b128 v[176:179], v207 offset:8192\n"
    "v_cvt_pk_bf16_f32 v252, v108, v109\n"
    "v_mfma_f32_16x16x32_bf16 a[64:67], v[164:167], v[144:147], a[64:67]\n"
    "v_cvt_pk_bf16_f32 v253, v110, v111\n"
    "v_accvgpr_read_b32 v96, a48\n"
    "v_mfma_f32_16x16x32_bf16 a[68:71], v[160:163], v[132:135], a[68:71]\n"
    "v_accvgpr_read_b32 v97, a49\n"
    "v_accvgpr_read_b32 v98, a50\n"
    "v_mfma_f32_16x16x32_bf16 a[68:71], v[164:167], v[148:151], a[68:71]\n"
    "v_accvgpr_read_b32 v99, a51\n"
    "v_fma_f32 v96, v96, v222, s94\n"
    "v_mfma_f32_16x16x32_bf16 a[72:75], v[160:163], v[136:139], a[72:75]\n"
    "ds_read_b128 v[180:183], v207 offset:9216\n"
    "v_fma_f32 v97, v97, v222, s94\n"
    "v_mfma_f32_16x16x32_bf16 a[72:75], v[164:167], v[152:155], a[72:75]\n"
    "v_fma_f32 v98, v98, v222, s94\n"
    "v_fma_f32 v99, v99, v222, s94\n"
    "v_mfma_f32_16x16x32_bf16 a[76:79], v[160:163], v[140:143], a[76:79]\n"
    "v_cvt_pk_bf16_f32 v186, v96, v97\n"
    "v_cvt_pk_bf16_f32 v187, v98, v99\n"
    "v_mfma_f32_16x16x32_bf16 a[76:79], v[164:167], v[156:159], a[76:79]\n"
    "s_waitcnt lgkmcnt(2)\n"
    "buffer_store_dwordx4 v[184:187], v205, s[28:31], 0 offen offset:64 sc0 sc1\n"
    "v_accvgpr_read_b32 v100, a52\n"
    "v_accvgpr_read_b32 v101, a53\n"
    "v_accvgpr_read_b32 v102, a54\n"
    "v_mfma_f32_16x16x32_bf16 a[76:79], v[168:171], v[44:47], a[76:79]\n"
    "ds_read_b128 v[160:163], v201 offset:10240\n"
    "v_accvgpr_read_b32 v103, a55\n"
    "v_mfma_f32_16x16x32_bf16 a[76:79], v[172:175], v[76:79], a[76:79]\n"
    "v_fma_f32 v100, v100, v224, s94\n"
    "v_fma_f32 v101, v101, v224, s94\n"
    "v_mfma_f32_16x16x32_bf16 a[72:75], v[168:171], v[40:43], a[72:75]\n"
    "v_fma_f32 v102, v102, v224, s94\n"
    "v_fma_f32 v103, v103, v224, s94\n"
    "v_mfma_f32_16x16x32_bf16 a[72:75], v[172:175], v[72:75], a[72:75]\n"
    "v_cvt_pk_bf16_f32 v190, v100, v101\n"
    "v_cvt_pk_bf16_f32 v191, v102, v103\n"
    "v_mfma_f32_16x16x32_bf16 a[68:71], v[168:171], v[36:39], a[68:71]\n"
    "ds_read_b128 v[164:167], v201 offset:11264\n"
    "v_mfma_f32_16x16x32_bf16 a[68:71], v[172:175], v[68:71], a[68:71]\n"
    "buffer_store_dwordx4 v[188:191], v205, s[28:31], s62 offen offset:64 sc0 sc1\n"
    "v_accvgpr_read_b32 v104, a56\n"
    "v_accvgpr_read_b32 v105, a57\n"
    "v_accvgpr_read_b32 v106, a58\n"
    "v_mfma_f32_16x16x32_bf16 a[64:67], v[168:171], v[32:35], a[64:67]\n"
    "v_accvgpr_read_b32 v107, a59\n"
    "v_fma_f32 v104, v104, v226, s94\n"
    "v_mfma_f32_16x16x32_bf16 a[64:67], v[172:175], v[64:67], a[64:67]\n"
    "s_waitcnt lgkmcnt(2)\n"
    "v_fma_f32 v105, v105, v226, s94\n"
    "v_fma_f32 v106, v106, v226, s94\n"
    "v_mfma_f32_16x16x32_bf16 a[64:67], v[176:179], v[232:235], a[64:67]\n"
    "ds_read_b128 v[168:171], v201 offset:34816\n"
    "v_fma_f32 v107, v107, v226, s94\n"
    "v_mfma_f32_16x16x32_bf16 a[64:67], v[180:183], v[80:83], a[64:67]\n"
    "v_cvt_pk_bf16_f32 v250, v104, v105\n"
    "v_cvt_pk_bf16_f32 v251, v106, v107\n"
    "v_mfma_f32_16x16x32_bf16 a[68:71], v[176:179], v[236:239], a[68:71]\n"
    "buffer_store_dwordx4 v[248:251], v205, s[28:31], s63 offen offset:64 sc0 sc1\n"
    "v_accvgpr_read_b32 v108, a60\n"
    "v_accvgpr_read_b32 v109, a61\n"
    "v_accvgpr_read_b32 v110, a62\n"
    "v_mfma_f32_16x16x32_bf16 a[68:71], v[180:183], v[84:87], a[68:71]\n"
    "v_accvgpr_read_b32 v111, a63\n"
    "v_fma_f32 v108, v108, v228, s94\n"
    "v_mfma_f32_16x16x32_bf16 a[72:75], v[176:179], v[240:243], a[72:75]\n"
    "ds_read_b128 v[172:175], v201 offset:35840\n"
    "v_fma_f32 v109, v109, v228, s94\n"
    "v_mfma_f32_16x16x32_bf16 a[72:75], v[180:183], v[88:91], a[72:75]\n"
    "v_fma_f32 v110, v110, v228, s94\n"
    "v_fma_f32 v111, v111, v228, s94\n"
    "v_mfma_f32_16x16x32_bf16 a[76:79], v[176:179], v[244:247], a[76:79]\n"
    "v_cvt_pk_bf16_f32 v254, v108, v109\n"
    "v_cvt_pk_bf16_f32 v255, v110, v111\n"
    "v_mfma_f32_16x16x32_bf16 a[76:79], v[180:183], v[92:95], a[76:79]\n"
    "s_waitcnt lgkmcnt(2)\n"
    "buffer_store_dwordx4 v[252:255], v205, s[28:31], s64 offen offset:64 sc0 sc1\n"
    "v_accvgpr_read_b32 v96, a64\n"
    "v_accvgpr_read_b32 v97, a65\n"
    "v_accvgpr_read_b32 v98, a66\n"
    "v_mfma_f32_16x16x32_bf16 a[80:83], v[160:163], v[128:131], a[80:83]\n"
    "ds_read_b128 v[176:179], v207 offset:10240\n"
    "v_accvgpr_read_b32 v99, a67\n"
    "v_mfma_f32_16x16x32_bf16 a[80:83], v[164:167], v[144:147], a[80:83]\n"
    "v_fma_f32 v96, v96, v222, s94\n"
    "v_fma_f32 v97, v97, v222, s94\n"
    "v_mfma_f32_16x16x32_bf16 a[84:87], v[160:163], v[132:135], a[84:87]\n"
    "v_fma_f32 v98, v98, v222, s94\n"
    "v_fma_f32 v99, v99, v222, s94\n"
    "v_mfma_f32_16x16x32_bf16 a[84:87], v[164:167], v[148:151], a[84:87]\n"
    "v_cvt_pk_bf16_f32 v48, v96, v97\n"
    "v_cvt_pk_bf16_f32 v49, v98, v99\n"
    "v_mfma_f32_16x16x32_bf16 a[88:91], v[160:163], v[136:139], a[88:91]\n"
    "ds_read_b128 v[180:183], v207 offset:11264\n"
    "v_accvgpr_read_b32 v100, a68\n"
    "v_mfma_f32_16x16x32_bf16 a[88:91], v[164:167], v[152:155], a[88:91]\n"
    "v_accvgpr_read_b32 v101, a69\n"
    "v_accvgpr_read_b32 v102, a70\n"
    "v_mfma_f32_16x16x32_bf16 a[92:95], v[160:163], v[140:143], a[92:95]\n"
    "v_accvgpr_read_b32 v103, a71\n"
    "v_fma_f32 v100, v100, v224, s94\n"
    "v_mfma_f32_16x16x32_bf16 a[92:95], v[164:167], v[156:159], a[92:95]\n"
    "s_waitcnt lgkmcnt(2)\n"
    "v_fma_f32 v101, v101, v224, s94\n"
    "v_fma_f32 v102, v102, v224, s94\n"
    "v_mfma_f32_16x16x32_bf16 a[92:95], v[168:171], v[44:47], a[92:95]\n"
    "ds_read_b128 v[160:163], v201 offset:12288\n"
    "v_fma_f32 v103, v103, v224, s94\n"
    "v_mfma_f32_16x16x32_bf16 a[92:95], v[172:175], v[76:79], a[92:95]\n"
    "v_cvt_pk_bf16_f32 v52, v100, v101\n"
    "v_cvt_pk_bf16_f32 v53, v102, v103\n"
    "v_mfma_f32_16x16x32_bf16 a[88:91], v[168:171], v[40:43], a[88:91]\n"
    "v_accvgpr_read_b32 v104, a72\n"
    "v_accvgpr_read_b32 v105, a73\n"
    "v_mfma_f32_16x16x32_bf16 a[88:91], v[172:175], v[72:75], a[88:91]\n"
    "v_accvgpr_read_b32 v106, a74\n"
    "v_accvgpr_read_b32 v107, a75\n"
    "v_mfma_f32_16x16x32_bf16 a[84:87], v[168:171], v[36:39], a[84:87]\n"
    "ds_read_b128 v[164:167], v201 offset:13312\n"
    "v_fma_f32 v104, v104, v226, s94\n"
    "v_mfma_f32_16x16x32_bf16 a[84:87], v[172:175], v[68:71], a[84:87]\n"
    "v_fma_f32 v105, v105, v226, s94\n"
    "v_fma_f32 v106, v106, v226, s94\n"
    "v_mfma_f32_16x16x32_bf16 a[80:83], v[168:171], v[32:35], a[80:83]\n"
    "v_fma_f32 v107, v107, v226, s94\n"
    "v_cvt_pk_bf16_f32 v56, v104, v105\n"
    "v_mfma_f32_16x16x32_bf16 a[80:83], v[172:175], v[64:67], a[80:83]\n"
    "s_waitcnt lgkmcnt(2)\n"
    "v_cvt_pk_bf16_f32 v57, v106, v107\n"
    "v_accvgpr_read_b32 v108, a76\n"
    "v_mfma_f32_16x16x32_bf16 a[80:83], v[176:179], v[232:235], a[80:83]\n"
    "ds_read_b128 v[168:171], v201 offset:36864\n"
    "v_accvgpr_read_b32 v109, a77\n"
    "v_mfma_f32_16x16x32_bf16 a[80:83], v[180:183], v[80:83], a[80:83]\n"
    "v_accvgpr_read_b32 v110, a78\n"
    "v_accvgpr_read_b32 v111, a79\n"
    "v_mfma_f32_16x16x32_bf16 a[84:87], v[176:179], v[236:239], a[84:87]\n"
    "v_fma_f32 v108, v108, v228, s94\n"
    "v_fma_f32 v109, v109, v228, s94\n"
    "v_mfma_f32_16x16x32_bf16 a[84:87], v[180:183], v[84:87], a[84:87]\n"
    "v_fma_f32 v110, v110, v228, s94\n"
    "v_fma_f32 v111, v111, v228, s94\n"
    "v_mfma_f32_16x16x32_bf16 a[88:91], v[176:179], v[240:243], a[88:91]\n"
    "ds_read_b128 v[172:175], v201 offset:37888\n"
    "v_cvt_pk_bf16_f32 v60, v108, v109\n"
    "v_mfma_f32_16x16x32_bf16 a[88:91], v[180:183], v[88:91], a[88:91]\n"
    "v_cvt_pk_bf16_f32 v61, v110, v111\n"
    "v_mfma_f32_16x16x32_bf16 a[92:95], v[176:179], v[244:247], a[92:95]\n"
    "v_mfma_f32_16x16x32_bf16 a[92:95], v[180:183], v[92:95], a[92:95]\n"
    "s_waitcnt lgkmcnt(2)\n"
    "v_accvgpr_read_b32 v96, a80\n"
    "v_accvgpr_read_b32 v97, a81\n"
    "v_mfma_f32_16x16x32_bf16 a[96:99], v[160:163], v[128:131], a[96:99]\n"
    "ds_read_b128 v[176:179], v207 offset:12288\n"
    "v_accvgpr_read_b32 v98, a82\n"
    "v_mfma_f32_16x16x32_bf16 a[96:99], v[164:167], v[144:147], a[96:99]\n"
    "v_accvgpr_read_b32 v99, a83\n"
    "v_fma_f32 v96, v96, v222, s94\n"
    "v_mfma_f32_16x16x32_bf16 a[100:103], v[160:163], v[132:135], a[100:103]\n"
    "v_fma_f32 v97, v97, v222, s94\n"
    "v_fma_f32 v98, v98, v222, s94\n"
    "v_mfma_f32_16x16x32_bf16 a[100:103], v[164:167], v[148:151], a[100:103]\n"
    "v_fma_f32 v99, v99, v222, s94\n"
    "v_cvt_pk_bf16_f32 v50, v96, v97\n"
    "v_mfma_f32_16x16x32_bf16 a[104:107], v[160:163], v[136:139], a[104:107]\n"
    "ds_read_b128 v[180:183], v207 offset:13312\n"
    "v_cvt_pk_bf16_f32 v51, v98, v99\n"
    "v_mfma_f32_16x16x32_bf16 a[104:107], v[164:167], v[152:155], a[104:107]\n"
    "buffer_store_dwordx4 v[48:51], v205, s[28:31], 0 offen offset:128 sc0 sc1\n"
    "v_accvgpr_read_b32 v100, a84\n"
    "v_accvgpr_read_b32 v101, a85\n"
    "v_accvgpr_read_b32 v102, a86\n"
    "v_mfma_f32_16x16x32_bf16 a[108:111], v[160:163], v[140:143], a[108:111]\n"
    "v_accvgpr_read_b32 v103, a87\n"
    "v_fma_f32 v100, v100, v224, s94\n"
    "v_mfma_f32_16x16x32_bf16 a[108:111], v[164:167], v[156:159], a[108:111]\n"
    "s_waitcnt lgkmcnt(2)\n"
    "s_cmp_eq_u32 s57, 0\n"
    "s_cselect_b32 s77, s74, 0\n"
    "v_mov_b32 v196, 0x40000000\n"
    "v_mov_b32 v197, 0x800\n"
    "v_cmp_gt_u32 vcc, s77, v231\n"
    "v_mov_b32 v198, 0\n"
    "v_mov_b32 v198, 0\n"
    "v_cndmask_b32 v197, v196, v197, vcc\n"
    "buffer_store_dword v221, v197, s[44:47], 0 offen sc1\n"
    "v_mfma_f32_16x16x32_bf16 a[108:111], v[168:171], v[44:47], a[108:111]\n"
    "ds_read_b128 v[160:163], v201 offset:14336\n"
    "v_fma_f32 v101, v101, v224, s94\n"
    "v_mfma_f32_16x16x32_bf16 a[108:111], v[172:175], v[76:79], a[108:111]\n"
    "v_fma_f32 v102, v102, v224, s94\n"
    "v_fma_f32 v103, v103, v224, s94\n"
    "v_mfma_f32_16x16x32_bf16 a[104:107], v[168:171], v[40:43], a[104:107]\n"
    "v_cvt_pk_bf16_f32 v54, v100, v101\n"
    "v_cvt_pk_bf16_f32 v55, v102, v103\n"
    "v_mfma_f32_16x16x32_bf16 a[104:107], v[172:175], v[72:75], a[104:107]\n"
    "buffer_store_dwordx4 v[52:55], v205, s[28:31], s62 offen offset:128 sc0 sc1\n"
    "v_accvgpr_read_b32 v104, a88\n"
    "v_accvgpr_read_b32 v105, a89\n"
    "v_accvgpr_read_b32 v106, a90\n"
    "v_mfma_f32_16x16x32_bf16 a[100:103], v[168:171], v[36:39], a[100:103]\n"
    "ds_read_b128 v[164:167], v201 offset:15360\n"
    "v_accvgpr_read_b32 v107, a91\n"
    "v_mfma_f32_16x16x32_bf16 a[100:103], v[172:175], v[68:71], a[100:103]\n"
    "v_fma_f32 v104, v104, v226, s94\n"
    "v_fma_f32 v105, v105, v226, s94\n"
    "v_mfma_f32_16x16x32_bf16 a[96:99], v[168:171], v[32:35], a[96:99]\n"
    "v_fma_f32 v106, v106, v226, s94\n"
    "v_fma_f32 v107, v107, v226, s94\n"
    "v_mfma_f32_16x16x32_bf16 a[96:99], v[172:175], v[64:67], a[96:99]\n"
    "s_waitcnt lgkmcnt(2)\n"
    "v_cvt_pk_bf16_f32 v58, v104, v105\n"
    "v_cvt_pk_bf16_f32 v59, v106, v107\n"
    "v_mfma_f32_16x16x32_bf16 a[96:99], v[176:179], v[232:235], a[96:99]\n"
    "ds_read_b128 v[168:171], v201 offset:38912\n"
    "v_mfma_f32_16x16x32_bf16 a[96:99], v[180:183], v[80:83], a[96:99]\n"
    "buffer_store_dwordx4 v[56:59], v205, s[28:31], s63 offen offset:128 sc0 sc1\n"
    "v_accvgpr_read_b32 v108, a92\n"
    "v_accvgpr_read_b32 v109, a93\n"
    "v_accvgpr_read_b32 v110, a94\n"
    "v_mfma_f32_16x16x32_bf16 a[100:103], v[176:179], v[236:239], a[100:103]\n"
    "v_accvgpr_read_b32 v111, a95\n"
    "v_fma_f32 v108, v108, v228, s94\n"
    "v_mfma_f32_16x16x32_bf16 a[100:103], v[180:183], v[84:87], a[100:103]\n"
    "v_fma_f32 v109, v109, v228, s94\n"
    "v_fma_f32 v110, v110, v228, s94\n"
    "v_mfma_f32_16x16x32_bf16 a[104:107], v[176:179], v[240:243], a[104:107]\n"
    "ds_read_b128 v[172:175], v201 offset:39936\n"
    "v_fma_f32 v111, v111, v228, s94\n"
    "v_mfma_f32_16x16x32_bf16 a[104:107], v[180:183], v[88:91], a[104:107]\n"
    "v_cvt_pk_bf16_f32 v62, v108, v109\n"
    "v_cvt_pk_bf16_f32 v63, v110, v111\n"
    "v_mfma_f32_16x16x32_bf16 a[108:111], v[176:179], v[244:247], a[108:111]\n"
    "buffer_store_dwordx4 v[60:63], v205, s[28:31], s64 offen offset:128 sc0 sc1\n"
    "v_mfma_f32_16x16x32_bf16 a[108:111], v[180:183], v[92:95], a[108:111]\n"
    "s_waitcnt lgkmcnt(2)\n"
    "v_accvgpr_read_b32 v96, a96\n"
    "v_accvgpr_read_b32 v97, a97\n"
    "v_mfma_f32_16x16x32_bf16 a[112:115], v[160:163], v[128:131], a[112:115]\n"
    "ds_read_b128 v[176:179], v207 offset:14336\n"
    "v_accvgpr_read_b32 v98, a98\n"
    "v_mfma_f32_16x16x32_bf16 a[112:115], v[164:167], v[144:147], a[112:115]\n"
    "v_accvgpr_read_b32 v99, a99\n"
    "v_fma_f32 v96, v96, v222, s94\n"
    "v_mfma_f32_16x16x32_bf16 a[116:119], v[160:163], v[132:135], a[116:119]\n"
    "v_fma_f32 v97, v97, v222, s94\n"
    "v_fma_f32 v98, v98, v222, s94\n"
    "v_mfma_f32_16x16x32_bf16 a[116:119], v[164:167], v[148:151], a[116:119]\n"
    "v_fma_f32 v99, v99, v222, s94\n"
    "v_cvt_pk_bf16_f32 v184, v96, v97\n"
    "v_mfma_f32_16x16x32_bf16 a[120:123], v[160:163], v[136:139], a[120:123]\n"
    "ds_read_b128 v[180:183], v207 offset:15360\n"
    "v_cvt_pk_bf16_f32 v185, v98, v99\n"
    "v_mfma_f32_16x16x32_bf16 a[120:123], v[164:167], v[152:155], a[120:123]\n"
    "v_accvgpr_read_b32 v100, a100\n"
    "v_accvgpr_read_b32 v101, a101\n"
    "v_mfma_f32_16x16x32_bf16 a[124:127], v[160:163], v[140:143], a[124:127]\n"
    "v_accvgpr_read_b32 v102, a102\n"
    "v_accvgpr_read_b32 v103, a103\n"
    "v_mfma_f32_16x16x32_bf16 a[124:127], v[164:167], v[156:159], a[124:127]\n"
    "s_waitcnt lgkmcnt(2)\n"
    "v_fma_f32 v100, v100, v224, s94\n"
    "v_fma_f32 v101, v101, v224, s94\n"
    "v_mfma_f32_16x16x32_bf16 a[124:127], v[168:171], v[44:47], a[124:127]\n"
    "ds_read_b128 v[160:163], v201 offset:16384\n"
    "v_fma_f32 v102, v102, v224, s94\n"
    "v_mfma_f32_16x16x32_bf16 a[124:127], v[172:175], v[76:79], a[124:127]\n"
    "v_fma_f32 v103, v103, v224, s94\n"
    "v_cvt_pk_bf16_f32 v188, v100, v101\n"
    "v_mfma_f32_16x16x32_bf16 a[120:123], v[168:171], v[40:43], a[120:123]\n"
    "v_cvt_pk_bf16_f32 v189, v102, v103\n"
    "v_accvgpr_read_b32 v104, a104\n"
    "v_mfma_f32_16x16x32_bf16 a[120:123], v[172:175], v[72:75], a[120:123]\n"
    "v_accvgpr_read_b32 v105, a105\n"
    "v_accvgpr_read_b32 v106, a106\n"
    "v_mfma_f32_16x16x32_bf16 a[116:119], v[168:171], v[36:39], a[116:119]\n"
    "ds_read_b128 v[164:167], v201 offset:17408\n"
    "v_accvgpr_read_b32 v107, a107\n"
    "v_mfma_f32_16x16x32_bf16 a[116:119], v[172:175], v[68:71], a[116:119]\n"
    "v_fma_f32 v104, v104, v226, s94\n"
    "v_fma_f32 v105, v105, v226, s94\n"
    "v_mfma_f32_16x16x32_bf16 a[112:115], v[168:171], v[32:35], a[112:115]\n"
    "v_fma_f32 v106, v106, v226, s94\n"
    "v_fma_f32 v107, v107, v226, s94\n"
    "v_mfma_f32_16x16x32_bf16 a[112:115], v[172:175], v[64:67], a[112:115]\n"
    "s_waitcnt lgkmcnt(2)\n"
    "v_cvt_pk_bf16_f32 v248, v104, v105\n"
    "v_cvt_pk_bf16_f32 v249, v106, v107\n"
    "v_mfma_f32_16x16x32_bf16 a[112:115], v[176:179], v[232:235], a[112:115]\n"
    "ds_read_b128 v[168:171], v201 offset:40960\n"
    "v_accvgpr_read_b32 v108, a108\n"
    "v_mfma_f32_16x16x32_bf16 a[112:115], v[180:183], v[80:83], a[112:115]\n"
    "v_accvgpr_read_b32 v109, a109\n"
    "v_accvgpr_read_b32 v110, a110\n"
    "v_mfma_f32_16x16x32_bf16 a[116:119], v[176:179], v[236:239], a[116:119]\n"
    "v_accvgpr_read_b32 v111, a111\n"
    "v_fma_f32 v108, v108, v228, s94\n"
    "v_mfma_f32_16x16x32_bf16 a[116:119], v[180:183], v[84:87], a[116:119]\n"
    "v_fma_f32 v109, v109, v228, s94\n"
    "v_fma_f32 v110, v110, v228, s94\n"
    "v_mfma_f32_16x16x32_bf16 a[120:123], v[176:179], v[240:243], a[120:123]\n"
    "ds_read_b128 v[172:175], v201 offset:41984\n"
    "v_fma_f32 v111, v111, v228, s94\n"
    "v_mfma_f32_16x16x32_bf16 a[120:123], v[180:183], v[88:91], a[120:123]\n"
    "v_cvt_pk_bf16_f32 v252, v108, v109\n"
    "v_cvt_pk_bf16_f32 v253, v110, v111\n"
    "v_mfma_f32_16x16x32_bf16 a[124:127], v[176:179], v[244:247], a[124:127]\n"
    "v_mfma_f32_16x16x32_bf16 a[124:127], v[180:183], v[92:95], a[124:127]\n"
    "s_waitcnt lgkmcnt(2)\n"
    "v_accvgpr_read_b32 v96, a112\n"
    "v_accvgpr_read_b32 v97, a113\n"
    "v_mfma_f32_16x16x32_bf16 a[128:131], v[160:163], v[128:131], a[128:131]\n"
    "ds_read_b128 v[176:179], v207 offset:16384\n"
    "v_accvgpr_read_b32 v98, a114\n"
    "v_mfma_f32_16x16x32_bf16 a[128:131], v[164:167], v[144:147], a[128:131]\n"
    "v_accvgpr_read_b32 v99, a115\n"
    "v_fma_f32 v96, v96, v222, s94\n"
    "v_mfma_f32_16x16x32_bf16 a[132:135], v[160:163], v[132:135], a[132:135]\n"
    "v_fma_f32 v97, v97, v222, s94\n"
    "v_fma_f32 v98, v98, v222, s94\n"
    "v_mfma_f32_16x16x32_bf16 a[132:135], v[164:167], v[148:151], a[132:135]\n"
    "v_fma_f32 v99, v99, v222, s94\n"
    "v_cvt_pk_bf16_f32 v186, v96, v97\n"
    "v_mfma_f32_16x16x32_bf16 a[136:139], v[160:163], v[136:139], a[136:139]\n"
    "ds_read_b128 v[180:183], v207 offset:17408\n"
    "v_cvt_pk_bf16_f32 v187, v98, v99\n"
    "v_mfma_f32_16x16x32_bf16 a[136:139], v[164:167], v[152:155], a[136:139]\n"
    "buffer_store_dwordx4 v[184:187], v205, s[28:31], 0 offen offset:192 sc0 sc1\n"
    "v_accvgpr_read_b32 v100, a116\n"
    "v_accvgpr_read_b32 v101, a117\n"
    "v_accvgpr_read_b32 v102, a118\n"
    "v_mfma_f32_16x16x32_bf16 a[140:143], v[160:163], v[140:143], a[140:143]\n"
    "v_accvgpr_read_b32 v103, a119\n"
    "v_fma_f32 v100, v100, v224, s94\n"
    "v_mfma_f32_16x16x32_bf16 a[140:143], v[164:167], v[156:159], a[140:143]\n"
    "s_waitcnt lgkmcnt(2)\n"
    "v_fma_f32 v101, v101, v224, s94\n"
    "v_fma_f32 v102, v102, v224, s94\n"
    "v_mfma_f32_16x16x32_bf16 a[140:143], v[168:171], v[44:47], a[140:143]\n"
    "ds_read_b128 v[160:163], v201 offset:18432\n"
    "v_fma_f32 v103, v103, v224, s94\n"
    "v_mfma_f32_16x16x32_bf16 a[140:143], v[172:175], v[76:79], a[140:143]\n"
    "v_cvt_pk_bf16_f32 v190, v100, v101\n"
    "v_cvt_pk_bf16_f32 v191, v102, v103\n"
    "v_mfma_f32_16x16x32_bf16 a[136:139], v[168:171], v[40:43], a[136:139]\n"
    "buffer_store_dwordx4 v[188:191], v205, s[28:31], s62 offen offset:192 sc0 sc1\n"
    "v_accvgpr_read_b32 v104, a120\n"
    "v_accvgpr_read_b32 v105, a121\n"
    "v_accvgpr_read_b32 v106, a122\n"
    "v_mfma_f32_16x16x32_bf16 a[136:139], v[172:175], v[72:75], a[136:139]\n"
    "v_accvgpr_read_b32 v107, a123\n"
    "v_fma_f32 v104, v104, v226, s94\n"
    "v_mfma_f32_16x16x32_bf16 a[132:135], v[168:171], v[36:39], a[132:135]\n"
    "ds_read_b128 v[164:167], v201 offset:19456\n"
    "v_fma_f32 v105, v105, v226, s94\n"
    "v_mfma_f32_16x16x32_bf16 a[132:135], v[172:175], v[68:71], a[132:135]\n"
    "v_fma_f32 v106, v106, v226, s94\n"
    "v_fma_f32 v107, v107, v226, s94\n"
    "v_mfma_f32_16x16x32_bf16 a[128:131], v[168:171], v[32:35], a[128:131]\n"
    "v_cvt_pk_bf16_f32 v250, v104, v105\n"
    "v_cvt_pk_bf16_f32 v251, v106, v107\n"
    "v_mfma_f32_16x16x32_bf16 a[128:131], v[172:175], v[64:67], a[128:131]\n"
    "s_waitcnt lgkmcnt(2)\n"
    "buffer_store_dwordx4 v[248:251], v205, s[28:31], s63 offen offset:192 sc0 sc1\n"
    "v_accvgpr_read_b32 v108, a124\n"
    "v_accvgpr_read_b32 v109, a125\n"
    "v_accvgpr_read_b32 v110, a126\n"
    "v_mfma_f32_16x16x32_bf16 a[128:131], v[176:179], v[232:235], a[128:131]\n"
    "ds_read_b128 v[168:171], v201 offset:43008\n"
    "v_accvgpr_read_b32 v111, a127\n"
    "v_mfma_f32_16x16x32_bf16 a[128:131], v[180:183], v[80:83], a[128:131]\n"
    "v_fma_f32 v108, v108, v228, s94\n"
    "v_fma_f32 v109, v109, v228, s94\n"
    "v_mfma_f32_16x16x32_bf16 a[132:135], v[176:179], v[236:239], a[132:135]\n"
    "v_fma_f32 v110, v110, v228, s94\n"
    "v_fma_f32 v111, v111, v228, s94\n"
    "v_mfma_f32_16x16x32_bf16 a[132:135], v[180:183], v[84:87], a[132:135]\n"
    "v_cvt_pk_bf16_f32 v254, v108, v109\n"
    "v_cvt_pk_bf16_f32 v255, v110, v111\n"
    "v_mfma_f32_16x16x32_bf16 a[136:139], v[176:179], v[240:243], a[136:139]\n"
    "ds_read_b128 v[172:175], v201 offset:44032\n"
    "v_mfma_f32_16x16x32_bf16 a[136:139], v[180:183], v[88:91], a[136:139]\n"
    "buffer_store_dwordx4 v[252:255], v205, s[28:31], s64 offen offset:192 sc0 sc1\n"
    "v_mfma_f32_16x16x32_bf16 a[140:143], v[176:179], v[244:247], a[140:143]\n"
    "v_mfma_f32_16x16x32_bf16 a[140:143], v[180:183], v[92:95], a[140:143]\n"
    "s_waitcnt lgkmcnt(2)\n"
    "v_accvgpr_read_b32 v96, a128\n"
    "v_accvgpr_read_b32 v97, a129\n"
    "v_mfma_f32_16x16x32_bf16 a[144:147], v[160:163], v[128:131], a[144:147]\n"
    "ds_read_b128 v[176:179], v207 offset:18432\n"
    "v_accvgpr_read_b32 v98, a130\n"
    "v_mfma_f32_16x16x32_bf16 a[144:147], v[164:167], v[144:147], a[144:147]\n"
    "v_accvgpr_read_b32 v99, a131\n"
    "v_fma_f32 v96, v96, v222, s94\n"
    "v_mfma_f32_16x16x32_bf16 a[148:151], v[160:163], v[132:135], a[148:151]\n"
    "v_fma_f32 v97, v97, v222, s94\n"
    "v_fma_f32 v98, v98, v222, s94\n"
    "v_mfma_f32_16x16x32_bf16 a[148:151], v[164:167], v[148:151], a[148:151]\n"
    "v_fma_f32 v99, v99, v222, s94\n"
    "v_cvt_pk_bf16_f32 v48, v96, v97\n"
    "v_mfma_f32_16x16x32_bf16 a[152:155], v[160:163], v[136:139], a[152:155]\n"
    "ds_read_b128 v[180:183], v207 offset:19456\n"
    "v_cvt_pk_bf16_f32 v49, v98, v99\n"
    "v_mfma_f32_16x16x32_bf16 a[152:155], v[164:167], v[152:155], a[152:155]\n"
    "v_accvgpr_read_b32 v100, a132\n"
    "v_accvgpr_read_b32 v101, a133\n"
    "v_mfma_f32_16x16x32_bf16 a[156:159], v[160:163], v[140:143], a[156:159]\n"
    "v_accvgpr_read_b32 v102, a134\n"
    "v_accvgpr_read_b32 v103, a135\n"
    "v_mfma_f32_16x16x32_bf16 a[156:159], v[164:167], v[156:159], a[156:159]\n"
    "s_waitcnt lgkmcnt(2)\n"
    "v_fma_f32 v100, v100, v224, s94\n"
    "v_fma_f32 v101, v101, v224, s94\n"
    "v_mfma_f32_16x16x32_bf16 a[156:159], v[168:171], v[44:47], a[156:159]\n"
    "ds_read_b128 v[160:163], v201 offset:20480\n"
    "v_fma_f32 v102, v102, v224, s94\n"
    "v_mfma_f32_16x16x32_bf16 a[156:159], v[172:175], v[76:79], a[156:159]\n"
    "v_fma_f32 v103, v103, v224, s94\n"
    "v_cvt_pk_bf16_f32 v52, v100, v101\n"
    "v_mfma_f32_16x16x32_bf16 a[152:155], v[168:171], v[40:43], a[152:155]\n"
    "v_cvt_pk_bf16_f32 v53, v102, v103\n"
    "v_accvgpr_read_b32 v104, a136\n"
    "v_mfma_f32_16x16x32_bf16 a[152:155], v[172:175], v[72:75], a[152:155]\n"
    "v_accvgpr_read_b32 v105, a137\n"
    "v_accvgpr_read_b32 v106, a138\n"
    "v_mfma_f32_16x16x32_bf16 a[148:151], v[168:171], v[36:39], a[148:151]\n"
    "ds_read_b128 v[164:167], v201 offset:21504\n"
    "v_accvgpr_read_b32 v107, a139\n"
    "v_mfma_f32_16x16x32_bf16 a[148:151], v[172:175], v[68:71], a[148:151]\n"
    "v_fma_f32 v104, v104, v226, s94\n"
    "v_fma_f32 v105, v105, v226, s94\n"
    "v_mfma_f32_16x16x32_bf16 a[144:147], v[168:171], v[32:35], a[144:147]\n"
    "v_fma_f32 v106, v106, v226, s94\n"
    "v_fma_f32 v107, v107, v226, s94\n"
    "v_mfma_f32_16x16x32_bf16 a[144:147], v[172:175], v[64:67], a[144:147]\n"
    "s_waitcnt lgkmcnt(2)\n"
    "v_cvt_pk_bf16_f32 v56, v104, v105\n"
    "v_cvt_pk_bf16_f32 v57, v106, v107\n"
    "v_mfma_f32_16x16x32_bf16 a[144:147], v[176:179], v[232:235], a[144:147]\n"
    "ds_read_b128 v[168:171], v201 offset:45056\n"
    "v_accvgpr_read_b32 v108, a140\n"
    "v_mfma_f32_16x16x32_bf16 a[144:147], v[180:183], v[80:83], a[144:147]\n"
    "v_accvgpr_read_b32 v109, a141\n"
    "v_accvgpr_read_b32 v110, a142\n"
    "v_mfma_f32_16x16x32_bf16 a[148:151], v[176:179], v[236:239], a[148:151]\n"
    "v_accvgpr_read_b32 v111, a143\n"
    "v_fma_f32 v108, v108, v228, s94\n"
    "v_mfma_f32_16x16x32_bf16 a[148:151], v[180:183], v[84:87], a[148:151]\n"
    "v_fma_f32 v109, v109, v228, s94\n"
    "v_fma_f32 v110, v110, v228, s94\n"
    "v_mfma_f32_16x16x32_bf16 a[152:155], v[176:179], v[240:243], a[152:155]\n"
    "ds_read_b128 v[172:175], v201 offset:46080\n"
    "v_fma_f32 v111, v111, v228, s94\n"
    "v_mfma_f32_16x16x32_bf16 a[152:155], v[180:183], v[88:91], a[152:155]\n"
    "v_cvt_pk_bf16_f32 v60, v108, v109\n"
    "v_cvt_pk_bf16_f32 v61, v110, v111\n"
    "v_mfma_f32_16x16x32_bf16 a[156:159], v[176:179], v[244:247], a[156:159]\n"
    "v_mfma_f32_16x16x32_bf16 a[156:159], v[180:183], v[92:95], a[156:159]\n"
    "s_waitcnt lgkmcnt(2)\n"
    "v_accvgpr_read_b32 v96, a144\n"
    "v_accvgpr_read_b32 v97, a145\n"
    "v_mfma_f32_16x16x32_bf16 a[160:163], v[160:163], v[128:131], a[160:163]\n"
    "ds_read_b128 v[176:179], v207 offset:20480\n"
    "v_accvgpr_read_b32 v98, a146\n"
    "v_mfma_f32_16x16x32_bf16 a[160:163], v[164:167], v[144:147], a[160:163]\n"
    "v_accvgpr_read_b32 v99, a147\n"
    "v_fma_f32 v96, v96, v222, s94\n"
    "v_mfma_f32_16x16x32_bf16 a[164:167], v[160:163], v[132:135], a[164:167]\n"
    "v_fma_f32 v97, v97, v222, s94\n"
    "v_fma_f32 v98, v98, v222, s94\n"
    "v_mfma_f32_16x16x32_bf16 a[164:167], v[164:167], v[148:151], a[164:167]\n"
    "v_fma_f32 v99, v99, v222, s94\n"
    "v_cvt_pk_bf16_f32 v50, v96, v97\n"
    "v_mfma_f32_16x16x32_bf16 a[168:171], v[160:163], v[136:139], a[168:171]\n"
    "ds_read_b128 v[180:183], v207 offset:21504\n"
    "v_cvt_pk_bf16_f32 v51, v98, v99\n"
    "v_mfma_f32_16x16x32_bf16 a[168:171], v[164:167], v[152:155], a[168:171]\n"
    "buffer_store_dwordx4 v[48:51], v205, s[28:31], 0 offen offset:256 sc0 sc1\n"
    "v_accvgpr_read_b32 v100, a148\n"
    "v_accvgpr_read_b32 v101, a149\n"
    "v_accvgpr_read_b32 v102, a150\n"
    "v_mfma_f32_16x16x32_bf16 a[172:175], v[160:163], v[140:143], a[172:175]\n"
    "v_accvgpr_read_b32 v103, a151\n"
    "v_fma_f32 v100, v100, v224, s94\n"
    "v_mfma_f32_16x16x32_bf16 a[172:175], v[164:167], v[156:159], a[172:175]\n"
    "s_waitcnt lgkmcnt(2)\n"
    "v_fma_f32 v101, v101, v224, s94\n"
    "v_fma_f32 v102, v102, v224, s94\n"
    "v_mfma_f32_16x16x32_bf16 a[172:175], v[168:171], v[44:47], a[172:175]\n"
    "ds_read_b128 v[160:163], v201 offset:22528\n"
    "v_fma_f32 v103, v103, v224, s94\n"
    "v_mfma_f32_16x16x32_bf16 a[172:175], v[172:175], v[76:79], a[172:175]\n"
    "v_cvt_pk_bf16_f32 v54, v100, v101\n"
    "v_cvt_pk_bf16_f32 v55, v102, v103\n"
    "v_mfma_f32_16x16x32_bf16 a[168:171], v[168:171], v[40:43], a[168:171]\n"
    "buffer_store_dwordx4 v[52:55], v205, s[28:31], s62 offen offset:256 sc0 sc1\n"
    "v_accvgpr_read_b32 v104, a152\n"
    "v_accvgpr_read_b32 v105, a153\n"
    "v_accvgpr_read_b32 v106, a154\n"
    "v_mfma_f32_16x16x32_bf16 a[168:171], v[172:175], v[72:75], a[168:171]\n"
    "v_accvgpr_read_b32 v107, a155\n"
    "v_fma_f32 v104, v104, v226, s94\n"
    "v_mfma_f32_16x16x32_bf16 a[164:167], v[168:171], v[36:39], a[164:167]\n"
    "ds_read_b128 v[164:167], v201 offset:23552\n"
    "v_fma_f32 v105, v105, v226, s94\n"
    "v_mfma_f32_16x16x32_bf16 a[164:167], v[172:175], v[68:71], a[164:167]\n"
    "v_fma_f32 v106, v106, v226, s94\n"
    "v_fma_f32 v107, v107, v226, s94\n"
    "v_mfma_f32_16x16x32_bf16 a[160:163], v[168:171], v[32:35], a[160:163]\n"
    "v_cvt_pk_bf16_f32 v58, v104, v105\n"
    "v_cvt_pk_bf16_f32 v59, v106, v107\n"
    "v_mfma_f32_16x16x32_bf16 a[160:163], v[172:175], v[64:67], a[160:163]\n"
    "s_waitcnt lgkmcnt(2)\n"
    "buffer_store_dwordx4 v[56:59], v205, s[28:31], s63 offen offset:256 sc0 sc1\n"
    "v_accvgpr_read_b32 v108, a156\n"
    "v_accvgpr_read_b32 v109, a157\n"
    "v_accvgpr_read_b32 v110, a158\n"
    "v_mfma_f32_16x16x32_bf16 a[160:163], v[176:179], v[232:235], a[160:163]\n"
    "ds_read_b128 v[168:171], v201 offset:47104\n"
    "v_accvgpr_read_b32 v111, a159\n"
    "v_mfma_f32_16x16x32_bf16 a[160:163], v[180:183], v[80:83], a[160:163]\n"
    "v_fma_f32 v108, v108, v228, s94\n"
    "v_fma_f32 v109, v109, v228, s94\n"
    "v_mfma_f32_16x16x32_bf16 a[164:167], v[176:179], v[236:239], a[164:167]\n"
    "v_fma_f32 v110, v110, v228, s94\n"
    "v_fma_f32 v111, v111, v228, s94\n"
    "v_mfma_f32_16x16x32_bf16 a[164:167], v[180:183], v[84:87], a[164:167]\n"
    "v_cvt_pk_bf16_f32 v62, v108, v109\n"
    "v_cvt_pk_bf16_f32 v63, v110, v111\n"
    "v_mfma_f32_16x16x32_bf16 a[168:171], v[176:179], v[240:243], a[168:171]\n"
    "ds_read_b128 v[172:175], v201 offset:48128\n"
    "v_mfma_f32_16x16x32_bf16 a[168:171], v[180:183], v[88:91], a[168:171]\n"
    "buffer_store_dwordx4 v[60:63], v205, s[28:31], s64 offen offset:256 sc0 sc1\n"
    "v_mfma_f32_16x16x32_bf16 a[172:175], v[176:179], v[244:247], a[172:175]\n"
    "v_mfma_f32_16x16x32_bf16 a[172:175], v[180:183], v[92:95], a[172:175]\n"
    "s_waitcnt lgkmcnt(2)\n"
    "v_accvgpr_read_b32 v96, a160\n"
    "v_accvgpr_read_b32 v97, a161\n"
    "v_mfma_f32_16x16x32_bf16 a[176:179], v[160:163], v[128:131], a[176:179]\n"
    "ds_read_b128 v[176:179], v207 offset:22528\n"
    "v_accvgpr_read_b32 v98, a162\n"
    "v_mfma_f32_16x16x32_bf16 a[176:179], v[164:167], v[144:147], a[176:179]\n"
    "v_accvgpr_read_b32 v99, a163\n"
    "v_fma_f32 v96, v96, v222, s94\n"
    "v_mfma_f32_16x16x32_bf16 a[180:183], v[160:163], v[132:135], a[180:183]\n"
    "v_fma_f32 v97, v97, v222, s94\n"
    "v_fma_f32 v98, v98, v222, s94\n"
    "v_mfma_f32_16x16x32_bf16 a[180:183], v[164:167], v[148:151], a[180:183]\n"
    "v_fma_f32 v99, v99, v222, s94\n"
    "v_cvt_pk_bf16_f32 v184, v96, v97\n"
    "v_mfma_f32_16x16x32_bf16 a[184:187], v[160:163], v[136:139], a[184:187]\n"
    "ds_read_b128 v[180:183], v207 offset:23552\n"
    "v_cvt_pk_bf16_f32 v185, v98, v99\n"
    "v_mfma_f32_16x16x32_bf16 a[184:187], v[164:167], v[152:155], a[184:187]\n"
    "v_accvgpr_read_b32 v100, a164\n"
    "v_accvgpr_read_b32 v101, a165\n"
    "v_mfma_f32_16x16x32_bf16 a[188:191], v[160:163], v[140:143], a[188:191]\n"
    "v_accvgpr_read_b32 v102, a166\n"
    "v_accvgpr_read_b32 v103, a167\n"
    "v_mfma_f32_16x16x32_bf16 a[188:191], v[164:167], v[156:159], a[188:191]\n"
    "s_waitcnt lgkmcnt(2)\n"
    "v_fma_f32 v100, v100, v224, s94\n"
    "v_fma_f32 v101, v101, v224, s94\n"
    "v_mfma_f32_16x16x32_bf16 a[188:191], v[168:171], v[44:47], a[188:191]\n"
    "v_fma_f32 v102, v102, v224, s94\n"
    "v_fma_f32 v103, v103, v224, s94\n"
    "v_mfma_f32_16x16x32_bf16 a[188:191], v[172:175], v[76:79], a[188:191]\n"
    "v_cvt_pk_bf16_f32 v188, v100, v101\n"
    "v_cvt_pk_bf16_f32 v189, v102, v103\n"
    "v_mfma_f32_16x16x32_bf16 a[184:187], v[168:171], v[40:43], a[184:187]\n"
    "v_accvgpr_read_b32 v104, a168\n"
    "v_accvgpr_read_b32 v105, a169\n"
    "v_mfma_f32_16x16x32_bf16 a[184:187], v[172:175], v[72:75], a[184:187]\n"
    "v_accvgpr_read_b32 v106, a170\n"
    "v_accvgpr_read_b32 v107, a171\n"
    "v_mfma_f32_16x16x32_bf16 a[180:183], v[168:171], v[36:39], a[180:183]\n"
    "v_fma_f32 v104, v104, v226, s94\n"
    "v_fma_f32 v105, v105, v226, s94\n"
    "v_mfma_f32_16x16x32_bf16 a[180:183], v[172:175], v[68:71], a[180:183]\n"
    "v_fma_f32 v106, v106, v226, s94\n"
    "v_fma_f32 v107, v107, v226, s94\n"
    "v_mfma_f32_16x16x32_bf16 a[176:179], v[168:171], v[32:35], a[176:179]\n"
    "v_cvt_pk_bf16_f32 v248, v104, v105\n"
    "v_cvt_pk_bf16_f32 v249, v106, v107\n"
    "v_mfma_f32_16x16x32_bf16 a[176:179], v[172:175], v[64:67], a[176:179]\n"
    "s_waitcnt lgkmcnt(0)\n"
    "v_accvgpr_read_b32 v108, a172\n"
    "v_accvgpr_read_b32 v109, a173\n"
    "v_mfma_f32_16x16x32_bf16 a[176:179], v[176:179], v[232:235], a[176:179]\n"
    "v_accvgpr_read_b32 v110, a174\n"
    "v_accvgpr_read_b32 v111, a175\n"
    "v_mfma_f32_16x16x32_bf16 a[176:179], v[180:183], v[80:83], a[176:179]\n"
    "v_fma_f32 v108, v108, v228, s94\n"
    "v_fma_f32 v109, v109, v228, s94\n"
    "v_mfma_f32_16x16x32_bf16 a[180:183], v[176:179], v[236:239], a[180:183]\n"
    "v_fma_f32 v110, v110, v228, s94\n"
    "v_fma_f32 v111, v111, v228, s94\n"
    "v_mfma_f32_16x16x32_bf16 a[180:183], v[180:183], v[84:87], a[180:183]\n"
    "v_cvt_pk_bf16_f32 v252, v108, v109\n"
    "v_cvt_pk_bf16_f32 v253, v110, v111\n"
    "v_mfma_f32_16x16x32_bf16 a[184:187], v[176:179], v[240:243], a[184:187]\n"
    "v_mfma_f32_16x16x32_bf16 a[184:187], v[180:183], v[88:91], a[184:187]\n"
    "v_mfma_f32_16x16x32_bf16 a[188:191], v[176:179], v[244:247], a[188:191]\n"
    "v_mfma_f32_16x16x32_bf16 a[188:191], v[180:183], v[92:95], a[188:191]\n"
    "v_accvgpr_read_b32 v96, a176\n"
    "v_accvgpr_read_b32 v97, a177\n"
    "v_accvgpr_read_b32 v98, a178\n"
    "v_accvgpr_read_b32 v99, a179\n"
    "v_fma_f32 v96, v96, v222, s94\n"
    "v_fma_f32 v97, v97, v222, s94\n"
    "v_fma_f32 v98, v98, v222, s94\n"
    "v_fma_f32 v99, v99, v222, s94\n"
    "v_cvt_pk_bf16_f32 v186, v96, v97\n"
    "v_cvt_pk_bf16_f32 v187, v98, v99\n"
    "buffer_store_dwordx4 v[184:187], v205, s[28:31], 0 offen offset:320 sc0 sc1\n"
    "v_accvgpr_read_b32 v100, a180\n"
    "v_accvgpr_read_b32 v101, a181\n"
    "v_accvgpr_read_b32 v102, a182\n"
    "v_accvgpr_read_b32 v103, a183\n"
    "v_fma_f32 v100, v100, v224, s94\n"
    "v_fma_f32 v101, v101, v224, s94\n"
    "v_fma_f32 v102, v102, v224, s94\n"
    "v_fma_f32 v103, v103, v224, s94\n"
    "v_cvt_pk_bf16_f32 v190, v100, v101\n"
    "v_cvt_pk_bf16_f32 v191, v102, v103\n"
    "buffer_store_dwordx4 v[188:191], v205, s[28:31], s62 offen offset:320 sc0 sc1\n"
    "v_accvgpr_read_b32 v104, a184\n"
    "v_accvgpr_read_b32 v105, a185\n"
    "v_accvgpr_read_b32 v106, a186\n"
    "v_accvgpr_read_b32 v107, a187\n"
    "v_fma_f32 v104, v104, v226, s94\n"
    "v_fma_f32 v105, v105, v226, s94\n"
    "v_fma_f32 v106, v106, v226, s94\n"
    "v_fma_f32 v107, v107, v226, s94\n"
    "v_cvt_pk_bf16_f32 v250, v104, v105\n"
    "v_cvt_pk_bf16_f32 v251, v106, v107\n"
    "buffer_store_dwordx4 v[248:251], v205, s[28:31], s63 offen offset:320 sc0 sc1\n"
    "v_accvgpr_read_b32 v108, a188\n"
    "v_accvgpr_read_b32 v109, a189\n"
    "v_accvgpr_read_b32 v110, a190\n"
    "v_accvgpr_read_b32 v111, a191\n"
    "v_fma_f32 v108, v108, v228, s94\n"
    "v_fma_f32 v109, v109, v228, s94\n"
    "v_fma_f32 v110, v110, v228, s94\n"
    "v_fma_f32 v111, v111, v228, s94\n"
    "v_cvt_pk_bf16_f32 v254, v108, v109\n"
    "v_cvt_pk_bf16_f32 v255, v110, v111\n"
    "buffer_store_dwordx4 v[252:255], v205, s[28:31], s64 offen offset:320 sc0 sc1\n"
    "v_add_f32 v196, v112, v116\n"
    "v_add_f32 v197, v120, v124\n"
    "v_add_f32 v196, v196, v197\n"
    "v_cmp_ngt_f32 vcc, 0x45800000, v196\n"
    "s_barrier\n"
    "s_cbranch_vccnz LSLOW\n"
    "LFIN:\n"
    "s_branch LEND\n"
    "LSPRE0:\n"
    "LSPIN0:\n"
    "s_sleep 1\n"
    "buffer_load_dwordx2 v[218:219], v213, s[44:47], 0 offen sc1\n"
    "s_waitcnt vmcnt(0)\n"
    "s_add_u32 s73, s73, 1\n"
    "s_cmp_gt_u32 s73, 200000\n"
    "s_cbranch_scc1 LSPB0\n"
    "v_cmp_ne_u32 s[80:81], s67, v219\n"
    "v_cmp_gt_u32 vcc, s66, v218\n"
    "s_or_b64 vcc, vcc, s[80:81]\n"
    "s_cbranch_vccnz LSPIN0\n"
    "s_branch LSPB0\n"
    "LSPRE1:\n"
    "LSPIN1:\n"
    "s_sleep 1\n"
    "buffer_load_dwordx2 v[218:219], v213, s[44:47], 0 offen sc1\n"
    "s_waitcnt vmcnt(0)\n"
    "s_add_u32 s73, s73, 1\n"
    "s_cmp_gt_u32 s73, 200000\n"
    "s_cbranch_scc1 LSPB1\n"
    "v_cmp_ne_u32 s[80:81], s67, v219\n"
    "v_cmp_gt_u32 vcc, s66, v218\n"
    "s_or_b64 vcc, vcc, s[80:81]\n"
    "s_cbranch_vccnz LSPIN1\n"
    "s_branch LSPB1\n"
    "LSPRE2:\n"
    "LSPIN2:\n"
    "s_sleep 1\n"
    "buffer_load_dwordx2 v[218:219], v213, s[44:47], 0 offen sc1\n"
    "s_waitcnt vmcnt(0)\n"
    "s_add_u32 s73, s73, 1\n"
    "s_cmp_gt_u32 s73, 200000\n"
    "s_cbranch_scc1 LSPB2\n"
    "v_cmp_ne_u32 s[80:81], s67, v219\n"
    "v_cmp_gt_u32 vcc, s66, v218\n"
    "s_or_b64 vcc, vcc, s[80:81]\n"
    "s_cbranch_vccnz LSPIN2\n"
    "s_branch LSPB2\n"
    "LSPRE3:\n"
    "LSPIN3:\n"
    "s_sleep 1\n"
    "buffer_load_dwordx2 v[218:219], v213, s[44:47], 0 offen sc1\n"
    "s_waitcnt vmcnt(0)\n"
    "s_add_u32 s73, s73, 1\n"
    "s_cmp_gt_u32 s73, 200000\n"
    "s_cbranch_scc1 LSPB3\n"
    "v_cmp_ne_u32 s[80:81], s67, v219\n"
    "v_cmp_gt_u32 vcc, s66, v218\n"
    "s_or_b64 vcc, vcc, s[80:81]\n"
    "s_cbranch_vccnz LSPIN3\n"
    "s_branch LSPB3\n"
    "LSPRE4:\n"
    "LSPIN4:\n"
    "s_sleep 1\n"
    "buffer_load_dwordx2 v[218:219], v213, s[44:47], 0 offen sc1\n"
    "s_waitcnt vmcnt(0)\n"
    "s_add_u32 s73, s73, 1\n"
    "s_cmp_gt_u32 s73, 200000\n"
    "s_cbranch_scc1 LSPB4\n"
    "v_cmp_ne_u32 s[80:81], s67, v219\n"
    "v_cmp_gt_u32 vcc, s66, v218\n"
    "s_or_b64 vcc, vcc, s[80:81]\n"
    "s_cbranch_vccnz LSPIN4\n"
    "s_branch LSPB4\n"
    "LSPRE5:\n"
    "LSPIN5:\n"
    "s_sleep 1\n"
    "buffer_load_dwordx2 v[218:219], v213, s[44:47], 0 offen sc1\n"
    "s_waitcnt vmcnt(0)\n"
    "s_add_u32 s73, s73, 1\n"
    "s_cmp_gt_u32 s73, 200000\n"
    "s_cbranch_scc1 LSPB5\n"
    "v_cmp_ne_u32 s[80:81], s67, v219\n"
    "v_cmp_gt_u32 vcc, s66, v218\n"
    "s_or_b64 vcc, vcc, s[80:81]\n"
    "s_cbranch_vccnz LSPIN5\n"
    "s_branch LSPB5\n"
    "LSLOW:\n"
    "s_waitcnt vmcnt(0)\n"
    "s_nop 15\n"
    "s_mul_i32 s62, s58, 0x9000\n"
    "v_add_u32 v205, s62, v200\n"
    "s_mov_b32 s63, 0\n"
    "v_mov_b32 v128, 0\n"
    "v_mov_b32 v129, 0\n"
    "v_mov_b32 v130, 0\n"
    "v_mov_b32 v131, 0\n"
    "v_mov_b32 v96, 0xff61b1e6\n"
    "v_mov_b32 v100, 0xff61b1e6\n"
    "v_mov_b32 v104, 0xff61b1e6\n"
    "v_mov_b32 v108, 0xff61b1e6\n"
    "v_mfma_f32_16x16x32_bf16 a[0:3], v[128:131], v[128:131], 0\n"
    "v_mfma_f32_16x16x32_bf16 a[4:7], v[128:131], v[128:131], 0\n"
    "v_mfma_f32_16x16x32_bf16 a[8:11], v[128:131], v[128:131], 0\n"
    "v_mfma_f32_16x16x32_bf16 a[12:15], v[128:131], v[128:131], 0\n"
    "v_mfma_f32_16x16x32_bf16 a[16:19], v[128:131], v[128:131], 0\n"
    "v_mfma_f32_16x16x32_bf16 a[20:23], v[128:131], v[128:131], 0\n"
    "v_mfma_f32_16x16x32_bf16 a[24:27], v[128:131], v[128:131], 0\n"
    "v_mfma_f32_16x16x32_bf16 a[28:31], v[128:131], v[128:131], 0\n"
    "v_mfma_f32_16x16x32_bf16 a[32:35], v[128:131], v[128:131], 0\n"
    "v_mfma_f32_16x16x32_bf16 a[36:39], v[128:131], v[128:131], 0\n"
    "v_mfma_f32_16x16x32_bf16 a[40:43], v[128:131], v[128:131], 0\n"
    "v_mfma_f32_16x16x32_bf16 a[44:47], v[128:131], v[128:131], 0\n"
    "v_mfma_f32_16x16x32_bf16 a[48:51], v[128:131], v[128:131], 0\n"
    "v_mfma_f32_16x16x32_bf16 a[52:55], v[128:131], v[128:131], 0\n"
    "v_mfma_f32_16x16x32_bf16 a[56:59], v[128:131], v[128:131], 0\n"
    "v_mfma_f32_16x16x32_bf16 a[60:63], v[128:131], v[128:131], 0\n"
    "v_mfma_f32_16x16x32_bf16 a[64:67], v[128:131], v[128:131], 0\n"
    "v_mfma_f32_16x16x32_bf16 a[68:71], v[128:131], v[128:131], 0\n"
    "v_mfma_f32_16x16x32_bf16 a[72:75], v[128:131], v[128:131], 0\n"
    "v_mfma_f32_16x16x32_bf16 a[76:79], v[128:131], v[128:131], 0\n"
    "v_mfma_f32_16x16x32_bf16 a[80:83], v[128:131], v[128:131], 0\n"
    "v_mfma_f32_16x16x32_bf16 a[84:87], v[128:131], v[128:131], 0\n"
    "v_mfma_f32_16x16x32_bf16 a[88:91], v[128:131], v[128:131], 0\n"
    "v_mfma_f32_16x16x32_bf16 a[92:95], v[128:131], v[128:131], 0\n"
    "v_mfma_f32_16x16x32_bf16 a[96:99], v[128:131], v[128:131], 0\n"
    "v_mfma_f32_16x16x32_bf16 a[100:103], v[128:131], v[128:131], 0\n"
    "v_mfma_f32_16x16x32_bf16 a[104:107], v[128:131], v[128:131], 0\n"
    "v_mfma_f32_16x16x32_bf16 a[108:111], v[128:131], v[128:131], 0\n"
    "v_mfma_f32_16x16x32_bf16 a[112:115], v[128:131], v[128:131], 0\n"
    "v_mfma_f32_16x16x32_bf16 a[116:119], v[128:131], v[128:131], 0\n"
    "v_mfma_f32_16x16x32_bf16 a[120:123], v[128:131], v[128:131], 0\n"
    "v_mfma_f32_16x16x32_bf16 a[124:127], v[128:131], v[128:131], 0\n"
    "v_mfma_f32_16x16x32_bf16 a[128:131], v[128:131], v[128:131], 0\n"
    "v_mfma_f32_16x16x32_bf16 a[132:135], v[128:131], v[128:131], 0\n"
    "v_mfma_f32_16x16x32_bf16 a[136:139], v[128:131], v[128:131], 0\n"
    "v_mfma_f32_16x16x32_bf16 a[140:143], v[128:131], v[128:131], 0\n"
    "v_mfma_f32_16x16x32_bf16 a[144:147], v[128:131], v[128:131], 0\n"
    "v_mfma_f32_16x16x32_bf16 a[148:151], v[128:131], v[128:131], 0\n"
    "v_mfma_f32_16x16x32_bf16 a[152:155], v[128:131], v[128:131], 0\n"
    "v_mfma_f32_16x16x32_bf16 a[156:159], v[128:131], v[128:131], 0\n"
    "v_mfma_f32_16x16x32_bf16 a[160:163], v[128:131], v[128:131], 0\n"
    "v_mfma_f32_16x16x32_bf16 a[164:167], v[128:131], v[128:131], 0\n"
    "v_mfma_f32_16x16x32_bf16 a[168:171], v[128:131], v[128:131], 0\n"
    "v_mfma_f32_16x16x32_bf16 a[172:175], v[128:131], v[128:131], 0\n"
    "v_mfma_f32_16x16x32_bf16 a[176:179], v[128:131], v[128:131], 0\n"
    "v_mfma_f32_16x16x32_bf16 a[180:183], v[128:131], v[128:131], 0\n"
    "v_mfma_f32_16x16x32_bf16 a[184:187], v[128:131], v[128:131], 0\n"
    "v_mfma_f32_16x16x32_bf16 a[188:191], v[128:131], v[128:131], 0\n"
    "v_mfma_f32_16x16x32_bf16 v[112:115], v[128:131], v[128:131], 0\n"
    "v_mfma_f32_16x16x32_bf16 v[116:119], v[128:131], v[128:131], 0\n"
    "v_mfma_f32_16x16x32_bf16 v[120:123], v[128:131], v[128:131], 0\n"
    "v_mfma_f32_16x16x32_bf16 v[124:127], v[128:131], v[128:131], 0\n"
    "s_nop 15\n"
    "LSLOOP:\n"
    "s_add_u32 s78, s63, 0\n"
    "s_add_u32 m0, s62, 0\n"
    "s_nop 0\n"
    "buffer_load_dwordx4 v200, s[36:39], s78 offen sc1 lds\n"
    "buffer_load_dwordx4 v200, s[36:39], s78 offen offset:1024 sc1 lds\n"
    "buffer_load_dwordx4 v200, s[36:39], s78 offen offset:2048 sc1 lds\n"
    "buffer_load_dwordx4 v200, s[36:39], s78 offen offset:3072 sc1 lds\n"
    "s_add_u32 s78, s63, 0x1000\n"
    "s_add_u32 m0, s62, 0x1000\n"
    "s_nop 0\n"
    "buffer_load_dwordx4 v200, s[36:39], s78 offen sc1 lds\n"
    "buffer_load_dwordx4 v200, s[36:39], s78 offen offset:1024 sc1 lds\n"
    "buffer_load_dwordx4 v200, s[36:39], s78 offen offset:2048 sc1 lds\n"
    "buffer_load_dwordx4 v200, s[36:39], s78 offen offset:3072 sc1 lds\n"
    "s_add_u32 s78, s63, 0x2000\n"
    "s_add_u32 m0, s62, 0x2000\n"
    "s_nop 0\n"
    "buffer_load_dwordx4 v200, s[36:39], s78 offen sc1 lds\n"
    "buffer_load_dwordx4 v200, s[36:39], s78 offen offset:1024 sc1 lds\n"
    "buffer_load_dwordx4 v200, s[36:39], s78 offen offset:2048 sc1 lds\n"
    "buffer_load_dwordx4 v200, s[36:39], s78 offen offset:3072 sc1 lds\n"
    "s_add_u32 s78, s63, 0\n"
    "s_add_u32 m0, s62, 0x3000\n"
    "s_nop 0\n"
    "buffer_load_dwordx4 v200, s[40:43], s78 offen sc1 lds\n"
    "buffer_load_dwordx4 v200, s[40:43], s78 offen offset:1024 sc1 lds\n"
    "buffer_load_dwordx4 v200, s[40:43], s78 offen offset:2048 sc1 lds\n"
    "buffer_load_dwordx4 v200, s[40:43], s78 offen offset:3072 sc1 lds\n"
    "s_add_u32 s78, s63, 0x1000\n"
    "s_add_u32 m0, s62, 0x4000\n"
    "s_nop 0\n"
    "buffer_load_dwordx4 v200, s[40:43], s78 offen sc1 lds\n"
    "buffer_load_dwordx4 v200, s[40:43], s78 offen offset:1024 sc1 lds\n"
    "buffer_load_dwordx4 v200, s[40:43], s78 offen offset:2048 sc1 lds\n"
    "buffer_load_dwordx4 v200, s[40:43], s78 offen offset:3072 sc1 lds\n"
    "s_add_u32 s78, s63, 0x2000\n"
    "s_add_u32 m0, s62, 0x5000\n"
    "s_nop 0\n"
    "buffer_load_dwordx4 v200, s[40:43], s78 offen sc1 lds\n"
    "buffer_load_dwordx4 v200, s[40:43], s78 offen offset:1024 sc1 lds\n"
    "buffer_load_dwordx4 v200, s[40:43], s78 offen offset:2048 sc1 lds\n"
    "buffer_load_dwordx4 v200, s[40:43], s78 offen offset:3072 sc1 lds\n"
    "s_add_u32 s78, s63, 0x3000\n"
    "s_add_u32 m0, s62, 0x6000\n"
    "s_nop 0\n"
    "buffer_load_dwordx4 v200, s[40:43], s78 offen sc1 lds\n"
    "buffer_load_dwordx4 v200, s[40:43], s78 offen offset:1024 sc1 lds\n"
    "buffer_load_dwordx4 v200, s[40:43], s78 offen offset:2048 sc1 lds\n"
    "buffer_load_dwordx4 v200, s[40:43], s78 offen offset:3072 sc1 lds\n"
    "s_add_u32 s78, s63, 0x4000\n"
    "s_add_u32 m0, s62, 0x7000\n"
    "s_nop 0\n"
    "buffer_load_dwordx4 v200, s[40:43], s78 offen sc1 lds\n"
    "buffer_load_dwordx4 v200, s[40:43], s78 offen offset:1024 sc1 lds\n"
    "buffer_load_dwordx4 v200, s[40:43], s78 offen offset:2048 sc1 lds\n"
    "buffer_load_dwordx4 v200, s[40:43], s78 offen offset:3072 sc1 lds\n"
    "s_add_u32 s78, s63, 0x5000\n"
    "s_add_u32 m0, s62, 0x8000\n"
    "s_nop 0\n"
    "buffer_load_dwordx4 v200, s[40:43], s78 offen sc1 lds\n"
    "buffer_load_dwordx4 v200, s[40:43], s78 offen offset:1024 sc1 lds\n"
    "buffer_load_dwordx4 v200, s[40:43], s78 offen offset:2048 sc1 lds\n"
    "buffer_load_dwordx4 v200, s[40:43], s78 offen offset:3072 sc1 lds\n"
    "s_waitcnt vmcnt(0)\n"
    "ds_read_b128 v[160:163], v205\n"
    "ds_read_b128 v[164:167], v205 offset:1024\n"
    "s_waitcnt lgkmcnt(1)\n"
    "v_mfma_f32_16x16x32_bf16 v[32:35], v[160:163], a[192:195], 0\n"
    "s_waitcnt lgkmcnt(0)\n"
    "v_mfma_f32_16x16x32_bf16 v[32:35], v[164:167], a[196:199], v[32:35]\n"
    "v_mfma_f32_16x16x32_bf16 v[36:39], v[164:167], a[220:223], 0\n"
    "v_mfma_f32_16x16x32_bf16 v[36:39], v[160:163], a[216:219], v[36:39]\n"
    "v_mfma_f32_16x16x32_bf16 v[40:43], v[160:163], a[240:243], 0\n"
    "v_mfma_f32_16x16x32_bf16 v[40:43], v[164:167], a[244:247], v[40:43]\n"
    "v_mfma_f32_16x16x32_bf16 v[44:47], v[164:167], v[12:15], 0\n"
    "v_mfma_f32_16x16x32_bf16 v[44:47], v[160:163], v[8:11], v[44:47]\n"
    "ds_read_b128 v[168:171], v205 offset:2048\n"
    "ds_read_b128 v[172:175], v205 offset:3072\n"
    "s_waitcnt lgkmcnt(1)\n"
    "v_mfma_f32_16x16x32_bf16 v[44:47], v[168:171], v[16:19], v[44:47]\n"
    "s_waitcnt lgkmcnt(0)\n"
    "v_mfma_f32_16x16x32_bf16 v[44:47], v[172:175], v[20:23], v[44:47]\n"
    "v_mfma_f32_16x16x32_bf16 v[40:43], v[172:175], a[252:255], v[40:43]\n"
    "v_mfma_f32_16x16x32_bf16 v[40:43], v[168:171], a[248:251], v[40:43]\n"
    "v_mfma_f32_16x16x32_bf16 v[36:39], v[168:171], a[224:227], v[36:39]\n"
    "v_mfma_f32_16x16x32_bf16 v[36:39], v[172:175], a[228:231], v[36:39]\n"
    "v_mfma_f32_16x16x32_bf16 v[32:35], v[172:175], a[204:207], v[32:35]\n"
    "v_mfma_f32_16x16x32_bf16 v[32:35], v[168:171], a[200:203], v[32:35]\n"
    "ds_read_b128 v[176:179], v205 offset:4096\n"
    "ds_read_b128 v[180:183], v205 offset:5120\n"
    "s_waitcnt lgkmcnt(1)\n"
    "v_mfma_f32_16x16x32_bf16 v[32:35], v[176:179], a[208:211], v[32:35]\n"
    "s_waitcnt lgkmcnt(0)\n"
    "v_mfma_f32_16x16x32_bf16 v[32:35], v[180:183], a[212:215], v[32:35]\n"
    "v_mfma_f32_16x16x32_bf16 v[36:39], v[180:183], a[236:239], v[36:39]\n"
    "v_mfma_f32_16x16x32_bf16 v[36:39], v[176:179], a[232:235], v[36:39]\n"
    "v_mfma_f32_16x16x32_bf16 v[40:43], v[176:179], v[0:3], v[40:43]\n"
    "v_mfma_f32_16x16x32_bf16 v[40:43], v[180:183], v[4:7], v[40:43]\n"
    "v_mfma_f32_16x16x32_bf16 v[44:47], v[180:183], v[28:31], v[44:47]\n"
    "v_mfma_f32_16x16x32_bf16 v[44:47], v[176:179], v[24:27], v[44:47]\n"
    "ds_read_b128 v[160:163], v205 offset:10240\n"
    "ds_read_b128 v[164:167], v205 offset:11264\n"
    "s_waitcnt lgkmcnt(1)\n"
    "v_mfma_f32_16x16x32_bf16 v[60:63], v[160:163], v[24:27], 0\n"
    "s_waitcnt lgkmcnt(0)\n"
    "v_mfma_f32_16x16x32_bf16 v[60:63], v[164:167], v[28:31], v[60:63]\n"
    "v_mfma_f32_16x16x32_bf16 v[56:59], v[164:167], v[4:7], 0\n"
    "v_mfma_f32_16x16x32_bf16 v[56:59], v[160:163], v[0:3], v[56:59]\n"
    "v_mfma_f32_16x16x32_bf16 v[52:55], v[160:163], a[232:235], 0\n"
    "v_mfma_f32_16x16x32_bf16 v[52:55], v[164:167], a[236:239], v[52:55]\n"
    "v_mfma_f32_16x16x32_bf16 v[48:51], v[164:167], a[212:215], 0\n"
    "v_mfma_f32_16x16x32_bf16 v[48:51], v[160:163], a[208:211], v[48:51]\n"
    "ds_read_b128 v[168:171], v205 offset:8192\n"
    "ds_read_b128 v[172:175], v205 offset:9216\n"
    "s_waitcnt lgkmcnt(1)\n"
    "v_mfma_f32_16x16x32_bf16 v[48:51], v[168:171], a[200:203], v[48:51]\n"
    "s_waitcnt lgkmcnt(0)\n"
    "v_mfma_f32_16x16x32_bf16 v[48:51], v[172:175], a[204:207], v[48:51]\n"
    "v_mfma_f32_16x16x32_bf16 v[52:55], v[172:175], a[228:231], v[52:55]\n"
    "v_mfma_f32_16x16x32_bf16 v[52:55], v[168:171], a[224:227], v[52:55]\n"
    "v_mfma_f32_16x16x32_bf16 v[56:59], v[168:171], a[248:251], v[56:59]\n"
    "v_mfma_f32_16x16x32_bf16 v[56:59], v[172:175], a[252:255], v[56:59]\n"
    "v_mfma_f32_16x16x32_bf16 v[60:63], v[172:175], v[20:23], v[60:63]\n"
    "v_mfma_f32_16x16x32_bf16 v[60:63], v[168:171], v[16:19], v[60:63]\n"
    "ds_read_b128 v[176:179], v205 offset:6144\n"
    "ds_read_b128 v[180:183], v205 offset:7168\n"
    "s_waitcnt lgkmcnt(1)\n"
    "v_mfma_f32_16x16x32_bf16 v[60:63], v[176:179], v[8:11], v[60:63]\n"
    "s_waitcnt lgkmcnt(0)\n"
    "v_mfma_f32_16x16x32_bf16 v[60:63], v[180:183], v[12:15], v[60:63]\n"
    "v_mfma_f32_16x16x32_bf16 v[56:59], v[180:183], a[244:247], v[56:59]\n"
    "v_mfma_f32_16x16x32_bf16 v[56:59], v[176:179], a[240:243], v[56:59]\n"
    "v_mfma_f32_16x16x32_bf16 v[52:55], v[176:179], a[216:219], v[52:55]\n"
    "v_mfma_f32_16x16x32_bf16 v[52:55], v[180:183], a[220:223], v[52:55]\n"
    "v_mfma_f32_16x16x32_bf16 v[48:51], v[180:183], a[196:199], v[48:51]\n"
    "v_mfma_f32_16x16x32_bf16 v[48:51], v[176:179], a[192:195], v[48:51]\n"
    "s_add_u32 s78, s63, 0x3000\n"
    "s_add_u32 m0, s62, 0\n"
    "s_nop 0\n"
    "buffer_load_dwordx4 v200, s[36:39], s78 offen sc1 lds\n"
    "buffer_load_dwordx4 v200, s[36:39], s78 offen offset:1024 sc1 lds\n"
    "buffer_load_dwordx4 v200, s[36:39], s78 offen offset:2048 sc1 lds\n"
    "buffer_load_dwordx4 v200, s[36:39], s78 offen offset:3072 sc1 lds\n"
    "s_add_u32 s78, s63, 0x4000\n"
    "s_add_u32 m0, s62, 0x1000\n"
    "s_nop 0\n"
    "buffer_load_dwordx4 v200, s[36:39], s78 offen sc1 lds\n"
    "buffer_load_dwordx4 v200, s[36:39], s78 offen offset:1024 sc1 lds\n"
    "buffer_load_dwordx4 v200, s[36:39], s78 offen offset:2048 sc1 lds\n"
    "buffer_load_dwordx4 v200, s[36:39], s78 offen offset:3072 sc1 lds\n"
    "s_add_u32 s78, s63, 0x5000\n"
    "s_add_u32 m0, s62, 0x2000\n"
    "s_nop 0\n"
    "buffer_load_dwordx4 v200, s[36:39], s78 offen sc1 lds\n"
    "buffer_load_dwordx4 v200, s[36:39], s78 offen offset:1024 sc1 lds\n"
    "buffer_load_dwordx4 v200, s[36:39], s78 offen offset:2048 sc1 lds\n"
    "buffer_load_dwordx4 v200, s[36:39], s78 offen offset:3072 sc1 lds\n"
    "s_waitcnt vmcnt(0)\n"
    "ds_read_b128 v[160:163], v205\n"
    "ds_read_b128 v[164:167], v205 offset:1024\n"
    "s_waitcnt lgkmcnt(1)\n"
    "v_mfma_f32_16x16x32_bf16 v[64:67], v[160:163], a[192:195], 0\n"
    "s_waitcnt lgkmcnt(0)\n"
    "v_mfma_f32_16x16x32_bf16 v[64:67], v[164:167], a[196:199], v[64:67]\n"
    "v_mfma_f32_16x16x32_bf16 v[68:71], v[164:167], a[220:223], 0\n"
    "v_mfma_f32_16x16x32_bf16 v[68:71], v[160:163], a[216:219], v[68:71]\n"
    "v_mfma_f32_16x16x32_bf16 v[72:75], v[160:163], a[240:243], 0\n"
    "v_mfma_f32_16x16x32_bf16 v[72:75], v[164:167], a[244:247], v[72:75]\n"
    "v_mfma_f32_16x16x32_bf16 v[76:79], v[164:167], v[12:15], 0\n"
    "v_mfma_f32_16x16x32_bf16 v[76:79], v[160:163], v[8:11], v[76:79]\n"
    "ds_read_b128 v[168:171], v205 offset:2048\n"
    "ds_read_b128 v[172:175], v205 offset:3072\n"
    "s_waitcnt lgkmcnt(1)\n"
    "v_mfma_f32_16x16x32_bf16 v[76:79], v[168:171], v[16:19], v[76:79]\n"
    "s_waitcnt lgkmcnt(0)\n"
    "v_mfma_f32_16x16x32_bf16 v[76:79], v[172:175], v[20:23], v[76:79]\n"
    "v_mfma_f32_16x16x32_bf16 v[72:75], v[172:175], a[252:255], v[72:75]\n"
    "v_mfma_f32_16x16x32_bf16 v[72:75], v[168:171], a[248:251], v[72:75]\n"
    "v_mfma_f32_16x16x32_bf16 v[68:71], v[168:171], a[224:227], v[68:71]\n"
    "v_mfma_f32_16x16x32_bf16 v[68:71], v[172:175], a[228:231], v[68:71]\n"
    "v_mfma_f32_16x16x32_bf16 v[64:67], v[172:175], a[204:207], v[64:67]\n"
    "v_mfma_f32_16x16x32_bf16 v[64:67], v[168:171], a[200:203], v[64:67]\n"
    "ds_read_b128 v[176:179], v205 offset:4096\n"
    "ds_read_b128 v[180:183], v205 offset:5120\n"
    "s_waitcnt lgkmcnt(1)\n"
    "v_mfma_f32_16x16x32_bf16 v[64:67], v[176:179], a[208:211], v[64:67]\n"
    "s_waitcnt lgkmcnt(0)\n"
    "v_mfma_f32_16x16x32_bf16 v[64:67], v[180:183], a[212:215], v[64:67]\n"
    "v_mfma_f32_16x16x32_bf16 v[68:71], v[180:183], a[236:239], v[68:71]\n"
    "v_mfma_f32_16x16x32_bf16 v[68:71], v[176:179], a[232:235], v[68:71]\n"
    "v_mfma_f32_16x16x32_bf16 v[72:75], v[176:179], v[0:3], v[72:75]\n"
    "v_mfma_f32_16x16x32_bf16 v[72:75], v[180:183], v[4:7], v[72:75]\n"
    "v_mfma_f32_16x16x32_bf16 v[76:79], v[180:183], v[28:31], v[76:79]\n"
    "v_mfma_f32_16x16x32_bf16 v[76:79], v[176:179], v[24:27], v[76:79]\n"
    "ds_read_b128 v[160:163], v205 offset:10240\n"
    "ds_read_b128 v[164:167], v205 offset:11264\n"
    "s_waitcnt lgkmcnt(1)\n"
    "v_mfma_f32_16x16x32_bf16 v[92:95], v[160:163], v[24:27], 0\n"
    "s_waitcnt lgkmcnt(0)\n"
    "v_mfma_f32_16x16x32_bf16 v[92:95], v[164:167], v[28:31], v[92:95]\n"
    "v_mfma_f32_16x16x32_bf16 v[88:91], v[164:167], v[4:7], 0\n"
    "v_mfma_f32_16x16x32_bf16 v[88:91], v[160:163], v[0:3], v[88:91]\n"
    "v_mfma_f32_16x16x32_bf16 v[84:87], v[160:163], a[232:235], 0\n"
    "v_mfma_f32_16x16x32_bf16 v[84:87], v[164:167], a[236:239], v[84:87]\n"
    "v_mfma_f32_16x16x32_bf16 v[80:83], v[164:167], a[212:215], 0\n"
    "v_mfma_f32_16x16x32_bf16 v[80:83], v[160:163], a[208:211], v[80:83]\n"
    "ds_read_b128 v[168:171], v205 offset:8192\n"
    "ds_read_b128 v[172:175], v205 offset:9216\n"
    "s_waitcnt lgkmcnt(1)\n"
    "v_mfma_f32_16x16x32_bf16 v[80:83], v[168:171], a[200:203], v[80:83]\n"
    "s_waitcnt lgkmcnt(0)\n"
    "v_mfma_f32_16x16x32_bf16 v[80:83], v[172:175], a[204:207], v[80:83]\n"
    "v_mfma_f32_16x16x32_bf16 v[84:87], v[172:175], a[228:231], v[84:87]\n"
    "v_mfma_f32_16x16x32_bf16 v[84:87], v[168:171], a[224:227], v[84:87]\n"
    "v_mfma_f32_16x16x32_bf16 v[88:91], v[168:171], a[248:251], v[88:91]\n"
    "v_mfma_f32_16x16x32_bf16 v[88:91], v[172:175], a[252:255], v[88:91]\n"
    "v_mfma_f32_16x16x32_bf16 v[92:95], v[172:175], v[20:23], v[92:95]\n"
    "v_mfma_f32_16x16x32_bf16 v[92:95], v[168:171], v[16:19], v[92:95]\n"
    "ds_read_b128 v[176:179], v205 offset:6144\n"
    "ds_read_b128 v[180:183], v205 offset:7168\n"
    "s_waitcnt lgkmcnt(1)\n"
    "v_mfma_f32_16x16x32_bf16 v[92:95], v[176:179], v[8:11], v[92:95]\n"
    "s_waitcnt lgkmcnt(0)\n"
    "v_mfma_f32_16x16x32_bf16 v[92:95], v[180:183], v[12:15], v[92:95]\n"
    "v_mfma_f32_16x16x32_bf16 v[88:91], v[180:183], a[244:247], v[88:91]\n"
    "v_mfma_f32_16x16x32_bf16 v[88:91], v[176:179], a[240:243], v[88:91]\n"
    "v_mfma_f32_16x16x32_bf16 v[84:87], v[176:179], a[216:219], v[84:87]\n"
    "v_mfma_f32_16x16x32_bf16 v[84:87], v[180:183], a[220:223], v[84:87]\n"
    "v_mfma_f32_16x16x32_bf16 v[80:83], v[180:183], a[196:199], v[80:83]\n"
    "v_mfma_f32_16x16x32_bf16 v[80:83], v[176:179], a[192:195], v[80:83]\n"
    "v_max3_f32 v196, v32, v33, v34\n"
    "v_max3_f32 v196, v196, v35, v48\n"
    "v_max3_f32 v196, v196, v49, v50\n"
    "v_max3_f32 v196, v196, v51, v64\n"
    "v_max3_f32 v196, v196, v65, v66\n"
    "s_nop 2\n"
    "v_max3_f32 v196, v196, v67, v80\n"
    "v_max3_f32 v196, v196, v81, v82\n"
    "v_max_f32 v196, v196, v83\n"
    "v_mov_b32 v197, v196\n"
    "s_nop 1\n"
    "v_permlane32_swap_b32 v196, v197\n"
    "v_max_f32 v196, v196, v197\n"
    "v_mov_b32 v197, v196\n"
    "s_nop 1\n"
    "v_permlane16_swap_b32 v196, v197\n"
    "v_max_f32 v196, v196, v197\n"
    "v_max_f32 v196, v196, v96\n"
    "v_sub_f32 v197, v96, v196\n"
    "v_mov_b32 v96, v196\n"
    "v_exp_f32 v197, v197\n"
    "v_sub_f32 v32, v32, v196\n"
    "v_sub_f32 v33, v33, v196\n"
    "v_sub_f32 v34, v34, v196\n"
    "v_sub_f32 v35, v35, v196\n"
    "v_sub_f32 v48, v48, v196\n"
    "v_sub_f32 v49, v49, v196\n"
    "v_sub_f32 v50, v50, v196\n"
    "v_sub_f32 v51, v51, v196\n"
    "v_sub_f32 v64, v64, v196\n"
    "v_sub_f32 v65, v65, v196\n"
    "v_sub_f32 v66, v66, v196\n"
    "v_sub_f32 v67, v67, v196\n"
    "v_sub_f32 v80, v80, v196\n"
    "v_sub_f32 v81, v81, v196\n"
    "v_sub_f32 v82, v82, v196\n"
    "v_sub_f32 v83, v83, v196\n"
    "v_mul_f32 v112, v112, v197\n"
    "v_mul_f32 v113, v113, v197\n"
    "v_mul_f32 v114, v114, v197\n"
    "v_mul_f32 v115, v115, v197\n"
    "v_accvgpr_read_b32 v198, a0\n"
    "v_accvgpr_read_b32 v199, a1\n"
    "v_mul_f32 v198, v198, v197\n"
    "v_mul_f32 v199, v199, v197\n"
    "v_accvgpr_write_b32 a0, v198\n"
    "v_accvgpr_write_b32 a1, v199\n"
    "v_accvgpr_read_b32 v198, a2\n"
    "v_accvgpr_read_b32 v199, a3\n"
    "v_mul_f32 v198, v198, v197\n"
    "v_mul_f32 v199, v199, v197\n"
    "v_accvgpr_write_b32 a2, v198\n"
    "v_accvgpr_write_b32 a3, v199\n"
    "v_accvgpr_read_b32 v198, a16\n"
    "v_accvgpr_read_b32 v199, a17\n"
    "v_mul_f32 v198, v198, v197\n"
    "v_mul_f32 v199, v199, v197\n"
    "v_accvgpr_write_b32 a16, v198\n"
    "v_accvgpr_write_b32 a17, v199\n"
    "v_accvgpr_read_b32 v198, a18\n"
    "v_accvgpr_read_b32 v199, a19\n"
    "v_mul_f32 v198, v198, v197\n"
    "v_mul_f32 v199, v199, v197\n"
    "v_accvgpr_write_b32 a18, v198\n"
    "v_accvgpr_write_b32 a19, v199\n"
    "v_accvgpr_read_b32 v198, a32\n"
    "v_accvgpr_read_b32 v199, a33\n"
    "v_mul_f32 v198, v198, v197\n"
    "v_mul_f32 v199, v199, v197\n"
    "v_accvgpr_write_b32 a32, v198\n"
    "v_accvgpr_write_b32 a33, v199\n"
    "v_accvgpr_read_b32 v198, a34\n"
    "v_accvgpr_read_b32 v199, a35\n"
    "v_mul_f32 v198, v198, v197\n"
    "v_mul_f32 v199, v199, v197\n"
    "v_accvgpr_write_b32 a34, v198\n"
    "v_accvgpr_write_b32 a35, v199\n"
    "v_accvgpr_read_b32 v198, a48\n"
    "v_accvgpr_read_b32 v199, a49\n"
    "v_mul_f32 v198, v198, v197\n"
    "v_mul_f32 v199, v199, v197\n"
    "v_accvgpr_write_b32 a48, v198\n"
    "v_accvgpr_write_b32 a49, v199\n"
    "v_accvgpr_read_b32 v198, a50\n"
    "v_accvgpr_read_b32 v199, a51\n"
    "v_mul_f32 v198, v198, v197\n"
    "v_mul_f32 v199, v199, v197\n"
    "v_accvgpr_write_b32 a50, v198\n"
    "v_accvgpr_write_b32 a51, v199\n"
    "v_accvgpr_read_b32 v198, a64\n"
    "v_accvgpr_read_b32 v199, a65\n"
    "v_mul_f32 v198, v198, v197\n"
    "v_mul_f32 v199, v199, v197\n"
    "v_accvgpr_write_b32 a64, v198\n"
    "v_accvgpr_write_b32 a65, v199\n"
    "v_accvgpr_read_b32 v198, a66\n"
    "v_accvgpr_read_b32 v199, a67\n"
    "v_mul_f32 v198, v198, v197\n"
    "v_mul_f32 v199, v199, v197\n"
    "v_accvgpr_write_b32 a66, v198\n"
    "v_accvgpr_write_b32 a67, v199\n"
    "v_accvgpr_read_b32 v198, a80\n"
    "v_accvgpr_read_b32 v199, a81\n"
    "v_mul_f32 v198, v198, v197\n"
    "v_mul_f32 v199, v199, v197\n"
    "v_accvgpr_write_b32 a80, v198\n"
    "v_accvgpr_write_b32 a81, v199\n"
    "v_accvgpr_read_b32 v198, a82\n"
    "v_accvgpr_read_b32 v199, a83\n"
    "v_mul_f32 v198, v198, v197\n"
    "v_mul_f32 v199, v199, v197\n"
    "v_accvgpr_write_b32 a82, v198\n"
    "v_accvgpr_write_b32 a83, v199\n"
    "v_accvgpr_read_b32 v198, a96\n"
    "v_accvgpr_read_b32 v199, a97\n"
    "v_mul_f32 v198, v198, v197\n"
    "v_mul_f32 v199, v199, v197\n"
    "v_accvgpr_write_b32 a96, v198\n"
    "v_accvgpr_write_b32 a97, v199\n"
    "v_accvgpr_read_b32 v198, a98\n"
    "v_accvgpr_read_b32 v199, a99\n"
    "v_mul_f32 v198, v198, v197\n"
    "v_mul_f32 v199, v199, v197\n"
    "v_accvgpr_write_b32 a98, v198\n"
    "v_accvgpr_write_b32 a99, v199\n"
    "v_accvgpr_read_b32 v198, a112\n"
    "v_accvgpr_read_b32 v199, a113\n"
    "v_mul_f32 v198, v198, v197\n"
    "v_mul_f32 v199, v199, v197\n"
    "v_accvgpr_write_b32 a112, v198\n"
    "v_accvgpr_write_b32 a113, v199\n"
    "v_accvgpr_read_b32 v198, a114\n"
    "v_accvgpr_read_b32 v199, a115\n"
    "v_mul_f32 v198, v198, v197\n"
    "v_mul_f32 v199, v199, v197\n"
    "v_accvgpr_write_b32 a114, v198\n"
    "v_accvgpr_write_b32 a115, v199\n"
    "v_accvgpr_read_b32 v198, a128\n"
    "v_accvgpr_read_b32 v199, a129\n"
    "v_mul_f32 v198, v198, v197\n"
    "v_mul_f32 v199, v199, v197\n"
    "v_accvgpr_write_b32 a128, v198\n"
    "v_accvgpr_write_b32 a129, v199\n"
    "v_accvgpr_read_b32 v198, a130\n"
    "v_accvgpr_read_b32 v199, a131\n"
    "v_mul_f32 v198, v198, v197\n"
    "v_mul_f32 v199, v199, v197\n"
    "v_accvgpr_write_b32 a130, v198\n"
    "v_accvgpr_write_b32 a131, v199\n"
    "v_accvgpr_read_b32 v198, a144\n"
    "v_accvgpr_read_b32 v199, a145\n"
    "v_mul_f32 v198, v198, v197\n"
    "v_mul_f32 v199, v199, v197\n"
    "v_accvgpr_write_b32 a144, v198\n"
    "v_accvgpr_write_b32 a145, v199\n"
    "v_accvgpr_read_b32 v198, a146\n"
    "v_accvgpr_read_b32 v199, a147\n"
    "v_mul_f32 v198, v198, v197\n"
    "v_mul_f32 v199, v199, v197\n"
    "v_accvgpr_write_b32 a146, v198\n"
    "v_accvgpr_write_b32 a147, v199\n"
    "v_accvgpr_read_b32 v198, a160\n"
    "v_accvgpr_read_b32 v199, a161\n"
    "v_mul_f32 v198, v198, v197\n"
    "v_mul_f32 v199, v199, v197\n"
    "v_accvgpr_write_b32 a160, v198\n"
    "v_accvgpr_write_b32 a161, v199\n"
    "v_accvgpr_read_b32 v198, a162\n"
    "v_accvgpr_read_b32 v199, a163\n"
    "v_mul_f32 v198, v198, v197\n"
    "v_mul_f32 v199, v199, v197\n"
    "v_accvgpr_write_b32 a162, v198\n"
    "v_accvgpr_write_b32 a163, v199\n"
    "v_accvgpr_read_b32 v198, a176\n"
    "v_accvgpr_read_b32 v199, a177\n"
    "v_mul_f32 v198, v198, v197\n"
    "v_mul_f32 v199, v199, v197\n"
    "v_accvgpr_write_b32 a176, v198\n"
    "v_accvgpr_write_b32 a177, v199\n"
    "v_accvgpr_read_b32 v198, a178\n"
    "v_accvgpr_read_b32 v199, a179\n"
    "v_mul_f32 v198, v198, v197\n"
    "v_mul_f32 v199, v199, v197\n"
    "v_accvgpr_write_b32 a178, v198\n"
    "v_accvgpr_write_b32 a179, v199\n"
    "v_max3_f32 v196, v36, v37, v38\n"
    "v_max3_f32 v196, v196, v39, v52\n"
    "v_max3_f32 v196, v196, v53, v54\n"
    "v_max3_f32 v196, v196, v55, v68\n"
    "v_max3_f32 v196, v196, v69, v70\n"
    "v_max3_f32 v196, v196, v71, v84\n"
    "v_max3_f32 v196, v196, v85, v86\n"
    "v_max_f32 v196, v196, v87\n"
    "v_mov_b32 v197, v196\n"
    "s_nop 1\n"
    "v_permlane32_swap_b32 v196, v197\n"
    "v_max_f32 v196, v196, v197\n"
    "v_mov_b32 v197, v196\n"
    "s_nop 1\n"
    "v_permlane16_swap_b32 v196, v197\n"
    "v_max_f32 v196, v196, v197\n"
    "v_max_f32 v196, v196, v100\n"
    "v_sub_f32 v197, v100, v196\n"
    "v_mov_b32 v100, v196\n"
    "v_exp_f32 v197, v197\n"
    "v_sub_f32 v36, v36, v196\n"
    "v_sub_f32 v37, v37, v196\n"
    "v_sub_f32 v38, v38, v196\n"
    "v_sub_f32 v39, v39, v196\n"
    "v_sub_f32 v52, v52, v196\n"
    "v_sub_f32 v53, v53, v196\n"
    "v_sub_f32 v54, v54, v196\n"
    "v_sub_f32 v55, v55, v196\n"
    "v_sub_f32 v68, v68, v196\n"
    "v_sub_f32 v69, v69, v196\n"
    "v_sub_f32 v70, v70, v196\n"
    "v_sub_f32 v71, v71, v196\n"
    "v_sub_f32 v84, v84, v196\n"
    "v_sub_f32 v85, v85, v196\n"
    "v_sub_f32 v86, v86, v196\n"
    "v_sub_f32 v87, v87, v196\n"
    "v_mul_f32 v116, v116, v197\n"
    "v_mul_f32 v117, v117, v197\n"
    "v_mul_f32 v118, v118, v197\n"
    "v_mul_f32 v119, v119, v197\n"
    "v_accvgpr_read_b32 v198, a4\n"
    "v_accvgpr_read_b32 v199, a5\n"
    "v_mul_f32 v198, v198, v197\n"
    "v_mul_f32 v199, v199, v197\n"
    "v_accvgpr_write_b32 a4, v198\n"
    "v_accvgpr_write_b32 a5, v199\n"
    "v_accvgpr_read_b32 v198, a6\n"
    "v_accvgpr_read_b32 v199, a7\n"
    "v_mul_f32 v198, v198, v197\n"
    "v_mul_f32 v199, v199, v197\n"
    "v_accvgpr_write_b32 a6, v198\n"
    "v_accvgpr_write_b32 a7, v199\n"
    "v_accvgpr_read_b32 v198, a20\n"
    "v_accvgpr_read_b32 v199, a21\n"
    "v_mul_f32 v198, v198, v197\n"
    "v_mul_f32 v199, v199, v197\n"
    "v_accvgpr_write_b32 a20, v198\n"
    "v_accvgpr_write_b32 a21, v199\n"
    "v_accvgpr_read_b32 v198, a22\n"
    "v_accvgpr_read_b32 v199, a23\n"
    "v_mul_f32 v198, v198, v197\n"
    "v_mul_f32 v199, v199, v197\n"
    "v_accvgpr_write_b32 a22, v198\n"
    "v_accvgpr_write_b32 a23, v199\n"
    "v_accvgpr_read_b32 v198, a36\n"
    "v_accvgpr_read_b32 v199, a37\n"
    "v_mul_f32 v198, v198, v197\n"
    "v_mul_f32 v199, v199, v197\n"
    "v_accvgpr_write_b32 a36, v198\n"
    "v_accvgpr_write_b32 a37, v199\n"
    "v_accvgpr_read_b32 v198, a38\n"
    "v_accvgpr_read_b32 v199, a39\n"
    "v_mul_f32 v198, v198, v197\n"
    "v_mul_f32 v199, v199, v197\n"
    "v_accvgpr_write_b32 a38, v198\n"
    "v_accvgpr_write_b32 a39, v199\n"
    "v_accvgpr_read_b32 v198, a52\n"
    "v_accvgpr_read_b32 v199, a53\n"
    "v_mul_f32 v198, v198, v197\n"
    "v_mul_f32 v199, v199, v197\n"
    "v_accvgpr_write_b32 a52, v198\n"
    "v_accvgpr_write_b32 a53, v199\n"
    "v_accvgpr_read_b32 v198, a54\n"
    "v_accvgpr_read_b32 v199, a55\n"
    "v_mul_f32 v198, v198, v197\n"
    "v_mul_f32 v199, v199, v197\n"
    "v_accvgpr_write_b32 a54, v198\n"
    "v_accvgpr_write_b32 a55, v199\n"
    "v_accvgpr_read_b32 v198, a68\n"
    "v_accvgpr_read_b32 v199, a69\n"
    "v_mul_f32 v198, v198, v197\n"
    "v_mul_f32 v199, v199, v197\n"
    "v_accvgpr_write_b32 a68, v198\n"
    "v_accvgpr_write_b32 a69, v199\n"
    "v_accvgpr_read_b32 v198, a70\n"
    "v_accvgpr_read_b32 v199, a71\n"
    "v_mul_f32 v198, v198, v197\n"
    "v_mul_f32 v199, v199, v197\n"
    "v_accvgpr_write_b32 a70, v198\n"
    "v_accvgpr_write_b32 a71, v199\n"
    "v_accvgpr_read_b32 v198, a84\n"
    "v_accvgpr_read_b32 v199, a85\n"
    "v_mul_f32 v198, v198, v197\n"
    "v_mul_f32 v199, v199, v197\n"
    "v_accvgpr_write_b32 a84, v198\n"
    "v_accvgpr_write_b32 a85, v199\n"
    "v_accvgpr_read_b32 v198, a86\n"
    "v_accvgpr_read_b32 v199, a87\n"
    "v_mul_f32 v198, v198, v197\n"
    "v_mul_f32 v199, v199, v197\n"
    "v_accvgpr_write_b32 a86, v198\n"
    "v_accvgpr_write_b32 a87, v199\n"
    "v_accvgpr_read_b32 v198, a100\n"
    "v_accvgpr_read_b32 v199, a101\n"
    "v_mul_f32 v198, v198, v197\n"
    "v_mul_f32 v199, v199, v197\n"
    "v_accvgpr_write_b32 a100, v198\n"
    "v_accvgpr_write_b32 a101, v199\n"
    "v_accvgpr_read_b32 v198, a102\n"
    "v_accvgpr_read_b32 v199, a103\n"
    "v_mul_f32 v198, v198, v197\n"
    "v_mul_f32 v199, v199, v197\n"
    "v_accvgpr_write_b32 a102, v198\n"
    "v_accvgpr_write_b32 a103, v199\n"
    "v_accvgpr_read_b32 v198, a116\n"
    "v_accvgpr_read_b32 v199, a117\n"
    "v_mul_f32 v198, v198, v197\n"
    "v_mul_f32 v199, v199, v197\n"
    "v_accvgpr_write_b32 a116, v198\n"
    "v_accvgpr_write_b32 a117, v199\n"
    "v_accvgpr_read_b32 v198, a118\n"
    "v_accvgpr_read_b32 v199, a119\n"
    "v_mul_f32 v198, v198, v197\n"
    "v_mul_f32 v199, v199, v197\n"
    "v_accvgpr_write_b32 a118, v198\n"
    "v_accvgpr_write_b32 a119, v199\n"
    "v_accvgpr_read_b32 v198, a132\n"
    "v_accvgpr_read_b32 v199, a133\n"
    "v_mul_f32 v198, v198, v197\n"
    "v_mul_f32 v199, v199, v197\n"
    "v_accvgpr_write_b32 a132, v198\n"
    "v_accvgpr_write_b32 a133, v199\n"
    "v_accvgpr_read_b32 v198, a134\n"
    "v_accvgpr_read_b32 v199, a135\n"
    "v_mul_f32 v198, v198, v197\n"
    "v_mul_f32 v199, v199, v197\n"
    "v_accvgpr_write_b32 a134, v198\n"
    "v_accvgpr_write_b32 a135, v199\n"
    "v_accvgpr_read_b32 v198, a148\n"
    "v_accvgpr_read_b32 v199, a149\n"
    "v_mul_f32 v198, v198, v197\n"
    "v_mul_f32 v199, v199, v197\n"
    "v_accvgpr_write_b32 a148, v198\n"
    "v_accvgpr_write_b32 a149, v199\n"
    "v_accvgpr_read_b32 v198, a150\n"
    "v_accvgpr_read_b32 v199, a151\n"
    "v_mul_f32 v198, v198, v197\n"
    "v_mul_f32 v199, v199, v197\n"
    "v_accvgpr_write_b32 a150, v198\n"
    "v_accvgpr_write_b32 a151, v199\n"
    "v_accvgpr_read_b32 v198, a164\n"
    "v_accvgpr_read_b32 v199, a165\n"
    "v_mul_f32 v198, v198, v197\n"
    "v_mul_f32 v199, v199, v197\n"
    "v_accvgpr_write_b32 a164, v198\n"
    "v_accvgpr_write_b32 a165, v199\n"
    "v_accvgpr_read_b32 v198, a166\n"
    "v_accvgpr_read_b32 v199, a167\n"
    "v_mul_f32 v198, v198, v197\n"
    "v_mul_f32 v199, v199, v197\n"
    "v_accvgpr_write_b32 a166, v198\n"
    "v_accvgpr_write_b32 a167, v199\n"
    "v_accvgpr_read_b32 v198, a180\n"
    "v_accvgpr_read_b32 v199, a181\n"
    "v_mul_f32 v198, v198, v197\n"
    "v_mul_f32 v199, v199, v197\n"
    "v_accvgpr_write_b32 a180, v198\n"
    "v_accvgpr_write_b32 a181, v199\n"
    "v_accvgpr_read_b32 v198, a182\n"
    "v_accvgpr_read_b32 v199, a183\n"
    "v_mul_f32 v198, v198, v197\n"
    "v_mul_f32 v199, v199, v197\n"
    "v_accvgpr_write_b32 a182, v198\n"
    "v_accvgpr_write_b32 a183, v199\n"
    "v_max3_f32 v196, v40, v41, v42\n"
    "v_max3_f32 v196, v196, v43, v56\n"
    "v_max3_f32 v196, v196, v57, v58\n"
    "v_max3_f32 v196, v196, v59, v72\n"
    "v_max3_f32 v196, v196, v73, v74\n"
    "v_max3_f32 v196, v196, v75, v88\n"
    "v_max3_f32 v196, v196, v89, v90\n"
    "v_max_f32 v196, v196, v91\n"
    "v_mov_b32 v197, v196\n"
    "s_nop 1\n"
    "v_permlane32_swap_b32 v196, v197\n"
    "v_max_f32 v196, v196, v197\n"
    "v_mov_b32 v197, v196\n"
    "s_nop 1\n"
    "v_permlane16_swap_b32 v196, v197\n"
    "v_max_f32 v196, v196, v197\n"
    "v_max_f32 v196, v196, v104\n"
    "v_sub_f32 v197, v104, v196\n"
    "v_mov_b32 v104, v196\n"
    "v_exp_f32 v197, v197\n"
    "v_sub_f32 v40, v40, v196\n"
    "v_sub_f32 v41, v41, v196\n"
    "v_sub_f32 v42, v42, v196\n"
    "v_sub_f32 v43, v43, v196\n"
    "v_sub_f32 v56, v56, v196\n"
    "v_sub_f32 v57, v57, v196\n"
    "v_sub_f32 v58, v58, v196\n"
    "v_sub_f32 v59, v59, v196\n"
    "v_sub_f32 v72, v72, v196\n"
    "v_sub_f32 v73, v73, v196\n"
    "v_sub_f32 v74, v74, v196\n"
    "v_sub_f32 v75, v75, v196\n"
    "v_sub_f32 v88, v88, v196\n"
    "v_sub_f32 v89, v89, v196\n"
    "v_sub_f32 v90, v90, v196\n"
    "v_sub_f32 v91, v91, v196\n"
    "v_mul_f32 v120, v120, v197\n"
    "v_mul_f32 v121, v121, v197\n"
    "v_mul_f32 v122, v122, v197\n"
    "v_mul_f32 v123, v123, v197\n"
    "v_accvgpr_read_b32 v198, a8\n"
    "v_accvgpr_read_b32 v199, a9\n"
    "v_mul_f32 v198, v198, v197\n"
    "v_mul_f32 v199, v199, v197\n"
    "v_accvgpr_write_b32 a8, v198\n"
    "v_accvgpr_write_b32 a9, v199\n"
    "v_accvgpr_read_b32 v198, a10\n"
    "v_accvgpr_read_b32 v199, a11\n"
    "v_mul_f32 v198, v198, v197\n"
    "v_mul_f32 v199, v199, v197\n"
    "v_accvgpr_write_b32 a10, v198\n"
    "v_accvgpr_write_b32 a11, v199\n"
    "v_accvgpr_read_b32 v198, a24\n"
    "v_accvgpr_read_b32 v199, a25\n"
    "v_mul_f32 v198, v198, v197\n"
    "v_mul_f32 v199, v199, v197\n"
    "v_accvgpr_write_b32 a24, v198\n"
    "v_accvgpr_write_b32 a25, v199\n"
    "v_accvgpr_read_b32 v198, a26\n"
    "v_accvgpr_read_b32 v199, a27\n"
    "v_mul_f32 v198, v198, v197\n"
    "v_mul_f32 v199, v199, v197\n"
    "v_accvgpr_write_b32 a26, v198\n"
    "v_accvgpr_write_b32 a27, v199\n"
    "v_accvgpr_read_b32 v198, a40\n"
    "v_accvgpr_read_b32 v199, a41\n"
    "v_mul_f32 v198, v198, v197\n"
    "v_mul_f32 v199, v199, v197\n"
    "v_accvgpr_write_b32 a40, v198\n"
    "v_accvgpr_write_b32 a41, v199\n"
    "v_accvgpr_read_b32 v198, a42\n"
    "v_accvgpr_read_b32 v199, a43\n"
    "v_mul_f32 v198, v198, v197\n"
    "v_mul_f32 v199, v199, v197\n"
    "v_accvgpr_write_b32 a42, v198\n"
    "v_accvgpr_write_b32 a43, v199\n"
    "v_accvgpr_read_b32 v198, a56\n"
    "v_accvgpr_read_b32 v199, a57\n"
    "v_mul_f32 v198, v198, v197\n"
    "v_mul_f32 v199, v199, v197\n"
    "v_accvgpr_write_b32 a56, v198\n"
    "v_accvgpr_write_b32 a57, v199\n"
    "v_accvgpr_read_b32 v198, a58\n"
    "v_accvgpr_read_b32 v199, a59\n"
    "v_mul_f32 v198, v198, v197\n"
    "v_mul_f32 v199, v199, v197\n"
    "v_accvgpr_write_b32 a58, v198\n"
    "v_accvgpr_write_b32 a59, v199\n"
    "v_accvgpr_read_b32 v198, a72\n"
    "v_accvgpr_read_b32 v199, a73\n"
    "v_mul_f32 v198, v198, v197\n"
    "v_mul_f32 v199, v199, v197\n"
    "v_accvgpr_write_b32 a72, v198\n"
    "v_accvgpr_write_b32 a73, v199\n"
    "v_accvgpr_read_b32 v198, a74\n"
    "v_accvgpr_read_b32 v199, a75\n"
    "v_mul_f32 v198, v198, v197\n"
    "v_mul_f32 v199, v199, v197\n"
    "v_accvgpr_write_b32 a74, v198\n"
    "v_accvgpr_write_b32 a75, v199\n"
    "v_accvgpr_read_b32 v198, a88\n"
    "v_accvgpr_read_b32 v199, a89\n"
    "v_mul_f32 v198, v198, v197\n"
    "v_mul_f32 v199, v199, v197\n"
    "v_accvgpr_write_b32 a88, v198\n"
    "v_accvgpr_write_b32 a89, v199\n"
    "v_accvgpr_read_b32 v198, a90\n"
    "v_accvgpr_read_b32 v199, a91\n"
    "v_mul_f32 v198, v198, v197\n"
    "v_mul_f32 v199, v199, v197\n"
    "v_accvgpr_write_b32 a90, v198\n"
    "v_accvgpr_write_b32 a91, v199\n"
    "v_accvgpr_read_b32 v198, a104\n"
    "v_accvgpr_read_b32 v199, a105\n"
    "v_mul_f32 v198, v198, v197\n"
    "v_mul_f32 v199, v199, v197\n"
    "v_accvgpr_write_b32 a104, v198\n"
    "v_accvgpr_write_b32 a105, v199\n"
    "v_accvgpr_read_b32 v198, a106\n"
    "v_accvgpr_read_b32 v199, a107\n"
    "v_mul_f32 v198, v198, v197\n"
    "v_mul_f32 v199, v199, v197\n"
    "v_accvgpr_write_b32 a106, v198\n"
    "v_accvgpr_write_b32 a107, v199\n"
    "v_accvgpr_read_b32 v198, a120\n"
    "v_accvgpr_read_b32 v199, a121\n"
    "v_mul_f32 v198, v198, v197\n"
    "v_mul_f32 v199, v199, v197\n"
    "v_accvgpr_write_b32 a120, v198\n"
    "v_accvgpr_write_b32 a121, v199\n"
    "v_accvgpr_read_b32 v198, a122\n"
    "v_accvgpr_read_b32 v199, a123\n"
    "v_mul_f32 v198, v198, v197\n"
    "v_mul_f32 v199, v199, v197\n"
    "v_accvgpr_write_b32 a122, v198\n"
    "v_accvgpr_write_b32 a123, v199\n"
    "v_accvgpr_read_b32 v198, a136\n"
    "v_accvgpr_read_b32 v199, a137\n"
    "v_mul_f32 v198, v198, v197\n"
    "v_mul_f32 v199, v199, v197\n"
    "v_accvgpr_write_b32 a136, v198\n"
    "v_accvgpr_write_b32 a137, v199\n"
    "v_accvgpr_read_b32 v198, a138\n"
    "v_accvgpr_read_b32 v199, a139\n"
    "v_mul_f32 v198, v198, v197\n"
    "v_mul_f32 v199, v199, v197\n"
    "v_accvgpr_write_b32 a138, v198\n"
    "v_accvgpr_write_b32 a139, v199\n"
    "v_accvgpr_read_b32 v198, a152\n"
    "v_accvgpr_read_b32 v199, a153\n"
    "v_mul_f32 v198, v198, v197\n"
    "v_mul_f32 v199, v199, v197\n"
    "v_accvgpr_write_b32 a152, v198\n"
    "v_accvgpr_write_b32 a153, v199\n"
    "v_accvgpr_read_b32 v198, a154\n"
    "v_accvgpr_read_b32 v199, a155\n"
    "v_mul_f32 v198, v198, v197\n"
    "v_mul_f32 v199, v199, v197\n"
    "v_accvgpr_write_b32 a154, v198\n"
    "v_accvgpr_write_b32 a155, v199\n"
    "v_accvgpr_read_b32 v198, a168\n"
    "v_accvgpr_read_b32 v199, a169\n"
    "v_mul_f32 v198, v198, v197\n"
    "v_mul_f32 v199, v199, v197\n"
    "v_accvgpr_write_b32 a168, v198\n"
    "v_accvgpr_write_b32 a169, v199\n"
    "v_accvgpr_read_b32 v198, a170\n"
    "v_accvgpr_read_b32 v199, a171\n"
    "v_mul_f32 v198, v198, v197\n"
    "v_mul_f32 v199, v199, v197\n"
    "v_accvgpr_write_b32 a170, v198\n"
    "v_accvgpr_write_b32 a171, v199\n"
    "v_accvgpr_read_b32 v198, a184\n"
    "v_accvgpr_read_b32 v199, a185\n"
    "v_mul_f32 v198, v198, v197\n"
    "v_mul_f32 v199, v199, v197\n"
    "v_accvgpr_write_b32 a184, v198\n"
    "v_accvgpr_write_b32 a185, v199\n"
    "v_accvgpr_read_b32 v198, a186\n"
    "v_accvgpr_read_b32 v199, a187\n"
    "v_mul_f32 v198, v198, v197\n"
    "v_mul_f32 v199, v199, v197\n"
    "v_accvgpr_write_b32 a186, v198\n"
    "v_accvgpr_write_b32 a187, v199\n"
    "v_max3_f32 v196, v44, v45, v46\n"
    "v_max3_f32 v196, v196, v47, v60\n"
    "v_max3_f32 v196, v196, v61, v62\n"
    "v_max3_f32 v196, v196, v63, v76\n"
    "v_max3_f32 v196, v196, v77, v78\n"
    "v_max3_f32 v196, v196, v79, v92\n"
    "v_max3_f32 v196, v196, v93, v94\n"
    "v_max_f32 v196, v196, v95\n"
    "v_mov_b32 v197, v196\n"
    "s_nop 1\n"
    "v_permlane32_swap_b32 v196, v197\n"
    "v_max_f32 v196, v196, v197\n"
    "v_mov_b32 v197, v196\n"
    "s_nop 1\n"
    "v_permlane16_swap_b32 v196, v197\n"
    "v_max_f32 v196, v196, v197\n"
    "v_max_f32 v196, v196, v108\n"
    "v_sub_f32 v197, v108, v196\n"
    "v_mov_b32 v108, v196\n"
    "v_exp_f32 v197, v197\n"
    "v_sub_f32 v44, v44, v196\n"
    "v_sub_f32 v45, v45, v196\n"
    "v_sub_f32 v46, v46, v196\n"
    "v_sub_f32 v47, v47, v196\n"
    "v_sub_f32 v60, v60, v196\n"
    "v_sub_f32 v61, v61, v196\n"
    "v_sub_f32 v62, v62, v196\n"
    "v_sub_f32 v63, v63, v196\n"
    "v_sub_f32 v76, v76, v196\n"
    "v_sub_f32 v77, v77, v196\n"
    "v_sub_f32 v78, v78, v196\n"
    "v_sub_f32 v79, v79, v196\n"
    "v_sub_f32 v92, v92, v196\n"
    "v_sub_f32 v93, v93, v196\n"
    "v_sub_f32 v94, v94, v196\n"
    "v_sub_f32 v95, v95, v196\n"
    "v_mul_f32 v124, v124, v197\n"
    "v_mul_f32 v125, v125, v197\n"
    "v_mul_f32 v126, v126, v197\n"
    "v_mul_f32 v127, v127, v197\n"
    "v_accvgpr_read_b32 v198, a12\n"
    "v_accvgpr_read_b32 v199, a13\n"
    "v_mul_f32 v198, v198, v197\n"
    "v_mul_f32 v199, v199, v197\n"
    "v_accvgpr_write_b32 a12, v198\n"
    "v_accvgpr_write_b32 a13, v199\n"
    "v_accvgpr_read_b32 v198, a14\n"
    "v_accvgpr_read_b32 v199, a15\n"
    "v_mul_f32 v198, v198, v197\n"
    "v_mul_f32 v199, v199, v197\n"
    "v_accvgpr_write_b32 a14, v198\n"
    "v_accvgpr_write_b32 a15, v199\n"
    "v_accvgpr_read_b32 v198, a28\n"
    "v_accvgpr_read_b32 v199, a29\n"
    "v_mul_f32 v198, v198, v197\n"
    "v_mul_f32 v199, v199, v197\n"
    "v_accvgpr_write_b32 a28, v198\n"
    "v_accvgpr_write_b32 a29, v199\n"
    "v_accvgpr_read_b32 v198, a30\n"
    "v_accvgpr_read_b32 v199, a31\n"
    "v_mul_f32 v198, v198, v197\n"
    "v_mul_f32 v199, v199, v197\n"
    "v_accvgpr_write_b32 a30, v198\n"
    "v_accvgpr_write_b32 a31, v199\n"
    "v_accvgpr_read_b32 v198, a44\n"
    "v_accvgpr_read_b32 v199, a45\n"
    "v_mul_f32 v198, v198, v197\n"
    "v_mul_f32 v199, v199, v197\n"
    "v_accvgpr_write_b32 a44, v198\n"
    "v_accvgpr_write_b32 a45, v199\n"
    "v_accvgpr_read_b32 v198, a46\n"
    "v_accvgpr_read_b32 v199, a47\n"
    "v_mul_f32 v198, v198, v197\n"
    "v_mul_f32 v199, v199, v197\n"
    "v_accvgpr_write_b32 a46, v198\n"
    "v_accvgpr_write_b32 a47, v199\n"
    "v_accvgpr_read_b32 v198, a60\n"
    "v_accvgpr_read_b32 v199, a61\n"
    "v_mul_f32 v198, v198, v197\n"
    "v_mul_f32 v199, v199, v197\n"
    "v_accvgpr_write_b32 a60, v198\n"
    "v_accvgpr_write_b32 a61, v199\n"
    "v_accvgpr_read_b32 v198, a62\n"
    "v_accvgpr_read_b32 v199, a63\n"
    "v_mul_f32 v198, v198, v197\n"
    "v_mul_f32 v199, v199, v197\n"
    "v_accvgpr_write_b32 a62, v198\n"
    "v_accvgpr_write_b32 a63, v199\n"
    "v_accvgpr_read_b32 v198, a76\n"
    "v_accvgpr_read_b32 v199, a77\n"
    "v_mul_f32 v198, v198, v197\n"
    "v_mul_f32 v199, v199, v197\n"
    "v_accvgpr_write_b32 a76, v198\n"
    "v_accvgpr_write_b32 a77, v199\n"
    "v_accvgpr_read_b32 v198, a78\n"
    "v_accvgpr_read_b32 v199, a79\n"
    "v_mul_f32 v198, v198, v197\n"
    "v_mul_f32 v199, v199, v197\n"
    "v_accvgpr_write_b32 a78, v198\n"
    "v_accvgpr_write_b32 a79, v199\n"
    "v_accvgpr_read_b32 v198, a92\n"
    "v_accvgpr_read_b32 v199, a93\n"
    "v_mul_f32 v198, v198, v197\n"
    "v_mul_f32 v199, v199, v197\n"
    "v_accvgpr_write_b32 a92, v198\n"
    "v_accvgpr_write_b32 a93, v199\n"
    "v_accvgpr_read_b32 v198, a94\n"
    "v_accvgpr_read_b32 v199, a95\n"
    "v_mul_f32 v198, v198, v197\n"
    "v_mul_f32 v199, v199, v197\n"
    "v_accvgpr_write_b32 a94, v198\n"
    "v_accvgpr_write_b32 a95, v199\n"
    "v_accvgpr_read_b32 v198, a108\n"
    "v_accvgpr_read_b32 v199, a109\n"
    "v_mul_f32 v198, v198, v197\n"
    "v_mul_f32 v199, v199, v197\n"
    "v_accvgpr_write_b32 a108, v198\n"
    "v_accvgpr_write_b32 a109, v199\n"
    "v_accvgpr_read_b32 v198, a110\n"
    "v_accvgpr_read_b32 v199, a111\n"
    "v_mul_f32 v198, v198, v197\n"
    "v_mul_f32 v199, v199, v197\n"
    "v_accvgpr_write_b32 a110, v198\n"
    "v_accvgpr_write_b32 a111, v199\n"
    "v_accvgpr_read_b32 v198, a124\n"
    "v_accvgpr_read_b32 v199, a125\n"
    "v_mul_f32 v198, v198, v197\n"
    "v_mul_f32 v199, v199, v197\n"
    "v_accvgpr_write_b32 a124, v198\n"
    "v_accvgpr_write_b32 a125, v199\n"
    "v_accvgpr_read_b32 v198, a126\n"
    "v_accvgpr_read_b32 v199, a127\n"
    "v_mul_f32 v198, v198, v197\n"
    "v_mul_f32 v199, v199, v197\n"
    "v_accvgpr_write_b32 a126, v198\n"
    "v_accvgpr_write_b32 a127, v199\n"
    "v_accvgpr_read_b32 v198, a140\n"
    "v_accvgpr_read_b32 v199, a141\n"
    "v_mul_f32 v198, v198, v197\n"
    "v_mul_f32 v199, v199, v197\n"
    "v_accvgpr_write_b32 a140, v198\n"
    "v_accvgpr_write_b32 a141, v199\n"
    "v_accvgpr_read_b32 v198, a142\n"
    "v_accvgpr_read_b32 v199, a143\n"
    "v_mul_f32 v198, v198, v197\n"
    "v_mul_f32 v199, v199, v197\n"
    "v_accvgpr_write_b32 a142, v198\n"
    "v_accvgpr_write_b32 a143, v199\n"
    "v_accvgpr_read_b32 v198, a156\n"
    "v_accvgpr_read_b32 v199, a157\n"
    "v_mul_f32 v198, v198, v197\n"
    "v_mul_f32 v199, v199, v197\n"
    "v_accvgpr_write_b32 a156, v198\n"
    "v_accvgpr_write_b32 a157, v199\n"
    "v_accvgpr_read_b32 v198, a158\n"
    "v_accvgpr_read_b32 v199, a159\n"
    "v_mul_f32 v198, v198, v197\n"
    "v_mul_f32 v199, v199, v197\n"
    "v_accvgpr_write_b32 a158, v198\n"
    "v_accvgpr_write_b32 a159, v199\n"
    "v_accvgpr_read_b32 v198, a172\n"
    "v_accvgpr_read_b32 v199, a173\n"
    "v_mul_f32 v198, v198, v197\n"
    "v_mul_f32 v199, v199, v197\n"
    "v_accvgpr_write_b32 a172, v198\n"
    "v_accvgpr_write_b32 a173, v199\n"
    "v_accvgpr_read_b32 v198, a174\n"
    "v_accvgpr_read_b32 v199, a175\n"
    "v_mul_f32 v198, v198, v197\n"
    "v_mul_f32 v199, v199, v197\n"
    "v_accvgpr_write_b32 a174, v198\n"
    "v_accvgpr_write_b32 a175, v199\n"
    "v_accvgpr_read_b32 v198, a188\n"
    "v_accvgpr_read_b32 v199, a189\n"
    "v_mul_f32 v198, v198, v197\n"
    "v_mul_f32 v199, v199, v197\n"
    "v_accvgpr_write_b32 a188, v198\n"
    "v_accvgpr_write_b32 a189, v199\n"
    "v_accvgpr_read_b32 v198, a190\n"
    "v_accvgpr_read_b32 v199, a191\n"
    "v_mul_f32 v198, v198, v197\n"
    "v_mul_f32 v199, v199, v197\n"
    "v_accvgpr_write_b32 a190, v198\n"
    "v_accvgpr_write_b32 a191, v199\n"
    "v_exp_f32 v32, v32\n"
    "v_exp_f32 v33, v33\n"
    "v_exp_f32 v34, v34\n"
    "v_cvt_pk_bf16_f32 v128, v32, v33\n"
    "v_exp_f32 v35, v35\n"
    "v_exp_f32 v36, v36\n"
    "v_cvt_pk_bf16_f32 v129, v34, v35\n"
    "v_exp_f32 v37, v37\n"
    "v_exp_f32 v38, v38\n"
    "v_cvt_pk_bf16_f32 v132, v36, v37\n"
    "v_exp_f32 v39, v39\n"
    "v_exp_f32 v40, v40\n"
    "v_cvt_pk_bf16_f32 v133, v38, v39\n"
    "v_exp_f32 v41, v41\n"
    "v_exp_f32 v42, v42\n"
    "v_cvt_pk_bf16_f32 v136, v40, v41\n"
    "v_exp_f32 v43, v43\n"
    "v_exp_f32 v44, v44\n"
    "v_cvt_pk_bf16_f32 v137, v42, v43\n"
    "v_exp_f32 v45, v45\n"
    "v_exp_f32 v46, v46\n"
    "v_cvt_pk_bf16_f32 v140, v44, v45\n"
    "v_exp_f32 v47, v47\n"
    "v_exp_f32 v48, v48\n"
    "v_cvt_pk_bf16_f32 v141, v46, v47\n"
    "v_exp_f32 v49, v49\n"
    "v_exp_f32 v50, v50\n"
    "v_cvt_pk_bf16_f32 v130, v48, v49\n"
    "v_exp_f32 v51, v51\n"
    "v_exp_f32 v52, v52\n"
    "v_cvt_pk_bf16_f32 v131, v50, v51\n"
    "v_exp_f32 v53, v53\n"
    "v_exp_f32 v54, v54\n"
    "v_cvt_pk_bf16_f32 v134, v52, v53\n"
    "v_exp_f32 v55, v55\n"
    "v_exp_f32 v56, v56\n"
    "v_cvt_pk_bf16_f32 v135, v54, v55\n"
    "v_exp_f32 v57, v57\n"
    "v_exp_f32 v58, v58\n"
    "v_cvt_pk_bf16_f32 v138, v56, v57\n"
    "v_exp_f32 v59, v59\n"
    "v_exp_f32 v60, v60\n"
    "v_cvt_pk_bf16_f32 v139, v58, v59\n"
    "v_exp_f32 v61, v61\n"
    "v_exp_f32 v62, v62\n"
    "v_cvt_pk_bf16_f32 v142, v60, v61\n"
    "v_exp_f32 v63, v63\n"
    "s_nop 0\n"
    "v_cvt_pk_bf16_f32 v143, v62, v63\n"
    "v_exp_f32 v64, v64\n"
    "v_exp_f32 v65, v65\n"
    "v_exp_f32 v66, v66\n"
    "v_cvt_pk_bf16_f32 v144, v64, v65\n"
    "v_exp_f32 v67, v67\n"
    "v_exp_f32 v68, v68\n"
    "v_cvt_pk_bf16_f32 v145, v66, v67\n"
    "v_exp_f32 v69, v69\n"
    "v_exp_f32 v70, v70\n"
    "v_cvt_pk_bf16_f32 v148, v68, v69\n"
    "v_exp_f32 v71, v71\n"
    "v_exp_f32 v72, v72\n"
    "v_cvt_pk_bf16_f32 v149, v70, v71\n"
    "v_exp_f32 v73, v73\n"
    "v_exp_f32 v74, v74\n"
    "v_cvt_pk_bf16_f32 v152, v72, v73\n"
    "v_exp_f32 v75, v75\n"
    "v_exp_f32 v76, v76\n"
    "v_cvt_pk_bf16_f32 v153, v74, v75\n"
    "v_exp_f32 v77, v77\n"
    "v_exp_f32 v78, v78\n"
    "v_cvt_pk_bf16_f32 v156, v76, v77\n"
    "v_exp_f32 v79, v79\n"
    "v_exp_f32 v80, v80\n"
    "v_cvt_pk_bf16_f32 v157, v78, v79\n"
    "v_exp_f32 v81, v81\n"
    "v_exp_f32 v82, v82\n"
    "v_cvt_pk_bf16_f32 v146, v80, v81\n"
    "v_exp_f32 v83, v83\n"
    "v_exp_f32 v84, v84\n"
    "v_cvt_pk_bf16_f32 v147, v82, v83\n"
    "v_exp_f32 v85, v85\n"
    "v_exp_f32 v86, v86\n"
    "v_cvt_pk_bf16_f32 v150, v84, v85\n"
    "v_exp_f32 v87, v87\n"
    "v_exp_f32 v88, v88\n"
    "v_cvt_pk_bf16_f32 v151, v86, v87\n"
    "v_exp_f32 v89, v89\n"
    "v_exp_f32 v90, v90\n"
    "v_cvt_pk_bf16_f32 v154, v88, v89\n"
    "v_exp_f32 v91, v91\n"
    "v_exp_f32 v92, v92\n"
    "v_cvt_pk_bf16_f32 v155, v90, v91\n"
    "v_exp_f32 v93, v93\n"
    "v_exp_f32 v94, v94\n"
    "v_cvt_pk_bf16_f32 v158, v92, v93\n"
    "v_exp_f32 v95, v95\n"
    "s_nop 0\n"
    "v_cvt_pk_bf16_f32 v159, v94, v95\n"
    "ds_read_b128 v[160:163], v205 offset:12288\n"
    "ds_read_b128 v[164:167], v205 offset:13312\n"
    "s_waitcnt lgkmcnt(1)\n"
    "v_mfma_f32_16x16x32_bf16 a[0:3], v[160:163], v[128:131], a[0:3]\n"
    "s_waitcnt lgkmcnt(0)\n"
    "v_mfma_f32_16x16x32_bf16 a[0:3], v[164:167], v[144:147], a[0:3]\n"
    "v_mfma_f32_16x16x32_bf16 a[4:7], v[160:163], v[132:135], a[4:7]\n"
    "v_mfma_f32_16x16x32_bf16 a[4:7], v[164:167], v[148:151], a[4:7]\n"
    "v_mfma_f32_16x16x32_bf16 a[8:11], v[160:163], v[136:139], a[8:11]\n"
    "v_mfma_f32_16x16x32_bf16 a[8:11], v[164:167], v[152:155], a[8:11]\n"
    "v_mfma_f32_16x16x32_bf16 a[12:15], v[160:163], v[140:143], a[12:15]\n"
    "v_mfma_f32_16x16x32_bf16 a[12:15], v[164:167], v[156:159], a[12:15]\n"
    "ds_read_b128 v[168:171], v205 offset:14336\n"
    "ds_read_b128 v[172:175], v205 offset:15360\n"
    "s_waitcnt lgkmcnt(1)\n"
    "v_mfma_f32_16x16x32_bf16 a[28:31], v[168:171], v[140:143], a[28:31]\n"
    "s_waitcnt lgkmcnt(0)\n"
    "v_mfma_f32_16x16x32_bf16 a[28:31], v[172:175], v[156:159], a[28:31]\n"
    "v_mfma_f32_16x16x32_bf16 a[24:27], v[172:175], v[152:155], a[24:27]\n"
    "v_mfma_f32_16x16x32_bf16 a[24:27], v[168:171], v[136:139], a[24:27]\n"
    "v_mfma_f32_16x16x32_bf16 a[20:23], v[168:171], v[132:135], a[20:23]\n"
    "v_mfma_f32_16x16x32_bf16 a[20:23], v[172:175], v[148:151], a[20:23]\n"
    "v_mfma_f32_16x16x32_bf16 a[16:19], v[172:175], v[144:147], a[16:19]\n"
    "v_mfma_f32_16x16x32_bf16 a[16:19], v[168:171], v[128:131], a[16:19]\n"
    "ds_read_b128 v[176:179], v205 offset:16384\n"
    "ds_read_b128 v[180:183], v205 offset:17408\n"
    "s_waitcnt lgkmcnt(1)\n"
    "v_mfma_f32_16x16x32_bf16 a[32:35], v[176:179], v[128:131], a[32:35]\n"
    "s_waitcnt lgkmcnt(0)\n"
    "v_mfma_f32_16x16x32_bf16 a[32:35], v[180:183], v[144:147], a[32:35]\n"
    "v_mfma_f32_16x16x32_bf16 a[36:39], v[180:183], v[148:151], a[36:39]\n"
    "v_mfma_f32_16x16x32_bf16 a[36:39], v[176:179], v[132:135], a[36:39]\n"
    "v_mfma_f32_16x16x32_bf16 a[40:43], v[176:179], v[136:139], a[40:43]\n"
    "v_mfma_f32_16x16x32_bf16 a[40:43], v[180:183], v[152:155], a[40:43]\n"
    "v_mfma_f32_16x16x32_bf16 a[44:47], v[180:183], v[156:159], a[44:47]\n"
    "v_mfma_f32_16x16x32_bf16 a[44:47], v[176:179], v[140:143], a[44:47]\n"
    "ds_read_b128 v[160:163], v205 offset:18432\n"
    "ds_read_b128 v[164:167], v205 offset:19456\n"
    "s_waitcnt lgkmcnt(1)\n"
    "v_mfma_f32_16x16x32_bf16 a[60:63], v[160:163], v[140:143], a[60:63]\n"
    "s_waitcnt lgkmcnt(0)\n"
    "v_mfma_f32_16x16x32_bf16 a[60:63], v[164:167], v[156:159], a[60:63]\n"
    "v_mfma_f32_16x16x32_bf16 a[56:59], v[164:167], v[152:155], a[56:59]\n"
    "v_mfma_f32_16x16x32_bf16 a[56:59], v[160:163], v[136:139], a[56:59]\n"
    "v_mfma_f32_16x16x32_bf16 a[52:55], v[160:163], v[132:135], a[52:55]\n"
    "v_mfma_f32_16x16x32_bf16 a[52:55], v[164:167], v[148:151], a[52:55]\n"
    "v_mfma_f32_16x16x32_bf16 a[48:51], v[164:167], v[144:147], a[48:51]\n"
    "v_mfma_f32_16x16x32_bf16 a[48:51], v[160:163], v[128:131], a[48:51]\n"
    "ds_read_b128 v[168:171], v205 offset:20480\n"
    "ds_read_b128 v[172:175], v205 offset:21504\n"
    "s_waitcnt lgkmcnt(1)\n"
    "v_mfma_f32_16x16x32_bf16 a[64:67], v[168:171], v[128:131], a[64:67]\n"
    "s_waitcnt lgkmcnt(0)\n"
    "v_mfma_f32_16x16x32_bf16 a[64:67], v[172:175], v[144:147], a[64:67]\n"
    "v_mfma_f32_16x16x32_bf16 a[68:71], v[172:175], v[148:151], a[68:71]\n"
    "v_mfma_f32_16x16x32_bf16 a[68:71], v[168:171], v[132:135], a[68:71]\n"
    "v_mfma_f32_16x16x32_bf16 a[72:75], v[168:171], v[136:139], a[72:75]\n"
    "v_mfma_f32_16x16x32_bf16 a[72:75], v[172:175], v[152:155], a[72:75]\n"
    "v_mfma_f32_16x16x32_bf16 a[76:79], v[172:175], v[156:159], a[76:79]\n"
    "v_mfma_f32_16x16x32_bf16 a[76:79], v[168:171], v[140:143], a[76:79]\n"
    "ds_read_b128 v[176:179], v205 offset:22528\n"
    "ds_read_b128 v[180:183], v205 offset:23552\n"
    "s_waitcnt lgkmcnt(1)\n"
    "v_mfma_f32_16x16x32_bf16 a[92:95], v[176:179], v[140:143], a[92:95]\n"
    "s_waitcnt lgkmcnt(0)\n"
    "v_mfma_f32_16x16x32_bf16 a[92:95], v[180:183], v[156:159], a[92:95]\n"
    "v_mfma_f32_16x16x32_bf16 a[88:91], v[180:183], v[152:155], a[88:91]\n"
    "v_mfma_f32_16x16x32_bf16 a[88:91], v[176:179], v[136:139], a[88:91]\n"
    "v_mfma_f32_16x16x32_bf16 a[84:87], v[176:179], v[132:135], a[84:87]\n"
    "v_mfma_f32_16x16x32_bf16 a[84:87], v[180:183], v[148:151], a[84:87]\n"
    "v_mfma_f32_16x16x32_bf16 a[80:83], v[180:183], v[144:147], a[80:83]\n"
    "v_mfma_f32_16x16x32_bf16 a[80:83], v[176:179], v[128:131], a[80:83]\n"
    "ds_read_b128 v[160:163], v205 offset:24576\n"
    "ds_read_b128 v[164:167], v205 offset:25600\n"
    "s_waitcnt lgkmcnt(1)\n"
    "v_mfma_f32_16x16x32_bf16 a[96:99], v[160:163], v[128:131], a[96:99]\n"
    "s_waitcnt lgkmcnt(0)\n"
    "v_mfma_f32_16x16x32_bf16 a[96:99], v[164:167], v[144:147], a[96:99]\n"
    "v_mfma_f32_16x16x32_bf16 a[100:103], v[164:167], v[148:151], a[100:103]\n"
    "v_mfma_f32_16x16x32_bf16 a[100:103], v[160:163], v[132:135], a[100:103]\n"
    "v_mfma_f32_16x16x32_bf16 a[104:107], v[160:163], v[136:139], a[104:107]\n"
    "v_mfma_f32_16x16x32_bf16 a[104:107], v[164:167], v[152:155], a[104:107]\n"
    "v_mfma_f32_16x16x32_bf16 a[108:111], v[164:167], v[156:159], a[108:111]\n"
    "v_mfma_f32_16x16x32_bf16 a[108:111], v[160:163], v[140:143], a[108:111]\n"
    "ds_read_b128 v[168:171], v205 offset:26624\n"
    "ds_read_b128 v[172:175], v205 offset:27648\n"
    "s_waitcnt lgkmcnt(1)\n"
    "v_mfma_f32_16x16x32_bf16 a[124:127], v[168:171], v[140:143], a[124:127]\n"
    "s_waitcnt lgkmcnt(0)\n"
    "v_mfma_f32_16x16x32_bf16 a[124:127], v[172:175], v[156:159], a[124:127]\n"
    "v_mfma_f32_16x16x32_bf16 a[120:123], v[172:175], v[152:155], a[120:123]\n"
    "v_mfma_f32_16x16x32_bf16 a[120:123], v[168:171], v[136:139], a[120:123]\n"
    "v_mfma_f32_16x16x32_bf16 a[116:119], v[168:171], v[132:135], a[116:119]\n"
    "v_mfma_f32_16x16x32_bf16 a[116:119], v[172:175], v[148:151], a[116:119]\n"
    "v_mfma_f32_16x16x32_bf16 a[112:115], v[172:175], v[144:147], a[112:115]\n"
    "v_mfma_f32_16x16x32_bf16 a[112:115], v[168:171], v[128:131], a[112:115]\n"
    "ds_read_b128 v[176:179], v205 offset:28672\n"
    "ds_read_b128 v[180:183], v205 offset:29696\n"
    "s_waitcnt lgkmcnt(1)\n"
    "v_mfma_f32_16x16x32_bf16 a[128:131], v[176:179], v[128:131], a[128:131]\n"
    "s_waitcnt lgkmcnt(0)\n"
    "v_mfma_f32_16x16x32_bf16 a[128:131], v[180:183], v[144:147], a[128:131]\n"
    "v_mfma_f32_16x16x32_bf16 a[132:135], v[180:183], v[148:151], a[132:135]\n"
    "v_mfma_f32_16x16x32_bf16 a[132:135], v[176:179], v[132:135], a[132:135]\n"
    "v_mfma_f32_16x16x32_bf16 a[136:139], v[176:179], v[136:139], a[136:139]\n"
    "v_mfma_f32_16x16x32_bf16 a[136:139], v[180:183], v[152:155], a[136:139]\n"
    "v_mfma_f32_16x16x32_bf16 a[140:143], v[180:183], v[156:159], a[140:143]\n"
    "v_mfma_f32_16x16x32_bf16 a[140:143], v[176:179], v[140:143], a[140:143]\n"
    "ds_read_b128 v[160:163], v205 offset:30720\n"
    "ds_read_b128 v[164:167], v205 offset:31744\n"
    "s_waitcnt lgkmcnt(1)\n"
    "v_mfma_f32_16x16x32_bf16 a[156:159], v[160:163], v[140:143], a[156:159]\n"
    "s_waitcnt lgkmcnt(0)\n"
    "v_mfma_f32_16x16x32_bf16 a[156:159], v[164:167], v[156:159], a[156:159]\n"
    "v_mfma_f32_16x16x32_bf16 a[152:155], v[164:167], v[152:155], a[152:155]\n"
    "v_mfma_f32_16x16x32_bf16 a[152:155], v[160:163], v[136:139], a[152:155]\n"
    "v_mfma_f32_16x16x32_bf16 a[148:151], v[160:163], v[132:135], a[148:151]\n"
    "v_mfma_f32_16x16x32_bf16 a[148:151], v[164:167], v[148:151], a[148:151]\n"
    "v_mfma_f32_16x16x32_bf16 a[144:147], v[164:167], v[144:147], a[144:147]\n"
    "v_mfma_f32_16x16x32_bf16 a[144:147], v[160:163], v[128:131], a[144:147]\n"
    "ds_read_b128 v[168:171], v205 offset:32768\n"
    "ds_read_b128 v[172:175], v205 offset:33792\n"
    "s_waitcnt lgkmcnt(1)\n"
    "v_mfma_f32_16x16x32_bf16 a[160:163], v[168:171], v[128:131], a[160:163]\n"
    "s_waitcnt lgkmcnt(0)\n"
    "v_mfma_f32_16x16x32_bf16 a[160:163], v[172:175], v[144:147], a[160:163]\n"
    "v_mfma_f32_16x16x32_bf16 a[164:167], v[172:175], v[148:151], a[164:167]\n"
    "v_mfma_f32_16x16x32_bf16 a[164:167], v[168:171], v[132:135], a[164:167]\n"
    "v_mfma_f32_16x16x32_bf16 a[168:171], v[168:171], v[136:139], a[168:171]\n"
    "v_mfma_f32_16x16x32_bf16 a[168:171], v[172:175], v[152:155], a[168:171]\n"
    "v_mfma_f32_16x16x32_bf16 a[172:175], v[172:175], v[156:159], a[172:175]\n"
    "v_mfma_f32_16x16x32_bf16 a[172:175], v[168:171], v[140:143], a[172:175]\n"
    "ds_read_b128 v[176:179], v205 offset:34816\n"
    "ds_read_b128 v[180:183], v205 offset:35840\n"
    "s_waitcnt lgkmcnt(1)\n"
    "v_mfma_f32_16x16x32_bf16 a[188:191], v[176:179], v[140:143], a[188:191]\n"
    "s_waitcnt lgkmcnt(0)\n"
    "v_mfma_f32_16x16x32_bf16 a[188:191], v[180:183], v[156:159], a[188:191]\n"
    "v_mfma_f32_16x16x32_bf16 a[184:187], v[176:179], v[136:139], a[184:187]\n"
    "v_mfma_f32_16x16x32_bf16 a[184:187], v[180:183], v[152:155], a[184:187]\n"
    "v_mfma_f32_16x16x32_bf16 a[180:183], v[176:179], v[132:135], a[180:183]\n"
    "v_mfma_f32_16x16x32_bf16 a[180:183], v[180:183], v[148:151], a[180:183]\n"
    "v_mfma_f32_16x16x32_bf16 a[176:179], v[176:179], v[128:131], a[176:179]\n"
    "v_mfma_f32_16x16x32_bf16 a[176:179], v[180:183], v[144:147], a[176:179]\n"
    "v_mfma_f32_16x16x32_bf16 v[112:115], v[192:195], v[128:131], v[112:115]\n"
    "v_mfma_f32_16x16x32_bf16 v[112:115], v[192:195], v[144:147], v[112:115]\n"
    "v_mfma_f32_16x16x32_bf16 v[116:119], v[192:195], v[132:135], v[116:119]\n"
    "v_mfma_f32_16x16x32_bf16 v[116:119], v[192:195], v[148:151], v[116:119]\n"
    "v_mfma_f32_16x16x32_bf16 v[120:123], v[192:195], v[136:139], v[120:123]\n"
    "v_mfma_f32_16x16x32_bf16 v[120:123], v[192:195], v[152:155], v[120:123]\n"
    "v_mfma_f32_16x16x32_bf16 v[124:127], v[192:195], v[140:143], v[124:127]\n"
    "v_mfma_f32_16x16x32_bf16 v[124:127], v[192:195], v[156:159], v[124:127]\n"
    "s_nop 15\n"
    "s_add_u32 s63, s63, 0x6000\n"
    "s_cmp_lt_u32 s63, 0xc0000\n"
    "s_cbranch_scc1 LSLOOP\n"
    "s_branch LEPI\n"
    "LEPI:\n"
    "s_lshl_b32 s76, s56, 3\n"
    "s_add_u32 s76, s76, s57\n"
    "s_mul_i32 s76, s76, 0x18000\n"
    "s_add_u32 s76, s76, s88\n"
    "s_add_u32 s28, s24, s76\n"
    "s_addc_u32 s29, s25, 0\n"
    "s_and_b32 s29, s29, 0xffff\n"
    "s_mov_b32 s30, 0x6000\n"
    "s_mov_b32 s31, 0x20000\n"
    "s_xor_b32 s94, s70, 0x80000000\n"
    "s_xor_b32 s95, s70, 0x80000000\n"
    "s_mov_b32 s62, 0x1800\n"
    "s_mov_b32 s63, 0x3000\n"
    "s_mov_b32 s64, 0x4800\n"
    "v_and_b32 v198, 15, v231\n"
    "v_mul_u32_u24 v205, 0x180, v198\n"
    "v_lshrrev_b32 v199, 4, v231\n"
    "v_lshl_add_u32 v205, v199, 4, v205\n"
    "v_mul_f32 v222, s68, v112\n"
    "v_rcp_f32 v222, v222\n"
    "v_mul_f32 v224, s68, v116\n"
    "v_rcp_f32 v224, v224\n"
    "v_mul_f32 v226, s68, v120\n"
    "v_rcp_f32 v226, v226\n"
    "v_mul_f32 v228, s68, v124\n"
    "v_rcp_f32 v228, v228\n"
    "v_mov_b32 v223, v222\n"
    "v_mov_b32 v225, v224\n"
    "v_mov_b32 v227, v226\n"
    "v_mov_b32 v229, v228\n"
    "v_accvgpr_read_b32 v96, a0\n"
    "v_accvgpr_read_b32 v97, a1\n"
    "v_accvgpr_read_b32 v98, a2\n"
    "v_accvgpr_read_b32 v99, a3\n"
    "v_pk_fma_f32 v[96:97], v[96:97], v[222:223], s[94:95]\n"
    "v_pk_fma_f32 v[98:99], v[98:99], v[222:223], s[94:95]\n"
    "v_cvt_pk_bf16_f32 v32, v96, v97\n"
    "v_cvt_pk_bf16_f32 v33, v98, v99\n"
    "v_accvgpr_read_b32 v100, a16\n"
    "v_accvgpr_read_b32 v101, a17\n"
    "v_accvgpr_read_b32 v102, a18\n"
    "v_accvgpr_read_b32 v103, a19\n"
    "v_pk_fma_f32 v[100:101], v[100:101], v[222:223], s[94:95]\n"
    "v_pk_fma_f32 v[102:103], v[102:103], v[222:223], s[94:95]\n"
    "v_cvt_pk_bf16_f32 v34, v100, v101\n"
    "v_cvt_pk_bf16_f32 v35, v102, v103\n"
    "buffer_store_dwordx4 v[32:35], v205, s[28:31], 0 offen sc0 sc1\n"
    "v_accvgpr_read_b32 v104, a32\n"
    "v_accvgpr_read_b32 v105, a33\n"
    "v_accvgpr_read_b32 v106, a34\n"
    "v_accvgpr_read_b32 v107, a35\n"
    "v_pk_fma_f32 v[104:105], v[104:105], v[222:223], s[94:95]\n"
    "v_pk_fma_f32 v[106:107], v[106:107], v[222:223], s[94:95]\n"
    "v_cvt_pk_bf16_f32 v36, v104, v105\n"
    "v_cvt_pk_bf16_f32 v37, v106, v107\n"
    "v_accvgpr_read_b32 v108, a48\n"
    "v_accvgpr_read_b32 v109, a49\n"
    "v_accvgpr_read_b32 v110, a50\n"
    "v_accvgpr_read_b32 v111, a51\n"
    "v_pk_fma_f32 v[108:109], v[108:109], v[222:223], s[94:95]\n"
    "v_pk_fma_f32 v[110:111], v[110:111], v[222:223], s[94:95]\n"
    "v_cvt_pk_bf16_f32 v38, v108, v109\n"
    "v_cvt_pk_bf16_f32 v39, v110, v111\n"
    "buffer_store_dwordx4 v[36:39], v205, s[28:31], 0 offen offset:64 sc0 sc1\n"
    "v_accvgpr_read_b32 v96, a64\n"
    "v_accvgpr_read_b32 v97, a65\n"
    "v_accvgpr_read_b32 v98, a66\n"
    "v_accvgpr_read_b32 v99, a67\n"
    "v_pk_fma_f32 v[96:97], v[96:97], v[222:223], s[94:95]\n"
    "v_pk_fma_f32 v[98:99], v[98:99], v[222:223], s[94:95]\n"
    "v_cvt_pk_bf16_f32 v40, v96, v97\n"
    "v_cvt_pk_bf16_f32 v41, v98, v99\n"
    "v_accvgpr_read_b32 v100, a80\n"
    "v_accvgpr_read_b32 v101, a81\n"
    "v_accvgpr_read_b32 v102, a82\n"
    "v_accvgpr_read_b32 v103, a83\n"
    "v_pk_fma_f32 v[100:101], v[100:101], v[222:223], s[94:95]\n"
    "v_pk_fma_f32 v[102:103], v[102:103], v[222:223], s[94:95]\n"
    "v_cvt_pk_bf16_f32 v42, v100, v101\n"
    "v_cvt_pk_bf16_f32 v43, v102, v103\n"
    "buffer_store_dwordx4 v[40:43], v205, s[28:31], 0 offen offset:128 sc0 sc1\n"
    "v_accvgpr_read_b32 v104, a96\n"
    "v_accvgpr_read_b32 v105, a97\n"
    "v_accvgpr_read_b32 v106, a98\n"
    "v_accvgpr_read_b32 v107, a99\n"
    "v_pk_fma_f32 v[104:105], v[104:105], v[222:223], s[94:95]\n"
    "v_pk_fma_f32 v[106:107], v[106:107], v[222:223], s[94:95]\n"
    "v_cvt_pk_bf16_f32 v44, v104, v105\n"
    "v_cvt_pk_bf16_f32 v45, v106, v107\n"
    "v_accvgpr_read_b32 v108, a112\n"
    "v_accvgpr_read_b32 v109, a113\n"
    "v_accvgpr_read_b32 v110, a114\n"
    "v_accvgpr_read_b32 v111, a115\n"
    "v_pk_fma_f32 v[108:109], v[108:109], v[222:223], s[94:95]\n"
    "v_pk_fma_f32 v[110:111], v[110:111], v[222:223], s[94:95]\n"
    "v_cvt_pk_bf16_f32 v46, v108, v109\n"
    "v_cvt_pk_bf16_f32 v47, v110, v111\n"
    "buffer_store_dwordx4 v[44:47], v205, s[28:31], 0 offen offset:192 sc0 sc1\n"
    "v_accvgpr_read_b32 v96, a128\n"
    "v_accvgpr_read_b32 v97, a129\n"
    "v_accvgpr_read_b32 v98, a130\n"
    "v_accvgpr_read_b32 v99, a131\n"
    "v_pk_fma_f32 v[96:97], v[96:97], v[222:223], s[94:95]\n"
    "v_pk_fma_f32 v[98:99], v[98:99], v[222:223], s[94:95]\n"
    "v_cvt_pk_bf16_f32 v48, v96, v97\n"
    "v_cvt_pk_bf16_f32 v49, v98, v99\n"
    "v_accvgpr_read_b32 v100, a144\n"
    "v_accvgpr_read_b32 v101, a145\n"
    "v_accvgpr_read_b32 v102, a146\n"
    "v_accvgpr_read_b32 v103, a147\n"
    "v_pk_fma_f32 v[100:101], v[100:101], v[222:223], s[94:95]\n"
    "v_pk_fma_f32 v[102:103], v[102:103], v[222:223], s[94:95]\n"
    "v_cvt_pk_bf16_f32 v50, v100, v101\n"
    "v_cvt_pk_bf16_f32 v51, v102, v103\n"
    "buffer_store_dwordx4 v[48:51], v205, s[28:31], 0 offen offset:256 sc0 sc1\n"
    "v_accvgpr_read_b32 v104, a160\n"
    "v_accvgpr_read_b32 v105, a161\n"
    "v_accvgpr_read_b32 v106, a162\n"
    "v_accvgpr_read_b32 v107, a163\n"
    "v_pk_fma_f32 v[104:105], v[104:105], v[222:223], s[94:95]\n"
    "v_pk_fma_f32 v[106:107], v[106:107], v[222:223], s[94:95]\n"
    "v_cvt_pk_bf16_f32 v52, v104, v105\n"
    "v_cvt_pk_bf16_f32 v53, v106, v107\n"
    "v_accvgpr_read_b32 v108, a176\n"
    "v_accvgpr_read_b32 v109, a177\n"
    "v_accvgpr_read_b32 v110, a178\n"
    "v_accvgpr_read_b32 v111, a179\n"
    "v_pk_fma_f32 v[108:109], v[108:109], v[222:223], s[94:95]\n"
    "v_pk_fma_f32 v[110:111], v[110:111], v[222:223], s[94:95]\n"
    "v_cvt_pk_bf16_f32 v54, v108, v109\n"
    "v_cvt_pk_bf16_f32 v55, v110, v111\n"
    "buffer_store_dwordx4 v[52:55], v205, s[28:31], 0 offen offset:320 sc0 sc1\n"
    "v_accvgpr_read_b32 v96, a4\n"
    "v_accvgpr_read_b32 v97, a5\n"
    "v_accvgpr_read_b32 v98, a6\n"
    "v_accvgpr_read_b32 v99, a7\n"
    "v_pk_fma_f32 v[96:97], v[96:97], v[224:225], s[94:95]\n"
    "v_pk_fma_f32 v[98:99], v[98:99], v[224:225], s[94:95]\n"
    "v_cvt_pk_bf16_f32 v56, v96, v97\n"
    "v_cvt_pk_bf16_f32 v57, v98, v99\n"
    "v_accvgpr_read_b32 v100, a20\n"
    "v_accvgpr_read_b32 v101, a21\n"
    "v_accvgpr_read_b32 v102, a22\n"
    "v_accvgpr_read_b32 v103, a23\n"
    "v_pk_fma_f32 v[100:101], v[100:101], v[224:225], s[94:95]\n"
    "v_pk_fma_f32 v[102:103], v[102:103], v[224:225], s[94:95]\n"
    "v_cvt_pk_bf16_f32 v58, v100, v101\n"
    "v_cvt_pk_bf16_f32 v59, v102, v103\n"
    "buffer_store_dwordx4 v[56:59], v205, s[28:31], s62 offen sc0 sc1\n"
    "v_accvgpr_read_b32 v104, a36\n"
    "v_accvgpr_read_b32 v105, a37\n"
    "v_accvgpr_read_b32 v106, a38\n"
    "v_accvgpr_read_b32 v107, a39\n"
    "v_pk_fma_f32 v[104:105], v[104:105], v[224:225], s[94:95]\n"
    "v_pk_fma_f32 v[106:107], v[106:107], v[224:225], s[94:95]\n"
    "v_cvt_pk_bf16_f32 v60, v104, v105\n"
    "v_cvt_pk_bf16_f32 v61, v106, v107\n"
    "v_accvgpr_read_b32 v108, a52\n"
    "v_accvgpr_read_b32 v109, a53\n"
    "v_accvgpr_read_b32 v110, a54\n"
    "v_accvgpr_read_b32 v111, a55\n"
    "v_pk_fma_f32 v[108:109], v[108:109], v[224:225], s[94:95]\n"
    "v_pk_fma_f32 v[110:111], v[110:111], v[224:225], s[94:95]\n"
    "v_cvt_pk_bf16_f32 v62, v108, v109\n"
    "v_cvt_pk_bf16_f32 v63, v110, v111\n"
    "buffer_store_dwordx4 v[60:63], v205, s[28:31], s62 offen offset:64 sc0 sc1\n"
    "v_accvgpr_read_b32 v96, a68\n"
    "v_accvgpr_read_b32 v97, a69\n"
    "v_accvgpr_read_b32 v98, a70\n"
    "v_accvgpr_read_b32 v99, a71\n"
    "v_pk_fma_f32 v[96:97], v[96:97], v[224:225], s[94:95]\n"
    "v_pk_fma_f32 v[98:99], v[98:99], v[224:225], s[94:95]\n"
    "v_cvt_pk_bf16_f32 v32, v96, v97\n"
    "v_cvt_pk_bf16_f32 v33, v98, v99\n"
    "v_accvgpr_read_b32 v100, a84\n"
    "v_accvgpr_read_b32 v101, a85\n"
    "v_accvgpr_read_b32 v102, a86\n"
    "v_accvgpr_read_b32 v103, a87\n"
    "v_pk_fma_f32 v[100:101], v[100:101], v[224:225], s[94:95]\n"
    "v_pk_fma_f32 v[102:103], v[102:103], v[224:225], s[94:95]\n"
    "v_cvt_pk_bf16_f32 v34, v100, v101\n"
    "v_cvt_pk_bf16_f32 v35, v102, v103\n"
    "buffer_store_dwordx4 v[32:35], v205, s[28:31], s62 offen offset:128 sc0 sc1\n"
    "v_accvgpr_read_b32 v104, a100\n"
    "v_accvgpr_read_b32 v105, a101\n"
    "v_accvgpr_read_b32 v106, a102\n"
    "v_accvgpr_read_b32 v107, a103\n"
    "v_pk_fma_f32 v[104:105], v[104:105], v[224:225], s[94:95]\n"
    "v_pk_fma_f32 v[106:107], v[106:107], v[224:225], s[94:95]\n"
    "v_cvt_pk_bf16_f32 v36, v104, v105\n"
    "v_cvt_pk_bf16_f32 v37, v106, v107\n"
    "v_accvgpr_read_b32 v108, a116\n"
    "v_accvgpr_read_b32 v109, a117\n"
    "v_accvgpr_read_b32 v110, a118\n"
    "v_accvgpr_read_b32 v111, a119\n"
    "v_pk_fma_f32 v[108:109], v[108:109], v[224:225], s[94:95]\n"
    "v_pk_fma_f32 v[110:111], v[110:111], v[224:225], s[94:95]\n"
    "v_cvt_pk_bf16_f32 v38, v108, v109\n"
    "v_cvt_pk_bf16_f32 v39, v110, v111\n"
    "buffer_store_dwordx4 v[36:39], v205, s[28:31], s62 offen offset:192 sc0 sc1\n"
    "v_accvgpr_read_b32 v96, a132\n"
    "v_accvgpr_read_b32 v97, a133\n"
    "v_accvgpr_read_b32 v98, a134\n"
    "v_accvgpr_read_b32 v99, a135\n"
    "v_pk_fma_f32 v[96:97], v[96:97], v[224:225], s[94:95]\n"
    "v_pk_fma_f32 v[98:99], v[98:99], v[224:225], s[94:95]\n"
    "v_cvt_pk_bf16_f32 v40, v96, v97\n"
    "v_cvt_pk_bf16_f32 v41, v98, v99\n"
    "v_accvgpr_read_b32 v100, a148\n"
    "v_accvgpr_read_b32 v101, a149\n"
    "v_accvgpr_read_b32 v102, a150\n"
    "v_accvgpr_read_b32 v103, a151\n"
    "v_pk_fma_f32 v[100:101], v[100:101], v[224:225], s[94:95]\n"
    "v_pk_fma_f32 v[102:103], v[102:103], v[224:225], s[94:95]\n"
    "v_cvt_pk_bf16_f32 v42, v100, v101\n"
    "v_cvt_pk_bf16_f32 v43, v102, v103\n"
    "buffer_store_dwordx4 v[40:43], v205, s[28:31], s62 offen offset:256 sc0 sc1\n"
    "v_accvgpr_read_b32 v104, a164\n"
    "v_accvgpr_read_b32 v105, a165\n"
    "v_accvgpr_read_b32 v106, a166\n"
    "v_accvgpr_read_b32 v107, a167\n"
    "v_pk_fma_f32 v[104:105], v[104:105], v[224:225], s[94:95]\n"
    "v_pk_fma_f32 v[106:107], v[106:107], v[224:225], s[94:95]\n"
    "v_cvt_pk_bf16_f32 v44, v104, v105\n"
    "v_cvt_pk_bf16_f32 v45, v106, v107\n"
    "v_accvgpr_read_b32 v108, a180\n"
    "v_accvgpr_read_b32 v109, a181\n"
    "v_accvgpr_read_b32 v110, a182\n"
    "v_accvgpr_read_b32 v111, a183\n"
    "v_pk_fma_f32 v[108:109], v[108:109], v[224:225], s[94:95]\n"
    "v_pk_fma_f32 v[110:111], v[110:111], v[224:225], s[94:95]\n"
    "v_cvt_pk_bf16_f32 v46, v108, v109\n"
    "v_cvt_pk_bf16_f32 v47, v110, v111\n"
    "buffer_store_dwordx4 v[44:47], v205, s[28:31], s62 offen offset:320 sc0 sc1\n"
    "v_accvgpr_read_b32 v96, a8\n"
    "v_accvgpr_read_b32 v97, a9\n"
    "v_accvgpr_read_b32 v98, a10\n"
    "v_accvgpr_read_b32 v99, a11\n"
    "v_pk_fma_f32 v[96:97], v[96:97], v[226:227], s[94:95]\n"
    "v_pk_fma_f32 v[98:99], v[98:99], v[226:227], s[94:95]\n"
    "v_cvt_pk_bf16_f32 v48, v96, v97\n"
    "v_cvt_pk_bf16_f32 v49, v98, v99\n"
    "v_accvgpr_read_b32 v100, a24\n"
    "v_accvgpr_read_b32 v101, a25\n"
    "v_accvgpr_read_b32 v102, a26\n"
    "v_accvgpr_read_b32 v103, a27\n"
    "v_pk_fma_f32 v[100:101], v[100:101], v[226:227], s[94:95]\n"
    "v_pk_fma_f32 v[102:103], v[102:103], v[226:227], s[94:95]\n"
    "v_cvt_pk_bf16_f32 v50, v100, v101\n"
    "v_cvt_pk_bf16_f32 v51, v102, v103\n"
    "buffer_store_dwordx4 v[48:51], v205, s[28:31], s63 offen sc0 sc1\n"
    "v_accvgpr_read_b32 v104, a40\n"
    "v_accvgpr_read_b32 v105, a41\n"
    "v_accvgpr_read_b32 v106, a42\n"
    "v_accvgpr_read_b32 v107, a43\n"
    "v_pk_fma_f32 v[104:105], v[104:105], v[226:227], s[94:95]\n"
    "v_pk_fma_f32 v[106:107], v[106:107], v[226:227], s[94:95]\n"
    "v_cvt_pk_bf16_f32 v52, v104, v105\n"
    "v_cvt_pk_bf16_f32 v53, v106, v107\n"
    "v_accvgpr_read_b32 v108, a56\n"
    "v_accvgpr_read_b32 v109, a57\n"
    "v_accvgpr_read_b32 v110, a58\n"
    "v_accvgpr_read_b32 v111, a59\n"
    "v_pk_fma_f32 v[108:109], v[108:109], v[226:227], s[94:95]\n"
    "v_pk_fma_f32 v[110:111], v[110:111], v[226:227], s[94:95]\n"
    "v_cvt_pk_bf16_f32 v54, v108, v109\n"
    "v_cvt_pk_bf16_f32 v55, v110, v111\n"
    "buffer_store_dwordx4 v[52:55], v205, s[28:31], s63 offen offset:64 sc0 sc1\n"
    "v_accvgpr_read_b32 v96, a72\n"
    "v_accvgpr_read_b32 v97, a73\n"
    "v_accvgpr_read_b32 v98, a74\n"
    "v_accvgpr_read_b32 v99, a75\n"
    "v_pk_fma_f32 v[96:97], v[96:97], v[226:227], s[94:95]\n"
    "v_pk_fma_f32 v[98:99], v[98:99], v[226:227], s[94:95]\n"
    "v_cvt_pk_bf16_f32 v56, v96, v97\n"
    "v_cvt_pk_bf16_f32 v57, v98, v99\n"
    "v_accvgpr_read_b32 v100, a88\n"
    "v_accvgpr_read_b32 v101, a89\n"
    "v_accvgpr_read_b32 v102, a90\n"
    "v_accvgpr_read_b32 v103, a91\n"
    "v_pk_fma_f32 v[100:101], v[100:101], v[226:227], s[94:95]\n"
    "v_pk_fma_f32 v[102:103], v[102:103], v[226:227], s[94:95]\n"
    "v_cvt_pk_bf16_f32 v58, v100, v101\n"
    "v_cvt_pk_bf16_f32 v59, v102, v103\n"
    "buffer_store_dwordx4 v[56:59], v205, s[28:31], s63 offen offset:128 sc0 sc1\n"
    "v_accvgpr_read_b32 v104, a104\n"
    "v_accvgpr_read_b32 v105, a105\n"
    "v_accvgpr_read_b32 v106, a106\n"
    "v_accvgpr_read_b32 v107, a107\n"
    "v_pk_fma_f32 v[104:105], v[104:105], v[226:227], s[94:95]\n"
    "v_pk_fma_f32 v[106:107], v[106:107], v[226:227], s[94:95]\n"
    "v_cvt_pk_bf16_f32 v60, v104, v105\n"
    "v_cvt_pk_bf16_f32 v61, v106, v107\n"
    "v_accvgpr_read_b32 v108, a120\n"
    "v_accvgpr_read_b32 v109, a121\n"
    "v_accvgpr_read_b32 v110, a122\n"
    "v_accvgpr_read_b32 v111, a123\n"
    "v_pk_fma_f32 v[108:109], v[108:109], v[226:227], s[94:95]\n"
    "v_pk_fma_f32 v[110:111], v[110:111], v[226:227], s[94:95]\n"
    "v_cvt_pk_bf16_f32 v62, v108, v109\n"
    "v_cvt_pk_bf16_f32 v63, v110, v111\n"
    "buffer_store_dwordx4 v[60:63], v205, s[28:31], s63 offen offset:192 sc0 sc1\n"
    "v_accvgpr_read_b32 v96, a136\n"
    "v_accvgpr_read_b32 v97, a137\n"
    "v_accvgpr_read_b32 v98, a138\n"
    "v_accvgpr_read_b32 v99, a139\n"
    "v_pk_fma_f32 v[96:97], v[96:97], v[226:227], s[94:95]\n"
    "v_pk_fma_f32 v[98:99], v[98:99], v[226:227], s[94:95]\n"
    "v_cvt_pk_bf16_f32 v32, v96, v97\n"
    "v_cvt_pk_bf16_f32 v33, v98, v99\n"
    "v_accvgpr_read_b32 v100, a152\n"
    "v_accvgpr_read_b32 v101, a153\n"
    "v_accvgpr_read_b32 v102, a154\n"
    "v_accvgpr_read_b32 v103, a155\n"
    "v_pk_fma_f32 v[100:101], v[100:101], v[226:227], s[94:95]\n"
    "v_pk_fma_f32 v[102:103], v[102:103], v[226:227], s[94:95]\n"
    "v_cvt_pk_bf16_f32 v34, v100, v101\n"
    "v_cvt_pk_bf16_f32 v35, v102, v103\n"
    "buffer_store_dwordx4 v[32:35], v205, s[28:31], s63 offen offset:256 sc0 sc1\n"
    "v_accvgpr_read_b32 v104, a168\n"
    "v_accvgpr_read_b32 v105, a169\n"
    "v_accvgpr_read_b32 v106, a170\n"
    "v_accvgpr_read_b32 v107, a171\n"
    "v_pk_fma_f32 v[104:105], v[104:105], v[226:227], s[94:95]\n"
    "v_pk_fma_f32 v[106:107], v[106:107], v[226:227], s[94:95]\n"
    "v_cvt_pk_bf16_f32 v36, v104, v105\n"
    "v_cvt_pk_bf16_f32 v37, v106, v107\n"
    "v_accvgpr_read_b32 v108, a184\n"
    "v_accvgpr_read_b32 v109, a185\n"
    "v_accvgpr_read_b32 v110, a186\n"
    "v_accvgpr_read_b32 v111, a187\n"
    "v_pk_fma_f32 v[108:109], v[108:109], v[226:227], s[94:95]\n"
    "v_pk_fma_f32 v[110:111], v[110:111], v[226:227], s[94:95]\n"
    "v_cvt_pk_bf16_f32 v38, v108, v109\n"
    "v_cvt_pk_bf16_f32 v39, v110, v111\n"
    "buffer_store_dwordx4 v[36:39], v205, s[28:31], s63 offen offset:320 sc0 sc1\n"
    "v_accvgpr_read_b32 v96, a12\n"
    "v_accvgpr_read_b32 v97, a13\n"
    "v_accvgpr_read_b32 v98, a14\n"
    "v_accvgpr_read_b32 v99, a15\n"
    "v_pk_fma_f32 v[96:97], v[96:97], v[228:229], s[94:95]\n"
    "v_pk_fma_f32 v[98:99], v[98:99], v[228:229], s[94:95]\n"
    "v_cvt_pk_bf16_f32 v40, v96, v97\n"
    "v_cvt_pk_bf16_f32 v41, v98, v99\n"
    "v_accvgpr_read_b32 v100, a28\n"
    "v_accvgpr_read_b32 v101, a29\n"
    "v_accvgpr_read_b32 v102, a30\n"
    "v_accvgpr_read_b32 v103, a31\n"
    "v_pk_fma_f32 v[100:101], v[100:101], v[228:229], s[94:95]\n"
    "v_pk_fma_f32 v[102:103], v[102:103], v[228:229], s[94:95]\n"
    "v_cvt_pk_bf16_f32 v42, v100, v101\n"
    "v_cvt_pk_bf16_f32 v43, v102, v103\n"
    "buffer_store_dwordx4 v[40:43], v205, s[28:31], s64 offen sc0 sc1\n"
    "v_accvgpr_read_b32 v104, a44\n"
    "v_accvgpr_read_b32 v105, a45\n"
    "v_accvgpr_read_b32 v106, a46\n"
    "v_accvgpr_read_b32 v107, a47\n"
    "v_pk_fma_f32 v[104:105], v[104:105], v[228:229], s[94:95]\n"
    "v_pk_fma_f32 v[106:107], v[106:107], v[228:229], s[94:95]\n"
    "v_cvt_pk_bf16_f32 v44, v104, v105\n"
    "v_cvt_pk_bf16_f32 v45, v106, v107\n"
    "v_accvgpr_read_b32 v108, a60\n"
    "v_accvgpr_read_b32 v109, a61\n"
    "v_accvgpr_read_b32 v110, a62\n"
    "v_accvgpr_read_b32 v111, a63\n"
    "v_pk_fma_f32 v[108:109], v[108:109], v[228:229], s[94:95]\n"
    "v_pk_fma_f32 v[110:111], v[110:111], v[228:229], s[94:95]\n"
    "v_cvt_pk_bf16_f32 v46, v108, v109\n"
    "v_cvt_pk_bf16_f32 v47, v110, v111\n"
    "buffer_store_dwordx4 v[44:47], v205, s[28:31], s64 offen offset:64 sc0 sc1\n"
    "v_accvgpr_read_b32 v96, a76\n"
    "v_accvgpr_read_b32 v97, a77\n"
    "v_accvgpr_read_b32 v98, a78\n"
    "v_accvgpr_read_b32 v99, a79\n"
    "v_pk_fma_f32 v[96:97], v[96:97], v[228:229], s[94:95]\n"
    "v_pk_fma_f32 v[98:99], v[98:99], v[228:229], s[94:95]\n"
    "v_cvt_pk_bf16_f32 v48, v96, v97\n"
    "v_cvt_pk_bf16_f32 v49, v98, v99\n"
    "v_accvgpr_read_b32 v100, a92\n"
    "v_accvgpr_read_b32 v101, a93\n"
    "v_accvgpr_read_b32 v102, a94\n"
    "v_accvgpr_read_b32 v103, a95\n"
    "v_pk_fma_f32 v[100:101], v[100:101], v[228:229], s[94:95]\n"
    "v_pk_fma_f32 v[102:103], v[102:103], v[228:229], s[94:95]\n"
    "v_cvt_pk_bf16_f32 v50, v100, v101\n"
    "v_cvt_pk_bf16_f32 v51, v102, v103\n"
    "buffer_store_dwordx4 v[48:51], v205, s[28:31], s64 offen offset:128 sc0 sc1\n"
    "v_accvgpr_read_b32 v104, a108\n"
    "v_accvgpr_read_b32 v105, a109\n"
    "v_accvgpr_read_b32 v106, a110\n"
    "v_accvgpr_read_b32 v107, a111\n"
    "v_pk_fma_f32 v[104:105], v[104:105], v[228:229], s[94:95]\n"
    "v_pk_fma_f32 v[106:107], v[106:107], v[228:229], s[94:95]\n"
    "v_cvt_pk_bf16_f32 v52, v104, v105\n"
    "v_cvt_pk_bf16_f32 v53, v106, v107\n"
    "v_accvgpr_read_b32 v108, a124\n"
    "v_accvgpr_read_b32 v109, a125\n"
    "v_accvgpr_read_b32 v110, a126\n"
    "v_accvgpr_read_b32 v111, a127\n"
    "v_pk_fma_f32 v[108:109], v[108:109], v[228:229], s[94:95]\n"
    "v_pk_fma_f32 v[110:111], v[110:111], v[228:229], s[94:95]\n"
    "v_cvt_pk_bf16_f32 v54, v108, v109\n"
    "v_cvt_pk_bf16_f32 v55, v110, v111\n"
    "buffer_store_dwordx4 v[52:55], v205, s[28:31], s64 offen offset:192 sc0 sc1\n"
    "v_accvgpr_read_b32 v96, a140\n"
    "v_accvgpr_read_b32 v97, a141\n"
    "v_accvgpr_read_b32 v98, a142\n"
    "v_accvgpr_read_b32 v99, a143\n"
    "v_pk_fma_f32 v[96:97], v[96:97], v[228:229], s[94:95]\n"
    "v_pk_fma_f32 v[98:99], v[98:99], v[228:229], s[94:95]\n"
    "v_cvt_pk_bf16_f32 v56, v96, v97\n"
    "v_cvt_pk_bf16_f32 v57, v98, v99\n"
    "v_accvgpr_read_b32 v100, a156\n"
    "v_accvgpr_read_b32 v101, a157\n"
    "v_accvgpr_read_b32 v102, a158\n"
    "v_accvgpr_read_b32 v103, a159\n"
    "v_pk_fma_f32 v[100:101], v[100:101], v[228:229], s[94:95]\n"
    "v_pk_fma_f32 v[102:103], v[102:103], v[228:229], s[94:95]\n"
    "v_cvt_pk_bf16_f32 v58, v100, v101\n"
    "v_cvt_pk_bf16_f32 v59, v102, v103\n"
    "buffer_store_dwordx4 v[56:59], v205, s[28:31], s64 offen offset:256 sc0 sc1\n"
    "v_accvgpr_read_b32 v104, a172\n"
    "v_accvgpr_read_b32 v105, a173\n"
    "v_accvgpr_read_b32 v106, a174\n"
    "v_accvgpr_read_b32 v107, a175\n"
    "v_pk_fma_f32 v[104:105], v[104:105], v[228:229], s[94:95]\n"
    "v_pk_fma_f32 v[106:107], v[106:107], v[228:229], s[94:95]\n"
    "v_cvt_pk_bf16_f32 v60, v104, v105\n"
    "v_cvt_pk_bf16_f32 v61, v106, v107\n"
    "v_accvgpr_read_b32 v108, a188\n"
    "v_accvgpr_read_b32 v109, a189\n"
    "v_accvgpr_read_b32 v110, a190\n"
    "v_accvgpr_read_b32 v111, a191\n"
    "v_pk_fma_f32 v[108:109], v[108:109], v[228:229], s[94:95]\n"
    "v_pk_fma_f32 v[110:111], v[110:111], v[228:229], s[94:95]\n"
    "v_cvt_pk_bf16_f32 v62, v108, v109\n"
    "v_cvt_pk_bf16_f32 v63, v110, v111\n"
    "buffer_store_dwordx4 v[60:63], v205, s[28:31], s64 offen offset:320 sc0 sc1\n"
    "s_branch LFIN\n"
    "LEND:\n"
    :
    : "s"(__builtin_amdgcn_kernarg_segment_ptr()), "s"(blockIdx.x), "s"(wid)
    : "v0", "v1", "v2", "v3", "v4", "v5", "v6", "v7", "v8", "v9", "v10", "v11", "v12", "v13", "v14", "v15", "v16", "v17", "v18", "v19", "v20", "v21", "v22", "v23", "v24", "v25", "v26", "v27", "v28", "v29", "v30", "v31", "v32", "v33", "v34", "v35", "v36", "v37", "v38", "v39", "v40", "v41", "v42", "v43", "v44", "v45", "v46", "v47", "v48", "v49", "v50", "v51", "v52", "v53", "v54", "v55", "v56", "v57", "v58", "v59", "v60", "v61", "v62", "v63", "v64", "v65", "v66", "v67", "v68", "v69", "v70", "v71", "v72", "v73", "v74", "v75", "v76", "v77", "v78", "v79", "v80", "v81", "v82", "v83", "v84", "v85", "v86", "v87", "v88", "v89", "v90", "v91", "v92", "v93", "v94", "v95", "v96", "v97", "v98", "v99", "v100", "v101", "v102", "v103", "v104", "v105", "v106", "v107", "v108", "v109", "v110", "v111", "v112", "v113", "v114", "v115", "v116", "v117", "v118", "v119", "v120", "v121", "v122", "v123", "v124", "v125", "v126", "v127", "v128", "v129", "v130", "v131", "v132", "v133", "v134", "v135", "v136", "v137", "v138", "v139", "v140", "v141", "v142", "v143", "v144", "v145", "v146", "v147", "v148", "v149", "v150", "v151", "v152", "v153", "v154", "v155", "v156", "v157", "v158", "v159", "v160", "v161", "v162", "v163", "v164", "v165", "v166", "v167", "v168", "v169", "v170", "v171", "v172", "v173", "v174", "v175", "v176", "v177", "v178", "v179", "v180", "v181", "v182", "v183", "v184", "v185", "v186", "v187", "v188", "v189", "v190", "v191", "v192", "v193", "v194", "v195", "v196", "v197", "v198", "v199", "v200", "v201", "v202", "v203", "v204", "v205", "v206", "v207", "v208", "v209", "v210", "v211", "v212", "v213", "v214", "v215", "v216", "v217", "v218", "v219", "v220", "v221", "v222", "v223", "v224", "v225", "v226", "v227", "v228", "v229", "v230", "v231", "v232", "v233", "v234", "v235", "v236", "v237", "v238", "v239", "v240", "v241", "v242", "v243", "v244", "v245", "v246", "v247", "v248", "v249", "v250", "v251", "v252", "v253", "v254", "v255", "a0", "a1", "a2", "a3", "a4", "a5", "a6", "a7", "a8", "a9", "a10", "a11", "a12", "a13", "a14", "a15", "a16", "a17", "a18", "a19", "a20", "a21", "a22", "a23", "a24", "a25", "a26", "a27", "a28", "a29", "a30", "a31", "a32", "a33", "a34", "a35", "a36", "a37", "a38", "a39", "a40", "a41", "a42", "a43", "a44", "a45", "a46", "a47", "a48", "a49", "a50", "a51", "a52", "a53", "a54", "a55", "a56", "a57", "a58", "a59", "a60", "a61", "a62", "a63", "a64", "a65", "a66", "a67", "a68", "a69", "a70", "a71", "a72", "a73", "a74", "a75", "a76", "a77", "a78", "a79", "a80", "a81", "a82", "a83", "a84", "a85", "a86", "a87", "a88", "a89", "a90", "a91", "a92", "a93", "a94", "a95", "a96", "a97", "a98", "a99", "a100", "a101", "a102", "a103", "a104", "a105", "a106", "a107", "a108", "a109", "a110", "a111", "a112", "a113", "a114", "a115", "a116", "a117", "a118", "a119", "a120", "a121", "a122", "a123", "a124", "a125", "a126", "a127", "a128", "a129", "a130", "a131", "a132", "a133", "a134", "a135", "a136", "a137", "a138", "a139", "a140", "a141", "a142", "a143", "a144", "a145", "a146", "a147", "a148", "a149", "a150", "a151", "a152", "a153", "a154", "a155", "a156", "a157", "a158", "a159", "a160", "a161", "a162", "a163", "a164", "a165", "a166", "a167", "a168", "a169", "a170", "a171", "a172", "a173", "a174", "a175", "a176", "a177", "a178", "a179", "a180", "a181", "a182", "a183", "a184", "a185", "a186", "a187", "a188", "a189", "a190", "a191", "a192", "a193", "a194", "a195", "a196", "a197", "a198", "a199", "a200", "a201", "a202", "a203", "a204", "a205", "a206", "a207", "a208", "a209", "a210", "a211", "a212", "a213", "a214", "a215", "a216", "a217", "a218", "a219", "a220", "a221", "a222", "a223", "a224", "a225", "a226", "a227", "a228", "a229", "a230", "a231", "a232", "a233", "a234", "a235", "a236", "a237", "a238", "a239", "a240", "a241", "a242", "a243", "a244", "a245", "a246", "a247", "a248", "a249", "a250", "a251", "a252", "a253", "a254", "a255", "s8", "s9", "s10", "s11", "s12", "s13", "s14", "s15", "s16", "s17", "s18", "s19", "s20", "s21", "s22", "s23", "s24", "s25", "s26", "s27", "s28", "s29", "s30", "s31", "s32", "s33", "s34", "s35", "s36", "s37", "s38", "s39", "s40", "s41", "s42", "s43", "s44", "s45", "s46", "s47", "s48", "s49", "s50", "s51", "s52", "s53", "s54", "s55", "s56", "s57", "s58", "s59", "s60", "s61", "s62", "s63", "s64", "s65", "s66", "s67", "s68", "s69", "s70", "s71", "s72", "s73", "s74", "s75", "s76", "s77", "s78", "s79", "s80", "s81", "s82", "s83", "s84", "s85", "s86", "s87", "s88", "s89", "s90", "s91", "s92", "s93", "s94", "s95", "s96", "s97", "s98", "s99", "s100", "s101", "vcc", "scc", "memory");
}

extern "C" void kernel_launch(void* const* d_in, const int* in_sizes, int n_in, void* d_out, int out_size, void* d_ws, size_t ws_size,
                              hipStream_t stream) {
  (void)hipFuncSetAttribute((const void*)attn_fused, hipFuncAttributeMaxDynamicSharedMemorySize, 153088);
  attn_fused<<<256, 256, 153088, stream>>>(d_in[0], d_in[1], d_in[2], d_in[3], d_in[4], d_in[5], d_in[6], d_ws, d_out);
}
```

```cpp
#include <hip/hip_runtime.h>

__global__ __launch_bounds__(256, 1) void attn_fused(const void* q, const void* qk, const void* qv, const void* ks, const void* kz,
                                                     const void* vs, const void* vz, void* ws, void* out) {
  const int wid = __builtin_amdgcn_readfirstlane(threadIdx.x >> 6);
  asm volatile(
    "s_load_dwordx16 s[8:23], %0, 0x0\n"
    "s_mov_b32 s58, %2\n"
    "s_and_b32 s76, %1, 7\n"
    "s_lshr_b32 s77, %1, 3\n"
    "s_and_b32 s78, s77, 3\n"
    "s_lshl_b32 s56, s76, 2\n"
    "s_add_u32 s56, s56, s78\n"
    "s_lshr_b32 s57, s77, 2\n"
    "v_mbcnt_lo_u32_b32 v231, -1, 0\n"
    "v_mbcnt_hi_u32_b32 v231, -1, v231\n"
    "s_getpc_b64 s[44:45]\n"
    "LPC:\n"
    "s_mov_b32 s46, LSLOW-LPC\n"
    "s_mov_b32 s47, 0x20000\n"
    "s_lshr_b32 s76, %1, 3\n"
    "s_lshl_b32 s76, s76, 2\n"
    "s_add_u32 s76, s76, s58\n"
    "s_lshl_b32 s76, s76, 9\n"
    "v_lshlrev_b32 v196, 3, v231\n"
    "buffer_load_dword v251, v196, s[44:47], s76 offen\n"
    "s_mul_i32 s72, s58, 0xc00\n"
    "s_lshr_b32 s86, s57, 2\n"
    "s_mul_i32 s64, s86, 0xc000\n"
    "s_lshl_b32 s86, s86, 3\n"
    "s_and_b32 s79, s57, 3\n"
    "s_add_u32 s86, s86, s79\n"
    "s_lshl_b32 s86, s86, 2\n"
    "s_xor_b32 s76, s57, 4\n"
    "s_and_b32 s77, s76, 6\n"
    "s_mul_i32 s77, s77, 15\n"
    "s_add_u32 s76, s76, s77\n"
    "s_and_b32 s77, s58, 1\n"
    "s_mul_i32 s78, s77, 12\n"
    "s_add_u32 s78, s78, s86\n"
    "s_lshl_b32 s78, s78, 1\n"
    "s_mul_i32 s77, s77, 0xc0\n"
    "s_movk_i32 s90, 0x556\n"
    "s_lshl_b32 s60, s56, 12\n"
    "s_cmp_lt_u32 s58, 2\n"
    "s_cselect_b32 s76, s76, s78\n"
    "s_cmovk_i32 s90, 0xaab\n"
    "s_cselect_b32 s91, 24, 0\n"
    "s_cselect_b32 s92, 1, 10\n"
    "s_cselect_b32 s89, 7, 0\n"
    "s_cselect_b32 s64, s64, 0\n"
    "s_add_u32 s60, s60, s76\n"
    "s_mul_i32 s60, s60, 0x180\n"
    "s_mul_i32 s76, s76, 0x180\n"
    "s_sub_u32 s34, 0x180000, s76\n"
    "s_mov_b32 s35, 0x20000\n"
    "v_add_u32 v144, s77, v231\n"
    "v_add_u32 v145, 64, v144\n"
    "v_add_u32 v146, 64, v145\n"
    "v_mul_u32_u24 v197, s90, v144\n"
    "v_lshrrev_b32 v197, 16, v197\n"
    "v_mul_u32_u24 v198, s92, v197\n"
    "v_lshrrev_b32 v198, 1, v198\n"
    "v_and_b32 v198, s89, v198\n"
    "v_xor_b32 v198, v198, v144\n"
    "v_mul_u32_u24 v197, s91, v197\n"
    "v_add_lshl_u32 v202, v197, v198, 4\n"
    "v_mul_u32_u24 v197, s90, v145\n"
    "v_lshrrev_b32 v197, 16, v197\n"
    "v_mul_u32_u24 v198, s92, v197\n"
    "v_lshrrev_b32 v198, 1, v198\n"
    "v_and_b32 v198, s89, v198\n"
    "v_xor_b32 v198, v198, v144\n"
    "v_mul_u32_u24 v197, s91, v197\n"
    "v_add_lshl_u32 v203, v197, v198, 4\n"
    "v_mul_u32_u24 v197, s90, v146\n"
    "v_lshrrev_b32 v197, 16, v197\n"
    "v_mul_u32_u24 v198, s92, v197\n"
    "v_lshrrev_b32 v198, 1, v198\n"
    "v_and_b32 v198, s89, v198\n"
    "v_xor_b32 v198, v198, v144\n"
    "v_mul_u32_u24 v197, s91, v197\n"
    "v_add_lshl_u32 v204, v197, v198, 4\n"
    "s_waitcnt lgkmcnt(0)\n"
    "s_cmp_lt_u32 s58, 2\n"
    "s_cselect_b64 s[80:81], s[10:11], s[12:13]\n"
    "s_cselect_b64 s[82:83], s[16:17], s[14:15]\n"
    "s_add_u32 s32, s80, s60\n"
    "s_addc_u32 s33, s81, 0\n"
    "s_cmp_eq_u32 s64, 0\n"
    "s_cbranch_scc1 LNOR\n"
    "s_add_u32 m0, s72, 0x12000\n"
    "s_nop 0\n"
    "buffer_load_dwordx4 v202, s[32:35], 0 offen nt lds\n"
    "buffer_load_dwordx4 v203, s[32:35], 0 offen offset:1024 nt lds\n"
    "buffer_load_dwordx4 v204, s[32:35], 0 offen offset:2048 nt lds\n"
    "LNOR:\n"
    "s_add_u32 m0, s72, 0x15000\n"
    "s_nop 0\n"
    "buffer_load_dwordx4 v202, s[32:35], s64 offen nt lds\n"
    "buffer_load_dwordx4 v203, s[32:35], s64 offen offset:1024 nt lds\n"
    "buffer_load_dwordx4 v204, s[32:35], s64 offen offset:2048 nt lds\n"
    "LR0:\n"
    "s_load_dwordx2 s[24:25], %0, 0x40\n"
    "s_mul_i32 s71, s58, 0x1800\n"
    "s_lshr_b32 s87, s57, 2\n"
    "s_mul_i32 s87, s87, 0x6000\n"
    "s_mul_i32 s61, s56, 0x3000\n"
    "s_lshl_b32 s62, s56, 12\n"
    "s_lshl_b32 s59, s56, 12\n"
    "s_mov_b32 s50, 0x3000\n"
    "s_mov_b32 s51, 0x20000\n"
    "s_and_b32 s77, s57, 1\n"
    "s_mul_i32 s77, s77, 0xc0\n"
    "v_mul_u32_u24 v197, 0x1556, v144\n"
    "v_lshrrev_b32 v197, 16, v197\n"
    "v_mul_u32_u24 v196, 12, v197\n"
    "v_sub_u32 v196, v144, v196\n"
    "v_mul_u32_u24 v248, 0x180, v197\n"
    "v_lshl_add_u32 v248, v196, 4, v248\n"
    "v_add_u32 v248, s77, v248\n"
    "s_add_u32 s48, s82, s61\n"
    "s_addc_u32 s49, s83, 0\n"
    "s_add_u32 m0, s72, 0x21000\n"
    "s_nop 0\n"
    "buffer_load_dwordx4 v248, s[48:51], 0 offen lds\n"
    "s_add_u32 s40, s20, s62\n"
    "s_addc_u32 s41, s21, 0\n"
    "s_add_u32 s52, s18, s62\n"
    "s_addc_u32 s53, s19, 0\n"
    "s_mov_b32 s42, 0x1000\n"
    "s_mov_b32 s43, 0x20000\n"
    "s_mov_b32 s54, 0x1000\n"
    "s_mov_b32 s55, 0x20000\n"
    "v_lshlrev_b32 v196, 1, v231\n"
    "buffer_load_ushort v44, v196, s[40:43], 0 offen\n"
    "buffer_load_ushort v45, v196, s[52:55], 0 offen\n"
    "s_lshl_b32 s77, s58, 6\n"
    "v_add_u32 v41, s77, v231\n"
    "v_bfe_u32 v197, v231, 2, 1\n"
    "v_mul_u32_u24 v197, 12, v197\n"
    "v_and_b32 v198, 7, v231\n"
    "v_add3_u32 v197, v197, v198, s86\n"
    "v_lshrrev_b32 v40, 3, v41\n"
    "v_lshlrev_b32 v40, 7, v40\n"
    "v_lshl_add_u32 v40, v197, 1, v40\n"
    "buffer_load_ushort v42, v40, s[40:43], 0 offen\n"
    "buffer_load_ushort v43, v40, s[52:55], 0 offen\n"
    "s_add_u32 s44, s22, s59\n"
    "s_addc_u32 s45, s23, 0\n"
    "s_mov_b32 s46, 0x900\n"
    "s_mov_b32 s47, 0x20000\n"
    "v_mov_b32 v196, 0x40000000\n"
    "s_cmp_eq_u32 s58, 0\n"
    "s_cselect_b32 s77, 64, 0\n"
    "s_cselect_b32 s74, 1, 0\n"
    "v_and_b32 v213, 7, v231\n"
    "v_mul_u32_u24 v213, 0x100, v213\n"
    "v_cmp_gt_u32 vcc, s77, v231\n"
    "v_mov_b32 v197, 0x800\n"
    "s_mul_i32 s77, s57, 0x100\n"
    "v_cndmask_b32 v213, v196, v213, vcc\n"
    "v_cndmask_b32 v197, v196, v197, vcc\n"
    "buffer_load_dword v221, v197, s[44:47], 0 offen sc1\n"
    "v_cmp_gt_u32 vcc, s74, v231\n"
    "v_mov_b32 v214, s77\n"
    "s_mul_i32 s66, s74, 3\n"
    "v_cndmask_b32 v214, v196, v214, vcc\n"
    "s_lshl_b32 s76, s56, 3\n"
    "s_add_u32 s76, s76, s57\n"
    "s_mul_i32 s76, s76, 0x18000\n"
    "s_add_u32 s28, s8, s76\n"
    "s_addc_u32 s29, s9, 0\n"
    "s_mov_b32 s30, 0x18000\n"
    "s_mov_b32 s31, 0x20000\n"
    "s_mul_i32 s88, s58, 0x6000\n"
    "v_mul_u32_u24 v197, 0xaab, v231\n"
    "v_lshrrev_b32 v197, 16, v197\n"
    "v_mul_u32_u24 v196, 24, v197\n"
    "v_sub_u32 v196, v231, v196\n"
    "v_mul_u32_u24 v152, 0x180, v197\n"
    "v_lshrrev_b32 v197, 1, v197\n"
    "v_xor_b32 v196, v196, v197\n"
    "v_lshl_add_u32 v152, v196, 4, v152\n"
    "v_add_u32 v198, 64, v231\n"
    "v_mul_u32_u24 v197, 0xaab, v198\n"
    "v_lshrrev_b32 v197, 16, v197\n"
    "v_mul_u32_u24 v196, 24, v197\n"
    "v_sub_u32 v196, v198, v196\n"
    "v_mul_u32_u24 v153, 0x180, v197\n"
    "v_lshrrev_b32 v197, 1, v197\n"
    "v_xor_b32 v196, v196, v197\n"
    "v_lshl_add_u32 v153, v196, 4, v153\n"
    "v_add_u32 v198, 0x80, v231\n"
    "v_mul_u32_u24 v197, 0xaab, v198\n"
    "v_lshrrev_b32 v197, 16, v197\n"
    "v_mul_u32_u24 v196, 24, v197\n"
    "v_sub_u32 v196, v198, v196\n"
    "v_mul_u32_u24 v154, 0x180, v197\n"
    "v_lshrrev_b32 v197, 1, v197\n"
    "v_xor_b32 v196, v196, v197\n"
    "v_lshl_add_u32 v154, v196, 4, v154\n"
    "s_mov_b32 s76, 0xc00\n"
    "v_xad_u32 v155, v152, 64, s76\n"
    "v_xad_u32 v156, v153, 64, s76\n"
    "v_xad_u32 v157, v154, 64, s76\n"
    "s_add_u32 s78, s88, 0\n"
    "s_add_u32 m0, s71, 0\n"
    "s_nop 0\n"
    "buffer_load_dwordx4 v152, s[28:31], s78 offen nt lds\n"
    "s_add_u32 s78, s88, 0\n"
    "s_add_u32 m0, s71, 0x400\n"
    "s_nop 0\n"
    "buffer_load_dwordx4 v153, s[28:31], s78 offen nt lds\n"
    "s_add_u32 s78, s64, 0xc000\n"
    "s_add_u32 m0, s72, 0x1e000\n"
    "s_nop 0\n"
    "buffer_load_dwordx4 v202, s[32:35], s78 offen nt lds\n"
    "buffer_load_dwordx4 v203, s[32:35], s78 offen offset:1024 nt lds\n"
    "buffer_load_dwordx4 v204, s[32:35], s78 offen offset:2048 nt lds\n"
    "s_add_u32 s78, s88, 0\n"
    "s_add_u32 m0, s71, 0x800\n"
    "s_nop 0\n"
    "buffer_load_dwordx4 v154, s[28:31], s78 offen nt lds\n"
    "s_add_u32 s78, s88, 0\n"
    "s_add_u32 m0, s71, 0xc00\n"
    "s_nop 0\n"
    "buffer_load_dwordx4 v155, s[28:31], s78 offen nt lds\n"
    "s_add_u32 s78, s88, 0\n"
    "s_add_u32 m0, s71, 0x1000\n"
    "s_nop 0\n"
    "buffer_load_dwordx4 v156, s[28:31], s78 offen nt lds\n"
    "v_mov_b32 v196, 0x40000000\n"
    "v_and_b32 v46, 15, v231\n"
    "v_lshrrev_b32 v47, 4, v231\n"
    "s_mov_b32 s75, 0x3dd53b95\n"
    "v_min_u32 v199, 2, v47\n"
    "v_lshlrev_b32 v197, 3, v199\n"
    "s_lshl_b32 s76, s58, 1\n"
    "v_add_u32 v197, s76, v197\n"
    "v_lshrrev_b32 v198, 1, v46\n"
    "v_xor_b32 v197, v197, v198\n"
    "v_mul_u32_u24 v205, 0x180, v46\n"
    "v_lshl_add_u32 v205, v197, 4, v205\n"
    "v_add_u32 v205, 0x12000, v205\n"
    "v_xor_b32 v206, 16, v205\n"
    "v_and_b32 v197, 7, v231\n"
    "v_lshl_add_u32 v209, v199, 5, v197\n"
    "s_lshl_b32 s76, s58, 3\n"
    "v_add_lshl_u32 v209, v209, s76, 1\n"
    "v_add_u32 v217, 0x21000, v209\n"
    "s_lshr_b32 s76, s57, 2\n"
    "s_mul_i32 s76, s76, 0xc0\n"
    "s_add_u32 s76, s76, 0x20f40\n"
    "v_add_u32 v209, s76, v209\n"
    "v_lshrrev_b32 v198, 3, v231\n"
    "s_mul_i32 s76, s58, 12\n"
    "v_lshl_add_u32 v199, v198, 1, s76\n"
    "v_mul_u32_u24 v207, 5, v197\n"
    "v_and_b32 v207, 9, v207\n"
    "v_xor_b32 v199, v199, v207\n"
    "v_mul_u32_u24 v207, 0x300, v197\n"
    "v_lshl_add_u32 v207, v199, 4, v207\n"
    "v_add_u32 v207, 0x13800, v207\n"
    "v_xor_b32 v208, 16, v207\n"
    "s_mul_i32 s76, s58, 0xc0\n"
    "s_add_u32 s76, s76, 0x13800\n"
    "v_lshl_add_u32 v207, v231, 2, s76\n"
    "v_lshlrev_b32 v210, 3, v197\n"
    "v_add_u32 v210, 0x24400, v210\n"
    "s_mul_i32 s76, s58, 0x480\n"
    "s_add_u32 s76, s76, 0x24400\n"
    "v_mul_u32_u24 v215, 0x90, v197\n"
    "v_lshl_add_u32 v215, v198, 4, v215\n"
    "v_add_u32 v215, s76, v215\n"
    "v_and_b32 v197, 1, v197\n"
    "v_lshlrev_b32 v197, 3, v197\n"
    "v_sub_u32 v215, v215, v197\n"
    "v_bfe_u32 v197, v231, 2, 2\n"
    "v_mul_u32_u24 v216, 0x90, v197\n"
    "v_lshl_add_u32 v216, v47, 5, v216\n"
    "v_and_b32 v197, 1, v197\n"
    "v_lshlrev_b32 v197, 3, v197\n"
    "v_sub_u32 v216, v216, v197\n"
    "v_and_b32 v197, 3, v231\n"
    "v_lshl_add_u32 v216, v197, 3, v216\n"
    "v_add_u32 v216, s76, v216\n"
    "s_lshr_b32 s76, s57, 1\n"
    "s_xor_b32 s76, s76, 2\n"
    "s_mul_i32 s76, s76, 0x1800\n"
    "s_and_b32 s77, s57, 1\n"
    "s_mul_i32 s77, s77, 0xc00\n"
    "s_add_u32 s76, s76, s77\n"
    "s_lshl_b32 s77, s58, 8\n"
    "s_add_u32 s76, s76, s77\n"
    "v_lshlrev_b32 v197, 4, v46\n"
    "v_lshl_add_u32 v211, v47, 10, v197\n"
    "v_add_u32 v211, s76, v211\n"
    "s_lshr_b32 s76, s57, 2\n"
    "s_lshl_b32 s76, s76, 10\n"
    "s_lshl_b32 s77, s79, 8\n"
    "s_add_u32 s76, s76, s77\n"
    "s_mul_i32 s77, s58, 48\n"
    "v_add_u32 v198, s77, v231\n"
    "v_lshrrev_b32 v197, 5, v198\n"
    "v_lshl_add_u32 v212, v197, 12, s76\n"
    "v_bfe_u32 v197, v198, 2, 1\n"
    "v_lshl_add_u32 v212, v197, 11, v212\n"
    "v_bfe_u32 v197, v198, 3, 2\n"
    "v_lshl_add_u32 v212, v197, 6, v212\n"
    "v_cmp_gt_u32 vcc, 48, v231\n"
    "v_and_b32 v197, 3, v198\n"
    "v_lshl_add_u32 v212, v197, 4, v212\n"
    "v_cndmask_b32 v211, v196, v211, vcc\n"
    "v_cndmask_b32 v212, v196, v212, vcc\n"
    "v_mov_b32 v222, 0\n"
    "v_mov_b32 v223, 0\n"
    "v_mov_b32 v224, 0\n"
    "v_mov_b32 v225, 0\n"
    "v_mov_b32 v160, 0\n"
    "v_mov_b32 v161, 0\n"
    "v_mov_b32 v162, 0\n"
    "v_mov_b32 v163, 0\n"
    "v_mov_b32 v164, 0\n"
    "v_mov_b32 v165, 0\n"
    "v_mov_b32 v172, 0\n"
    "v_mov_b32 v173, 0\n"
    "v_mov_b32 v174, 0\n"
    "v_mov_b32 v175, 0\n"
    "v_lshlrev_b32 v41, 3, v41\n"
    "v_add_u32 v41, 0x24400, v41\n"
    "s_mul_i32 s76, s56, 0xc0000\n"
    "s_add_u32 s77, s76, 0x40000\n"
    "s_add_u32 s36, s22, s77\n"
    "s_addc_u32 s37, s23, 0\n"
    "s_mov_b32 s38, 0xc0000\n"
    "s_mov_b32 s39, 0x20000\n"
    "s_cmp_eq_u32 s87, 0\n"
    "s_cselect_b32 s62, 0x40000000, 0\n"
    "v_lshlrev_b32 v200, 4, v231\n"
    "v_add_u32 v201, 0xc000, v200\n"
    "v_mul_u32_u24 v198, 0x180, v46\n"
    "v_add_u32 v198, s71, v198\n"
    "v_lshrrev_b32 v197, 1, v46\n"
    "v_xor_b32 v197, v197, v47\n"
    "v_lshl_add_u32 v158, v197, 4, v198\n"
    "v_xor_b32 v159, 64, v158\n"
    "v_and_b32 v197, 3, v231\n"
    "v_cmp_eq_u32 vcc, 0, v197\n"
    "v_mov_b32 v198, 0x3f803f80\n"
    "s_nop 0\n"
    "v_cndmask_b32 v192, 0, v198, vcc\n"
    "v_cndmask_b32 v193, 0, v198, vcc\n"
    "v_cndmask_b32 v194, 0, v198, vcc\n"
    "v_cndmask_b32 v195, 0, v198, vcc\n"
    "s_and_b32 s77, s57, 1\n"
    "s_mul_i32 s77, s77, 0xc0\n"
    "s_sub_u32 s77, s77, 0x400\n"
    "v_mul_u32_u24 v197, 0x1556, v145\n"
    "v_lshrrev_b32 v197, 16, v197\n"
    "v_mul_u32_u24 v196, 12, v197\n"
    "v_sub_u32 v196, v145, v196\n"
    "v_mul_u32_u24 v249, 0x180, v197\n"
    "v_lshl_add_u32 v249, v196, 4, v249\n"
    "v_add_u32 v249, s77, v249\n"
    "s_and_b32 s77, s57, 1\n"
    "s_mul_i32 s77, s77, 0xc0\n"
    "s_sub_u32 s77, s77, 0x800\n"
    "v_mul_u32_u24 v197, 0x1556, v146\n"
    "v_lshrrev_b32 v197, 16, v197\n"
    "v_mul_u32_u24 v196, 12, v197\n"
    "v_sub_u32 v196, v146, v196\n"
    "v_mul_u32_u24 v250, 0x180, v197\n"
    "v_lshl_add_u32 v250, v196, 4, v250\n"
    "v_add_u32 v250, s77, v250\n"
    "s_waitcnt vmcnt(12)\n"
    "v_lshlrev_b32 v44, 16, v44\n"
    "s_waitcnt vmcnt(11)\n"
    "v_lshlrev_b32 v45, 16, v45\n"
    "v_mul_f32 v197, v44, v45\n"
    "s_waitcnt vmcnt(10)\n"
    "v_lshlrev_b32 v42, 16, v42\n"
    "s_waitcnt vmcnt(9)\n"
    "v_lshlrev_b32 v43, 16, v43\n"
    "v_min_f32_dpp v197, v197, v197 row_ror:8 row_mask:0xf bank_mask:0xf\n"
    "s_mul_i32 s76, s56, 0xc0000\n"
    "s_add_u32 s77, s76, 0x1840000\n"
    "v_min_f32_dpp v197, v197, v197 row_ror:4 row_mask:0xf bank_mask:0xf\n"
    "s_add_u32 s40, s22, s77\n"
    "s_addc_u32 s41, s23, 0\n"
    "v_min_f32_dpp v197, v197, v197 row_ror:2 row_mask:0xf bank_mask:0xf\n"
    "s_mov_b32 s42, 0xc0000\n"
    "s_mov_b32 s43, 0x20000\n"
    "v_min_f32_dpp v197, v197, v197 row_ror:1 row_mask:0xf bank_mask:0xf\n"
    "v_mov_b32 v198, v197\n"
    "s_nop 1\n"
    "v_permlane32_swap_b32 v197, v198\n"
    "v_min_f32 v197, v197, v198\n"
    "v_mov_b32 v198, v197\n"
    "s_nop 1\n"
    "v_permlane16_swap_b32 v197, v198\n"
    "v_min_f32 v197, v197, v198\n"
    "v_rcp_f32 v198, v197\n"
    "v_subrev_u32 v199, 0x37800000, v197\n"
    "v_mul_f32 v197, 0x3fcccccd, v197\n"
    "v_cmp_gt_u32 vcc, 0x10000000, v199\n"
    "v_mul_f32 v198, 0x3f3425ed, v198\n"
    "v_mov_b32 v199, 0x3f800000\n"
    "v_cndmask_b32 v197, 0, v197, vcc\n"
    "v_cndmask_b32 v198, v199, v198, vcc\n"
    "v_readfirstlane_b32 s70, v197\n"
    "v_mul_f32 v197, v197, v198\n"
    "v_readfirstlane_b32 s68, v198\n"
    "v_readfirstlane_b32 s69, v197\n"
    "v_mul_f32 v44, v198, v43\n"
    "v_fma_f32 v45, -v42, v44, v197\n"
    "ds_write_b64 v41, v[44:45]\n"
    "s_waitcnt lgkmcnt(0)\n"
    "s_barrier\n"
    "ds_read_u16_d16_hi v172, v217 offset:6144\n"
    "ds_read_u16_d16_hi v173, v217\n"
    "ds_read_u16_d16_hi v174, v217 offset:6336\n"
    "ds_read_u16_d16_hi v175, v217 offset:192\n"
    "ds_read_b64 v[228:229], v210\n"
    "ds_read2st64_b32 v[240:241], v207 offset0:48 offset1:51\n"
    "ds_read2st64_b32 v[242:243], v207 offset0:54 offset1:57\n"
    "ds_read2st64_b32 v[244:245], v207 offset0:60 offset1:63\n"
    "ds_read2st64_b32 v[246:247], v207 offset0:66 offset1:69\n"
    "ds_read_u16_d16_hi v223, v209 offset:6144\n"
    "ds_read_u16_d16_hi v222, v209\n"
    "ds_read_b128 v[232:235], v205\n"
    "ds_read_b128 v[236:239], v206\n"
    "ds_read_u16_d16_hi v161, v209 offset:6336\n"
    "ds_read_u16_d16_hi v160, v209 offset:192\n"
    "s_waitcnt lgkmcnt(11)\n"
    "ds_read_b128 v[64:67], v205 offset:12288\n"
    "ds_read_b128 v[68:71], v206 offset:12288\n"
    "v_mul_f32 v197, v172, v173\n"
    "v_mul_f32 v198, v174, v175\n"
    "v_min_f32 v197, v197, v198\n"
    "v_max_f32 v197, 0, v197\n"
    "v_mul_f32 v217, 0x3e3fe8d2, v197\n"
    "s_waitcnt lgkmcnt(7)\n"
    "v_mul_f32 v226, s75, v223\n"
    "s_waitcnt lgkmcnt(6)\n"
    "v_fma_f32 v227, -v222, v226, v217\n"
    "s_waitcnt lgkmcnt(5)\n"
    "v_cvt_f32_i32 v232, v232\n"
    "v_cvt_f32_i32 v233, v233\n"
    "v_cvt_f32_i32 v234, v234\n"
    "v_cvt_f32_i32 v235, v235\n"
    "s_waitcnt lgkmcnt(4)\n"
    "v_cvt_f32_i32 v236, v236\n"
    "v_cvt_f32_i32 v237, v237\n"
    "v_cvt_f32_i32 v238, v238\n"
    "v_cvt_f32_i32 v239, v239\n"
    "v_mul_f32_dpp v232, v226, v232 row_newbcast:0 row_mask:0xf bank_mask:0xf\n"
    "v_mul_f32_dpp v233, v226, v233 row_newbcast:1 row_mask:0xf bank_mask:0xf\n"
    "v_mul_f32_dpp v234, v226, v234 row_newbcast:2 row_mask:0xf bank_mask:0xf\n"
    "v_mul_f32_dpp v235, v226, v235 row_newbcast:3 row_mask:0xf bank_mask:0xf\n"
    "v_mul_f32_dpp v236, v226, v236 row_newbcast:4 row_mask:0xf bank_mask:0xf\n"
    "v_mul_f32_dpp v237, v226, v237 row_newbcast:5 row_mask:0xf bank_mask:0xf\n"
    "v_mul_f32_dpp v238, v226, v238 row_newbcast:6 row_mask:0xf bank_mask:0xf\n"
    "v_mul_f32_dpp v239, v226, v239 row_newbcast:7 row_mask:0xf bank_mask:0xf\n"
    "v_add_f32_dpp v232, v227, v232 row_newbcast:0 row_mask:0xf bank_mask:0xf\n"
    "v_add_f32_dpp v233, v227, v233 row_newbcast:1 row_mask:0xf bank_mask:0xf\n"
    "v_add_f32_dpp v234, v227, v234 row_newbcast:2 row_mask:0xf bank_mask:0xf\n"
    "v_add_f32_dpp v235, v227, v235 row_newbcast:3 row_mask:0xf bank_mask:0xf\n"
    "v_add_f32_dpp v236, v227, v236 row_newbcast:4 row_mask:0xf bank_mask:0xf\n"
    "v_add_f32_dpp v237, v227, v237 row_newbcast:5 row_mask:0xf bank_mask:0xf\n"
    "v_add_f32_dpp v238, v227, v238 row_newbcast:6 row_mask:0xf bank_mask:0xf\n"
    "v_add_f32_dpp v239, v227, v239 row_newbcast:7 row_mask:0xf bank_mask:0xf\n"
    "v_cvt_pk_bf16_f32 v232, v232, v233\n"
    "v_cvt_pk_bf16_f32 v233, v234, v235\n"
    "v_cvt_pk_bf16_f32 v234, v236, v237\n"
    "v_cvt_pk_bf16_f32 v235, v238, v239\n"
    "buffer_store_dwordx4 v[232:235], v211, s[36:39], s62 offen sc1\n"
    "v_cvt_f32_i32 v240, v240\n"
    "v_cvt_f32_i32 v241, v241\n"
    "v_cvt_f32_i32 v242, v242\n"
    "v_cvt_f32_i32 v243, v243\n"
    "v_cvt_f32_i32 v244, v244\n"
    "v_cvt_f32_i32 v245, v245\n"
    "v_cvt_f32_i32 v246, v246\n"
    "v_cvt_f32_i32 v247, v247\n"
    "v_mul_f32_dpp v240, v228, v240 row_newbcast:0 row_mask:0xf bank_mask:0xf\n"
    "v_mul_f32_dpp v241, v228, v241 row_newbcast:1 row_mask:0xf bank_mask:0xf\n"
    "v_mul_f32_dpp v242, v228, v242 row_newbcast:2 row_mask:0xf bank_mask:0xf\n"
    "v_mul_f32_dpp v243, v228, v243 row_newbcast:3 row_mask:0xf bank_mask:0xf\n"
    "v_mul_f32_dpp v244, v228, v244 row_newbcast:4 row_mask:0xf bank_mask:0xf\n"
    "v_mul_f32_dpp v245, v228, v245 row_newbcast:5 row_mask:0xf bank_mask:0xf\n"
    "v_mul_f32_dpp v246, v228, v246 row_newbcast:6 row_mask:0xf bank_mask:0xf\n"
    "v_mul_f32_dpp v247, v228, v247 row_newbcast:7 row_mask:0xf bank_mask:0xf\n"
    "v_add_f32_dpp v240, v229, v240 row_newbcast:0 row_mask:0xf bank_mask:0xf\n"
    "v_add_f32_dpp v241, v229, v241 row_newbcast:1 row_mask:0xf bank_mask:0xf\n"
    "v_add_f32_dpp v242, v229, v242 row_newbcast:2 row_mask:0xf bank_mask:0xf\n"
    "v_add_f32_dpp v243, v229, v243 row_newbcast:3 row_mask:0xf bank_mask:0xf\n"
    "v_add_f32_dpp v244, v229, v244 row_newbcast:4 row_mask:0xf bank_mask:0xf\n"
    "v_add_f32_dpp v245, v229, v245 row_newbcast:5 row_mask:0xf bank_mask:0xf\n"
    "v_add_f32_dpp v246, v229, v246 row_newbcast:6 row_mask:0xf bank_mask:0xf\n"
    "v_add_f32_dpp v247, v229, v247 row_newbcast:7 row_mask:0xf bank_mask:0xf\n"
    "v_cvt_pk_bf16_f32 v240, v240, v241\n"
    "v_cvt_pk_bf16_f32 v241, v242, v243\n"
    "v_cvt_pk_bf16_f32 v242, v244, v245\n"
    "v_cvt_pk_bf16_f32 v243, v246, v247\n"
    "s_mov_b32 s78, 0\n"
    "buffer_store_dwordx4 v[240:243], v212, s[40:43], s78 offen sc1\n"
    "s_waitcnt lgkmcnt(3)\n"
    "v_mul_f32 v166, s75, v161\n"
    "s_waitcnt lgkmcnt(2)\n"
    "v_fma_f32 v167, -v160, v166, v217\n"
    "s_waitcnt lgkmcnt(1)\n"
    "v_cvt_f32_i32 v64, v64\n"
    "v_cvt_f32_i32 v65, v65\n"
    "v_cvt_f32_i32 v66, v66\n"
    "v_cvt_f32_i32 v67, v67\n"
    "s_waitcnt lgkmcnt(0)\n"
    "v_cvt_f32_i32 v68, v68\n"
    "v_cvt_f32_i32 v69, v69\n"
    "v_cvt_f32_i32 v70, v70\n"
    "v_cvt_f32_i32 v71, v71\n"
    "v_mul_f32_dpp v64, v166, v64 row_newbcast:0 row_mask:0xf bank_mask:0xf\n"
    "v_mul_f32_dpp v65, v166, v65 row_newbcast:1 row_mask:0xf bank_mask:0xf\n"
    "v_mul_f32_dpp v66, v166, v66 row_newbcast:2 row_mask:0xf bank_mask:0xf\n"
    "v_mul_f32_dpp v67, v166, v67 row_newbcast:3 row_mask:0xf bank_mask:0xf\n"
    "v_mul_f32_dpp v68, v166, v68 row_newbcast:4 row_mask:0xf bank_mask:0xf\n"
    "v_mul_f32_dpp v69, v166, v69 row_newbcast:5 row_mask:0xf bank_mask:0xf\n"
    "v_mul_f32_dpp v70, v166, v70 row_newbcast:6 row_mask:0xf bank_mask:0xf\n"
    "v_mul_f32_dpp v71, v166, v71 row_newbcast:7 row_mask:0xf bank_mask:0xf\n"
    "v_add_f32_dpp v64, v167, v64 row_newbcast:0 row_mask:0xf bank_mask:0xf\n"
    "v_add_f32_dpp v65, v167, v65 row_newbcast:1 row_mask:0xf bank_mask:0xf\n"
    "v_add_f32_dpp v66, v167, v66 row_newbcast:2 row_mask:0xf bank_mask:0xf\n"
    "v_add_f32_dpp v67, v167, v67 row_newbcast:3 row_mask:0xf bank_mask:0xf\n"
    "v_add_f32_dpp v68, v167, v68 row_newbcast:4 row_mask:0xf bank_mask:0xf\n"
    "v_add_f32_dpp v69, v167, v69 row_newbcast:5 row_mask:0xf bank_mask:0xf\n"
    "v_add_f32_dpp v70, v167, v70 row_newbcast:6 row_mask:0xf bank_mask:0xf\n"
    "v_add_f32_dpp v71, v167, v71 row_newbcast:7 row_mask:0xf bank_mask:0xf\n"
    "v_cvt_pk_bf16_f32 v64, v64, v65\n"
    "v_cvt_pk_bf16_f32 v65, v66, v67\n"
    "v_cvt_pk_bf16_f32 v66, v68, v69\n"
    "v_cvt_pk_bf16_f32 v67, v70, v71\n"
    "s_add_u32 s78, s87, 0\n"
    "buffer_store_dwordx4 v[64:67], v211, s[36:39], s78 offen sc1\n"
    "s_waitcnt vmcnt(11)\n"
    "v_add_u32 v221, s74, v221\n"
    "s_mov_b32 s73, 0\n"
    "v_readfirstlane_b32 s67, v221\n"
    "s_mul_i32 s66, s74, 2\n"
    "s_waitcnt vmcnt(0)\n"
    "s_barrier\n"
    "v_mov_b32 v220, 2\n"
    "buffer_store_dwordx2 v[220:221], v214, s[44:47], 0 offen sc1\n"
    "s_add_u32 s78, s64, 0x18000\n"
    "s_add_u32 m0, s72, 0x1b000\n"
    "s_nop 0\n"
    "buffer_load_dwordx4 v202, s[32:35], s78 offen nt lds\n"
    "buffer_load_dwordx4 v203, s[32:35], s78 offen offset:1024 nt lds\n"
    "buffer_load_dwordx4 v204, s[32:35], s78 offen offset:2048 nt lds\n"
    "s_add_u32 s78, s88, 0\n"
    "s_add_u32 m0, s71, 0x1400\n"
    "s_nop 0\n"
    "buffer_load_dwordx4 v157, s[28:31], s78 offen nt lds\n"
    "s_add_u32 s78, s88, 0x1800\n"
    "s_add_u32 m0, s71, 0x6000\n"
    "s_nop 0\n"
    "buffer_load_dwordx4 v152, s[28:31], s78 offen nt lds\n"
    "s_add_u32 s78, s88, 0x1800\n"
    "s_add_u32 m0, s71, 0x6400\n"
    "s_nop 0\n"
    "buffer_load_dwordx4 v153, s[28:31], s78 offen nt lds\n"
    "s_add_u32 s78, s88, 0x1800\n"
    "s_add_u32 m0, s71, 0x6800\n"
    "s_nop 0\n"
    "buffer_load_dwordx4 v154, s[28:31], s78 offen nt lds\n"
    "s_barrier\n"
    "ds_read_u16_d16_hi v163, v209 offset:6528\n"
    "ds_read_u16_d16_hi v162, v209 offset:384\n"
    "ds_read_b128 v[72:75], v205 offset:49152\n"
    "ds_read_b128 v[76:79], v206 offset:49152\n"
    "ds_read_b64 v[170:171], v210 offset:64\n"
    "ds_read2st64_b32 v[80:81], v207 offset0:192 offset1:195\n"
    "ds_read2st64_b32 v[82:83], v207 offset0:198 offset1:201\n"
    "ds_read2st64_b32 v[84:85], v207 offset0:204 offset1:207\n"
    "ds_read2st64_b32 v[86:87], v207 offset0:210 offset1:213\n"
    "s_waitcnt lgkmcnt(8)\n"
    "v_mul_f32 v168, s75, v163\n"
    "s_waitcnt lgkmcnt(7)\n"
    "v_fma_f32 v169, -v162, v168, v217\n"
    "s_waitcnt lgkmcnt(6)\n"
    "v_cvt_f32_i32 v72, v72\n"
    "v_cvt_f32_i32 v73, v73\n"
    "v_cvt_f32_i32 v74, v74\n"
    "v_cvt_f32_i32 v75, v75\n"
    "s_waitcnt lgkmcnt(5)\n"
    "v_cvt_f32_i32 v76, v76\n"
    "v_cvt_f32_i32 v77, v77\n"
    "v_cvt_f32_i32 v78, v78\n"
    "v_cvt_f32_i32 v79, v79\n"
    "v_mul_f32_dpp v72, v168, v72 row_newbcast:0 row_mask:0xf bank_mask:0xf\n"
    "v_mul_f32_dpp v73, v168, v73 row_newbcast:1 row_mask:0xf bank_mask:0xf\n"
    "v_mul_f32_dpp v74, v168, v74 row_newbcast:2 row_mask:0xf bank_mask:0xf\n"
    "v_mul_f32_dpp v75, v168, v75 row_newbcast:3 row_mask:0xf bank_mask:0xf\n"
    "v_mul_f32_dpp v76, v168, v76 row_newbcast:4 row_mask:0xf bank_mask:0xf\n"
    "v_mul_f32_dpp v77, v168, v77 row_newbcast:5 row_mask:0xf bank_mask:0xf\n"
    "v_mul_f32_dpp v78, v168, v78 row_newbcast:6 row_mask:0xf bank_mask:0xf\n"
    "v_mul_f32_dpp v79, v168, v79 row_newbcast:7 row_mask:0xf bank_mask:0xf\n"
    "v_add_f32_dpp v72, v169, v72 row_newbcast:0 row_mask:0xf bank_mask:0xf\n"
    "v_add_f32_dpp v73, v169, v73 row_newbcast:1 row_mask:0xf bank_mask:0xf\n"
    "v_add_f32_dpp v74, v169, v74 row_newbcast:2 row_mask:0xf bank_mask:0xf\n"
    "v_add_f32_dpp v75, v169, v75 row_newbcast:3 row_mask:0xf bank_mask:0xf\n"
    "v_add_f32_dpp v76, v169, v76 row_newbcast:4 row_mask:0xf bank_mask:0xf\n"
    "v_add_f32_dpp v77, v169, v77 row_newbcast:5 row_mask:0xf bank_mask:0xf\n"
    "v_add_f32_dpp v78, v169, v78 row_newbcast:6 row_mask:0xf bank_mask:0xf\n"
    "v_add_f32_dpp v79, v169, v79 row_newbcast:7 row_mask:0xf bank_mask:0xf\n"
    "v_cvt_pk_bf16_f32 v72, v72, v73\n"
    "v_cvt_pk_bf16_f32 v73, v74, v75\n"
    "v_cvt_pk_bf16_f32 v74, v76, v77\n"
    "v_cvt_pk_bf16_f32 v75, v78, v79\n"
    "s_add_u32 s78, s87, 0x6000\n"
    "buffer_store_dwordx4 v[72:75], v211, s[36:39], s78 offen sc1\n"
    "s_waitcnt lgkmcnt(3)\n"
    "v_cvt_f32_i32 v80, v80\n"
    "v_cvt_f32_i32 v81, v81\n"
    "s_waitcnt lgkmcnt(2)\n"
    "v_cvt_f32_i32 v82, v82\n"
    "v_cvt_f32_i32 v83, v83\n"
    "s_waitcnt lgkmcnt(1)\n"
    "v_cvt_f32_i32 v84, v84\n"
    "v_cvt_f32_i32 v85, v85\n"
    "s_waitcnt lgkmcnt(0)\n"
    "v_cvt_f32_i32 v86, v86\n"
    "v_cvt_f32_i32 v87, v87\n"
    "v_mul_f32_dpp v80, v170, v80 row_newbcast:0 row_mask:0xf bank_mask:0xf\n"
    "v_mul_f32_dpp v81, v170, v81 row_newbcast:1 row_mask:0xf bank_mask:0xf\n"
    "v_mul_f32_dpp v82, v170, v82 row_newbcast:2 row_mask:0xf bank_mask:0xf\n"
    "v_mul_f32_dpp v83, v170, v83 row_newbcast:3 row_mask:0xf bank_mask:0xf\n"
    "v_mul_f32_dpp v84, v170, v84 row_newbcast:4 row_mask:0xf bank_mask:0xf\n"
    "v_mul_f32_dpp v85, v170, v85 row_newbcast:5 row_mask:0xf bank_mask:0xf\n"
    "v_mul_f32_dpp v86, v170, v86 row_newbcast:6 row_mask:0xf bank_mask:0xf\n"
    "v_mul_f32_dpp v87, v170, v87 row_newbcast:7 row_mask:0xf bank_mask:0xf\n"
    "v_add_f32_dpp v80, v171, v80 row_newbcast:0 row_mask:0xf bank_mask:0xf\n"
    "v_add_f32_dpp v81, v171, v81 row_newbcast:1 row_mask:0xf bank_mask:0xf\n"
    "v_add_f32_dpp v82, v171, v82 row_newbcast:2 row_mask:0xf bank_mask:0xf\n"
    "v_add_f32_dpp v83, v171, v83 row_newbcast:3 row_mask:0xf bank_mask:0xf\n"
    "v_add_f32_dpp v84, v171, v84 row_newbcast:4 row_mask:0xf bank_mask:0xf\n"
    "v_add_f32_dpp v85, v171, v85 row_newbcast:5 row_mask:0xf bank_mask:0xf\n"
    "v_add_f32_dpp v86, v171, v86 row_newbcast:6 row_mask:0xf bank_mask:0xf\n"
    "v_add_f32_dpp v87, v171, v87 row_newbcast:7 row_mask:0xf bank_mask:0xf\n"
    "v_cvt_pk_bf16_f32 v80, v80, v81\n"
    "v_cvt_pk_bf16_f32 v81, v82, v83\n"
    "v_cvt_pk_bf16_f32 v82, v84, v85\n"
    "v_cvt_pk_bf16_f32 v83, v86, v87\n"
    "s_mov_b32 s78, 0x6000\n"
    "buffer_store_dwordx4 v[80:83], v212, s[40:43], s78 offen sc1\n"
    "buffer_load_dwordx2 v[218:219], v213, s[44:47], 0 offen sc1\n"
    "s_add_u32 s78, s88, 0x1800\n"
    "s_add_u32 m0, s71, 0x6c00\n"
    "s_nop 0\n"
    "buffer_load_dwordx4 v155, s[28:31], s78 offen nt lds\n"
    "s_add_u32 s78, s88, 0x1800\n"
    "s_add_u32 m0, s71, 0x7000\n"
    "s_nop 0\n"
    "buffer_load_dwordx4 v156, s[28:31], s78 offen nt lds\n"
    "s_add_u32 s78, s88, 0x1800\n"
    "s_add_u32 m0, s71, 0x7400\n"
    "s_nop 0\n"
    "buffer_load_dwordx4 v157, s[28:31], s78 offen nt lds\n"
    "s_waitcnt vmcnt(9)\n"
    "ds_read_b128 a[192:195], v158\n"
    "ds_read_b128 a[196:199], v159\n"
    "ds_read_b128 a[200:203], v158 offset:128\n"
    "ds_read_b128 a[204:207], v159 offset:128\n"
    "ds_read_b128 a[208:211], v158 offset:256\n"
    "ds_read_b128 a[212:215], v159 offset:256\n"
    "s_waitcnt vmcnt(3)\n"
    "v_cmp_ne_u32 s[80:81], s67, v219\n"
    "v_cmp_gt_u32 vcc, s66, v218\n"
    "s_or_b64 vcc, vcc, s[80:81]\n"
    "s_cbranch_vccnz LSPRE0\n"
    "LSPB0:\n"
    "s_waitcnt lgkmcnt(0)\n"
    "s_barrier\n"
    "v_mov_b32 v220, 3\n"
    "buffer_store_dwordx2 v[220:221], v214, s[44:47], 0 offen sc1\n"
    "s_add_u32 m0, s72, 0x1e000\n"
    "s_nop 0\n"
    "buffer_load_dwordx4 v200, s[36:39], s72 offen sc1 lds\n"
    "s_add_u32 m0, s72, 0x1e000\n"
    "s_nop 0\n"
    "buffer_load_dwordx4 v200, s[36:39], s72 offen offset:1024 sc1 lds\n"
    "s_add_u32 m0, s72, 0x1e000\n"
    "s_nop 0\n"
    "buffer_load_dwordx4 v200, s[36:39], s72 offen offset:2048 sc1 lds\n"
    "s_add_u32 s65, s71, 0x3000\n"
    "s_add_u32 m0, s71, 0\n"
    "s_nop 0\n"
    "buffer_load_dwordx4 v200, s[36:39], s65 offen sc1 lds\n"
    "s_add_u32 s65, s71, 0x3400\n"
    "s_add_u32 m0, s71, 0x400\n"
    "s_nop 0\n"
    "buffer_load_dwordx4 v200, s[36:39], s65 offen sc1 lds\n"
    "s_add_u32 s65, s71, 0x3800\n"
    "s_add_u32 m0, s71, 0x800\n"
    "s_nop 0\n"
    "buffer_load_dwordx4 v200, s[36:39], s65 offen sc1 lds\n"
    "s_add_u32 s65, s71, 0x3c00\n"
    "s_add_u32 m0, s71, 0xc00\n"
    "s_nop 0\n"
    "buffer_load_dwordx4 v200, s[36:39], s65 offen sc1 lds\n"
    "s_add_u32 s65, s71, 0x4000\n"
    "s_add_u32 m0, s71, 0x1000\n"
    "s_nop 0\n"
    "buffer_load_dwordx4 v200, s[36:39], s65 offen sc1 lds\n"
    "s_add_u32 s65, s71, 0x4400\n"
    "s_add_u32 m0, s71, 0x1400\n"
    "s_nop 0\n"
    "buffer_load_dwordx4 v200, s[36:39], s65 offen sc1 lds\n"
    "s_add_u32 s65, s71, 0\n"
    "s_add_u32 m0, s71, 0x12000\n"
    "s_nop 0\n"
    "buffer_load_dwordx4 v200, s[40:43], s65 offen sc1 lds\n"
    "s_add_u32 s65, s71, 0x400\n"
    "s_add_u32 m0, s71, 0x12400\n"
    "s_nop 0\n"
    "buffer_load_dwordx4 v200, s[40:43], s65 offen sc1 lds\n"
    "s_add_u32 s65, s71, 0x800\n"
    "s_add_u32 m0, s71, 0x12800\n"
    "s_nop 0\n"
    "buffer_load_dwordx4 v200, s[40:43], s65 offen sc1 lds\n"
    "s_add_u32 s65, s71, 0xc00\n"
    "s_add_u32 m0, s71, 0x12c00\n"
    "s_nop 0\n"
    "buffer_load_dwordx4 v200, s[40:43], s65 offen sc1 lds\n"
    "s_add_u32 s65, s71, 0x1000\n"
    "s_add_u32 m0, s71, 0x13000\n"
    "s_nop 0\n"
    "buffer_load_dwordx4 v200, s[40:43], s65 offen sc1 lds\n"
    "s_add_u32 s65, s71, 0x1400\n"
    "s_add_u32 m0, s71, 0x13400\n"
    "s_nop 0\n"
    "buffer_load_dwordx4 v200, s[40:43], s65 offen sc1 lds\n"
    "s_add_u32 s78, s64, 0x24000\n"
    "s_add_u32 m0, s72, 0x18000\n"
    "s_nop 0\n"
    "buffer_load_dwordx4 v202, s[32:35], s78 offen nt lds\n"
    "buffer_load_dwordx4 v203, s[32:35], s78 offen offset:1024 nt lds\n"
    "buffer_load_dwordx4 v204, s[32:35], s78 offen offset:2048 nt lds\n"
    "s_add_u32 s78, s88, 0x3000\n"
    "s_add_u32 m0, s71, 0xc000\n"
    "s_nop 0\n"
    "buffer_load_dwordx4 v152, s[28:31], s78 offen nt lds\n"
    "s_mul_i32 s66, s74, 3\n"
    "s_waitcnt vmcnt(20)\n"
    "ds_read_b128 a[216:219], v158 offset:24576\n"
    "ds_read_b128 a[220:223], v159 offset:24576\n"
    "ds_read_b128 a[224:227], v158 offset:24704\n"
    "ds_read_b128 a[228:231], v159 offset:24704\n"
    "ds_read_b128 a[232:235], v158 offset:24832\n"
    "ds_read_b128 a[236:239], v159 offset:24832\n"
    "s_add_u32 m0, s72, 0x21000\n"
    "s_nop 0\n"
    "buffer_load_dwordx4 v249, s[48:51], 0 offen offset:1024 lds\n"
    "buffer_load_dwordx2 v[218:219], v213, s[44:47], 0 offen sc1\n"
    "v_add_u32 v255, 0x1e000, v200\n"
    "s_waitcnt vmcnt(12) lgkmcnt(0)\n"
    "s_add_u32 s60, s71, 0x9000\n"
    "s_add_u32 s61, s71, 0x6000\n"
    "s_add_u32 s62, s87, 0x12000\n"
    "s_mov_b32 s63, 0x12000\n"
    "s_mov_b32 s59, 0\n"
    "s_mul_i32 s66, s74, 3\n"
    "s_mul_i32 s90, s58, 0x1c00\n"
    "s_sub_u32 s90, s90, 0x1000\n"
    "s_sub_u32 s92, s90, s71\n"
    "s_barrier\n"
    "ds_read_b128 v[160:163], v255\n"
    "ds_read_b128 v[164:167], v255 offset:1024\n"
    "ds_read_b128 v[168:171], v255 offset:2048\n"
    "ds_read_b128 v[172:175], v255 offset:3072\n"
    "ds_read_b128 v[176:179], v255 offset:4096\n"
    "ds_read_b128 v[180:183], v255 offset:5120\n"
    "s_add_u32 s76, s87, 0xc000\n"
    "s_mov_b32 s77, 0xc000\n"
    "s_waitcnt lgkmcnt(4)\n"
    "v_mfma_f32_16x16x32_bf16 v[40:43], v[160:163], a[192:195], 0\n"
    "ds_read_b128 v[184:187], v255 offset:6144\n"
    "ds_read_b64 v[228:229], v210 offset:128\n"
    "v_mfma_f32_16x16x32_bf16 v[40:43], v[164:167], a[196:199], v[40:43]\n"
    "s_add_u32 s78, s88, 0x3000\n"
    "ds_read2st64_b32 v[240:241], v207 offset0:144 offset1:147\n"
    "ds_read2st64_b32 v[242:243], v207 offset0:150 offset1:153\n"
    "v_mfma_f32_16x16x32_bf16 v[44:47], v[160:163], a[216:219], 0\n"
    "s_add_u32 m0, s71, 0xc400\n"
    "ds_read_b128 v[188:191], v255 offset:7168\n"
    "ds_read2st64_b32 v[244:245], v207 offset0:156 offset1:159\n"
    "v_mfma_f32_16x16x32_bf16 v[44:47], v[164:167], a[220:223], v[44:47]\n"
    "s_waitcnt lgkmcnt(8)\n"
    "buffer_load_dwordx4 v153, s[28:31], s78 offen nt lds\n"
    "v_mfma_f32_16x16x32_bf16 v[40:43], v[168:171], a[200:203], v[40:43]\n"
    "ds_read_b128 v[160:163], v255 offset:8192\n"
    "ds_read2st64_b32 v[246:247], v207 offset0:162 offset1:165\n"
    "v_mfma_f32_16x16x32_bf16 v[40:43], v[172:175], a[204:207], v[40:43]\n"
    "ds_read_u16_d16_hi v223, v209 offset:6720\n"
    "ds_read_u16_d16_hi v222, v209 offset:576\n"
    "v_mfma_f32_16x16x32_bf16 v[44:47], v[168:171], a[224:227], v[44:47]\n"
    "ds_read_b128 v[164:167], v255 offset:9216\n"
    "ds_read_b128 v[232:235], v205 offset:36864\n"
    "v_mfma_f32_16x16x32_bf16 v[44:47], v[172:175], a[228:231], v[44:47]\n"
    "s_waitcnt lgkmcnt(12)\n"
    "ds_read_b128 v[236:239], v206 offset:36864\n"
    "v_mfma_f32_16x16x32_bf16 v[40:43], v[176:179], a[208:211], v[40:43]\n"
    "ds_read_b128 v[168:171], v255 offset:10240\n"
    "v_mfma_f32_16x16x32_bf16 v[40:43], v[180:183], a[212:215], v[40:43]\n"
    "v_mfma_f32_16x16x32_bf16 v[44:47], v[176:179], a[232:235], v[44:47]\n"
    "ds_read_b128 v[172:175], v255 offset:11264\n"
    "v_mfma_f32_16x16x32_bf16 v[44:47], v[180:183], a[236:239], v[44:47]\n"
    "s_waitcnt lgkmcnt(10)\n"
    "s_add_u32 s78, s88, 0x3000\n"
    "v_cvt_f32_i32 v240, v240\n"
    "v_mfma_f32_16x16x32_bf16 v[56:59], v[184:187], a[192:195], 0\n"
    "s_add_u32 m0, s71, 0xc800\n"
    "ds_read_b128 v[176:179], v200\n"
    "v_cvt_f32_i32 v241, v241\n"
    "v_mfma_f32_16x16x32_bf16 v[56:59], v[188:191], a[196:199], v[56:59]\n"
    "buffer_load_dwordx4 v154, s[28:31], s78 offen nt lds\n"
    "v_mfma_f32_16x16x32_bf16 v[60:63], v[184:187], a[216:219], 0\n"
    "ds_read_b128 v[180:183], v200 offset:1024\n"
    "v_cvt_f32_i32 v242, v242\n"
    "v_mfma_f32_16x16x32_bf16 v[60:63], v[188:191], a[220:223], v[60:63]\n"
    "s_waitcnt lgkmcnt(6)\n"
    "v_cvt_f32_i32 v243, v243\n"
    "v_cvt_f32_i32 v244, v244\n"
    "v_mfma_f32_16x16x32_bf16 v[56:59], v[160:163], a[200:203], v[56:59]\n"
    "ds_read_b128 v[184:187], v200 offset:2048\n"
    "v_cvt_f32_i32 v245, v245\n"
    "v_mfma_f32_16x16x32_bf16 v[56:59], v[164:167], a[204:207], v[56:59]\n"
    "v_cvt_f32_i32 v246, v246\n"
    "v_mul_f32 v226, s75, v223\n"
    "v_mfma_f32_16x16x32_bf16 v[60:63], v[160:163], a[224:227], v[60:63]\n"
    "ds_read_b128 v[188:191], v200 offset:3072\n"
    "v_cvt_f32_i32 v247, v247\n"
    "v_mfma_f32_16x16x32_bf16 v[60:63], v[164:167], a[228:231], v[60:63]\n"
    "s_waitcnt lgkmcnt(4)\n"
    "v_fma_f32 v227, -v222, v226, v217\n"
    "v_mul_f32_dpp v240, v228, v240 row_newbcast:0 row_mask:0xf bank_mask:0xf\n"
    "v_mfma_f32_16x16x32_bf16 v[56:59], v[168:171], a[208:211], v[56:59]\n"
    "ds_read_b128 v[160:163], v200 offset:4096\n"
    "v_cvt_f32_i32 v232, v232\n"
    "v_mfma_f32_16x16x32_bf16 v[56:59], v[172:175], a[212:215], v[56:59]\n"
    "s_add_u32 s78, s88, 0x3000\n"
    "v_mul_f32_dpp v241, v228, v241 row_newbcast:1 row_mask:0xf bank_mask:0xf\n"
    "v_cvt_f32_i32 v233, v233\n"
    "v_mfma_f32_16x16x32_bf16 v[60:63], v[168:171], a[232:235], v[60:63]\n"
    "s_add_u32 m0, s71, 0xcc00\n"
    "ds_read_b128 v[164:167], v200 offset:5120\n"
    "v_mul_f32_dpp v242, v228, v242 row_newbcast:2 row_mask:0xf bank_mask:0xf\n"
    "v_mfma_f32_16x16x32_bf16 v[60:63], v[172:175], a[236:239], v[60:63]\n"
    "s_waitcnt lgkmcnt(4)\n"
    "buffer_load_dwordx4 v155, s[28:31], s78 offen nt lds\n"
    "v_mfma_f32_16x16x32_bf16 v[64:67], v[176:179], a[192:195], 0\n"
    "ds_read_b128 v[168:171], v200 offset:6144\n"
    "v_cvt_f32_i32 v234, v234\n"
    "v_mul_f32_dpp v243, v228, v243 row_newbcast:3 row_mask:0xf bank_mask:0xf\n"
    "v_cvt_f32_i32 v235, v235\n"
    "v_mfma_f32_16x16x32_bf16 v[64:67], v[180:183], a[196:199], v[64:67]\n"
    "v_mul_f32_dpp v244, v228, v244 row_newbcast:4 row_mask:0xf bank_mask:0xf\n"
    "v_cvt_f32_i32 v236, v236\n"
    "v_mul_f32_dpp v245, v228, v245 row_newbcast:5 row_mask:0xf bank_mask:0xf\n"
    "v_cvt_f32_i32 v237, v237\n"
    "v_mfma_f32_16x16x32_bf16 v[68:71], v[176:179], a[216:219], 0\n"
    "ds_read_b128 v[172:175], v200 offset:7168\n"
    "v_mul_f32_dpp v246, v228, v246 row_newbcast:6 row_mask:0xf bank_mask:0xf\n"
    "v_cvt_f32_i32 v238, v238\n"
    "v_mul_f32_dpp v247, v228, v247 row_newbcast:7 row_mask:0xf bank_mask:0xf\n"
    "v_mfma_f32_16x16x32_bf16 v[68:71], v[180:183], a[220:223], v[68:71]\n"
    "s_waitcnt lgkmcnt(4)\n"
    "v_cvt_f32_i32 v239, v239\n"
    "v_add_f32_dpp v240, v229, v240 row_newbcast:0 row_mask:0xf bank_mask:0xf\n"
    "v_mul_f32_dpp v232, v226, v232 row_newbcast:0 row_mask:0xf bank_mask:0xf\n"
    "v_add_f32_dpp v241, v229, v241 row_newbcast:1 row_mask:0xf bank_mask:0xf\n"
    "v_mfma_f32_16x16x32_bf16 v[64:67], v[184:187], a[200:203], v[64:67]\n"
    "ds_read_b128 v[176:179], v200 offset:8192\n"
    "v_mul_f32_dpp v233, v226, v233 row_newbcast:1 row_mask:0xf bank_mask:0xf\n"
    "v_add_f32_dpp v242, v229, v242 row_newbcast:2 row_mask:0xf bank_mask:0xf\n"
    "v_mul_f32_dpp v234, v226, v234 row_newbcast:2 row_mask:0xf bank_mask:0xf\n"
    "v_mfma_f32_16x16x32_bf16 v[64:67], v[188:191], a[204:207], v[64:67]\n"
    "v_add_f32_dpp v243, v229, v243 row_newbcast:3 row_mask:0xf bank_mask:0xf\n"
    "v_mul_f32_dpp v235, v226, v235 row_newbcast:3 row_mask:0xf bank_mask:0xf\n"
    "v_add_f32_dpp v244, v229, v244 row_newbcast:4 row_mask:0xf bank_mask:0xf\n"
    "v_mul_f32_dpp v236, v226, v236 row_newbcast:4 row_mask:0xf bank_mask:0xf\n"
    "v_mfma_f32_16x16x32_bf16 v[68:71], v[184:187], a[224:227], v[68:71]\n"
    "ds_read_b128 v[180:183], v200 offset:9216\n"
    "v_add_f32_dpp v245, v229, v245 row_newbcast:5 row_mask:0xf bank_mask:0xf\n"
    "v_mul_f32_dpp v237, v226, v237 row_newbcast:5 row_mask:0xf bank_mask:0xf\n"
    "v_add_f32_dpp v246, v229, v246 row_newbcast:6 row_mask:0xf bank_mask:0xf\n"
    "v_mul_f32_dpp v238, v226, v238 row_newbcast:6 row_mask:0xf bank_mask:0xf\n"
    "v_add_f32_dpp v247, v229, v247 row_newbcast:7 row_mask:0xf bank_mask:0xf\n"
    "v_mul_f32_dpp v239, v226, v239 row_newbcast:7 row_mask:0xf bank_mask:0xf\n"
    "v_cvt_pk_bf16_f32 v240, v240, v241\n"
    "v_add_f32_dpp v232, v227, v232 row_newbcast:0 row_mask:0xf bank_mask:0xf\n"
    "v_cvt_pk_bf16_f32 v241, v242, v243\n"
    "v_add_f32_dpp v233, v227, v233 row_newbcast:1 row_mask:0xf bank_mask:0xf\n"
    "v_cvt_pk_bf16_f32 v242, v244, v245\n"
    "v_add_f32_dpp v234, v227, v234 row_newbcast:2 row_mask:0xf bank_mask:0xf\n"
    "v_cvt_pk_bf16_f32 v243, v246, v247\n"
    "v_add_f32_dpp v235, v227, v235 row_newbcast:3 row_mask:0xf bank_mask:0xf\n"
    "v_add_f32_dpp v236, v227, v236 row_newbcast:4 row_mask:0xf bank_mask:0xf\n"
    "v_add_f32_dpp v237, v227, v237 row_newbcast:5 row_mask:0xf bank_mask:0xf\n"
    "v_add_f32_dpp v238, v227, v238 row_newbcast:6 row_mask:0xf bank_mask:0xf\n"
    "v_add_f32_dpp v239, v227, v239 row_newbcast:7 row_mask:0xf bank_mask:0xf\n"
    "v_cvt_pk_bf16_f32 v232, v232, v233\n"
    "v_cvt_pk_bf16_f32 v233, v234, v235\n"
    "v_cvt_pk_bf16_f32 v234, v236, v237\n"
    "v_cvt_pk_bf16_f32 v235, v238, v239\n"
    "v_mfma_f32_16x16x32_bf16 v[68:71], v[188:191], a[228:231], v[68:71]\n"
    "s_waitcnt lgkmcnt(4)\n"
    "s_add_u32 s78, s88, 0x3000\n"
    "v_max3_f32 v196, v40, v41, v42\n"
    "v_max3_f32 v196, v196, v43, v56\n"
    "v_max3_f32 v196, v196, v57, v58\n"
    "buffer_store_dwordx4 v[232:235], v211, s[36:39], s76 offen sc1\n"
    "v_mfma_f32_16x16x32_bf16 v[64:67], v[160:163], a[208:211], v[64:67]\n"
    "s_add_u32 m0, s71, 0xd000\n"
    "ds_read_b128 v[184:187], v200 offset:10240\n"
    "v_max_f32 v196, v196, v59\n"
    "v_max3_f32 v198, v44, v45, v46\n"
    "v_mov_b32 v197, v196\n"
    "v_mfma_f32_16x16x32_bf16 v[64:67], v[164:167], a[212:215], v[64:67]\n"
    "buffer_load_dwordx4 v156, s[28:31], s78 offen nt lds\n"
    "v_max3_f32 v198, v198, v47, v60\n"
    "v_permlane32_swap_b32 v196, v197\n"
    "v_mfma_f32_16x16x32_bf16 v[68:71], v[160:163], a[232:235], v[68:71]\n"
    "ds_read_b128 v[188:191], v200 offset:11264\n"
    "v_max3_f32 v198, v198, v61, v62\n"
    "v_max_f32 v196, v196, v197\n"
    "v_max_f32 v198, v198, v63\n"
    "v_mfma_f32_16x16x32_bf16 v[68:71], v[164:167], a[236:239], v[68:71]\n"
    "s_waitcnt lgkmcnt(4)\n"
    "v_mov_b32 v197, v196\n"
    "v_mov_b32 v199, v198\n"
    "s_nop 0\n"
    "v_permlane16_swap_b32 v196, v197\n"
    "v_permlane32_swap_b32 v198, v199\n"
    "v_mfma_f32_16x16x32_bf16 v[80:83], v[168:171], a[192:195], 0\n"
    "ds_read_b128 v[160:163], v200 offset:12288\n"
    "v_max_f32 v196, v196, v197\n"
    "v_max_f32 v198, v198, v199\n"
    "v_add_f32 v196, 0x42640000, v196\n"
    "v_mfma_f32_16x16x32_bf16 v[80:83], v[172:175], a[196:199], v[80:83]\n"
    "buffer_store_dwordx4 v[240:243], v212, s[40:43], s77 offen sc1\n"
    "v_mov_b32 v199, v198\n"
    "v_sub_f32 v96, 0, v196\n"
    "s_nop 0\n"
    "v_permlane16_swap_b32 v198, v199\n"
    "v_sub_f32 v97, 0, v196\n"
    "v_max_f32 v198, v198, v199\n"
    "v_mfma_f32_16x16x32_bf16 v[84:87], v[168:171], a[216:219], 0\n"
    "ds_read_b128 v[164:167], v200 offset:13312\n"
    "v_sub_f32 v98, 0, v196\n"
    "v_add_f32 v198, 0x42640000, v198\n"
    "v_sub_f32 v99, 0, v196\n"
    "v_mfma_f32_16x16x32_bf16 v[84:87], v[172:175], a[220:223], v[84:87]\n"
    "s_waitcnt lgkmcnt(4)\n"
    "v_sub_f32 v100, 0, v198\n"
    "v_add_f32 v40, v40, v96\n"
    "v_sub_f32 v101, 0, v198\n"
    "v_add_f32 v41, v41, v96\n"
    "v_mfma_f32_16x16x32_bf16 v[80:83], v[176:179], a[200:203], v[80:83]\n"
    "ds_read_b128 v[168:171], v200 offset:14336\n"
    "v_sub_f32 v102, 0, v198\n"
    "v_add_f32 v42, v42, v96\n"
    "v_sub_f32 v103, 0, v198\n"
    "v_mfma_f32_16x16x32_bf16 v[80:83], v[180:183], a[204:207], v[80:83]\n"
    "s_add_u32 s78, s88, 0x3000\n"
    "v_add_f32 v43, v43, v96\n"
    "v_add_f32 v44, v44, v100\n"
    "v_add_f32 v56, v56, v96\n"
    "v_add_f32 v45, v45, v100\n"
    "v_mfma_f32_16x16x32_bf16 v[84:87], v[176:179], a[224:227], v[84:87]\n"
    "s_add_u32 m0, s71, 0xd400\n"
    "ds_read_b128 v[172:175], v200 offset:15360\n"
    "v_add_f32 v57, v57, v96\n"
    "v_add_f32 v46, v46, v100\n"
    "v_add_f32 v58, v58, v96\n"
    "v_mfma_f32_16x16x32_bf16 v[84:87], v[180:183], a[228:231], v[84:87]\n"
    "s_waitcnt lgkmcnt(4)\n"
    "buffer_load_dwordx4 v157, s[28:31], s78 offen nt lds\n"
    "v_add_f32 v47, v47, v100\n"
    "v_add_f32 v59, v59, v96\n"
    "v_mfma_f32_16x16x32_bf16 v[80:83], v[184:187], a[208:211], v[80:83]\n"
    "ds_read_b128 v[176:179], v200 offset:16384\n"
    "v_add_f32 v60, v60, v100\n"
    "v_exp_f32 v40, v40\n"
    "v_mfma_f32_16x16x32_bf16 v[80:83], v[188:191], a[212:215], v[80:83]\n"
    "v_add_f32 v61, v61, v100\n"
    "v_add_f32 v62, v62, v100\n"
    "v_exp_f32 v41, v41\n"
    "v_mfma_f32_16x16x32_bf16 v[84:87], v[184:187], a[232:235], v[84:87]\n"
    "ds_read_b128 v[180:183], v200 offset:17408\n"
    "v_add_f32 v63, v63, v100\n"
    "v_exp_f32 v44, v44\n"
    "v_mfma_f32_16x16x32_bf16 v[84:87], v[188:191], a[236:239], v[84:87]\n"
    "s_waitcnt lgkmcnt(4)\n"
    "v_exp_f32 v42, v42\n"
    "v_exp_f32 v45, v45\n"
    "v_mfma_f32_16x16x32_bf16 v[32:35], v[160:163], a[192:195], v[96:99]\n"
    "ds_read_b128 v[184:187], v200 offset:18432\n"
    "v_exp_f32 v43, v43\n"
    "v_mfma_f32_16x16x32_bf16 v[32:35], v[164:167], a[196:199], v[32:35]\n"
    "v_exp_f32 v46, v46\n"
    "v_exp_f32 v56, v56\n"
    "v_mfma_f32_16x16x32_bf16 v[36:39], v[160:163], a[216:219], v[100:103]\n"
    "ds_read_b128 v[188:191], v200 offset:19456\n"
    "v_exp_f32 v47, v47\n"
    "v_mfma_f32_16x16x32_bf16 v[36:39], v[164:167], a[220:223], v[36:39]\n"
    "s_waitcnt lgkmcnt(4)\n"
    "s_add_u32 s78, s88, 0x4800\n"
    "v_exp_f32 v57, v57\n"
    "v_mfma_f32_16x16x32_bf16 v[32:35], v[168:171], a[200:203], v[32:35]\n"
    "s_add_u32 m0, s71, 0x6000\n"
    "ds_read_b128 v[160:163], v200 offset:20480\n"
    "v_exp_f32 v60, v60\n"
    "v_mfma_f32_16x16x32_bf16 v[32:35], v[172:175], a[204:207], v[32:35]\n"
    "buffer_load_dwordx4 v152, s[28:31], s78 offen nt lds\n"
    "v_exp_f32 v58, v58\n"
    "v_mfma_f32_16x16x32_bf16 v[36:39], v[168:171], a[224:227], v[36:39]\n"
    "ds_read_b128 v[164:167], v200 offset:21504\n"
    "v_exp_f32 v61, v61\n"
    "v_mfma_f32_16x16x32_bf16 v[36:39], v[172:175], a[228:231], v[36:39]\n"
    "s_waitcnt lgkmcnt(4)\n"
    "v_exp_f32 v59, v59\n"
    "v_exp_f32 v62, v62\n"
    "v_mfma_f32_16x16x32_bf16 v[32:35], v[176:179], a[208:211], v[32:35]\n"
    "ds_read_b128 v[168:171], v200 offset:22528\n"
    "v_cvt_pk_bf16_f32 v128, v40, v41\n"
    "v_cvt_pk_bf16_f32 v129, v42, v43\n"
    "v_cvt_pk_bf16_f32 v130, v56, v57\n"
    "v_mfma_f32_16x16x32_bf16 v[32:35], v[180:183], a[212:215], v[32:35]\n"
    "v_exp_f32 v63, v63\n"
    "v_cvt_pk_bf16_f32 v131, v58, v59\n"
    "v_cvt_pk_bf16_f32 v132, v44, v45\n"
    "v_mfma_f32_16x16x32_bf16 v[36:39], v[176:179], a[232:235], v[36:39]\n"
    "ds_read_b128 v[172:175], v200 offset:23552\n"
    "v_add_f32 v64, v64, v96\n"
    "v_cvt_pk_bf16_f32 v133, v46, v47\n"
    "v_add_f32 v65, v65, v96\n"
    "v_mfma_f32_16x16x32_bf16 v[36:39], v[180:183], a[236:239], v[36:39]\n"
    "s_waitcnt lgkmcnt(4)\n"
    "v_cvt_pk_bf16_f32 v134, v60, v61\n"
    "v_add_f32 v66, v66, v96\n"
    "s_waitcnt vmcnt(11)\n"
    "s_barrier\n"
    "v_mfma_f32_16x16x32_bf16 v[48:51], v[184:187], a[192:195], v[96:99]\n"
    "ds_read_b128 v[176:179], v201 offset:24576\n"
    "v_cvt_pk_bf16_f32 v135, v62, v63\n"
    "v_add_f32 v67, v67, v96\n"
    "v_add_f32 v68, v68, v100\n"
    "v_mfma_f32_16x16x32_bf16 v[48:51], v[188:191], a[196:199], v[48:51]\n"
    "s_add_u32 s78, s88, 0x4800\n"
    "v_add_f32 v80, v80, v96\n"
    "v_add_f32 v69, v69, v100\n"
    "v_add_f32 v81, v81, v96\n"
    "v_add_f32 v70, v70, v100\n"
    "v_mfma_f32_16x16x32_bf16 v[52:55], v[184:187], a[216:219], v[100:103]\n"
    "s_add_u32 m0, s71, 0x6400\n"
    "ds_read_b128 v[180:183], v201 offset:25600\n"
    "v_add_f32 v82, v82, v96\n"
    "v_add_f32 v71, v71, v100\n"
    "v_add_f32 v83, v83, v96\n"
    "v_mfma_f32_16x16x32_bf16 v[52:55], v[188:191], a[220:223], v[52:55]\n"
    "s_waitcnt lgkmcnt(4)\n"
    "buffer_load_dwordx4 v153, s[28:31], s78 offen nt lds\n"
    "v_add_f32 v84, v84, v100\n"
    "v_add_f32 v85, v85, v100\n"
    "v_mfma_f32_16x16x32_bf16 v[48:51], v[160:163], a[200:203], v[48:51]\n"
    "ds_read_b128 v[184:187], v201 offset:26624\n"
    "v_exp_f32 v64, v64\n"
    "v_add_f32 v86, v86, v100\n"
    "v_mfma_f32_16x16x32_bf16 v[48:51], v[164:167], a[204:207], v[48:51]\n"
    "v_exp_f32 v65, v65\n"
    "v_add_f32 v87, v87, v100\n"
    "ds_read_b64 v[228:229], v210 offset:192\n"
    "v_mfma_f32_16x16x32_bf16 v[52:55], v[160:163], a[224:227], v[52:55]\n"
    "ds_read_b128 v[188:191], v201 offset:27648\n"
    "v_exp_f32 v68, v68\n"
    "ds_read2st64_b32 v[240:241], v207 offset0:96 offset1:99\n"
    "v_mfma_f32_16x16x32_bf16 v[52:55], v[164:167], a[228:231], v[52:55]\n"
    "s_waitcnt lgkmcnt(6)\n"
    "v_exp_f32 v66, v66\n"
    "v_exp_f32 v69, v69\n"
    "v_mfma_f32_16x16x32_bf16 v[48:51], v[168:171], a[208:211], v[48:51]\n"
    "ds_read_b128 v[160:163], v201 offset:28672\n"
    "v_exp_f32 v67, v67\n"
    "ds_read2st64_b32 v[242:243], v207 offset0:102 offset1:105\n"
    "v_mfma_f32_16x16x32_bf16 v[48:51], v[172:175], a[212:215], v[48:51]\n"
    "v_exp_f32 v70, v70\n"
    "v_exp_f32 v80, v80\n"
    "v_mfma_f32_16x16x32_bf16 v[52:55], v[168:171], a[232:235], v[52:55]\n"
    "ds_read_b128 v[164:167], v201 offset:29696\n"
    "v_exp_f32 v71, v71\n"
    "ds_read2st64_b32 v[244:245], v207 offset0:108 offset1:111\n"
    "v_exp_f32 v81, v81\n"
    "v_exp_f32 v82, v82\n"
    "v_exp_f32 v83, v83\n"
    "v_cvt_pk_bf16_f32 v144, v64, v65\n"
    "v_cvt_pk_bf16_f32 v145, v66, v67\n"
    "v_cvt_pk_bf16_f32 v146, v80, v81\n"
    "v_cvt_pk_bf16_f32 v147, v82, v83\n"
    "v_mfma_f32_16x16x32_bf16 v[52:55], v[172:175], a[236:239], v[52:55]\n"
    "s_waitcnt lgkmcnt(8)\n"
    "s_add_u32 s78, s88, 0x4800\n"
    "v_exp_f32 v84, v84\n"
    "ds_read2st64_b32 v[246:247], v207 offset0:114 offset1:117\n"
    "v_mfma_f32_16x16x32_bf16 a[0:3], v[176:179], v[128:131], 0\n"
    "s_add_u32 m0, s71, 0x6800\n"
    "ds_read_b128 v[168:171], v201 offset:30720\n"
    "ds_read_u16_d16_hi v223, v209 offset:6912\n"
    "v_exp_f32 v85, v85\n"
    "v_exp_f32 v86, v86\n"
    "v_exp_f32 v87, v87\n"
    "v_cvt_pk_bf16_f32 v148, v68, v69\n"
    "v_cvt_pk_bf16_f32 v149, v70, v71\n"
    "v_cvt_pk_bf16_f32 v150, v84, v85\n"
    "v_cvt_pk_bf16_f32 v151, v86, v87\n"
    "v_mfma_f32_16x16x32_bf16 a[0:3], v[180:183], v[144:147], a[0:3]\n"
    "buffer_load_dwordx4 v154, s[28:31], s78 offen nt lds\n"
    "v_mfma_f32_16x16x32_bf16 a[4:7], v[176:179], v[132:135], 0\n"
    "ds_read_b128 v[172:175], v201 offset:31744\n"
    "ds_read_u16_d16_hi v222, v209 offset:768\n"
    "v_mfma_f32_16x16x32_bf16 a[4:7], v[180:183], v[148:151], a[4:7]\n"
    "s_waitcnt lgkmcnt(10)\n"
    "v_exp_f32 v32, v32\n"
    "v_mfma_f32_16x16x32_bf16 a[16:19], v[184:187], v[128:131], 0\n"
    "ds_read_b128 v[176:179], v201 offset:32768\n"
    "ds_read_b128 v[232:235], v205 offset:24576\n"
    "v_mfma_f32_16x16x32_bf16 a[16:19], v[188:191], v[144:147], a[16:19]\n"
    "ds_read_b128 v[236:239], v206 offset:24576\n"
    "v_mfma_f32_16x16x32_bf16 a[20:23], v[184:187], v[132:135], 0\n"
    "ds_read_b128 v[180:183], v201 offset:33792\n"
    "s_waitcnt lgkmcnt(13)\n"
    "v_cvt_f32_i32 v240, v240\n"
    "v_mfma_f32_16x16x32_bf16 a[20:23], v[188:191], v[148:151], a[20:23]\n"
    "s_waitcnt lgkmcnt(10)\n"
    "v_exp_f32 v33, v33\n"
    "v_mfma_f32_16x16x32_bf16 a[32:35], v[160:163], v[128:131], 0\n"
    "ds_read_b128 v[184:187], v201 offset:34816\n"
    "v_cvt_f32_i32 v241, v241\n"
    "v_mfma_f32_16x16x32_bf16 a[32:35], v[164:167], v[144:147], a[32:35]\n"
    "s_add_u32 s78, s88, 0x4800\n"
    "v_exp_f32 v34, v34\n"
    "v_mfma_f32_16x16x32_bf16 a[36:39], v[160:163], v[132:135], 0\n"
    "s_add_u32 m0, s71, 0x6c00\n"
    "ds_read_b128 v[188:191], v201 offset:35840\n"
    "v_cvt_f32_i32 v242, v242\n"
    "v_mfma_f32_16x16x32_bf16 a[36:39], v[164:167], v[148:151], a[36:39]\n"
    "buffer_load_dwordx4 v155, s[28:31], s78 offen nt lds\n"
    "v_mfma_f32_16x16x32_bf16 v[112:115], v[192:195], v[128:131], 0\n"
    "v_exp_f32 v35, v35\n"
    "v_mfma_f32_16x16x32_bf16 v[112:115], v[192:195], v[144:147], v[112:115]\n"
    "s_waitcnt lgkmcnt(7)\n"
    "v_exp_f32 v36, v36\n"
    "v_mfma_f32_16x16x32_bf16 a[48:51], v[168:171], v[128:131], 0\n"
    "ds_read_b128 v[160:163], v201 offset:36864\n"
    "v_cvt_f32_i32 v243, v243\n"
    "v_mfma_f32_16x16x32_bf16 a[48:51], v[172:175], v[144:147], a[48:51]\n"
    "v_cvt_f32_i32 v244, v244\n"
    "v_cvt_f32_i32 v245, v245\n"
    "v_mfma_f32_16x16x32_bf16 a[52:55], v[168:171], v[132:135], 0\n"
    "ds_read_b128 v[164:167], v201 offset:37888\n"
    "v_cvt_f32_i32 v246, v246\n"
    "v_mfma_f32_16x16x32_bf16 a[52:55], v[172:175], v[148:151], a[52:55]\n"
    "v_exp_f32 v37, v37\n"
    "v_mfma_f32_16x16x32_bf16 v[116:119], v[192:195], v[132:135], 0\n"
    "v_exp_f32 v38, v38\n"
    "v_mfma_f32_16x16x32_bf16 v[116:119], v[192:195], v[148:151], v[116:119]\n"
    "s_waitcnt lgkmcnt(4)\n"
    "s_add_u32 s78, s88, 0x4800\n"
    "v_mul_f32 v226, s75, v223\n"
    "v_mfma_f32_16x16x32_bf16 a[64:67], v[176:179], v[128:131], 0\n"
    "s_add_u32 m0, s71, 0x7000\n"
    "ds_read_b128 v[168:171], v201 offset:38912\n"
    "v_cvt_f32_i32 v247, v247\n"
    "v_mfma_f32_16x16x32_bf16 a[64:67], v[180:183], v[144:147], a[64:67]\n"
    "buffer_load_dwordx4 v156, s[28:31], s78 offen nt lds\n"
    "v_mfma_f32_16x16x32_bf16 a[68:71], v[176:179], v[132:135], 0\n"
    "ds_read_b128 v[172:175], v201 offset:39936\n"
    "v_fma_f32 v227, -v222, v226, v217\n"
    "v_mfma_f32_16x16x32_bf16 a[68:71], v[180:183], v[148:151], a[68:71]\n"
    "s_waitcnt lgkmcnt(4)\n"
    "v_mul_f32_dpp v240, v228, v240 row_newbcast:0 row_mask:0xf bank_mask:0xf\n"
    "v_cvt_f32_i32 v232, v232\n"
    "v_mfma_f32_16x16x32_bf16 a[80:83], v[184:187], v[128:131], 0\n"
    "ds_read_b128 v[176:179], v201 offset:40960\n"
    "v_mul_f32_dpp v241, v228, v241 row_newbcast:1 row_mask:0xf bank_mask:0xf\n"
    "v_mfma_f32_16x16x32_bf16 a[80:83], v[188:191], v[144:147], a[80:83]\n"
    "v_exp_f32 v39, v39\n"
    "v_mfma_f32_16x16x32_bf16 a[84:87], v[184:187], v[132:135], 0\n"
    "ds_read_b128 v[180:183], v201 offset:41984\n"
    "v_cvt_f32_i32 v233, v233\n"
    "v_mfma_f32_16x16x32_bf16 a[84:87], v[188:191], v[148:151], a[84:87]\n"
    "s_waitcnt lgkmcnt(4)\n"
    "v_mul_f32_dpp v242, v228, v242 row_newbcast:2 row_mask:0xf bank_mask:0xf\n"
    "v_cvt_f32_i32 v234, v234\n"
    "v_mfma_f32_16x16x32_bf16 a[96:99], v[160:163], v[128:131], 0\n"
    "ds_read_b128 v[184:187], v201 offset:43008\n"
    "v_mul_f32_dpp v243, v228, v243 row_newbcast:3 row_mask:0xf bank_mask:0xf\n"
    "v_mfma_f32_16x16x32_bf16 a[96:99], v[164:167], v[144:147], a[96:99]\n"
    "s_add_u32 s78, s88, 0x4800\n"
    "v_exp_f32 v48, v48\n"
    "v_mfma_f32_16x16x32_bf16 a[100:103], v[160:163], v[132:135], 0\n"
    "s_add_u32 m0, s71, 0x7400\n"
    "ds_read_b128 v[188:191], v201 offset:44032\n"
    "v_cvt_f32_i32 v235, v235\n"
    "v_mfma_f32_16x16x32_bf16 a[100:103], v[164:167], v[148:151], a[100:103]\n"
    "s_waitcnt lgkmcnt(4)\n"
    "buffer_load_dwordx4 v157, s[28:31], s78 offen nt lds\n"
    "v_mfma_f32_16x16x32_bf16 a[112:115], v[168:171], v[128:131], 0\n"
    "ds_read_b128 v[160:163], v201 offset:45056\n"
    "v_mul_f32_dpp v244, v228, v244 row_newbcast:4 row_mask:0xf bank_mask:0xf\n"
    "v_mfma_f32_16x16x32_bf16 a[112:115], v[172:175], v[144:147], a[112:115]\n"
    "v_exp_f32 v49, v49\n"
    "v_mfma_f32_16x16x32_bf16 a[116:119], v[168:171], v[132:135], 0\n"
    "ds_read_b128 v[164:167], v201 offset:46080\n"
    "v_cvt_f32_i32 v236, v236\n"
    "v_mfma_f32_16x16x32_bf16 a[116:119], v[172:175], v[148:151], a[116:119]\n"
    "s_waitcnt lgkmcnt(4)\n"
    "v_mul_f32_dpp v245, v228, v245 row_newbcast:5 row_mask:0xf bank_mask:0xf\n"
    "v_cvt_f32_i32 v237, v237\n"
    "v_mfma_f32_16x16x32_bf16 a[128:131], v[176:179], v[128:131], 0\n"
    "ds_read_b128 v[168:171], v201 offset:47104\n"
    "v_mul_f32_dpp v246, v228, v246 row_newbcast:6 row_mask:0xf bank_mask:0xf\n"
    "v_mfma_f32_16x16x32_bf16 a[128:131], v[180:183], v[144:147], a[128:131]\n"
    "v_exp_f32 v50, v50\n"
    "v_mfma_f32_16x16x32_bf16 a[132:135], v[176:179], v[132:135], 0\n"
    "ds_read_b128 v[172:175], v201 offset:48128\n"
    "v_cvt_f32_i32 v238, v238\n"
    "v_mfma_f32_16x16x32_bf16 a[132:135], v[180:183], v[148:151], a[132:135]\n"
    "s_waitcnt lgkmcnt(4)\n"
    "v_mul_f32_dpp v247, v228, v247 row_newbcast:7 row_mask:0xf bank_mask:0xf\n"
    "v_cvt_f32_i32 v239, v239\n"
    "v_mfma_f32_16x16x32_bf16 a[144:147], v[184:187], v[128:131], 0\n"
    "v_exp_f32 v51, v51\n"
    "v_mfma_f32_16x16x32_bf16 a[144:147], v[188:191], v[144:147], a[144:147]\n"
    "v_add_f32_dpp v240, v229, v240 row_newbcast:0 row_mask:0xf bank_mask:0xf\n"
    "v_mul_f32_dpp v232, v226, v232 row_newbcast:0 row_mask:0xf bank_mask:0xf\n"
    "v_mfma_f32_16x16x32_bf16 a[148:151], v[184:187], v[132:135], 0\n"
    "v_add_f32_dpp v241, v229, v241 row_newbcast:1 row_mask:0xf bank_mask:0xf\n"
    "v_mul_f32_dpp v233, v226, v233 row_newbcast:1 row_mask:0xf bank_mask:0xf\n"
    "v_mfma_f32_16x16x32_bf16 a[148:151], v[188:191], v[148:151], a[148:151]\n"
    "s_waitcnt lgkmcnt(2)\n"
    "v_exp_f32 v52, v52\n"
    "v_mfma_f32_16x16x32_bf16 a[160:163], v[160:163], v[128:131], 0\n"
    "v_add_f32_dpp v242, v229, v242 row_newbcast:2 row_mask:0xf bank_mask:0xf\n"
    "v_mul_f32_dpp v234, v226, v234 row_newbcast:2 row_mask:0xf bank_mask:0xf\n"
    "v_mfma_f32_16x16x32_bf16 a[160:163], v[164:167], v[144:147], a[160:163]\n"
    "v_add_f32_dpp v243, v229, v243 row_newbcast:3 row_mask:0xf bank_mask:0xf\n"
    "v_mul_f32_dpp v235, v226, v235 row_newbcast:3 row_mask:0xf bank_mask:0xf\n"
    "v_mfma_f32_16x16x32_bf16 a[164:167], v[160:163], v[132:135], 0\n"
    "v_exp_f32 v53, v53\n"
    "v_mfma_f32_16x16x32_bf16 a[164:167], v[164:167], v[148:151], a[164:167]\n"
    "s_waitcnt lgkmcnt(0)\n"
    "v_add_f32_dpp v244, v229, v244 row_newbcast:4 row_mask:0xf bank_mask:0xf\n"
    "v_mul_f32_dpp v236, v226, v236 row_newbcast:4 row_mask:0xf bank_mask:0xf\n"
    "v_mfma_f32_16x16x32_bf16 a[176:179], v[168:171], v[128:131], 0\n"
    "v_add_f32_dpp v245, v229, v245 row_newbcast:5 row_mask:0xf bank_mask:0xf\n"
    "v_mul_f32_dpp v237, v226, v237 row_newbcast:5 row_mask:0xf bank_mask:0xf\n"
    "v_mfma_f32_16x16x32_bf16 a[176:179], v[172:175], v[144:147], a[176:179]\n"
    "v_exp_f32 v54, v54\n"
    "v_mfma_f32_16x16x32_bf16 a[180:183], v[168:171], v[132:135], 0\n"
    "v_add_f32_dpp v246, v229, v246 row_newbcast:6 row_mask:0xf bank_mask:0xf\n"
    "v_mul_f32_dpp v238, v226, v238 row_newbcast:6 row_mask:0xf bank_mask:0xf\n"
    "v_add_f32_dpp v247, v229, v247 row_newbcast:7 row_mask:0xf bank_mask:0xf\n"
    "v_exp_f32 v55, v55\n"
    "v_mul_f32_dpp v239, v226, v239 row_newbcast:7 row_mask:0xf bank_mask:0xf\n"
    "v_cvt_pk_bf16_f32 v240, v240, v241\n"
    "v_add_f32_dpp v232, v227, v232 row_newbcast:0 row_mask:0xf bank_mask:0xf\n"
    "v_cvt_pk_bf16_f32 v241, v242, v243\n"
    "v_add_f32_dpp v233, v227, v233 row_newbcast:1 row_mask:0xf bank_mask:0xf\n"
    "v_cvt_pk_bf16_f32 v242, v244, v245\n"
    "v_add_f32_dpp v234, v227, v234 row_newbcast:2 row_mask:0xf bank_mask:0xf\n"
    "v_cvt_pk_bf16_f32 v243, v246, v247\n"
    "v_add_f32_dpp v235, v227, v235 row_newbcast:3 row_mask:0xf bank_mask:0xf\n"
    "v_add_f32_dpp v236, v227, v236 row_newbcast:4 row_mask:0xf bank_mask:0xf\n"
    "v_add_f32_dpp v237, v227, v237 row_newbcast:5 row_mask:0xf bank_mask:0xf\n"
    "v_add_f32_dpp v238, v227, v238 row_newbcast:6 row_mask:0xf bank_mask:0xf\n"
    "v_add_f32_dpp v239, v227, v239 row_newbcast:7 row_mask:0xf bank_mask:0xf\n"
    "v_cvt_pk_bf16_f32 v232, v232, v233\n"
    "v_cvt_pk_bf16_f32 v233, v234, v235\n"
    "v_cvt_pk_bf16_f32 v234, v236, v237\n"
    "v_cvt_pk_bf16_f32 v235, v238, v239\n"
    "v_mfma_f32_16x16x32_bf16 a[180:183], v[172:175], v[148:151], a[180:183]\n"
    "v_add_f32 v113, v113, v32\n"
    "v_add_f32 v114, v114, v33\n"
    "v_add_f32 v113, v113, v34\n"
    "v_add_f32 v114, v114, v35\n"
    "v_add_f32 v117, v117, v36\n"
    "v_add_f32 v118, v118, v37\n"
    "v_add_f32 v117, v117, v38\n"
    "v_add_f32 v118, v118, v39\n"
    "v_add_f32 v113, v113, v48\n"
    "v_add_f32 v114, v114, v49\n"
    "v_add_f32 v113, v113, v50\n"
    "v_add_f32 v114, v114, v51\n"
    "v_add_f32 v117, v117, v52\n"
    "buffer_store_dwordx4 v[232:235], v211, s[36:39], s62 offen sc1\n"
    "buffer_store_dwordx4 v[240:243], v212, s[40:43], s63 offen sc1\n"
    "v_add_f32 v118, v118, v53\n"
    "v_add_f32 v117, v117, v54\n"
    "v_add_f32 v118, v118, v55\n"
    "s_waitcnt vmcnt(8)\n"
    "ds_read_b128 a[240:243], v158 offset:49152\n"
    "ds_read_b128 a[244:247], v159 offset:49152\n"
    "ds_read_b128 a[248:251], v158 offset:49280\n"
    "ds_read_b128 a[252:255], v159 offset:49280\n"
    "ds_read_b128 v[0:3], v158 offset:49408\n"
    "ds_read_b128 v[4:7], v159 offset:49408\n"
    "v_cmp_ne_u32 s[80:81], s67, v219\n"
    "v_cmp_gt_u32 vcc, s66, v218\n"
    "s_or_b64 vcc, vcc, s[80:81]\n"
    "s_cbranch_vccnz LSPRE1\n"
    "LSPB1:\n"
    "v_mov_b32 v220, 4\n"
    "s_add_u32 s66, s66, s74\n"
    "s_waitcnt lgkmcnt(0)\n"
    "s_barrier\n"
    "buffer_store_dwordx2 v[220:221], v214, s[44:47], 0 offen sc1\n"
    "ds_read_b128 v[160:163], v255\n"
    "ds_read_b128 v[164:167], v255 offset:1024\n"
    "ds_read_b128 v[168:171], v255 offset:2048\n"
    "ds_read_b128 v[172:175], v255 offset:3072\n"
    "ds_read_b128 v[176:179], v255 offset:4096\n"
    "ds_read_b128 v[180:183], v255 offset:5120\n"
    "s_waitcnt lgkmcnt(4)\n"
    "v_mfma_f32_16x16x32_bf16 v[44:47], v[160:163], a[240:243], 0\n"
    "ds_read_b128 v[184:187], v255 offset:6144\n"
    "v_mfma_f32_16x16x32_bf16 v[44:47], v[164:167], a[244:247], v[44:47]\n"
    "s_waitcnt lgkmcnt(3)\n"
    "ds_read_b128 v[188:191], v255 offset:7168\n"
    "v_mfma_f32_16x16x32_bf16 v[44:47], v[168:171], a[248:251], v[44:47]\n"
    "s_add_u32 m0, s71, 0xc000\n"
    "ds_read_b128 v[160:163], v255 offset:8192\n"
    "v_mfma_f32_16x16x32_bf16 v[44:47], v[172:175], a[252:255], v[44:47]\n"
    "s_waitcnt lgkmcnt(3)\n"
    "ds_read_b128 v[164:167], v255 offset:9216\n"
    "buffer_load_dwordx4 v200, s[40:43], s61 offen sc1 lds\n"
    "v_mfma_f32_16x16x32_bf16 v[44:47], v[176:179], v[0:3], v[44:47]\n"
    "ds_read_b128 v[168:171], v255 offset:10240\n"
    "v_mfma_f32_16x16x32_bf16 v[44:47], v[180:183], v[4:7], v[44:47]\n"
    "s_waitcnt lgkmcnt(3)\n"
    "ds_read_b128 v[172:175], v255 offset:11264\n"
    "v_mfma_f32_16x16x32_bf16 v[60:63], v[184:187], a[240:243], 0\n"
    "ds_read_b128 v[176:179], v200\n"
    "v_mfma_f32_16x16x32_bf16 v[60:63], v[188:191], a[244:247], v[60:63]\n"
    "s_waitcnt lgkmcnt(3)\n"
    "ds_read_b128 v[180:183], v200 offset:1024\n"
    "v_mfma_f32_16x16x32_bf16 v[60:63], v[160:163], a[248:251], v[60:63]\n"
    "ds_read_b128 v[184:187], v200 offset:2048\n"
    "buffer_load_dwordx4 v200, s[40:43], s61 offen offset:1024 sc1 lds\n"
    "v_mfma_f32_16x16x32_bf16 v[60:63], v[164:167], a[252:255], v[60:63]\n"
    "s_waitcnt lgkmcnt(3)\n"
    "ds_read_b128 v[188:191], v200 offset:3072\n"
    "v_mfma_f32_16x16x32_bf16 v[60:63], v[168:171], v[0:3], v[60:63]\n"
    "ds_read_b128 v[160:163], v200 offset:4096\n"
    "v_mfma_f32_16x16x32_bf16 v[60:63], v[172:175], v[4:7], v[60:63]\n"
    "s_waitcnt lgkmcnt(3)\n"
    "ds_read_b128 v[164:167], v200 offset:5120\n"
    "v_mfma_f32_16x16x32_bf16 v[72:75], v[176:179], a[240:243], 0\n"
    "ds_read_b128 v[168:171], v200 offset:6144\n"
    "v_mfma_f32_16x16x32_bf16 v[72:75], v[180:183], a[244:247], v[72:75]\n"
    "s_waitcnt lgkmcnt(3)\n"
    "ds_read_b128 v[172:175], v200 offset:7168\n"
    "buffer_load_dwordx4 v200, s[40:43], s61 offen offset:2048 sc1 lds\n"
    "v_mfma_f32_16x16x32_bf16 v[72:75], v[184:187], a[248:251], v[72:75]\n"
    "ds_read_b128 v[176:179], v200 offset:8192\n"
    "v_mfma_f32_16x16x32_bf16 v[72:75], v[188:191], a[252:255], v[72:75]\n"
    "s_waitcnt lgkmcnt(3)\n"
    "ds_read_b128 v[180:183], v200 offset:9216\n"
    "v_mfma_f32_16x16x32_bf16 v[72:75], v[160:163], v[0:3], v[72:75]\n"
    "ds_read_b128 v[184:187], v200 offset:10240\n"
    "v_mfma_f32_16x16x32_bf16 v[72:75], v[164:167], v[4:7], v[72:75]\n"
    "s_waitcnt lgkmcnt(3)\n"
    "ds_read_b128 v[188:191], v200 offset:11264\n"
    "v_max3_f32 v196, v44, v45, v46\n"
    "v_max3_f32 v196, v196, v47, v60\n"
    "v_max3_f32 v196, v196, v61, v62\n"
    "v_mfma_f32_16x16x32_bf16 v[88:91], v[168:171], a[240:243], 0\n"
    "ds_read_b128 v[160:163], v200 offset:12288\n"
    "buffer_load_dwordx4 v200, s[40:43], s61 offen offset:3072 sc1 lds\n"
    "v_max_f32 v196, v196, v63\n"
    "v_mfma_f32_16x16x32_bf16 v[88:91], v[172:175], a[244:247], v[88:91]\n"
    "s_waitcnt lgkmcnt(3)\n"
    "ds_read_b128 v[164:167], v200 offset:13312\n"
    "v_mov_b32 v197, v196\n"
    "s_nop 1\n"
    "v_permlane32_swap_b32 v196, v197\n"
    "v_max_f32 v196, v196, v197\n"
    "v_mfma_f32_16x16x32_bf16 v[88:91], v[176:179], a[248:251], v[88:91]\n"
    "ds_read_b128 v[168:171], v200 offset:14336\n"
    "v_mov_b32 v197, v196\n"
    "s_nop 1\n"
    "v_permlane16_swap_b32 v196, v197\n"
    "v_max_f32 v196, v196, v197\n"
    "v_mfma_f32_16x16x32_bf16 v[88:91], v[180:183], a[252:255], v[88:91]\n"
    "s_waitcnt lgkmcnt(3)\n"
    "s_add_u32 s65, s61, 0x1000\n"
    "ds_read_b128 v[172:175], v200 offset:15360\n"
    "v_add_f32 v196, 0x42640000, v196\n"
    "v_sub_f32 v104, 0, v196\n"
    "v_mfma_f32_16x16x32_bf16 v[88:91], v[184:187], v[0:3], v[88:91]\n"
    "s_add_u32 m0, s71, 0xd000\n"
    "ds_read_b128 v[176:179], v200 offset:16384\n"
    "v_sub_f32 v105, 0, v196\n"
    "v_sub_f32 v106, 0, v196\n"
    "v_sub_f32 v107, 0, v196\n"
    "v_mfma_f32_16x16x32_bf16 v[88:91], v[188:191], v[4:7], v[88:91]\n"
    "s_waitcnt lgkmcnt(3)\n"
    "ds_read_b128 v[180:183], v200 offset:17408\n"
    "buffer_load_dwordx4 v200, s[40:43], s65 offen sc1 lds\n"
    "v_add_f32 v44, v44, v104\n"
    "v_mfma_f32_16x16x32_bf16 v[40:43], v[160:163], a[240:243], v[104:107]\n"
    "ds_read_b128 v[184:187], v200 offset:18432\n"
    "v_add_f32 v45, v45, v104\n"
    "v_add_f32 v46, v46, v104\n"
    "v_add_f32 v47, v47, v104\n"
    "v_mfma_f32_16x16x32_bf16 v[40:43], v[164:167], a[244:247], v[40:43]\n"
    "s_waitcnt lgkmcnt(3)\n"
    "ds_read_b128 v[188:191], v200 offset:19456\n"
    "v_add_f32 v60, v60, v104\n"
    "v_add_f32 v61, v61, v104\n"
    "v_add_f32 v62, v62, v104\n"
    "v_mfma_f32_16x16x32_bf16 v[40:43], v[168:171], a[248:251], v[40:43]\n"
    "ds_read_b128 v[160:163], v200 offset:20480\n"
    "v_add_f32 v63, v63, v104\n"
    "v_exp_f32 v44, v44\n"
    "v_mfma_f32_16x16x32_bf16 v[40:43], v[172:175], a[252:255], v[40:43]\n"
    "s_waitcnt lgkmcnt(3)\n"
    "ds_read_b128 v[164:167], v200 offset:21504\n"
    "v_exp_f32 v45, v45\n"
    "v_mfma_f32_16x16x32_bf16 v[40:43], v[176:179], v[0:3], v[40:43]\n"
    "ds_read_b128 v[168:171], v200 offset:22528\n"
    "buffer_load_dwordx4 v200, s[40:43], s65 offen offset:1024 sc1 lds\n"
    "v_mfma_f32_16x16x32_bf16 v[40:43], v[180:183], v[4:7], v[40:43]\n"
    "s_waitcnt lgkmcnt(3)\n"
    "ds_read_b128 v[172:175], v200 offset:23552\n"
    "v_exp_f32 v46, v46\n"
    "v_mfma_f32_16x16x32_bf16 v[56:59], v[184:187], a[240:243], v[104:107]\n"
    "ds_read_b128 v[176:179], v201 offset:24576\n"
    "v_exp_f32 v47, v47\n"
    "v_mfma_f32_16x16x32_bf16 v[56:59], v[188:191], a[244:247], v[56:59]\n"
    "s_waitcnt lgkmcnt(3)\n"
    "s_add_u32 s78, s64, 0x30000\n"
    "ds_read_b128 v[180:183], v201 offset:25600\n"
    "v_exp_f32 v60, v60\n"
    "v_mfma_f32_16x16x32_bf16 v[56:59], v[160:163], a[248:251], v[56:59]\n"
    "s_add_u32 m0, s72, 0x1b000\n"
    "ds_read_b128 v[184:187], v201 offset:26624\n"
    "v_exp_f32 v61, v61\n"
    "v_mfma_f32_16x16x32_bf16 v[56:59], v[164:167], a[252:255], v[56:59]\n"
    "s_waitcnt lgkmcnt(3)\n"
    "ds_read_b128 v[188:191], v201 offset:27648\n"
    "buffer_load_dwordx4 v202, s[32:35], s78 offen nt lds\n"
    "v_mfma_f32_16x16x32_bf16 v[56:59], v[168:171], v[0:3], v[56:59]\n"
    "ds_read_b128 v[160:163], v201 offset:28672\n"
    "v_exp_f32 v62, v62\n"
    "v_exp_f32 v63, v63\n"
    "v_cvt_pk_bf16_f32 v136, v44, v45\n"
    "v_cvt_pk_bf16_f32 v137, v46, v47\n"
    "v_cvt_pk_bf16_f32 v138, v60, v61\n"
    "v_cvt_pk_bf16_f32 v139, v62, v63\n"
    "v_add_f32 v72, v72, v104\n"
    "v_add_f32 v73, v73, v104\n"
    "v_add_f32 v74, v74, v104\n"
    "v_add_f32 v75, v75, v104\n"
    "v_add_f32 v88, v88, v104\n"
    "v_add_f32 v89, v89, v104\n"
    "v_add_f32 v90, v90, v104\n"
    "v_add_f32 v91, v91, v104\n"
    "v_exp_f32 v72, v72\n"
    "v_exp_f32 v73, v73\n"
    "v_exp_f32 v74, v74\n"
    "v_exp_f32 v75, v75\n"
    "v_exp_f32 v88, v88\n"
    "v_exp_f32 v89, v89\n"
    "v_exp_f32 v90, v90\n"
    "v_exp_f32 v91, v91\n"
    "v_cvt_pk_bf16_f32 v152, v72, v73\n"
    "v_cvt_pk_bf16_f32 v153, v74, v75\n"
    "v_cvt_pk_bf16_f32 v154, v88, v89\n"
    "v_cvt_pk_bf16_f32 v155, v90, v91\n"
    "v_mfma_f32_16x16x32_bf16 v[56:59], v[172:175], v[4:7], v[56:59]\n"
    "s_waitcnt lgkmcnt(3)\n"
    "ds_read_b128 v[164:167], v201 offset:29696\n"
    "v_exp_f32 v40, v40\n"
    "v_mfma_f32_16x16x32_bf16 a[8:11], v[176:179], v[136:139], 0\n"
    "ds_read_b128 v[168:171], v201 offset:30720\n"
    "v_mfma_f32_16x16x32_bf16 a[8:11], v[180:183], v[152:155], a[8:11]\n"
    "s_waitcnt lgkmcnt(3)\n"
    "ds_read_b128 v[172:175], v201 offset:31744\n"
    "v_mfma_f32_16x16x32_bf16 a[24:27], v[184:187], v[136:139], 0\n"
    "ds_read_b128 v[176:179], v201 offset:32768\n"
    "buffer_load_dwordx4 v203, s[32:35], s78 offen offset:1024 nt lds\n"
    "v_mfma_f32_16x16x32_bf16 a[24:27], v[188:191], v[152:155], a[24:27]\n"
    "s_waitcnt lgkmcnt(3)\n"
    "ds_read_b128 v[180:183], v201 offset:33792\n"
    "v_mfma_f32_16x16x32_bf16 a[40:43], v[160:163], v[136:139], 0\n"
    "ds_read_b128 v[184:187], v201 offset:34816\n"
    "v_mfma_f32_16x16x32_bf16 a[40:43], v[164:167], v[152:155], a[40:43]\n"
    "ds_read_b128 v[188:191], v201 offset:35840\n"
    "v_mfma_f32_16x16x32_bf16 v[120:123], v[192:195], v[136:139], 0\n"
    "v_exp_f32 v41, v41\n"
    "v_mfma_f32_16x16x32_bf16 v[120:123], v[192:195], v[152:155], v[120:123]\n"
    "s_waitcnt lgkmcnt(4)\n"
    "buffer_load_dwordx4 v204, s[32:35], s78 offen offset:2048 nt lds\n"
    "v_mfma_f32_16x16x32_bf16 a[56:59], v[168:171], v[136:139], 0\n"
    "ds_read_b128 v[160:163], v201 offset:36864\n"
    "v_mfma_f32_16x16x32_bf16 a[56:59], v[172:175], v[152:155], a[56:59]\n"
    "s_waitcnt lgkmcnt(3)\n"
    "ds_read_b128 v[164:167], v201 offset:37888\n"
    "v_mfma_f32_16x16x32_bf16 a[72:75], v[176:179], v[136:139], 0\n"
    "ds_read_b128 v[168:171], v201 offset:38912\n"
    "v_mfma_f32_16x16x32_bf16 a[72:75], v[180:183], v[152:155], a[72:75]\n"
    "s_waitcnt lgkmcnt(3)\n"
    "ds_read_b128 v[172:175], v201 offset:39936\n"
    "v_mfma_f32_16x16x32_bf16 a[88:91], v[184:187], v[136:139], 0\n"
    "ds_read_b128 v[176:179], v201 offset:40960\n"
    "v_mfma_f32_16x16x32_bf16 a[88:91], v[188:191], v[152:155], a[88:91]\n"
    "s_waitcnt lgkmcnt(3)\n"
    "ds_read_b128 v[180:183], v201 offset:41984\n"
    "v_mfma_f32_16x16x32_bf16 a[104:107], v[160:163], v[136:139], 0\n"
    "ds_read_b128 v[184:187], v201 offset:43008\n"
    "v_mfma_f32_16x16x32_bf16 a[104:107], v[164:167], v[152:155], a[104:107]\n"
    "s_waitcnt lgkmcnt(3)\n"
    "ds_read_b128 v[188:191], v201 offset:44032\n"
    "v_mfma_f32_16x16x32_bf16 a[120:123], v[168:171], v[136:139], 0\n"
    "ds_read_b128 v[160:163], v201 offset:45056\n"
    "v_mfma_f32_16x16x32_bf16 a[120:123], v[172:175], v[152:155], a[120:123]\n"
    "s_waitcnt lgkmcnt(3)\n"
    "ds_read_b128 v[164:167], v201 offset:46080\n"
    "v_mfma_f32_16x16x32_bf16 a[136:139], v[176:179], v[136:139], 0\n"
    "ds_read_b128 v[168:171], v201 offset:47104\n"
    "v_mfma_f32_16x16x32_bf16 a[136:139], v[180:183], v[152:155], a[136:139]\n"
    "s_waitcnt lgkmcnt(3)\n"
    "ds_read_b128 v[172:175], v201 offset:48128\n"
    "v_mfma_f32_16x16x32_bf16 a[152:155], v[184:187], v[136:139], 0\n"
    "v_exp_f32 v42, v42\n"
    "v_mfma_f32_16x16x32_bf16 a[152:155], v[188:191], v[152:155], a[152:155]\n"
    "s_waitcnt lgkmcnt(2)\n"
    "v_exp_f32 v43, v43\n"
    "v_mfma_f32_16x16x32_bf16 a[168:171], v[160:163], v[136:139], 0\n"
    "v_exp_f32 v56, v56\n"
    "v_mfma_f32_16x16x32_bf16 a[168:171], v[164:167], v[152:155], a[168:171]\n"
    "s_waitcnt lgkmcnt(0)\n"
    "v_exp_f32 v57, v57\n"
    "v_mfma_f32_16x16x32_bf16 a[184:187], v[168:171], v[136:139], 0\n"
    "v_exp_f32 v58, v58\n"
    "v_exp_f32 v59, v59\n"
    "v_mfma_f32_16x16x32_bf16 a[184:187], v[172:175], v[152:155], a[184:187]\n"
    "v_add_f32 v121, v121, v40\n"
    "v_add_f32 v122, v122, v41\n"
    "v_add_f32 v121, v121, v42\n"
    "v_add_f32 v122, v122, v43\n"
    "v_add_f32 v121, v121, v56\n"
    "v_add_f32 v122, v122, v57\n"
    "s_waitcnt vmcnt(12)\n"
    "ds_read_b128 v[8:11], v158 offset:24576\n"
    "ds_read_b128 v[12:15], v159 offset:24576\n"
    "ds_read_b128 v[16:19], v158 offset:24704\n"
    "ds_read_b128 v[20:23], v159 offset:24704\n"
    "ds_read_b128 v[24:27], v158 offset:24832\n"
    "ds_read_b128 v[28:31], v159 offset:24832\n"
    "s_waitcnt lgkmcnt(0)\n"
    "ds_read_b128 v[160:163], v255\n"
    "ds_read_b128 v[164:167], v255 offset:1024\n"
    "ds_read_b128 v[168:171], v255 offset:2048\n"
    "ds_read_b128 v[172:175], v255 offset:3072\n"
    "ds_read_b128 v[176:179], v255 offset:4096\n"
    "ds_read_b128 v[180:183], v255 offset:5120\n"
    "s_waitcnt lgkmcnt(4)\n"
    "v_mfma_f32_16x16x32_bf16 v[64:67], v[160:163], v[8:11], 0\n"
    "ds_read_b128 v[184:187], v255 offset:6144\n"
    "v_mfma_f32_16x16x32_bf16 v[64:67], v[164:167], v[12:15], v[64:67]\n"
    "s_waitcnt lgkmcnt(3)\n"
    "s_add_u32 m0, s71, 0x6000\n"
    "ds_read_b128 v[188:191], v255 offset:7168\n"
    "v_mfma_f32_16x16x32_bf16 v[64:67], v[168:171], v[16:19], v[64:67]\n"
    "ds_read_b128 v[160:163], v255 offset:8192\n"
    "buffer_load_dwordx4 v200, s[36:39], s60 offen sc1 lds\n"
    "v_mfma_f32_16x16x32_bf16 v[64:67], v[172:175], v[20:23], v[64:67]\n"
    "s_waitcnt lgkmcnt(3)\n"
    "ds_read_b128 v[164:167], v255 offset:9216\n"
    "v_mfma_f32_16x16x32_bf16 v[64:67], v[176:179], v[24:27], v[64:67]\n"
    "ds_read_b128 v[168:171], v255 offset:10240\n"
    "v_mfma_f32_16x16x32_bf16 v[64:67], v[180:183], v[28:31], v[64:67]\n"
    "s_waitcnt lgkmcnt(3)\n"
    "ds_read_b128 v[172:175], v255 offset:11264\n"
    "buffer_load_dwordx4 v200, s[36:39], s60 offen offset:1024 sc1 lds\n"
    "v_mfma_f32_16x16x32_bf16 v[80:83], v[184:187], v[8:11], 0\n"
    "ds_read_b128 v[176:179], v200\n"
    "v_mfma_f32_16x16x32_bf16 v[80:83], v[188:191], v[12:15], v[80:83]\n"
    "s_waitcnt lgkmcnt(3)\n"
    "ds_read_b128 v[180:183], v200 offset:1024\n"
    "v_mfma_f32_16x16x32_bf16 v[80:83], v[160:163], v[16:19], v[80:83]\n"
    "ds_read_b128 v[184:187], v200 offset:2048\n"
    "buffer_load_dwordx4 v200, s[36:39], s60 offen offset:2048 sc1 lds\n"
    "v_mfma_f32_16x16x32_bf16 v[80:83], v[164:167], v[20:23], v[80:83]\n"
    "s_waitcnt lgkmcnt(3)\n"
    "ds_read_b128 v[188:191], v200 offset:3072\n"
    "v_mfma_f32_16x16x32_bf16 v[80:83], v[168:171], v[24:27], v[80:83]\n"
    "ds_read_b128 v[160:163], v200 offset:4096\n"
    "v_mfma_f32_16x16x32_bf16 v[80:83], v[172:175], v[28:31], v[80:83]\n"
    "s_waitcnt lgkmcnt(3)\n"
    "ds_read_b128 v[164:167], v200 offset:5120\n"
    "buffer_load_dwordx4 v200, s[36:39], s60 offen offset:3072 sc1 lds\n"
    "v_mfma_f32_16x16x32_bf16 v[76:79], v[176:179], v[8:11], 0\n"
    "s_add_u32 s65, s60, 0x1000\n"
    "ds_read_b128 v[168:171], v200 offset:6144\n"
    "v_mfma_f32_16x16x32_bf16 v[76:79], v[180:183], v[12:15], v[76:79]\n"
    "s_waitcnt lgkmcnt(3)\n"
    "s_add_u32 m0, s71, 0x7000\n"
    "ds_read_b128 v[172:175], v200 offset:7168\n"
    "v_mfma_f32_16x16x32_bf16 v[76:79], v[184:187], v[16:19], v[76:79]\n"
    "ds_read_b128 v[176:179], v200 offset:8192\n"
    "buffer_load_dwordx4 v200, s[36:39], s65 offen sc1 lds\n"
    "v_mfma_f32_16x16x32_bf16 v[76:79], v[188:191], v[20:23], v[76:79]\n"
    "s_waitcnt lgkmcnt(3)\n"
    "ds_read_b128 v[180:183], v200 offset:9216\n"
    "v_mfma_f32_16x16x32_bf16 v[76:79], v[160:163], v[24:27], v[76:79]\n"
    "ds_read_b128 v[184:187], v200 offset:10240\n"
    "v_mfma_f32_16x16x32_bf16 v[76:79], v[164:167], v[28:31], v[76:79]\n"
    "s_waitcnt lgkmcnt(3)\n"
    "ds_read_b128 v[188:191], v200 offset:11264\n"
    "buffer_load_dwordx4 v200, s[36:39], s65 offen offset:1024 sc1 lds\n"
    "v_max3_f32 v196, v64, v65, v66\n"
    "v_mfma_f32_16x16x32_bf16 v[92:95], v[168:171], v[8:11], 0\n"
    "ds_read_b128 v[160:163], v200 offset:12288\n"
    "v_max3_f32 v196, v196, v67, v80\n"
    "v_max3_f32 v196, v196, v81, v82\n"
    "v_max_f32 v196, v196, v83\n"
    "v_mfma_f32_16x16x32_bf16 v[92:95], v[172:175], v[12:15], v[92:95]\n"
    "s_waitcnt lgkmcnt(3)\n"
    "ds_read_b128 v[164:167], v200 offset:13312\n"
    "v_mov_b32 v197, v196\n"
    "s_nop 1\n"
    "v_permlane32_swap_b32 v196, v197\n"
    "v_max_f32 v196, v196, v197\n"
    "v_mfma_f32_16x16x32_bf16 v[92:95], v[176:179], v[16:19], v[92:95]\n"
    "ds_read_b128 v[168:171], v200 offset:14336\n"
    "v_mov_b32 v197, v196\n"
    "s_nop 1\n"
    "v_permlane16_swap_b32 v196, v197\n"
    "v_max_f32 v196, v196, v197\n"
    "v_mfma_f32_16x16x32_bf16 v[92:95], v[180:183], v[20:23], v[92:95]\n"
    "s_waitcnt lgkmcnt(3)\n"
    "ds_read_b128 v[172:175], v200 offset:15360\n"
    "v_add_f32 v196, 0x42640000, v196\n"
    "v_sub_f32 v108, 0, v196\n"
    "v_sub_f32 v109, 0, v196\n"
    "v_mfma_f32_16x16x32_bf16 v[92:95], v[184:187], v[24:27], v[92:95]\n"
    "ds_read_b128 v[176:179], v200 offset:16384\n"
    "v_sub_f32 v110, 0, v196\n"
    "v_sub_f32 v111, 0, v196\n"
    "v_add_f32 v64, v64, v108\n"
    "v_mfma_f32_16x16x32_bf16 v[92:95], v[188:191], v[28:31], v[92:95]\n"
    "s_waitcnt lgkmcnt(3)\n"
    "ds_read_b128 v[180:183], v200 offset:17408\n"
    "v_add_f32 v65, v65, v108\n"
    "v_add_f32 v66, v66, v108\n"
    "v_add_f32 v67, v67, v108\n"
    "v_mfma_f32_16x16x32_bf16 v[44:47], v[160:163], v[8:11], v[108:111]\n"
    "ds_read_b128 v[184:187], v200 offset:18432\n"
    "v_add_f32 v80, v80, v108\n"
    "v_add_f32 v81, v81, v108\n"
    "v_add_f32 v82, v82, v108\n"
    "v_mfma_f32_16x16x32_bf16 v[44:47], v[164:167], v[12:15], v[44:47]\n"
    "s_waitcnt lgkmcnt(3)\n"
    "ds_read_b128 v[188:191], v200 offset:19456\n"
    "v_add_f32 v83, v83, v108\n"
    "v_exp_f32 v64, v64\n"
    "v_mfma_f32_16x16x32_bf16 v[44:47], v[168:171], v[16:19], v[44:47]\n"
    "ds_read_b128 v[160:163], v200 offset:20480\n"
    "buffer_load_dwordx2 v[218:219], v213, s[44:47], 0 offen sc1\n"
    "v_mfma_f32_16x16x32_bf16 v[44:47], v[172:175], v[20:23], v[44:47]\n"
    "s_waitcnt lgkmcnt(3)\n"
    "ds_read_b128 v[164:167], v200 offset:21504\n"
    "v_exp_f32 v65, v65\n"
    "v_mfma_f32_16x16x32_bf16 v[44:47], v[176:179], v[24:27], v[44:47]\n"
    "ds_read_b128 v[168:171], v200 offset:22528\n"
    "v_exp_f32 v66, v66\n"
    "v_mfma_f32_16x16x32_bf16 v[44:47], v[180:183], v[28:31], v[44:47]\n"
    "s_waitcnt lgkmcnt(3)\n"
    "ds_read_b128 v[172:175], v200 offset:23552\n"
    "v_exp_f32 v67, v67\n"
    "v_mfma_f32_16x16x32_bf16 v[60:63], v[184:187], v[8:11], v[108:111]\n"
    "ds_read_b128 v[176:179], v201 offset:24576\n"
    "v_exp_f32 v80, v80\n"
    "v_mfma_f32_16x16x32_bf16 v[60:63], v[188:191], v[12:15], v[60:63]\n"
    "s_waitcnt lgkmcnt(3)\n"
    "ds_read_b128 v[180:183], v201 offset:25600\n"
    "v_exp_f32 v81, v81\n"
    "v_mfma_f32_16x16x32_bf16 v[60:63], v[160:163], v[16:19], v[60:63]\n"
    "ds_read_b128 v[184:187], v201 offset:26624\n"
    "v_exp_f32 v82, v82\n"
    "v_mfma_f32_16x16x32_bf16 v[60:63], v[164:167], v[20:23], v[60:63]\n"
    "s_waitcnt lgkmcnt(3)\n"
    "ds_read_b128 v[188:191], v201 offset:27648\n"
    "v_exp_f32 v83, v83\n"
    "v_cvt_pk_bf16_f32 v140, v64, v65\n"
    "v_mfma_f32_16x16x32_bf16 v[60:63], v[168:171], v[24:27], v[60:63]\n"
    "ds_read_b128 v[160:163], v201 offset:28672\n"
    "v_cvt_pk_bf16_f32 v141, v66, v67\n"
    "v_cvt_pk_bf16_f32 v142, v80, v81\n"
    "v_cvt_pk_bf16_f32 v143, v82, v83\n"
    "v_add_f32 v76, v76, v108\n"
    "v_add_f32 v77, v77, v108\n"
    "v_add_f32 v78, v78, v108\n"
    "v_add_f32 v79, v79, v108\n"
    "v_add_f32 v92, v92, v108\n"
    "v_add_f32 v93, v93, v108\n"
    "v_add_f32 v94, v94, v108\n"
    "v_add_f32 v95, v95, v108\n"
    "v_exp_f32 v76, v76\n"
    "v_exp_f32 v77, v77\n"
    "v_exp_f32 v78, v78\n"
    "v_exp_f32 v79, v79\n"
    "v_exp_f32 v92, v92\n"
    "v_exp_f32 v93, v93\n"
    "v_exp_f32 v94, v94\n"
    "v_exp_f32 v95, v95\n"
    "v_cvt_pk_bf16_f32 v156, v76, v77\n"
    "v_cvt_pk_bf16_f32 v157, v78, v79\n"
    "v_cvt_pk_bf16_f32 v158, v92, v93\n"
    "v_cvt_pk_bf16_f32 v159, v94, v95\n"
    "v_mfma_f32_16x16x32_bf16 v[60:63], v[172:175], v[28:31], v[60:63]\n"
    "s_waitcnt lgkmcnt(3)\n"
    "ds_read_b128 v[164:167], v201 offset:29696\n"
    "v_exp_f32 v44, v44\n"
    "v_mfma_f32_16x16x32_bf16 a[12:15], v[176:179], v[140:143], 0\n"
    "ds_read_b128 v[168:171], v201 offset:30720\n"
    "v_mfma_f32_16x16x32_bf16 a[12:15], v[180:183], v[156:159], a[12:15]\n"
    "s_waitcnt lgkmcnt(3)\n"
    "ds_read_b128 v[172:175], v201 offset:31744\n"
    "v_mfma_f32_16x16x32_bf16 a[28:31], v[184:187], v[140:143], 0\n"
    "ds_read_b128 v[176:179], v201 offset:32768\n"
    "v_mfma_f32_16x16x32_bf16 a[28:31], v[188:191], v[156:159], a[28:31]\n"
    "s_waitcnt lgkmcnt(3)\n"
    "ds_read_b128 v[180:183], v201 offset:33792\n"
    "v_mfma_f32_16x16x32_bf16 a[44:47], v[160:163], v[140:143], 0\n"
    "ds_read_b128 v[184:187], v201 offset:34816\n"
    "v_mfma_f32_16x16x32_bf16 a[44:47], v[164:167], v[156:159], a[44:47]\n"
    "ds_read_b128 v[188:191], v201 offset:35840\n"
    "v_mfma_f32_16x16x32_bf16 v[124:127], v[192:195], v[140:143], 0\n"
    "v_exp_f32 v45, v45\n"
    "v_mfma_f32_16x16x32_bf16 v[124:127], v[192:195], v[156:159], v[124:127]\n"
    "s_waitcnt lgkmcnt(4)\n"
    "v_exp_f32 v46, v46\n"
    "v_mfma_f32_16x16x32_bf16 a[60:63], v[168:171], v[140:143], 0\n"
    "ds_read_b128 v[160:163], v201 offset:36864\n"
    "v_mfma_f32_16x16x32_bf16 a[60:63], v[172:175], v[156:159], a[60:63]\n"
    "s_waitcnt lgkmcnt(3)\n"
    "ds_read_b128 v[164:167], v201 offset:37888\n"
    "v_mfma_f32_16x16x32_bf16 a[76:79], v[176:179], v[140:143], 0\n"
    "ds_read_b128 v[168:171], v201 offset:38912\n"
    "v_mfma_f32_16x16x32_bf16 a[76:79], v[180:183], v[156:159], a[76:79]\n"
    "s_waitcnt lgkmcnt(3)\n"
    "ds_read_b128 v[172:175], v201 offset:39936\n"
    "v_mfma_f32_16x16x32_bf16 a[92:95], v[184:187], v[140:143], 0\n"
    "ds_read_b128 v[176:179], v201 offset:40960\n"
    "v_mfma_f32_16x16x32_bf16 a[92:95], v[188:191], v[156:159], a[92:95]\n"
    "s_waitcnt lgkmcnt(3)\n"
    "ds_read_b128 v[180:183], v201 offset:41984\n"
    "v_mfma_f32_16x16x32_bf16 a[108:111], v[160:163], v[140:143], 0\n"
    "ds_read_b128 v[184:187], v201 offset:43008\n"
    "v_mfma_f32_16x16x32_bf16 a[108:111], v[164:167], v[156:159], a[108:111]\n"
    "s_waitcnt lgkmcnt(3)\n"
    "ds_read_b128 v[188:191], v201 offset:44032\n"
    "v_mfma_f32_16x16x32_bf16 a[124:127], v[168:171], v[140:143], 0\n"
    "ds_read_b128 v[160:163], v201 offset:45056\n"
    "v_mfma_f32_16x16x32_bf16 a[124:127], v[172:175], v[156:159], a[124:127]\n"
    "s_waitcnt lgkmcnt(3)\n"
    "ds_read_b128 v[164:167], v201 offset:46080\n"
    "v_mfma_f32_16x16x32_bf16 a[140:143], v[176:179], v[140:143], 0\n"
    "ds_read_b128 v[168:171], v201 offset:47104\n"
    "v_mfma_f32_16x16x32_bf16 a[140:143], v[180:183], v[156:159], a[140:143]\n"
    "s_waitcnt lgkmcnt(3)\n"
    "ds_read_b128 v[172:175], v201 offset:48128\n"
    "v_mfma_f32_16x16x32_bf16 a[156:159], v[184:187], v[140:143], 0\n"
    "v_exp_f32 v47, v47\n"
    "v_mfma_f32_16x16x32_bf16 a[156:159], v[188:191], v[156:159], a[156:159]\n"
    "s_waitcnt lgkmcnt(2)\n"
    "v_exp_f32 v60, v60\n"
    "v_mfma_f32_16x16x32_bf16 a[172:175], v[160:163], v[140:143], 0\n"
    "v_exp_f32 v61, v61\n"
    "v_mfma_f32_16x16x32_bf16 a[172:175], v[164:167], v[156:159], a[172:175]\n"
    "s_waitcnt lgkmcnt(0)\n"
    "v_exp_f32 v62, v62\n"
    "v_mfma_f32_16x16x32_bf16 a[188:191], v[168:171], v[140:143], 0\n"
    "v_exp_f32 v63, v63\n"
    "v_mfma_f32_16x16x32_bf16 a[188:191], v[172:175], v[156:159], a[188:191]\n"
    "v_add_f32 v125, v125, v44\n"
    "v_add_f32 v126, v126, v45\n"
    "v_add_f32 v125, v125, v46\n"
    "v_add_f32 v126, v126, v47\n"
    "s_add_u32 s64, s64, 0x3c000\n"
    "s_add_u32 s62, s62, 0x6000\n"
    "s_add_u32 s63, s63, 0x6000\n"
    "s_add_u32 s60, s60, 0x6000\n"
    "s_add_u32 s61, s61, 0x6000\n"
    "v_add_u32 v209, 0x300, v209\n"
    "v_add_u32 v210, 0xc0, v210\n"
    "v_mov_b32 v220, 5\n"
    "s_waitcnt vmcnt(0)\n"
    "v_cmp_ne_u32 s[80:81], s67, v219\n"
    "v_cmp_gt_u32 vcc, s66, v218\n"
    "s_or_b64 vcc, vcc, s[80:81]\n"
    "s_cbranch_vccnz LSPRE2\n"
    "LSPB2:\n"
    "s_barrier\n"
    "s_cmp_eq_u32 s58, 0\n"
    "s_cbranch_scc1 LENT0\n"
    "LENT1:\n"
    "s_mov_b32 s59, 1\n"
    "s_add_u32 m0, s72, 0x21000\n"
    "s_nop 0\n"
    "buffer_load_dwordx4 v250, s[48:51], 0 offen offset:2048 lds\n"
    "s_add_u32 m0, s72, 0x18000\n"
    "s_nop 0\n"
    "buffer_load_dwordx4 v202, s[32:35], s64 offen lds\n"
    "buffer_load_dwordx4 v203, s[32:35], s64 offen offset:1024 lds\n"
    "buffer_load_dwordx4 v204, s[32:35], s64 offen offset:2048 lds\n"
    "s_add_u32 s64, s64, 0xc000\n"
    "s_mov_b32 s94, 0x3000\n"
    "s_mov_b32 s95, 0x6000\n"
    "s_add_u32 s51, s72, 0x18000\n"
    "v_add_u32 v205, 0x3000, v205\n"
    "v_add_u32 v206, 0x3000, v206\n"
    "v_add_u32 v207, 0x3000, v207\n"
    "ds_read_u16_d16_hi v223, v209 offset:6336\n"
    "ds_read_u16_d16_hi v222, v209 offset:192\n"
    "ds_read_b128 v[160:163], v200 offset:24576\n"
    "v_add_f32 v121, v121, v58\n"
    "ds_read_b128 v[232:235], v205 offset:24576\n"
    "v_add_f32 v122, v122, v59\n"
    "ds_read_b128 v[164:167], v200 offset:25600\n"
    "ds_read_b128 v[236:239], v206 offset:24576\n"
    "v_add_f32 v125, v125, v60\n"
    "v_add_f32 v126, v126, v61\n"
    "v_add_f32 v125, v125, v62\n"
    "v_add_f32 v126, v126, v63\n"
    "ds_read_b64 v[228:229], v210 offset:64\n"
    "ds_read2st64_b32 v[240:241], v207 offset0:96 offset1:99\n"
    "ds_read_b128 v[168:171], v200 offset:26624\n"
    "ds_read2st64_b32 v[242:243], v207 offset0:102 offset1:105\n"
    "ds_read2st64_b32 v[244:245], v207 offset0:108 offset1:111\n"
    "ds_read2st64_b32 v[246:247], v207 offset0:114 offset1:117\n"
    "v_cvt_pk_bf16_f32 v140, v44, v45\n"
    "v_cvt_pk_bf16_f32 v141, v46, v47\n"
    "s_waitcnt lgkmcnt(6)\n"
    "v_mul_f32 v226, s75, v223\n"
    "ds_read_b128 v[172:175], v200 offset:27648\n"
    "v_fma_f32 v227, -v222, v226, v217\n"
    "v_cvt_f32_i32 v232, v232\n"
    "v_cvt_f32_i32 v233, v233\n"
    "v_cvt_f32_i32 v234, v234\n"
    "v_cvt_f32_i32 v235, v235\n"
    "s_nop 7\n"
    ".p2align 6\n"
    "LLOOP1:\n"
    "v_mfma_f32_16x16x32_bf16 v[64:67], v[160:163], a[192:195], v[96:99]\n"
    "ds_read_b128 v[176:179], v200 offset:28672\n"
    "v_cvt_f32_i32 v236, v236\n"
    "v_mfma_f32_16x16x32_bf16 v[64:67], v[164:167], a[196:199], v[64:67]\n"
    "s_waitcnt lgkmcnt(1)\n"
    "v_cvt_f32_i32 v237, v237\n"
    "v_cvt_f32_i32 v238, v238\n"
    "v_mfma_f32_16x16x32_bf16 v[64:67], v[168:171], a[200:203], v[64:67]\n"
    "v_cvt_f32_i32 v239, v239\n"
    "v_mul_f32_dpp v232, v226, v232 row_newbcast:0 row_mask:0xf bank_mask:0xf\n"
    "v_mfma_f32_16x16x32_bf16 v[68:71], v[168:171], a[224:227], v[100:103]\n"
    "v_mul_f32_dpp v233, v226, v233 row_newbcast:1 row_mask:0xf bank_mask:0xf\n"
    "v_mul_f32_dpp v234, v226, v234 row_newbcast:2 row_mask:0xf bank_mask:0xf\n"
    "v_mfma_f32_16x16x32_bf16 v[68:71], v[164:167], a[220:223], v[68:71]\n"
    "ds_read_b128 v[180:183], v200 offset:29696\n"
    "v_mul_f32_dpp v235, v226, v235 row_newbcast:3 row_mask:0xf bank_mask:0xf\n"
    "v_mfma_f32_16x16x32_bf16 v[68:71], v[160:163], a[216:219], v[68:71]\n"
    "v_mul_f32_dpp v236, v226, v236 row_newbcast:4 row_mask:0xf bank_mask:0xf\n"
    "v_mul_f32_dpp v237, v226, v237 row_newbcast:5 row_mask:0xf bank_mask:0xf\n"
    "v_mfma_f32_16x16x32_bf16 v[72:75], v[160:163], a[240:243], v[104:107]\n"
    "v_mul_f32_dpp v238, v226, v238 row_newbcast:6 row_mask:0xf bank_mask:0xf\n"
    "v_mul_f32_dpp v239, v226, v239 row_newbcast:7 row_mask:0xf bank_mask:0xf\n"
    "v_mfma_f32_16x16x32_bf16 v[72:75], v[164:167], a[244:247], v[72:75]\n"
    "v_add_f32_dpp v232, v227, v232 row_newbcast:0 row_mask:0xf bank_mask:0xf\n"
    "v_add_f32_dpp v233, v227, v233 row_newbcast:1 row_mask:0xf bank_mask:0xf\n"
    "v_mfma_f32_16x16x32_bf16 v[72:75], v[168:171], a[248:251], v[72:75]\n"
    "v_add_f32_dpp v234, v227, v234 row_newbcast:2 row_mask:0xf bank_mask:0xf\n"
    "v_add_f32_dpp v235, v227, v235 row_newbcast:3 row_mask:0xf bank_mask:0xf\n"
    "v_mfma_f32_16x16x32_bf16 v[76:79], v[168:171], v[16:19], v[108:111]\n"
    "v_add_f32_dpp v236, v227, v236 row_newbcast:4 row_mask:0xf bank_mask:0xf\n"
    "v_add_f32_dpp v237, v227, v237 row_newbcast:5 row_mask:0xf bank_mask:0xf\n"
    "v_mfma_f32_16x16x32_bf16 v[76:79], v[164:167], v[12:15], v[76:79]\n"
    "v_add_f32_dpp v238, v227, v238 row_newbcast:6 row_mask:0xf bank_mask:0xf\n"
    "v_add_f32_dpp v239, v227, v239 row_newbcast:7 row_mask:0xf bank_mask:0xf\n"
    "v_mfma_f32_16x16x32_bf16 v[76:79], v[160:163], v[8:11], v[76:79]\n"
    "s_waitcnt lgkmcnt(1)\n"
    "v_cvt_pk_bf16_f32 v232, v232, v233\n"
    "v_cvt_f32_i32 v240, v240\n"
    "v_mfma_f32_16x16x32_bf16 v[76:79], v[172:175], v[20:23], v[76:79]\n"
    "ds_read_b128 v[160:163], v200 offset:33792\n"
    "v_cvt_pk_bf16_f32 v233, v234, v235\n"
    "v_mfma_f32_16x16x32_bf16 v[76:79], v[176:179], v[24:27], v[76:79]\n"
    "s_waitcnt lgkmcnt(1)\n"
    "v_cvt_f32_i32 v241, v241\n"
    "v_cvt_pk_bf16_f32 v234, v236, v237\n"
    "v_mfma_f32_16x16x32_bf16 v[76:79], v[180:183], v[28:31], v[76:79]\n"
    "v_cvt_f32_i32 v242, v242\n"
    "v_cvt_pk_bf16_f32 v235, v238, v239\n"
    "v_mfma_f32_16x16x32_bf16 v[72:75], v[180:183], v[4:7], v[72:75]\n"
    "ds_read_b128 v[164:167], v200 offset:34816\n"
    "v_cvt_f32_i32 v243, v243\n"
    "v_mfma_f32_16x16x32_bf16 v[72:75], v[176:179], v[0:3], v[72:75]\n"
    "v_cvt_f32_i32 v244, v244\n"
    "v_cvt_f32_i32 v245, v245\n"
    "v_mfma_f32_16x16x32_bf16 v[72:75], v[172:175], a[252:255], v[72:75]\n"
    "buffer_store_dwordx4 v[232:235], v211, s[36:39], s62 offen sc1\n"
    "v_cvt_f32_i32 v246, v246\n"
    "v_cvt_f32_i32 v247, v247\n"
    "v_mul_f32_dpp v240, v228, v240 row_newbcast:0 row_mask:0xf bank_mask:0xf\n"
    "v_mfma_f32_16x16x32_bf16 v[68:71], v[172:175], a[228:231], v[68:71]\n"
    "ds_read_b128 v[168:171], v200 offset:35840\n"
    "v_mul_f32_dpp v241, v228, v241 row_newbcast:1 row_mask:0xf bank_mask:0xf\n"
    "v_mfma_f32_16x16x32_bf16 v[68:71], v[176:179], a[232:235], v[68:71]\n"
    "v_mul_f32_dpp v242, v228, v242 row_newbcast:2 row_mask:0xf bank_mask:0xf\n"
    "v_mul_f32_dpp v243, v228, v243 row_newbcast:3 row_mask:0xf bank_mask:0xf\n"
    "v_mfma_f32_16x16x32_bf16 v[68:71], v[180:183], a[236:239], v[68:71]\n"
    "v_mul_f32_dpp v244, v228, v244 row_newbcast:4 row_mask:0xf bank_mask:0xf\n"
    "v_mul_f32_dpp v245, v228, v245 row_newbcast:5 row_mask:0xf bank_mask:0xf\n"
    "v_mfma_f32_16x16x32_bf16 v[64:67], v[180:183], a[212:215], v[64:67]\n"
    "v_mul_f32_dpp v246, v228, v246 row_newbcast:6 row_mask:0xf bank_mask:0xf\n"
    "v_mul_f32_dpp v247, v228, v247 row_newbcast:7 row_mask:0xf bank_mask:0xf\n"
    "v_mfma_f32_16x16x32_bf16 v[64:67], v[176:179], a[208:211], v[64:67]\n"
    "v_add_f32_dpp v240, v229, v240 row_newbcast:0 row_mask:0xf bank_mask:0xf\n"
    "v_add_f32_dpp v241, v229, v241 row_newbcast:1 row_mask:0xf bank_mask:0xf\n"
    "v_mfma_f32_16x16x32_bf16 v[64:67], v[172:175], a[204:207], v[64:67]\n"
    "s_waitcnt lgkmcnt(1)\n"
    "v_add_f32_dpp v242, v229, v242 row_newbcast:2 row_mask:0xf bank_mask:0xf\n"
    "v_add_f32_dpp v243, v229, v243 row_newbcast:3 row_mask:0xf bank_mask:0xf\n"
    "v_mfma_f32_16x16x32_bf16 v[80:83], v[160:163], a[204:207], v[96:99]\n"
    "ds_read_b128 v[172:175], v200 offset:30720\n"
    "v_add_f32_dpp v244, v229, v244 row_newbcast:4 row_mask:0xf bank_mask:0xf\n"
    "v_mfma_f32_16x16x32_bf16 v[80:83], v[164:167], a[208:211], v[80:83]\n"
    "s_waitcnt lgkmcnt(1)\n"
    "v_add_f32_dpp v245, v229, v245 row_newbcast:5 row_mask:0xf bank_mask:0xf\n"
    "v_add_f32_dpp v246, v229, v246 row_newbcast:6 row_mask:0xf bank_mask:0xf\n"
    "v_mfma_f32_16x16x32_bf16 v[80:83], v[168:171], a[212:215], v[80:83]\n"
    "v_add_f32_dpp v247, v229, v247 row_newbcast:7 row_mask:0xf bank_mask:0xf\n"
    "v_cvt_pk_bf16_f32 v240, v240, v241\n"
    "v_mfma_f32_16x16x32_bf16 v[84:87], v[168:171], a[236:239], v[100:103]\n"
    "ds_read_b128 v[176:179], v200 offset:31744\n"
    "v_cvt_pk_bf16_f32 v241, v242, v243\n"
    "v_mfma_f32_16x16x32_bf16 v[84:87], v[164:167], a[232:235], v[84:87]\n"
    "v_cvt_pk_bf16_f32 v242, v244, v245\n"
    "v_cvt_pk_bf16_f32 v243, v246, v247\n"
    "v_mfma_f32_16x16x32_bf16 v[84:87], v[160:163], a[228:231], v[84:87]\n"
    "buffer_store_dwordx4 v[240:243], v212, s[40:43], s63 offen sc1\n"
    "v_cvt_pk_bf16_f32 v136, v40, v41\n"
    "v_cvt_pk_bf16_f32 v137, v42, v43\n"
    "v_cvt_pk_bf16_f32 v132, v36, v37\n"
    "v_mfma_f32_16x16x32_bf16 v[88:91], v[160:163], a[252:255], v[104:107]\n"
    "ds_read_b128 v[180:183], v200 offset:32768\n"
    "v_cvt_pk_bf16_f32 v133, v38, v39\n"
    "v_mfma_f32_16x16x32_bf16 v[88:91], v[164:167], v[0:3], v[88:91]\n"
    "v_cvt_pk_bf16_f32 v128, v32, v33\n"
    "v_cvt_pk_bf16_f32 v129, v34, v35\n"
    "v_mfma_f32_16x16x32_bf16 v[88:91], v[168:171], v[4:7], v[88:91]\n"
    "v_exp_f32 v64, v64\n"
    "v_mfma_f32_16x16x32_bf16 v[92:95], v[168:171], v[28:31], v[108:111]\n"
    "v_exp_f32 v65, v65\n"
    "v_mfma_f32_16x16x32_bf16 v[92:95], v[164:167], v[24:27], v[92:95]\n"
    "v_exp_f32 v66, v66\n"
    "v_mfma_f32_16x16x32_bf16 v[92:95], v[160:163], v[20:23], v[92:95]\n"
    "s_waitcnt lgkmcnt(1)\n"
    "v_exp_f32 v67, v67\n"
    "v_mfma_f32_16x16x32_bf16 v[92:95], v[172:175], v[8:11], v[92:95]\n"
    "ds_read_b128 v[160:163], v200 offset:36864\n"
    "v_cvt_pk_bf16_f32 v142, v60, v61\n"
    "v_mfma_f32_16x16x32_bf16 v[92:95], v[176:179], v[12:15], v[92:95]\n"
    "s_waitcnt lgkmcnt(1)\n"
    "s_add_u32 s65, s60, s92\n"
    "v_cvt_pk_bf16_f32 v143, v62, v63\n"
    "v_mfma_f32_16x16x32_bf16 v[92:95], v[180:183], v[16:19], v[92:95]\n"
    "s_add_u32 m0, s90, 0\n"
    "v_exp_f32 v68, v68\n"
    "v_mfma_f32_16x16x32_bf16 v[88:91], v[180:183], a[248:251], v[88:91]\n"
    "ds_read_b128 v[164:167], v200 offset:37888\n"
    "v_cvt_pk_bf16_f32 v138, v56, v57\n"
    "v_mfma_f32_16x16x32_bf16 v[88:91], v[176:179], a[244:247], v[88:91]\n"
    "buffer_load_dwordx4 v200, s[36:39], s65 offen sc1 lds\n"
    "v_mfma_f32_16x16x32_bf16 v[88:91], v[172:175], a[240:243], v[88:91]\n"
    "v_exp_f32 v69, v69\n"
    "v_mfma_f32_16x16x32_bf16 v[84:87], v[172:175], a[216:219], v[84:87]\n"
    "ds_read_b128 v[168:171], v200 offset:38912\n"
    "v_cvt_pk_bf16_f32 v139, v58, v59\n"
    "v_mfma_f32_16x16x32_bf16 v[84:87], v[176:179], a[220:223], v[84:87]\n"
    "v_exp_f32 v70, v70\n"
    "v_mfma_f32_16x16x32_bf16 v[84:87], v[180:183], a[224:227], v[84:87]\n"
    "buffer_load_dwordx4 v200, s[36:39], s65 offen offset:1024 sc1 lds\n"
    "v_mfma_f32_16x16x32_bf16 v[80:83], v[180:183], a[200:203], v[80:83]\n"
    "v_exp_f32 v71, v71\n"
    "v_mfma_f32_16x16x32_bf16 v[80:83], v[176:179], a[196:199], v[80:83]\n"
    "v_exp_f32 v72, v72\n"
    "v_mfma_f32_16x16x32_bf16 v[80:83], v[172:175], a[192:195], v[80:83]\n"
    "s_waitcnt lgkmcnt(1)\n"
    "v_cvt_pk_bf16_f32 v134, v52, v53\n"
    "v_cvt_pk_bf16_f32 v135, v54, v55\n"
    "v_mfma_f32_16x16x32_bf16 v[32:35], v[160:163], a[192:195], v[96:99]\n"
    "ds_read_b128 v[172:175], v200 offset:39936\n"
    "v_cvt_pk_bf16_f32 v130, v48, v49\n"
    "v_mfma_f32_16x16x32_bf16 v[32:35], v[164:167], a[196:199], v[32:35]\n"
    "s_waitcnt lgkmcnt(1)\n"
    "buffer_load_dwordx4 v200, s[36:39], s65 offen offset:2048 sc1 lds\n"
    "v_mfma_f32_16x16x32_bf16 v[32:35], v[168:171], a[200:203], v[32:35]\n"
    "v_exp_f32 v73, v73\n"
    "v_mfma_f32_16x16x32_bf16 v[36:39], v[168:171], a[224:227], v[100:103]\n"
    "ds_read_b128 v[176:179], v200 offset:40960\n"
    "v_cvt_pk_bf16_f32 v131, v50, v51\n"
    "v_mfma_f32_16x16x32_bf16 v[36:39], v[164:167], a[220:223], v[36:39]\n"
    "v_exp_f32 v74, v74\n"
    "v_mfma_f32_16x16x32_bf16 v[36:39], v[160:163], a[216:219], v[36:39]\n"
    "buffer_load_dwordx4 v200, s[36:39], s65 offen offset:3072 sc1 lds\n"
    "v_mfma_f32_16x16x32_bf16 v[40:43], v[160:163], a[240:243], v[104:107]\n"
    "s_add_u32 s65, s65, 0x1000\n"
    "ds_read_b128 v[180:183], v200 offset:41984\n"
    "v_cvt_pk_bf16_f32 v144, v64, v65\n"
    "v_mfma_f32_16x16x32_bf16 v[40:43], v[164:167], a[244:247], v[40:43]\n"
    "s_add_u32 m0, s90, 0x1000\n"
    "v_exp_f32 v75, v75\n"
    "v_mfma_f32_16x16x32_bf16 v[40:43], v[168:171], a[248:251], v[40:43]\n"
    "v_exp_f32 v76, v76\n"
    "v_mfma_f32_16x16x32_bf16 v[44:47], v[168:171], v[16:19], v[108:111]\n"
    "buffer_load_dwordx4 v200, s[36:39], s65 offen sc1 lds\n"
    "v_mfma_f32_16x16x32_bf16 v[44:47], v[164:167], v[12:15], v[44:47]\n"
    "v_exp_f32 v77, v77\n"
    "v_mfma_f32_16x16x32_bf16 v[44:47], v[160:163], v[8:11], v[44:47]\n"
    "s_waitcnt lgkmcnt(1)\n"
    "v_exp_f32 v78, v78\n"
    "v_mfma_f32_16x16x32_bf16 v[44:47], v[172:175], v[20:23], v[44:47]\n"
    "ds_read_b128 v[160:163], v200 offset:46080\n"
    "v_cvt_pk_bf16_f32 v145, v66, v67\n"
    "v_mfma_f32_16x16x32_bf16 v[44:47], v[176:179], v[24:27], v[44:47]\n"
    "s_waitcnt lgkmcnt(1)\n"
    "buffer_load_dwordx4 v200, s[36:39], s65 offen offset:1024 sc1 lds\n"
    "v_mfma_f32_16x16x32_bf16 v[44:47], v[180:183], v[28:31], v[44:47]\n"
    "v_exp_f32 v79, v79\n"
    "v_mfma_f32_16x16x32_bf16 v[40:43], v[180:183], v[4:7], v[40:43]\n"
    "ds_read_b128 v[164:167], v200 offset:47104\n"
    "v_cvt_pk_bf16_f32 v148, v68, v69\n"
    "v_mfma_f32_16x16x32_bf16 v[40:43], v[176:179], v[0:3], v[40:43]\n"
    "v_exp_f32 v80, v80\n"
    "v_mfma_f32_16x16x32_bf16 v[40:43], v[172:175], a[252:255], v[40:43]\n"
    "buffer_load_dwordx4 v200, s[36:39], s65 offen offset:2048 sc1 lds\n"
    "v_mfma_f32_16x16x32_bf16 v[36:39], v[172:175], a[228:231], v[36:39]\n"
    "ds_read_b128 v[168:171], v200 offset:48128\n"
    "v_cvt_pk_bf16_f32 v149, v70, v71\n"
    "v_mfma_f32_16x16x32_bf16 v[36:39], v[176:179], a[232:235], v[36:39]\n"
    "s_add_u32 m0, s71, 0x12000\n"
    "v_exp_f32 v81, v81\n"
    "v_mfma_f32_16x16x32_bf16 v[36:39], v[180:183], a[236:239], v[36:39]\n"
    "v_exp_f32 v82, v82\n"
    "v_mfma_f32_16x16x32_bf16 v[32:35], v[180:183], a[212:215], v[32:35]\n"
    "buffer_load_dwordx4 v200, s[40:43], s61 offen sc1 lds\n"
    "v_mfma_f32_16x16x32_bf16 v[32:35], v[176:179], a[208:211], v[32:35]\n"
    "v_exp_f32 v83, v83\n"
    "v_mfma_f32_16x16x32_bf16 v[32:35], v[172:175], a[204:207], v[32:35]\n"
    "s_waitcnt lgkmcnt(1)\n"
    "v_exp_f32 v84, v84\n"
    "v_mfma_f32_16x16x32_bf16 v[48:51], v[160:163], a[204:207], v[96:99]\n"
    "ds_read_b128 v[172:175], v200 offset:43008\n"
    "v_cvt_pk_bf16_f32 v152, v72, v73\n"
    "v_mfma_f32_16x16x32_bf16 v[48:51], v[164:167], a[208:211], v[48:51]\n"
    "s_waitcnt lgkmcnt(1)\n"
    "buffer_load_dwordx4 v200, s[40:43], s61 offen offset:1024 sc1 lds\n"
    "v_mfma_f32_16x16x32_bf16 v[48:51], v[168:171], a[212:215], v[48:51]\n"
    "v_exp_f32 v85, v85\n"
    "v_mfma_f32_16x16x32_bf16 v[52:55], v[168:171], a[236:239], v[100:103]\n"
    "ds_read_b128 v[176:179], v200 offset:44032\n"
    "v_cvt_pk_bf16_f32 v153, v74, v75\n"
    "v_mfma_f32_16x16x32_bf16 v[52:55], v[164:167], a[232:235], v[52:55]\n"
    "v_exp_f32 v86, v86\n"
    "v_mfma_f32_16x16x32_bf16 v[52:55], v[160:163], a[228:231], v[52:55]\n"
    "buffer_load_dwordx4 v200, s[40:43], s61 offen offset:2048 sc1 lds\n"
    "v_mfma_f32_16x16x32_bf16 v[56:59], v[160:163], a[252:255], v[104:107]\n"
    "ds_read_b128 v[180:183], v200 offset:45056\n"
    "v_cvt_pk_bf16_f32 v156, v76, v77\n"
    "v_mfma_f32_16x16x32_bf16 v[56:59], v[164:167], v[0:3], v[56:59]\n"
    "v_exp_f32 v87, v87\n"
    "v_mfma_f32_16x16x32_bf16 v[56:59], v[168:171], v[4:7], v[56:59]\n"
    "v_exp_f32 v88, v88\n"
    "v_mfma_f32_16x16x32_bf16 v[60:63], v[168:171], v[28:31], v[108:111]\n"
    "buffer_load_dwordx4 v200, s[40:43], s61 offen offset:3072 sc1 lds\n"
    "v_mfma_f32_16x16x32_bf16 v[60:63], v[164:167], v[24:27], v[60:63]\n"
    "s_add_u32 s65, s61, 0x1000\n"
    "v_exp_f32 v89, v89\n"
    "v_mfma_f32_16x16x32_bf16 v[60:63], v[160:163], v[20:23], v[60:63]\n"
    "s_waitcnt lgkmcnt(1)\n"
    "s_add_u32 m0, s71, 0x13000\n"
    "v_cvt_pk_bf16_f32 v157, v78, v79\n"
    "v_mfma_f32_16x16x32_bf16 v[60:63], v[172:175], v[8:11], v[60:63]\n"
    "ds_read_b128 v[160:163], v201\n"
    "v_cvt_pk_bf16_f32 v146, v80, v81\n"
    "v_mfma_f32_16x16x32_bf16 v[60:63], v[176:179], v[12:15], v[60:63]\n"
    "s_waitcnt lgkmcnt(1)\n"
    "buffer_load_dwordx4 v200, s[40:43], s65 offen sc1 lds\n"
    "v_mfma_f32_16x16x32_bf16 v[60:63], v[180:183], v[16:19], v[60:63]\n"
    "v_exp_f32 v90, v90\n"
    "v_mfma_f32_16x16x32_bf16 v[56:59], v[180:183], a[248:251], v[56:59]\n"
    "ds_read_b128 v[164:167], v201 offset:1024\n"
    "v_cvt_pk_bf16_f32 v147, v82, v83\n"
    "v_mfma_f32_16x16x32_bf16 v[56:59], v[176:179], a[244:247], v[56:59]\n"
    "v_exp_f32 v91, v91\n"
    "v_mfma_f32_16x16x32_bf16 v[56:59], v[172:175], a[240:243], v[56:59]\n"
    "buffer_load_dwordx4 v200, s[40:43], s65 offen offset:1024 sc1 lds\n"
    "v_mfma_f32_16x16x32_bf16 v[52:55], v[172:175], a[216:219], v[52:55]\n"
    "ds_read_b128 v[168:171], v201 offset:2048\n"
    "v_cvt_pk_bf16_f32 v150, v84, v85\n"
    "v_mfma_f32_16x16x32_bf16 v[52:55], v[176:179], a[220:223], v[52:55]\n"
    "v_exp_f32 v92, v92\n"
    "v_mfma_f32_16x16x32_bf16 v[52:55], v[180:183], a[224:227], v[52:55]\n"
    "v_exp_f32 v93, v93\n"
    "v_mfma_f32_16x16x32_bf16 v[48:51], v[180:183], a[200:203], v[48:51]\n"
    "v_cvt_pk_bf16_f32 v151, v86, v87\n"
    "v_cvt_pk_bf16_f32 v154, v88, v89\n"
    "v_mfma_f32_16x16x32_bf16 v[48:51], v[176:179], a[196:199], v[48:51]\n"
    "v_exp_f32 v94, v94\n"
    "v_mfma_f32_16x16x32_bf16 v[48:51], v[172:175], a[192:195], v[48:51]\n"
    "s_waitcnt lgkmcnt(1)\n"
    "v_cvt_pk_bf16_f32 v155, v90, v91\n"
    "v_cvt_pk_bf16_f32 v158, v92, v93\n"
    "v_mfma_f32_16x16x32_bf16 a[0:3], v[160:163], v[128:131], a[0:3]\n"
    "ds_read_b128 v[172:175], v201 offset:3072\n"
    "ds_read_b128 v[176:179], v201 offset:4096\n"
    "v_mfma_f32_16x16x32_bf16 a[0:3], v[164:167], v[144:147], a[0:3]\n"
    "v_exp_f32 v95, v95\n"
    "v_mfma_f32_16x16x32_bf16 a[4:7], v[160:163], v[132:135], a[4:7]\n"
    "v_cvt_pk_bf16_f32 v159, v94, v95\n"
    "v_add_f32 v113, v113, v64\n"
    "v_mfma_f32_16x16x32_bf16 a[4:7], v[164:167], v[148:151], a[4:7]\n"
    "v_exp_f32 v32, v32\n"
    "v_mfma_f32_16x16x32_bf16 a[8:11], v[160:163], v[136:139], a[8:11]\n"
    "ds_read_b128 v[180:183], v201 offset:5120\n"
    "v_add_f32 v114, v114, v65\n"
    "v_mfma_f32_16x16x32_bf16 a[8:11], v[164:167], v[152:155], a[8:11]\n"
    "v_exp_f32 v33, v33\n"
    "v_mfma_f32_16x16x32_bf16 a[12:15], v[160:163], v[140:143], a[12:15]\n"
    "v_exp_f32 v34, v34\n"
    "v_mfma_f32_16x16x32_bf16 a[12:15], v[164:167], v[156:159], a[12:15]\n"
    "s_waitcnt lgkmcnt(2)\n"
    "v_exp_f32 v35, v35\n"
    "v_mfma_f32_16x16x32_bf16 a[28:31], v[168:171], v[140:143], a[28:31]\n"
    "ds_read_b128 v[160:163], v201 offset:6144\n"
    "v_add_f32 v113, v113, v66\n"
    "v_mfma_f32_16x16x32_bf16 a[28:31], v[172:175], v[156:159], a[28:31]\n"
    "v_exp_f32 v36, v36\n"
    "v_mfma_f32_16x16x32_bf16 a[24:27], v[172:175], v[152:155], a[24:27]\n"
    "v_exp_f32 v37, v37\n"
    "v_mfma_f32_16x16x32_bf16 a[24:27], v[168:171], v[136:139], a[24:27]\n"
    "v_exp_f32 v38, v38\n"
    "v_mfma_f32_16x16x32_bf16 a[20:23], v[168:171], v[132:135], a[20:23]\n"
    "ds_read_b128 v[164:167], v201 offset:7168\n"
    "v_add_f32 v114, v114, v67\n"
    "v_mfma_f32_16x16x32_bf16 a[20:23], v[172:175], v[148:151], a[20:23]\n"
    "v_exp_f32 v39, v39\n"
    "v_mfma_f32_16x16x32_bf16 a[16:19], v[172:175], v[144:147], a[16:19]\n"
    "v_exp_f32 v40, v40\n"
    "v_mfma_f32_16x16x32_bf16 a[16:19], v[168:171], v[128:131], a[16:19]\n"
    "s_waitcnt lgkmcnt(2)\n"
    "v_exp_f32 v41, v41\n"
    "v_mfma_f32_16x16x32_bf16 a[32:35], v[176:179], v[128:131], a[32:35]\n"
    "ds_read_b128 v[168:171], v201 offset:8192\n"
    "v_add_f32 v117, v117, v68\n"
    "v_mfma_f32_16x16x32_bf16 a[32:35], v[180:183], v[144:147], a[32:35]\n"
    "v_exp_f32 v42, v42\n"
    "v_mfma_f32_16x16x32_bf16 a[36:39], v[180:183], v[148:151], a[36:39]\n"
    "v_exp_f32 v43, v43\n"
    "v_mfma_f32_16x16x32_bf16 a[36:39], v[176:179], v[132:135], a[36:39]\n"
    "v_exp_f32 v44, v44\n"
    "v_mfma_f32_16x16x32_bf16 a[40:43], v[176:179], v[136:139], a[40:43]\n"
    "ds_read_b128 v[172:175], v201 offset:9216\n"
    "v_add_f32 v118, v118, v69\n"
    "v_mfma_f32_16x16x32_bf16 a[40:43], v[180:183], v[152:155], a[40:43]\n"
    "v_exp_f32 v45, v45\n"
    "v_mfma_f32_16x16x32_bf16 a[44:47], v[180:183], v[156:159], a[44:47]\n"
    "v_exp_f32 v46, v46\n"
    "v_mfma_f32_16x16x32_bf16 a[44:47], v[176:179], v[140:143], a[44:47]\n"
    "s_waitcnt lgkmcnt(2)\n"
    "v_exp_f32 v47, v47\n"
    "v_mfma_f32_16x16x32_bf16 a[60:63], v[160:163], v[140:143], a[60:63]\n"
    "ds_read_b128 v[176:179], v201 offset:10240\n"
    "v_add_f32 v117, v117, v70\n"
    "v_mfma_f32_16x16x32_bf16 a[60:63], v[164:167], v[156:159], a[60:63]\n"
    "v_exp_f32 v48, v48\n"
    "v_mfma_f32_16x16x32_bf16 a[56:59], v[164:167], v[152:155], a[56:59]\n"
    "v_exp_f32 v49, v49\n"
    "v_mfma_f32_16x16x32_bf16 a[56:59], v[160:163], v[136:139], a[56:59]\n"
    "v_exp_f32 v50, v50\n"
    "v_mfma_f32_16x16x32_bf16 a[52:55], v[160:163], v[132:135], a[52:55]\n"
    "ds_read_b128 v[180:183], v201 offset:11264\n"
    "v_add_f32 v113, v113, v32\n"
    "v_mfma_f32_16x16x32_bf16 a[52:55], v[164:167], v[148:151], a[52:55]\n"
    "v_exp_f32 v51, v51\n"
    "v_mfma_f32_16x16x32_bf16 a[48:51], v[164:167], v[144:147], a[48:51]\n"
    "v_exp_f32 v52, v52\n"
    "v_mfma_f32_16x16x32_bf16 a[48:51], v[160:163], v[128:131], a[48:51]\n"
    "s_waitcnt lgkmcnt(2)\n"
    "v_add_f32 v118, v118, v71\n"
    "v_add_f32 v114, v114, v33\n"
    "v_mfma_f32_16x16x32_bf16 a[64:67], v[168:171], v[128:131], a[64:67]\n"
    "ds_read_b128 v[160:163], v201 offset:12288\n"
    "v_add_f32 v121, v121, v72\n"
    "v_mfma_f32_16x16x32_bf16 a[64:67], v[172:175], v[144:147], a[64:67]\n"
    "v_add_f32 v113, v113, v34\n"
    "v_add_f32 v122, v122, v73\n"
    "v_mfma_f32_16x16x32_bf16 a[68:71], v[172:175], v[148:151], a[68:71]\n"
    "v_exp_f32 v53, v53\n"
    "v_mfma_f32_16x16x32_bf16 a[68:71], v[168:171], v[132:135], a[68:71]\n"
    "s_add_u32 m0, s51, s95\n"
    "v_add_f32 v114, v114, v35\n"
    "v_add_f32 v121, v121, v74\n"
    "v_mfma_f32_16x16x32_bf16 a[72:75], v[168:171], v[136:139], a[72:75]\n"
    "ds_read_b128 v[164:167], v201 offset:13312\n"
    "v_add_f32 v117, v117, v36\n"
    "v_mfma_f32_16x16x32_bf16 a[72:75], v[172:175], v[152:155], a[72:75]\n"
    "buffer_load_dwordx4 v202, s[32:35], s64 offen lds\n"
    "v_mfma_f32_16x16x32_bf16 a[76:79], v[172:175], v[156:159], a[76:79]\n"
    "v_exp_f32 v54, v54\n"
    "v_mfma_f32_16x16x32_bf16 a[76:79], v[168:171], v[140:143], a[76:79]\n"
    "s_waitcnt lgkmcnt(2)\n"
    "v_add_f32 v122, v122, v75\n"
    "v_add_f32 v118, v118, v37\n"
    "v_mfma_f32_16x16x32_bf16 a[92:95], v[176:179], v[140:143], a[92:95]\n"
    "ds_read_b128 v[168:171], v201 offset:14336\n"
    "v_add_f32 v125, v125, v76\n"
    "v_mfma_f32_16x16x32_bf16 a[92:95], v[180:183], v[156:159], a[92:95]\n"
    "buffer_load_dwordx4 v203, s[32:35], s64 offen offset:1024 lds\n"
    "v_mfma_f32_16x16x32_bf16 a[88:91], v[180:183], v[152:155], a[88:91]\n"
    "v_add_f32 v117, v117, v38\n"
    "v_add_f32 v126, v126, v77\n"
    "v_mfma_f32_16x16x32_bf16 a[88:91], v[176:179], v[136:139], a[88:91]\n"
    "v_exp_f32 v55, v55\n"
    "v_mfma_f32_16x16x32_bf16 a[84:87], v[176:179], v[132:135], a[84:87]\n"
    "ds_read_b128 v[172:175], v201 offset:15360\n"
    "v_add_f32 v118, v118, v39\n"
    "v_mfma_f32_16x16x32_bf16 a[84:87], v[180:183], v[148:151], a[84:87]\n"
    "buffer_load_dwordx4 v204, s[32:35], s64 offen offset:2048 lds\n"
    "v_mfma_f32_16x16x32_bf16 a[80:83], v[180:183], v[144:147], a[80:83]\n"
    "v_add_f32 v125, v125, v78\n"
    "v_add_f32 v121, v121, v40\n"
    "v_mfma_f32_16x16x32_bf16 a[80:83], v[176:179], v[128:131], a[80:83]\n"
    "s_waitcnt lgkmcnt(2)\n"
    "v_exp_f32 v56, v56\n"
    "v_mfma_f32_16x16x32_bf16 a[96:99], v[160:163], v[128:131], a[96:99]\n"
    "s_mov_b32 s95, s94\n"
    "ds_read_b128 v[176:179], v201 offset:16384\n"
    "v_add_f32 v126, v126, v79\n"
    "v_mfma_f32_16x16x32_bf16 a[96:99], v[164:167], v[144:147], a[96:99]\n"
    "s_sub_u32 s94, s94, 0x3000\n"
    "v_add_f32 v122, v122, v41\n"
    "v_add_f32 v113, v113, v80\n"
    "v_mfma_f32_16x16x32_bf16 a[100:103], v[164:167], v[148:151], a[100:103]\n"
    "s_min_u32 s94, s94, 0x6000\n"
    "v_add_f32 v121, v121, v42\n"
    "v_add_f32 v114, v114, v81\n"
    "v_mfma_f32_16x16x32_bf16 a[100:103], v[160:163], v[132:135], a[100:103]\n"
    "s_sub_u32 s93, s94, s95\n"
    "v_exp_f32 v57, v57\n"
    "v_mfma_f32_16x16x32_bf16 a[104:107], v[160:163], v[136:139], a[104:107]\n"
    "ds_read_b128 v[180:183], v201 offset:17408\n"
    "v_add_f32 v122, v122, v43\n"
    "v_mfma_f32_16x16x32_bf16 a[104:107], v[164:167], v[152:155], a[104:107]\n"
    "v_add_f32 v113, v113, v82\n"
    "v_add_f32 v125, v125, v44\n"
    "v_mfma_f32_16x16x32_bf16 a[108:111], v[164:167], v[156:159], a[108:111]\n"
    "v_exp_f32 v58, v58\n"
    "v_mfma_f32_16x16x32_bf16 a[108:111], v[160:163], v[140:143], a[108:111]\n"
    "s_waitcnt lgkmcnt(2)\n"
    "v_add_f32 v114, v114, v83\n"
    "v_add_f32 v126, v126, v45\n"
    "v_mfma_f32_16x16x32_bf16 a[124:127], v[168:171], v[140:143], a[124:127]\n"
    "ds_read_b128 v[160:163], v201 offset:18432\n"
    "v_add_f32 v117, v117, v84\n"
    "v_mfma_f32_16x16x32_bf16 a[124:127], v[172:175], v[156:159], a[124:127]\n"
    "v_add_f32 v125, v125, v46\n"
    "v_add_f32 v118, v118, v85\n"
    "v_mfma_f32_16x16x32_bf16 a[120:123], v[172:175], v[152:155], a[120:123]\n"
    "v_exp_f32 v59, v59\n"
    "v_mfma_f32_16x16x32_bf16 a[120:123], v[168:171], v[136:139], a[120:123]\n"
    "v_add_f32 v126, v126, v47\n"
    "v_add_f32 v117, v117, v86\n"
    "v_mfma_f32_16x16x32_bf16 a[116:119], v[168:171], v[132:135], a[116:119]\n"
    "ds_read_b128 v[164:167], v201 offset:19456\n"
    "v_add_f32 v113, v113, v48\n"
    "v_mfma_f32_16x16x32_bf16 a[116:119], v[172:175], v[148:151], a[116:119]\n"
    "v_exp_f32 v60, v60\n"
    "v_mfma_f32_16x16x32_bf16 a[112:115], v[172:175], v[144:147], a[112:115]\n"
    "v_add_u32 v205, s93, v205\n"
    "v_add_f32 v118, v118, v87\n"
    "v_mfma_f32_16x16x32_bf16 a[112:115], v[168:171], v[128:131], a[112:115]\n"
    "s_waitcnt lgkmcnt(2)\n"
    "v_add_f32 v114, v114, v49\n"
    "v_add_u32 v209, 0x180, v209\n"
    "v_mfma_f32_16x16x32_bf16 a[128:131], v[176:179], v[128:131], a[128:131]\n"
    "ds_read_b128 v[168:171], v201 offset:20480\n"
    "v_add_u32 v206, s93, v206\n"
    "v_mfma_f32_16x16x32_bf16 a[128:131], v[180:183], v[144:147], a[128:131]\n"
    "v_add_f32 v121, v121, v88\n"
    "v_add_f32 v113, v113, v50\n"
    "v_mfma_f32_16x16x32_bf16 a[132:135], v[180:183], v[148:151], a[132:135]\n"
    "s_add_u32 s64, s64, 0xc000\n"
    "v_exp_f32 v61, v61\n"
    "v_mfma_f32_16x16x32_bf16 a[132:135], v[176:179], v[132:135], a[132:135]\n"
    "s_add_u32 s62, s62, 0x6000\n"
    "v_add_u32 v210, 0x80, v210\n"
    "v_add_u32 v207, s93, v207\n"
    "v_mfma_f32_16x16x32_bf16 a[136:139], v[176:179], v[136:139], a[136:139]\n"
    "s_add_u32 s63, s63, 0x6000\n"
    "ds_read_b128 v[172:175], v201 offset:21504\n"
    "v_add_f32 v122, v122, v89\n"
    "v_mfma_f32_16x16x32_bf16 a[136:139], v[180:183], v[152:155], a[136:139]\n"
    "s_add_u32 s60, s60, 0x6000\n"
    "v_add_f32 v114, v114, v51\n"
    "v_add_f32 v121, v121, v90\n"
    "v_mfma_f32_16x16x32_bf16 a[140:143], v[180:183], v[156:159], a[140:143]\n"
    "s_add_u32 s61, s61, 0x6000\n"
    "v_add_f32 v117, v117, v52\n"
    "v_add_f32 v122, v122, v91\n"
    "v_mfma_f32_16x16x32_bf16 a[140:143], v[176:179], v[140:143], a[140:143]\n"
    "s_waitcnt lgkmcnt(2)\n"
    "v_exp_f32 v62, v62\n"
    "v_mfma_f32_16x16x32_bf16 a[156:159], v[160:163], v[140:143], a[156:159]\n"
    "ds_read_b128 v[176:179], v201 offset:22528\n"
    "v_add_f32 v118, v118, v53\n"
    "v_mfma_f32_16x16x32_bf16 a[156:159], v[164:167], v[156:159], a[156:159]\n"
    "s_waitcnt vmcnt(3)\n"
    "ds_read_b128 v[180:183], v201 offset:23552\n"
    "v_add_f32 v125, v125, v92\n"
    "v_mfma_f32_16x16x32_bf16 a[152:155], v[164:167], v[152:155], a[152:155]\n"
    "v_add_f32 v117, v117, v54\n"
    "v_add_f32 v126, v126, v93\n"
    "v_mfma_f32_16x16x32_bf16 a[152:155], v[160:163], v[136:139], a[152:155]\n"
    "v_exp_f32 v63, v63\n"
    "v_mfma_f32_16x16x32_bf16 a[148:151], v[160:163], v[132:135], a[148:151]\n"
    "v_add_f32 v118, v118, v55\n"
    "v_add_f32 v125, v125, v94\n"
    "v_mfma_f32_16x16x32_bf16 a[148:151], v[164:167], v[148:151], a[148:151]\n"
    "v_add_f32 v121, v121, v56\n"
    "v_add_f32 v126, v126, v95\n"
    "v_mfma_f32_16x16x32_bf16 a[144:147], v[164:167], v[144:147], a[144:147]\n"
    "v_add_f32 v122, v122, v57\n"
    "v_mfma_f32_16x16x32_bf16 a[144:147], v[160:163], v[128:131], a[144:147]\n"
    "s_waitcnt lgkmcnt(0)\n"
    "v_mfma_f32_16x16x32_bf16 a[160:163], v[168:171], v[128:131], a[160:163]\n"
    "s_barrier\n"
    "v_mfma_f32_16x16x32_bf16 a[160:163], v[172:175], v[144:147], a[160:163]\n"
    "ds_read_u16_d16_hi v223, v209 offset:6144\n"
    "ds_read_u16_d16_hi v222, v209\n"
    "v_mfma_f32_16x16x32_bf16 a[164:167], v[172:175], v[148:151], a[164:167]\n"
    "ds_read_b128 v[160:163], v200\n"
    "v_add_f32 v121, v121, v58\n"
    "v_mfma_f32_16x16x32_bf16 a[164:167], v[168:171], v[132:135], a[164:167]\n"
    "ds_read_b128 v[232:235], v205 offset:24576\n"
    "v_add_f32 v122, v122, v59\n"
    "v_mfma_f32_16x16x32_bf16 a[168:171], v[168:171], v[136:139], a[168:171]\n"
    "ds_read_b128 v[164:167], v200 offset:1024\n"
    "ds_read_b128 v[236:239], v206 offset:24576\n"
    "v_mfma_f32_16x16x32_bf16 a[168:171], v[172:175], v[152:155], a[168:171]\n"
    "v_add_f32 v125, v125, v60\n"
    "v_add_f32 v126, v126, v61\n"
    "v_mfma_f32_16x16x32_bf16 a[172:175], v[172:175], v[156:159], a[172:175]\n"
    "v_add_f32 v125, v125, v62\n"
    "v_add_f32 v126, v126, v63\n"
    "v_mfma_f32_16x16x32_bf16 a[172:175], v[168:171], v[140:143], a[172:175]\n"
    "ds_read_b64 v[228:229], v210\n"
    "ds_read2st64_b32 v[240:241], v207 offset0:96 offset1:99\n"
    "v_mfma_f32_16x16x32_bf16 a[188:191], v[176:179], v[140:143], a[188:191]\n"
    "ds_read_b128 v[168:171], v200 offset:2048\n"
    "ds_read2st64_b32 v[242:243], v207 offset0:102 offset1:105\n"
    "v_mfma_f32_16x16x32_bf16 a[188:191], v[180:183], v[156:159], a[188:191]\n"
    "ds_read2st64_b32 v[244:245], v207 offset0:108 offset1:111\n"
    "ds_read2st64_b32 v[246:247], v207 offset0:114 offset1:117\n"
    "v_mfma_f32_16x16x32_bf16 a[184:187], v[176:179], v[136:139], a[184:187]\n"
    "v_cvt_pk_bf16_f32 v140, v44, v45\n"
    "v_cvt_pk_bf16_f32 v141, v46, v47\n"
    "v_mfma_f32_16x16x32_bf16 a[184:187], v[180:183], v[152:155], a[184:187]\n"
    "s_waitcnt lgkmcnt(6)\n"
    "v_mul_f32 v226, s75, v223\n"
    "v_mfma_f32_16x16x32_bf16 a[180:183], v[176:179], v[132:135], a[180:183]\n"
    "ds_read_b128 v[172:175], v200 offset:3072\n"
    "v_fma_f32 v227, -v222, v226, v217\n"
    "v_mfma_f32_16x16x32_bf16 a[180:183], v[180:183], v[148:151], a[180:183]\n"
    "v_cvt_f32_i32 v232, v232\n"
    "v_cvt_f32_i32 v233, v233\n"
    "v_mfma_f32_16x16x32_bf16 a[176:179], v[176:179], v[128:131], a[176:179]\n"
    "v_cvt_f32_i32 v234, v234\n"
    "v_cvt_f32_i32 v235, v235\n"
    "v_mfma_f32_16x16x32_bf16 a[176:179], v[180:183], v[144:147], a[176:179]\n"
    "v_cvt_f32_i32 v236, v236\n"
    "v_cvt_f32_i32 v237, v237\n"
    "LM01:\n"
    "v_mfma_f32_16x16x32_bf16 v[64:67], v[160:163], a[192:195], v[96:99]\n"
    "ds_read_b128 v[176:179], v200 offset:4096\n"
    "v_cvt_f32_i32 v238, v238\n"
    "v_mfma_f32_16x16x32_bf16 v[64:67], v[164:167], a[196:199], v[64:67]\n"
    "s_waitcnt lgkmcnt(1)\n"
    "v_cvt_f32_i32 v239, v239\n"
    "v_mul_f32_dpp v232, v226, v232 row_newbcast:0 row_mask:0xf bank_mask:0xf\n"
    "v_mfma_f32_16x16x32_bf16 v[64:67], v[168:171], a[200:203], v[64:67]\n"
    "v_mul_f32_dpp v233, v226, v233 row_newbcast:1 row_mask:0xf bank_mask:0xf\n"
    "v_mul_f32_dpp v234, v226, v234 row_newbcast:2 row_mask:0xf bank_mask:0xf\n"
    "v_mfma_f32_16x16x32_bf16 v[68:71], v[168:171], a[224:227], v[100:103]\n"
    "v_mul_f32_dpp v235, v226, v235 row_newbcast:3 row_mask:0xf bank_mask:0xf\n"
    "v_mul_f32_dpp v236, v226, v236 row_newbcast:4 row_mask:0xf bank_mask:0xf\n"
    "v_mfma_f32_16x16x32_bf16 v[68:71], v[164:167], a[220:223], v[68:71]\n"
    "ds_read_b128 v[180:183], v200 offset:5120\n"
    "v_mul_f32_dpp v237, v226, v237 row_newbcast:5 row_mask:0xf bank_mask:0xf\n"
    "v_mfma_f32_16x16x32_bf16 v[68:71], v[160:163], a[216:219], v[68:71]\n"
    "v_mul_f32_dpp v238, v226, v238 row_newbcast:6 row_mask:0xf bank_mask:0xf\n"
    "v_mul_f32_dpp v239, v226, v239 row_newbcast:7 row_mask:0xf bank_mask:0xf\n"
    "v_mfma_f32_16x16x32_bf16 v[72:75], v[160:163], a[240:243], v[104:107]\n"
    "v_add_f32_dpp v232, v227, v232 row_newbcast:0 row_mask:0xf bank_mask:0xf\n"
    "v_add_f32_dpp v233, v227, v233 row_newbcast:1 row_mask:0xf bank_mask:0xf\n"
    "v_mfma_f32_16x16x32_bf16 v[72:75], v[164:167], a[244:247], v[72:75]\n"
    "v_add_f32_dpp v234, v227, v234 row_newbcast:2 row_mask:0xf bank_mask:0xf\n"
    "v_add_f32_dpp v235, v227, v235 row_newbcast:3 row_mask:0xf bank_mask:0xf\n"
    "v_mfma_f32_16x16x32_bf16 v[72:75], v[168:171], a[248:251], v[72:75]\n"
    "v_add_f32_dpp v236, v227, v236 row_newbcast:4 row_mask:0xf bank_mask:0xf\n"
    "v_add_f32_dpp v237, v227, v237 row_newbcast:5 row_mask:0xf bank_mask:0xf\n"
    "v_mfma_f32_16x16x32_bf16 v[76:79], v[168:171], v[16:19], v[108:111]\n"
    "v_add_f32_dpp v238, v227, v238 row_newbcast:6 row_mask:0xf bank_mask:0xf\n"
    "v_add_f32_dpp v239, v227, v239 row_newbcast:7 row_mask:0xf bank_mask:0xf\n"
    "v_mfma_f32_16x16x32_bf16 v[76:79], v[164:167], v[12:15], v[76:79]\n"
    "v_cvt_pk_bf16_f32 v232, v232, v233\n"
    "v_cvt_f32_i32 v240, v240\n"
    "v_mfma_f32_16x16x32_bf16 v[76:79], v[160:163], v[8:11], v[76:79]\n"
    "s_waitcnt lgkmcnt(1)\n"
    "v_cvt_pk_bf16_f32 v233, v234, v235\n"
    "v_cvt_f32_i32 v241, v241\n"
    "v_mfma_f32_16x16x32_bf16 v[76:79], v[172:175], v[20:23], v[76:79]\n"
    "ds_read_b128 v[160:163], v200 offset:9216\n"
    "v_cvt_pk_bf16_f32 v234, v236, v237\n"
    "v_mfma_f32_16x16x32_bf16 v[76:79], v[176:179], v[24:27], v[76:79]\n"
    "s_waitcnt lgkmcnt(1)\n"
    "v_cvt_f32_i32 v242, v242\n"
    "v_cvt_pk_bf16_f32 v235, v238, v239\n"
    "v_mfma_f32_16x16x32_bf16 v[76:79], v[180:183], v[28:31], v[76:79]\n"
    "v_cvt_f32_i32 v243, v243\n"
    "v_cvt_f32_i32 v244, v244\n"
    "v_mfma_f32_16x16x32_bf16 v[72:75], v[180:183], v[4:7], v[72:75]\n"
    "ds_read_b128 v[164:167], v200 offset:10240\n"
    "v_cvt_f32_i32 v245, v245\n"
    "v_mfma_f32_16x16x32_bf16 v[72:75], v[176:179], v[0:3], v[72:75]\n"
    "buffer_store_dwordx4 v[232:235], v211, s[36:39], s62 offen sc1\n"
    "v_cvt_f32_i32 v246, v246\n"
    "v_cvt_f32_i32 v247, v247\n"
    "v_mul_f32_dpp v240, v228, v240 row_newbcast:0 row_mask:0xf bank_mask:0xf\n"
    "v_mfma_f32_16x16x32_bf16 v[72:75], v[172:175], a[252:255], v[72:75]\n"
    "v_mul_f32_dpp v241, v228, v241 row_newbcast:1 row_mask:0xf bank_mask:0xf\n"
    "v_mul_f32_dpp v242, v228, v242 row_newbcast:2 row_mask:0xf bank_mask:0xf\n"
    "v_mfma_f32_16x16x32_bf16 v[68:71], v[172:175], a[228:231], v[68:71]\n"
    "ds_read_b128 v[168:171], v200 offset:11264\n"
    "v_mul_f32_dpp v243, v228, v243 row_newbcast:3 row_mask:0xf bank_mask:0xf\n"
    "v_mfma_f32_16x16x32_bf16 v[68:71], v[176:179], a[232:235], v[68:71]\n"
    "v_mul_f32_dpp v244, v228, v244 row_newbcast:4 row_mask:0xf bank_mask:0xf\n"
    "v_mul_f32_dpp v245, v228, v245 row_newbcast:5 row_mask:0xf bank_mask:0xf\n"
    "v_mfma_f32_16x16x32_bf16 v[68:71], v[180:183], a[236:239], v[68:71]\n"
    "v_mul_f32_dpp v246, v228, v246 row_newbcast:6 row_mask:0xf bank_mask:0xf\n"
    "v_mul_f32_dpp v247, v228, v247 row_newbcast:7 row_mask:0xf bank_mask:0xf\n"
    "v_mfma_f32_16x16x32_bf16 v[64:67], v[180:183], a[212:215], v[64:67]\n"
    "v_add_f32_dpp v240, v229, v240 row_newbcast:0 row_mask:0xf bank_mask:0xf\n"
    "v_add_f32_dpp v241, v229, v241 row_newbcast:1 row_mask:0xf bank_mask:0xf\n"
    "v_mfma_f32_16x16x32_bf16 v[64:67], v[176:179], a[208:211], v[64:67]\n"
    "v_add_f32_dpp v242, v229, v242 row_newbcast:2 row_mask:0xf bank_mask:0xf\n"
    "v_add_f32_dpp v243, v229, v243 row_newbcast:3 row_mask:0xf bank_mask:0xf\n"
    "v_mfma_f32_16x16x32_bf16 v[64:67], v[172:175], a[204:207], v[64:67]\n"
    "s_waitcnt lgkmcnt(1)\n"
    "v_add_f32_dpp v244, v229, v244 row_newbcast:4 row_mask:0xf bank_mask:0xf\n"
    "v_add_f32_dpp v245, v229, v245 row_newbcast:5 row_mask:0xf bank_mask:0xf\n"
    "v_mfma_f32_16x16x32_bf16 v[80:83], v[160:163], a[204:207], v[96:99]\n"
    "ds_read_b128 v[172:175], v200 offset:6144\n"
    "v_add_f32_dpp v246, v229, v246 row_newbcast:6 row_mask:0xf bank_mask:0xf\n"
    "v_mfma_f32_16x16x32_bf16 v[80:83], v[164:167], a[208:211], v[80:83]\n"
    "s_waitcnt lgkmcnt(1)\n"
    "v_add_f32_dpp v247, v229, v247 row_newbcast:7 row_mask:0xf bank_mask:0xf\n"
    "v_cvt_pk_bf16_f32 v240, v240, v241\n"
    "v_mfma_f32_16x16x32_bf16 v[80:83], v[168:171], a[212:215], v[80:83]\n"
    "v_cvt_pk_bf16_f32 v241, v242, v243\n"
    "v_cvt_pk_bf16_f32 v242, v244, v245\n"
    "v_mfma_f32_16x16x32_bf16 v[84:87], v[168:171], a[236:239], v[100:103]\n"
    "ds_read_b128 v[176:179], v200 offset:7168\n"
    "v_cvt_pk_bf16_f32 v243, v246, v247\n"
    "v_mfma_f32_16x16x32_bf16 v[84:87], v[164:167], a[232:235], v[84:87]\n"
    "buffer_store_dwordx4 v[240:243], v212, s[40:43], s63 offen sc1\n"
    "v_cvt_pk_bf16_f32 v136, v40, v41\n"
    "v_cvt_pk_bf16_f32 v137, v42, v43\n"
    "v_cvt_pk_bf16_f32 v132, v36, v37\n"
    "v_mfma_f32_16x16x32_bf16 v[84:87], v[160:163], a[228:231], v[84:87]\n"
    "v_cvt_pk_bf16_f32 v133, v38, v39\n"
    "v_cvt_pk_bf16_f32 v128, v32, v33\n"
    "v_mfma_f32_16x16x32_bf16 v[88:91], v[160:163], a[252:255], v[104:107]\n"
    "ds_read_b128 v[180:183], v200 offset:8192\n"
    "v_cvt_pk_bf16_f32 v129, v34, v35\n"
    "v_mfma_f32_16x16x32_bf16 v[88:91], v[164:167], v[0:3], v[88:91]\n"
    "v_exp_f32 v64, v64\n"
    "v_mfma_f32_16x16x32_bf16 v[88:91], v[168:171], v[4:7], v[88:91]\n"
    "v_exp_f32 v65, v65\n"
    "v_mfma_f32_16x16x32_bf16 v[92:95], v[168:171], v[28:31], v[108:111]\n"
    "v_exp_f32 v66, v66\n"
    "v_mfma_f32_16x16x32_bf16 v[92:95], v[164:167], v[24:27], v[92:95]\n"
    "v_exp_f32 v67, v67\n"
    "v_mfma_f32_16x16x32_bf16 v[92:95], v[160:163], v[20:23], v[92:95]\n"
    "s_waitcnt lgkmcnt(1)\n"
    "v_exp_f32 v68, v68\n"
    "v_mfma_f32_16x16x32_bf16 v[92:95], v[172:175], v[8:11], v[92:95]\n"
    "ds_read_b128 v[160:163], v200 offset:12288\n"
    "v_cvt_pk_bf16_f32 v142, v60, v61\n"
    "v_mfma_f32_16x16x32_bf16 v[92:95], v[176:179], v[12:15], v[92:95]\n"
    "s_waitcnt lgkmcnt(1)\n"
    "s_add_u32 s65, s60, s92\n"
    "v_cvt_pk_bf16_f32 v143, v62, v63\n"
    "v_mfma_f32_16x16x32_bf16 v[92:95], v[180:183], v[16:19], v[92:95]\n"
    "s_add_u32 m0, s90, 0x6000\n"
    "v_exp_f32 v69, v69\n"
    "v_mfma_f32_16x16x32_bf16 v[88:91], v[180:183], a[248:251], v[88:91]\n"
    "ds_read_b128 v[164:167], v200 offset:13312\n"
    "v_cvt_pk_bf16_f32 v138, v56, v57\n"
    "v_mfma_f32_16x16x32_bf16 v[88:91], v[176:179], a[244:247], v[88:91]\n"
    "buffer_load_dwordx4 v200, s[36:39], s65 offen sc1 lds\n"
    "v_mfma_f32_16x16x32_bf16 v[88:91], v[172:175], a[240:243], v[88:91]\n"
    "v_exp_f32 v70, v70\n"
    "v_mfma_f32_16x16x32_bf16 v[84:87], v[172:175], a[216:219], v[84:87]\n"
    "ds_read_b128 v[168:171], v200 offset:14336\n"
    "v_cvt_pk_bf16_f32 v139, v58, v59\n"
    "v_mfma_f32_16x16x32_bf16 v[84:87], v[176:179], a[220:223], v[84:87]\n"
    "v_exp_f32 v71, v71\n"
    "v_mfma_f32_16x16x32_bf16 v[84:87], v[180:183], a[224:227], v[84:87]\n"
    "buffer_load_dwordx4 v200, s[36:39], s65 offen offset:1024 sc1 lds\n"
    "v_mfma_f32_16x16x32_bf16 v[80:83], v[180:183], a[200:203], v[80:83]\n"
    "v_exp_f32 v72, v72\n"
    "v_mfma_f32_16x16x32_bf16 v[80:83], v[176:179], a[196:199], v[80:83]\n"
    "v_cvt_pk_bf16_f32 v134, v52, v53\n"
    "v_cvt_pk_bf16_f32 v135, v54, v55\n"
    "v_mfma_f32_16x16x32_bf16 v[80:83], v[172:175], a[192:195], v[80:83]\n"
    "s_waitcnt lgkmcnt(1)\n"
    "v_exp_f32 v73, v73\n"
    "v_mfma_f32_16x16x32_bf16 v[32:35], v[160:163], a[192:195], v[96:99]\n"
    "ds_read_b128 v[172:175], v200 offset:15360\n"
    "v_cvt_pk_bf16_f32 v130, v48, v49\n"
    "v_mfma_f32_16x16x32_bf16 v[32:35], v[164:167], a[196:199], v[32:35]\n"
    "s_waitcnt lgkmcnt(1)\n"
    "buffer_load_dwordx4 v200, s[36:39], s65 offen offset:2048 sc1 lds\n"
    "v_mfma_f32_16x16x32_bf16 v[32:35], v[168:171], a[200:203], v[32:35]\n"
    "v_cvt_pk_bf16_f32 v131, v50, v51\n"
    "v_cvt_pk_bf16_f32 v144, v64, v65\n"
    "v_mfma_f32_16x16x32_bf16 v[36:39], v[168:171], a[224:227], v[100:103]\n"
    "ds_read_b128 v[176:179], v200 offset:16384\n"
    "v_cvt_pk_bf16_f32 v145, v66, v67\n"
    "v_mfma_f32_16x16x32_bf16 v[36:39], v[164:167], a[220:223], v[36:39]\n"
    "v_exp_f32 v74, v74\n"
    "v_mfma_f32_16x16x32_bf16 v[36:39], v[160:163], a[216:219], v[36:39]\n"
    "buffer_load_dwordx4 v200, s[36:39], s65 offen offset:3072 sc1 lds\n"
    "v_mfma_f32_16x16x32_bf16 v[40:43], v[160:163], a[240:243], v[104:107]\n"
    "s_add_u32 s65, s65, 0x1000\n"
    "ds_read_b128 v[180:183], v200 offset:17408\n"
    "v_cvt_pk_bf16_f32 v148, v68, v69\n"
    "v_mfma_f32_16x16x32_bf16 v[40:43], v[164:167], a[244:247], v[40:43]\n"
    "s_add_u32 m0, s90, 0x7000\n"
    "v_exp_f32 v75, v75\n"
    "v_mfma_f32_16x16x32_bf16 v[40:43], v[168:171], a[248:251], v[40:43]\n"
    "v_exp_f32 v76, v76\n"
    "v_mfma_f32_16x16x32_bf16 v[44:47], v[168:171], v[16:19], v[108:111]\n"
    "buffer_load_dwordx4 v200, s[36:39], s65 offen sc1 lds\n"
    "v_mfma_f32_16x16x32_bf16 v[44:47], v[164:167], v[12:15], v[44:47]\n"
    "v_exp_f32 v77, v77\n"
    "v_mfma_f32_16x16x32_bf16 v[44:47], v[160:163], v[8:11], v[44:47]\n"
    "s_waitcnt lgkmcnt(1)\n"
    "v_exp_f32 v78, v78\n"
    "v_mfma_f32_16x16x32_bf16 v[44:47], v[172:175], v[20:23], v[44:47]\n"
    "ds_read_b128 v[160:163], v200 offset:21504\n"
    "v_cvt_pk_bf16_f32 v149, v70, v71\n"
    "v_mfma_f32_16x16x32_bf16 v[44:47], v[176:179], v[24:27], v[44:47]\n"
    "s_waitcnt lgkmcnt(1)\n"
    "buffer_load_dwordx4 v200, s[36:39], s65 offen offset:1024 sc1 lds\n"
    "v_mfma_f32_16x16x32_bf16 v[44:47], v[180:183], v[28:31], v[44:47]\n"
    "v_exp_f32 v79, v79\n"
    "v_mfma_f32_16x16x32_bf16 v[40:43], v[180:183], v[4:7], v[40:43]\n"
    "ds_read_b128 v[164:167], v200 offset:22528\n"
    "v_cvt_pk_bf16_f32 v152, v72, v73\n"
    "v_mfma_f32_16x16x32_bf16 v[40:43], v[176:179], v[0:3], v[40:43]\n"
    "v_exp_f32 v80, v80\n"
    "v_mfma_f32_16x16x32_bf16 v[40:43], v[172:175], a[252:255], v[40:43]\n"
    "buffer_load_dwordx4 v200, s[36:39], s65 offen offset:2048 sc1 lds\n"
    "v_mfma_f32_16x16x32_bf16 v[36:39], v[172:175], a[228:231], v[36:39]\n"
    "ds_read_b128 v[168:171], v200 offset:23552\n"
    "v_cvt_pk_bf16_f32 v153, v74, v75\n"
    "v_mfma_f32_16x16x32_bf16 v[36:39], v[176:179], a[232:235], v[36:39]\n"
    "s_add_u32 m0, s71, 0xc000\n"
    "v_exp_f32 v81, v81\n"
    "v_mfma_f32_16x16x32_bf16 v[36:39], v[180:183], a[236:239], v[36:39]\n"
    "v_exp_f32 v82, v82\n"
    "v_mfma_f32_16x16x32_bf16 v[32:35], v[180:183], a[212:215], v[32:35]\n"
    "buffer_load_dwordx4 v200, s[40:43], s61 offen sc1 lds\n"
    "v_mfma_f32_16x16x32_bf16 v[32:35], v[176:179], a[208:211], v[32:35]\n"
    "v_exp_f32 v83, v83\n"
    "v_mfma_f32_16x16x32_bf16 v[32:35], v[172:175], a[204:207], v[32:35]\n"
    "s_waitcnt lgkmcnt(1)\n"
    "v_exp_f32 v84, v84\n"
    "v_mfma_f32_16x16x32_bf16 v[48:51], v[160:163], a[204:207], v[96:99]\n"
    "ds_read_b128 v[172:175], v200 offset:18432\n"
    "v_cvt_pk_bf16_f32 v156, v76, v77\n"
    "v_mfma_f32_16x16x32_bf16 v[48:51], v[164:167], a[208:211], v[48:51]\n"
    "s_waitcnt lgkmcnt(1)\n"
    "buffer_load_dwordx4 v200, s[40:43], s61 offen offset:1024 sc1 lds\n"
    "v_mfma_f32_16x16x32_bf16 v[48:51], v[168:171], a[212:215], v[48:51]\n"
    "v_exp_f32 v85, v85\n"
    "v_mfma_f32_16x16x32_bf16 v[52:55], v[168:171], a[236:239], v[100:103]\n"
    "ds_read_b128 v[176:179], v200 offset:19456\n"
    "v_cvt_pk_bf16_f32 v157, v78, v79\n"
    "v_mfma_f32_16x16x32_bf16 v[52:55], v[164:167], a[232:235], v[52:55]\n"
    "v_exp_f32 v86, v86\n"
    "v_mfma_f32_16x16x32_bf16 v[52:55], v[160:163], a[228:231], v[52:55]\n"
    "buffer_load_dwordx4 v200, s[40:43], s61 offen offset:2048 sc1 lds\n"
    "v_mfma_f32_16x16x32_bf16 v[56:59], v[160:163], a[252:255], v[104:107]\n"
    "ds_read_b128 v[180:183], v200 offset:20480\n"
    "v_cvt_pk_bf16_f32 v146, v80, v81\n"
    "v_mfma_f32_16x16x32_bf16 v[56:59], v[164:167], v[0:3], v[56:59]\n"
    "v_exp_f32 v87, v87\n"
    "v_mfma_f32_16x16x32_bf16 v[56:59], v[168:171], v[4:7], v[56:59]\n"
    "v_exp_f32 v88, v88\n"
    "v_mfma_f32_16x16x32_bf16 v[60:63], v[168:171], v[28:31], v[108:111]\n"
    "buffer_load_dwordx4 v200, s[40:43], s61 offen offset:3072 sc1 lds\n"
    "v_mfma_f32_16x16x32_bf16 v[60:63], v[164:167], v[24:27], v[60:63]\n"
    "s_add_u32 s65, s61, 0x1000\n"
    "v_exp_f32 v89, v89\n"
    "v_mfma_f32_16x16x32_bf16 v[60:63], v[160:163], v[20:23], v[60:63]\n"
    "s_waitcnt lgkmcnt(1)\n"
    "s_add_u32 m0, s71, 0xd000\n"
    "v_cvt_pk_bf16_f32 v147, v82, v83\n"
    "v_mfma_f32_16x16x32_bf16 v[60:63], v[172:175], v[8:11], v[60:63]\n"
    "ds_read_b128 v[160:163], v201 offset:24576\n"
    "v_cvt_pk_bf16_f32 v150, v84, v85\n"
    "v_mfma_f32_16x16x32_bf16 v[60:63], v[176:179], v[12:15], v[60:63]\n"
    "s_waitcnt lgkmcnt(1)\n"
    "buffer_load_dwordx4 v200, s[40:43], s65 offen sc1 lds\n"
    "v_mfma_f32_16x16x32_bf16 v[60:63], v[180:183], v[16:19], v[60:63]\n"
    "v_exp_f32 v90, v90\n"
    "v_mfma_f32_16x16x32_bf16 v[56:59], v[180:183], a[248:251], v[56:59]\n"
    "ds_read_b128 v[164:167], v201 offset:25600\n"
    "v_cvt_pk_bf16_f32 v151, v86, v87\n"
    "v_mfma_f32_16x16x32_bf16 v[56:59], v[176:179], a[244:247], v[56:59]\n"
    "v_exp_f32 v91, v91\n"
    "v_mfma_f32_16x16x32_bf16 v[56:59], v[172:175], a[240:243], v[56:59]\n"
    "buffer_load_dwordx4 v200, s[40:43], s65 offen offset:1024 sc1 lds\n"
    "v_mfma_f32_16x16x32_bf16 v[52:55], v[172:175], a[216:219], v[52:55]\n"
    "ds_read_b128 v[168:171], v201 offset:26624\n"
    "v_cvt_pk_bf16_f32 v154, v88, v89\n"
    "v_mfma_f32_16x16x32_bf16 v[52:55], v[176:179], a[220:223], v[52:55]\n"
    "v_exp_f32 v92, v92\n"
    "v_mfma_f32_16x16x32_bf16 v[52:55], v[180:183], a[224:227], v[52:55]\n"
    "v_exp_f32 v93, v93\n"
    "v_mfma_f32_16x16x32_bf16 v[48:51], v[180:183], a[200:203], v[48:51]\n"
    "v_exp_f32 v94, v94\n"
    "v_mfma_f32_16x16x32_bf16 v[48:51], v[176:179], a[196:199], v[48:51]\n"
    "v_cvt_pk_bf16_f32 v155, v90, v91\n"
    "v_cvt_pk_bf16_f32 v158, v92, v93\n"
    "v_mfma_f32_16x16x32_bf16 v[48:51], v[172:175], a[192:195], v[48:51]\n"
    "s_waitcnt lgkmcnt(1)\n"
    "v_exp_f32 v95, v95\n"
    "v_mfma_f32_16x16x32_bf16 a[0:3], v[160:163], v[128:131], a[0:3]\n"
    "ds_read_b128 v[172:175], v201 offset:27648\n"
    "ds_read_b128 v[176:179], v201 offset:28672\n"
    "v_mfma_f32_16x16x32_bf16 a[0:3], v[164:167], v[144:147], a[0:3]\n"
    "v_cvt_pk_bf16_f32 v159, v94, v95\n"
    "v_add_f32 v113, v113, v64\n"
    "v_mfma_f32_16x16x32_bf16 a[4:7], v[160:163], v[132:135], a[4:7]\n"
    "v_exp_f32 v32, v32\n"
    "v_mfma_f32_16x16x32_bf16 a[4:7], v[164:167], v[148:151], a[4:7]\n"
    "v_exp_f32 v33, v33\n"
    "v_mfma_f32_16x16x32_bf16 a[8:11], v[160:163], v[136:139], a[8:11]\n"
    "ds_read_b128 v[180:183], v201 offset:29696\n"
    "v_add_f32 v114, v114, v65\n"
    "v_mfma_f32_16x16x32_bf16 a[8:11], v[164:167], v[152:155], a[8:11]\n"
    "v_exp_f32 v34, v34\n"
    "v_mfma_f32_16x16x32_bf16 a[12:15], v[160:163], v[140:143], a[12:15]\n"
    "v_exp_f32 v35, v35\n"
    "v_mfma_f32_16x16x32_bf16 a[12:15], v[164:167], v[156:159], a[12:15]\n"
    "s_waitcnt lgkmcnt(2)\n"
    "v_exp_f32 v36, v36\n"
    "v_mfma_f32_16x16x32_bf16 a[28:31], v[168:171], v[140:143], a[28:31]\n"
    "ds_read_b128 v[160:163], v201 offset:30720\n"
    "v_add_f32 v113, v113, v66\n"
    "v_mfma_f32_16x16x32_bf16 a[28:31], v[172:175], v[156:159], a[28:31]\n"
    "v_exp_f32 v37, v37\n"
    "v_mfma_f32_16x16x32_bf16 a[24:27], v[172:175], v[152:155], a[24:27]\n"
    "v_exp_f32 v38, v38\n"
    "v_mfma_f32_16x16x32_bf16 a[24:27], v[168:171], v[136:139], a[24:27]\n"
    "v_exp_f32 v39, v39\n"
    "v_mfma_f32_16x16x32_bf16 a[20:23], v[168:171], v[132:135], a[20:23]\n"
    "ds_read_b128 v[164:167], v201 offset:31744\n"
    "v_add_f32 v114, v114, v67\n"
    "v_mfma_f32_16x16x32_bf16 a[20:23], v[172:175], v[148:151], a[20:23]\n"
    "v_exp_f32 v40, v40\n"
    "v_mfma_f32_16x16x32_bf16 a[16:19], v[172:175], v[144:147], a[16:19]\n"
    "v_exp_f32 v41, v41\n"
    "v_mfma_f32_16x16x32_bf16 a[16:19], v[168:171], v[128:131], a[16:19]\n"
    "s_waitcnt lgkmcnt(2)\n"
    "v_exp_f32 v42, v42\n"
    "v_mfma_f32_16x16x32_bf16 a[32:35], v[176:179], v[128:131], a[32:35]\n"
    "ds_read_b128 v[168:171], v201 offset:32768\n"
    "v_add_f32 v117, v117, v68\n"
    "v_mfma_f32_16x16x32_bf16 a[32:35], v[180:183], v[144:147], a[32:35]\n"
    "v_exp_f32 v43, v43\n"
    "v_mfma_f32_16x16x32_bf16 a[36:39], v[180:183], v[148:151], a[36:39]\n"
    "v_exp_f32 v44, v44\n"
    "v_mfma_f32_16x16x32_bf16 a[36:39], v[176:179], v[132:135], a[36:39]\n"
    "v_exp_f32 v45, v45\n"
    "v_mfma_f32_16x16x32_bf16 a[40:43], v[176:179], v[136:139], a[40:43]\n"
    "ds_read_b128 v[172:175], v201 offset:33792\n"
    "v_add_f32 v118, v118, v69\n"
    "v_mfma_f32_16x16x32_bf16 a[40:43], v[180:183], v[152:155], a[40:43]\n"
    "v_exp_f32 v46, v46\n"
    "v_mfma_f32_16x16x32_bf16 a[44:47], v[180:183], v[156:159], a[44:47]\n"
    "v_exp_f32 v47, v47\n"
    "v_mfma_f32_16x16x32_bf16 a[44:47], v[176:179], v[140:143], a[44:47]\n"
    "s_waitcnt lgkmcnt(2)\n"
    "v_exp_f32 v48, v48\n"
    "v_mfma_f32_16x16x32_bf16 a[60:63], v[160:163], v[140:143], a[60:63]\n"
    "ds_read_b128 v[176:179], v201 offset:34816\n"
    "v_add_f32 v117, v117, v70\n"
    "v_mfma_f32_16x16x32_bf16 a[60:63], v[164:167], v[156:159], a[60:63]\n"
    "v_exp_f32 v49, v49\n"
    "v_mfma_f32_16x16x32_bf16 a[56:59], v[164:167], v[152:155], a[56:59]\n"
    "v_exp_f32 v50, v50\n"
    "v_mfma_f32_16x16x32_bf16 a[56:59], v[160:163], v[136:139], a[56:59]\n"
    "v_exp_f32 v51, v51\n"
    "v_mfma_f32_16x16x32_bf16 a[52:55], v[160:163], v[132:135], a[52:55]\n"
    "ds_read_b128 v[180:183], v201 offset:35840\n"
    "v_add_f32 v113, v113, v32\n"
    "v_mfma_f32_16x16x32_bf16 a[52:55], v[164:167], v[148:151], a[52:55]\n"
    "v_exp_f32 v52, v52\n"
    "v_mfma_f32_16x16x32_bf16 a[48:51], v[164:167], v[144:147], a[48:51]\n"
    "v_add_f32 v118, v118, v71\n"
    "v_add_f32 v114, v114, v33\n"
    "v_mfma_f32_16x16x32_bf16 a[48:51], v[160:163], v[128:131], a[48:51]\n"
    "s_waitcnt lgkmcnt(2)\n"
    "v_add_f32 v121, v121, v72\n"
    "v_add_f32 v113, v113, v34\n"
    "v_mfma_f32_16x16x32_bf16 a[64:67], v[168:171], v[128:131], a[64:67]\n"
    "ds_read_b128 v[160:163], v201 offset:36864\n"
    "v_add_f32 v122, v122, v73\n"
    "v_mfma_f32_16x16x32_bf16 a[64:67], v[172:175], v[144:147], a[64:67]\n"
    "v_exp_f32 v53, v53\n"
    "v_mfma_f32_16x16x32_bf16 a[68:71], v[172:175], v[148:151], a[68:71]\n"
    "v_add_f32 v114, v114, v35\n"
    "v_add_f32 v121, v121, v74\n"
    "v_mfma_f32_16x16x32_bf16 a[68:71], v[168:171], v[132:135], a[68:71]\n"
    "s_add_u32 m0, s51, s95\n"
    "v_add_f32 v117, v117, v36\n"
    "v_add_f32 v122, v122, v75\n"
    "v_mfma_f32_16x16x32_bf16 a[72:75], v[168:171], v[136:139], a[72:75]\n"
    "ds_read_b128 v[164:167], v201 offset:37888\n"
    "v_add_f32 v118, v118, v37\n"
    "v_mfma_f32_16x16x32_bf16 a[72:75], v[172:175], v[152:155], a[72:75]\n"
    "buffer_load_dwordx4 v202, s[32:35], s64 offen lds\n"
    "v_mfma_f32_16x16x32_bf16 a[76:79], v[172:175], v[156:159], a[76:79]\n"
    "v_exp_f32 v54, v54\n"
    "v_mfma_f32_16x16x32_bf16 a[76:79], v[168:171], v[140:143], a[76:79]\n"
    "s_waitcnt lgkmcnt(2)\n"
    "v_add_f32 v125, v125, v76\n"
    "v_add_f32 v117, v117, v38\n"
    "v_mfma_f32_16x16x32_bf16 a[92:95], v[176:179], v[140:143], a[92:95]\n"
    "ds_read_b128 v[168:171], v201 offset:38912\n"
    "v_add_f32 v126, v126, v77\n"
    "v_mfma_f32_16x16x32_bf16 a[92:95], v[180:183], v[156:159], a[92:95]\n"
    "buffer_load_dwordx4 v203, s[32:35], s64 offen offset:1024 lds\n"
    "v_mfma_f32_16x16x32_bf16 a[88:91], v[180:183], v[152:155], a[88:91]\n"
    "v_exp_f32 v55, v55\n"
    "v_mfma_f32_16x16x32_bf16 a[88:91], v[176:179], v[136:139], a[88:91]\n"
    "v_add_f32 v118, v118, v39\n"
    "v_add_f32 v125, v125, v78\n"
    "v_mfma_f32_16x16x32_bf16 a[84:87], v[176:179], v[132:135], a[84:87]\n"
    "ds_read_b128 v[172:175], v201 offset:39936\n"
    "v_add_f32 v121, v121, v40\n"
    "v_mfma_f32_16x16x32_bf16 a[84:87], v[180:183], v[148:151], a[84:87]\n"
    "buffer_load_dwordx4 v204, s[32:35], s64 offen offset:2048 lds\n"
    "v_mfma_f32_16x16x32_bf16 a[80:83], v[180:183], v[144:147], a[80:83]\n"
    "v_exp_f32 v56, v56\n"
    "v_mfma_f32_16x16x32_bf16 a[80:83], v[176:179], v[128:131], a[80:83]\n"
    "s_waitcnt lgkmcnt(2)\n"
    "v_add_f32 v126, v126, v79\n"
    "v_add_f32 v122, v122, v41\n"
    "v_mfma_f32_16x16x32_bf16 a[96:99], v[160:163], v[128:131], a[96:99]\n"
    "s_mov_b32 s95, s94\n"
    "ds_read_b128 v[176:179], v201 offset:40960\n"
    "v_add_f32 v113, v113, v80\n"
    "v_mfma_f32_16x16x32_bf16 a[96:99], v[164:167], v[144:147], a[96:99]\n"
    "s_sub_u32 s94, s94, 0x3000\n"
    "v_add_f32 v121, v121, v42\n"
    "v_add_f32 v114, v114, v81\n"
    "v_mfma_f32_16x16x32_bf16 a[100:103], v[164:167], v[148:151], a[100:103]\n"
    "s_min_u32 s94, s94, 0x6000\n"
    "v_exp_f32 v57, v57\n"
    "v_mfma_f32_16x16x32_bf16 a[100:103], v[160:163], v[132:135], a[100:103]\n"
    "s_sub_u32 s93, s94, s95\n"
    "v_add_f32 v122, v122, v43\n"
    "v_add_f32 v113, v113, v82\n"
    "v_mfma_f32_16x16x32_bf16 a[104:107], v[160:163], v[136:139], a[104:107]\n"
    "ds_read_b128 v[180:183], v201 offset:41984\n"
    "v_add_f32 v125, v125, v44\n"
    "v_mfma_f32_16x16x32_bf16 a[104:107], v[164:167], v[152:155], a[104:107]\n"
    "v_exp_f32 v58, v58\n"
    "v_mfma_f32_16x16x32_bf16 a[108:111], v[164:167], v[156:159], a[108:111]\n"
    "v_add_f32 v114, v114, v83\n"
    "v_add_f32 v126, v126, v45\n"
    "v_mfma_f32_16x16x32_bf16 a[108:111], v[160:163], v[140:143], a[108:111]\n"
    "s_waitcnt lgkmcnt(2)\n"
    "v_add_f32 v117, v117, v84\n"
    "v_add_f32 v125, v125, v46\n"
    "v_mfma_f32_16x16x32_bf16 a[124:127], v[168:171], v[140:143], a[124:127]\n"
    "ds_read_b128 v[160:163], v201 offset:43008\n"
    "v_add_f32 v118, v118, v85\n"
    "v_mfma_f32_16x16x32_bf16 a[124:127], v[172:175], v[156:159], a[124:127]\n"
    "v_exp_f32 v59, v59\n"
    "v_mfma_f32_16x16x32_bf16 a[120:123], v[172:175], v[152:155], a[120:123]\n"
    "v_add_f32 v126, v126, v47\n"
    "v_add_f32 v117, v117, v86\n"
    "v_mfma_f32_16x16x32_bf16 a[120:123], v[168:171], v[136:139], a[120:123]\n"
    "v_add_f32 v113, v113, v48\n"
    "v_add_u32 v205, s93, v205\n"
    "v_mfma_f32_16x16x32_bf16 a[116:119], v[168:171], v[132:135], a[116:119]\n"
    "ds_read_b128 v[164:167], v201 offset:44032\n"
    "v_add_f32 v118, v118, v87\n"
    "v_mfma_f32_16x16x32_bf16 a[116:119], v[172:175], v[148:151], a[116:119]\n"
    "v_exp_f32 v60, v60\n"
    "v_mfma_f32_16x16x32_bf16 a[112:115], v[172:175], v[144:147], a[112:115]\n"
    "v_add_f32 v114, v114, v49\n"
    "v_add_u32 v206, s93, v206\n"
    "v_mfma_f32_16x16x32_bf16 a[112:115], v[168:171], v[128:131], a[112:115]\n"
    "s_waitcnt lgkmcnt(2)\n"
    "v_add_f32 v121, v121, v88\n"
    "v_add_f32 v113, v113, v50\n"
    "v_mfma_f32_16x16x32_bf16 a[128:131], v[176:179], v[128:131], a[128:131]\n"
    "ds_read_b128 v[168:171], v201 offset:45056\n"
    "v_add_u32 v207, s93, v207\n"
    "v_mfma_f32_16x16x32_bf16 a[128:131], v[180:183], v[144:147], a[128:131]\n"
    "v_exp_f32 v61, v61\n"
    "v_mfma_f32_16x16x32_bf16 a[132:135], v[180:183], v[148:151], a[132:135]\n"
    "s_add_u32 s64, s64, 0xc000\n"
    "v_add_f32 v122, v122, v89\n"
    "v_add_f32 v114, v114, v51\n"
    "v_mfma_f32_16x16x32_bf16 a[132:135], v[176:179], v[132:135], a[132:135]\n"
    "s_add_u32 s62, s62, 0x6000\n"
    "v_add_f32 v121, v121, v90\n"
    "v_add_f32 v117, v117, v52\n"
    "v_mfma_f32_16x16x32_bf16 a[136:139], v[176:179], v[136:139], a[136:139]\n"
    "s_add_u32 s63, s63, 0x6000\n"
    "ds_read_b128 v[172:175], v201 offset:46080\n"
    "v_add_f32 v122, v122, v91\n"
    "v_mfma_f32_16x16x32_bf16 a[136:139], v[180:183], v[152:155], a[136:139]\n"
    "s_add_u32 s60, s60, 0x6000\n"
    "v_exp_f32 v62, v62\n"
    "v_mfma_f32_16x16x32_bf16 a[140:143], v[180:183], v[156:159], a[140:143]\n"
    "s_add_u32 s61, s61, 0x6000\n"
    "v_add_f32 v118, v118, v53\n"
    "v_add_f32 v125, v125, v92\n"
    "v_mfma_f32_16x16x32_bf16 a[140:143], v[176:179], v[140:143], a[140:143]\n"
    "s_waitcnt lgkmcnt(2)\n"
    "v_add_f32 v117, v117, v54\n"
    "v_add_f32 v126, v126, v93\n"
    "v_mfma_f32_16x16x32_bf16 a[156:159], v[160:163], v[140:143], a[156:159]\n"
    "ds_read_b128 v[176:179], v201 offset:47104\n"
    "v_add_f32 v118, v118, v55\n"
    "v_mfma_f32_16x16x32_bf16 a[156:159], v[164:167], v[156:159], a[156:159]\n"
    "s_waitcnt vmcnt(3)\n"
    "ds_read_b128 v[180:183], v201 offset:48128\n"
    "v_add_f32 v125, v125, v94\n"
    "v_mfma_f32_16x16x32_bf16 a[152:155], v[164:167], v[152:155], a[152:155]\n"
    "v_exp_f32 v63, v63\n"
    "v_mfma_f32_16x16x32_bf16 a[152:155], v[160:163], v[136:139], a[152:155]\n"
    "v_add_f32 v121, v121, v56\n"
    "v_add_f32 v126, v126, v95\n"
    "v_mfma_f32_16x16x32_bf16 a[148:151], v[160:163], v[132:135], a[148:151]\n"
    "v_add_f32 v122, v122, v57\n"
    "v_mfma_f32_16x16x32_bf16 a[148:151], v[164:167], v[148:151], a[148:151]\n"
    "v_mfma_f32_16x16x32_bf16 a[144:147], v[164:167], v[144:147], a[144:147]\n"
    "v_mfma_f32_16x16x32_bf16 a[144:147], v[160:163], v[128:131], a[144:147]\n"
    "s_waitcnt lgkmcnt(0)\n"
    "v_mfma_f32_16x16x32_bf16 a[160:163], v[168:171], v[128:131], a[160:163]\n"
    "s_barrier\n"
    "LMID1:\n"
    "v_mfma_f32_16x16x32_bf16 a[160:163], v[172:175], v[144:147], a[160:163]\n"
    "ds_read_u16_d16_hi v223, v209 offset:6336\n"
    "ds_read_u16_d16_hi v222, v209 offset:192\n"
    "v_mfma_f32_16x16x32_bf16 a[164:167], v[172:175], v[148:151], a[164:167]\n"
    "ds_read_b128 v[160:163], v200 offset:24576\n"
    "v_add_f32 v121, v121, v58\n"
    "v_mfma_f32_16x16x32_bf16 a[164:167], v[168:171], v[132:135], a[164:167]\n"
    "ds_read_b128 v[232:235], v205 offset:24576\n"
    "v_add_f32 v122, v122, v59\n"
    "v_mfma_f32_16x16x32_bf16 a[168:171], v[168:171], v[136:139], a[168:171]\n"
    "ds_read_b128 v[164:167], v200 offset:25600\n"
    "ds_read_b128 v[236:239], v206 offset:24576\n"
    "v_mfma_f32_16x16x32_bf16 a[168:171], v[172:175], v[152:155], a[168:171]\n"
    "v_add_f32 v125, v125, v60\n"
    "v_add_f32 v126, v126, v61\n"
    "v_mfma_f32_16x16x32_bf16 a[172:175], v[172:175], v[156:159], a[172:175]\n"
    "v_add_f32 v125, v125, v62\n"
    "v_add_f32 v126, v126, v63\n"
    "v_mfma_f32_16x16x32_bf16 a[172:175], v[168:171], v[140:143], a[172:175]\n"
    "ds_read_b64 v[228:229], v210 offset:64\n"
    "ds_read2st64_b32 v[240:241], v207 offset0:96 offset1:99\n"
    "v_mfma_f32_16x16x32_bf16 a[188:191], v[176:179], v[140:143], a[188:191]\n"
    "ds_read_b128 v[168:171], v200 offset:26624\n"
    "ds_read2st64_b32 v[242:243], v207 offset0:102 offset1:105\n"
    "v_mfma_f32_16x16x32_bf16 a[188:191], v[180:183], v[156:159], a[188:191]\n"
    "ds_read2st64_b32 v[244:245], v207 offset0:108 offset1:111\n"
    "ds_read2st64_b32 v[246:247], v207 offset0:114 offset1:117\n"
    "v_mfma_f32_16x16x32_bf16 a[184:187], v[176:179], v[136:139], a[184:187]\n"
    "v_cvt_pk_bf16_f32 v140, v44, v45\n"
    "v_cvt_pk_bf16_f32 v141, v46, v47\n"
    "v_mfma_f32_16x16x32_bf16 a[184:187], v[180:183], v[152:155], a[184:187]\n"
    "s_waitcnt lgkmcnt(6)\n"
    "v_mul_f32 v226, s75, v223\n"
    "v_mfma_f32_16x16x32_bf16 a[180:183], v[176:179], v[132:135], a[180:183]\n"
    "s_add_u32 s59, s59, 1\n"
    "ds_read_b128 v[172:175], v200 offset:27648\n"
    "v_fma_f32 v227, -v222, v226, v217\n"
    "v_mfma_f32_16x16x32_bf16 a[180:183], v[180:183], v[148:151], a[180:183]\n"
    "v_cvt_f32_i32 v232, v232\n"
    "v_cvt_f32_i32 v233, v233\n"
    "v_mfma_f32_16x16x32_bf16 a[176:179], v[176:179], v[128:131], a[176:179]\n"
    "s_cmp_lt_u32 s59, 15\n"
    "v_cvt_f32_i32 v234, v234\n"
    "v_cvt_f32_i32 v235, v235\n"
    "v_mfma_f32_16x16x32_bf16 a[176:179], v[180:183], v[144:147], a[176:179]\n"
    "s_cbranch_scc1 LLOOP1\n"
    "s_branch LTAIL\n"
    "LENT0:\n"
    "s_mov_b32 s59, 1\n"
    "s_add_u32 m0, s72, 0x21000\n"
    "s_nop 0\n"
    "buffer_load_dwordx4 v250, s[48:51], 0 offen offset:2048 lds\n"
    "s_add_u32 m0, s72, 0x18000\n"
    "s_nop 0\n"
    "buffer_load_dwordx4 v202, s[32:35], s64 offen lds\n"
    "buffer_load_dwordx4 v203, s[32:35], s64 offen offset:1024 lds\n"
    "buffer_load_dwordx4 v204, s[32:35], s64 offen offset:2048 lds\n"
    "s_add_u32 s64, s64, 0xc000\n"
    "s_mov_b32 s94, 0x3000\n"
    "s_mov_b32 s95, 0x6000\n"
    "s_add_u32 s51, s72, 0x18000\n"
    "v_add_u32 v205, 0x3000, v205\n"
    "v_add_u32 v206, 0x3000, v206\n"
    "v_add_u32 v207, 0x3000, v207\n"
    "buffer_store_dwordx2 v[220:221], v214, s[44:47], 0 offen sc1\n"
    "ds_read_b128 v[160:163], v200 offset:24576\n"
    "ds_read_u16_d16_hi v223, v209 offset:6336\n"
    "ds_read_u16_d16_hi v222, v209 offset:192\n"
    "v_add_f32 v121, v121, v58\n"
    "ds_read_b128 v[164:167], v200 offset:25600\n"
    "ds_read_b128 v[232:235], v205 offset:24576\n"
    "v_add_f32 v122, v122, v59\n"
    "ds_read_b128 v[236:239], v206 offset:24576\n"
    "v_add_f32 v125, v125, v60\n"
    "v_add_f32 v126, v126, v61\n"
    "v_add_f32 v125, v125, v62\n"
    "v_add_f32 v126, v126, v63\n"
    "ds_read_b128 v[168:171], v200 offset:26624\n"
    "ds_read_b64 v[228:229], v210 offset:64\n"
    "ds_read2st64_b32 v[240:241], v207 offset0:96 offset1:99\n"
    "ds_read2st64_b32 v[242:243], v207 offset0:102 offset1:105\n"
    "ds_read2st64_b32 v[244:245], v207 offset0:108 offset1:111\n"
    "ds_read2st64_b32 v[246:247], v207 offset0:114 offset1:117\n"
    "v_cvt_pk_bf16_f32 v140, v44, v45\n"
    "v_cvt_pk_bf16_f32 v141, v46, v47\n"
    "ds_read_b128 v[172:175], v200 offset:27648\n"
    "s_waitcnt lgkmcnt(7)\n"
    "v_mul_f32 v226, s75, v223\n"
    "v_fma_f32 v227, -v222, v226, v217\n"
    "v_cvt_f32_i32 v232, v232\n"
    "v_cvt_f32_i32 v233, v233\n"
    "s_nop 7\n"
    ".p2align 6\n"
    "LLOOP:\n"
    "v_mfma_f32_16x16x32_bf16 v[64:67], v[160:163], a[192:195], v[96:99]\n"
    "ds_read_b128 v[176:179], v200 offset:28672\n"
    "v_cvt_f32_i32 v234, v234\n"
    "v_mfma_f32_16x16x32_bf16 v[64:67], v[164:167], a[196:199], v[64:67]\n"
    "s_waitcnt lgkmcnt(1)\n"
    "v_cvt_f32_i32 v235, v235\n"
    "v_cvt_f32_i32 v236, v236\n"
    "v_mfma_f32_16x16x32_bf16 v[64:67], v[168:171], a[200:203], v[64:67]\n"
    "v_cvt_f32_i32 v237, v237\n"
    "v_cvt_f32_i32 v238, v238\n"
    "v_mfma_f32_16x16x32_bf16 v[68:71], v[168:171], a[224:227], v[100:103]\n"
    "v_cvt_f32_i32 v239, v239\n"
    "v_mul_f32_dpp v232, v226, v232 row_newbcast:0 row_mask:0xf bank_mask:0xf\n"
    "v_mfma_f32_16x16x32_bf16 v[68:71], v[164:167], a[220:223], v[68:71]\n"
    "ds_read_b128 v[180:183], v200 offset:29696\n"
    "v_mul_f32_dpp v233, v226, v233 row_newbcast:1 row_mask:0xf bank_mask:0xf\n"
    "v_mfma_f32_16x16x32_bf16 v[68:71], v[160:163], a[216:219], v[68:71]\n"
    "v_mul_f32_dpp v234, v226, v234 row_newbcast:2 row_mask:0xf bank_mask:0xf\n"
    "v_mul_f32_dpp v235, v226, v235 row_newbcast:3 row_mask:0xf bank_mask:0xf\n"
    "v_mfma_f32_16x16x32_bf16 v[72:75], v[160:163], a[240:243], v[104:107]\n"
    "v_mul_f32_dpp v236, v226, v236 row_newbcast:4 row_mask:0xf bank_mask:0xf\n"
    "v_mul_f32_dpp v237, v226, v237 row_newbcast:5 row_mask:0xf bank_mask:0xf\n"
    "v_mfma_f32_16x16x32_bf16 v[72:75], v[164:167], a[244:247], v[72:75]\n"
    "v_mul_f32_dpp v238, v226, v238 row_newbcast:6 row_mask:0xf bank_mask:0xf\n"
    "v_mul_f32_dpp v239, v226, v239 row_newbcast:7 row_mask:0xf bank_mask:0xf\n"
    "v_mfma_f32_16x16x32_bf16 v[72:75], v[168:171], a[248:251], v[72:75]\n"
    "v_add_f32_dpp v232, v227, v232 row_newbcast:0 row_mask:0xf bank_mask:0xf\n"
    "v_add_f32_dpp v233, v227, v233 row_newbcast:1 row_mask:0xf bank_mask:0xf\n"
    "v_mfma_f32_16x16x32_bf16 v[76:79], v[168:171], v[16:19], v[108:111]\n"
    "v_add_f32_dpp v234, v227, v234 row_newbcast:2 row_mask:0xf bank_mask:0xf\n"
    "v_add_f32_dpp v235, v227, v235 row_newbcast:3 row_mask:0xf bank_mask:0xf\n"
    "v_mfma_f32_16x16x32_bf16 v[76:79], v[164:167], v[12:15], v[76:79]\n"
    "v_add_f32_dpp v236, v227, v236 row_newbcast:4 row_mask:0xf bank_mask:0xf\n"
    "v_add_f32_dpp v237, v227, v237 row_newbcast:5 row_mask:0xf bank_mask:0xf\n"
    "v_mfma_f32_16x16x32_bf16 v[76:79], v[160:163], v[8:11], v[76:79]\n"
    "s_waitcnt lgkmcnt(1)\n"
    "v_add_f32_dpp v238, v227, v238 row_newbcast:6 row_mask:0xf bank_mask:0xf\n"
    "v_add_f32_dpp v239, v227, v239 row_newbcast:7 row_mask:0xf bank_mask:0xf\n"
    "v_mfma_f32_16x16x32_bf16 v[76:79], v[172:175], v[20:23], v[76:79]\n"
    "ds_read_b128 v[160:163], v200 offset:33792\n"
    "v_cvt_pk_bf16_f32 v232, v232, v233\n"
    "v_mfma_f32_16x16x32_bf16 v[76:79], v[176:179], v[24:27], v[76:79]\n"
    "s_waitcnt lgkmcnt(1)\n"
    "v_cvt_f32_i32 v240, v240\n"
    "v_cvt_pk_bf16_f32 v233, v234, v235\n"
    "v_mfma_f32_16x16x32_bf16 v[76:79], v[180:183], v[28:31], v[76:79]\n"
    "v_cvt_f32_i32 v241, v241\n"
    "v_cvt_pk_bf16_f32 v234, v236, v237\n"
    "v_mfma_f32_16x16x32_bf16 v[72:75], v[180:183], v[4:7], v[72:75]\n"
    "ds_read_b128 v[164:167], v200 offset:34816\n"
    "v_cvt_f32_i32 v242, v242\n"
    "v_mfma_f32_16x16x32_bf16 v[72:75], v[176:179], v[0:3], v[72:75]\n"
    "v_cvt_pk_bf16_f32 v235, v238, v239\n"
    "v_cvt_f32_i32 v243, v243\n"
    "v_mfma_f32_16x16x32_bf16 v[72:75], v[172:175], a[252:255], v[72:75]\n"
    "v_cvt_f32_i32 v244, v244\n"
    "v_cvt_f32_i32 v245, v245\n"
    "v_mfma_f32_16x16x32_bf16 v[68:71], v[172:175], a[228:231], v[68:71]\n"
    "ds_read_b128 v[168:171], v200 offset:35840\n"
    "v_cvt_f32_i32 v246, v246\n"
    "v_mfma_f32_16x16x32_bf16 v[68:71], v[176:179], a[232:235], v[68:71]\n"
    "v_cvt_f32_i32 v247, v247\n"
    "v_mul_f32_dpp v240, v228, v240 row_newbcast:0 row_mask:0xf bank_mask:0xf\n"
    "v_mfma_f32_16x16x32_bf16 v[68:71], v[180:183], a[236:239], v[68:71]\n"
    "v_mul_f32_dpp v241, v228, v241 row_newbcast:1 row_mask:0xf bank_mask:0xf\n"
    "v_mul_f32_dpp v242, v228, v242 row_newbcast:2 row_mask:0xf bank_mask:0xf\n"
    "v_mfma_f32_16x16x32_bf16 v[64:67], v[180:183], a[212:215], v[64:67]\n"
    "v_mul_f32_dpp v243, v228, v243 row_newbcast:3 row_mask:0xf bank_mask:0xf\n"
    "v_mul_f32_dpp v244, v228, v244 row_newbcast:4 row_mask:0xf bank_mask:0xf\n"
    "v_mfma_f32_16x16x32_bf16 v[64:67], v[176:179], a[208:211], v[64:67]\n"
    "buffer_store_dwordx4 v[232:235], v211, s[36:39], s62 offen sc1\n"
    "v_mul_f32_dpp v245, v228, v245 row_newbcast:5 row_mask:0xf bank_mask:0xf\n"
    "v_mul_f32_dpp v246, v228, v246 row_newbcast:6 row_mask:0xf bank_mask:0xf\n"
    "v_mul_f32_dpp v247, v228, v247 row_newbcast:7 row_mask:0xf bank_mask:0xf\n"
    "v_mfma_f32_16x16x32_bf16 v[64:67], v[172:175], a[204:207], v[64:67]\n"
    "s_waitcnt lgkmcnt(1)\n"
    "v_add_f32_dpp v240, v229, v240 row_newbcast:0 row_mask:0xf bank_mask:0xf\n"
    "v_add_f32_dpp v241, v229, v241 row_newbcast:1 row_mask:0xf bank_mask:0xf\n"
    "v_mfma_f32_16x16x32_bf16 v[80:83], v[160:163], a[204:207], v[96:99]\n"
    "ds_read_b128 v[172:175], v200 offset:30720\n"
    "v_add_f32_dpp v242, v229, v242 row_newbcast:2 row_mask:0xf bank_mask:0xf\n"
    "v_mfma_f32_16x16x32_bf16 v[80:83], v[164:167], a[208:211], v[80:83]\n"
    "s_waitcnt lgkmcnt(1)\n"
    "v_add_f32_dpp v243, v229, v243 row_newbcast:3 row_mask:0xf bank_mask:0xf\n"
    "v_add_f32_dpp v244, v229, v244 row_newbcast:4 row_mask:0xf bank_mask:0xf\n"
    "v_mfma_f32_16x16x32_bf16 v[80:83], v[168:171], a[212:215], v[80:83]\n"
    "v_add_f32_dpp v245, v229, v245 row_newbcast:5 row_mask:0xf bank_mask:0xf\n"
    "v_add_f32_dpp v246, v229, v246 row_newbcast:6 row_mask:0xf bank_mask:0xf\n"
    "v_mfma_f32_16x16x32_bf16 v[84:87], v[168:171], a[236:239], v[100:103]\n"
    "ds_read_b128 v[176:179], v200 offset:31744\n"
    "v_add_f32_dpp v247, v229, v247 row_newbcast:7 row_mask:0xf bank_mask:0xf\n"
    "v_mfma_f32_16x16x32_bf16 v[84:87], v[164:167], a[232:235], v[84:87]\n"
    "v_cvt_pk_bf16_f32 v240, v240, v241\n"
    "v_cvt_pk_bf16_f32 v241, v242, v243\n"
    "v_mfma_f32_16x16x32_bf16 v[84:87], v[160:163], a[228:231], v[84:87]\n"
    "v_cvt_pk_bf16_f32 v242, v244, v245\n"
    "v_cvt_pk_bf16_f32 v243, v246, v247\n"
    "v_mfma_f32_16x16x32_bf16 v[88:91], v[160:163], a[252:255], v[104:107]\n"
    "ds_read_b128 v[180:183], v200 offset:32768\n"
    "v_cvt_pk_bf16_f32 v136, v40, v41\n"
    "v_mfma_f32_16x16x32_bf16 v[88:91], v[164:167], v[0:3], v[88:91]\n"
    "v_cvt_pk_bf16_f32 v137, v42, v43\n"
    "v_cvt_pk_bf16_f32 v132, v36, v37\n"
    "v_mfma_f32_16x16x32_bf16 v[88:91], v[168:171], v[4:7], v[88:91]\n"
    "v_cvt_pk_bf16_f32 v133, v38, v39\n"
    "v_cvt_pk_bf16_f32 v128, v32, v33\n"
    "v_mfma_f32_16x16x32_bf16 v[92:95], v[168:171], v[28:31], v[108:111]\n"
    "v_cvt_pk_bf16_f32 v129, v34, v35\n"
    "v_cvt_pk_bf16_f32 v142, v60, v61\n"
    "v_mfma_f32_16x16x32_bf16 v[92:95], v[164:167], v[24:27], v[92:95]\n"
    "v_exp_f32 v64, v64\n"
    "v_mfma_f32_16x16x32_bf16 v[92:95], v[160:163], v[20:23], v[92:95]\n"
    "s_waitcnt lgkmcnt(1)\n"
    "v_exp_f32 v65, v65\n"
    "v_mfma_f32_16x16x32_bf16 v[92:95], v[172:175], v[8:11], v[92:95]\n"
    "ds_read_b128 v[160:163], v200 offset:36864\n"
    "v_cvt_pk_bf16_f32 v143, v62, v63\n"
    "v_mfma_f32_16x16x32_bf16 v[92:95], v[176:179], v[12:15], v[92:95]\n"
    "s_waitcnt lgkmcnt(1)\n"
    "v_exp_f32 v66, v66\n"
    "v_mfma_f32_16x16x32_bf16 v[92:95], v[180:183], v[16:19], v[92:95]\n"
    "buffer_store_dwordx4 v[240:243], v212, s[40:43], s63 offen sc1\n"
    "v_exp_f32 v67, v67\n"
    "v_cvt_pk_bf16_f32 v138, v56, v57\n"
    "v_mfma_f32_16x16x32_bf16 v[88:91], v[180:183], a[248:251], v[88:91]\n"
    "ds_read_b128 v[164:167], v200 offset:37888\n"
    "v_cvt_pk_bf16_f32 v139, v58, v59\n"
    "v_mfma_f32_16x16x32_bf16 v[88:91], v[176:179], a[244:247], v[88:91]\n"
    "v_exp_f32 v68, v68\n"
    "v_mfma_f32_16x16x32_bf16 v[88:91], v[172:175], a[240:243], v[88:91]\n"
    "v_exp_f32 v69, v69\n"
    "v_mfma_f32_16x16x32_bf16 v[84:87], v[172:175], a[216:219], v[84:87]\n"
    "ds_read_b128 v[168:171], v200 offset:38912\n"
    "v_cvt_pk_bf16_f32 v134, v52, v53\n"
    "v_mfma_f32_16x16x32_bf16 v[84:87], v[176:179], a[220:223], v[84:87]\n"
    "v_exp_f32 v70, v70\n"
    "v_mfma_f32_16x16x32_bf16 v[84:87], v[180:183], a[224:227], v[84:87]\n"
    "v_exp_f32 v71, v71\n"
    "v_mfma_f32_16x16x32_bf16 v[80:83], v[180:183], a[200:203], v[80:83]\n"
    "v_exp_f32 v72, v72\n"
    "v_mfma_f32_16x16x32_bf16 v[80:83], v[176:179], a[196:199], v[80:83]\n"
    "v_cvt_pk_bf16_f32 v135, v54, v55\n"
    "v_cvt_pk_bf16_f32 v130, v48, v49\n"
    "v_mfma_f32_16x16x32_bf16 v[80:83], v[172:175], a[192:195], v[80:83]\n"
    "s_waitcnt lgkmcnt(1)\n"
    "v_exp_f32 v73, v73\n"
    "v_mfma_f32_16x16x32_bf16 v[32:35], v[160:163], a[192:195], v[96:99]\n"
    "ds_read_b128 v[172:175], v200 offset:39936\n"
    "v_cvt_pk_bf16_f32 v131, v50, v51\n"
    "v_mfma_f32_16x16x32_bf16 v[32:35], v[164:167], a[196:199], v[32:35]\n"
    "s_waitcnt lgkmcnt(1)\n"
    "v_exp_f32 v74, v74\n"
    "v_mfma_f32_16x16x32_bf16 v[32:35], v[168:171], a[200:203], v[32:35]\n"
    "v_exp_f32 v75, v75\n"
    "v_mfma_f32_16x16x32_bf16 v[36:39], v[168:171], a[224:227], v[100:103]\n"
    "ds_read_b128 v[176:179], v200 offset:40960\n"
    "v_cvt_pk_bf16_f32 v144, v64, v65\n"
    "v_mfma_f32_16x16x32_bf16 v[36:39], v[164:167], a[220:223], v[36:39]\n"
    "v_exp_f32 v76, v76\n"
    "v_mfma_f32_16x16x32_bf16 v[36:39], v[160:163], a[216:219], v[36:39]\n"
    "v_exp_f32 v77, v77\n"
    "v_mfma_f32_16x16x32_bf16 v[40:43], v[160:163], a[240:243], v[104:107]\n"
    "ds_read_b128 v[180:183], v200 offset:41984\n"
    "v_cvt_pk_bf16_f32 v145, v66, v67\n"
    "v_mfma_f32_16x16x32_bf16 v[40:43], v[164:167], a[244:247], v[40:43]\n"
    "v_exp_f32 v78, v78\n"
    "v_mfma_f32_16x16x32_bf16 v[40:43], v[168:171], a[248:251], v[40:43]\n"
    "v_exp_f32 v79, v79\n"
    "v_mfma_f32_16x16x32_bf16 v[44:47], v[168:171], v[16:19], v[108:111]\n"
    "v_exp_f32 v80, v80\n"
    "v_mfma_f32_16x16x32_bf16 v[44:47], v[164:167], v[12:15], v[44:47]\n"
    "v_exp_f32 v81, v81\n"
    "v_mfma_f32_16x16x32_bf16 v[44:47], v[160:163], v[8:11], v[44:47]\n"
    "s_waitcnt lgkmcnt(1)\n"
    "v_exp_f32 v82, v82\n"
    "v_mfma_f32_16x16x32_bf16 v[44:47], v[172:175], v[20:23], v[44:47]\n"
    "ds_read_b128 v[160:163], v200 offset:46080\n"
    "v_cvt_pk_bf16_f32 v148, v68, v69\n"
    "v_mfma_f32_16x16x32_bf16 v[44:47], v[176:179], v[24:27], v[44:47]\n"
    "s_waitcnt lgkmcnt(1)\n"
    "v_exp_f32 v83, v83\n"
    "v_mfma_f32_16x16x32_bf16 v[44:47], v[180:183], v[28:31], v[44:47]\n"
    "v_exp_f32 v84, v84\n"
    "v_mfma_f32_16x16x32_bf16 v[40:43], v[180:183], v[4:7], v[40:43]\n"
    "ds_read_b128 v[164:167], v200 offset:47104\n"
    "v_cvt_pk_bf16_f32 v149, v70, v71\n"
    "v_mfma_f32_16x16x32_bf16 v[40:43], v[176:179], v[0:3], v[40:43]\n"
    "v_exp_f32 v85, v85\n"
    "v_mfma_f32_16x16x32_bf16 v[40:43], v[172:175], a[252:255], v[40:43]\n"
    "v_exp_f32 v86, v86\n"
    "v_mfma_f32_16x16x32_bf16 v[36:39], v[172:175], a[228:231], v[36:39]\n"
    "ds_read_b128 v[168:171], v200 offset:48128\n"
    "v_cvt_pk_bf16_f32 v152, v72, v73\n"
    "v_mfma_f32_16x16x32_bf16 v[36:39], v[176:179], a[232:235], v[36:39]\n"
    "v_exp_f32 v87, v87\n"
    "v_mfma_f32_16x16x32_bf16 v[36:39], v[180:183], a[236:239], v[36:39]\n"
    "v_exp_f32 v88, v88\n"
    "v_mfma_f32_16x16x32_bf16 v[32:35], v[180:183], a[212:215], v[32:35]\n"
    "v_exp_f32 v89, v89\n"
    "v_mfma_f32_16x16x32_bf16 v[32:35], v[176:179], a[208:211], v[32:35]\n"
    "v_cvt_pk_bf16_f32 v153, v74, v75\n"
    "v_cvt_pk_bf16_f32 v156, v76, v77\n"
    "v_mfma_f32_16x16x32_bf16 v[32:35], v[172:175], a[204:207], v[32:35]\n"
    "s_waitcnt lgkmcnt(1)\n"
    "s_mov_b32 m0, 0\n"
    "v_cvt_pk_bf16_f32 v157, v78, v79\n"
    "v_mfma_f32_16x16x32_bf16 v[48:51], v[160:163], a[204:207], v[96:99]\n"
    "ds_read_b128 v[172:175], v200 offset:43008\n"
    "v_cvt_pk_bf16_f32 v146, v80, v81\n"
    "v_mfma_f32_16x16x32_bf16 v[48:51], v[164:167], a[208:211], v[48:51]\n"
    "s_waitcnt lgkmcnt(1)\n"
    "buffer_load_dwordx4 v200, s[36:39], s60 offen sc1 lds\n"
    "v_mfma_f32_16x16x32_bf16 v[48:51], v[168:171], a[212:215], v[48:51]\n"
    "v_exp_f32 v90, v90\n"
    "v_mfma_f32_16x16x32_bf16 v[52:55], v[168:171], a[236:239], v[100:103]\n"
    "ds_read_b128 v[176:179], v200 offset:44032\n"
    "v_cvt_pk_bf16_f32 v147, v82, v83\n"
    "v_mfma_f32_16x16x32_bf16 v[52:55], v[164:167], a[232:235], v[52:55]\n"
    "v_exp_f32 v91, v91\n"
    "v_mfma_f32_16x16x32_bf16 v[52:55], v[160:163], a[228:231], v[52:55]\n"
    "buffer_load_dwordx4 v200, s[36:39], s60 offen offset:1024 sc1 lds\n"
    "v_mfma_f32_16x16x32_bf16 v[56:59], v[160:163], a[252:255], v[104:107]\n"
    "ds_read_b128 v[180:183], v200 offset:45056\n"
    "v_cvt_pk_bf16_f32 v150, v84, v85\n"
    "v_mfma_f32_16x16x32_bf16 v[56:59], v[164:167], v[0:3], v[56:59]\n"
    "v_exp_f32 v92, v92\n"
    "v_mfma_f32_16x16x32_bf16 v[56:59], v[168:171], v[4:7], v[56:59]\n"
    "v_exp_f32 v93, v93\n"
    "v_mfma_f32_16x16x32_bf16 v[60:63], v[168:171], v[28:31], v[108:111]\n"
    "buffer_load_dwordx4 v200, s[36:39], s60 offen offset:2048 sc1 lds\n"
    "v_mfma_f32_16x16x32_bf16 v[60:63], v[164:167], v[24:27], v[60:63]\n"
    "v_cvt_pk_bf16_f32 v151, v86, v87\n"
    "v_cvt_pk_bf16_f32 v154, v88, v89\n"
    "v_mfma_f32_16x16x32_bf16 v[60:63], v[160:163], v[20:23], v[60:63]\n"
    "s_waitcnt lgkmcnt(1)\n"
    "s_add_u32 m0, s71, 0x12000\n"
    "v_cvt_pk_bf16_f32 v155, v90, v91\n"
    "v_mfma_f32_16x16x32_bf16 v[60:63], v[172:175], v[8:11], v[60:63]\n"
    "ds_read_b128 v[160:163], v201\n"
    "v_cvt_pk_bf16_f32 v158, v92, v93\n"
    "v_mfma_f32_16x16x32_bf16 v[60:63], v[176:179], v[12:15], v[60:63]\n"
    "s_waitcnt lgkmcnt(1)\n"
    "buffer_load_dwordx4 v200, s[40:43], s61 offen sc1 lds\n"
    "v_mfma_f32_16x16x32_bf16 v[60:63], v[180:183], v[16:19], v[60:63]\n"
    "v_exp_f32 v94, v94\n"
    "v_mfma_f32_16x16x32_bf16 v[56:59], v[180:183], a[248:251], v[56:59]\n"
    "ds_read_b128 v[164:167], v201 offset:1024\n"
    "v_add_f32 v113, v113, v64\n"
    "v_mfma_f32_16x16x32_bf16 v[56:59], v[176:179], a[244:247], v[56:59]\n"
    "v_exp_f32 v95, v95\n"
    "v_mfma_f32_16x16x32_bf16 v[56:59], v[172:175], a[240:243], v[56:59]\n"
    "buffer_load_dwordx4 v200, s[40:43], s61 offen offset:1024 sc1 lds\n"
    "v_mfma_f32_16x16x32_bf16 v[52:55], v[172:175], a[216:219], v[52:55]\n"
    "ds_read_b128 v[168:171], v201 offset:2048\n"
    "v_cvt_pk_bf16_f32 v159, v94, v95\n"
    "v_mfma_f32_16x16x32_bf16 v[52:55], v[176:179], a[220:223], v[52:55]\n"
    "v_exp_f32 v32, v32\n"
    "v_mfma_f32_16x16x32_bf16 v[52:55], v[180:183], a[224:227], v[52:55]\n"
    "v_exp_f32 v33, v33\n"
    "v_mfma_f32_16x16x32_bf16 v[48:51], v[180:183], a[200:203], v[48:51]\n"
    "buffer_load_dwordx4 v200, s[40:43], s61 offen offset:2048 sc1 lds\n"
    "v_mfma_f32_16x16x32_bf16 v[48:51], v[176:179], a[196:199], v[48:51]\n"
    "v_exp_f32 v34, v34\n"
    "v_mfma_f32_16x16x32_bf16 v[48:51], v[172:175], a[192:195], v[48:51]\n"
    "s_waitcnt lgkmcnt(1)\n"
    "v_exp_f32 v35, v35\n"
    "v_mfma_f32_16x16x32_bf16 a[0:3], v[160:163], v[128:131], a[0:3]\n"
    "ds_read_b128 v[172:175], v201 offset:3072\n"
    "ds_read_b128 v[176:179], v201 offset:4096\n"
    "v_mfma_f32_16x16x32_bf16 a[0:3], v[164:167], v[144:147], a[0:3]\n"
    "buffer_load_dwordx4 v200, s[40:43], s61 offen offset:3072 sc1 lds\n"
    "v_mfma_f32_16x16x32_bf16 a[4:7], v[160:163], v[132:135], a[4:7]\n"
    "s_add_u32 s65, s61, 0x1000\n"
    "v_exp_f32 v36, v36\n"
    "v_mfma_f32_16x16x32_bf16 a[4:7], v[164:167], v[148:151], a[4:7]\n"
    "s_add_u32 m0, s71, 0x13000\n"
    "v_exp_f32 v37, v37\n"
    "v_mfma_f32_16x16x32_bf16 a[8:11], v[160:163], v[136:139], a[8:11]\n"
    "ds_read_b128 v[180:183], v201 offset:5120\n"
    "v_add_f32 v114, v114, v65\n"
    "v_mfma_f32_16x16x32_bf16 a[8:11], v[164:167], v[152:155], a[8:11]\n"
    "buffer_load_dwordx4 v200, s[40:43], s65 offen sc1 lds\n"
    "v_mfma_f32_16x16x32_bf16 a[12:15], v[160:163], v[140:143], a[12:15]\n"
    "v_exp_f32 v38, v38\n"
    "v_mfma_f32_16x16x32_bf16 a[12:15], v[164:167], v[156:159], a[12:15]\n"
    "s_waitcnt lgkmcnt(2)\n"
    "v_exp_f32 v39, v39\n"
    "v_mfma_f32_16x16x32_bf16 a[28:31], v[168:171], v[140:143], a[28:31]\n"
    "ds_read_b128 v[160:163], v201 offset:6144\n"
    "v_add_f32 v113, v113, v66\n"
    "v_mfma_f32_16x16x32_bf16 a[28:31], v[172:175], v[156:159], a[28:31]\n"
    "buffer_load_dwordx4 v200, s[40:43], s65 offen offset:1024 sc1 lds\n"
    "v_mfma_f32_16x16x32_bf16 a[24:27], v[172:175], v[152:155], a[24:27]\n"
    "v_exp_f32 v40, v40\n"
    "v_mfma_f32_16x16x32_bf16 a[24:27], v[168:171], v[136:139], a[24:27]\n"
    "v_exp_f32 v41, v41\n"
    "v_mfma_f32_16x16x32_bf16 a[20:23], v[168:171], v[132:135], a[20:23]\n"
    "ds_read_b128 v[164:167], v201 offset:7168\n"
    "v_add_f32 v114, v114, v67\n"
    "v_mfma_f32_16x16x32_bf16 a[20:23], v[172:175], v[148:151], a[20:23]\n"
    "v_exp_f32 v42, v42\n"
    "v_mfma_f32_16x16x32_bf16 a[16:19], v[172:175], v[144:147], a[16:19]\n"
    "v_exp_f32 v43, v43\n"
    "v_mfma_f32_16x16x32_bf16 a[16:19], v[168:171], v[128:131], a[16:19]\n"
    "s_waitcnt lgkmcnt(2)\n"
    "v_exp_f32 v44, v44\n"
    "v_mfma_f32_16x16x32_bf16 a[32:35], v[176:179], v[128:131], a[32:35]\n"
    "ds_read_b128 v[168:171], v201 offset:8192\n"
    "v_add_f32 v117, v117, v68\n"
    "v_mfma_f32_16x16x32_bf16 a[32:35], v[180:183], v[144:147], a[32:35]\n"
    "buffer_load_dwordx2 v[218:219], v213, s[44:47], 0 offen sc1\n"
    "v_mfma_f32_16x16x32_bf16 a[36:39], v[180:183], v[148:151], a[36:39]\n"
    "v_exp_f32 v45, v45\n"
    "v_mfma_f32_16x16x32_bf16 a[36:39], v[176:179], v[132:135], a[36:39]\n"
    "v_exp_f32 v46, v46\n"
    "v_mfma_f32_16x16x32_bf16 a[40:43], v[176:179], v[136:139], a[40:43]\n"
    "ds_read_b128 v[172:175], v201 offset:9216\n"
    "v_add_f32 v118, v118, v69\n"
    "v_mfma_f32_16x16x32_bf16 a[40:43], v[180:183], v[152:155], a[40:43]\n"
    "v_exp_f32 v47, v47\n"
    "v_mfma_f32_16x16x32_bf16 a[44:47], v[180:183], v[156:159], a[44:47]\n"
    "v_exp_f32 v48, v48\n"
    "v_mfma_f32_16x16x32_bf16 a[44:47], v[176:179], v[140:143], a[44:47]\n"
    "s_waitcnt lgkmcnt(2)\n"
    "v_exp_f32 v49, v49\n"
    "v_mfma_f32_16x16x32_bf16 a[60:63], v[160:163], v[140:143], a[60:63]\n"
    "ds_read_b128 v[176:179], v201 offset:10240\n"
    "v_add_f32 v117, v117, v70\n"
    "v_mfma_f32_16x16x32_bf16 a[60:63], v[164:167], v[156:159], a[60:63]\n"
    "v_exp_f32 v50, v50\n"
    "v_mfma_f32_16x16x32_bf16 a[56:59], v[164:167], v[152:155], a[56:59]\n"
    "v_exp_f32 v51, v51\n"
    "v_mfma_f32_16x16x32_bf16 a[56:59], v[160:163], v[136:139], a[56:59]\n"
    "v_add_f32 v113, v113, v32\n"
    "v_add_f32 v118, v118, v71\n"
    "v_mfma_f32_16x16x32_bf16 a[52:55], v[160:163], v[132:135], a[52:55]\n"
    "ds_read_b128 v[180:183], v201 offset:11264\n"
    "v_add_f32 v114, v114, v33\n"
    "v_mfma_f32_16x16x32_bf16 a[52:55], v[164:167], v[148:151], a[52:55]\n"
    "v_exp_f32 v52, v52\n"
    "v_mfma_f32_16x16x32_bf16 a[48:51], v[164:167], v[144:147], a[48:51]\n"
    "v_add_f32 v121, v121, v72\n"
    "v_add_f32 v113, v113, v34\n"
    "v_mfma_f32_16x16x32_bf16 a[48:51], v[160:163], v[128:131], a[48:51]\n"
    "s_waitcnt lgkmcnt(2)\n"
    "v_exp_f32 v53, v53\n"
    "v_mfma_f32_16x16x32_bf16 a[64:67], v[168:171], v[128:131], a[64:67]\n"
    "ds_read_b128 v[160:163], v201 offset:12288\n"
    "v_add_f32 v122, v122, v73\n"
    "v_mfma_f32_16x16x32_bf16 a[64:67], v[172:175], v[144:147], a[64:67]\n"
    "v_add_f32 v114, v114, v35\n"
    "v_add_f32 v121, v121, v74\n"
    "v_mfma_f32_16x16x32_bf16 a[68:71], v[172:175], v[148:151], a[68:71]\n"
    "v_add_f32 v117, v117, v36\n"
    "v_add_f32 v122, v122, v75\n"
    "v_mfma_f32_16x16x32_bf16 a[68:71], v[168:171], v[132:135], a[68:71]\n"
    "s_add_u32 m0, s51, s95\n"
    "v_exp_f32 v54, v54\n"
    "v_mfma_f32_16x16x32_bf16 a[72:75], v[168:171], v[136:139], a[72:75]\n"
    "ds_read_b128 v[164:167], v201 offset:13312\n"
    "v_add_f32 v118, v118, v37\n"
    "v_mfma_f32_16x16x32_bf16 a[72:75], v[172:175], v[152:155], a[72:75]\n"
    "buffer_load_dwordx4 v202, s[32:35], s64 offen lds\n"
    "v_mfma_f32_16x16x32_bf16 a[76:79], v[172:175], v[156:159], a[76:79]\n"
    "v_add_f32 v125, v125, v76\n"
    "v_add_f32 v117, v117, v38\n"
    "v_mfma_f32_16x16x32_bf16 a[76:79], v[168:171], v[140:143], a[76:79]\n"
    "s_waitcnt lgkmcnt(2)\n"
    "v_exp_f32 v55, v55\n"
    "v_mfma_f32_16x16x32_bf16 a[92:95], v[176:179], v[140:143], a[92:95]\n"
    "ds_read_b128 v[168:171], v201 offset:14336\n"
    "v_add_f32 v126, v126, v77\n"
    "v_mfma_f32_16x16x32_bf16 a[92:95], v[180:183], v[156:159], a[92:95]\n"
    "buffer_load_dwordx4 v203, s[32:35], s64 offen offset:1024 lds\n"
    "v_mfma_f32_16x16x32_bf16 a[88:91], v[180:183], v[152:155], a[88:91]\n"
    "v_add_f32 v118, v118, v39\n"
    "v_add_f32 v125, v125, v78\n"
    "v_mfma_f32_16x16x32_bf16 a[88:91], v[176:179], v[136:139], a[88:91]\n"
    "v_add_f32 v121, v121, v40\n"
    "v_add_f32 v126, v126, v79\n"
    "v_mfma_f32_16x16x32_bf16 a[84:87], v[176:179], v[132:135], a[84:87]\n"
    "ds_read_b128 v[172:175], v201 offset:15360\n"
    "v_add_f32 v122, v122, v41\n"
    "v_mfma_f32_16x16x32_bf16 a[84:87], v[180:183], v[148:151], a[84:87]\n"
    "buffer_load_dwordx4 v204, s[32:35], s64 offen offset:2048 lds\n"
    "v_mfma_f32_16x16x32_bf16 a[80:83], v[180:183], v[144:147], a[80:83]\n"
    "v_exp_f32 v56, v56\n"
    "v_mfma_f32_16x16x32_bf16 a[80:83], v[176:179], v[128:131], a[80:83]\n"
    "s_waitcnt lgkmcnt(2)\n"
    "v_add_f32 v113, v113, v80\n"
    "v_add_f32 v121, v121, v42\n"
    "v_mfma_f32_16x16x32_bf16 a[96:99], v[160:163], v[128:131], a[96:99]\n"
    "s_mov_b32 s95, s94\n"
    "ds_read_b128 v[176:179], v201 offset:16384\n"
    "v_add_f32 v114, v114, v81\n"
    "v_mfma_f32_16x16x32_bf16 a[96:99], v[164:167], v[144:147], a[96:99]\n"
    "s_sub_u32 s94, s94, 0x3000\n"
    "v_exp_f32 v57, v57\n"
    "v_mfma_f32_16x16x32_bf16 a[100:103], v[164:167], v[148:151], a[100:103]\n"
    "s_min_u32 s94, s94, 0x6000\n"
    "v_add_f32 v122, v122, v43\n"
    "v_add_f32 v113, v113, v82\n"
    "v_mfma_f32_16x16x32_bf16 a[100:103], v[160:163], v[132:135], a[100:103]\n"
    "s_sub_u32 s93, s94, s95\n"
    "v_add_f32 v125, v125, v44\n"
    "v_add_f32 v114, v114, v83\n"
    "v_mfma_f32_16x16x32_bf16 a[104:107], v[160:163], v[136:139], a[104:107]\n"
    "ds_read_b128 v[180:183], v201 offset:17408\n"
    "v_add_f32 v126, v126, v45\n"
    "v_mfma_f32_16x16x32_bf16 a[104:107], v[164:167], v[152:155], a[104:107]\n"
    "v_exp_f32 v58, v58\n"
    "v_mfma_f32_16x16x32_bf16 a[108:111], v[164:167], v[156:159], a[108:111]\n"
    "v_add_f32 v117, v117, v84\n"
    "v_add_f32 v125, v125, v46\n"
    "v_mfma_f32_16x16x32_bf16 a[108:111], v[160:163], v[140:143], a[108:111]\n"
    "s_waitcnt lgkmcnt(2)\n"
    "v_exp_f32 v59, v59\n"
    "v_mfma_f32_16x16x32_bf16 a[124:127], v[168:171], v[140:143], a[124:127]\n"
    "ds_read_b128 v[160:163], v201 offset:18432\n"
    "v_add_f32 v118, v118, v85\n"
    "v_mfma_f32_16x16x32_bf16 a[124:127], v[172:175], v[156:159], a[124:127]\n"
    "v_add_f32 v126, v126, v47\n"
    "v_add_f32 v117, v117, v86\n"
    "v_mfma_f32_16x16x32_bf16 a[120:123], v[172:175], v[152:155], a[120:123]\n"
    "v_add_f32 v113, v113, v48\n"
    "v_add_u32 v220, 1, v220\n"
    "v_mfma_f32_16x16x32_bf16 a[120:123], v[168:171], v[136:139], a[120:123]\n"
    "v_exp_f32 v60, v60\n"
    "v_mfma_f32_16x16x32_bf16 a[116:119], v[168:171], v[132:135], a[116:119]\n"
    "ds_read_b128 v[164:167], v201 offset:19456\n"
    "v_add_u32 v205, s93, v205\n"
    "v_mfma_f32_16x16x32_bf16 a[116:119], v[172:175], v[148:151], a[116:119]\n"
    "v_add_f32 v118, v118, v87\n"
    "v_add_f32 v114, v114, v49\n"
    "v_mfma_f32_16x16x32_bf16 a[112:115], v[172:175], v[144:147], a[112:115]\n"
    "v_add_u32 v209, 0x180, v209\n"
    "v_add_u32 v206, s93, v206\n"
    "v_mfma_f32_16x16x32_bf16 a[112:115], v[168:171], v[128:131], a[112:115]\n"
    "s_waitcnt lgkmcnt(2)\n"
    "v_add_f32 v121, v121, v88\n"
    "v_add_f32 v113, v113, v50\n"
    "v_mfma_f32_16x16x32_bf16 a[128:131], v[176:179], v[128:131], a[128:131]\n"
    "s_add_u32 s64, s64, 0xc000\n"
    "ds_read_b128 v[168:171], v201 offset:20480\n"
    "v_add_u32 v210, 0x80, v210\n"
    "v_mfma_f32_16x16x32_bf16 a[128:131], v[180:183], v[144:147], a[128:131]\n"
    "s_add_u32 s62, s62, 0x6000\n"
    "v_exp_f32 v61, v61\n"
    "v_mfma_f32_16x16x32_bf16 a[132:135], v[180:183], v[148:151], a[132:135]\n"
    "s_add_u32 s63, s63, 0x6000\n"
    "v_add_u32 v207, s93, v207\n"
    "v_add_f32 v122, v122, v89\n"
    "v_mfma_f32_16x16x32_bf16 a[132:135], v[176:179], v[132:135], a[132:135]\n"
    "s_add_u32 s60, s60, 0x6000\n"
    "v_add_f32 v114, v114, v51\n"
    "v_add_f32 v121, v121, v90\n"
    "v_mfma_f32_16x16x32_bf16 a[136:139], v[176:179], v[136:139], a[136:139]\n"
    "s_add_u32 s61, s61, 0x6000\n"
    "ds_read_b128 v[172:175], v201 offset:21504\n"
    "v_add_f32 v117, v117, v52\n"
    "v_mfma_f32_16x16x32_bf16 a[136:139], v[180:183], v[152:155], a[136:139]\n"
    "s_add_u32 s66, s66, s74\n"
    "v_exp_f32 v62, v62\n"
    "v_mfma_f32_16x16x32_bf16 a[140:143], v[180:183], v[156:159], a[140:143]\n"
    "s_min_u32 s66, s66, 33\n"
    "v_add_f32 v122, v122, v91\n"
    "v_add_f32 v118, v118, v53\n"
    "v_mfma_f32_16x16x32_bf16 a[140:143], v[176:179], v[140:143], a[140:143]\n"
    "s_waitcnt lgkmcnt(2)\n"
    "v_add_f32 v125, v125, v92\n"
    "v_add_f32 v117, v117, v54\n"
    "v_mfma_f32_16x16x32_bf16 a[156:159], v[160:163], v[140:143], a[156:159]\n"
    "ds_read_b128 v[176:179], v201 offset:22528\n"
    "v_add_f32 v126, v126, v93\n"
    "v_mfma_f32_16x16x32_bf16 a[156:159], v[164:167], v[156:159], a[156:159]\n"
    "s_waitcnt vmcnt(3)\n"
    "ds_read_b128 v[180:183], v201 offset:23552\n"
    "v_add_f32 v118, v118, v55\n"
    "v_mfma_f32_16x16x32_bf16 a[152:155], v[164:167], v[152:155], a[152:155]\n"
    "v_cmp_ne_u32 s[80:81], s67, v219\n"
    "v_add_f32 v125, v125, v94\n"
    "v_mfma_f32_16x16x32_bf16 a[152:155], v[160:163], v[136:139], a[152:155]\n"
    "v_cmp_gt_u32 vcc, s66, v218\n"
    "v_add_f32 v121, v121, v56\n"
    "v_mfma_f32_16x16x32_bf16 a[148:151], v[160:163], v[132:135], a[148:151]\n"
    "v_exp_f32 v63, v63\n"
    "v_mfma_f32_16x16x32_bf16 a[148:151], v[164:167], v[148:151], a[148:151]\n"
    "s_or_b64 vcc, vcc, s[80:81]\n"
    "s_cbranch_vccnz LSPRE4\n"
    "LSPB4:\n"
    "v_add_f32 v126, v126, v95\n"
    "v_mfma_f32_16x16x32_bf16 a[144:147], v[164:167], v[144:147], a[144:147]\n"
    "v_add_f32 v122, v122, v57\n"
    "v_mfma_f32_16x16x32_bf16 a[144:147], v[160:163], v[128:131], a[144:147]\n"
    "s_waitcnt lgkmcnt(0)\n"
    "v_mfma_f32_16x16x32_bf16 a[160:163], v[168:171], v[128:131], a[160:163]\n"
    "s_barrier\n"
    "v_mfma_f32_16x16x32_bf16 a[160:163], v[172:175], v[144:147], a[160:163]\n"
    "buffer_store_dwordx2 v[220:221], v214, s[44:47], 0 offen sc1\n"
    "v_mfma_f32_16x16x32_bf16 a[164:167], v[172:175], v[148:151], a[164:167]\n"
    "ds_read_b128 v[160:163], v200\n"
    "ds_read_u16_d16_hi v223, v209 offset:6144\n"
    "v_mfma_f32_16x16x32_bf16 a[164:167], v[168:171], v[132:135], a[164:167]\n"
    "ds_read_u16_d16_hi v222, v209\n"
    "v_add_f32 v121, v121, v58\n"
    "v_mfma_f32_16x16x32_bf16 a[168:171], v[168:171], v[136:139], a[168:171]\n"
    "ds_read_b128 v[164:167], v200 offset:1024\n"
    "ds_read_b128 v[232:235], v205 offset:24576\n"
    "v_mfma_f32_16x16x32_bf16 a[168:171], v[172:175], v[152:155], a[168:171]\n"
    "v_add_f32 v122, v122, v59\n"
    "ds_read_b128 v[236:239], v206 offset:24576\n"
    "v_mfma_f32_16x16x32_bf16 a[172:175], v[172:175], v[156:159], a[172:175]\n"
    "v_add_f32 v125, v125, v60\n"
    "v_add_f32 v126, v126, v61\n"
    "v_mfma_f32_16x16x32_bf16 a[172:175], v[168:171], v[140:143], a[172:175]\n"
    "v_add_f32 v125, v125, v62\n"
    "v_add_f32 v126, v126, v63\n"
    "v_mfma_f32_16x16x32_bf16 a[188:191], v[176:179], v[140:143], a[188:191]\n"
    "ds_read_b128 v[168:171], v200 offset:2048\n"
    "ds_read_b64 v[228:229], v210\n"
    "v_mfma_f32_16x16x32_bf16 a[188:191], v[180:183], v[156:159], a[188:191]\n"
    "ds_read2st64_b32 v[240:241], v207 offset0:96 offset1:99\n"
    "ds_read2st64_b32 v[242:243], v207 offset0:102 offset1:105\n"
    "v_mfma_f32_16x16x32_bf16 a[184:187], v[176:179], v[136:139], a[184:187]\n"
    "ds_read2st64_b32 v[244:245], v207 offset0:108 offset1:111\n"
    "ds_read2st64_b32 v[246:247], v207 offset0:114 offset1:117\n"
    "v_mfma_f32_16x16x32_bf16 a[184:187], v[180:183], v[152:155], a[184:187]\n"
    "v_cvt_pk_bf16_f32 v140, v44, v45\n"
    "v_cvt_pk_bf16_f32 v141, v46, v47\n"
    "v_mfma_f32_16x16x32_bf16 a[180:183], v[176:179], v[132:135], a[180:183]\n"
    "ds_read_b128 v[172:175], v200 offset:3072\n"
    "s_waitcnt lgkmcnt(7)\n"
    "v_mfma_f32_16x16x32_bf16 a[180:183], v[180:183], v[148:151], a[180:183]\n"
    "v_mul_f32 v226, s75, v223\n"
    "v_fma_f32 v227, -v222, v226, v217\n"
    "v_mfma_f32_16x16x32_bf16 a[176:179], v[176:179], v[128:131], a[176:179]\n"
    "v_cvt_f32_i32 v232, v232\n"
    "v_cvt_f32_i32 v233, v233\n"
    "v_mfma_f32_16x16x32_bf16 a[176:179], v[180:183], v[144:147], a[176:179]\n"
    "v_cvt_f32_i32 v234, v234\n"
    "v_cvt_f32_i32 v235, v235\n"
    "LM0:\n"
    "v_mfma_f32_16x16x32_bf16 v[64:67], v[160:163], a[192:195], v[96:99]\n"
    "ds_read_b128 v[176:179], v200 offset:4096\n"
    "v_cvt_f32_i32 v236, v236\n"
    "v_mfma_f32_16x16x32_bf16 v[64:67], v[164:167], a[196:199], v[64:67]\n"
    "s_waitcnt lgkmcnt(1)\n"
    "v_cvt_f32_i32 v237, v237\n"
    "v_cvt_f32_i32 v238, v238\n"
    "v_mfma_f32_16x16x32_bf16 v[64:67], v[168:171], a[200:203], v[64:67]\n"
    "v_cvt_f32_i32 v239, v239\n"
    "v_mul_f32_dpp v232, v226, v232 row_newbcast:0 row_mask:0xf bank_mask:0xf\n"
    "v_mfma_f32_16x16x32_bf16 v[68:71], v[168:171], a[224:227], v[100:103]\n"
    "v_mul_f32_dpp v233, v226, v233 row_newbcast:1 row_mask:0xf bank_mask:0xf\n"
    "v_mul_f32_dpp v234, v226, v234 row_newbcast:2 row_mask:0xf bank_mask:0xf\n"
    "v_mfma_f32_16x16x32_bf16 v[68:71], v[164:167], a[220:223], v[68:71]\n"
    "ds_read_b128 v[180:183], v200 offset:5120\n"
    "v_mul_f32_dpp v235, v226, v235 row_newbcast:3 row_mask:0xf bank_mask:0xf\n"
    "v_mfma_f32_16x16x32_bf16 v[68:71], v[160:163], a[216:219], v[68:71]\n"
    "v_mul_f32_dpp v236, v226, v236 row_newbcast:4 row_mask:0xf bank_mask:0xf\n"
    "v_mul_f32_dpp v237, v226, v237 row_newbcast:5 row_mask:0xf bank_mask:0xf\n"
    "v_mfma_f32_16x16x32_bf16 v[72:75], v[160:163], a[240:243], v[104:107]\n"
    "v_mul_f32_dpp v238, v226, v238 row_newbcast:6 row_mask:0xf bank_mask:0xf\n"
    "v_mul_f32_dpp v239, v226, v239 row_newbcast:7 row_mask:0xf bank_mask:0xf\n"
    "v_mfma_f32_16x16x32_bf16 v[72:75], v[164:167], a[244:247], v[72:75]\n"
    "v_add_f32_dpp v232, v227, v232 row_newbcast:0 row_mask:0xf bank_mask:0xf\n"
    "v_add_f32_dpp v233, v227, v233 row_newbcast:1 row_mask:0xf bank_mask:0xf\n"
    "v_mfma_f32_16x16x32_bf16 v[72:75], v[168:171], a[248:251], v[72:75]\n"
    "v_add_f32_dpp v234, v227, v234 row_newbcast:2 row_mask:0xf bank_mask:0xf\n"
    "v_add_f32_dpp v235, v227, v235 row_newbcast:3 row_mask:0xf bank_mask:0xf\n"
    "v_mfma_f32_16x16x32_bf16 v[76:79], v[168:171], v[16:19], v[108:111]\n"
    "v_add_f32_dpp v236, v227, v236 row_newbcast:4 row_mask:0xf bank_mask:0xf\n"
    "v_add_f32_dpp v237, v227, v237 row_newbcast:5 row_mask:0xf bank_mask:0xf\n"
    "v_mfma_f32_16x16x32_bf16 v[76:79], v[164:167], v[12:15], v[76:79]\n"
    "v_add_f32_dpp v238, v227, v238 row_newbcast:6 row_mask:0xf bank_mask:0xf\n"
    "v_add_f32_dpp v239, v227, v239 row_newbcast:7 row_mask:0xf bank_mask:0xf\n"
    "v_mfma_f32_16x16x32_bf16 v[76:79], v[160:163], v[8:11], v[76:79]\n"
    "s_waitcnt lgkmcnt(1)\n"
    "v_cvt_pk_bf16_f32 v232, v232, v233\n"
    "v_cvt_f32_i32 v240, v240\n"
    "v_mfma_f32_16x16x32_bf16 v[76:79], v[172:175], v[20:23], v[76:79]\n"
    "ds_read_b128 v[160:163], v200 offset:9216\n"
    "v_cvt_pk_bf16_f32 v233, v234, v235\n"
    "v_mfma_f32_16x16x32_bf16 v[76:79], v[176:179], v[24:27], v[76:79]\n"
    "s_waitcnt lgkmcnt(1)\n"
    "v_cvt_f32_i32 v241, v241\n"
    "v_cvt_pk_bf16_f32 v234, v236, v237\n"
    "v_mfma_f32_16x16x32_bf16 v[76:79], v[180:183], v[28:31], v[76:79]\n"
    "v_cvt_f32_i32 v242, v242\n"
    "v_cvt_pk_bf16_f32 v235, v238, v239\n"
    "v_mfma_f32_16x16x32_bf16 v[72:75], v[180:183], v[4:7], v[72:75]\n"
    "ds_read_b128 v[164:167], v200 offset:10240\n"
    "v_cvt_f32_i32 v243, v243\n"
    "v_mfma_f32_16x16x32_bf16 v[72:75], v[176:179], v[0:3], v[72:75]\n"
    "v_cvt_f32_i32 v244, v244\n"
    "v_cvt_f32_i32 v245, v245\n"
    "v_mfma_f32_16x16x32_bf16 v[72:75], v[172:175], a[252:255], v[72:75]\n"
    "v_cvt_f32_i32 v246, v246\n"
    "v_cvt_f32_i32 v247, v247\n"
    "v_mfma_f32_16x16x32_bf16 v[68:71], v[172:175], a[228:231], v[68:71]\n"
    "ds_read_b128 v[168:171], v200 offset:11264\n"
    "v_mul_f32_dpp v240, v228, v240 row_newbcast:0 row_mask:0xf bank_mask:0xf\n"
    "v_mfma_f32_16x16x32_bf16 v[68:71], v[176:179], a[232:235], v[68:71]\n"
    "v_mul_f32_dpp v241, v228, v241 row_newbcast:1 row_mask:0xf bank_mask:0xf\n"
    "v_mul_f32_dpp v242, v228, v242 row_newbcast:2 row_mask:0xf bank_mask:0xf\n"
    "v_mfma_f32_16x16x32_bf16 v[68:71], v[180:183], a[236:239], v[68:71]\n"
    "v_mul_f32_dpp v243, v228, v243 row_newbcast:3 row_mask:0xf bank_mask:0xf\n"
    "v_mul_f32_dpp v244, v228, v244 row_newbcast:4 row_mask:0xf bank_mask:0xf\n"
    "v_mfma_f32_16x16x32_bf16 v[64:67], v[180:183], a[212:215], v[64:67]\n"
    "v_mul_f32_dpp v245, v228, v245 row_newbcast:5 row_mask:0xf bank_mask:0xf\n"
    "v_mul_f32_dpp v246, v228, v246 row_newbcast:6 row_mask:0xf bank_mask:0xf\n"
    "v_mfma_f32_16x16x32_bf16 v[64:67], v[176:179], a[208:211], v[64:67]\n"
    "buffer_store_dwordx4 v[232:235], v211, s[36:39], s62 offen sc1\n"
    "v_mul_f32_dpp v247, v228, v247 row_newbcast:7 row_mask:0xf bank_mask:0xf\n"
    "v_add_f32_dpp v240, v229, v240 row_newbcast:0 row_mask:0xf bank_mask:0xf\n"
    "v_add_f32_dpp v241, v229, v241 row_newbcast:1 row_mask:0xf bank_mask:0xf\n"
    "v_mfma_f32_16x16x32_bf16 v[64:67], v[172:175], a[204:207], v[64:67]\n"
    "s_waitcnt lgkmcnt(1)\n"
    "v_add_f32_dpp v242, v229, v242 row_newbcast:2 row_mask:0xf bank_mask:0xf\n"
    "v_add_f32_dpp v243, v229, v243 row_newbcast:3 row_mask:0xf bank_mask:0xf\n"
    "v_mfma_f32_16x16x32_bf16 v[80:83], v[160:163], a[204:207], v[96:99]\n"
    "ds_read_b128 v[172:175], v200 offset:6144\n"
    "v_add_f32_dpp v244, v229, v244 row_newbcast:4 row_mask:0xf bank_mask:0xf\n"
    "v_mfma_f32_16x16x32_bf16 v[80:83], v[164:167], a[208:211], v[80:83]\n"
    "s_waitcnt lgkmcnt(1)\n"
    "v_add_f32_dpp v245, v229, v245 row_newbcast:5 row_mask:0xf bank_mask:0xf\n"
    "v_add_f32_dpp v246, v229, v246 row_newbcast:6 row_mask:0xf bank_mask:0xf\n"
    "v_mfma_f32_16x16x32_bf16 v[80:83], v[168:171], a[212:215], v[80:83]\n"
    "v_add_f32_dpp v247, v229, v247 row_newbcast:7 row_mask:0xf bank_mask:0xf\n"
    "v_cvt_pk_bf16_f32 v240, v240, v241\n"
    "v_mfma_f32_16x16x32_bf16 v[84:87], v[168:171], a[236:239], v[100:103]\n"
    "ds_read_b128 v[176:179], v200 offset:7168\n"
    "v_cvt_pk_bf16_f32 v241, v242, v243\n"
    "v_mfma_f32_16x16x32_bf16 v[84:87], v[164:167], a[232:235], v[84:87]\n"
    "v_cvt_pk_bf16_f32 v242, v244, v245\n"
    "v_cvt_pk_bf16_f32 v243, v246, v247\n"
    "v_mfma_f32_16x16x32_bf16 v[84:87], v[160:163], a[228:231], v[84:87]\n"
    "v_cvt_pk_bf16_f32 v136, v40, v41\n"
    "v_cvt_pk_bf16_f32 v137, v42, v43\n"
    "v_mfma_f32_16x16x32_bf16 v[88:91], v[160:163], a[252:255], v[104:107]\n"
    "ds_read_b128 v[180:183], v200 offset:8192\n"
    "v_cvt_pk_bf16_f32 v132, v36, v37\n"
    "v_mfma_f32_16x16x32_bf16 v[88:91], v[164:167], v[0:3], v[88:91]\n"
    "v_cvt_pk_bf16_f32 v133, v38, v39\n"
    "v_cvt_pk_bf16_f32 v128, v32, v33\n"
    "v_mfma_f32_16x16x32_bf16 v[88:91], v[168:171], v[4:7], v[88:91]\n"
    "v_cvt_pk_bf16_f32 v129, v34, v35\n"
    "v_cvt_pk_bf16_f32 v142, v60, v61\n"
    "v_mfma_f32_16x16x32_bf16 v[92:95], v[168:171], v[28:31], v[108:111]\n"
    "v_exp_f32 v64, v64\n"
    "v_mfma_f32_16x16x32_bf16 v[92:95], v[164:167], v[24:27], v[92:95]\n"
    "v_exp_f32 v65, v65\n"
    "v_mfma_f32_16x16x32_bf16 v[92:95], v[160:163], v[20:23], v[92:95]\n"
    "s_waitcnt lgkmcnt(1)\n"
    "v_exp_f32 v66, v66\n"
    "v_mfma_f32_16x16x32_bf16 v[92:95], v[172:175], v[8:11], v[92:95]\n"
    "ds_read_b128 v[160:163], v200 offset:12288\n"
    "v_cvt_pk_bf16_f32 v143, v62, v63\n"
    "v_mfma_f32_16x16x32_bf16 v[92:95], v[176:179], v[12:15], v[92:95]\n"
    "s_waitcnt lgkmcnt(1)\n"
    "v_exp_f32 v67, v67\n"
    "v_mfma_f32_16x16x32_bf16 v[92:95], v[180:183], v[16:19], v[92:95]\n"
    "buffer_store_dwordx4 v[240:243], v212, s[40:43], s63 offen sc1\n"
    "v_exp_f32 v68, v68\n"
    "v_cvt_pk_bf16_f32 v138, v56, v57\n"
    "v_mfma_f32_16x16x32_bf16 v[88:91], v[180:183], a[248:251], v[88:91]\n"
    "ds_read_b128 v[164:167], v200 offset:13312\n"
    "v_cvt_pk_bf16_f32 v139, v58, v59\n"
    "v_mfma_f32_16x16x32_bf16 v[88:91], v[176:179], a[244:247], v[88:91]\n"
    "v_exp_f32 v69, v69\n"
    "v_mfma_f32_16x16x32_bf16 v[88:91], v[172:175], a[240:243], v[88:91]\n"
    "v_exp_f32 v70, v70\n"
    "v_mfma_f32_16x16x32_bf16 v[84:87], v[172:175], a[216:219], v[84:87]\n"
    "ds_read_b128 v[168:171], v200 offset:14336\n"
    "v_cvt_pk_bf16_f32 v134, v52, v53\n"
    "v_mfma_f32_16x16x32_bf16 v[84:87], v[176:179], a[220:223], v[84:87]\n"
    "v_exp_f32 v71, v71\n"
    "v_mfma_f32_16x16x32_bf16 v[84:87], v[180:183], a[224:227], v[84:87]\n"
    "v_exp_f32 v72, v72\n"
    "v_mfma_f32_16x16x32_bf16 v[80:83], v[180:183], a[200:203], v[80:83]\n"
    "v_cvt_pk_bf16_f32 v135, v54, v55\n"
    "v_cvt_pk_bf16_f32 v130, v48, v49\n"
    "v_mfma_f32_16x16x32_bf16 v[80:83], v[176:179], a[196:199], v[80:83]\n"
    "v_exp_f32 v73, v73\n"
    "v_mfma_f32_16x16x32_bf16 v[80:83], v[172:175], a[192:195], v[80:83]\n"
    "s_waitcnt lgkmcnt(1)\n"
    "v_cvt_pk_bf16_f32 v131, v50, v51\n"
    "v_cvt_pk_bf16_f32 v144, v64, v65\n"
    "v_mfma_f32_16x16x32_bf16 v[32:35], v[160:163], a[192:195], v[96:99]\n"
    "ds_read_b128 v[172:175], v200 offset:15360\n"
    "v_cvt_pk_bf16_f32 v145, v66, v67\n"
    "v_mfma_f32_16x16x32_bf16 v[32:35], v[164:167], a[196:199], v[32:35]\n"
    "s_waitcnt lgkmcnt(1)\n"
    "v_exp_f32 v74, v74\n"
    "v_mfma_f32_16x16x32_bf16 v[32:35], v[168:171], a[200:203], v[32:35]\n"
    "v_exp_f32 v75, v75\n"
    "v_mfma_f32_16x16x32_bf16 v[36:39], v[168:171], a[224:227], v[100:103]\n"
    "ds_read_b128 v[176:179], v200 offset:16384\n"
    "v_cvt_pk_bf16_f32 v148, v68, v69\n"
    "v_mfma_f32_16x16x32_bf16 v[36:39], v[164:167], a[220:223], v[36:39]\n"
    "v_exp_f32 v76, v76\n"
    "v_mfma_f32_16x16x32_bf16 v[36:39], v[160:163], a[216:219], v[36:39]\n"
    "v_exp_f32 v77, v77\n"
    "v_mfma_f32_16x16x32_bf16 v[40:43], v[160:163], a[240:243], v[104:107]\n"
    "ds_read_b128 v[180:183], v200 offset:17408\n"
    "v_cvt_pk_bf16_f32 v149, v70, v71\n"
    "v_mfma_f32_16x16x32_bf16 v[40:43], v[164:167], a[244:247], v[40:43]\n"
    "v_exp_f32 v78, v78\n"
    "v_mfma_f32_16x16x32_bf16 v[40:43], v[168:171], a[248:251], v[40:43]\n"
    "v_exp_f32 v79, v79\n"
    "v_mfma_f32_16x16x32_bf16 v[44:47], v[168:171], v[16:19], v[108:111]\n"
    "v_exp_f32 v80, v80\n"
    "v_mfma_f32_16x16x32_bf16 v[44:47], v[164:167], v[12:15], v[44:47]\n"
    "v_exp_f32 v81, v81\n"
    "v_mfma_f32_16x16x32_bf16 v[44:47], v[160:163], v[8:11], v[44:47]\n"
    "s_waitcnt lgkmcnt(1)\n"
    "v_exp_f32 v82, v82\n"
    "v_mfma_f32_16x16x32_bf16 v[44:47], v[172:175], v[20:23], v[44:47]\n"
    "ds_read_b128 v[160:163], v200 offset:21504\n"
    "v_cvt_pk_bf16_f32 v152, v72, v73\n"
    "v_mfma_f32_16x16x32_bf16 v[44:47], v[176:179], v[24:27], v[44:47]\n"
    "s_waitcnt lgkmcnt(1)\n"
    "v_exp_f32 v83, v83\n"
    "v_mfma_f32_16x16x32_bf16 v[44:47], v[180:183], v[28:31], v[44:47]\n"
    "v_exp_f32 v84, v84\n"
    "v_mfma_f32_16x16x32_bf16 v[40:43], v[180:183], v[4:7], v[40:43]\n"
    "ds_read_b128 v[164:167], v200 offset:22528\n"
    "v_cvt_pk_bf16_f32 v153, v74, v75\n"
    "v_mfma_f32_16x16x32_bf16 v[40:43], v[176:179], v[0:3], v[40:43]\n"
    "v_exp_f32 v85, v85\n"
    "v_mfma_f32_16x16x32_bf16 v[40:43], v[172:175], a[252:255], v[40:43]\n"
    "v_exp_f32 v86, v86\n"
    "v_mfma_f32_16x16x32_bf16 v[36:39], v[172:175], a[228:231], v[36:39]\n"
    "ds_read_b128 v[168:171], v200 offset:23552\n"
    "v_cvt_pk_bf16_f32 v156, v76, v77\n"
    "v_mfma_f32_16x16x32_bf16 v[36:39], v[176:179], a[232:235], v[36:39]\n"
    "v_exp_f32 v87, v87\n"
    "v_mfma_f32_16x16x32_bf16 v[36:39], v[180:183], a[236:239], v[36:39]\n"
    "v_exp_f32 v88, v88\n"
    "v_mfma_f32_16x16x32_bf16 v[32:35], v[180:183], a[212:215], v[32:35]\n"
    "v_exp_f32 v89, v89\n"
    "v_mfma_f32_16x16x32_bf16 v[32:35], v[176:179], a[208:211], v[32:35]\n"
    "v_exp_f32 v90, v90\n"
    "v_mfma_f32_16x16x32_bf16 v[32:35], v[172:175], a[204:207], v[32:35]\n"
    "s_waitcnt lgkmcnt(1)\n"
    "s_mov_b32 m0, 0x6000\n"
    "v_cvt_pk_bf16_f32 v157, v78, v79\n"
    "v_mfma_f32_16x16x32_bf16 v[48:51], v[160:163], a[204:207], v[96:99]\n"
    "ds_read_b128 v[172:175], v200 offset:18432\n"
    "v_cvt_pk_bf16_f32 v146, v80, v81\n"
    "v_mfma_f32_16x16x32_bf16 v[48:51], v[164:167], a[208:211], v[48:51]\n"
    "s_waitcnt lgkmcnt(1)\n"
    "buffer_load_dwordx4 v200, s[36:39], s60 offen sc1 lds\n"
    "v_mfma_f32_16x16x32_bf16 v[48:51], v[168:171], a[212:215], v[48:51]\n"
    "v_exp_f32 v91, v91\n"
    "v_mfma_f32_16x16x32_bf16 v[52:55], v[168:171], a[236:239], v[100:103]\n"
    "ds_read_b128 v[176:179], v200 offset:19456\n"
    "v_cvt_pk_bf16_f32 v147, v82, v83\n"
    "v_mfma_f32_16x16x32_bf16 v[52:55], v[164:167], a[232:235], v[52:55]\n"
    "v_exp_f32 v92, v92\n"
    "v_mfma_f32_16x16x32_bf16 v[52:55], v[160:163], a[228:231], v[52:55]\n"
    "buffer_load_dwordx4 v200, s[36:39], s60 offen offset:1024 sc1 lds\n"
    "v_mfma_f32_16x16x32_bf16 v[56:59], v[160:163], a[252:255], v[104:107]\n"
    "ds_read_b128 v[180:183], v200 offset:20480\n"
    "v_cvt_pk_bf16_f32 v150, v84, v85\n"
    "v_mfma_f32_16x16x32_bf16 v[56:59], v[164:167], v[0:3], v[56:59]\n"
    "v_exp_f32 v93, v93\n"
    "v_mfma_f32_16x16x32_bf16 v[56:59], v[168:171], v[4:7], v[56:59]\n"
    "v_cvt_pk_bf16_f32 v151, v86, v87\n"
    "v_cvt_pk_bf16_f32 v154, v88, v89\n"
    "v_mfma_f32_16x16x32_bf16 v[60:63], v[168:171], v[28:31], v[108:111]\n"
    "buffer_load_dwordx4 v200, s[36:39], s60 offen offset:2048 sc1 lds\n"
    "v_mfma_f32_16x16x32_bf16 v[60:63], v[164:167], v[24:27], v[60:63]\n"
    "v_exp_f32 v94, v94\n"
    "v_mfma_f32_16x16x32_bf16 v[60:63], v[160:163], v[20:23], v[60:63]\n"
    "s_waitcnt lgkmcnt(1)\n"
    "s_add_u32 m0, s71, 0xc000\n"
    "v_cvt_pk_bf16_f32 v155, v90, v91\n"
    "v_mfma_f32_16x16x32_bf16 v[60:63], v[172:175], v[8:11], v[60:63]\n"
    "ds_read_b128 v[160:163], v201 offset:24576\n"
    "v_cvt_pk_bf16_f32 v158, v92, v93\n"
    "v_mfma_f32_16x16x32_bf16 v[60:63], v[176:179], v[12:15], v[60:63]\n"
    "s_waitcnt lgkmcnt(1)\n"
    "buffer_load_dwordx4 v200, s[40:43], s61 offen sc1 lds\n"
    "v_mfma_f32_16x16x32_bf16 v[60:63], v[180:183], v[16:19], v[60:63]\n"
    "v_exp_f32 v95, v95\n"
    "v_mfma_f32_16x16x32_bf16 v[56:59], v[180:183], a[248:251], v[56:59]\n"
    "ds_read_b128 v[164:167], v201 offset:25600\n"
    "v_cvt_pk_bf16_f32 v159, v94, v95\n"
    "v_mfma_f32_16x16x32_bf16 v[56:59], v[176:179], a[244:247], v[56:59]\n"
    "v_exp_f32 v32, v32\n"
    "v_mfma_f32_16x16x32_bf16 v[56:59], v[172:175], a[240:243], v[56:59]\n"
    "buffer_load_dwordx4 v200, s[40:43], s61 offen offset:1024 sc1 lds\n"
    "v_mfma_f32_16x16x32_bf16 v[52:55], v[172:175], a[216:219], v[52:55]\n"
    "ds_read_b128 v[168:171], v201 offset:26624\n"
    "v_add_f32 v113, v113, v64\n"
    "v_mfma_f32_16x16x32_bf16 v[52:55], v[176:179], a[220:223], v[52:55]\n"
    "v_exp_f32 v33, v33\n"
    "v_mfma_f32_16x16x32_bf16 v[52:55], v[180:183], a[224:227], v[52:55]\n"
    "v_exp_f32 v34, v34\n"
    "v_mfma_f32_16x16x32_bf16 v[48:51], v[180:183], a[200:203], v[48:51]\n"
    "buffer_load_dwordx4 v200, s[40:43], s61 offen offset:2048 sc1 lds\n"
    "v_mfma_f32_16x16x32_bf16 v[48:51], v[176:179], a[196:199], v[48:51]\n"
    "v_exp_f32 v35, v35\n"
    "v_mfma_f32_16x16x32_bf16 v[48:51], v[172:175], a[192:195], v[48:51]\n"
    "s_waitcnt lgkmcnt(1)\n"
    "v_exp_f32 v36, v36\n"
    "v_mfma_f32_16x16x32_bf16 a[0:3], v[160:163], v[128:131], a[0:3]\n"
    "ds_read_b128 v[172:175], v201 offset:27648\n"
    "ds_read_b128 v[176:179], v201 offset:28672\n"
    "v_mfma_f32_16x16x32_bf16 a[0:3], v[164:167], v[144:147], a[0:3]\n"
    "buffer_load_dwordx4 v200, s[40:43], s61 offen offset:3072 sc1 lds\n"
    "v_mfma_f32_16x16x32_bf16 a[4:7], v[160:163], v[132:135], a[4:7]\n"
    "s_add_u32 s65, s61, 0x1000\n"
    "v_exp_f32 v37, v37\n"
    "v_mfma_f32_16x16x32_bf16 a[4:7], v[164:167], v[148:151], a[4:7]\n"
    "s_add_u32 m0, s71, 0xd000\n"
    "v_exp_f32 v38, v38\n"
    "v_mfma_f32_16x16x32_bf16 a[8:11], v[160:163], v[136:139], a[8:11]\n"
    "ds_read_b128 v[180:183], v201 offset:29696\n"
    "v_add_f32 v114, v114, v65\n"
    "v_mfma_f32_16x16x32_bf16 a[8:11], v[164:167], v[152:155], a[8:11]\n"
    "buffer_load_dwordx4 v200, s[40:43], s65 offen sc1 lds\n"
    "v_mfma_f32_16x16x32_bf16 a[12:15], v[160:163], v[140:143], a[12:15]\n"
    "v_exp_f32 v39, v39\n"
    "v_mfma_f32_16x16x32_bf16 a[12:15], v[164:167], v[156:159], a[12:15]\n"
    "s_waitcnt lgkmcnt(2)\n"
    "v_exp_f32 v40, v40\n"
    "v_mfma_f32_16x16x32_bf16 a[28:31], v[168:171], v[140:143], a[28:31]\n"
    "ds_read_b128 v[160:163], v201 offset:30720\n"
    "v_add_f32 v113, v113, v66\n"
    "v_mfma_f32_16x16x32_bf16 a[28:31], v[172:175], v[156:159], a[28:31]\n"
    "buffer_load_dwordx4 v200, s[40:43], s65 offen offset:1024 sc1 lds\n"
    "v_mfma_f32_16x16x32_bf16 a[24:27], v[172:175], v[152:155], a[24:27]\n"
    "v_exp_f32 v41, v41\n"
    "v_mfma_f32_16x16x32_bf16 a[24:27], v[168:171], v[136:139], a[24:27]\n"
    "v_exp_f32 v42, v42\n"
    "v_mfma_f32_16x16x32_bf16 a[20:23], v[168:171], v[132:135], a[20:23]\n"
    "ds_read_b128 v[164:167], v201 offset:31744\n"
    "v_add_f32 v114, v114, v67\n"
    "v_mfma_f32_16x16x32_bf16 a[20:23], v[172:175], v[148:151], a[20:23]\n"
    "v_exp_f32 v43, v43\n"
    "v_mfma_f32_16x16x32_bf16 a[16:19], v[172:175], v[144:147], a[16:19]\n"
    "v_exp_f32 v44, v44\n"
    "v_mfma_f32_16x16x32_bf16 a[16:19], v[168:171], v[128:131], a[16:19]\n"
    "s_waitcnt lgkmcnt(2)\n"
    "v_exp_f32 v45, v45\n"
    "v_mfma_f32_16x16x32_bf16 a[32:35], v[176:179], v[128:131], a[32:35]\n"
    "ds_read_b128 v[168:171], v201 offset:32768\n"
    "v_add_f32 v117, v117, v68\n"
    "v_mfma_f32_16x16x32_bf16 a[32:35], v[180:183], v[144:147], a[32:35]\n"
    "buffer_load_dwordx2 v[218:219], v213, s[44:47], 0 offen sc1\n"
    "v_mfma_f32_16x16x32_bf16 a[36:39], v[180:183], v[148:151], a[36:39]\n"
    "v_exp_f32 v46, v46\n"
    "v_mfma_f32_16x16x32_bf16 a[36:39], v[176:179], v[132:135], a[36:39]\n"
    "v_exp_f32 v47, v47\n"
    "v_mfma_f32_16x16x32_bf16 a[40:43], v[176:179], v[136:139], a[40:43]\n"
    "ds_read_b128 v[172:175], v201 offset:33792\n"
    "v_add_f32 v118, v118, v69\n"
    "v_mfma_f32_16x16x32_bf16 a[40:43], v[180:183], v[152:155], a[40:43]\n"
    "v_exp_f32 v48, v48\n"
    "v_mfma_f32_16x16x32_bf16 a[44:47], v[180:183], v[156:159], a[44:47]\n"
    "v_exp_f32 v49, v49\n"
    "v_mfma_f32_16x16x32_bf16 a[44:47], v[176:179], v[140:143], a[44:47]\n"
    "s_waitcnt lgkmcnt(2)\n"
    "v_exp_f32 v50, v50\n"
    "v_mfma_f32_16x16x32_bf16 a[60:63], v[160:163], v[140:143], a[60:63]\n"
    "ds_read_b128 v[176:179], v201 offset:34816\n"
    "v_add_f32 v117, v117, v70\n"
    "v_mfma_f32_16x16x32_bf16 a[60:63], v[164:167], v[156:159], a[60:63]\n"
    "v_exp_f32 v51, v51\n"
    "v_mfma_f32_16x16x32_bf16 a[56:59], v[164:167], v[152:155], a[56:59]\n"
    "v_add_f32 v113, v113, v32\n"
    "v_add_f32 v118, v118, v71\n"
    "v_mfma_f32_16x16x32_bf16 a[56:59], v[160:163], v[136:139], a[56:59]\n"
    "v_exp_f32 v52, v52\n"
    "v_mfma_f32_16x16x32_bf16 a[52:55], v[160:163], v[132:135], a[52:55]\n"
    "ds_read_b128 v[180:183], v201 offset:35840\n"
    "v_add_f32 v114, v114, v33\n"
    "v_mfma_f32_16x16x32_bf16 a[52:55], v[164:167], v[148:151], a[52:55]\n"
    "v_add_f32 v121, v121, v72\n"
    "v_add_f32 v113, v113, v34\n"
    "v_mfma_f32_16x16x32_bf16 a[48:51], v[164:167], v[144:147], a[48:51]\n"
    "v_exp_f32 v53, v53\n"
    "v_mfma_f32_16x16x32_bf16 a[48:51], v[160:163], v[128:131], a[48:51]\n"
    "s_waitcnt lgkmcnt(2)\n"
    "v_add_f32 v122, v122, v73\n"
    "v_add_f32 v114, v114, v35\n"
    "v_mfma_f32_16x16x32_bf16 a[64:67], v[168:171], v[128:131], a[64:67]\n"
    "ds_read_b128 v[160:163], v201 offset:36864\n"
    "v_add_f32 v121, v121, v74\n"
    "v_mfma_f32_16x16x32_bf16 a[64:67], v[172:175], v[144:147], a[64:67]\n"
    "v_add_f32 v117, v117, v36\n"
    "v_add_f32 v122, v122, v75\n"
    "v_mfma_f32_16x16x32_bf16 a[68:71], v[172:175], v[148:151], a[68:71]\n"
    "v_exp_f32 v54, v54\n"
    "v_mfma_f32_16x16x32_bf16 a[68:71], v[168:171], v[132:135], a[68:71]\n"
    "s_add_u32 m0, s51, s95\n"
    "v_add_f32 v118, v118, v37\n"
    "v_add_f32 v125, v125, v76\n"
    "v_mfma_f32_16x16x32_bf16 a[72:75], v[168:171], v[136:139], a[72:75]\n"
    "ds_read_b128 v[164:167], v201 offset:37888\n"
    "v_add_f32 v117, v117, v38\n"
    "v_mfma_f32_16x16x32_bf16 a[72:75], v[172:175], v[152:155], a[72:75]\n"
    "buffer_load_dwordx4 v202, s[32:35], s64 offen lds\n"
    "v_mfma_f32_16x16x32_bf16 a[76:79], v[172:175], v[156:159], a[76:79]\n"
    "v_exp_f32 v55, v55\n"
    "v_mfma_f32_16x16x32_bf16 a[76:79], v[168:171], v[140:143], a[76:79]\n"
    "s_waitcnt lgkmcnt(2)\n"
    "v_add_f32 v126, v126, v77\n"
    "v_add_f32 v118, v118, v39\n"
    "v_mfma_f32_16x16x32_bf16 a[92:95], v[176:179], v[140:143], a[92:95]\n"
    "ds_read_b128 v[168:171], v201 offset:38912\n"
    "v_add_f32 v125, v125, v78\n"
    "v_mfma_f32_16x16x32_bf16 a[92:95], v[180:183], v[156:159], a[92:95]\n"
    "buffer_load_dwordx4 v203, s[32:35], s64 offen offset:1024 lds\n"
    "v_mfma_f32_16x16x32_bf16 a[88:91], v[180:183], v[152:155], a[88:91]\n"
    "v_add_f32 v121, v121, v40\n"
    "v_add_f32 v126, v126, v79\n"
    "v_mfma_f32_16x16x32_bf16 a[88:91], v[176:179], v[136:139], a[88:91]\n"
    "v_exp_f32 v56, v56\n"
    "v_mfma_f32_16x16x32_bf16 a[84:87], v[176:179], v[132:135], a[84:87]\n"
    "ds_read_b128 v[172:175], v201 offset:39936\n"
    "v_add_f32 v122, v122, v41\n"
    "v_mfma_f32_16x16x32_bf16 a[84:87], v[180:183], v[148:151], a[84:87]\n"
    "buffer_load_dwordx4 v204, s[32:35], s64 offen offset:2048 lds\n"
    "v_mfma_f32_16x16x32_bf16 a[80:83], v[180:183], v[144:147], a[80:83]\n"
    "v_add_f32 v113, v113, v80\n"
    "v_add_f32 v121, v121, v42\n"
    "v_mfma_f32_16x16x32_bf16 a[80:83], v[176:179], v[128:131], a[80:83]\n"
    "s_waitcnt lgkmcnt(2)\n"
    "v_exp_f32 v57, v57\n"
    "v_mfma_f32_16x16x32_bf16 a[96:99], v[160:163], v[128:131], a[96:99]\n"
    "s_mov_b32 s95, s94\n"
    "ds_read_b128 v[176:179], v201 offset:40960\n"
    "v_add_f32 v114, v114, v81\n"
    "v_mfma_f32_16x16x32_bf16 a[96:99], v[164:167], v[144:147], a[96:99]\n"
    "s_sub_u32 s94, s94, 0x3000\n"
    "v_add_f32 v122, v122, v43\n"
    "v_add_f32 v113, v113, v82\n"
    "v_mfma_f32_16x16x32_bf16 a[100:103], v[164:167], v[148:151], a[100:103]\n"
    "s_min_u32 s94, s94, 0x6000\n"
    "v_add_f32 v125, v125, v44\n"
    "v_add_f32 v114, v114, v83\n"
    "v_mfma_f32_16x16x32_bf16 a[100:103], v[160:163], v[132:135], a[100:103]\n"
    "s_sub_u32 s93, s94, s95\n"
    "v_exp_f32 v58, v58\n"
    "v_mfma_f32_16x16x32_bf16 a[104:107], v[160:163], v[136:139], a[104:107]\n"
    "ds_read_b128 v[180:183], v201 offset:41984\n"
    "v_add_f32 v126, v126, v45\n"
    "v_mfma_f32_16x16x32_bf16 a[104:107], v[164:167], v[152:155], a[104:107]\n"
    "v_add_f32 v117, v117, v84\n"
    "v_add_f32 v125, v125, v46\n"
    "v_mfma_f32_16x16x32_bf16 a[108:111], v[164:167], v[156:159], a[108:111]\n"
    "v_exp_f32 v59, v59\n"
    "v_mfma_f32_16x16x32_bf16 a[108:111], v[160:163], v[140:143], a[108:111]\n"
    "s_waitcnt lgkmcnt(2)\n"
    "v_add_f32 v118, v118, v85\n"
    "v_add_f32 v126, v126, v47\n"
    "v_mfma_f32_16x16x32_bf16 a[124:127], v[168:171], v[140:143], a[124:127]\n"
    "ds_read_b128 v[160:163], v201 offset:43008\n"
    "v_add_f32 v117, v117, v86\n"
    "v_mfma_f32_16x16x32_bf16 a[124:127], v[172:175], v[156:159], a[124:127]\n"
    "v_add_f32 v113, v113, v48\n"
    "v_add_u32 v205, s93, v205\n"
    "v_mfma_f32_16x16x32_bf16 a[120:123], v[172:175], v[152:155], a[120:123]\n"
    "v_exp_f32 v60, v60\n"
    "v_mfma_f32_16x16x32_bf16 a[120:123], v[168:171], v[136:139], a[120:123]\n"
    "v_add_f32 v118, v118, v87\n"
    "v_add_f32 v114, v114, v49\n"
    "v_mfma_f32_16x16x32_bf16 a[116:119], v[168:171], v[132:135], a[116:119]\n"
    "ds_read_b128 v[164:167], v201 offset:44032\n"
    "v_add_u32 v206, s93, v206\n"
    "v_mfma_f32_16x16x32_bf16 a[116:119], v[172:175], v[148:151], a[116:119]\n"
    "v_add_f32 v121, v121, v88\n"
    "v_add_f32 v113, v113, v50\n"
    "v_mfma_f32_16x16x32_bf16 a[112:115], v[172:175], v[144:147], a[112:115]\n"
    "v_exp_f32 v61, v61\n"
    "v_mfma_f32_16x16x32_bf16 a[112:115], v[168:171], v[128:131], a[112:115]\n"
    "s_waitcnt lgkmcnt(2)\n"
    "v_add_u32 v220, 1, v220\n"
    "v_add_u32 v207, s93, v207\n"
    "v_mfma_f32_16x16x32_bf16 a[128:131], v[176:179], v[128:131], a[128:131]\n"
    "s_add_u32 s64, s64, 0xc000\n"
    "ds_read_b128 v[168:171], v201 offset:45056\n"
    "v_add_f32 v122, v122, v89\n"
    "v_mfma_f32_16x16x32_bf16 a[128:131], v[180:183], v[144:147], a[128:131]\n"
    "s_add_u32 s62, s62, 0x6000\n"
    "v_add_f32 v114, v114, v51\n"
    "v_add_f32 v121, v121, v90\n"
    "v_mfma_f32_16x16x32_bf16 a[132:135], v[180:183], v[148:151], a[132:135]\n"
    "s_add_u32 s63, s63, 0x6000\n"
    "v_add_f32 v117, v117, v52\n"
    "v_add_f32 v122, v122, v91\n"
    "v_mfma_f32_16x16x32_bf16 a[132:135], v[176:179], v[132:135], a[132:135]\n"
    "s_add_u32 s60, s60, 0x6000\n"
    "v_exp_f32 v62, v62\n"
    "v_mfma_f32_16x16x32_bf16 a[136:139], v[176:179], v[136:139], a[136:139]\n"
    "s_add_u32 s61, s61, 0x6000\n"
    "ds_read_b128 v[172:175], v201 offset:46080\n"
    "v_add_f32 v118, v118, v53\n"
    "v_mfma_f32_16x16x32_bf16 a[136:139], v[180:183], v[152:155], a[136:139]\n"
    "s_add_u32 s66, s66, s74\n"
    "v_add_f32 v125, v125, v92\n"
    "v_add_f32 v117, v117, v54\n"
    "v_mfma_f32_16x16x32_bf16 a[140:143], v[180:183], v[156:159], a[140:143]\n"
    "s_min_u32 s66, s66, 33\n"
    "v_exp_f32 v63, v63\n"
    "v_mfma_f32_16x16x32_bf16 a[140:143], v[176:179], v[140:143], a[140:143]\n"
    "s_waitcnt lgkmcnt(2)\n"
    "v_add_f32 v126, v126, v93\n"
    "v_add_f32 v118, v118, v55\n"
    "v_mfma_f32_16x16x32_bf16 a[156:159], v[160:163], v[140:143], a[156:159]\n"
    "ds_read_b128 v[176:179], v201 offset:47104\n"
    "v_add_f32 v125, v125, v94\n"
    "v_mfma_f32_16x16x32_bf16 a[156:159], v[164:167], v[156:159], a[156:159]\n"
    "s_waitcnt vmcnt(3)\n"
    "ds_read_b128 v[180:183], v201 offset:48128\n"
    "v_add_f32 v121, v121, v56\n"
    "v_mfma_f32_16x16x32_bf16 a[152:155], v[164:167], v[152:155], a[152:155]\n"
    "v_cmp_ne_u32 s[80:81], s67, v219\n"
    "v_add_f32 v126, v126, v95\n"
    "v_mfma_f32_16x16x32_bf16 a[152:155], v[160:163], v[136:139], a[152:155]\n"
    "v_cmp_gt_u32 vcc, s66, v218\n"
    "v_add_f32 v122, v122, v57\n"
    "v_mfma_f32_16x16x32_bf16 a[148:151], v[160:163], v[132:135], a[148:151]\n"
    "v_mfma_f32_16x16x32_bf16 a[148:151], v[164:167], v[148:151], a[148:151]\n"
    "s_or_b64 vcc, vcc, s[80:81]\n"
    "s_cbranch_vccnz LSPRE3\n"
    "LSPB3:\n"
    "v_mfma_f32_16x16x32_bf16 a[144:147], v[164:167], v[144:147], a[144:147]\n"
    "v_mfma_f32_16x16x32_bf16 a[144:147], v[160:163], v[128:131], a[144:147]\n"
    "s_waitcnt lgkmcnt(0)\n"
    "v_mfma_f32_16x16x32_bf16 a[160:163], v[168:171], v[128:131], a[160:163]\n"
    "s_barrier\n"
    "LMID:\n"
    "v_mfma_f32_16x16x32_bf16 a[160:163], v[172:175], v[144:147], a[160:163]\n"
    "buffer_store_dwordx2 v[220:221], v214, s[44:47], 0 offen sc1\n"
    "v_mfma_f32_16x16x32_bf16 a[164:167], v[172:175], v[148:151], a[164:167]\n"
    "ds_read_b128 v[160:163], v200 offset:24576\n"
    "ds_read_u16_d16_hi v223, v209 offset:6336\n"
    "v_mfma_f32_16x16x32_bf16 a[164:167], v[168:171], v[132:135], a[164:167]\n"
    "ds_read_u16_d16_hi v222, v209 offset:192\n"
    "v_add_f32 v121, v121, v58\n"
    "v_mfma_f32_16x16x32_bf16 a[168:171], v[168:171], v[136:139], a[168:171]\n"
    "ds_read_b128 v[164:167], v200 offset:25600\n"
    "ds_read_b128 v[232:235], v205 offset:24576\n"
    "v_mfma_f32_16x16x32_bf16 a[168:171], v[172:175], v[152:155], a[168:171]\n"
    "v_add_f32 v122, v122, v59\n"
    "ds_read_b128 v[236:239], v206 offset:24576\n"
    "v_mfma_f32_16x16x32_bf16 a[172:175], v[172:175], v[156:159], a[172:175]\n"
    "v_add_f32 v125, v125, v60\n"
    "v_add_f32 v126, v126, v61\n"
    "v_mfma_f32_16x16x32_bf16 a[172:175], v[168:171], v[140:143], a[172:175]\n"
    "v_add_f32 v125, v125, v62\n"
    "v_add_f32 v126, v126, v63\n"
    "v_mfma_f32_16x16x32_bf16 a[188:191], v[176:179], v[140:143], a[188:191]\n"
    "ds_read_b128 v[168:171], v200 offset:26624\n"
    "ds_read_b64 v[228:229], v210 offset:64\n"
    "v_mfma_f32_16x16x32_bf16 a[188:191], v[180:183], v[156:159], a[188:191]\n"
    "ds_read2st64_b32 v[240:241], v207 offset0:96 offset1:99\n"
    "ds_read2st64_b32 v[242:243], v207 offset0:102 offset1:105\n"
    "v_mfma_f32_16x16x32_bf16 a[184:187], v[176:179], v[136:139], a[184:187]\n"
    "ds_read2st64_b32 v[244:245], v207 offset0:108 offset1:111\n"
    "ds_read2st64_b32 v[246:247], v207 offset0:114 offset1:117\n"
    "v_mfma_f32_16x16x32_bf16 a[184:187], v[180:183], v[152:155], a[184:187]\n"
    "v_cvt_pk_bf16_f32 v140, v44, v45\n"
    "v_cvt_pk_bf16_f32 v141, v46, v47\n"
    "v_mfma_f32_16x16x32_bf16 a[180:183], v[176:179], v[132:135], a[180:183]\n"
    "s_add_u32 s59, s59, 1\n"
    "ds_read_b128 v[172:175], v200 offset:27648\n"
    "s_waitcnt lgkmcnt(7)\n"
    "v_mfma_f32_16x16x32_bf16 a[180:183], v[180:183], v[148:151], a[180:183]\n"
    "v_mul_f32 v226, s75, v223\n"
    "v_fma_f32 v227, -v222, v226, v217\n"
    "v_mfma_f32_16x16x32_bf16 a[176:179], v[176:179], v[128:131], a[176:179]\n"
    "s_cmp_lt_u32 s59, 15\n"
    "v_cvt_f32_i32 v232, v232\n"
    "v_cvt_f32_i32 v233, v233\n"
    "v_mfma_f32_16x16x32_bf16 a[176:179], v[180:183], v[144:147], a[176:179]\n"
    "s_cbranch_scc1 LLOOP\n"
    "LTAIL:\n"
    "s_waitcnt lgkmcnt(0)\n"
    "s_nop 1\n"
    "v_mfma_f32_16x16x32_bf16 v[64:67], v[160:163], a[192:195], v[96:99]\n"
    "ds_read_b128 v[176:179], v200 offset:28672\n"
    "v_cvt_pk_bf16_f32 v128, v32, v33\n"
    "v_mfma_f32_16x16x32_bf16 v[64:67], v[164:167], a[196:199], v[64:67]\n"
    "s_add_u32 m0, s71, 0\n"
    "v_cvt_pk_bf16_f32 v129, v34, v35\n"
    "v_cvt_pk_bf16_f32 v132, v36, v37\n"
    "v_mfma_f32_16x16x32_bf16 v[68:71], v[164:167], a[220:223], v[100:103]\n"
    "buffer_load_dwordx4 v200, s[36:39], s60 offen sc1 lds\n"
    "v_mfma_f32_16x16x32_bf16 v[68:71], v[160:163], a[216:219], v[68:71]\n"
    "v_cvt_pk_bf16_f32 v133, v38, v39\n"
    "v_cvt_pk_bf16_f32 v136, v40, v41\n"
    "v_mfma_f32_16x16x32_bf16 v[72:75], v[160:163], a[240:243], v[104:107]\n"
    "ds_read_b128 v[180:183], v200 offset:29696\n"
    "v_cvt_pk_bf16_f32 v137, v42, v43\n"
    "v_mfma_f32_16x16x32_bf16 v[72:75], v[164:167], a[244:247], v[72:75]\n"
    "v_cvt_pk_bf16_f32 v130, v48, v49\n"
    "v_cvt_pk_bf16_f32 v131, v50, v51\n"
    "v_mfma_f32_16x16x32_bf16 v[76:79], v[164:167], v[12:15], v[108:111]\n"
    "buffer_load_dwordx4 v200, s[36:39], s60 offen offset:1024 sc1 lds\n"
    "v_mfma_f32_16x16x32_bf16 v[76:79], v[160:163], v[8:11], v[76:79]\n"
    "v_cvt_pk_bf16_f32 v134, v52, v53\n"
    "v_cvt_pk_bf16_f32 v135, v54, v55\n"
    "v_mfma_f32_16x16x32_bf16 v[76:79], v[168:171], v[16:19], v[76:79]\n"
    "ds_read_b128 v[160:163], v200 offset:34816\n"
    "v_cvt_pk_bf16_f32 v138, v56, v57\n"
    "v_mfma_f32_16x16x32_bf16 v[76:79], v[172:175], v[20:23], v[76:79]\n"
    "v_cvt_pk_bf16_f32 v139, v58, v59\n"
    "v_cvt_pk_bf16_f32 v142, v60, v61\n"
    "v_mfma_f32_16x16x32_bf16 v[72:75], v[172:175], a[252:255], v[72:75]\n"
    "buffer_load_dwordx4 v200, s[36:39], s60 offen offset:2048 sc1 lds\n"
    "v_mfma_f32_16x16x32_bf16 v[72:75], v[168:171], a[248:251], v[72:75]\n"
    "v_cvt_pk_bf16_f32 v143, v62, v63\n"
    "v_add_f32 v113, v113, v114\n"
    "v_mfma_f32_16x16x32_bf16 v[68:71], v[168:171], a[224:227], v[68:71]\n"
    "ds_read_b128 v[164:167], v200 offset:35840\n"
    "v_add_f32 v117, v117, v118\n"
    "v_mfma_f32_16x16x32_bf16 v[68:71], v[172:175], a[228:231], v[68:71]\n"
    "v_add_f32 v121, v121, v122\n"
    "v_add_f32 v125, v125, v126\n"
    "v_mfma_f32_16x16x32_bf16 v[64:67], v[172:175], a[204:207], v[64:67]\n"
    "buffer_load_dwordx4 v200, s[36:39], s60 offen offset:3072 sc1 lds\n"
    "v_mfma_f32_16x16x32_bf16 v[64:67], v[168:171], a[200:203], v[64:67]\n"
    "s_waitcnt lgkmcnt(2)\n"
    "v_mov_b32 v115, v113\n"
    "v_mov_b32 v119, v117\n"
    "v_mfma_f32_16x16x32_bf16 v[64:67], v[176:179], a[208:211], v[64:67]\n"
    "s_add_u32 s65, s60, 0x1000\n"
    "ds_read_b128 v[168:171], v200 offset:32768\n"
    "v_mov_b32 v123, v121\n"
    "v_mfma_f32_16x16x32_bf16 v[64:67], v[180:183], a[212:215], v[64:67]\n"
    "s_add_u32 m0, s71, 0x1000\n"
    "v_mov_b32 v127, v125\n"
    "v_permlane32_swap_b32 v113, v115\n"
    "v_mfma_f32_16x16x32_bf16 v[68:71], v[180:183], a[236:239], v[68:71]\n"
    "buffer_load_dwordx4 v200, s[36:39], s65 offen sc1 lds\n"
    "v_mfma_f32_16x16x32_bf16 v[68:71], v[176:179], a[232:235], v[68:71]\n"
    "v_permlane32_swap_b32 v117, v119\n"
    "v_permlane32_swap_b32 v121, v123\n"
    "v_mfma_f32_16x16x32_bf16 v[72:75], v[176:179], v[0:3], v[72:75]\n"
    "ds_read_b128 v[172:175], v200 offset:33792\n"
    "v_permlane32_swap_b32 v125, v127\n"
    "v_mfma_f32_16x16x32_bf16 v[72:75], v[180:183], v[4:7], v[72:75]\n"
    "v_add_f32 v113, v113, v115\n"
    "v_add_f32 v117, v117, v119\n"
    "v_mfma_f32_16x16x32_bf16 v[76:79], v[180:183], v[28:31], v[76:79]\n"
    "buffer_load_dwordx4 v200, s[36:39], s65 offen offset:1024 sc1 lds\n"
    "v_mfma_f32_16x16x32_bf16 v[76:79], v[176:179], v[24:27], v[76:79]\n"
    "s_waitcnt lgkmcnt(2)\n"
    "v_add_f32 v121, v121, v123\n"
    "v_add_f32 v125, v125, v127\n"
    "v_mfma_f32_16x16x32_bf16 v[92:95], v[160:163], v[24:27], v[108:111]\n"
    "ds_read_b128 v[176:179], v200 offset:30720\n"
    "v_mov_b32 v115, v113\n"
    "v_mfma_f32_16x16x32_bf16 v[92:95], v[164:167], v[28:31], v[92:95]\n"
    "s_add_u32 m0, s71, 0x12000\n"
    "v_exp_f32 v64, v64\n"
    "v_mfma_f32_16x16x32_bf16 v[88:91], v[164:167], v[4:7], v[104:107]\n"
    "buffer_load_dwordx4 v200, s[40:43], s61 offen sc1 lds\n"
    "v_mfma_f32_16x16x32_bf16 v[88:91], v[160:163], v[0:3], v[88:91]\n"
    "v_exp_f32 v65, v65\n"
    "v_mfma_f32_16x16x32_bf16 v[84:87], v[160:163], a[232:235], v[100:103]\n"
    "ds_read_b128 v[180:183], v200 offset:31744\n"
    "v_cvt_pk_bf16_f32 v144, v64, v65\n"
    "v_mfma_f32_16x16x32_bf16 v[84:87], v[164:167], a[236:239], v[84:87]\n"
    "v_exp_f32 v66, v66\n"
    "v_mfma_f32_16x16x32_bf16 v[80:83], v[164:167], a[212:215], v[96:99]\n"
    "buffer_load_dwordx4 v200, s[40:43], s61 offen offset:1024 sc1 lds\n"
    "v_mfma_f32_16x16x32_bf16 v[80:83], v[160:163], a[208:211], v[80:83]\n"
    "s_waitcnt lgkmcnt(2)\n"
    "v_exp_f32 v67, v67\n"
    "v_mfma_f32_16x16x32_bf16 v[80:83], v[168:171], a[200:203], v[80:83]\n"
    "ds_read_b128 v[160:163], v200 offset:36864\n"
    "v_cvt_pk_bf16_f32 v145, v66, v67\n"
    "v_mfma_f32_16x16x32_bf16 v[80:83], v[172:175], a[204:207], v[80:83]\n"
    "v_exp_f32 v68, v68\n"
    "v_mfma_f32_16x16x32_bf16 v[84:87], v[172:175], a[228:231], v[84:87]\n"
    "buffer_load_dwordx4 v200, s[40:43], s61 offen offset:2048 sc1 lds\n"
    "v_mfma_f32_16x16x32_bf16 v[84:87], v[168:171], a[224:227], v[84:87]\n"
    "v_exp_f32 v69, v69\n"
    "v_mfma_f32_16x16x32_bf16 v[88:91], v[168:171], a[248:251], v[88:91]\n"
    "ds_read_b128 v[164:167], v200 offset:37888\n"
    "v_cvt_pk_bf16_f32 v148, v68, v69\n"
    "v_mfma_f32_16x16x32_bf16 v[88:91], v[172:175], a[252:255], v[88:91]\n"
    "v_exp_f32 v70, v70\n"
    "v_mfma_f32_16x16x32_bf16 v[92:95], v[172:175], v[20:23], v[92:95]\n"
    "buffer_load_dwordx4 v200, s[40:43], s61 offen offset:3072 sc1 lds\n"
    "v_mfma_f32_16x16x32_bf16 v[92:95], v[168:171], v[16:19], v[92:95]\n"
    "s_waitcnt lgkmcnt(2)\n"
    "v_exp_f32 v71, v71\n"
    "v_mfma_f32_16x16x32_bf16 v[92:95], v[176:179], v[8:11], v[92:95]\n"
    "s_add_u32 s65, s61, 0x1000\n"
    "ds_read_b128 v[168:171], v200 offset:38912\n"
    "v_cvt_pk_bf16_f32 v149, v70, v71\n"
    "v_mfma_f32_16x16x32_bf16 v[92:95], v[180:183], v[12:15], v[92:95]\n"
    "s_add_u32 m0, s71, 0x13000\n"
    "v_exp_f32 v72, v72\n"
    "v_mfma_f32_16x16x32_bf16 v[88:91], v[180:183], a[244:247], v[88:91]\n"
    "buffer_load_dwordx4 v200, s[40:43], s65 offen sc1 lds\n"
    "v_mfma_f32_16x16x32_bf16 v[88:91], v[176:179], a[240:243], v[88:91]\n"
    "v_exp_f32 v73, v73\n"
    "v_mfma_f32_16x16x32_bf16 v[84:87], v[176:179], a[216:219], v[84:87]\n"
    "ds_read_b128 v[172:175], v200 offset:39936\n"
    "v_cvt_pk_bf16_f32 v152, v72, v73\n"
    "v_mfma_f32_16x16x32_bf16 v[84:87], v[180:183], a[220:223], v[84:87]\n"
    "v_exp_f32 v74, v74\n"
    "v_mfma_f32_16x16x32_bf16 v[80:83], v[180:183], a[196:199], v[80:83]\n"
    "buffer_load_dwordx4 v200, s[40:43], s65 offen offset:1024 sc1 lds\n"
    "v_mfma_f32_16x16x32_bf16 v[80:83], v[176:179], a[192:195], v[80:83]\n"
    "s_waitcnt lgkmcnt(2)\n"
    "v_exp_f32 v75, v75\n"
    "v_mfma_f32_16x16x32_bf16 v[32:35], v[160:163], a[192:195], v[96:99]\n"
    "ds_read_b128 v[176:179], v200 offset:40960\n"
    "v_cvt_pk_bf16_f32 v153, v74, v75\n"
    "v_mfma_f32_16x16x32_bf16 v[32:35], v[164:167], a[196:199], v[32:35]\n"
    "v_exp_f32 v76, v76\n"
    "v_mfma_f32_16x16x32_bf16 v[36:39], v[164:167], a[220:223], v[100:103]\n"
    "v_exp_f32 v77, v77\n"
    "v_mfma_f32_16x16x32_bf16 v[36:39], v[160:163], a[216:219], v[36:39]\n"
    "v_exp_f32 v78, v78\n"
    "v_mfma_f32_16x16x32_bf16 v[40:43], v[160:163], a[240:243], v[104:107]\n"
    "ds_read_b128 v[180:183], v200 offset:41984\n"
    "v_cvt_pk_bf16_f32 v156, v76, v77\n"
    "v_mfma_f32_16x16x32_bf16 v[40:43], v[164:167], a[244:247], v[40:43]\n"
    "v_exp_f32 v79, v79\n"
    "v_mfma_f32_16x16x32_bf16 v[44:47], v[164:167], v[12:15], v[108:111]\n"
    "v_exp_f32 v80, v80\n"
    "v_mfma_f32_16x16x32_bf16 v[44:47], v[160:163], v[8:11], v[44:47]\n"
    "s_waitcnt lgkmcnt(2)\n"
    "v_exp_f32 v81, v81\n"
    "v_mfma_f32_16x16x32_bf16 v[44:47], v[168:171], v[16:19], v[44:47]\n"
    "ds_read_b128 v[160:163], v200 offset:47104\n"
    "v_cvt_pk_bf16_f32 v157, v78, v79\n"
    "v_mfma_f32_16x16x32_bf16 v[44:47], v[172:175], v[20:23], v[44:47]\n"
    "v_exp_f32 v82, v82\n"
    "v_mfma_f32_16x16x32_bf16 v[40:43], v[172:175], a[252:255], v[40:43]\n"
    "v_exp_f32 v83, v83\n"
    "v_mfma_f32_16x16x32_bf16 v[40:43], v[168:171], a[248:251], v[40:43]\n"
    "v_exp_f32 v84, v84\n"
    "v_mfma_f32_16x16x32_bf16 v[36:39], v[168:171], a[224:227], v[36:39]\n"
    "ds_read_b128 v[164:167], v200 offset:48128\n"
    "v_cvt_pk_bf16_f32 v146, v80, v81\n"
    "v_mfma_f32_16x16x32_bf16 v[36:39], v[172:175], a[228:231], v[36:39]\n"
    "v_exp_f32 v85, v85\n"
    "v_mfma_f32_16x16x32_bf16 v[32:35], v[172:175], a[204:207], v[32:35]\n"
    "v_exp_f32 v86, v86\n"
    "v_mfma_f32_16x16x32_bf16 v[32:35], v[168:171], a[200:203], v[32:35]\n"
    "s_waitcnt lgkmcnt(2)\n"
    "v_exp_f32 v87, v87\n"
    "v_mfma_f32_16x16x32_bf16 v[32:35], v[176:179], a[208:211], v[32:35]\n"
    "ds_read_b128 v[168:171], v200 offset:45056\n"
    "v_cvt_pk_bf16_f32 v147, v82, v83\n"
    "v_mfma_f32_16x16x32_bf16 v[32:35], v[180:183], a[212:215], v[32:35]\n"
    "v_exp_f32 v88, v88\n"
    "v_mfma_f32_16x16x32_bf16 v[36:39], v[180:183], a[236:239], v[36:39]\n"
    "v_exp_f32 v89, v89\n"
    "v_mfma_f32_16x16x32_bf16 v[36:39], v[176:179], a[232:235], v[36:39]\n"
    "v_exp_f32 v90, v90\n"
    "v_mfma_f32_16x16x32_bf16 v[40:43], v[176:179], v[0:3], v[40:43]\n"
    "ds_read_b128 v[172:175], v200 offset:46080\n"
    "v_cvt_pk_bf16_f32 v150, v84, v85\n"
    "v_mfma_f32_16x16x32_bf16 v[40:43], v[180:183], v[4:7], v[40:43]\n"
    "v_exp_f32 v91, v91\n"
    "v_mfma_f32_16x16x32_bf16 v[44:47], v[180:183], v[28:31], v[44:47]\n"
    "v_exp_f32 v92, v92\n"
    "v_mfma_f32_16x16x32_bf16 v[44:47], v[176:179], v[24:27], v[44:47]\n"
    "s_waitcnt lgkmcnt(2)\n"
    "v_exp_f32 v93, v93\n"
    "v_mfma_f32_16x16x32_bf16 v[60:63], v[160:163], v[24:27], v[108:111]\n"
    "ds_read_b128 v[176:179], v200 offset:43008\n"
    "v_cvt_pk_bf16_f32 v151, v86, v87\n"
    "v_mfma_f32_16x16x32_bf16 v[60:63], v[164:167], v[28:31], v[60:63]\n"
    "v_exp_f32 v94, v94\n"
    "v_mfma_f32_16x16x32_bf16 v[56:59], v[164:167], v[4:7], v[104:107]\n"
    "v_exp_f32 v95, v95\n"
    "v_mfma_f32_16x16x32_bf16 v[56:59], v[160:163], v[0:3], v[56:59]\n"
    "v_exp_f32 v32, v32\n"
    "v_mfma_f32_16x16x32_bf16 v[52:55], v[160:163], a[232:235], v[100:103]\n"
    "ds_read_b128 v[180:183], v200 offset:44032\n"
    "v_cvt_pk_bf16_f32 v154, v88, v89\n"
    "v_mfma_f32_16x16x32_bf16 v[52:55], v[164:167], a[236:239], v[52:55]\n"
    "v_exp_f32 v33, v33\n"
    "v_mfma_f32_16x16x32_bf16 v[48:51], v[164:167], a[212:215], v[96:99]\n"
    "s_waitcnt vmcnt(6)\n"
    "v_exp_f32 v34, v34\n"
    "v_mfma_f32_16x16x32_bf16 v[48:51], v[160:163], a[208:211], v[48:51]\n"
    "s_waitcnt lgkmcnt(2)\n"
    "v_exp_f32 v35, v35\n"
    "v_mfma_f32_16x16x32_bf16 v[48:51], v[168:171], a[200:203], v[48:51]\n"
    "s_barrier\n"
    "v_exp_f32 v36, v36\n"
    "v_mfma_f32_16x16x32_bf16 v[48:51], v[172:175], a[204:207], v[48:51]\n"
    "ds_read_b128 v[160:163], v200\n"
    "v_cvt_pk_bf16_f32 v155, v90, v91\n"
    "v_mfma_f32_16x16x32_bf16 v[52:55], v[172:175], a[228:231], v[52:55]\n"
    "v_exp_f32 v37, v37\n"
    "v_mfma_f32_16x16x32_bf16 v[52:55], v[168:171], a[224:227], v[52:55]\n"
    "v_exp_f32 v38, v38\n"
    "v_mfma_f32_16x16x32_bf16 v[56:59], v[168:171], a[248:251], v[56:59]\n"
    "ds_read_b128 v[164:167], v200 offset:1024\n"
    "v_cvt_pk_bf16_f32 v158, v92, v93\n"
    "v_mfma_f32_16x16x32_bf16 v[56:59], v[172:175], a[252:255], v[56:59]\n"
    "buffer_load_dwordx2 v[218:219], v213, s[44:47], 0 offen sc1\n"
    "v_mfma_f32_16x16x32_bf16 v[60:63], v[172:175], v[20:23], v[60:63]\n"
    "v_exp_f32 v39, v39\n"
    "v_mfma_f32_16x16x32_bf16 v[60:63], v[168:171], v[16:19], v[60:63]\n"
    "s_waitcnt lgkmcnt(2)\n"
    "v_exp_f32 v40, v40\n"
    "v_mfma_f32_16x16x32_bf16 v[60:63], v[176:179], v[8:11], v[60:63]\n"
    "ds_read_b128 v[168:171], v200 offset:2048\n"
    "v_cvt_pk_bf16_f32 v159, v94, v95\n"
    "v_mfma_f32_16x16x32_bf16 v[60:63], v[180:183], v[12:15], v[60:63]\n"
    "v_exp_f32 v41, v41\n"
    "v_mfma_f32_16x16x32_bf16 v[56:59], v[180:183], a[244:247], v[56:59]\n"
    "v_exp_f32 v42, v42\n"
    "v_mfma_f32_16x16x32_bf16 v[56:59], v[176:179], a[240:243], v[56:59]\n"
    "v_exp_f32 v43, v43\n"
    "v_mfma_f32_16x16x32_bf16 v[52:55], v[176:179], a[216:219], v[52:55]\n"
    "ds_read_b128 v[172:175], v200 offset:3072\n"
    "v_cvt_pk_bf16_f32 v32, v32, v33\n"
    "v_mfma_f32_16x16x32_bf16 v[52:55], v[180:183], a[220:223], v[52:55]\n"
    "v_exp_f32 v44, v44\n"
    "v_mfma_f32_16x16x32_bf16 v[48:51], v[180:183], a[196:199], v[48:51]\n"
    "v_exp_f32 v45, v45\n"
    "v_mfma_f32_16x16x32_bf16 v[48:51], v[176:179], a[192:195], v[48:51]\n"
    "s_waitcnt lgkmcnt(2)\n"
    "v_exp_f32 v46, v46\n"
    "v_mfma_f32_16x16x32_bf16 v[64:67], v[160:163], a[192:195], v[96:99]\n"
    "ds_read_b128 v[176:179], v200 offset:4096\n"
    "v_cvt_pk_bf16_f32 v33, v34, v35\n"
    "v_mfma_f32_16x16x32_bf16 v[64:67], v[164:167], a[196:199], v[64:67]\n"
    "v_exp_f32 v47, v47\n"
    "v_mfma_f32_16x16x32_bf16 v[68:71], v[164:167], a[220:223], v[100:103]\n"
    "v_cvt_pk_bf16_f32 v36, v36, v37\n"
    "v_cvt_pk_bf16_f32 v37, v38, v39\n"
    "v_mfma_f32_16x16x32_bf16 v[68:71], v[160:163], a[216:219], v[68:71]\n"
    "v_cvt_pk_bf16_f32 v40, v40, v41\n"
    "v_cvt_pk_bf16_f32 v41, v42, v43\n"
    "v_mfma_f32_16x16x32_bf16 v[72:75], v[160:163], a[240:243], v[104:107]\n"
    "ds_read_b128 v[180:183], v200 offset:5120\n"
    "v_cvt_pk_bf16_f32 v44, v44, v45\n"
    "v_mfma_f32_16x16x32_bf16 v[72:75], v[164:167], a[244:247], v[72:75]\n"
    "v_cvt_pk_bf16_f32 v45, v46, v47\n"
    "v_mov_b32 v119, v117\n"
    "v_mfma_f32_16x16x32_bf16 v[76:79], v[164:167], v[12:15], v[108:111]\n"
    "v_exp_f32 v48, v48\n"
    "v_mfma_f32_16x16x32_bf16 v[76:79], v[160:163], v[8:11], v[76:79]\n"
    "s_waitcnt lgkmcnt(2)\n"
    "v_exp_f32 v49, v49\n"
    "v_mfma_f32_16x16x32_bf16 v[76:79], v[168:171], v[16:19], v[76:79]\n"
    "ds_read_b128 v[160:163], v200 offset:10240\n"
    "v_cvt_pk_bf16_f32 v34, v48, v49\n"
    "v_mfma_f32_16x16x32_bf16 v[76:79], v[172:175], v[20:23], v[76:79]\n"
    "v_exp_f32 v50, v50\n"
    "v_mfma_f32_16x16x32_bf16 v[72:75], v[172:175], a[252:255], v[72:75]\n"
    "v_exp_f32 v51, v51\n"
    "v_mfma_f32_16x16x32_bf16 v[72:75], v[168:171], a[248:251], v[72:75]\n"
    "v_exp_f32 v52, v52\n"
    "v_mfma_f32_16x16x32_bf16 v[68:71], v[168:171], a[224:227], v[68:71]\n"
    "ds_read_b128 v[164:167], v200 offset:11264\n"
    "v_cvt_pk_bf16_f32 v35, v50, v51\n"
    "v_mfma_f32_16x16x32_bf16 v[68:71], v[172:175], a[228:231], v[68:71]\n"
    "v_exp_f32 v53, v53\n"
    "v_mfma_f32_16x16x32_bf16 v[64:67], v[172:175], a[204:207], v[64:67]\n"
    "v_exp_f32 v54, v54\n"
    "v_mfma_f32_16x16x32_bf16 v[64:67], v[168:171], a[200:203], v[64:67]\n"
    "s_waitcnt lgkmcnt(2)\n"
    "v_exp_f32 v55, v55\n"
    "v_mfma_f32_16x16x32_bf16 v[64:67], v[176:179], a[208:211], v[64:67]\n"
    "s_add_u32 s66, s66, s74\n"
    "ds_read_b128 v[168:171], v200 offset:8192\n"
    "v_cvt_pk_bf16_f32 v38, v52, v53\n"
    "v_mfma_f32_16x16x32_bf16 v[64:67], v[180:183], a[212:215], v[64:67]\n"
    "s_add_u32 s60, s72, 0xbd000\n"
    "v_exp_f32 v56, v56\n"
    "v_mfma_f32_16x16x32_bf16 v[68:71], v[180:183], a[236:239], v[68:71]\n"
    "s_add_u32 s61, s71, 0xba000\n"
    "v_exp_f32 v57, v57\n"
    "v_mfma_f32_16x16x32_bf16 v[68:71], v[176:179], a[232:235], v[68:71]\n"
    "v_exp_f32 v58, v58\n"
    "v_mfma_f32_16x16x32_bf16 v[72:75], v[176:179], v[0:3], v[72:75]\n"
    "ds_read_b128 v[172:175], v200 offset:9216\n"
    "v_cvt_pk_bf16_f32 v39, v54, v55\n"
    "v_mfma_f32_16x16x32_bf16 v[72:75], v[180:183], v[4:7], v[72:75]\n"
    "v_exp_f32 v59, v59\n"
    "v_mfma_f32_16x16x32_bf16 v[76:79], v[180:183], v[28:31], v[76:79]\n"
    "s_waitcnt vmcnt(0)\n"
    "v_cmp_ne_u32 s[80:81], s67, v219\n"
    "v_cvt_pk_bf16_f32 v42, v56, v57\n"
    "v_mfma_f32_16x16x32_bf16 v[76:79], v[176:179], v[24:27], v[76:79]\n"
    "s_waitcnt lgkmcnt(2)\n"
    "v_cmp_gt_u32 vcc, s66, v218\n"
    "v_cvt_pk_bf16_f32 v43, v58, v59\n"
    "v_mfma_f32_16x16x32_bf16 v[92:95], v[160:163], v[24:27], v[108:111]\n"
    "ds_read_b128 v[176:179], v200 offset:6144\n"
    "v_mov_b32 v123, v121\n"
    "v_mfma_f32_16x16x32_bf16 v[92:95], v[164:167], v[28:31], v[92:95]\n"
    "s_or_b64 vcc, vcc, s[80:81]\n"
    "v_exp_f32 v64, v64\n"
    "v_mfma_f32_16x16x32_bf16 v[88:91], v[164:167], v[4:7], v[104:107]\n"
    "s_cbranch_vccnz LSPRE5\n"
    "LSPB5:\n"
    "v_exp_f32 v65, v65\n"
    "v_mfma_f32_16x16x32_bf16 v[88:91], v[160:163], v[0:3], v[88:91]\n"
    "v_exp_f32 v66, v66\n"
    "v_mfma_f32_16x16x32_bf16 v[84:87], v[160:163], a[232:235], v[100:103]\n"
    "s_barrier\n"
    "ds_read_b128 v[180:183], v200 offset:7168\n"
    "v_cvt_pk_bf16_f32 v64, v64, v65\n"
    "v_mfma_f32_16x16x32_bf16 v[84:87], v[164:167], a[236:239], v[84:87]\n"
    "s_add_u32 m0, s72, 0x6000\n"
    "v_exp_f32 v67, v67\n"
    "v_mfma_f32_16x16x32_bf16 v[80:83], v[164:167], a[212:215], v[96:99]\n"
    "buffer_load_dwordx4 v200, s[36:39], s60 offen sc1 lds\n"
    "v_mfma_f32_16x16x32_bf16 v[80:83], v[160:163], a[208:211], v[80:83]\n"
    "s_waitcnt lgkmcnt(2)\n"
    "v_exp_f32 v68, v68\n"
    "v_mfma_f32_16x16x32_bf16 v[80:83], v[168:171], a[200:203], v[80:83]\n"
    "ds_read_b128 v[160:163], v200 offset:12288\n"
    "v_cvt_pk_bf16_f32 v65, v66, v67\n"
    "v_mfma_f32_16x16x32_bf16 v[80:83], v[172:175], a[204:207], v[80:83]\n"
    "buffer_load_dwordx4 v200, s[36:39], s60 offen offset:1024 sc1 lds\n"
    "v_mfma_f32_16x16x32_bf16 v[84:87], v[172:175], a[228:231], v[84:87]\n"
    "v_exp_f32 v69, v69\n"
    "v_mfma_f32_16x16x32_bf16 v[84:87], v[168:171], a[224:227], v[84:87]\n"
    "buffer_load_dwordx4 v200, s[36:39], s60 offen offset:2048 sc1 lds\n"
    "v_mfma_f32_16x16x32_bf16 v[88:91], v[168:171], a[248:251], v[88:91]\n"
    "ds_read_b128 v[164:167], v200 offset:13312\n"
    "v_cvt_pk_bf16_f32 v68, v68, v69\n"
    "v_mfma_f32_16x16x32_bf16 v[88:91], v[172:175], a[252:255], v[88:91]\n"
    "v_exp_f32 v60, v60\n"
    "v_mfma_f32_16x16x32_bf16 v[92:95], v[172:175], v[20:23], v[92:95]\n"
    "s_add_u32 m0, s71, 0x18000\n"
    "v_exp_f32 v70, v70\n"
    "v_mfma_f32_16x16x32_bf16 v[92:95], v[168:171], v[16:19], v[92:95]\n"
    "s_waitcnt lgkmcnt(2)\n"
    "buffer_load_dwordx4 v200, s[40:43], s61 offen sc1 lds\n"
    "v_mfma_f32_16x16x32_bf16 v[92:95], v[176:179], v[8:11], v[92:95]\n"
    "ds_read_b128 v[168:171], v200 offset:14336\n"
    "v_mov_b32 v127, v125\n"
    "v_mfma_f32_16x16x32_bf16 v[92:95], v[180:183], v[12:15], v[92:95]\n"
    "v_exp_f32 v61, v61\n"
    "v_mfma_f32_16x16x32_bf16 v[88:91], v[180:183], a[244:247], v[88:91]\n"
    "v_exp_f32 v71, v71\n"
    "v_mfma_f32_16x16x32_bf16 v[88:91], v[176:179], a[240:243], v[88:91]\n"
    "v_exp_f32 v62, v62\n"
    "v_mfma_f32_16x16x32_bf16 v[84:87], v[176:179], a[216:219], v[84:87]\n"
    "ds_read_b128 v[172:175], v200 offset:15360\n"
    "v_cvt_pk_bf16_f32 v46, v60, v61\n"
    "v_mfma_f32_16x16x32_bf16 v[84:87], v[180:183], a[220:223], v[84:87]\n"
    "v_exp_f32 v72, v72\n"
    "v_mfma_f32_16x16x32_bf16 v[80:83], v[180:183], a[196:199], v[80:83]\n"
    "v_exp_f32 v63, v63\n"
    "v_mfma_f32_16x16x32_bf16 v[80:83], v[176:179], a[192:195], v[80:83]\n"
    "s_waitcnt lgkmcnt(2)\n"
    "buffer_load_dwordx4 v200, s[40:43], s61 offen offset:1024 sc1 lds\n"
    "v_mfma_f32_16x16x32_bf16 v[232:235], v[160:163], a[192:195], v[96:99]\n"
    "ds_read_b128 v[176:179], v200 offset:16384\n"
    "v_cvt_pk_bf16_f32 v47, v62, v63\n"
    "v_mfma_f32_16x16x32_bf16 v[232:235], v[164:167], a[196:199], v[232:235]\n"
    "v_exp_f32 v73, v73\n"
    "v_mfma_f32_16x16x32_bf16 v[236:239], v[164:167], a[220:223], v[100:103]\n"
    "v_exp_f32 v74, v74\n"
    "v_mfma_f32_16x16x32_bf16 v[236:239], v[160:163], a[216:219], v[236:239]\n"
    "v_exp_f32 v75, v75\n"
    "v_mfma_f32_16x16x32_bf16 v[240:243], v[160:163], a[240:243], v[104:107]\n"
    "ds_read_b128 v[180:183], v200 offset:17408\n"
    "v_cvt_pk_bf16_f32 v69, v70, v71\n"
    "v_mfma_f32_16x16x32_bf16 v[240:243], v[164:167], a[244:247], v[240:243]\n"
    "v_exp_f32 v76, v76\n"
    "v_mfma_f32_16x16x32_bf16 v[244:247], v[164:167], v[12:15], v[108:111]\n"
    "v_exp_f32 v77, v77\n"
    "v_mfma_f32_16x16x32_bf16 v[244:247], v[160:163], v[8:11], v[244:247]\n"
    "s_waitcnt lgkmcnt(2)\n"
    "buffer_load_dwordx4 v200, s[40:43], s61 offen offset:2048 sc1 lds\n"
    "v_mfma_f32_16x16x32_bf16 v[244:247], v[168:171], v[16:19], v[244:247]\n"
    "ds_read_b128 v[160:163], v200 offset:22528\n"
    "v_cvt_pk_bf16_f32 v72, v72, v73\n"
    "v_mfma_f32_16x16x32_bf16 v[244:247], v[172:175], v[20:23], v[244:247]\n"
    "v_exp_f32 v78, v78\n"
    "v_mfma_f32_16x16x32_bf16 v[240:243], v[172:175], a[252:255], v[240:243]\n"
    "v_exp_f32 v79, v79\n"
    "v_mfma_f32_16x16x32_bf16 v[240:243], v[168:171], a[248:251], v[240:243]\n"
    "v_exp_f32 v80, v80\n"
    "v_mfma_f32_16x16x32_bf16 v[236:239], v[168:171], a[224:227], v[236:239]\n"
    "ds_read_b128 v[164:167], v200 offset:23552\n"
    "v_cvt_pk_bf16_f32 v73, v74, v75\n"
    "v_mfma_f32_16x16x32_bf16 v[236:239], v[172:175], a[228:231], v[236:239]\n"
    "v_exp_f32 v81, v81\n"
    "v_mfma_f32_16x16x32_bf16 v[232:235], v[172:175], a[204:207], v[232:235]\n"
    "v_exp_f32 v82, v82\n"
    "v_mfma_f32_16x16x32_bf16 v[232:235], v[168:171], a[200:203], v[232:235]\n"
    "s_waitcnt lgkmcnt(2)\n"
    "buffer_load_dwordx4 v200, s[40:43], s61 offen offset:3072 sc1 lds\n"
    "v_mfma_f32_16x16x32_bf16 v[232:235], v[176:179], a[208:211], v[232:235]\n"
    "ds_read_b128 v[168:171], v200 offset:20480\n"
    "v_cvt_pk_bf16_f32 v76, v76, v77\n"
    "v_mfma_f32_16x16x32_bf16 v[232:235], v[180:183], a[212:215], v[232:235]\n"
    "v_exp_f32 v83, v83\n"
    "v_mfma_f32_16x16x32_bf16 v[236:239], v[180:183], a[236:239], v[236:239]\n"
    "v_exp_f32 v84, v84\n"
    "v_mfma_f32_16x16x32_bf16 v[236:239], v[176:179], a[232:235], v[236:239]\n"
    "v_exp_f32 v85, v85\n"
    "v_mfma_f32_16x16x32_bf16 v[240:243], v[176:179], v[0:3], v[240:243]\n"
    "ds_read_b128 v[172:175], v200 offset:21504\n"
    "v_cvt_pk_bf16_f32 v77, v78, v79\n"
    "v_mfma_f32_16x16x32_bf16 v[240:243], v[180:183], v[4:7], v[240:243]\n"
    "s_add_u32 s65, s61, 0x1000\n"
    "v_exp_f32 v86, v86\n"
    "v_mfma_f32_16x16x32_bf16 v[244:247], v[180:183], v[28:31], v[244:247]\n"
    "s_add_u32 m0, s71, 0x19000\n"
    "v_exp_f32 v87, v87\n"
    "v_mfma_f32_16x16x32_bf16 v[244:247], v[176:179], v[24:27], v[244:247]\n"
    "s_waitcnt lgkmcnt(2)\n"
    "buffer_load_dwordx4 v200, s[40:43], s65 offen sc1 lds\n"
    "v_mfma_f32_16x16x32_bf16 v[60:63], v[160:163], v[24:27], v[108:111]\n"
    "ds_read_b128 v[176:179], v200 offset:18432\n"
    "v_cvt_pk_bf16_f32 v66, v80, v81\n"
    "v_mfma_f32_16x16x32_bf16 v[60:63], v[164:167], v[28:31], v[60:63]\n"
    "v_exp_f32 v88, v88\n"
    "v_mfma_f32_16x16x32_bf16 v[56:59], v[164:167], v[4:7], v[104:107]\n"
    "v_exp_f32 v89, v89\n"
    "v_mfma_f32_16x16x32_bf16 v[56:59], v[160:163], v[0:3], v[56:59]\n"
    "v_exp_f32 v90, v90\n"
    "v_mfma_f32_16x16x32_bf16 v[52:55], v[160:163], a[232:235], v[100:103]\n"
    "ds_read_b128 v[180:183], v200 offset:19456\n"
    "v_cvt_pk_bf16_f32 v67, v82, v83\n"
    "v_mfma_f32_16x16x32_bf16 v[52:55], v[164:167], a[236:239], v[52:55]\n"
    "v_exp_f32 v91, v91\n"
    "v_mfma_f32_16x16x32_bf16 v[48:51], v[164:167], a[212:215], v[96:99]\n"
    "s_waitcnt vmcnt(5)\n"
    "v_exp_f32 v92, v92\n"
    "v_mfma_f32_16x16x32_bf16 v[48:51], v[160:163], a[208:211], v[48:51]\n"
    "s_waitcnt lgkmcnt(2)\n"
    "buffer_load_dwordx4 v200, s[40:43], s65 offen offset:1024 sc1 lds\n"
    "v_mfma_f32_16x16x32_bf16 v[48:51], v[168:171], a[200:203], v[48:51]\n"
    "s_barrier\n"
    "v_exp_f32 v93, v93\n"
    "v_mfma_f32_16x16x32_bf16 v[48:51], v[172:175], a[204:207], v[48:51]\n"
    "ds_read_b128 v[160:163], v200 offset:24576\n"
    "v_cvt_pk_bf16_f32 v70, v84, v85\n"
    "v_mfma_f32_16x16x32_bf16 v[52:55], v[172:175], a[228:231], v[52:55]\n"
    "v_exp_f32 v94, v94\n"
    "v_mfma_f32_16x16x32_bf16 v[52:55], v[168:171], a[224:227], v[52:55]\n"
    "v_exp_f32 v95, v95\n"
    "v_mfma_f32_16x16x32_bf16 v[56:59], v[168:171], a[248:251], v[56:59]\n"
    "ds_read_b128 v[164:167], v200 offset:25600\n"
    "v_cvt_pk_bf16_f32 v71, v86, v87\n"
    "v_mfma_f32_16x16x32_bf16 v[56:59], v[172:175], a[252:255], v[56:59]\n"
    "v_exp_f32 v232, v232\n"
    "v_mfma_f32_16x16x32_bf16 v[60:63], v[172:175], v[20:23], v[60:63]\n"
    "v_exp_f32 v233, v233\n"
    "v_mfma_f32_16x16x32_bf16 v[60:63], v[168:171], v[16:19], v[60:63]\n"
    "s_waitcnt lgkmcnt(2)\n"
    "v_exp_f32 v234, v234\n"
    "v_mfma_f32_16x16x32_bf16 v[60:63], v[176:179], v[8:11], v[60:63]\n"
    "s_lshl_b32 s76, s56, 3\n"
    "ds_read_b128 v[168:171], v200 offset:26624\n"
    "v_cvt_pk_bf16_f32 v74, v88, v89\n"
    "v_mfma_f32_16x16x32_bf16 v[60:63], v[180:183], v[12:15], v[60:63]\n"
    "s_add_u32 s76, s76, s57\n"
    "v_exp_f32 v235, v235\n"
    "v_mfma_f32_16x16x32_bf16 v[56:59], v[180:183], a[244:247], v[56:59]\n"
    "s_mul_i32 s76, s76, 0x18000\n"
    "v_exp_f32 v236, v236\n"
    "v_mfma_f32_16x16x32_bf16 v[56:59], v[176:179], a[240:243], v[56:59]\n"
    "s_add_u32 s76, s76, s88\n"
    "v_exp_f32 v237, v237\n"
    "v_mfma_f32_16x16x32_bf16 v[52:55], v[176:179], a[216:219], v[52:55]\n"
    "s_add_u32 s28, s24, s76\n"
    "ds_read_b128 v[172:175], v200 offset:27648\n"
    "v_cvt_pk_bf16_f32 v75, v90, v91\n"
    "v_mfma_f32_16x16x32_bf16 v[52:55], v[180:183], a[220:223], v[52:55]\n"
    "s_addc_u32 s29, s25, 0\n"
    "v_exp_f32 v238, v238\n"
    "v_mfma_f32_16x16x32_bf16 v[48:51], v[180:183], a[196:199], v[48:51]\n"
    "s_and_b32 s29, s29, 0xffff\n"
    "v_exp_f32 v239, v239\n"
    "v_mfma_f32_16x16x32_bf16 v[48:51], v[176:179], a[192:195], v[48:51]\n"
    "s_waitcnt lgkmcnt(2)\n"
    "v_exp_f32 v240, v240\n"
    "v_mfma_f32_16x16x32_bf16 v[80:83], v[160:163], a[192:195], v[96:99]\n"
    "s_mov_b32 s30, 0x6000\n"
    "ds_read_b128 v[176:179], v200 offset:28672\n"
    "v_cvt_pk_bf16_f32 v78, v92, v93\n"
    "v_mfma_f32_16x16x32_bf16 v[80:83], v[164:167], a[196:199], v[80:83]\n"
    "s_mov_b32 s31, 0x20000\n"
    "v_cvt_pk_bf16_f32 v79, v94, v95\n"
    "v_permlane16_swap_b32 v113, v115\n"
    "v_mfma_f32_16x16x32_bf16 v[84:87], v[164:167], a[220:223], v[100:103]\n"
    "s_xor_b32 s94, s70, 0x80000000\n"
    "v_exp_f32 v241, v241\n"
    "v_mfma_f32_16x16x32_bf16 v[84:87], v[160:163], a[216:219], v[84:87]\n"
    "s_xor_b32 s95, s70, 0x80000000\n"
    "v_exp_f32 v242, v242\n"
    "v_mfma_f32_16x16x32_bf16 v[88:91], v[160:163], a[240:243], v[104:107]\n"
    "s_mov_b32 s62, 0x1800\n"
    "ds_read_b128 v[180:183], v200 offset:29696\n"
    "v_permlane16_swap_b32 v117, v119\n"
    "v_mfma_f32_16x16x32_bf16 v[88:91], v[164:167], a[244:247], v[88:91]\n"
    "s_mov_b32 s63, 0x3000\n"
    "v_exp_f32 v243, v243\n"
    "v_mfma_f32_16x16x32_bf16 v[92:95], v[164:167], v[12:15], v[108:111]\n"
    "s_mov_b32 s64, 0x4800\n"
    "v_exp_f32 v244, v244\n"
    "v_mfma_f32_16x16x32_bf16 v[92:95], v[160:163], v[8:11], v[92:95]\n"
    "s_waitcnt lgkmcnt(2)\n"
    "v_exp_f32 v245, v245\n"
    "v_mfma_f32_16x16x32_bf16 v[92:95], v[168:171], v[16:19], v[92:95]\n"
    "ds_read_b128 v[160:163], v200 offset:34816\n"
    "v_permlane16_swap_b32 v121, v123\n"
    "v_mfma_f32_16x16x32_bf16 v[92:95], v[172:175], v[20:23], v[92:95]\n"
    "v_exp_f32 v246, v246\n"
    "v_mfma_f32_16x16x32_bf16 v[88:91], v[172:175], a[252:255], v[88:91]\n"
    "v_exp_f32 v247, v247\n"
    "v_mfma_f32_16x16x32_bf16 v[88:91], v[168:171], a[248:251], v[88:91]\n"
    "v_exp_f32 v48, v48\n"
    "v_mfma_f32_16x16x32_bf16 v[84:87], v[168:171], a[224:227], v[84:87]\n"
    "ds_read_b128 v[164:167], v200 offset:35840\n"
    "v_permlane16_swap_b32 v125, v127\n"
    "v_mfma_f32_16x16x32_bf16 v[84:87], v[172:175], a[228:231], v[84:87]\n"
    "v_exp_f32 v49, v49\n"
    "v_mfma_f32_16x16x32_bf16 v[80:83], v[172:175], a[204:207], v[80:83]\n"
    "v_exp_f32 v50, v50\n"
    "v_mfma_f32_16x16x32_bf16 v[80:83], v[168:171], a[200:203], v[80:83]\n"
    "s_waitcnt lgkmcnt(2)\n"
    "v_exp_f32 v51, v51\n"
    "v_mfma_f32_16x16x32_bf16 v[80:83], v[176:179], a[208:211], v[80:83]\n"
    "ds_read_b128 v[168:171], v200 offset:32768\n"
    "v_add_f32 v113, v113, v115\n"
    "v_mfma_f32_16x16x32_bf16 v[80:83], v[180:183], a[212:215], v[80:83]\n"
    "v_exp_f32 v52, v52\n"
    "v_mfma_f32_16x16x32_bf16 v[84:87], v[180:183], a[236:239], v[84:87]\n"
    "v_exp_f32 v53, v53\n"
    "v_mfma_f32_16x16x32_bf16 v[84:87], v[176:179], a[232:235], v[84:87]\n"
    "v_exp_f32 v54, v54\n"
    "v_mfma_f32_16x16x32_bf16 v[88:91], v[176:179], v[0:3], v[88:91]\n"
    "ds_read_b128 v[172:175], v200 offset:33792\n"
    "v_add_f32 v117, v117, v119\n"
    "v_mfma_f32_16x16x32_bf16 v[88:91], v[180:183], v[4:7], v[88:91]\n"
    "v_add_f32 v121, v121, v123\n"
    "v_add_f32 v125, v125, v127\n"
    "v_mfma_f32_16x16x32_bf16 v[92:95], v[180:183], v[28:31], v[92:95]\n"
    "v_exp_f32 v55, v55\n"
    "v_mfma_f32_16x16x32_bf16 v[92:95], v[176:179], v[24:27], v[92:95]\n"
    "s_waitcnt lgkmcnt(2)\n"
    "v_add_f32 v112, v112, v113\n"
    "v_add_f32 v116, v116, v117\n"
    "v_mfma_f32_16x16x32_bf16 v[252:255], v[160:163], v[24:27], v[108:111]\n"
    "ds_read_b128 v[176:179], v200 offset:30720\n"
    "v_add_f32 v120, v120, v121\n"
    "v_mfma_f32_16x16x32_bf16 v[252:255], v[164:167], v[28:31], v[252:255]\n"
    "v_add_f32 v124, v124, v125\n"
    "v_and_b32 v198, 15, v231\n"
    "v_mfma_f32_16x16x32_bf16 v[248:251], v[164:167], v[4:7], v[104:107]\n"
    "v_mul_u32_u24 v205, 0x180, v198\n"
    "v_lshrrev_b32 v199, 4, v231\n"
    "v_mfma_f32_16x16x32_bf16 v[248:251], v[160:163], v[0:3], v[248:251]\n"
    "v_lshl_add_u32 v205, v199, 4, v205\n"
    "v_add_u32 v207, 0x18000, v200\n"
    "v_mfma_f32_16x16x32_bf16 v[188:191], v[160:163], a[232:235], v[100:103]\n"
    "ds_read_b128 v[180:183], v200 offset:31744\n"
    "v_cvt_pk_bf16_f32 v232, v232, v233\n"
    "v_mfma_f32_16x16x32_bf16 v[188:191], v[164:167], a[236:239], v[188:191]\n"
    "v_exp_f32 v56, v56\n"
    "v_mfma_f32_16x16x32_bf16 v[184:187], v[164:167], a[212:215], v[96:99]\n"
    "v_exp_f32 v57, v57\n"
    "v_mfma_f32_16x16x32_bf16 v[184:187], v[160:163], a[208:211], v[184:187]\n"
    "s_waitcnt lgkmcnt(2)\n"
    "v_exp_f32 v58, v58\n"
    "v_mfma_f32_16x16x32_bf16 v[184:187], v[168:171], a[200:203], v[184:187]\n"
    "ds_read_b128 v[160:163], v201\n"
    "v_cvt_pk_bf16_f32 v233, v234, v235\n"
    "v_mfma_f32_16x16x32_bf16 v[184:187], v[172:175], a[204:207], v[184:187]\n"
    "v_exp_f32 v59, v59\n"
    "v_mfma_f32_16x16x32_bf16 v[188:191], v[172:175], a[228:231], v[188:191]\n"
    "v_exp_f32 v60, v60\n"
    "v_mfma_f32_16x16x32_bf16 v[188:191], v[168:171], a[224:227], v[188:191]\n"
    "v_exp_f32 v61, v61\n"
    "v_mfma_f32_16x16x32_bf16 v[248:251], v[168:171], a[248:251], v[248:251]\n"
    "ds_read_b128 v[164:167], v201 offset:1024\n"
    "v_cvt_pk_bf16_f32 v236, v236, v237\n"
    "v_mfma_f32_16x16x32_bf16 v[248:251], v[172:175], a[252:255], v[248:251]\n"
    "v_exp_f32 v62, v62\n"
    "v_mfma_f32_16x16x32_bf16 v[252:255], v[172:175], v[20:23], v[252:255]\n"
    "v_exp_f32 v63, v63\n"
    "v_mfma_f32_16x16x32_bf16 v[252:255], v[168:171], v[16:19], v[252:255]\n"
    "s_waitcnt lgkmcnt(2)\n"
    "v_exp_f32 v80, v80\n"
    "v_mfma_f32_16x16x32_bf16 v[252:255], v[176:179], v[8:11], v[252:255]\n"
    "ds_read_b128 v[168:171], v201 offset:24576\n"
    "v_cvt_pk_bf16_f32 v237, v238, v239\n"
    "v_mfma_f32_16x16x32_bf16 v[252:255], v[180:183], v[12:15], v[252:255]\n"
    "v_exp_f32 v81, v81\n"
    "v_mfma_f32_16x16x32_bf16 v[248:251], v[180:183], a[244:247], v[248:251]\n"
    "v_exp_f32 v82, v82\n"
    "v_mfma_f32_16x16x32_bf16 v[248:251], v[176:179], a[240:243], v[248:251]\n"
    "v_exp_f32 v83, v83\n"
    "v_mfma_f32_16x16x32_bf16 v[188:191], v[176:179], a[216:219], v[188:191]\n"
    "ds_read_b128 v[172:175], v201 offset:25600\n"
    "v_cvt_pk_bf16_f32 v240, v240, v241\n"
    "v_mfma_f32_16x16x32_bf16 v[188:191], v[180:183], a[220:223], v[188:191]\n"
    "v_exp_f32 v84, v84\n"
    "v_mfma_f32_16x16x32_bf16 v[184:187], v[180:183], a[196:199], v[184:187]\n"
    "v_exp_f32 v85, v85\n"
    "v_mfma_f32_16x16x32_bf16 v[184:187], v[176:179], a[192:195], v[184:187]\n"
    "s_waitcnt lgkmcnt(2)\n"
    "v_exp_f32 v86, v86\n"
    "v_mfma_f32_16x16x32_bf16 a[0:3], v[160:163], v[128:131], a[0:3]\n"
    "ds_read_b128 v[176:179], v201 offset:2048\n"
    "v_cvt_pk_bf16_f32 v241, v242, v243\n"
    "v_mfma_f32_16x16x32_bf16 a[0:3], v[164:167], v[144:147], a[0:3]\n"
    "v_exp_f32 v87, v87\n"
    "v_mfma_f32_16x16x32_bf16 a[4:7], v[160:163], v[132:135], a[4:7]\n"
    "v_exp_f32 v88, v88\n"
    "v_mfma_f32_16x16x32_bf16 a[4:7], v[164:167], v[148:151], a[4:7]\n"
    "v_exp_f32 v89, v89\n"
    "v_mfma_f32_16x16x32_bf16 a[8:11], v[160:163], v[136:139], a[8:11]\n"
    "ds_read_b128 v[180:183], v201 offset:3072\n"
    "v_cvt_pk_bf16_f32 v244, v244, v245\n"
    "v_mfma_f32_16x16x32_bf16 a[8:11], v[164:167], v[152:155], a[8:11]\n"
    "v_cvt_pk_bf16_f32 v245, v246, v247\n"
    "v_cvt_pk_bf16_f32 v234, v48, v49\n"
    "v_mfma_f32_16x16x32_bf16 a[12:15], v[160:163], v[140:143], a[12:15]\n"
    "v_cvt_pk_bf16_f32 v235, v50, v51\n"
    "v_cvt_pk_bf16_f32 v238, v52, v53\n"
    "v_mfma_f32_16x16x32_bf16 a[12:15], v[164:167], v[156:159], a[12:15]\n"
    "v_cvt_pk_bf16_f32 v239, v54, v55\n"
    "v_cvt_pk_bf16_f32 v242, v56, v57\n"
    "v_mfma_f32_16x16x32_bf16 v[112:115], v[192:195], v[144:147], v[112:115]\n"
    "v_exp_f32 v90, v90\n"
    "v_mfma_f32_16x16x32_bf16 v[112:115], v[192:195], v[32:35], v[112:115]\n"
    "v_cvt_pk_bf16_f32 v243, v58, v59\n"
    "v_cvt_pk_bf16_f32 v246, v60, v61\n"
    "v_mfma_f32_16x16x32_bf16 v[112:115], v[192:195], v[64:67], v[112:115]\n"
    "v_cvt_pk_bf16_f32 v247, v62, v63\n"
    "v_cvt_pk_bf16_f32 v80, v80, v81\n"
    "v_mfma_f32_16x16x32_bf16 v[116:119], v[192:195], v[148:151], v[116:119]\n"
    "v_exp_f32 v91, v91\n"
    "v_mfma_f32_16x16x32_bf16 v[116:119], v[192:195], v[36:39], v[116:119]\n"
    "v_exp_f32 v92, v92\n"
    "v_mfma_f32_16x16x32_bf16 v[116:119], v[192:195], v[68:71], v[116:119]\n"
    "s_waitcnt lgkmcnt(2)\n"
    "v_exp_f32 v93, v93\n"
    "v_mfma_f32_16x16x32_bf16 a[0:3], v[168:171], v[32:35], a[0:3]\n"
    "ds_read_b128 v[160:163], v201 offset:26624\n"
    "v_cvt_pk_bf16_f32 v81, v82, v83\n"
    "v_mfma_f32_16x16x32_bf16 a[0:3], v[172:175], v[64:67], a[0:3]\n"
    "v_exp_f32 v94, v94\n"
    "v_mfma_f32_16x16x32_bf16 a[4:7], v[168:171], v[36:39], a[4:7]\n"
    "v_exp_f32 v95, v95\n"
    "v_mfma_f32_16x16x32_bf16 a[4:7], v[172:175], v[68:71], a[4:7]\n"
    "v_exp_f32 v184, v184\n"
    "v_mfma_f32_16x16x32_bf16 a[8:11], v[168:171], v[40:43], a[8:11]\n"
    "ds_read_b128 v[164:167], v201 offset:27648\n"
    "v_cvt_pk_bf16_f32 v84, v84, v85\n"
    "v_mfma_f32_16x16x32_bf16 a[8:11], v[172:175], v[72:75], a[8:11]\n"
    "v_exp_f32 v185, v185\n"
    "v_mfma_f32_16x16x32_bf16 a[12:15], v[168:171], v[44:47], a[12:15]\n"
    "v_exp_f32 v186, v186\n"
    "v_mfma_f32_16x16x32_bf16 a[12:15], v[172:175], v[76:79], a[12:15]\n"
    "v_exp_f32 v187, v187\n"
    "v_mfma_f32_16x16x32_bf16 v[120:123], v[192:195], v[152:155], v[120:123]\n"
    "v_exp_f32 v188, v188\n"
    "v_mfma_f32_16x16x32_bf16 v[120:123], v[192:195], v[40:43], v[120:123]\n"
    "v_exp_f32 v189, v189\n"
    "v_mfma_f32_16x16x32_bf16 v[120:123], v[192:195], v[72:75], v[120:123]\n"
    "v_exp_f32 v190, v190\n"
    "v_mfma_f32_16x16x32_bf16 v[124:127], v[192:195], v[156:159], v[124:127]\n"
    "v_exp_f32 v191, v191\n"
    "v_mfma_f32_16x16x32_bf16 v[124:127], v[192:195], v[44:47], v[124:127]\n"
    "s_waitcnt vmcnt(0)\n"
    "v_exp_f32 v248, v248\n"
    "v_mfma_f32_16x16x32_bf16 v[124:127], v[192:195], v[76:79], v[124:127]\n"
    "s_waitcnt lgkmcnt(2)\n"
    "v_exp_f32 v249, v249\n"
    "v_mfma_f32_16x16x32_bf16 a[16:19], v[176:179], v[128:131], a[16:19]\n"
    "s_barrier\n"
    "v_exp_f32 v250, v250\n"
    "v_mfma_f32_16x16x32_bf16 a[16:19], v[180:183], v[144:147], a[16:19]\n"
    "ds_read_b128 v[168:171], v207\n"
    "v_cvt_pk_bf16_f32 v85, v86, v87\n"
    "v_mfma_f32_16x16x32_bf16 a[20:23], v[176:179], v[132:135], a[20:23]\n"
    "v_exp_f32 v251, v251\n"
    "v_mfma_f32_16x16x32_bf16 a[20:23], v[180:183], v[148:151], a[20:23]\n"
    "v_exp_f32 v252, v252\n"
    "v_mfma_f32_16x16x32_bf16 a[24:27], v[176:179], v[136:139], a[24:27]\n"
    "ds_read_b128 v[172:175], v207 offset:1024\n"
    "v_cvt_pk_bf16_f32 v88, v88, v89\n"
    "v_mfma_f32_16x16x32_bf16 a[24:27], v[180:183], v[152:155], a[24:27]\n"
    "v_exp_f32 v253, v253\n"
    "v_mfma_f32_16x16x32_bf16 a[28:31], v[176:179], v[140:143], a[28:31]\n"
    "v_exp_f32 v254, v254\n"
    "v_mfma_f32_16x16x32_bf16 a[28:31], v[180:183], v[156:159], a[28:31]\n"
    "s_waitcnt lgkmcnt(2)\n"
    "v_cvt_pk_bf16_f32 v89, v90, v91\n"
    "v_cvt_pk_bf16_f32 v92, v92, v93\n"
    "v_mfma_f32_16x16x32_bf16 a[16:19], v[160:163], v[32:35], a[16:19]\n"
    "ds_read_b128 v[176:179], v207 offset:2048\n"
    "v_cvt_pk_bf16_f32 v93, v94, v95\n"
    "v_mfma_f32_16x16x32_bf16 a[16:19], v[164:167], v[64:67], a[16:19]\n"
    "ds_read_b128 v[180:183], v207 offset:3072\n"
    "v_cvt_pk_bf16_f32 v82, v184, v185\n"
    "v_mfma_f32_16x16x32_bf16 a[20:23], v[160:163], v[36:39], a[20:23]\n"
    "v_cvt_pk_bf16_f32 v83, v186, v187\n"
    "v_cvt_pk_bf16_f32 v86, v188, v189\n"
    "v_mfma_f32_16x16x32_bf16 a[20:23], v[164:167], v[68:71], a[20:23]\n"
    "v_exp_f32 v255, v255\n"
    "v_mfma_f32_16x16x32_bf16 a[24:27], v[160:163], v[40:43], a[24:27]\n"
    "v_cvt_pk_bf16_f32 v87, v190, v191\n"
    "v_cvt_pk_bf16_f32 v90, v248, v249\n"
    "v_mfma_f32_16x16x32_bf16 a[24:27], v[164:167], v[72:75], a[24:27]\n"
    "v_cvt_pk_bf16_f32 v91, v250, v251\n"
    "v_cvt_pk_bf16_f32 v94, v252, v253\n"
    "v_mfma_f32_16x16x32_bf16 a[28:31], v[160:163], v[44:47], a[28:31]\n"
    "v_cvt_pk_bf16_f32 v95, v254, v255\n"
    "v_mfma_f32_16x16x32_bf16 a[28:31], v[164:167], v[76:79], a[28:31]\n"
    "v_mfma_f32_16x16x32_bf16 v[112:115], v[192:195], v[232:235], v[112:115]\n"
    "ds_read_b128 v[160:163], v201 offset:4096\n"
    "v_mfma_f32_16x16x32_bf16 v[112:115], v[192:195], v[80:83], v[112:115]\n"
    "s_waitcnt lgkmcnt(3)\n"
    "v_mfma_f32_16x16x32_bf16 a[0:3], v[168:171], v[232:235], a[0:3]\n"
    "v_mfma_f32_16x16x32_bf16 a[0:3], v[172:175], v[80:83], a[0:3]\n"
    "s_waitcnt lgkmcnt(1)\n"
    "v_mfma_f32_16x16x32_bf16 a[16:19], v[176:179], v[232:235], a[16:19]\n"
    "ds_read_b128 v[164:167], v201 offset:5120\n"
    "v_mfma_f32_16x16x32_bf16 a[16:19], v[180:183], v[80:83], a[16:19]\n"
    "v_mfma_f32_16x16x32_bf16 v[116:119], v[192:195], v[236:239], v[116:119]\n"
    "v_mul_f32 v222, s68, v112\n"
    "v_rcp_f32 v222, v222\n"
    "v_mfma_f32_16x16x32_bf16 v[116:119], v[192:195], v[84:87], v[116:119]\n"
    "v_accvgpr_read_b32 v96, a0\n"
    "v_accvgpr_read_b32 v97, a1\n"
    "v_accvgpr_read_b32 v98, a2\n"
    "v_mfma_f32_16x16x32_bf16 a[4:7], v[168:171], v[236:239], a[4:7]\n"
    "v_accvgpr_read_b32 v99, a3\n"
    "v_fma_f32 v96, v96, v222, s94\n"
    "v_fma_f32 v97, v97, v222, s94\n"
    "v_mfma_f32_16x16x32_bf16 a[4:7], v[172:175], v[84:87], a[4:7]\n"
    "v_fma_f32 v98, v98, v222, s94\n"
    "v_fma_f32 v99, v99, v222, s94\n"
    "v_cvt_pk_bf16_f32 v48, v96, v97\n"
    "v_mfma_f32_16x16x32_bf16 a[20:23], v[176:179], v[236:239], a[20:23]\n"
    "v_cvt_pk_bf16_f32 v49, v98, v99\n"
    "v_accvgpr_read_b32 v100, a16\n"
    "v_accvgpr_read_b32 v101, a17\n"
    "v_mfma_f32_16x16x32_bf16 a[20:23], v[180:183], v[84:87], a[20:23]\n"
    "v_accvgpr_read_b32 v102, a18\n"
    "v_accvgpr_read_b32 v103, a19\n"
    "v_fma_f32 v100, v100, v222, s94\n"
    "v_mfma_f32_16x16x32_bf16 v[120:123], v[192:195], v[240:243], v[120:123]\n"
    "v_fma_f32 v101, v101, v222, s94\n"
    "v_fma_f32 v102, v102, v222, s94\n"
    "v_fma_f32 v103, v103, v222, s94\n"
    "v_mfma_f32_16x16x32_bf16 v[120:123], v[192:195], v[88:91], v[120:123]\n"
    "v_cvt_pk_bf16_f32 v50, v100, v101\n"
    "v_cvt_pk_bf16_f32 v51, v102, v103\n"
    "v_mfma_f32_16x16x32_bf16 a[8:11], v[168:171], v[240:243], a[8:11]\n"
    "buffer_store_dwordx4 v[48:51], v205, s[28:31], 0 offen sc0 sc1\n"
    "v_mul_f32 v224, s68, v116\n"
    "v_rcp_f32 v224, v224\n"
    "v_accvgpr_read_b32 v104, a4\n"
    "v_mfma_f32_16x16x32_bf16 a[8:11], v[172:175], v[88:91], a[8:11]\n"
    "v_accvgpr_read_b32 v105, a5\n"
    "v_accvgpr_read_b32 v106, a6\n"
    "v_accvgpr_read_b32 v107, a7\n"
    "v_mfma_f32_16x16x32_bf16 a[24:27], v[176:179], v[240:243], a[24:27]\n"
    "v_fma_f32 v104, v104, v224, s94\n"
    "v_fma_f32 v105, v105, v224, s94\n"
    "v_fma_f32 v106, v106, v224, s94\n"
    "v_mfma_f32_16x16x32_bf16 a[24:27], v[180:183], v[88:91], a[24:27]\n"
    "v_fma_f32 v107, v107, v224, s94\n"
    "v_cvt_pk_bf16_f32 v52, v104, v105\n"
    "v_cvt_pk_bf16_f32 v53, v106, v107\n"
    "v_mfma_f32_16x16x32_bf16 v[124:127], v[192:195], v[244:247], v[124:127]\n"
    "v_accvgpr_read_b32 v108, a20\n"
    "v_accvgpr_read_b32 v109, a21\n"
    "v_accvgpr_read_b32 v110, a22\n"
    "v_mfma_f32_16x16x32_bf16 v[124:127], v[192:195], v[92:95], v[124:127]\n"
    "v_accvgpr_read_b32 v111, a23\n"
    "v_fma_f32 v108, v108, v224, s94\n"
    "v_fma_f32 v109, v109, v224, s94\n"
    "v_mfma_f32_16x16x32_bf16 a[12:15], v[168:171], v[244:247], a[12:15]\n"
    "v_fma_f32 v110, v110, v224, s94\n"
    "v_fma_f32 v111, v111, v224, s94\n"
    "v_mfma_f32_16x16x32_bf16 a[12:15], v[172:175], v[92:95], a[12:15]\n"
    "v_cvt_pk_bf16_f32 v54, v108, v109\n"
    "v_cvt_pk_bf16_f32 v55, v110, v111\n"
    "v_mfma_f32_16x16x32_bf16 a[28:31], v[176:179], v[244:247], a[28:31]\n"
    "ds_read_b128 v[168:171], v201 offset:28672\n"
    "v_mfma_f32_16x16x32_bf16 a[28:31], v[180:183], v[92:95], a[28:31]\n"
    "s_waitcnt lgkmcnt(1)\n"
    "ds_read_b128 v[172:175], v201 offset:29696\n"
    "v_mfma_f32_16x16x32_bf16 a[32:35], v[160:163], v[128:131], a[32:35]\n"
    "ds_read_b128 v[176:179], v207 offset:4096\n"
    "v_mfma_f32_16x16x32_bf16 a[32:35], v[164:167], v[144:147], a[32:35]\n"
    "buffer_store_dwordx4 v[52:55], v205, s[28:31], s62 offen sc0 sc1\n"
    "v_mul_f32 v226, s68, v120\n"
    "v_rcp_f32 v226, v226\n"
    "v_mfma_f32_16x16x32_bf16 a[36:39], v[160:163], v[132:135], a[36:39]\n"
    "v_accvgpr_read_b32 v96, a8\n"
    "v_accvgpr_read_b32 v97, a9\n"
    "v_mfma_f32_16x16x32_bf16 a[36:39], v[164:167], v[148:151], a[36:39]\n"
    "v_accvgpr_read_b32 v98, a10\n"
    "v_accvgpr_read_b32 v99, a11\n"
    "v_mfma_f32_16x16x32_bf16 a[40:43], v[160:163], v[136:139], a[40:43]\n"
    "ds_read_b128 v[180:183], v207 offset:5120\n"
    "v_fma_f32 v96, v96, v226, s94\n"
    "v_mfma_f32_16x16x32_bf16 a[40:43], v[164:167], v[152:155], a[40:43]\n"
    "v_fma_f32 v97, v97, v226, s94\n"
    "v_fma_f32 v98, v98, v226, s94\n"
    "v_mfma_f32_16x16x32_bf16 a[44:47], v[160:163], v[140:143], a[44:47]\n"
    "v_fma_f32 v99, v99, v226, s94\n"
    "v_cvt_pk_bf16_f32 v56, v96, v97\n"
    "v_mfma_f32_16x16x32_bf16 a[44:47], v[164:167], v[156:159], a[44:47]\n"
    "s_waitcnt lgkmcnt(2)\n"
    "v_cvt_pk_bf16_f32 v57, v98, v99\n"
    "v_accvgpr_read_b32 v100, a24\n"
    "v_mfma_f32_16x16x32_bf16 a[44:47], v[168:171], v[44:47], a[44:47]\n"
    "ds_read_b128 v[160:163], v201 offset:6144\n"
    "v_accvgpr_read_b32 v101, a25\n"
    "v_mfma_f32_16x16x32_bf16 a[44:47], v[172:175], v[76:79], a[44:47]\n"
    "v_accvgpr_read_b32 v102, a26\n"
    "v_accvgpr_read_b32 v103, a27\n"
    "v_mfma_f32_16x16x32_bf16 a[40:43], v[168:171], v[40:43], a[40:43]\n"
    "v_fma_f32 v100, v100, v226, s94\n"
    "v_fma_f32 v101, v101, v226, s94\n"
    "v_mfma_f32_16x16x32_bf16 a[40:43], v[172:175], v[72:75], a[40:43]\n"
    "v_fma_f32 v102, v102, v226, s94\n"
    "v_fma_f32 v103, v103, v226, s94\n"
    "v_mfma_f32_16x16x32_bf16 a[36:39], v[168:171], v[36:39], a[36:39]\n"
    "ds_read_b128 v[164:167], v201 offset:7168\n"
    "v_cvt_pk_bf16_f32 v58, v100, v101\n"
    "v_mfma_f32_16x16x32_bf16 a[36:39], v[172:175], v[68:71], a[36:39]\n"
    "v_cvt_pk_bf16_f32 v59, v102, v103\n"
    "v_mfma_f32_16x16x32_bf16 a[32:35], v[168:171], v[32:35], a[32:35]\n"
    "buffer_store_dwordx4 v[56:59], v205, s[28:31], s63 offen sc0 sc1\n"
    "v_mul_f32 v228, s68, v124\n"
    "v_rcp_f32 v228, v228\n"
    "v_mfma_f32_16x16x32_bf16 a[32:35], v[172:175], v[64:67], a[32:35]\n"
    "s_waitcnt lgkmcnt(2)\n"
    "v_accvgpr_read_b32 v104, a12\n"
    "v_accvgpr_read_b32 v105, a13\n"
    "v_mfma_f32_16x16x32_bf16 a[32:35], v[176:179], v[232:235], a[32:35]\n"
    "ds_read_b128 v[168:171], v201 offset:30720\n"
    "v_accvgpr_read_b32 v106, a14\n"
    "v_mfma_f32_16x16x32_bf16 a[32:35], v[180:183], v[80:83], a[32:35]\n"
    "v_accvgpr_read_b32 v107, a15\n"
    "v_fma_f32 v104, v104, v228, s94\n"
    "v_mfma_f32_16x16x32_bf16 a[36:39], v[176:179], v[236:239], a[36:39]\n"
    "v_fma_f32 v105, v105, v228, s94\n"
    "v_fma_f32 v106, v106, v228, s94\n"
    "v_mfma_f32_16x16x32_bf16 a[36:39], v[180:183], v[84:87], a[36:39]\n"
    "v_fma_f32 v107, v107, v228, s94\n"
    "v_cvt_pk_bf16_f32 v60, v104, v105\n"
    "v_mfma_f32_16x16x32_bf16 a[40:43], v[176:179], v[240:243], a[40:43]\n"
    "ds_read_b128 v[172:175], v201 offset:31744\n"
    "v_cvt_pk_bf16_f32 v61, v106, v107\n"
    "v_mfma_f32_16x16x32_bf16 a[40:43], v[180:183], v[88:91], a[40:43]\n"
    "v_accvgpr_read_b32 v108, a28\n"
    "v_accvgpr_read_b32 v109, a29\n"
    "v_mfma_f32_16x16x32_bf16 a[44:47], v[176:179], v[244:247], a[44:47]\n"
    "v_accvgpr_read_b32 v110, a30\n"
    "v_accvgpr_read_b32 v111, a31\n"
    "v_mfma_f32_16x16x32_bf16 a[44:47], v[180:183], v[92:95], a[44:47]\n"
    "s_waitcnt lgkmcnt(2)\n"
    "v_fma_f32 v108, v108, v228, s94\n"
    "v_fma_f32 v109, v109, v228, s94\n"
    "v_mfma_f32_16x16x32_bf16 a[48:51], v[160:163], v[128:131], a[48:51]\n"
    "ds_read_b128 v[176:179], v207 offset:6144\n"
    "v_fma_f32 v110, v110, v228, s94\n"
    "v_mfma_f32_16x16x32_bf16 a[48:51], v[164:167], v[144:147], a[48:51]\n"
    "v_fma_f32 v111, v111, v228, s94\n"
    "v_cvt_pk_bf16_f32 v62, v108, v109\n"
    "v_mfma_f32_16x16x32_bf16 a[52:55], v[160:163], v[132:135], a[52:55]\n"
    "v_cvt_pk_bf16_f32 v63, v110, v111\n"
    "v_mfma_f32_16x16x32_bf16 a[52:55], v[164:167], v[148:151], a[52:55]\n"
    "buffer_store_dwordx4 v[60:63], v205, s[28:31], s64 offen sc0 sc1\n"
    "v_accvgpr_read_b32 v96, a32\n"
    "v_accvgpr_read_b32 v97, a33\n"
    "v_accvgpr_read_b32 v98, a34\n"
    "v_mfma_f32_16x16x32_bf16 a[56:59], v[160:163], v[136:139], a[56:59]\n"
    "ds_read_b128 v[180:183], v207 offset:7168\n"
    "v_accvgpr_read_b32 v99, a35\n"
    "v_mfma_f32_16x16x32_bf16 a[56:59], v[164:167], v[152:155], a[56:59]\n"
    "v_fma_f32 v96, v96, v222, s94\n"
    "v_fma_f32 v97, v97, v222, s94\n"
    "v_mfma_f32_16x16x32_bf16 a[60:63], v[160:163], v[140:143], a[60:63]\n"
    "v_fma_f32 v98, v98, v222, s94\n"
    "v_fma_f32 v99, v99, v222, s94\n"
    "v_mfma_f32_16x16x32_bf16 a[60:63], v[164:167], v[156:159], a[60:63]\n"
    "s_waitcnt lgkmcnt(2)\n"
    "v_cvt_pk_bf16_f32 v184, v96, v97\n"
    "v_cvt_pk_bf16_f32 v185, v98, v99\n"
    "v_mfma_f32_16x16x32_bf16 a[60:63], v[168:171], v[44:47], a[60:63]\n"
    "ds_read_b128 v[160:163], v201 offset:8192\n"
    "v_accvgpr_read_b32 v100, a36\n"
    "v_mfma_f32_16x16x32_bf16 a[60:63], v[172:175], v[76:79], a[60:63]\n"
    "v_accvgpr_read_b32 v101, a37\n"
    "v_accvgpr_read_b32 v102, a38\n"
    "v_mfma_f32_16x16x32_bf16 a[56:59], v[168:171], v[40:43], a[56:59]\n"
    "v_accvgpr_read_b32 v103, a39\n"
    "v_fma_f32 v100, v100, v224, s94\n"
    "v_mfma_f32_16x16x32_bf16 a[56:59], v[172:175], v[72:75], a[56:59]\n"
    "v_fma_f32 v101, v101, v224, s94\n"
    "v_fma_f32 v102, v102, v224, s94\n"
    "v_mfma_f32_16x16x32_bf16 a[52:55], v[168:171], v[36:39], a[52:55]\n"
    "ds_read_b128 v[164:167], v201 offset:9216\n"
    "v_fma_f32 v103, v103, v224, s94\n"
    "v_mfma_f32_16x16x32_bf16 a[52:55], v[172:175], v[68:71], a[52:55]\n"
    "v_cvt_pk_bf16_f32 v188, v100, v101\n"
    "v_cvt_pk_bf16_f32 v189, v102, v103\n"
    "v_mfma_f32_16x16x32_bf16 a[48:51], v[168:171], v[32:35], a[48:51]\n"
    "v_accvgpr_read_b32 v104, a40\n"
    "v_accvgpr_read_b32 v105, a41\n"
    "v_mfma_f32_16x16x32_bf16 a[48:51], v[172:175], v[64:67], a[48:51]\n"
    "s_waitcnt lgkmcnt(2)\n"
    "v_accvgpr_read_b32 v106, a42\n"
    "v_accvgpr_read_b32 v107, a43\n"
    "v_mfma_f32_16x16x32_bf16 a[48:51], v[176:179], v[232:235], a[48:51]\n"
    "ds_read_b128 v[168:171], v201 offset:32768\n"
    "v_fma_f32 v104, v104, v226, s94\n"
    "v_mfma_f32_16x16x32_bf16 a[48:51], v[180:183], v[80:83], a[48:51]\n"
    "v_fma_f32 v105, v105, v226, s94\n"
    "v_fma_f32 v106, v106, v226, s94\n"
    "v_mfma_f32_16x16x32_bf16 a[52:55], v[176:179], v[236:239], a[52:55]\n"
    "v_fma_f32 v107, v107, v226, s94\n"
    "v_cvt_pk_bf16_f32 v248, v104, v105\n"
    "v_mfma_f32_16x16x32_bf16 a[52:55], v[180:183], v[84:87], a[52:55]\n"
    "v_cvt_pk_bf16_f32 v249, v106, v107\n"
    "v_accvgpr_read_b32 v108, a44\n"
    "v_mfma_f32_16x16x32_bf16 a[56:59], v[176:179], v[240:243], a[56:59]\n"
    "ds_read_b128 v[172:175], v201 offset:33792\n"
    "v_accvgpr_read_b32 v109, a45\n"
    "v_mfma_f32_16x16x32_bf16 a[56:59], v[180:183], v[88:91], a[56:59]\n"
    "v_accvgpr_read_b32 v110, a46\n"
    "v_accvgpr_read_b32 v111, a47\n"
    "v_mfma_f32_16x16x32_bf16 a[60:63], v[176:179], v[244:247], a[60:63]\n"
    "v_fma_f32 v108, v108, v228, s94\n"
    "v_fma_f32 v109, v109, v228, s94\n"
    "v_mfma_f32_16x16x32_bf16 a[60:63], v[180:183], v[92:95], a[60:63]\n"
    "s_waitcnt lgkmcnt(2)\n"
    "v_fma_f32 v110, v110, v228, s94\n"
    "v_fma_f32 v111, v111, v228, s94\n"
    "v_mfma_f32_16x16x32_bf16 a[64:67], v[160:163], v[128:131], a[64:67]\n"
    "ds_read_b128 v[176:179], v207 offset:8192\n"
    "v_cvt_pk_bf16_f32 v252, v108, v109\n"
    "v_mfma_f32_16x16x32_bf16 a[64:67], v[164:167], v[144:147], a[64:67]\n"
    "v_cvt_pk_bf16_f32 v253, v110, v111\n"
    "v_accvgpr_read_b32 v96, a48\n"
    "v_mfma_f32_16x16x32_bf16 a[68:71], v[160:163], v[132:135], a[68:71]\n"
    "v_accvgpr_read_b32 v97, a49\n"
    "v_accvgpr_read_b32 v98, a50\n"
    "v_mfma_f32_16x16x32_bf16 a[68:71], v[164:167], v[148:151], a[68:71]\n"
    "v_accvgpr_read_b32 v99, a51\n"
    "v_fma_f32 v96, v96, v222, s94\n"
    "v_mfma_f32_16x16x32_bf16 a[72:75], v[160:163], v[136:139], a[72:75]\n"
    "ds_read_b128 v[180:183], v207 offset:9216\n"
    "v_fma_f32 v97, v97, v222, s94\n"
    "v_mfma_f32_16x16x32_bf16 a[72:75], v[164:167], v[152:155], a[72:75]\n"
    "v_fma_f32 v98, v98, v222, s94\n"
    "v_fma_f32 v99, v99, v222, s94\n"
    "v_mfma_f32_16x16x32_bf16 a[76:79], v[160:163], v[140:143], a[76:79]\n"
    "v_cvt_pk_bf16_f32 v186, v96, v97\n"
    "v_cvt_pk_bf16_f32 v187, v98, v99\n"
    "v_mfma_f32_16x16x32_bf16 a[76:79], v[164:167], v[156:159], a[76:79]\n"
    "s_waitcnt lgkmcnt(2)\n"
    "buffer_store_dwordx4 v[184:187], v205, s[28:31], 0 offen offset:64 sc0 sc1\n"
    "v_accvgpr_read_b32 v100, a52\n"
    "v_accvgpr_read_b32 v101, a53\n"
    "v_accvgpr_read_b32 v102, a54\n"
    "v_mfma_f32_16x16x32_bf16 a[76:79], v[168:171], v[44:47], a[76:79]\n"
    "ds_read_b128 v[160:163], v201 offset:10240\n"
    "v_accvgpr_read_b32 v103, a55\n"
    "v_mfma_f32_16x16x32_bf16 a[76:79], v[172:175], v[76:79], a[76:79]\n"
    "v_fma_f32 v100, v100, v224, s94\n"
    "v_fma_f32 v101, v101, v224, s94\n"
    "v_mfma_f32_16x16x32_bf16 a[72:75], v[168:171], v[40:43], a[72:75]\n"
    "v_fma_f32 v102, v102, v224, s94\n"
    "v_fma_f32 v103, v103, v224, s94\n"
    "v_mfma_f32_16x16x32_bf16 a[72:75], v[172:175], v[72:75], a[72:75]\n"
    "v_cvt_pk_bf16_f32 v190, v100, v101\n"
    "v_cvt_pk_bf16_f32 v191, v102, v103\n"
    "v_mfma_f32_16x16x32_bf16 a[68:71], v[168:171], v[36:39], a[68:71]\n"
    "ds_read_b128 v[164:167], v201 offset:11264\n"
    "v_mfma_f32_16x16x32_bf16 a[68:71], v[172:175], v[68:71], a[68:71]\n"
    "buffer_store_dwordx4 v[188:191], v205, s[28:31], s62 offen offset:64 sc0 sc1\n"
    "v_accvgpr_read_b32 v104, a56\n"
    "v_accvgpr_read_b32 v105, a57\n"
    "v_accvgpr_read_b32 v106, a58\n"
    "v_mfma_f32_16x16x32_bf16 a[64:67], v[168:171], v[32:35], a[64:67]\n"
    "v_accvgpr_read_b32 v107, a59\n"
    "v_fma_f32 v104, v104, v226, s94\n"
    "v_mfma_f32_16x16x32_bf16 a[64:67], v[172:175], v[64:67], a[64:67]\n"
    "s_waitcnt lgkmcnt(2)\n"
    "v_fma_f32 v105, v105, v226, s94\n"
    "v_fma_f32 v106, v106, v226, s94\n"
    "v_mfma_f32_16x16x32_bf16 a[64:67], v[176:179], v[232:235], a[64:67]\n"
    "ds_read_b128 v[168:171], v201 offset:34816\n"
    "v_fma_f32 v107, v107, v226, s94\n"
    "v_mfma_f32_16x16x32_bf16 a[64:67], v[180:183], v[80:83], a[64:67]\n"
    "v_cvt_pk_bf16_f32 v250, v104, v105\n"
    "v_cvt_pk_bf16_f32 v251, v106, v107\n"
    "v_mfma_f32_16x16x32_bf16 a[68:71], v[176:179], v[236:239], a[68:71]\n"
    "buffer_store_dwordx4 v[248:251], v205, s[28:31], s63 offen offset:64 sc0 sc1\n"
    "v_accvgpr_read_b32 v108, a60\n"
    "v_accvgpr_read_b32 v109, a61\n"
    "v_accvgpr_read_b32 v110, a62\n"
    "v_mfma_f32_16x16x32_bf16 a[68:71], v[180:183], v[84:87], a[68:71]\n"
    "v_accvgpr_read_b32 v111, a63\n"
    "v_fma_f32 v108, v108, v228, s94\n"
    "v_mfma_f32_16x16x32_bf16 a[72:75], v[176:179], v[240:243], a[72:75]\n"
    "ds_read_b128 v[172:175], v201 offset:35840\n"
    "v_fma_f32 v109, v109, v228, s94\n"
    "v_mfma_f32_16x16x32_bf16 a[72:75], v[180:183], v[88:91], a[72:75]\n"
    "v_fma_f32 v110, v110, v228, s94\n"
    "v_fma_f32 v111, v111, v228, s94\n"
    "v_mfma_f32_16x16x32_bf16 a[76:79], v[176:179], v[244:247], a[76:79]\n"
    "v_cvt_pk_bf16_f32 v254, v108, v109\n"
    "v_cvt_pk_bf16_f32 v255, v110, v111\n"
    "v_mfma_f32_16x16x32_bf16 a[76:79], v[180:183], v[92:95], a[76:79]\n"
    "s_waitcnt lgkmcnt(2)\n"
    "buffer_store_dwordx4 v[252:255], v205, s[28:31], s64 offen offset:64 sc0 sc1\n"
    "v_accvgpr_read_b32 v96, a64\n"
    "v_accvgpr_read_b32 v97, a65\n"
    "v_accvgpr_read_b32 v98, a66\n"
    "v_mfma_f32_16x16x32_bf16 a[80:83], v[160:163], v[128:131], a[80:83]\n"
    "ds_read_b128 v[176:179], v207 offset:10240\n"
    "v_accvgpr_read_b32 v99, a67\n"
    "v_mfma_f32_16x16x32_bf16 a[80:83], v[164:167], v[144:147], a[80:83]\n"
    "v_fma_f32 v96, v96, v222, s94\n"
    "v_fma_f32 v97, v97, v222, s94\n"
    "v_mfma_f32_16x16x32_bf16 a[84:87], v[160:163], v[132:135], a[84:87]\n"
    "v_fma_f32 v98, v98, v222, s94\n"
    "v_fma_f32 v99, v99, v222, s94\n"
    "v_mfma_f32_16x16x32_bf16 a[84:87], v[164:167], v[148:151], a[84:87]\n"
    "v_cvt_pk_bf16_f32 v48, v96, v97\n"
    "v_cvt_pk_bf16_f32 v49, v98, v99\n"
    "v_mfma_f32_16x16x32_bf16 a[88:91], v[160:163], v[136:139], a[88:91]\n"
    "ds_read_b128 v[180:183], v207 offset:11264\n"
    "v_accvgpr_read_b32 v100, a68\n"
    "v_mfma_f32_16x16x32_bf16 a[88:91], v[164:167], v[152:155], a[88:91]\n"
    "v_accvgpr_read_b32 v101, a69\n"
    "v_accvgpr_read_b32 v102, a70\n"
    "v_mfma_f32_16x16x32_bf16 a[92:95], v[160:163], v[140:143], a[92:95]\n"
    "v_accvgpr_read_b32 v103, a71\n"
    "v_fma_f32 v100, v100, v224, s94\n"
    "v_mfma_f32_16x16x32_bf16 a[92:95], v[164:167], v[156:159], a[92:95]\n"
    "s_waitcnt lgkmcnt(2)\n"
    "v_fma_f32 v101, v101, v224, s94\n"
    "v_fma_f32 v102, v102, v224, s94\n"
    "v_mfma_f32_16x16x32_bf16 a[92:95], v[168:171], v[44:47], a[92:95]\n"
    "ds_read_b128 v[160:163], v201 offset:12288\n"
    "v_fma_f32 v103, v103, v224, s94\n"
    "v_mfma_f32_16x16x32_bf16 a[92:95], v[172:175], v[76:79], a[92:95]\n"
    "v_cvt_pk_bf16_f32 v52, v100, v101\n"
    "v_cvt_pk_bf16_f32 v53, v102, v103\n"
    "v_mfma_f32_16x16x32_bf16 a[88:91], v[168:171], v[40:43], a[88:91]\n"
    "v_accvgpr_read_b32 v104, a72\n"
    "v_accvgpr_read_b32 v105, a73\n"
    "v_mfma_f32_16x16x32_bf16 a[88:91], v[172:175], v[72:75], a[88:91]\n"
    "v_accvgpr_read_b32 v106, a74\n"
    "v_accvgpr_read_b32 v107, a75\n"
    "v_mfma_f32_16x16x32_bf16 a[84:87], v[168:171], v[36:39], a[84:87]\n"
    "ds_read_b128 v[164:167], v201 offset:13312\n"
    "v_fma_f32 v104, v104, v226, s94\n"
    "v_mfma_f32_16x16x32_bf16 a[84:87], v[172:175], v[68:71], a[84:87]\n"
    "v_fma_f32 v105, v105, v226, s94\n"
    "v_fma_f32 v106, v106, v226, s94\n"
    "v_mfma_f32_16x16x32_bf16 a[80:83], v[168:171], v[32:35], a[80:83]\n"
    "v_fma_f32 v107, v107, v226, s94\n"
    "v_cvt_pk_bf16_f32 v56, v104, v105\n"
    "v_mfma_f32_16x16x32_bf16 a[80:83], v[172:175], v[64:67], a[80:83]\n"
    "s_waitcnt lgkmcnt(2)\n"
    "v_cvt_pk_bf16_f32 v57, v106, v107\n"
    "v_accvgpr_read_b32 v108, a76\n"
    "v_mfma_f32_16x16x32_bf16 a[80:83], v[176:179], v[232:235], a[80:83]\n"
    "ds_read_b128 v[168:171], v201 offset:36864\n"
    "v_accvgpr_read_b32 v109, a77\n"
    "v_mfma_f32_16x16x32_bf16 a[80:83], v[180:183], v[80:83], a[80:83]\n"
    "v_accvgpr_read_b32 v110, a78\n"
    "v_accvgpr_read_b32 v111, a79\n"
    "v_mfma_f32_16x16x32_bf16 a[84:87], v[176:179], v[236:239], a[84:87]\n"
    "v_fma_f32 v108, v108, v228, s94\n"
    "v_fma_f32 v109, v109, v228, s94\n"
    "v_mfma_f32_16x16x32_bf16 a[84:87], v[180:183], v[84:87], a[84:87]\n"
    "v_fma_f32 v110, v110, v228, s94\n"
    "v_fma_f32 v111, v111, v228, s94\n"
    "v_mfma_f32_16x16x32_bf16 a[88:91], v[176:179], v[240:243], a[88:91]\n"
    "ds_read_b128 v[172:175], v201 offset:37888\n"
    "v_cvt_pk_bf16_f32 v60, v108, v109\n"
    "v_mfma_f32_16x16x32_bf16 a[88:91], v[180:183], v[88:91], a[88:91]\n"
    "v_cvt_pk_bf16_f32 v61, v110, v111\n"
    "v_mfma_f32_16x16x32_bf16 a[92:95], v[176:179], v[244:247], a[92:95]\n"
    "v_mfma_f32_16x16x32_bf16 a[92:95], v[180:183], v[92:95], a[92:95]\n"
    "s_waitcnt lgkmcnt(2)\n"
    "v_accvgpr_read_b32 v96, a80\n"
    "v_accvgpr_read_b32 v97, a81\n"
    "v_mfma_f32_16x16x32_bf16 a[96:99], v[160:163], v[128:131], a[96:99]\n"
    "ds_read_b128 v[176:179], v207 offset:12288\n"
    "v_accvgpr_read_b32 v98, a82\n"
    "v_mfma_f32_16x16x32_bf16 a[96:99], v[164:167], v[144:147], a[96:99]\n"
    "v_accvgpr_read_b32 v99, a83\n"
    "v_fma_f32 v96, v96, v222, s94\n"
    "v_mfma_f32_16x16x32_bf16 a[100:103], v[160:163], v[132:135], a[100:103]\n"
    "v_fma_f32 v97, v97, v222, s94\n"
    "v_fma_f32 v98, v98, v222, s94\n"
    "v_mfma_f32_16x16x32_bf16 a[100:103], v[164:167], v[148:151], a[100:103]\n"
    "v_fma_f32 v99, v99, v222, s94\n"
    "v_cvt_pk_bf16_f32 v50, v96, v97\n"
    "v_mfma_f32_16x16x32_bf16 a[104:107], v[160:163], v[136:139], a[104:107]\n"
    "ds_read_b128 v[180:183], v207 offset:13312\n"
    "v_cvt_pk_bf16_f32 v51, v98, v99\n"
    "v_mfma_f32_16x16x32_bf16 a[104:107], v[164:167], v[152:155], a[104:107]\n"
    "buffer_store_dwordx4 v[48:51], v205, s[28:31], 0 offen offset:128 sc0 sc1\n"
    "v_accvgpr_read_b32 v100, a84\n"
    "v_accvgpr_read_b32 v101, a85\n"
    "v_accvgpr_read_b32 v102, a86\n"
    "v_mfma_f32_16x16x32_bf16 a[108:111], v[160:163], v[140:143], a[108:111]\n"
    "v_accvgpr_read_b32 v103, a87\n"
    "v_fma_f32 v100, v100, v224, s94\n"
    "v_mfma_f32_16x16x32_bf16 a[108:111], v[164:167], v[156:159], a[108:111]\n"
    "s_waitcnt lgkmcnt(2)\n"
    "s_cmp_eq_u32 s57, 0\n"
    "s_cselect_b32 s77, s74, 0\n"
    "v_mov_b32 v196, 0x40000000\n"
    "v_mov_b32 v197, 0x800\n"
    "v_cmp_gt_u32 vcc, s77, v231\n"
    "v_mov_b32 v198, 0\n"
    "v_mov_b32 v198, 0\n"
    "v_cndmask_b32 v197, v196, v197, vcc\n"
    "buffer_store_dword v221, v197, s[44:47], 0 offen sc1\n"
    "v_mfma_f32_16x16x32_bf16 a[108:111], v[168:171], v[44:47], a[108:111]\n"
    "ds_read_b128 v[160:163], v201 offset:14336\n"
    "v_fma_f32 v101, v101, v224, s94\n"
    "v_mfma_f32_16x16x32_bf16 a[108:111], v[172:175], v[76:79], a[108:111]\n"
    "v_fma_f32 v102, v102, v224, s94\n"
    "v_fma_f32 v103, v103, v224, s94\n"
    "v_mfma_f32_16x16x32_bf16 a[104:107], v[168:171], v[40:43], a[104:107]\n"
    "v_cvt_pk_bf16_f32 v54, v100, v101\n"
    "v_cvt_pk_bf16_f32 v55, v102, v103\n"
    "v_mfma_f32_16x16x32_bf16 a[104:107], v[172:175], v[72:75], a[104:107]\n"
    "buffer_store_dwordx4 v[52:55], v205, s[28:31], s62 offen offset:128 sc0 sc1\n"
    "v_accvgpr_read_b32 v104, a88\n"
    "v_accvgpr_read_b32 v105, a89\n"
    "v_accvgpr_read_b32 v106, a90\n"
    "v_mfma_f32_16x16x32_bf16 a[100:103], v[168:171], v[36:39], a[100:103]\n"
    "ds_read_b128 v[164:167], v201 offset:15360\n"
    "v_accvgpr_read_b32 v107, a91\n"
    "v_mfma_f32_16x16x32_bf16 a[100:103], v[172:175], v[68:71], a[100:103]\n"
    "v_fma_f32 v104, v104, v226, s94\n"
    "v_fma_f32 v105, v105, v226, s94\n"
    "v_mfma_f32_16x16x32_bf16 a[96:99], v[168:171], v[32:35], a[96:99]\n"
    "v_fma_f32 v106, v106, v226, s94\n"
    "v_fma_f32 v107, v107, v226, s94\n"
    "v_mfma_f32_16x16x32_bf16 a[96:99], v[172:175], v[64:67], a[96:99]\n"
    "s_waitcnt lgkmcnt(2)\n"
    "v_cvt_pk_bf16_f32 v58, v104, v105\n"
    "v_cvt_pk_bf16_f32 v59, v106, v107\n"
    "v_mfma_f32_16x16x32_bf16 a[96:99], v[176:179], v[232:235], a[96:99]\n"
    "ds_read_b128 v[168:171], v201 offset:38912\n"
    "v_mfma_f32_16x16x32_bf16 a[96:99], v[180:183], v[80:83], a[96:99]\n"
    "buffer_store_dwordx4 v[56:59], v205, s[28:31], s63 offen offset:128 sc0 sc1\n"
    "v_accvgpr_read_b32 v108, a92\n"
    "v_accvgpr_read_b32 v109, a93\n"
    "v_accvgpr_read_b32 v110, a94\n"
    "v_mfma_f32_16x16x32_bf16 a[100:103], v[176:179], v[236:239], a[100:103]\n"
    "v_accvgpr_read_b32 v111, a95\n"
    "v_fma_f32 v108, v108, v228, s94\n"
    "v_mfma_f32_16x16x32_bf16 a[100:103], v[180:183], v[84:87], a[100:103]\n"
    "v_fma_f32 v109, v109, v228, s94\n"
    "v_fma_f32 v110, v110, v228, s94\n"
    "v_mfma_f32_16x16x32_bf16 a[104:107], v[176:179], v[240:243], a[104:107]\n"
    "ds_read_b128 v[172:175], v201 offset:39936\n"
    "v_fma_f32 v111, v111, v228, s94\n"
    "v_mfma_f32_16x16x32_bf16 a[104:107], v[180:183], v[88:91], a[104:107]\n"
    "v_cvt_pk_bf16_f32 v62, v108, v109\n"
    "v_cvt_pk_bf16_f32 v63, v110, v111\n"
    "v_mfma_f32_16x16x32_bf16 a[108:111], v[176:179], v[244:247], a[108:111]\n"
    "buffer_store_dwordx4 v[60:63], v205, s[28:31], s64 offen offset:128 sc0 sc1\n"
    "v_mfma_f32_16x16x32_bf16 a[108:111], v[180:183], v[92:95], a[108:111]\n"
    "s_waitcnt lgkmcnt(2)\n"
    "v_accvgpr_read_b32 v96, a96\n"
    "v_accvgpr_read_b32 v97, a97\n"
    "v_mfma_f32_16x16x32_bf16 a[112:115], v[160:163], v[128:131], a[112:115]\n"
    "ds_read_b128 v[176:179], v207 offset:14336\n"
    "v_accvgpr_read_b32 v98, a98\n"
    "v_mfma_f32_16x16x32_bf16 a[112:115], v[164:167], v[144:147], a[112:115]\n"
    "v_accvgpr_read_b32 v99, a99\n"
    "v_fma_f32 v96, v96, v222, s94\n"
    "v_mfma_f32_16x16x32_bf16 a[116:119], v[160:163], v[132:135], a[116:119]\n"
    "v_fma_f32 v97, v97, v222, s94\n"
    "v_fma_f32 v98, v98, v222, s94\n"
    "v_mfma_f32_16x16x32_bf16 a[116:119], v[164:167], v[148:151], a[116:119]\n"
    "v_fma_f32 v99, v99, v222, s94\n"
    "v_cvt_pk_bf16_f32 v184, v96, v97\n"
    "v_mfma_f32_16x16x32_bf16 a[120:123], v[160:163], v[136:139], a[120:123]\n"
    "ds_read_b128 v[180:183], v207 offset:15360\n"
    "v_cvt_pk_bf16_f32 v185, v98, v99\n"
    "v_mfma_f32_16x16x32_bf16 a[120:123], v[164:167], v[152:155], a[120:123]\n"
    "v_accvgpr_read_b32 v100, a100\n"
    "v_accvgpr_read_b32 v101, a101\n"
    "v_mfma_f32_16x16x32_bf16 a[124:127], v[160:163], v[140:143], a[124:127]\n"
    "v_accvgpr_read_b32 v102, a102\n"
    "v_accvgpr_read_b32 v103, a103\n"
    "v_mfma_f32_16x16x32_bf16 a[124:127], v[164:167], v[156:159], a[124:127]\n"
    "s_waitcnt lgkmcnt(2)\n"
    "v_fma_f32 v100, v100, v224, s94\n"
    "v_fma_f32 v101, v101, v224, s94\n"
    "v_mfma_f32_16x16x32_bf16 a[124:127], v[168:171], v[44:47], a[124:127]\n"
    "ds_read_b128 v[160:163], v201 offset:16384\n"
    "v_fma_f32 v102, v102, v224, s94\n"
    "v_mfma_f32_16x16x32_bf16 a[124:127], v[172:175], v[76:79], a[124:127]\n"
    "v_fma_f32 v103, v103, v224, s94\n"
    "v_cvt_pk_bf16_f32 v188, v100, v101\n"
    "v_mfma_f32_16x16x32_bf16 a[120:123], v[168:171], v[40:43], a[120:123]\n"
    "v_cvt_pk_bf16_f32 v189, v102, v103\n"
    "v_accvgpr_read_b32 v104, a104\n"
    "v_mfma_f32_16x16x32_bf16 a[120:123], v[172:175], v[72:75], a[120:123]\n"
    "v_accvgpr_read_b32 v105, a105\n"
    "v_accvgpr_read_b32 v106, a106\n"
    "v_mfma_f32_16x16x32_bf16 a[116:119], v[168:171], v[36:39], a[116:119]\n"
    "ds_read_b128 v[164:167], v201 offset:17408\n"
    "v_accvgpr_read_b32 v107, a107\n"
    "v_mfma_f32_16x16x32_bf16 a[116:119], v[172:175], v[68:71], a[116:119]\n"
    "v_fma_f32 v104, v104, v226, s94\n"
    "v_fma_f32 v105, v105, v226, s94\n"
    "v_mfma_f32_16x16x32_bf16 a[112:115], v[168:171], v[32:35], a[112:115]\n"
    "v_fma_f32 v106, v106, v226, s94\n"
    "v_fma_f32 v107, v107, v226, s94\n"
    "v_mfma_f32_16x16x32_bf16 a[112:115], v[172:175], v[64:67], a[112:115]\n"
    "s_waitcnt lgkmcnt(2)\n"
    "v_cvt_pk_bf16_f32 v248, v104, v105\n"
    "v_cvt_pk_bf16_f32 v249, v106, v107\n"
    "v_mfma_f32_16x16x32_bf16 a[112:115], v[176:179], v[232:235], a[112:115]\n"
    "ds_read_b128 v[168:171], v201 offset:40960\n"
    "v_accvgpr_read_b32 v108, a108\n"
    "v_mfma_f32_16x16x32_bf16 a[112:115], v[180:183], v[80:83], a[112:115]\n"
    "v_accvgpr_read_b32 v109, a109\n"
    "v_accvgpr_read_b32 v110, a110\n"
    "v_mfma_f32_16x16x32_bf16 a[116:119], v[176:179], v[236:239], a[116:119]\n"
    "v_accvgpr_read_b32 v111, a111\n"
    "v_fma_f32 v108, v108, v228, s94\n"
    "v_mfma_f32_16x16x32_bf16 a[116:119], v[180:183], v[84:87], a[116:119]\n"
    "v_fma_f32 v109, v109, v228, s94\n"
    "v_fma_f32 v110, v110, v228, s94\n"
    "v_mfma_f32_16x16x32_bf16 a[120:123], v[176:179], v[240:243], a[120:123]\n"
    "ds_read_b128 v[172:175], v201 offset:41984\n"
    "v_fma_f32 v111, v111, v228, s94\n"
    "v_mfma_f32_16x16x32_bf16 a[120:123], v[180:183], v[88:91], a[120:123]\n"
    "v_cvt_pk_bf16_f32 v252, v108, v109\n"
    "v_cvt_pk_bf16_f32 v253, v110, v111\n"
    "v_mfma_f32_16x16x32_bf16 a[124:127], v[176:179], v[244:247], a[124:127]\n"
    "v_mfma_f32_16x16x32_bf16 a[124:127], v[180:183], v[92:95], a[124:127]\n"
    "s_waitcnt lgkmcnt(2)\n"
    "v_accvgpr_read_b32 v96, a112\n"
    "v_accvgpr_read_b32 v97, a113\n"
    "v_mfma_f32_16x16x32_bf16 a[128:131], v[160:163], v[128:131], a[128:131]\n"
    "ds_read_b128 v[176:179], v207 offset:16384\n"
    "v_accvgpr_read_b32 v98, a114\n"
    "v_mfma_f32_16x16x32_bf16 a[128:131], v[164:167], v[144:147], a[128:131]\n"
    "v_accvgpr_read_b32 v99, a115\n"
    "v_fma_f32 v96, v96, v222, s94\n"
    "v_mfma_f32_16x16x32_bf16 a[132:135], v[160:163], v[132:135], a[132:135]\n"
    "v_fma_f32 v97, v97, v222, s94\n"
    "v_fma_f32 v98, v98, v222, s94\n"
    "v_mfma_f32_16x16x32_bf16 a[132:135], v[164:167], v[148:151], a[132:135]\n"
    "v_fma_f32 v99, v99, v222, s94\n"
    "v_cvt_pk_bf16_f32 v186, v96, v97\n"
    "v_mfma_f32_16x16x32_bf16 a[136:139], v[160:163], v[136:139], a[136:139]\n"
    "ds_read_b128 v[180:183], v207 offset:17408\n"
    "v_cvt_pk_bf16_f32 v187, v98, v99\n"
    "v_mfma_f32_16x16x32_bf16 a[136:139], v[164:167], v[152:155], a[136:139]\n"
    "buffer_store_dwordx4 v[184:187], v205, s[28:31], 0 offen offset:192 sc0 sc1\n"
    "v_accvgpr_read_b32 v100, a116\n"
    "v_accvgpr_read_b32 v101, a117\n"
    "v_accvgpr_read_b32 v102, a118\n"
    "v_mfma_f32_16x16x32_bf16 a[140:143], v[160:163], v[140:143], a[140:143]\n"
    "v_accvgpr_read_b32 v103, a119\n"
    "v_fma_f32 v100, v100, v224, s94\n"
    "v_mfma_f32_16x16x32_bf16 a[140:143], v[164:167], v[156:159], a[140:143]\n"
    "s_waitcnt lgkmcnt(2)\n"
    "v_fma_f32 v101, v101, v224, s94\n"
    "v_fma_f32 v102, v102, v224, s94\n"
    "v_mfma_f32_16x16x32_bf16 a[140:143], v[168:171], v[44:47], a[140:143]\n"
    "ds_read_b128 v[160:163], v201 offset:18432\n"
    "v_fma_f32 v103, v103, v224, s94\n"
    "v_mfma_f32_16x16x32_bf16 a[140:143], v[172:175], v[76:79], a[140:143]\n"
    "v_cvt_pk_bf16_f32 v190, v100, v101\n"
    "v_cvt_pk_bf16_f32 v191, v102, v103\n"
    "v_mfma_f32_16x16x32_bf16 a[136:139], v[168:171], v[40:43], a[136:139]\n"
    "buffer_store_dwordx4 v[188:191], v205, s[28:31], s62 offen offset:192 sc0 sc1\n"
    "v_accvgpr_read_b32 v104, a120\n"
    "v_accvgpr_read_b32 v105, a121\n"
    "v_accvgpr_read_b32 v106, a122\n"
    "v_mfma_f32_16x16x32_bf16 a[136:139], v[172:175], v[72:75], a[136:139]\n"
    "v_accvgpr_read_b32 v107, a123\n"
    "v_fma_f32 v104, v104, v226, s94\n"
    "v_mfma_f32_16x16x32_bf16 a[132:135], v[168:171], v[36:39], a[132:135]\n"
    "ds_read_b128 v[164:167], v201 offset:19456\n"
    "v_fma_f32 v105, v105, v226, s94\n"
    "v_mfma_f32_16x16x32_bf16 a[132:135], v[172:175], v[68:71], a[132:135]\n"
    "v_fma_f32 v106, v106, v226, s94\n"
    "v_fma_f32 v107, v107, v226, s94\n"
    "v_mfma_f32_16x16x32_bf16 a[128:131], v[168:171], v[32:35], a[128:131]\n"
    "v_cvt_pk_bf16_f32 v250, v104, v105\n"
    "v_cvt_pk_bf16_f32 v251, v106, v107\n"
    "v_mfma_f32_16x16x32_bf16 a[128:131], v[172:175], v[64:67], a[128:131]\n"
    "s_waitcnt lgkmcnt(2)\n"
    "buffer_store_dwordx4 v[248:251], v205, s[28:31], s63 offen offset:192 sc0 sc1\n"
    "v_accvgpr_read_b32 v108, a124\n"
    "v_accvgpr_read_b32 v109, a125\n"
    "v_accvgpr_read_b32 v110, a126\n"
    "v_mfma_f32_16x16x32_bf16 a[128:131], v[176:179], v[232:235], a[128:131]\n"
    "ds_read_b128 v[168:171], v201 offset:43008\n"
    "v_accvgpr_read_b32 v111, a127\n"
    "v_mfma_f32_16x16x32_bf16 a[128:131], v[180:183], v[80:83], a[128:131]\n"
    "v_fma_f32 v108, v108, v228, s94\n"
    "v_fma_f32 v109, v109, v228, s94\n"
    "v_mfma_f32_16x16x32_bf16 a[132:135], v[176:179], v[236:239], a[132:135]\n"
    "v_fma_f32 v110, v110, v228, s94\n"
    "v_fma_f32 v111, v111, v228, s94\n"
    "v_mfma_f32_16x16x32_bf16 a[132:135], v[180:183], v[84:87], a[132:135]\n"
    "v_cvt_pk_bf16_f32 v254, v108, v109\n"
    "v_cvt_pk_bf16_f32 v255, v110, v111\n"
    "v_mfma_f32_16x16x32_bf16 a[136:139], v[176:179], v[240:243], a[136:139]\n"
    "ds_read_b128 v[172:175], v201 offset:44032\n"
    "v_mfma_f32_16x16x32_bf16 a[136:139], v[180:183], v[88:91], a[136:139]\n"
    "buffer_store_dwordx4 v[252:255], v205, s[28:31], s64 offen offset:192 sc0 sc1\n"
    "v_mfma_f32_16x16x32_bf16 a[140:143], v[176:179], v[244:247], a[140:143]\n"
    "v_mfma_f32_16x16x32_bf16 a[140:143], v[180:183], v[92:95], a[140:143]\n"
    "s_waitcnt lgkmcnt(2)\n"
    "v_accvgpr_read_b32 v96, a128\n"
    "v_accvgpr_read_b32 v97, a129\n"
    "v_mfma_f32_16x16x32_bf16 a[144:147], v[160:163], v[128:131], a[144:147]\n"
    "ds_read_b128 v[176:179], v207 offset:18432\n"
    "v_accvgpr_read_b32 v98, a130\n"
    "v_mfma_f32_16x16x32_bf16 a[144:147], v[164:167], v[144:147], a[144:147]\n"
    "v_accvgpr_read_b32 v99, a131\n"
    "v_fma_f32 v96, v96, v222, s94\n"
    "v_mfma_f32_16x16x32_bf16 a[148:151], v[160:163], v[132:135], a[148:151]\n"
    "v_fma_f32 v97, v97, v222, s94\n"
    "v_fma_f32 v98, v98, v222, s94\n"
    "v_mfma_f32_16x16x32_bf16 a[148:151], v[164:167], v[148:151], a[148:151]\n"
    "v_fma_f32 v99, v99, v222, s94\n"
    "v_cvt_pk_bf16_f32 v48, v96, v97\n"
    "v_mfma_f32_16x16x32_bf16 a[152:155], v[160:163], v[136:139], a[152:155]\n"
    "ds_read_b128 v[180:183], v207 offset:19456\n"
    "v_cvt_pk_bf16_f32 v49, v98, v99\n"
    "v_mfma_f32_16x16x32_bf16 a[152:155], v[164:167], v[152:155], a[152:155]\n"
    "v_accvgpr_read_b32 v100, a132\n"
    "v_accvgpr_read_b32 v101, a133\n"
    "v_mfma_f32_16x16x32_bf16 a[156:159], v[160:163], v[140:143], a[156:159]\n"
    "v_accvgpr_read_b32 v102, a134\n"
    "v_accvgpr_read_b32 v103, a135\n"
    "v_mfma_f32_16x16x32_bf16 a[156:159], v[164:167], v[156:159], a[156:159]\n"
    "s_waitcnt lgkmcnt(2)\n"
    "v_fma_f32 v100, v100, v224, s94\n"
    "v_fma_f32 v101, v101, v224, s94\n"
    "v_mfma_f32_16x16x32_bf16 a[156:159], v[168:171], v[44:47], a[156:159]\n"
    "ds_read_b128 v[160:163], v201 offset:20480\n"
    "v_fma_f32 v102, v102, v224, s94\n"
    "v_mfma_f32_16x16x32_bf16 a[156:159], v[172:175], v[76:79], a[156:159]\n"
    "v_fma_f32 v103, v103, v224, s94\n"
    "v_cvt_pk_bf16_f32 v52, v100, v101\n"
    "v_mfma_f32_16x16x32_bf16 a[152:155], v[168:171], v[40:43], a[152:155]\n"
    "v_cvt_pk_bf16_f32 v53, v102, v103\n"
    "v_accvgpr_read_b32 v104, a136\n"
    "v_mfma_f32_16x16x32_bf16 a[152:155], v[172:175], v[72:75], a[152:155]\n"
    "v_accvgpr_read_b32 v105, a137\n"
    "v_accvgpr_read_b32 v106, a138\n"
    "v_mfma_f32_16x16x32_bf16 a[148:151], v[168:171], v[36:39], a[148:151]\n"
    "ds_read_b128 v[164:167], v201 offset:21504\n"
    "v_accvgpr_read_b32 v107, a139\n"
    "v_mfma_f32_16x16x32_bf16 a[148:151], v[172:175], v[68:71], a[148:151]\n"
    "v_fma_f32 v104, v104, v226, s94\n"
    "v_fma_f32 v105, v105, v226, s94\n"
    "v_mfma_f32_16x16x32_bf16 a[144:147], v[168:171], v[32:35], a[144:147]\n"
    "v_fma_f32 v106, v106, v226, s94\n"
    "v_fma_f32 v107, v107, v226, s94\n"
    "v_mfma_f32_16x16x32_bf16 a[144:147], v[172:175], v[64:67], a[144:147]\n"
    "s_waitcnt lgkmcnt(2)\n"
    "v_cvt_pk_bf16_f32 v56, v104, v105\n"
    "v_cvt_pk_bf16_f32 v57, v106, v107\n"
    "v_mfma_f32_16x16x32_bf16 a[144:147], v[176:179], v[232:235], a[144:147]\n"
    "ds_read_b128 v[168:171], v201 offset:45056\n"
    "v_accvgpr_read_b32 v108, a140\n"
    "v_mfma_f32_16x16x32_bf16 a[144:147], v[180:183], v[80:83], a[144:147]\n"
    "v_accvgpr_read_b32 v109, a141\n"
    "v_accvgpr_read_b32 v110, a142\n"
    "v_mfma_f32_16x16x32_bf16 a[148:151], v[176:179], v[236:239], a[148:151]\n"
    "v_accvgpr_read_b32 v111, a143\n"
    "v_fma_f32 v108, v108, v228, s94\n"
    "v_mfma_f32_16x16x32_bf16 a[148:151], v[180:183], v[84:87], a[148:151]\n"
    "v_fma_f32 v109, v109, v228, s94\n"
    "v_fma_f32 v110, v110, v228, s94\n"
    "v_mfma_f32_16x16x32_bf16 a[152:155], v[176:179], v[240:243], a[152:155]\n"
    "ds_read_b128 v[172:175], v201 offset:46080\n"
    "v_fma_f32 v111, v111, v228, s94\n"
    "v_mfma_f32_16x16x32_bf16 a[152:155], v[180:183], v[88:91], a[152:155]\n"
    "v_cvt_pk_bf16_f32 v60, v108, v109\n"
    "v_cvt_pk_bf16_f32 v61, v110, v111\n"
    "v_mfma_f32_16x16x32_bf16 a[156:159], v[176:179], v[244:247], a[156:159]\n"
    "v_mfma_f32_16x16x32_bf16 a[156:159], v[180:183], v[92:95], a[156:159]\n"
    "s_waitcnt lgkmcnt(2)\n"
    "v_accvgpr_read_b32 v96, a144\n"
    "v_accvgpr_read_b32 v97, a145\n"
    "v_mfma_f32_16x16x32_bf16 a[160:163], v[160:163], v[128:131], a[160:163]\n"
    "ds_read_b128 v[176:179], v207 offset:20480\n"
    "v_accvgpr_read_b32 v98, a146\n"
    "v_mfma_f32_16x16x32_bf16 a[160:163], v[164:167], v[144:147], a[160:163]\n"
    "v_accvgpr_read_b32 v99, a147\n"
    "v_fma_f32 v96, v96, v222, s94\n"
    "v_mfma_f32_16x16x32_bf16 a[164:167], v[160:163], v[132:135], a[164:167]\n"
    "v_fma_f32 v97, v97, v222, s94\n"
    "v_fma_f32 v98, v98, v222, s94\n"
    "v_mfma_f32_16x16x32_bf16 a[164:167], v[164:167], v[148:151], a[164:167]\n"
    "v_fma_f32 v99, v99, v222, s94\n"
    "v_cvt_pk_bf16_f32 v50, v96, v97\n"
    "v_mfma_f32_16x16x32_bf16 a[168:171], v[160:163], v[136:139], a[168:171]\n"
    "ds_read_b128 v[180:183], v207 offset:21504\n"
    "v_cvt_pk_bf16_f32 v51, v98, v99\n"
    "v_mfma_f32_16x16x32_bf16 a[168:171], v[164:167], v[152:155], a[168:171]\n"
    "buffer_store_dwordx4 v[48:51], v205, s[28:31], 0 offen offset:256 sc0 sc1\n"
    "v_accvgpr_read_b32 v100, a148\n"
    "v_accvgpr_read_b32 v101, a149\n"
    "v_accvgpr_read_b32 v102, a150\n"
    "v_mfma_f32_16x16x32_bf16 a[172:175], v[160:163], v[140:143], a[172:175]\n"
    "v_accvgpr_read_b32 v103, a151\n"
    "v_fma_f32 v100, v100, v224, s94\n"
    "v_mfma_f32_16x16x32_bf16 a[172:175], v[164:167], v[156:159], a[172:175]\n"
    "s_waitcnt lgkmcnt(2)\n"
    "v_fma_f32 v101, v101, v224, s94\n"
    "v_fma_f32 v102, v102, v224, s94\n"
    "v_mfma_f32_16x16x32_bf16 a[172:175], v[168:171], v[44:47], a[172:175]\n"
    "ds_read_b128 v[160:163], v201 offset:22528\n"
    "v_fma_f32 v103, v103, v224, s94\n"
    "v_mfma_f32_16x16x32_bf16 a[172:175], v[172:175], v[76:79], a[172:175]\n"
    "v_cvt_pk_bf16_f32 v54, v100, v101\n"
    "v_cvt_pk_bf16_f32 v55, v102, v103\n"
    "v_mfma_f32_16x16x32_bf16 a[168:171], v[168:171], v[40:43], a[168:171]\n"
    "buffer_store_dwordx4 v[52:55], v205, s[28:31], s62 offen offset:256 sc0 sc1\n"
    "v_accvgpr_read_b32 v104, a152\n"
    "v_accvgpr_read_b32 v105, a153\n"
    "v_accvgpr_read_b32 v106, a154\n"
    "v_mfma_f32_16x16x32_bf16 a[168:171], v[172:175], v[72:75], a[168:171]\n"
    "v_accvgpr_read_b32 v107, a155\n"
    "v_fma_f32 v104, v104, v226, s94\n"
    "v_mfma_f32_16x16x32_bf16 a[164:167], v[168:171], v[36:39], a[164:167]\n"
    "ds_read_b128 v[164:167], v201 offset:23552\n"
    "v_fma_f32 v105, v105, v226, s94\n"
    "v_mfma_f32_16x16x32_bf16 a[164:167], v[172:175], v[68:71], a[164:167]\n"
    "v_fma_f32 v106, v106, v226, s94\n"
    "v_fma_f32 v107, v107, v226, s94\n"
    "v_mfma_f32_16x16x32_bf16 a[160:163], v[168:171], v[32:35], a[160:163]\n"
    "v_cvt_pk_bf16_f32 v58, v104, v105\n"
    "v_cvt_pk_bf16_f32 v59, v106, v107\n"
    "v_mfma_f32_16x16x32_bf16 a[160:163], v[172:175], v[64:67], a[160:163]\n"
    "s_waitcnt lgkmcnt(2)\n"
    "buffer_store_dwordx4 v[56:59], v205, s[28:31], s63 offen offset:256 sc0 sc1\n"
    "v_accvgpr_read_b32 v108, a156\n"
    "v_accvgpr_read_b32 v109, a157\n"
    "v_accvgpr_read_b32 v110, a158\n"
    "v_mfma_f32_16x16x32_bf16 a[160:163], v[176:179], v[232:235], a[160:163]\n"
    "ds_read_b128 v[168:171], v201 offset:47104\n"
    "v_accvgpr_read_b32 v111, a159\n"
    "v_mfma_f32_16x16x32_bf16 a[160:163], v[180:183], v[80:83], a[160:163]\n"
    "v_fma_f32 v108, v108, v228, s94\n"
    "v_fma_f32 v109, v109, v228, s94\n"
    "v_mfma_f32_16x16x32_bf16 a[164:167], v[176:179], v[236:239], a[164:167]\n"
    "v_fma_f32 v110, v110, v228, s94\n"
    "v_fma_f32 v111, v111, v228, s94\n"
    "v_mfma_f32_16x16x32_bf16 a[164:167], v[180:183], v[84:87], a[164:167]\n"
    "v_cvt_pk_bf16_f32 v62, v108, v109\n"
    "v_cvt_pk_bf16_f32 v63, v110, v111\n"
    "v_mfma_f32_16x16x32_bf16 a[168:171], v[176:179], v[240:243], a[168:171]\n"
    "ds_read_b128 v[172:175], v201 offset:48128\n"
    "v_mfma_f32_16x16x32_bf16 a[168:171], v[180:183], v[88:91], a[168:171]\n"
    "buffer_store_dwordx4 v[60:63], v205, s[28:31], s64 offen offset:256 sc0 sc1\n"
    "v_mfma_f32_16x16x32_bf16 a[172:175], v[176:179], v[244:247], a[172:175]\n"
    "v_mfma_f32_16x16x32_bf16 a[172:175], v[180:183], v[92:95], a[172:175]\n"
    "s_waitcnt lgkmcnt(2)\n"
    "v_accvgpr_read_b32 v96, a160\n"
    "v_accvgpr_read_b32 v97, a161\n"
    "v_mfma_f32_16x16x32_bf16 a[176:179], v[160:163], v[128:131], a[176:179]\n"
    "ds_read_b128 v[176:179], v207 offset:22528\n"
    "v_accvgpr_read_b32 v98, a162\n"
    "v_mfma_f32_16x16x32_bf16 a[176:179], v[164:167], v[144:147], a[176:179]\n"
    "v_accvgpr_read_b32 v99, a163\n"
    "v_fma_f32 v96, v96, v222, s94\n"
    "v_mfma_f32_16x16x32_bf16 a[180:183], v[160:163], v[132:135], a[180:183]\n"
    "v_fma_f32 v97, v97, v222, s94\n"
    "v_fma_f32 v98, v98, v222, s94\n"
    "v_mfma_f32_16x16x32_bf16 a[180:183], v[164:167], v[148:151], a[180:183]\n"
    "v_fma_f32 v99, v99, v222, s94\n"
    "v_cvt_pk_bf16_f32 v184, v96, v97\n"
    "v_mfma_f32_16x16x32_bf16 a[184:187], v[160:163], v[136:139], a[184:187]\n"
    "ds_read_b128 v[180:183], v207 offset:23552\n"
    "v_cvt_pk_bf16_f32 v185, v98, v99\n"
    "v_mfma_f32_16x16x32_bf16 a[184:187], v[164:167], v[152:155], a[184:187]\n"
    "v_accvgpr_read_b32 v100, a164\n"
    "v_accvgpr_read_b32 v101, a165\n"
    "v_mfma_f32_16x16x32_bf16 a[188:191], v[160:163], v[140:143], a[188:191]\n"
    "v_accvgpr_read_b32 v102, a166\n"
    "v_accvgpr_read_b32 v103, a167\n"
    "v_mfma_f32_16x16x32_bf16 a[188:191], v[164:167], v[156:159], a[188:191]\n"
    "s_waitcnt lgkmcnt(2)\n"
    "v_fma_f32 v100, v100, v224, s94\n"
    "v_fma_f32 v101, v101, v224, s94\n"
    "v_mfma_f32_16x16x32_bf16 a[188:191], v[168:171], v[44:47], a[188:191]\n"
    "v_fma_f32 v102, v102, v224, s94\n"
    "v_fma_f32 v103, v103, v224, s94\n"
    "v_mfma_f32_16x16x32_bf16 a[188:191], v[172:175], v[76:79], a[188:191]\n"
    "v_cvt_pk_bf16_f32 v188, v100, v101\n"
    "v_cvt_pk_bf16_f32 v189, v102, v103\n"
    "v_mfma_f32_16x16x32_bf16 a[184:187], v[168:171], v[40:43], a[184:187]\n"
    "v_accvgpr_read_b32 v104, a168\n"
    "v_accvgpr_read_b32 v105, a169\n"
    "v_mfma_f32_16x16x32_bf16 a[184:187], v[172:175], v[72:75], a[184:187]\n"
    "v_accvgpr_read_b32 v106, a170\n"
    "v_accvgpr_read_b32 v107, a171\n"
    "v_mfma_f32_16x16x32_bf16 a[180:183], v[168:171], v[36:39], a[180:183]\n"
    "v_fma_f32 v104, v104, v226, s94\n"
    "v_fma_f32 v105, v105, v226, s94\n"
    "v_mfma_f32_16x16x32_bf16 a[180:183], v[172:175], v[68:71], a[180:183]\n"
    "v_fma_f32 v106, v106, v226, s94\n"
    "v_fma_f32 v107, v107, v226, s94\n"
    "v_mfma_f32_16x16x32_bf16 a[176:179], v[168:171], v[32:35], a[176:179]\n"
    "v_cvt_pk_bf16_f32 v248, v104, v105\n"
    "v_cvt_pk_bf16_f32 v249, v106, v107\n"
    "v_mfma_f32_16x16x32_bf16 a[176:179], v[172:175], v[64:67], a[176:179]\n"
    "s_waitcnt lgkmcnt(0)\n"
    "v_accvgpr_read_b32 v108, a172\n"
    "v_accvgpr_read_b32 v109, a173\n"
    "v_mfma_f32_16x16x32_bf16 a[176:179], v[176:179], v[232:235], a[176:179]\n"
    "v_accvgpr_read_b32 v110, a174\n"
    "v_accvgpr_read_b32 v111, a175\n"
    "v_mfma_f32_16x16x32_bf16 a[176:179], v[180:183], v[80:83], a[176:179]\n"
    "v_fma_f32 v108, v108, v228, s94\n"
    "v_fma_f32 v109, v109, v228, s94\n"
    "v_mfma_f32_16x16x32_bf16 a[180:183], v[176:179], v[236:239], a[180:183]\n"
    "v_fma_f32 v110, v110, v228, s94\n"
    "v_fma_f32 v111, v111, v228, s94\n"
    "v_mfma_f32_16x16x32_bf16 a[180:183], v[180:183], v[84:87], a[180:183]\n"
    "v_cvt_pk_bf16_f32 v252, v108, v109\n"
    "v_cvt_pk_bf16_f32 v253, v110, v111\n"
    "v_mfma_f32_16x16x32_bf16 a[184:187], v[176:179], v[240:243], a[184:187]\n"
    "v_mfma_f32_16x16x32_bf16 a[184:187], v[180:183], v[88:91], a[184:187]\n"
    "v_mfma_f32_16x16x32_bf16 a[188:191], v[176:179], v[244:247], a[188:191]\n"
    "v_mfma_f32_16x16x32_bf16 a[188:191], v[180:183], v[92:95], a[188:191]\n"
    "v_accvgpr_read_b32 v96, a176\n"
    "v_accvgpr_read_b32 v97, a177\n"
    "v_accvgpr_read_b32 v98, a178\n"
    "v_accvgpr_read_b32 v99, a179\n"
    "v_fma_f32 v96, v96, v222, s94\n"
    "v_fma_f32 v97, v97, v222, s94\n"
    "v_fma_f32 v98, v98, v222, s94\n"
    "v_fma_f32 v99, v99, v222, s94\n"
    "v_cvt_pk_bf16_f32 v186, v96, v97\n"
    "v_cvt_pk_bf16_f32 v187, v98, v99\n"
    "buffer_store_dwordx4 v[184:187], v205, s[28:31], 0 offen offset:320 sc0 sc1\n"
    "v_accvgpr_read_b32 v100, a180\n"
    "v_accvgpr_read_b32 v101, a181\n"
    "v_accvgpr_read_b32 v102, a182\n"
    "v_accvgpr_read_b32 v103, a183\n"
    "v_fma_f32 v100, v100, v224, s94\n"
    "v_fma_f32 v101, v101, v224, s94\n"
    "v_fma_f32 v102, v102, v224, s94\n"
    "v_fma_f32 v103, v103, v224, s94\n"
    "v_cvt_pk_bf16_f32 v190, v100, v101\n"
    "v_cvt_pk_bf16_f32 v191, v102, v103\n"
    "buffer_store_dwordx4 v[188:191], v205, s[28:31], s62 offen offset:320 sc0 sc1\n"
    "v_accvgpr_read_b32 v104, a184\n"
    "v_accvgpr_read_b32 v105, a185\n"
    "v_accvgpr_read_b32 v106, a186\n"
    "v_accvgpr_read_b32 v107, a187\n"
    "v_fma_f32 v104, v104, v226, s94\n"
    "v_fma_f32 v105, v105, v226, s94\n"
    "v_fma_f32 v106, v106, v226, s94\n"
    "v_fma_f32 v107, v107, v226, s94\n"
    "v_cvt_pk_bf16_f32 v250, v104, v105\n"
    "v_cvt_pk_bf16_f32 v251, v106, v107\n"
    "buffer_store_dwordx4 v[248:251], v205, s[28:31], s63 offen offset:320 sc0 sc1\n"
    "v_accvgpr_read_b32 v108, a188\n"
    "v_accvgpr_read_b32 v109, a189\n"
    "v_accvgpr_read_b32 v110, a190\n"
    "v_accvgpr_read_b32 v111, a191\n"
    "v_fma_f32 v108, v108, v228, s94\n"
    "v_fma_f32 v109, v109, v228, s94\n"
    "v_fma_f32 v110, v110, v228, s94\n"
    "v_fma_f32 v111, v111, v228, s94\n"
    "v_cvt_pk_bf16_f32 v254, v108, v109\n"
    "v_cvt_pk_bf16_f32 v255, v110, v111\n"
    "buffer_store_dwordx4 v[252:255], v205, s[28:31], s64 offen offset:320 sc0 sc1\n"
    "v_add_f32 v196, v112, v116\n"
    "v_add_f32 v197, v120, v124\n"
    "v_add_f32 v196, v196, v197\n"
    "v_cmp_ngt_f32 vcc, 0x45800000, v196\n"
    "s_barrier\n"
    "s_cbranch_vccnz LSLOW\n"
    "LFIN:\n"
    "s_branch LEND\n"
    "LSPRE0:\n"
    "LSPIN0:\n"
    "s_sleep 1\n"
    "buffer_load_dwordx2 v[218:219], v213, s[44:47], 0 offen sc1\n"
    "s_waitcnt vmcnt(0)\n"
    "s_add_u32 s73, s73, 1\n"
    "s_cmp_gt_u32 s73, 200000\n"
    "s_cbranch_scc1 LSPB0\n"
    "v_cmp_ne_u32 s[80:81], s67, v219\n"
    "v_cmp_gt_u32 vcc, s66, v218\n"
    "s_or_b64 vcc, vcc, s[80:81]\n"
    "s_cbranch_vccnz LSPIN0\n"
    "s_branch LSPB0\n"
    "LSPRE1:\n"
    "LSPIN1:\n"
    "s_sleep 1\n"
    "buffer_load_dwordx2 v[218:219], v213, s[44:47], 0 offen sc1\n"
    "s_waitcnt vmcnt(0)\n"
    "s_add_u32 s73, s73, 1\n"
    "s_cmp_gt_u32 s73, 200000\n"
    "s_cbranch_scc1 LSPB1\n"
    "v_cmp_ne_u32 s[80:81], s67, v219\n"
    "v_cmp_gt_u32 vcc, s66, v218\n"
    "s_or_b64 vcc, vcc, s[80:81]\n"
    "s_cbranch_vccnz LSPIN1\n"
    "s_branch LSPB1\n"
    "LSPRE2:\n"
    "LSPIN2:\n"
    "s_sleep 1\n"
    "buffer_load_dwordx2 v[218:219], v213, s[44:47], 0 offen sc1\n"
    "s_waitcnt vmcnt(0)\n"
    "s_add_u32 s73, s73, 1\n"
    "s_cmp_gt_u32 s73, 200000\n"
    "s_cbranch_scc1 LSPB2\n"
    "v_cmp_ne_u32 s[80:81], s67, v219\n"
    "v_cmp_gt_u32 vcc, s66, v218\n"
    "s_or_b64 vcc, vcc, s[80:81]\n"
    "s_cbranch_vccnz LSPIN2\n"
    "s_branch LSPB2\n"
    "LSPRE3:\n"
    "LSPIN3:\n"
    "s_sleep 1\n"
    "buffer_load_dwordx2 v[218:219], v213, s[44:47], 0 offen sc1\n"
    "s_waitcnt vmcnt(0)\n"
    "s_add_u32 s73, s73, 1\n"
    "s_cmp_gt_u32 s73, 200000\n"
    "s_cbranch_scc1 LSPB3\n"
    "v_cmp_ne_u32 s[80:81], s67, v219\n"
    "v_cmp_gt_u32 vcc, s66, v218\n"
    "s_or_b64 vcc, vcc, s[80:81]\n"
    "s_cbranch_vccnz LSPIN3\n"
    "s_branch LSPB3\n"
    "LSPRE4:\n"
    "LSPIN4:\n"
    "s_sleep 1\n"
    "buffer_load_dwordx2 v[218:219], v213, s[44:47], 0 offen sc1\n"
    "s_waitcnt vmcnt(0)\n"
    "s_add_u32 s73, s73, 1\n"
    "s_cmp_gt_u32 s73, 200000\n"
    "s_cbranch_scc1 LSPB4\n"
    "v_cmp_ne_u32 s[80:81], s67, v219\n"
    "v_cmp_gt_u32 vcc, s66, v218\n"
    "s_or_b64 vcc, vcc, s[80:81]\n"
    "s_cbranch_vccnz LSPIN4\n"
    "s_branch LSPB4\n"
    "LSPRE5:\n"
    "LSPIN5:\n"
    "s_sleep 1\n"
    "buffer_load_dwordx2 v[218:219], v213, s[44:47], 0 offen sc1\n"
    "s_waitcnt vmcnt(0)\n"
    "s_add_u32 s73, s73, 1\n"
    "s_cmp_gt_u32 s73, 200000\n"
    "s_cbranch_scc1 LSPB5\n"
    "v_cmp_ne_u32 s[80:81], s67, v219\n"
    "v_cmp_gt_u32 vcc, s66, v218\n"
    "s_or_b64 vcc, vcc, s[80:81]\n"
    "s_cbranch_vccnz LSPIN5\n"
    "s_branch LSPB5\n"
    "LSLOW:\n"
    "s_waitcnt vmcnt(0)\n"
    "s_nop 15\n"
    "s_mul_i32 s62, s58, 0x9000\n"
    "v_add_u32 v205, s62, v200\n"
    "s_mov_b32 s63, 0\n"
    "v_mov_b32 v128, 0\n"
    "v_mov_b32 v129, 0\n"
    "v_mov_b32 v130, 0\n"
    "v_mov_b32 v131, 0\n"
    "v_mov_b32 v96, 0xff61b1e6\n"
    "v_mov_b32 v100, 0xff61b1e6\n"
    "v_mov_b32 v104, 0xff61b1e6\n"
    "v_mov_b32 v108, 0xff61b1e6\n"
    "v_mfma_f32_16x16x32_bf16 a[0:3], v[128:131], v[128:131], 0\n"
    "v_mfma_f32_16x16x32_bf16 a[4:7], v[128:131], v[128:131], 0\n"
    "v_mfma_f32_16x16x32_bf16 a[8:11], v[128:131], v[128:131], 0\n"
    "v_mfma_f32_16x16x32_bf16 a[12:15], v[128:131], v[128:131], 0\n"
    "v_mfma_f32_16x16x32_bf16 a[16:19], v[128:131], v[128:131], 0\n"
    "v_mfma_f32_16x16x32_bf16 a[20:23], v[128:131], v[128:131], 0\n"
    "v_mfma_f32_16x16x32_bf16 a[24:27], v[128:131], v[128:131], 0\n"
    "v_mfma_f32_16x16x32_bf16 a[28:31], v[128:131], v[128:131], 0\n"
    "v_mfma_f32_16x16x32_bf16 a[32:35], v[128:131], v[128:131], 0\n"
    "v_mfma_f32_16x16x32_bf16 a[36:39], v[128:131], v[128:131], 0\n"
    "v_mfma_f32_16x16x32_bf16 a[40:43], v[128:131], v[128:131], 0\n"
    "v_mfma_f32_16x16x32_bf16 a[44:47], v[128:131], v[128:131], 0\n"
    "v_mfma_f32_16x16x32_bf16 a[48:51], v[128:131], v[128:131], 0\n"
    "v_mfma_f32_16x16x32_bf16 a[52:55], v[128:131], v[128:131], 0\n"
    "v_mfma_f32_16x16x32_bf16 a[56:59], v[128:131], v[128:131], 0\n"
    "v_mfma_f32_16x16x32_bf16 a[60:63], v[128:131], v[128:131], 0\n"
    "v_mfma_f32_16x16x32_bf16 a[64:67], v[128:131], v[128:131], 0\n"
    "v_mfma_f32_16x16x32_bf16 a[68:71], v[128:131], v[128:131], 0\n"
    "v_mfma_f32_16x16x32_bf16 a[72:75], v[128:131], v[128:131], 0\n"
    "v_mfma_f32_16x16x32_bf16 a[76:79], v[128:131], v[128:131], 0\n"
    "v_mfma_f32_16x16x32_bf16 a[80:83], v[128:131], v[128:131], 0\n"
    "v_mfma_f32_16x16x32_bf16 a[84:87], v[128:131], v[128:131], 0\n"
    "v_mfma_f32_16x16x32_bf16 a[88:91], v[128:131], v[128:131], 0\n"
    "v_mfma_f32_16x16x32_bf16 a[92:95], v[128:131], v[128:131], 0\n"
    "v_mfma_f32_16x16x32_bf16 a[96:99], v[128:131], v[128:131], 0\n"
    "v_mfma_f32_16x16x32_bf16 a[100:103], v[128:131], v[128:131], 0\n"
    "v_mfma_f32_16x16x32_bf16 a[104:107], v[128:131], v[128:131], 0\n"
    "v_mfma_f32_16x16x32_bf16 a[108:111], v[128:131], v[128:131], 0\n"
    "v_mfma_f32_16x16x32_bf16 a[112:115], v[128:131], v[128:131], 0\n"
    "v_mfma_f32_16x16x32_bf16 a[116:119], v[128:131], v[128:131], 0\n"
    "v_mfma_f32_16x16x32_bf16 a[120:123], v[128:131], v[128:131], 0\n"
    "v_mfma_f32_16x16x32_bf16 a[124:127], v[128:131], v[128:131], 0\n"
    "v_mfma_f32_16x16x32_bf16 a[128:131], v[128:131], v[128:131], 0\n"
    "v_mfma_f32_16x16x32_bf16 a[132:135], v[128:131], v[128:131], 0\n"
    "v_mfma_f32_16x16x32_bf16 a[136:139], v[128:131], v[128:131], 0\n"
    "v_mfma_f32_16x16x32_bf16 a[140:143], v[128:131], v[128:131], 0\n"
    "v_mfma_f32_16x16x32_bf16 a[144:147], v[128:131], v[128:131], 0\n"
    "v_mfma_f32_16x16x32_bf16 a[148:151], v[128:131], v[128:131], 0\n"
    "v_mfma_f32_16x16x32_bf16 a[152:155], v[128:131], v[128:131], 0\n"
    "v_mfma_f32_16x16x32_bf16 a[156:159], v[128:131], v[128:131], 0\n"
    "v_mfma_f32_16x16x32_bf16 a[160:163], v[128:131], v[128:131], 0\n"
    "v_mfma_f32_16x16x32_bf16 a[164:167], v[128:131], v[128:131], 0\n"
    "v_mfma_f32_16x16x32_bf16 a[168:171], v[128:131], v[128:131], 0\n"
    "v_mfma_f32_16x16x32_bf16 a[172:175], v[128:131], v[128:131], 0\n"
    "v_mfma_f32_16x16x32_bf16 a[176:179], v[128:131], v[128:131], 0\n"
    "v_mfma_f32_16x16x32_bf16 a[180:183], v[128:131], v[128:131], 0\n"
    "v_mfma_f32_16x16x32_bf16 a[184:187], v[128:131], v[128:131], 0\n"
    "v_mfma_f32_16x16x32_bf16 a[188:191], v[128:131], v[128:131], 0\n"
    "v_mfma_f32_16x16x32_bf16 v[112:115], v[128:131], v[128:131], 0\n"
    "v_mfma_f32_16x16x32_bf16 v[116:119], v[128:131], v[128:131], 0\n"
    "v_mfma_f32_16x16x32_bf16 v[120:123], v[128:131], v[128:131], 0\n"
    "v_mfma_f32_16x16x32_bf16 v[124:127], v[128:131], v[128:131], 0\n"
    "s_nop 15\n"
    "LSLOOP:\n"
    "s_add_u32 s78, s63, 0\n"
    "s_add_u32 m0, s62, 0\n"
    "s_nop 0\n"
    "buffer_load_dwordx4 v200, s[36:39], s78 offen sc1 lds\n"
    "buffer_load_dwordx4 v200, s[36:39], s78 offen offset:1024 sc1 lds\n"
    "buffer_load_dwordx4 v200, s[36:39], s78 offen offset:2048 sc1 lds\n"
    "buffer_load_dwordx4 v200, s[36:39], s78 offen offset:3072 sc1 lds\n"
    "s_add_u32 s78, s63, 0x1000\n"
    "s_add_u32 m0, s62, 0x1000\n"
    "s_nop 0\n"
    "buffer_load_dwordx4 v200, s[36:39], s78 offen sc1 lds\n"
    "buffer_load_dwordx4 v200, s[36:39], s78 offen offset:1024 sc1 lds\n"
    "buffer_load_dwordx4 v200, s[36:39], s78 offen offset:2048 sc1 lds\n"
    "buffer_load_dwordx4 v200, s[36:39], s78 offen offset:3072 sc1 lds\n"
    "s_add_u32 s78, s63, 0x2000\n"
    "s_add_u32 m0, s62, 0x2000\n"
    "s_nop 0\n"
    "buffer_load_dwordx4 v200, s[36:39], s78 offen sc1 lds\n"
    "buffer_load_dwordx4 v200, s[36:39], s78 offen offset:1024 sc1 lds\n"
    "buffer_load_dwordx4 v200, s[36:39], s78 offen offset:2048 sc1 lds\n"
    "buffer_load_dwordx4 v200, s[36:39], s78 offen offset:3072 sc1 lds\n"
    "s_add_u32 s78, s63, 0\n"
    "s_add_u32 m0, s62, 0x3000\n"
    "s_nop 0\n"
    "buffer_load_dwordx4 v200, s[40:43], s78 offen sc1 lds\n"
    "buffer_load_dwordx4 v200, s[40:43], s78 offen offset:1024 sc1 lds\n"
    "buffer_load_dwordx4 v200, s[40:43], s78 offen offset:2048 sc1 lds\n"
    "buffer_load_dwordx4 v200, s[40:43], s78 offen offset:3072 sc1 lds\n"
    "s_add_u32 s78, s63, 0x1000\n"
    "s_add_u32 m0, s62, 0x4000\n"
    "s_nop 0\n"
    "buffer_load_dwordx4 v200, s[40:43], s78 offen sc1 lds\n"
    "buffer_load_dwordx4 v200, s[40:43], s78 offen offset:1024 sc1 lds\n"
    "buffer_load_dwordx4 v200, s[40:43], s78 offen offset:2048 sc1 lds\n"
    "buffer_load_dwordx4 v200, s[40:43], s78 offen offset:3072 sc1 lds\n"
    "s_add_u32 s78, s63, 0x2000\n"
    "s_add_u32 m0, s62, 0x5000\n"
    "s_nop 0\n"
    "buffer_load_dwordx4 v200, s[40:43], s78 offen sc1 lds\n"
    "buffer_load_dwordx4 v200, s[40:43], s78 offen offset:1024 sc1 lds\n"
    "buffer_load_dwordx4 v200, s[40:43], s78 offen offset:2048 sc1 lds\n"
    "buffer_load_dwordx4 v200, s[40:43], s78 offen offset:3072 sc1 lds\n"
    "s_add_u32 s78, s63, 0x3000\n"
    "s_add_u32 m0, s62, 0x6000\n"
    "s_nop 0\n"
    "buffer_load_dwordx4 v200, s[40:43], s78 offen sc1 lds\n"
    "buffer_load_dwordx4 v200, s[40:43], s78 offen offset:1024 sc1 lds\n"
    "buffer_load_dwordx4 v200, s[40:43], s78 offen offset:2048 sc1 lds\n"
    "buffer_load_dwordx4 v200, s[40:43], s78 offen offset:3072 sc1 lds\n"
    "s_add_u32 s78, s63, 0x4000\n"
    "s_add_u32 m0, s62, 0x7000\n"
    "s_nop 0\n"
    "buffer_load_dwordx4 v200, s[40:43], s78 offen sc1 lds\n"
    "buffer_load_dwordx4 v200, s[40:43], s78 offen offset:1024 sc1 lds\n"
    "buffer_load_dwordx4 v200, s[40:43], s78 offen offset:2048 sc1 lds\n"
    "buffer_load_dwordx4 v200, s[40:43], s78 offen offset:3072 sc1 lds\n"
    "s_add_u32 s78, s63, 0x5000\n"
    "s_add_u32 m0, s62, 0x8000\n"
    "s_nop 0\n"
    "buffer_load_dwordx4 v200, s[40:43], s78 offen sc1 lds\n"
    "buffer_load_dwordx4 v200, s[40:43], s78 offen offset:1024 sc1 lds\n"
    "buffer_load_dwordx4 v200, s[40:43], s78 offen offset:2048 sc1 lds\n"
    "buffer_load_dwordx4 v200, s[40:43], s78 offen offset:3072 sc1 lds\n"
    "s_waitcnt vmcnt(0)\n"
    "ds_read_b128 v[160:163], v205\n"
    "ds_read_b128 v[164:167], v205 offset:1024\n"
    "s_waitcnt lgkmcnt(1)\n"
    "v_mfma_f32_16x16x32_bf16 v[32:35], v[160:163], a[192:195], 0\n"
    "s_waitcnt lgkmcnt(0)\n"
    "v_mfma_f32_16x16x32_bf16 v[32:35], v[164:167], a[196:199], v[32:35]\n"
    "v_mfma_f32_16x16x32_bf16 v[36:39], v[164:167], a[220:223], 0\n"
    "v_mfma_f32_16x16x32_bf16 v[36:39], v[160:163], a[216:219], v[36:39]\n"
    "v_mfma_f32_16x16x32_bf16 v[40:43], v[160:163], a[240:243], 0\n"
    "v_mfma_f32_16x16x32_bf16 v[40:43], v[164:167], a[244:247], v[40:43]\n"
    "v_mfma_f32_16x16x32_bf16 v[44:47], v[164:167], v[12:15], 0\n"
    "v_mfma_f32_16x16x32_bf16 v[44:47], v[160:163], v[8:11], v[44:47]\n"
    "ds_read_b128 v[168:171], v205 offset:2048\n"
    "ds_read_b128 v[172:175], v205 offset:3072\n"
    "s_waitcnt lgkmcnt(1)\n"
    "v_mfma_f32_16x16x32_bf16 v[44:47], v[168:171], v[16:19], v[44:47]\n"
    "s_waitcnt lgkmcnt(0)\n"
    "v_mfma_f32_16x16x32_bf16 v[44:47], v[172:175], v[20:23], v[44:47]\n"
    "v_mfma_f32_16x16x32_bf16 v[40:43], v[172:175], a[252:255], v[40:43]\n"
    "v_mfma_f32_16x16x32_bf16 v[40:43], v[168:171], a[248:251], v[40:43]\n"
    "v_mfma_f32_16x16x32_bf16 v[36:39], v[168:171], a[224:227], v[36:39]\n"
    "v_mfma_f32_16x16x32_bf16 v[36:39], v[172:175], a[228:231], v[36:39]\n"
    "v_mfma_f32_16x16x32_bf16 v[32:35], v[172:175], a[204:207], v[32:35]\n"
    "v_mfma_f32_16x16x32_bf16 v[32:35], v[168:171], a[200:203], v[32:35]\n"
    "ds_read_b128 v[176:179], v205 offset:4096\n"
    "ds_read_b128 v[180:183], v205 offset:5120\n"
    "s_waitcnt lgkmcnt(1)\n"
    "v_mfma_f32_16x16x32_bf16 v[32:35], v[176:179], a[208:211], v[32:35]\n"
    "s_waitcnt lgkmcnt(0)\n"
    "v_mfma_f32_16x16x32_bf16 v[32:35], v[180:183], a[212:215], v[32:35]\n"
    "v_mfma_f32_16x16x32_bf16 v[36:39], v[180:183], a[236:239], v[36:39]\n"
    "v_mfma_f32_16x16x32_bf16 v[36:39], v[176:179], a[232:235], v[36:39]\n"
    "v_mfma_f32_16x16x32_bf16 v[40:43], v[176:179], v[0:3], v[40:43]\n"
    "v_mfma_f32_16x16x32_bf16 v[40:43], v[180:183], v[4:7], v[40:43]\n"
    "v_mfma_f32_16x16x32_bf16 v[44:47], v[180:183], v[28:31], v[44:47]\n"
    "v_mfma_f32_16x16x32_bf16 v[44:47], v[176:179], v[24:27], v[44:47]\n"
    "ds_read_b128 v[160:163], v205 offset:10240\n"
    "ds_read_b128 v[164:167], v205 offset:11264\n"
    "s_waitcnt lgkmcnt(1)\n"
    "v_mfma_f32_16x16x32_bf16 v[60:63], v[160:163], v[24:27], 0\n"
    "s_waitcnt lgkmcnt(0)\n"
    "v_mfma_f32_16x16x32_bf16 v[60:63], v[164:167], v[28:31], v[60:63]\n"
    "v_mfma_f32_16x16x32_bf16 v[56:59], v[164:167], v[4:7], 0\n"
    "v_mfma_f32_16x16x32_bf16 v[56:59], v[160:163], v[0:3], v[56:59]\n"
    "v_mfma_f32_16x16x32_bf16 v[52:55], v[160:163], a[232:235], 0\n"
    "v_mfma_f32_16x16x32_bf16 v[52:55], v[164:167], a[236:239], v[52:55]\n"
    "v_mfma_f32_16x16x32_bf16 v[48:51], v[164:167], a[212:215], 0\n"
    "v_mfma_f32_16x16x32_bf16 v[48:51], v[160:163], a[208:211], v[48:51]\n"
    "ds_read_b128 v[168:171], v205 offset:8192\n"
    "ds_read_b128 v[172:175], v205 offset:9216\n"
    "s_waitcnt lgkmcnt(1)\n"
    "v_mfma_f32_16x16x32_bf16 v[48:51], v[168:171], a[200:203], v[48:51]\n"
    "s_waitcnt lgkmcnt(0)\n"
    "v_mfma_f32_16x16x32_bf16 v[48:51], v[172:175], a[204:207], v[48:51]\n"
    "v_mfma_f32_16x16x32_bf16 v[52:55], v[172:175], a[228:231], v[52:55]\n"
    "v_mfma_f32_16x16x32_bf16 v[52:55], v[168:171], a[224:227], v[52:55]\n"
    "v_mfma_f32_16x16x32_bf16 v[56:59], v[168:171], a[248:251], v[56:59]\n"
    "v_mfma_f32_16x16x32_bf16 v[56:59], v[172:175], a[252:255], v[56:59]\n"
    "v_mfma_f32_16x16x32_bf16 v[60:63], v[172:175], v[20:23], v[60:63]\n"
    "v_mfma_f32_16x16x32_bf16 v[60:63], v[168:171], v[16:19], v[60:63]\n"
    "ds_read_b128 v[176:179], v205 offset:6144\n"
    "ds_read_b128 v[180:183], v205 offset:7168\n"
    "s_waitcnt lgkmcnt(1)\n"
    "v_mfma_f32_16x16x32_bf16 v[60:63], v[176:179], v[8:11], v[60:63]\n"
    "s_waitcnt lgkmcnt(0)\n"
    "v_mfma_f32_16x16x32_bf16 v[60:63], v[180:183], v[12:15], v[60:63]\n"
    "v_mfma_f32_16x16x32_bf16 v[56:59], v[180:183], a[244:247], v[56:59]\n"
    "v_mfma_f32_16x16x32_bf16 v[56:59], v[176:179], a[240:243], v[56:59]\n"
    "v_mfma_f32_16x16x32_bf16 v[52:55], v[176:179], a[216:219], v[52:55]\n"
    "v_mfma_f32_16x16x32_bf16 v[52:55], v[180:183], a[220:223], v[52:55]\n"
    "v_mfma_f32_16x16x32_bf16 v[48:51], v[180:183], a[196:199], v[48:51]\n"
    "v_mfma_f32_16x16x32_bf16 v[48:51], v[176:179], a[192:195], v[48:51]\n"
    "s_add_u32 s78, s63, 0x3000\n"
    "s_add_u32 m0, s62, 0\n"
    "s_nop 0\n"
    "buffer_load_dwordx4 v200, s[36:39], s78 offen sc1 lds\n"
    "buffer_load_dwordx4 v200, s[36:39], s78 offen offset:1024 sc1 lds\n"
    "buffer_load_dwordx4 v200, s[36:39], s78 offen offset:2048 sc1 lds\n"
    "buffer_load_dwordx4 v200, s[36:39], s78 offen offset:3072 sc1 lds\n"
    "s_add_u32 s78, s63, 0x4000\n"
    "s_add_u32 m0, s62, 0x1000\n"
    "s_nop 0\n"
    "buffer_load_dwordx4 v200, s[36:39], s78 offen sc1 lds\n"
    "buffer_load_dwordx4 v200, s[36:39], s78 offen offset:1024 sc1 lds\n"
    "buffer_load_dwordx4 v200, s[36:39], s78 offen offset:2048 sc1 lds\n"
    "buffer_load_dwordx4 v200, s[36:39], s78 offen offset:3072 sc1 lds\n"
    "s_add_u32 s78, s63, 0x5000\n"
    "s_add_u32 m0, s62, 0x2000\n"
    "s_nop 0\n"
    "buffer_load_dwordx4 v200, s[36:39], s78 offen sc1 lds\n"
    "buffer_load_dwordx4 v200, s[36:39], s78 offen offset:1024 sc1 lds\n"
    "buffer_load_dwordx4 v200, s[36:39], s78 offen offset:2048 sc1 lds\n"
    "buffer_load_dwordx4 v200, s[36:39], s78 offen offset:3072 sc1 lds\n"
    "s_waitcnt vmcnt(0)\n"
    "ds_read_b128 v[160:163], v205\n"
    "ds_read_b128 v[164:167], v205 offset:1024\n"
    "s_waitcnt lgkmcnt(1)\n"
    "v_mfma_f32_16x16x32_bf16 v[64:67], v[160:163], a[192:195], 0\n"
    "s_waitcnt lgkmcnt(0)\n"
    "v_mfma_f32_16x16x32_bf16 v[64:67], v[164:167], a[196:199], v[64:67]\n"
    "v_mfma_f32_16x16x32_bf16 v[68:71], v[164:167], a[220:223], 0\n"
    "v_mfma_f32_16x16x32_bf16 v[68:71], v[160:163], a[216:219], v[68:71]\n"
    "v_mfma_f32_16x16x32_bf16 v[72:75], v[160:163], a[240:243], 0\n"
    "v_mfma_f32_16x16x32_bf16 v[72:75], v[164:167], a[244:247], v[72:75]\n"
    "v_mfma_f32_16x16x32_bf16 v[76:79], v[164:167], v[12:15], 0\n"
    "v_mfma_f32_16x16x32_bf16 v[76:79], v[160:163], v[8:11], v[76:79]\n"
    "ds_read_b128 v[168:171], v205 offset:2048\n"
    "ds_read_b128 v[172:175], v205 offset:3072\n"
    "s_waitcnt lgkmcnt(1)\n"
    "v_mfma_f32_16x16x32_bf16 v[76:79], v[168:171], v[16:19], v[76:79]\n"
    "s_waitcnt lgkmcnt(0)\n"
    "v_mfma_f32_16x16x32_bf16 v[76:79], v[172:175], v[20:23], v[76:79]\n"
    "v_mfma_f32_16x16x32_bf16 v[72:75], v[172:175], a[252:255], v[72:75]\n"
    "v_mfma_f32_16x16x32_bf16 v[72:75], v[168:171], a[248:251], v[72:75]\n"
    "v_mfma_f32_16x16x32_bf16 v[68:71], v[168:171], a[224:227], v[68:71]\n"
    "v_mfma_f32_16x16x32_bf16 v[68:71], v[172:175], a[228:231], v[68:71]\n"
    "v_mfma_f32_16x16x32_bf16 v[64:67], v[172:175], a[204:207], v[64:67]\n"
    "v_mfma_f32_16x16x32_bf16 v[64:67], v[168:171], a[200:203], v[64:67]\n"
    "ds_read_b128 v[176:179], v205 offset:4096\n"
    "ds_read_b128 v[180:183], v205 offset:5120\n"
    "s_waitcnt lgkmcnt(1)\n"
    "v_mfma_f32_16x16x32_bf16 v[64:67], v[176:179], a[208:211], v[64:67]\n"
    "s_waitcnt lgkmcnt(0)\n"
    "v_mfma_f32_16x16x32_bf16 v[64:67], v[180:183], a[212:215], v[64:67]\n"
    "v_mfma_f32_16x16x32_bf16 v[68:71], v[180:183], a[236:239], v[68:71]\n"
    "v_mfma_f32_16x16x32_bf16 v[68:71], v[176:179], a[232:235], v[68:71]\n"
    "v_mfma_f32_16x16x32_bf16 v[72:75], v[176:179], v[0:3], v[72:75]\n"
    "v_mfma_f32_16x16x32_bf16 v[72:75], v[180:183], v[4:7], v[72:75]\n"
    "v_mfma_f32_16x16x32_bf16 v[76:79], v[180:183], v[28:31], v[76:79]\n"
    "v_mfma_f32_16x16x32_bf16 v[76:79], v[176:179], v[24:27], v[76:79]\n"
    "ds_read_b128 v[160:163], v205 offset:10240\n"
    "ds_read_b128 v[164:167], v205 offset:11264\n"
    "s_waitcnt lgkmcnt(1)\n"
    "v_mfma_f32_16x16x32_bf16 v[92:95], v[160:163], v[24:27], 0\n"
    "s_waitcnt lgkmcnt(0)\n"
    "v_mfma_f32_16x16x32_bf16 v[92:95], v[164:167], v[28:31], v[92:95]\n"
    "v_mfma_f32_16x16x32_bf16 v[88:91], v[164:167], v[4:7], 0\n"
    "v_mfma_f32_16x16x32_bf16 v[88:91], v[160:163], v[0:3], v[88:91]\n"
    "v_mfma_f32_16x16x32_bf16 v[84:87], v[160:163], a[232:235], 0\n"
    "v_mfma_f32_16x16x32_bf16 v[84:87], v[164:167], a[236:239], v[84:87]\n"
    "v_mfma_f32_16x16x32_bf16 v[80:83], v[164:167], a[212:215], 0\n"
    "v_mfma_f32_16x16x32_bf16 v[80:83], v[160:163], a[208:211], v[80:83]\n"
    "ds_read_b128 v[168:171], v205 offset:8192\n"
    "ds_read_b128 v[172:175], v205 offset:9216\n"
    "s_waitcnt lgkmcnt(1)\n"
    "v_mfma_f32_16x16x32_bf16 v[80:83], v[168:171], a[200:203], v[80:83]\n"
    "s_waitcnt lgkmcnt(0)\n"
    "v_mfma_f32_16x16x32_bf16 v[80:83], v[172:175], a[204:207], v[80:83]\n"
    "v_mfma_f32_16x16x32_bf16 v[84:87], v[172:175], a[228:231], v[84:87]\n"
    "v_mfma_f32_16x16x32_bf16 v[84:87], v[168:171], a[224:227], v[84:87]\n"
    "v_mfma_f32_16x16x32_bf16 v[88:91], v[168:171], a[248:251], v[88:91]\n"
    "v_mfma_f32_16x16x32_bf16 v[88:91], v[172:175], a[252:255], v[88:91]\n"
    "v_mfma_f32_16x16x32_bf16 v[92:95], v[172:175], v[20:23], v[92:95]\n"
    "v_mfma_f32_16x16x32_bf16 v[92:95], v[168:171], v[16:19], v[92:95]\n"
    "ds_read_b128 v[176:179], v205 offset:6144\n"
    "ds_read_b128 v[180:183], v205 offset:7168\n"
    "s_waitcnt lgkmcnt(1)\n"
    "v_mfma_f32_16x16x32_bf16 v[92:95], v[176:179], v[8:11], v[92:95]\n"
    "s_waitcnt lgkmcnt(0)\n"
    "v_mfma_f32_16x16x32_bf16 v[92:95], v[180:183], v[12:15], v[92:95]\n"
    "v_mfma_f32_16x16x32_bf16 v[88:91], v[180:183], a[244:247], v[88:91]\n"
    "v_mfma_f32_16x16x32_bf16 v[88:91], v[176:179], a[240:243], v[88:91]\n"
    "v_mfma_f32_16x16x32_bf16 v[84:87], v[176:179], a[216:219], v[84:87]\n"
    "v_mfma_f32_16x16x32_bf16 v[84:87], v[180:183], a[220:223], v[84:87]\n"
    "v_mfma_f32_16x16x32_bf16 v[80:83], v[180:183], a[196:199], v[80:83]\n"
    "v_mfma_f32_16x16x32_bf16 v[80:83], v[176:179], a[192:195], v[80:83]\n"
    "v_max3_f32 v196, v32, v33, v34\n"
    "v_max3_f32 v196, v196, v35, v48\n"
    "v_max3_f32 v196, v196, v49, v50\n"
    "v_max3_f32 v196, v196, v51, v64\n"
    "v_max3_f32 v196, v196, v65, v66\n"
    "s_nop 2\n"
    "v_max3_f32 v196, v196, v67, v80\n"
    "v_max3_f32 v196, v196, v81, v82\n"
    "v_max_f32 v196, v196, v83\n"
    "v_mov_b32 v197, v196\n"
    "s_nop 1\n"
    "v_permlane32_swap_b32 v196, v197\n"
    "v_max_f32 v196, v196, v197\n"
    "v_mov_b32 v197, v196\n"
    "s_nop 1\n"
    "v_permlane16_swap_b32 v196, v197\n"
    "v_max_f32 v196, v196, v197\n"
    "v_max_f32 v196, v196, v96\n"
    "v_sub_f32 v197, v96, v196\n"
    "v_mov_b32 v96, v196\n"
    "v_exp_f32 v197, v197\n"
    "v_sub_f32 v32, v32, v196\n"
    "v_sub_f32 v33, v33, v196\n"
    "v_sub_f32 v34, v34, v196\n"
    "v_sub_f32 v35, v35, v196\n"
    "v_sub_f32 v48, v48, v196\n"
    "v_sub_f32 v49, v49, v196\n"
    "v_sub_f32 v50, v50, v196\n"
    "v_sub_f32 v51, v51, v196\n"
    "v_sub_f32 v64, v64, v196\n"
    "v_sub_f32 v65, v65, v196\n"
    "v_sub_f32 v66, v66, v196\n"
    "v_sub_f32 v67, v67, v196\n"
    "v_sub_f32 v80, v80, v196\n"
    "v_sub_f32 v81, v81, v196\n"
    "v_sub_f32 v82, v82, v196\n"
    "v_sub_f32 v83, v83, v196\n"
    "v_mul_f32 v112, v112, v197\n"
    "v_mul_f32 v113, v113, v197\n"
    "v_mul_f32 v114, v114, v197\n"
    "v_mul_f32 v115, v115, v197\n"
    "v_accvgpr_read_b32 v198, a0\n"
    "v_accvgpr_read_b32 v199, a1\n"
    "v_mul_f32 v198, v198, v197\n"
    "v_mul_f32 v199, v199, v197\n"
    "v_accvgpr_write_b32 a0, v198\n"
    "v_accvgpr_write_b32 a1, v199\n"
    "v_accvgpr_read_b32 v198, a2\n"
    "v_accvgpr_read_b32 v199, a3\n"
    "v_mul_f32 v198, v198, v197\n"
    "v_mul_f32 v199, v199, v197\n"
    "v_accvgpr_write_b32 a2, v198\n"
    "v_accvgpr_write_b32 a3, v199\n"
    "v_accvgpr_read_b32 v198, a16\n"
    "v_accvgpr_read_b32 v199, a17\n"
    "v_mul_f32 v198, v198, v197\n"
    "v_mul_f32 v199, v199, v197\n"
    "v_accvgpr_write_b32 a16, v198\n"
    "v_accvgpr_write_b32 a17, v199\n"
    "v_accvgpr_read_b32 v198, a18\n"
    "v_accvgpr_read_b32 v199, a19\n"
    "v_mul_f32 v198, v198, v197\n"
    "v_mul_f32 v199, v199, v197\n"
    "v_accvgpr_write_b32 a18, v198\n"
    "v_accvgpr_write_b32 a19, v199\n"
    "v_accvgpr_read_b32 v198, a32\n"
    "v_accvgpr_read_b32 v199, a33\n"
    "v_mul_f32 v198, v198, v197\n"
    "v_mul_f32 v199, v199, v197\n"
    "v_accvgpr_write_b32 a32, v198\n"
    "v_accvgpr_write_b32 a33, v199\n"
    "v_accvgpr_read_b32 v198, a34\n"
    "v_accvgpr_read_b32 v199, a35\n"
    "v_mul_f32 v198, v198, v197\n"
    "v_mul_f32 v199, v199, v197\n"
    "v_accvgpr_write_b32 a34, v198\n"
    "v_accvgpr_write_b32 a35, v199\n"
    "v_accvgpr_read_b32 v198, a48\n"
    "v_accvgpr_read_b32 v199, a49\n"
    "v_mul_f32 v198, v198, v197\n"
    "v_mul_f32 v199, v199, v197\n"
    "v_accvgpr_write_b32 a48, v198\n"
    "v_accvgpr_write_b32 a49, v199\n"
    "v_accvgpr_read_b32 v198, a50\n"
    "v_accvgpr_read_b32 v199, a51\n"
    "v_mul_f32 v198, v198, v197\n"
    "v_mul_f32 v199, v199, v197\n"
    "v_accvgpr_write_b32 a50, v198\n"
    "v_accvgpr_write_b32 a51, v199\n"
    "v_accvgpr_read_b32 v198, a64\n"
    "v_accvgpr_read_b32 v199, a65\n"
    "v_mul_f32 v198, v198, v197\n"
    "v_mul_f32 v199, v199, v197\n"
    "v_accvgpr_write_b32 a64, v198\n"
    "v_accvgpr_write_b32 a65, v199\n"
    "v_accvgpr_read_b32 v198, a66\n"
    "v_accvgpr_read_b32 v199, a67\n"
    "v_mul_f32 v198, v198, v197\n"
    "v_mul_f32 v199, v199, v197\n"
    "v_accvgpr_write_b32 a66, v198\n"
    "v_accvgpr_write_b32 a67, v199\n"
    "v_accvgpr_read_b32 v198, a80\n"
    "v_accvgpr_read_b32 v199, a81\n"
    "v_mul_f32 v198, v198, v197\n"
    "v_mul_f32 v199, v199, v197\n"
    "v_accvgpr_write_b32 a80, v198\n"
    "v_accvgpr_write_b32 a81, v199\n"
    "v_accvgpr_read_b32 v198, a82\n"
    "v_accvgpr_read_b32 v199, a83\n"
    "v_mul_f32 v198, v198, v197\n"
    "v_mul_f32 v199, v199, v197\n"
    "v_accvgpr_write_b32 a82, v198\n"
    "v_accvgpr_write_b32 a83, v199\n"
    "v_accvgpr_read_b32 v198, a96\n"
    "v_accvgpr_read_b32 v199, a97\n"
    "v_mul_f32 v198, v198, v197\n"
    "v_mul_f32 v199, v199, v197\n"
    "v_accvgpr_write_b32 a96, v198\n"
    "v_accvgpr_write_b32 a97, v199\n"
    "v_accvgpr_read_b32 v198, a98\n"
    "v_accvgpr_read_b32 v199, a99\n"
    "v_mul_f32 v198, v198, v197\n"
    "v_mul_f32 v199, v199, v197\n"
    "v_accvgpr_write_b32 a98, v198\n"
    "v_accvgpr_write_b32 a99, v199\n"
    "v_accvgpr_read_b32 v198, a112\n"
    "v_accvgpr_read_b32 v199, a113\n"
    "v_mul_f32 v198, v198, v197\n"
    "v_mul_f32 v199, v199, v197\n"
    "v_accvgpr_write_b32 a112, v198\n"
    "v_accvgpr_write_b32 a113, v199\n"
    "v_accvgpr_read_b32 v198, a114\n"
    "v_accvgpr_read_b32 v199, a115\n"
    "v_mul_f32 v198, v198, v197\n"
    "v_mul_f32 v199, v199, v197\n"
    "v_accvgpr_write_b32 a114, v198\n"
    "v_accvgpr_write_b32 a115, v199\n"
    "v_accvgpr_read_b32 v198, a128\n"
    "v_accvgpr_read_b32 v199, a129\n"
    "v_mul_f32 v198, v198, v197\n"
    "v_mul_f32 v199, v199, v197\n"
    "v_accvgpr_write_b32 a128, v198\n"
    "v_accvgpr_write_b32 a129, v199\n"
    "v_accvgpr_read_b32 v198, a130\n"
    "v_accvgpr_read_b32 v199, a131\n"
    "v_mul_f32 v198, v198, v197\n"
    "v_mul_f32 v199, v199, v197\n"
    "v_accvgpr_write_b32 a130, v198\n"
    "v_accvgpr_write_b32 a131, v199\n"
    "v_accvgpr_read_b32 v198, a144\n"
    "v_accvgpr_read_b32 v199, a145\n"
    "v_mul_f32 v198, v198, v197\n"
    "v_mul_f32 v199, v199, v197\n"
    "v_accvgpr_write_b32 a144, v198\n"
    "v_accvgpr_write_b32 a145, v199\n"
    "v_accvgpr_read_b32 v198, a146\n"
    "v_accvgpr_read_b32 v199, a147\n"
    "v_mul_f32 v198, v198, v197\n"
    "v_mul_f32 v199, v199, v197\n"
    "v_accvgpr_write_b32 a146, v198\n"
    "v_accvgpr_write_b32 a147, v199\n"
    "v_accvgpr_read_b32 v198, a160\n"
    "v_accvgpr_read_b32 v199, a161\n"
    "v_mul_f32 v198, v198, v197\n"
    "v_mul_f32 v199, v199, v197\n"
    "v_accvgpr_write_b32 a160, v198\n"
    "v_accvgpr_write_b32 a161, v199\n"
    "v_accvgpr_read_b32 v198, a162\n"
    "v_accvgpr_read_b32 v199, a163\n"
    "v_mul_f32 v198, v198, v197\n"
    "v_mul_f32 v199, v199, v197\n"
    "v_accvgpr_write_b32 a162, v198\n"
    "v_accvgpr_write_b32 a163, v199\n"
    "v_accvgpr_read_b32 v198, a176\n"
    "v_accvgpr_read_b32 v199, a177\n"
    "v_mul_f32 v198, v198, v197\n"
    "v_mul_f32 v199, v199, v197\n"
    "v_accvgpr_write_b32 a176, v198\n"
    "v_accvgpr_write_b32 a177, v199\n"
    "v_accvgpr_read_b32 v198, a178\n"
    "v_accvgpr_read_b32 v199, a179\n"
    "v_mul_f32 v198, v198, v197\n"
    "v_mul_f32 v199, v199, v197\n"
    "v_accvgpr_write_b32 a178, v198\n"
    "v_accvgpr_write_b32 a179, v199\n"
    "v_max3_f32 v196, v36, v37, v38\n"
    "v_max3_f32 v196, v196, v39, v52\n"
    "v_max3_f32 v196, v196, v53, v54\n"
    "v_max3_f32 v196, v196, v55, v68\n"
    "v_max3_f32 v196, v196, v69, v70\n"
    "v_max3_f32 v196, v196, v71, v84\n"
    "v_max3_f32 v196, v196, v85, v86\n"
    "v_max_f32 v196, v196, v87\n"
    "v_mov_b32 v197, v196\n"
    "s_nop 1\n"
    "v_permlane32_swap_b32 v196, v197\n"
    "v_max_f32 v196, v196, v197\n"
    "v_mov_b32 v197, v196\n"
    "s_nop 1\n"
    "v_permlane16_swap_b32 v196, v197\n"
    "v_max_f32 v196, v196, v197\n"
    "v_max_f32 v196, v196, v100\n"
    "v_sub_f32 v197, v100, v196\n"
    "v_mov_b32 v100, v196\n"
    "v_exp_f32 v197, v197\n"
    "v_sub_f32 v36, v36, v196\n"
    "v_sub_f32 v37, v37, v196\n"
    "v_sub_f32 v38, v38, v196\n"
    "v_sub_f32 v39, v39, v196\n"
    "v_sub_f32 v52, v52, v196\n"
    "v_sub_f32 v53, v53, v196\n"
    "v_sub_f32 v54, v54, v196\n"
    "v_sub_f32 v55, v55, v196\n"
    "v_sub_f32 v68, v68, v196\n"
    "v_sub_f32 v69, v69, v196\n"
    "v_sub_f32 v70, v70, v196\n"
    "v_sub_f32 v71, v71, v196\n"
    "v_sub_f32 v84, v84, v196\n"
    "v_sub_f32 v85, v85, v196\n"
    "v_sub_f32 v86, v86, v196\n"
    "v_sub_f32 v87, v87, v196\n"
    "v_mul_f32 v116, v116, v197\n"
    "v_mul_f32 v117, v117, v197\n"
    "v_mul_f32 v118, v118, v197\n"
    "v_mul_f32 v119, v119, v197\n"
    "v_accvgpr_read_b32 v198, a4\n"
    "v_accvgpr_read_b32 v199, a5\n"
    "v_mul_f32 v198, v198, v197\n"
    "v_mul_f32 v199, v199, v197\n"
    "v_accvgpr_write_b32 a4, v198\n"
    "v_accvgpr_write_b32 a5, v199\n"
    "v_accvgpr_read_b32 v198, a6\n"
    "v_accvgpr_read_b32 v199, a7\n"
    "v_mul_f32 v198, v198, v197\n"
    "v_mul_f32 v199, v199, v197\n"
    "v_accvgpr_write_b32 a6, v198\n"
    "v_accvgpr_write_b32 a7, v199\n"
    "v_accvgpr_read_b32 v198, a20\n"
    "v_accvgpr_read_b32 v199, a21\n"
    "v_mul_f32 v198, v198, v197\n"
    "v_mul_f32 v199, v199, v197\n"
    "v_accvgpr_write_b32 a20, v198\n"
    "v_accvgpr_write_b32 a21, v199\n"
    "v_accvgpr_read_b32 v198, a22\n"
    "v_accvgpr_read_b32 v199, a23\n"
    "v_mul_f32 v198, v198, v197\n"
    "v_mul_f32 v199, v199, v197\n"
    "v_accvgpr_write_b32 a22, v198\n"
    "v_accvgpr_write_b32 a23, v199\n"
    "v_accvgpr_read_b32 v198, a36\n"
    "v_accvgpr_read_b32 v199, a37\n"
    "v_mul_f32 v198, v198, v197\n"
    "v_mul_f32 v199, v199, v197\n"
    "v_accvgpr_write_b32 a36, v198\n"
    "v_accvgpr_write_b32 a37, v199\n"
    "v_accvgpr_read_b32 v198, a38\n"
    "v_accvgpr_read_b32 v199, a39\n"
    "v_mul_f32 v198, v198, v197\n"
    "v_mul_f32 v199, v199, v197\n"
    "v_accvgpr_write_b32 a38, v198\n"
    "v_accvgpr_write_b32 a39, v199\n"
    "v_accvgpr_read_b32 v198, a52\n"
    "v_accvgpr_read_b32 v199, a53\n"
    "v_mul_f32 v198, v198, v197\n"
    "v_mul_f32 v199, v199, v197\n"
    "v_accvgpr_write_b32 a52, v198\n"
    "v_accvgpr_write_b32 a53, v199\n"
    "v_accvgpr_read_b32 v198, a54\n"
    "v_accvgpr_read_b32 v199, a55\n"
    "v_mul_f32 v198, v198, v197\n"
    "v_mul_f32 v199, v199, v197\n"
    "v_accvgpr_write_b32 a54, v198\n"
    "v_accvgpr_write_b32 a55, v199\n"
    "v_accvgpr_read_b32 v198, a68\n"
    "v_accvgpr_read_b32 v199, a69\n"
    "v_mul_f32 v198, v198, v197\n"
    "v_mul_f32 v199, v199, v197\n"
    "v_accvgpr_write_b32 a68, v198\n"
    "v_accvgpr_write_b32 a69, v199\n"
    "v_accvgpr_read_b32 v198, a70\n"
    "v_accvgpr_read_b32 v199, a71\n"
    "v_mul_f32 v198, v198, v197\n"
    "v_mul_f32 v199, v199, v197\n"
    "v_accvgpr_write_b32 a70, v198\n"
    "v_accvgpr_write_b32 a71, v199\n"
    "v_accvgpr_read_b32 v198, a84\n"
    "v_accvgpr_read_b32 v199, a85\n"
    "v_mul_f32 v198, v198, v197\n"
    "v_mul_f32 v199, v199, v197\n"
    "v_accvgpr_write_b32 a84, v198\n"
    "v_accvgpr_write_b32 a85, v199\n"
    "v_accvgpr_read_b32 v198, a86\n"
    "v_accvgpr_read_b32 v199, a87\n"
    "v_mul_f32 v198, v198, v197\n"
    "v_mul_f32 v199, v199, v197\n"
    "v_accvgpr_write_b32 a86, v198\n"
    "v_accvgpr_write_b32 a87, v199\n"
    "v_accvgpr_read_b32 v198, a100\n"
    "v_accvgpr_read_b32 v199, a101\n"
    "v_mul_f32 v198, v198, v197\n"
    "v_mul_f32 v199, v199, v197\n"
    "v_accvgpr_write_b32 a100, v198\n"
    "v_accvgpr_write_b32 a101, v199\n"
    "v_accvgpr_read_b32 v198, a102\n"
    "v_accvgpr_read_b32 v199, a103\n"
    "v_mul_f32 v198, v198, v197\n"
    "v_mul_f32 v199, v199, v197\n"
    "v_accvgpr_write_b32 a102, v198\n"
    "v_accvgpr_write_b32 a103, v199\n"
    "v_accvgpr_read_b32 v198, a116\n"
    "v_accvgpr_read_b32 v199, a117\n"
    "v_mul_f32 v198, v198, v197\n"
    "v_mul_f32 v199, v199, v197\n"
    "v_accvgpr_write_b32 a116, v198\n"
    "v_accvgpr_write_b32 a117, v199\n"
    "v_accvgpr_read_b32 v198, a118\n"
    "v_accvgpr_read_b32 v199, a119\n"
    "v_mul_f32 v198, v198, v197\n"
    "v_mul_f32 v199, v199, v197\n"
    "v_accvgpr_write_b32 a118, v198\n"
    "v_accvgpr_write_b32 a119, v199\n"
    "v_accvgpr_read_b32 v198, a132\n"
    "v_accvgpr_read_b32 v199, a133\n"
    "v_mul_f32 v198, v198, v197\n"
    "v_mul_f32 v199, v199, v197\n"
    "v_accvgpr_write_b32 a132, v198\n"
    "v_accvgpr_write_b32 a133, v199\n"
    "v_accvgpr_read_b32 v198, a134\n"
    "v_accvgpr_read_b32 v199, a135\n"
    "v_mul_f32 v198, v198, v197\n"
    "v_mul_f32 v199, v199, v197\n"
    "v_accvgpr_write_b32 a134, v198\n"
    "v_accvgpr_write_b32 a135, v199\n"
    "v_accvgpr_read_b32 v198, a148\n"
    "v_accvgpr_read_b32 v199, a149\n"
    "v_mul_f32 v198, v198, v197\n"
    "v_mul_f32 v199, v199, v197\n"
    "v_accvgpr_write_b32 a148, v198\n"
    "v_accvgpr_write_b32 a149, v199\n"
    "v_accvgpr_read_b32 v198, a150\n"
    "v_accvgpr_read_b32 v199, a151\n"
    "v_mul_f32 v198, v198, v197\n"
    "v_mul_f32 v199, v199, v197\n"
    "v_accvgpr_write_b32 a150, v198\n"
    "v_accvgpr_write_b32 a151, v199\n"
    "v_accvgpr_read_b32 v198, a164\n"
    "v_accvgpr_read_b32 v199, a165\n"
    "v_mul_f32 v198, v198, v197\n"
    "v_mul_f32 v199, v199, v197\n"
    "v_accvgpr_write_b32 a164, v198\n"
    "v_accvgpr_write_b32 a165, v199\n"
    "v_accvgpr_read_b32 v198, a166\n"
    "v_accvgpr_read_b32 v199, a167\n"
    "v_mul_f32 v198, v198, v197\n"
    "v_mul_f32 v199, v199, v197\n"
    "v_accvgpr_write_b32 a166, v198\n"
    "v_accvgpr_write_b32 a167, v199\n"
    "v_accvgpr_read_b32 v198, a180\n"
    "v_accvgpr_read_b32 v199, a181\n"
    "v_mul_f32 v198, v198, v197\n"
    "v_mul_f32 v199, v199, v197\n"
    "v_accvgpr_write_b32 a180, v198\n"
    "v_accvgpr_write_b32 a181, v199\n"
    "v_accvgpr_read_b32 v198, a182\n"
    "v_accvgpr_read_b32 v199, a183\n"
    "v_mul_f32 v198, v198, v197\n"
    "v_mul_f32 v199, v199, v197\n"
    "v_accvgpr_write_b32 a182, v198\n"
    "v_accvgpr_write_b32 a183, v199\n"
    "v_max3_f32 v196, v40, v41, v42\n"
    "v_max3_f32 v196, v196, v43, v56\n"
    "v_max3_f32 v196, v196, v57, v58\n"
    "v_max3_f32 v196, v196, v59, v72\n"
    "v_max3_f32 v196, v196, v73, v74\n"
    "v_max3_f32 v196, v196, v75, v88\n"
    "v_max3_f32 v196, v196, v89, v90\n"
    "v_max_f32 v196, v196, v91\n"
    "v_mov_b32 v197, v196\n"
    "s_nop 1\n"
    "v_permlane32_swap_b32 v196, v197\n"
    "v_max_f32 v196, v196, v197\n"
    "v_mov_b32 v197, v196\n"
    "s_nop 1\n"
    "v_permlane16_swap_b32 v196, v197\n"
    "v_max_f32 v196, v196, v197\n"
    "v_max_f32 v196, v196, v104\n"
    "v_sub_f32 v197, v104, v196\n"
    "v_mov_b32 v104, v196\n"
    "v_exp_f32 v197, v197\n"
    "v_sub_f32 v40, v40, v196\n"
    "v_sub_f32 v41, v41, v196\n"
    "v_sub_f32 v42, v42, v196\n"
    "v_sub_f32 v43, v43, v196\n"
    "v_sub_f32 v56, v56, v196\n"
    "v_sub_f32 v57, v57, v196\n"
    "v_sub_f32 v58, v58, v196\n"
    "v_sub_f32 v59, v59, v196\n"
    "v_sub_f32 v72, v72, v196\n"
    "v_sub_f32 v73, v73, v196\n"
    "v_sub_f32 v74, v74, v196\n"
    "v_sub_f32 v75, v75, v196\n"
    "v_sub_f32 v88, v88, v196\n"
    "v_sub_f32 v89, v89, v196\n"
    "v_sub_f32 v90, v90, v196\n"
    "v_sub_f32 v91, v91, v196\n"
    "v_mul_f32 v120, v120, v197\n"
    "v_mul_f32 v121, v121, v197\n"
    "v_mul_f32 v122, v122, v197\n"
    "v_mul_f32 v123, v123, v197\n"
    "v_accvgpr_read_b32 v198, a8\n"
    "v_accvgpr_read_b32 v199, a9\n"
    "v_mul_f32 v198, v198, v197\n"
    "v_mul_f32 v199, v199, v197\n"
    "v_accvgpr_write_b32 a8, v198\n"
    "v_accvgpr_write_b32 a9, v199\n"
    "v_accvgpr_read_b32 v198, a10\n"
    "v_accvgpr_read_b32 v199, a11\n"
    "v_mul_f32 v198, v198, v197\n"
    "v_mul_f32 v199, v199, v197\n"
    "v_accvgpr_write_b32 a10, v198\n"
    "v_accvgpr_write_b32 a11, v199\n"
    "v_accvgpr_read_b32 v198, a24\n"
    "v_accvgpr_read_b32 v199, a25\n"
    "v_mul_f32 v198, v198, v197\n"
    "v_mul_f32 v199, v199, v197\n"
    "v_accvgpr_write_b32 a24, v198\n"
    "v_accvgpr_write_b32 a25, v199\n"
    "v_accvgpr_read_b32 v198, a26\n"
    "v_accvgpr_read_b32 v199, a27\n"
    "v_mul_f32 v198, v198, v197\n"
    "v_mul_f32 v199, v199, v197\n"
    "v_accvgpr_write_b32 a26, v198\n"
    "v_accvgpr_write_b32 a27, v199\n"
    "v_accvgpr_read_b32 v198, a40\n"
    "v_accvgpr_read_b32 v199, a41\n"
    "v_mul_f32 v198, v198, v197\n"
    "v_mul_f32 v199, v199, v197\n"
    "v_accvgpr_write_b32 a40, v198\n"
    "v_accvgpr_write_b32 a41, v199\n"
    "v_accvgpr_read_b32 v198, a42\n"
    "v_accvgpr_read_b32 v199, a43\n"
    "v_mul_f32 v198, v198, v197\n"
    "v_mul_f32 v199, v199, v197\n"
    "v_accvgpr_write_b32 a42, v198\n"
    "v_accvgpr_write_b32 a43, v199\n"
    "v_accvgpr_read_b32 v198, a56\n"
    "v_accvgpr_read_b32 v199, a57\n"
    "v_mul_f32 v198, v198, v197\n"
    "v_mul_f32 v199, v199, v197\n"
    "v_accvgpr_write_b32 a56, v198\n"
    "v_accvgpr_write_b32 a57, v199\n"
    "v_accvgpr_read_b32 v198, a58\n"
    "v_accvgpr_read_b32 v199, a59\n"
    "v_mul_f32 v198, v198, v197\n"
    "v_mul_f32 v199, v199, v197\n"
    "v_accvgpr_write_b32 a58, v198\n"
    "v_accvgpr_write_b32 a59, v199\n"
    "v_accvgpr_read_b32 v198, a72\n"
    "v_accvgpr_read_b32 v199, a73\n"
    "v_mul_f32 v198, v198, v197\n"
    "v_mul_f32 v199, v199, v197\n"
    "v_accvgpr_write_b32 a72, v198\n"
    "v_accvgpr_write_b32 a73, v199\n"
    "v_accvgpr_read_b32 v198, a74\n"
    "v_accvgpr_read_b32 v199, a75\n"
    "v_mul_f32 v198, v198, v197\n"
    "v_mul_f32 v199, v199, v197\n"
    "v_accvgpr_write_b32 a74, v198\n"
    "v_accvgpr_write_b32 a75, v199\n"
    "v_accvgpr_read_b32 v198, a88\n"
    "v_accvgpr_read_b32 v199, a89\n"
    "v_mul_f32 v198, v198, v197\n"
    "v_mul_f32 v199, v199, v197\n"
    "v_accvgpr_write_b32 a88, v198\n"
    "v_accvgpr_write_b32 a89, v199\n"
    "v_accvgpr_read_b32 v198, a90\n"
    "v_accvgpr_read_b32 v199, a91\n"
    "v_mul_f32 v198, v198, v197\n"
    "v_mul_f32 v199, v199, v197\n"
    "v_accvgpr_write_b32 a90, v198\n"
    "v_accvgpr_write_b32 a91, v199\n"
    "v_accvgpr_read_b32 v198, a104\n"
    "v_accvgpr_read_b32 v199, a105\n"
    "v_mul_f32 v198, v198, v197\n"
    "v_mul_f32 v199, v199, v197\n"
    "v_accvgpr_write_b32 a104, v198\n"
    "v_accvgpr_write_b32 a105, v199\n"
    "v_accvgpr_read_b32 v198, a106\n"
    "v_accvgpr_read_b32 v199, a107\n"
    "v_mul_f32 v198, v198, v197\n"
    "v_mul_f32 v199, v199, v197\n"
    "v_accvgpr_write_b32 a106, v198\n"
    "v_accvgpr_write_b32 a107, v199\n"
    "v_accvgpr_read_b32 v198, a120\n"
    "v_accvgpr_read_b32 v199, a121\n"
    "v_mul_f32 v198, v198, v197\n"
    "v_mul_f32 v199, v199, v197\n"
    "v_accvgpr_write_b32 a120, v198\n"
    "v_accvgpr_write_b32 a121, v199\n"
    "v_accvgpr_read_b32 v198, a122\n"
    "v_accvgpr_read_b32 v199, a123\n"
    "v_mul_f32 v198, v198, v197\n"
    "v_mul_f32 v199, v199, v197\n"
    "v_accvgpr_write_b32 a122, v198\n"
    "v_accvgpr_write_b32 a123, v199\n"
    "v_accvgpr_read_b32 v198, a136\n"
    "v_accvgpr_read_b32 v199, a137\n"
    "v_mul_f32 v198, v198, v197\n"
    "v_mul_f32 v199, v199, v197\n"
    "v_accvgpr_write_b32 a136, v198\n"
    "v_accvgpr_write_b32 a137, v199\n"
    "v_accvgpr_read_b32 v198, a138\n"
    "v_accvgpr_read_b32 v199, a139\n"
    "v_mul_f32 v198, v198, v197\n"
    "v_mul_f32 v199, v199, v197\n"
    "v_accvgpr_write_b32 a138, v198\n"
    "v_accvgpr_write_b32 a139, v199\n"
    "v_accvgpr_read_b32 v198, a152\n"
    "v_accvgpr_read_b32 v199, a153\n"
    "v_mul_f32 v198, v198, v197\n"
    "v_mul_f32 v199, v199, v197\n"
    "v_accvgpr_write_b32 a152, v198\n"
    "v_accvgpr_write_b32 a153, v199\n"
    "v_accvgpr_read_b32 v198, a154\n"
    "v_accvgpr_read_b32 v199, a155\n"
    "v_mul_f32 v198, v198, v197\n"
    "v_mul_f32 v199, v199, v197\n"
    "v_accvgpr_write_b32 a154, v198\n"
    "v_accvgpr_write_b32 a155, v199\n"
    "v_accvgpr_read_b32 v198, a168\n"
    "v_accvgpr_read_b32 v199, a169\n"
    "v_mul_f32 v198, v198, v197\n"
    "v_mul_f32 v199, v199, v197\n"
    "v_accvgpr_write_b32 a168, v198\n"
    "v_accvgpr_write_b32 a169, v199\n"
    "v_accvgpr_read_b32 v198, a170\n"
    "v_accvgpr_read_b32 v199, a171\n"
    "v_mul_f32 v198, v198, v197\n"
    "v_mul_f32 v199, v199, v197\n"
    "v_accvgpr_write_b32 a170, v198\n"
    "v_accvgpr_write_b32 a171, v199\n"
    "v_accvgpr_read_b32 v198, a184\n"
    "v_accvgpr_read_b32 v199, a185\n"
    "v_mul_f32 v198, v198, v197\n"
    "v_mul_f32 v199, v199, v197\n"
    "v_accvgpr_write_b32 a184, v198\n"
    "v_accvgpr_write_b32 a185, v199\n"
    "v_accvgpr_read_b32 v198, a186\n"
    "v_accvgpr_read_b32 v199, a187\n"
    "v_mul_f32 v198, v198, v197\n"
    "v_mul_f32 v199, v199, v197\n"
    "v_accvgpr_write_b32 a186, v198\n"
    "v_accvgpr_write_b32 a187, v199\n"
    "v_max3_f32 v196, v44, v45, v46\n"
    "v_max3_f32 v196, v196, v47, v60\n"
    "v_max3_f32 v196, v196, v61, v62\n"
    "v_max3_f32 v196, v196, v63, v76\n"
    "v_max3_f32 v196, v196, v77, v78\n"
    "v_max3_f32 v196, v196, v79, v92\n"
    "v_max3_f32 v196, v196, v93, v94\n"
    "v_max_f32 v196, v196, v95\n"
    "v_mov_b32 v197, v196\n"
    "s_nop 1\n"
    "v_permlane32_swap_b32 v196, v197\n"
    "v_max_f32 v196, v196, v197\n"
    "v_mov_b32 v197, v196\n"
    "s_nop 1\n"
    "v_permlane16_swap_b32 v196, v197\n"
    "v_max_f32 v196, v196, v197\n"
    "v_max_f32 v196, v196, v108\n"
    "v_sub_f32 v197, v108, v196\n"
    "v_mov_b32 v108, v196\n"
    "v_exp_f32 v197, v197\n"
    "v_sub_f32 v44, v44, v196\n"
    "v_sub_f32 v45, v45, v196\n"
    "v_sub_f32 v46, v46, v196\n"
    "v_sub_f32 v47, v47, v196\n"
    "v_sub_f32 v60, v60, v196\n"
    "v_sub_f32 v61, v61, v196\n"
    "v_sub_f32 v62, v62, v196\n"
    "v_sub_f32 v63, v63, v196\n"
    "v_sub_f32 v76, v76, v196\n"
    "v_sub_f32 v77, v77, v196\n"
    "v_sub_f32 v78, v78, v196\n"
    "v_sub_f32 v79, v79, v196\n"
    "v_sub_f32 v92, v92, v196\n"
    "v_sub_f32 v93, v93, v196\n"
    "v_sub_f32 v94, v94, v196\n"
    "v_sub_f32 v95, v95, v196\n"
    "v_mul_f32 v124, v124, v197\n"
    "v_mul_f32 v125, v125, v197\n"
    "v_mul_f32 v126, v126, v197\n"
    "v_mul_f32 v127, v127, v197\n"
    "v_accvgpr_read_b32 v198, a12\n"
    "v_accvgpr_read_b32 v199, a13\n"
    "v_mul_f32 v198, v198, v197\n"
    "v_mul_f32 v199, v199, v197\n"
    "v_accvgpr_write_b32 a12, v198\n"
    "v_accvgpr_write_b32 a13, v199\n"
    "v_accvgpr_read_b32 v198, a14\n"
    "v_accvgpr_read_b32 v199, a15\n"
    "v_mul_f32 v198, v198, v197\n"
    "v_mul_f32 v199, v199, v197\n"
    "v_accvgpr_write_b32 a14, v198\n"
    "v_accvgpr_write_b32 a15, v199\n"
    "v_accvgpr_read_b32 v198, a28\n"
    "v_accvgpr_read_b32 v199, a29\n"
    "v_mul_f32 v198, v198, v197\n"
    "v_mul_f32 v199, v199, v197\n"
    "v_accvgpr_write_b32 a28, v198\n"
    "v_accvgpr_write_b32 a29, v199\n"
    "v_accvgpr_read_b32 v198, a30\n"
    "v_accvgpr_read_b32 v199, a31\n"
    "v_mul_f32 v198, v198, v197\n"
    "v_mul_f32 v199, v199, v197\n"
    "v_accvgpr_write_b32 a30, v198\n"
    "v_accvgpr_write_b32 a31, v199\n"
    "v_accvgpr_read_b32 v198, a44\n"
    "v_accvgpr_read_b32 v199, a45\n"
    "v_mul_f32 v198, v198, v197\n"
    "v_mul_f32 v199, v199, v197\n"
    "v_accvgpr_write_b32 a44, v198\n"
    "v_accvgpr_write_b32 a45, v199\n"
    "v_accvgpr_read_b32 v198, a46\n"
    "v_accvgpr_read_b32 v199, a47\n"
    "v_mul_f32 v198, v198, v197\n"
    "v_mul_f32 v199, v199, v197\n"
    "v_accvgpr_write_b32 a46, v198\n"
    "v_accvgpr_write_b32 a47, v199\n"
    "v_accvgpr_read_b32 v198, a60\n"
    "v_accvgpr_read_b32 v199, a61\n"
    "v_mul_f32 v198, v198, v197\n"
    "v_mul_f32 v199, v199, v197\n"
    "v_accvgpr_write_b32 a60, v198\n"
    "v_accvgpr_write_b32 a61, v199\n"
    "v_accvgpr_read_b32 v198, a62\n"
    "v_accvgpr_read_b32 v199, a63\n"
    "v_mul_f32 v198, v198, v197\n"
    "v_mul_f32 v199, v199, v197\n"
    "v_accvgpr_write_b32 a62, v198\n"
    "v_accvgpr_write_b32 a63, v199\n"
    "v_accvgpr_read_b32 v198, a76\n"
    "v_accvgpr_read_b32 v199, a77\n"
    "v_mul_f32 v198, v198, v197\n"
    "v_mul_f32 v199, v199, v197\n"
    "v_accvgpr_write_b32 a76, v198\n"
    "v_accvgpr_write_b32 a77, v199\n"
    "v_accvgpr_read_b32 v198, a78\n"
    "v_accvgpr_read_b32 v199, a79\n"
    "v_mul_f32 v198, v198, v197\n"
    "v_mul_f32 v199, v199, v197\n"
    "v_accvgpr_write_b32 a78, v198\n"
    "v_accvgpr_write_b32 a79, v199\n"
    "v_accvgpr_read_b32 v198, a92\n"
    "v_accvgpr_read_b32 v199, a93\n"
    "v_mul_f32 v198, v198, v197\n"
    "v_mul_f32 v199, v199, v197\n"
    "v_accvgpr_write_b32 a92, v198\n"
    "v_accvgpr_write_b32 a93, v199\n"
    "v_accvgpr_read_b32 v198, a94\n"
    "v_accvgpr_read_b32 v199, a95\n"
    "v_mul_f32 v198, v198, v197\n"
    "v_mul_f32 v199, v199, v197\n"
    "v_accvgpr_write_b32 a94, v198\n"
    "v_accvgpr_write_b32 a95, v199\n"
    "v_accvgpr_read_b32 v198, a108\n"
    "v_accvgpr_read_b32 v199, a109\n"
    "v_mul_f32 v198, v198, v197\n"
    "v_mul_f32 v199, v199, v197\n"
    "v_accvgpr_write_b32 a108, v198\n"
    "v_accvgpr_write_b32 a109, v199\n"
    "v_accvgpr_read_b32 v198, a110\n"
    "v_accvgpr_read_b32 v199, a111\n"
    "v_mul_f32 v198, v198, v197\n"
    "v_mul_f32 v199, v199, v197\n"
    "v_accvgpr_write_b32 a110, v198\n"
    "v_accvgpr_write_b32 a111, v199\n"
    "v_accvgpr_read_b32 v198, a124\n"
    "v_accvgpr_read_b32 v199, a125\n"
    "v_mul_f32 v198, v198, v197\n"
    "v_mul_f32 v199, v199, v197\n"
    "v_accvgpr_write_b32 a124, v198\n"
    "v_accvgpr_write_b32 a125, v199\n"
    "v_accvgpr_read_b32 v198, a126\n"
    "v_accvgpr_read_b32 v199, a127\n"
    "v_mul_f32 v198, v198, v197\n"
    "v_mul_f32 v199, v199, v197\n"
    "v_accvgpr_write_b32 a126, v198\n"
    "v_accvgpr_write_b32 a127, v199\n"
    "v_accvgpr_read_b32 v198, a140\n"
    "v_accvgpr_read_b32 v199, a141\n"
    "v_mul_f32 v198, v198, v197\n"
    "v_mul_f32 v199, v199, v197\n"
    "v_accvgpr_write_b32 a140, v198\n"
    "v_accvgpr_write_b32 a141, v199\n"
    "v_accvgpr_read_b32 v198, a142\n"
    "v_accvgpr_read_b32 v199, a143\n"
    "v_mul_f32 v198, v198, v197\n"
    "v_mul_f32 v199, v199, v197\n"
    "v_accvgpr_write_b32 a142, v198\n"
    "v_accvgpr_write_b32 a143, v199\n"
    "v_accvgpr_read_b32 v198, a156\n"
    "v_accvgpr_read_b32 v199, a157\n"
    "v_mul_f32 v198, v198, v197\n"
    "v_mul_f32 v199, v199, v197\n"
    "v_accvgpr_write_b32 a156, v198\n"
    "v_accvgpr_write_b32 a157, v199\n"
    "v_accvgpr_read_b32 v198, a158\n"
    "v_accvgpr_read_b32 v199, a159\n"
    "v_mul_f32 v198, v198, v197\n"
    "v_mul_f32 v199, v199, v197\n"
    "v_accvgpr_write_b32 a158, v198\n"
    "v_accvgpr_write_b32 a159, v199\n"
    "v_accvgpr_read_b32 v198, a172\n"
    "v_accvgpr_read_b32 v199, a173\n"
    "v_mul_f32 v198, v198, v197\n"
    "v_mul_f32 v199, v199, v197\n"
    "v_accvgpr_write_b32 a172, v198\n"
    "v_accvgpr_write_b32 a173, v199\n"
    "v_accvgpr_read_b32 v198, a174\n"
    "v_accvgpr_read_b32 v199, a175\n"
    "v_mul_f32 v198, v198, v197\n"
    "v_mul_f32 v199, v199, v197\n"
    "v_accvgpr_write_b32 a174, v198\n"
    "v_accvgpr_write_b32 a175, v199\n"
    "v_accvgpr_read_b32 v198, a188\n"
    "v_accvgpr_read_b32 v199, a189\n"
    "v_mul_f32 v198, v198, v197\n"
    "v_mul_f32 v199, v199, v197\n"
    "v_accvgpr_write_b32 a188, v198\n"
    "v_accvgpr_write_b32 a189, v199\n"
    "v_accvgpr_read_b32 v198, a190\n"
    "v_accvgpr_read_b32 v199, a191\n"
    "v_mul_f32 v198, v198, v197\n"
    "v_mul_f32 v199, v199, v197\n"
    "v_accvgpr_write_b32 a190, v198\n"
    "v_accvgpr_write_b32 a191, v199\n"
    "v_exp_f32 v32, v32\n"
    "v_exp_f32 v33, v33\n"
    "v_exp_f32 v34, v34\n"
    "v_cvt_pk_bf16_f32 v128, v32, v33\n"
    "v_exp_f32 v35, v35\n"
    "v_exp_f32 v36, v36\n"
    "v_cvt_pk_bf16_f32 v129, v34, v35\n"
    "v_exp_f32 v37, v37\n"
    "v_exp_f32 v38, v38\n"
    "v_cvt_pk_bf16_f32 v132, v36, v37\n"
    "v_exp_f32 v39, v39\n"
    "v_exp_f32 v40, v40\n"
    "v_cvt_pk_bf16_f32 v133, v38, v39\n"
    "v_exp_f32 v41, v41\n"
    "v_exp_f32 v42, v42\n"
    "v_cvt_pk_bf16_f32 v136, v40, v41\n"
    "v_exp_f32 v43, v43\n"
    "v_exp_f32 v44, v44\n"
    "v_cvt_pk_bf16_f32 v137, v42, v43\n"
    "v_exp_f32 v45, v45\n"
    "v_exp_f32 v46, v46\n"
    "v_cvt_pk_bf16_f32 v140, v44, v45\n"
    "v_exp_f32 v47, v47\n"
    "v_exp_f32 v48, v48\n"
    "v_cvt_pk_bf16_f32 v141, v46, v47\n"
    "v_exp_f32 v49, v49\n"
    "v_exp_f32 v50, v50\n"
    "v_cvt_pk_bf16_f32 v130, v48, v49\n"
    "v_exp_f32 v51, v51\n"
    "v_exp_f32 v52, v52\n"
    "v_cvt_pk_bf16_f32 v131, v50, v51\n"
    "v_exp_f32 v53, v53\n"
    "v_exp_f32 v54, v54\n"
    "v_cvt_pk_bf16_f32 v134, v52, v53\n"
    "v_exp_f32 v55, v55\n"
    "v_exp_f32 v56, v56\n"
    "v_cvt_pk_bf16_f32 v135, v54, v55\n"
    "v_exp_f32 v57, v57\n"
    "v_exp_f32 v58, v58\n"
    "v_cvt_pk_bf16_f32 v138, v56, v57\n"
    "v_exp_f32 v59, v59\n"
    "v_exp_f32 v60, v60\n"
    "v_cvt_pk_bf16_f32 v139, v58, v59\n"
    "v_exp_f32 v61, v61\n"
    "v_exp_f32 v62, v62\n"
    "v_cvt_pk_bf16_f32 v142, v60, v61\n"
    "v_exp_f32 v63, v63\n"
    "s_nop 0\n"
    "v_cvt_pk_bf16_f32 v143, v62, v63\n"
    "v_exp_f32 v64, v64\n"
    "v_exp_f32 v65, v65\n"
    "v_exp_f32 v66, v66\n"
    "v_cvt_pk_bf16_f32 v144, v64, v65\n"
    "v_exp_f32 v67, v67\n"
    "v_exp_f32 v68, v68\n"
    "v_cvt_pk_bf16_f32 v145, v66, v67\n"
    "v_exp_f32 v69, v69\n"
    "v_exp_f32 v70, v70\n"
    "v_cvt_pk_bf16_f32 v148, v68, v69\n"
    "v_exp_f32 v71, v71\n"
    "v_exp_f32 v72, v72\n"
    "v_cvt_pk_bf16_f32 v149, v70, v71\n"
    "v_exp_f32 v73, v73\n"
    "v_exp_f32 v74, v74\n"
    "v_cvt_pk_bf16_f32 v152, v72, v73\n"
    "v_exp_f32 v75, v75\n"
    "v_exp_f32 v76, v76\n"
    "v_cvt_pk_bf16_f32 v153, v74, v75\n"
    "v_exp_f32 v77, v77\n"
    "v_exp_f32 v78, v78\n"
    "v_cvt_pk_bf16_f32 v156, v76, v77\n"
    "v_exp_f32 v79, v79\n"
    "v_exp_f32 v80, v80\n"
    "v_cvt_pk_bf16_f32 v157, v78, v79\n"
    "v_exp_f32 v81, v81\n"
    "v_exp_f32 v82, v82\n"
    "v_cvt_pk_bf16_f32 v146, v80, v81\n"
    "v_exp_f32 v83, v83\n"
    "v_exp_f32 v84, v84\n"
    "v_cvt_pk_bf16_f32 v147, v82, v83\n"
    "v_exp_f32 v85, v85\n"
    "v_exp_f32 v86, v86\n"
    "v_cvt_pk_bf16_f32 v150, v84, v85\n"
    "v_exp_f32 v87, v87\n"
    "v_exp_f32 v88, v88\n"
    "v_cvt_pk_bf16_f32 v151, v86, v87\n"
    "v_exp_f32 v89, v89\n"
    "v_exp_f32 v90, v90\n"
    "v_cvt_pk_bf16_f32 v154, v88, v89\n"
    "v_exp_f32 v91, v91\n"
    "v_exp_f32 v92, v92\n"
    "v_cvt_pk_bf16_f32 v155, v90, v91\n"
    "v_exp_f32 v93, v93\n"
    "v_exp_f32 v94, v94\n"
    "v_cvt_pk_bf16_f32 v158, v92, v93\n"
    "v_exp_f32 v95, v95\n"
    "s_nop 0\n"
    "v_cvt_pk_bf16_f32 v159, v94, v95\n"
    "ds_read_b128 v[160:163], v205 offset:12288\n"
    "ds_read_b128 v[164:167], v205 offset:13312\n"
    "s_waitcnt lgkmcnt(1)\n"
    "v_mfma_f32_16x16x32_bf16 a[0:3], v[160:163], v[128:131], a[0:3]\n"
    "s_waitcnt lgkmcnt(0)\n"
    "v_mfma_f32_16x16x32_bf16 a[0:3], v[164:167], v[144:147], a[0:3]\n"
    "v_mfma_f32_16x16x32_bf16 a[4:7], v[160:163], v[132:135], a[4:7]\n"
    "v_mfma_f32_16x16x32_bf16 a[4:7], v[164:167], v[148:151], a[4:7]\n"
    "v_mfma_f32_16x16x32_bf16 a[8:11], v[160:163], v[136:139], a[8:11]\n"
    "v_mfma_f32_16x16x32_bf16 a[8:11], v[164:167], v[152:155], a[8:11]\n"
    "v_mfma_f32_16x16x32_bf16 a[12:15], v[160:163], v[140:143], a[12:15]\n"
    "v_mfma_f32_16x16x32_bf16 a[12:15], v[164:167], v[156:159], a[12:15]\n"
    "ds_read_b128 v[168:171], v205 offset:14336\n"
    "ds_read_b128 v[172:175], v205 offset:15360\n"
    "s_waitcnt lgkmcnt(1)\n"
    "v_mfma_f32_16x16x32_bf16 a[28:31], v[168:171], v[140:143], a[28:31]\n"
    "s_waitcnt lgkmcnt(0)\n"
    "v_mfma_f32_16x16x32_bf16 a[28:31], v[172:175], v[156:159], a[28:31]\n"
    "v_mfma_f32_16x16x32_bf16 a[24:27], v[172:175], v[152:155], a[24:27]\n"
    "v_mfma_f32_16x16x32_bf16 a[24:27], v[168:171], v[136:139], a[24:27]\n"
    "v_mfma_f32_16x16x32_bf16 a[20:23], v[168:171], v[132:135], a[20:23]\n"
    "v_mfma_f32_16x16x32_bf16 a[20:23], v[172:175], v[148:151], a[20:23]\n"
    "v_mfma_f32_16x16x32_bf16 a[16:19], v[172:175], v[144:147], a[16:19]\n"
    "v_mfma_f32_16x16x32_bf16 a[16:19], v[168:171], v[128:131], a[16:19]\n"
    "ds_read_b128 v[176:179], v205 offset:16384\n"
    "ds_read_b128 v[180:183], v205 offset:17408\n"
    "s_waitcnt lgkmcnt(1)\n"
    "v_mfma_f32_16x16x32_bf16 a[32:35], v[176:179], v[128:131], a[32:35]\n"
    "s_waitcnt lgkmcnt(0)\n"
    "v_mfma_f32_16x16x32_bf16 a[32:35], v[180:183], v[144:147], a[32:35]\n"
    "v_mfma_f32_16x16x32_bf16 a[36:39], v[180:183], v[148:151], a[36:39]\n"
    "v_mfma_f32_16x16x32_bf16 a[36:39], v[176:179], v[132:135], a[36:39]\n"
    "v_mfma_f32_16x16x32_bf16 a[40:43], v[176:179], v[136:139], a[40:43]\n"
    "v_mfma_f32_16x16x32_bf16 a[40:43], v[180:183], v[152:155], a[40:43]\n"
    "v_mfma_f32_16x16x32_bf16 a[44:47], v[180:183], v[156:159], a[44:47]\n"
    "v_mfma_f32_16x16x32_bf16 a[44:47], v[176:179], v[140:143], a[44:47]\n"
    "ds_read_b128 v[160:163], v205 offset:18432\n"
    "ds_read_b128 v[164:167], v205 offset:19456\n"
    "s_waitcnt lgkmcnt(1)\n"
    "v_mfma_f32_16x16x32_bf16 a[60:63], v[160:163], v[140:143], a[60:63]\n"
    "s_waitcnt lgkmcnt(0)\n"
    "v_mfma_f32_16x16x32_bf16 a[60:63], v[164:167], v[156:159], a[60:63]\n"
    "v_mfma_f32_16x16x32_bf16 a[56:59], v[164:167], v[152:155], a[56:59]\n"
    "v_mfma_f32_16x16x32_bf16 a[56:59], v[160:163], v[136:139], a[56:59]\n"
    "v_mfma_f32_16x16x32_bf16 a[52:55], v[160:163], v[132:135], a[52:55]\n"
    "v_mfma_f32_16x16x32_bf16 a[52:55], v[164:167], v[148:151], a[52:55]\n"
    "v_mfma_f32_16x16x32_bf16 a[48:51], v[164:167], v[144:147], a[48:51]\n"
    "v_mfma_f32_16x16x32_bf16 a[48:51], v[160:163], v[128:131], a[48:51]\n"
    "ds_read_b128 v[168:171], v205 offset:20480\n"
    "ds_read_b128 v[172:175], v205 offset:21504\n"
    "s_waitcnt lgkmcnt(1)\n"
    "v_mfma_f32_16x16x32_bf16 a[64:67], v[168:171], v[128:131], a[64:67]\n"
    "s_waitcnt lgkmcnt(0)\n"
    "v_mfma_f32_16x16x32_bf16 a[64:67], v[172:175], v[144:147], a[64:67]\n"
    "v_mfma_f32_16x16x32_bf16 a[68:71], v[172:175], v[148:151], a[68:71]\n"
    "v_mfma_f32_16x16x32_bf16 a[68:71], v[168:171], v[132:135], a[68:71]\n"
    "v_mfma_f32_16x16x32_bf16 a[72:75], v[168:171], v[136:139], a[72:75]\n"
    "v_mfma_f32_16x16x32_bf16 a[72:75], v[172:175], v[152:155], a[72:75]\n"
    "v_mfma_f32_16x16x32_bf16 a[76:79], v[172:175], v[156:159], a[76:79]\n"
    "v_mfma_f32_16x16x32_bf16 a[76:79], v[168:171], v[140:143], a[76:79]\n"
    "ds_read_b128 v[176:179], v205 offset:22528\n"
    "ds_read_b128 v[180:183], v205 offset:23552\n"
    "s_waitcnt lgkmcnt(1)\n"
    "v_mfma_f32_16x16x32_bf16 a[92:95], v[176:179], v[140:143], a[92:95]\n"
    "s_waitcnt lgkmcnt(0)\n"
    "v_mfma_f32_16x16x32_bf16 a[92:95], v[180:183], v[156:159], a[92:95]\n"
    "v_mfma_f32_16x16x32_bf16 a[88:91], v[180:183], v[152:155], a[88:91]\n"
    "v_mfma_f32_16x16x32_bf16 a[88:91], v[176:179], v[136:139], a[88:91]\n"
    "v_mfma_f32_16x16x32_bf16 a[84:87], v[176:179], v[132:135], a[84:87]\n"
    "v_mfma_f32_16x16x32_bf16 a[84:87], v[180:183], v[148:151], a[84:87]\n"
    "v_mfma_f32_16x16x32_bf16 a[80:83], v[180:183], v[144:147], a[80:83]\n"
    "v_mfma_f32_16x16x32_bf16 a[80:83], v[176:179], v[128:131], a[80:83]\n"
    "ds_read_b128 v[160:163], v205 offset:24576\n"
    "ds_read_b128 v[164:167], v205 offset:25600\n"
    "s_waitcnt lgkmcnt(1)\n"
    "v_mfma_f32_16x16x32_bf16 a[96:99], v[160:163], v[128:131], a[96:99]\n"
    "s_waitcnt lgkmcnt(0)\n"
    "v_mfma_f32_16x16x32_bf16 a[96:99], v[164:167], v[144:147], a[96:99]\n"
    "v_mfma_f32_16x16x32_bf16 a[100:103], v[164:167], v[148:151], a[100:103]\n"
    "v_mfma_f32_16x16x32_bf16 a[100:103], v[160:163], v[132:135], a[100:103]\n"
    "v_mfma_f32_16x16x32_bf16 a[104:107], v[160:163], v[136:139], a[104:107]\n"
    "v_mfma_f32_16x16x32_bf16 a[104:107], v[164:167], v[152:155], a[104:107]\n"
    "v_mfma_f32_16x16x32_bf16 a[108:111], v[164:167], v[156:159], a[108:111]\n"
    "v_mfma_f32_16x16x32_bf16 a[108:111], v[160:163], v[140:143], a[108:111]\n"
    "ds_read_b128 v[168:171], v205 offset:26624\n"
    "ds_read_b128 v[172:175], v205 offset:27648\n"
    "s_waitcnt lgkmcnt(1)\n"
    "v_mfma_f32_16x16x32_bf16 a[124:127], v[168:171], v[140:143], a[124:127]\n"
    "s_waitcnt lgkmcnt(0)\n"
    "v_mfma_f32_16x16x32_bf16 a[124:127], v[172:175], v[156:159], a[124:127]\n"
    "v_mfma_f32_16x16x32_bf16 a[120:123], v[172:175], v[152:155], a[120:123]\n"
    "v_mfma_f32_16x16x32_bf16 a[120:123], v[168:171], v[136:139], a[120:123]\n"
    "v_mfma_f32_16x16x32_bf16 a[116:119], v[168:171], v[132:135], a[116:119]\n"
    "v_mfma_f32_16x16x32_bf16 a[116:119], v[172:175], v[148:151], a[116:119]\n"
    "v_mfma_f32_16x16x32_bf16 a[112:115], v[172:175], v[144:147], a[112:115]\n"
    "v_mfma_f32_16x16x32_bf16 a[112:115], v[168:171], v[128:131], a[112:115]\n"
    "ds_read_b128 v[176:179], v205 offset:28672\n"
    "ds_read_b128 v[180:183], v205 offset:29696\n"
    "s_waitcnt lgkmcnt(1)\n"
    "v_mfma_f32_16x16x32_bf16 a[128:131], v[176:179], v[128:131], a[128:131]\n"
    "s_waitcnt lgkmcnt(0)\n"
    "v_mfma_f32_16x16x32_bf16 a[128:131], v[180:183], v[144:147], a[128:131]\n"
    "v_mfma_f32_16x16x32_bf16 a[132:135], v[180:183], v[148:151], a[132:135]\n"
    "v_mfma_f32_16x16x32_bf16 a[132:135], v[176:179], v[132:135], a[132:135]\n"
    "v_mfma_f32_16x16x32_bf16 a[136:139], v[176:179], v[136:139], a[136:139]\n"
    "v_mfma_f32_16x16x32_bf16 a[136:139], v[180:183], v[152:155], a[136:139]\n"
    "v_mfma_f32_16x16x32_bf16 a[140:143], v[180:183], v[156:159], a[140:143]\n"
    "v_mfma_f32_16x16x32_bf16 a[140:143], v[176:179], v[140:143], a[140:143]\n"
    "ds_read_b128 v[160:163], v205 offset:30720\n"
    "ds_read_b128 v[164:167], v205 offset:31744\n"
    "s_waitcnt lgkmcnt(1)\n"
    "v_mfma_f32_16x16x32_bf16 a[156:159], v[160:163], v[140:143], a[156:159]\n"
    "s_waitcnt lgkmcnt(0)\n"
    "v_mfma_f32_16x16x32_bf16 a[156:159], v[164:167], v[156:159], a[156:159]\n"
    "v_mfma_f32_16x16x32_bf16 a[152:155], v[164:167], v[152:155], a[152:155]\n"
    "v_mfma_f32_16x16x32_bf16 a[152:155], v[160:163], v[136:139], a[152:155]\n"
    "v_mfma_f32_16x16x32_bf16 a[148:151], v[160:163], v[132:135], a[148:151]\n"
    "v_mfma_f32_16x16x32_bf16 a[148:151], v[164:167], v[148:151], a[148:151]\n"
    "v_mfma_f32_16x16x32_bf16 a[144:147], v[164:167], v[144:147], a[144:147]\n"
    "v_mfma_f32_16x16x32_bf16 a[144:147], v[160:163], v[128:131], a[144:147]\n"
    "ds_read_b128 v[168:171], v205 offset:32768\n"
    "ds_read_b128 v[172:175], v205 offset:33792\n"
    "s_waitcnt lgkmcnt(1)\n"
    "v_mfma_f32_16x16x32_bf16 a[160:163], v[168:171], v[128:131], a[160:163]\n"
    "s_waitcnt lgkmcnt(0)\n"
    "v_mfma_f32_16x16x32_bf16 a[160:163], v[172:175], v[144:147], a[160:163]\n"
    "v_mfma_f32_16x16x32_bf16 a[164:167], v[172:175], v[148:151], a[164:167]\n"
    "v_mfma_f32_16x16x32_bf16 a[164:167], v[168:171], v[132:135], a[164:167]\n"
    "v_mfma_f32_16x16x32_bf16 a[168:171], v[168:171], v[136:139], a[168:171]\n"
    "v_mfma_f32_16x16x32_bf16 a[168:171], v[172:175], v[152:155], a[168:171]\n"
    "v_mfma_f32_16x16x32_bf16 a[172:175], v[172:175], v[156:159], a[172:175]\n"
    "v_mfma_f32_16x16x32_bf16 a[172:175], v[168:171], v[140:143], a[172:175]\n"
    "ds_read_b128 v[176:179], v205 offset:34816\n"
    "ds_read_b128 v[180:183], v205 offset:35840\n"
    "s_waitcnt lgkmcnt(1)\n"
    "v_mfma_f32_16x16x32_bf16 a[188:191], v[176:179], v[140:143], a[188:191]\n"
    "s_waitcnt lgkmcnt(0)\n"
    "v_mfma_f32_16x16x32_bf16 a[188:191], v[180:183], v[156:159], a[188:191]\n"
    "v_mfma_f32_16x16x32_bf16 a[184:187], v[176:179], v[136:139], a[184:187]\n"
    "v_mfma_f32_16x16x32_bf16 a[184:187], v[180:183], v[152:155], a[184:187]\n"
    "v_mfma_f32_16x16x32_bf16 a[180:183], v[176:179], v[132:135], a[180:183]\n"
    "v_mfma_f32_16x16x32_bf16 a[180:183], v[180:183], v[148:151], a[180:183]\n"
    "v_mfma_f32_16x16x32_bf16 a[176:179], v[176:179], v[128:131], a[176:179]\n"
    "v_mfma_f32_16x16x32_bf16 a[176:179], v[180:183], v[144:147], a[176:179]\n"
    "v_mfma_f32_16x16x32_bf16 v[112:115], v[192:195], v[128:131], v[112:115]\n"
    "v_mfma_f32_16x16x32_bf16 v[112:115], v[192:195], v[144:147], v[112:115]\n"
    "v_mfma_f32_16x16x32_bf16 v[116:119], v[192:195], v[132:135], v[116:119]\n"
    "v_mfma_f32_16x16x32_bf16 v[116:119], v[192:195], v[148:151], v[116:119]\n"
    "v_mfma_f32_16x16x32_bf16 v[120:123], v[192:195], v[136:139], v[120:123]\n"
    "v_mfma_f32_16x16x32_bf16 v[120:123], v[192:195], v[152:155], v[120:123]\n"
    "v_mfma_f32_16x16x32_bf16 v[124:127], v[192:195], v[140:143], v[124:127]\n"
    "v_mfma_f32_16x16x32_bf16 v[124:127], v[192:195], v[156:159], v[124:127]\n"
    "s_nop 15\n"
    "s_add_u32 s63, s63, 0x6000\n"
    "s_cmp_lt_u32 s63, 0xc0000\n"
    "s_cbranch_scc1 LSLOOP\n"
    "s_branch LEPI\n"
    "LEPI:\n"
    "s_lshl_b32 s76, s56, 3\n"
    "s_add_u32 s76, s76, s57\n"
    "s_mul_i32 s76, s76, 0x18000\n"
    "s_add_u32 s76, s76, s88\n"
    "s_add_u32 s28, s24, s76\n"
    "s_addc_u32 s29, s25, 0\n"
    "s_and_b32 s29, s29, 0xffff\n"
    "s_mov_b32 s30, 0x6000\n"
    "s_mov_b32 s31, 0x20000\n"
    "s_xor_b32 s94, s70, 0x80000000\n"
    "s_xor_b32 s95, s70, 0x80000000\n"
    "s_mov_b32 s62, 0x1800\n"
    "s_mov_b32 s63, 0x3000\n"
    "s_mov_b32 s64, 0x4800\n"
    "v_and_b32 v198, 15, v231\n"
    "v_mul_u32_u24 v205, 0x180, v198\n"
    "v_lshrrev_b32 v199, 4, v231\n"
    "v_lshl_add_u32 v205, v199, 4, v205\n"
    "v_mul_f32 v222, s68, v112\n"
    "v_rcp_f32 v222, v222\n"
    "v_mul_f32 v224, s68, v116\n"
    "v_rcp_f32 v224, v224\n"
    "v_mul_f32 v226, s68, v120\n"
    "v_rcp_f32 v226, v226\n"
    "v_mul_f32 v228, s68, v124\n"
    "v_rcp_f32 v228, v228\n"
    "v_mov_b32 v223, v222\n"
    "v_mov_b32 v225, v224\n"
    "v_mov_b32 v227, v226\n"
    "v_mov_b32 v229, v228\n"
    "v_accvgpr_read_b32 v96, a0\n"
    "v_accvgpr_read_b32 v97, a1\n"
    "v_accvgpr_read_b32 v98, a2\n"
    "v_accvgpr_read_b32 v99, a3\n"
    "v_pk_fma_f32 v[96:97], v[96:97], v[222:223], s[94:95]\n"
    "v_pk_fma_f32 v[98:99], v[98:99], v[222:223], s[94:95]\n"
    "v_cvt_pk_bf16_f32 v32, v96, v97\n"
    "v_cvt_pk_bf16_f32 v33, v98, v99\n"
    "v_accvgpr_read_b32 v100, a16\n"
    "v_accvgpr_read_b32 v101, a17\n"
    "v_accvgpr_read_b32 v102, a18\n"
    "v_accvgpr_read_b32 v103, a19\n"
    "v_pk_fma_f32 v[100:101], v[100:101], v[222:223], s[94:95]\n"
    "v_pk_fma_f32 v[102:103], v[102:103], v[222:223], s[94:95]\n"
    "v_cvt_pk_bf16_f32 v34, v100, v101\n"
    "v_cvt_pk_bf16_f32 v35, v102, v103\n"
    "buffer_store_dwordx4 v[32:35], v205, s[28:31], 0 offen sc0 sc1\n"
    "v_accvgpr_read_b32 v104, a32\n"
    "v_accvgpr_read_b32 v105, a33\n"
    "v_accvgpr_read_b32 v106, a34\n"
    "v_accvgpr_read_b32 v107, a35\n"
    "v_pk_fma_f32 v[104:105], v[104:105], v[222:223], s[94:95]\n"
    "v_pk_fma_f32 v[106:107], v[106:107], v[222:223], s[94:95]\n"
    "v_cvt_pk_bf16_f32 v36, v104, v105\n"
    "v_cvt_pk_bf16_f32 v37, v106, v107\n"
    "v_accvgpr_read_b32 v108, a48\n"
    "v_accvgpr_read_b32 v109, a49\n"
    "v_accvgpr_read_b32 v110, a50\n"
    "v_accvgpr_read_b32 v111, a51\n"
    "v_pk_fma_f32 v[108:109], v[108:109], v[222:223], s[94:95]\n"
    "v_pk_fma_f32 v[110:111], v[110:111], v[222:223], s[94:95]\n"
    "v_cvt_pk_bf16_f32 v38, v108, v109\n"
    "v_cvt_pk_bf16_f32 v39, v110, v111\n"
    "buffer_store_dwordx4 v[36:39], v205, s[28:31], 0 offen offset:64 sc0 sc1\n"
    "v_accvgpr_read_b32 v96, a64\n"
    "v_accvgpr_read_b32 v97, a65\n"
    "v_accvgpr_read_b32 v98, a66\n"
    "v_accvgpr_read_b32 v99, a67\n"
    "v_pk_fma_f32 v[96:97], v[96:97], v[222:223], s[94:95]\n"
    "v_pk_fma_f32 v[98:99], v[98:99], v[222:223], s[94:95]\n"
    "v_cvt_pk_bf16_f32 v40, v96, v97\n"
    "v_cvt_pk_bf16_f32 v41, v98, v99\n"
    "v_accvgpr_read_b32 v100, a80\n"
    "v_accvgpr_read_b32 v101, a81\n"
    "v_accvgpr_read_b32 v102, a82\n"
    "v_accvgpr_read_b32 v103, a83\n"
    "v_pk_fma_f32 v[100:101], v[100:101], v[222:223], s[94:95]\n"
    "v_pk_fma_f32 v[102:103], v[102:103], v[222:223], s[94:95]\n"
    "v_cvt_pk_bf16_f32 v42, v100, v101\n"
    "v_cvt_pk_bf16_f32 v43, v102, v103\n"
    "buffer_store_dwordx4 v[40:43], v205, s[28:31], 0 offen offset:128 sc0 sc1\n"
    "v_accvgpr_read_b32 v104, a96\n"
    "v_accvgpr_read_b32 v105, a97\n"
    "v_accvgpr_read_b32 v106, a98\n"
    "v_accvgpr_read_b32 v107, a99\n"
    "v_pk_fma_f32 v[104:105], v[104:105], v[222:223], s[94:95]\n"
    "v_pk_fma_f32 v[106:107], v[106:107], v[222:223], s[94:95]\n"
    "v_cvt_pk_bf16_f32 v44, v104, v105\n"
    "v_cvt_pk_bf16_f32 v45, v106, v107\n"
    "v_accvgpr_read_b32 v108, a112\n"
    "v_accvgpr_read_b32 v109, a113\n"
    "v_accvgpr_read_b32 v110, a114\n"
    "v_accvgpr_read_b32 v111, a115\n"
    "v_pk_fma_f32 v[108:109], v[108:109], v[222:223], s[94:95]\n"
    "v_pk_fma_f32 v[110:111], v[110:111], v[222:223], s[94:95]\n"
    "v_cvt_pk_bf16_f32 v46, v108, v109\n"
    "v_cvt_pk_bf16_f32 v47, v110, v111\n"
    "buffer_store_dwordx4 v[44:47], v205, s[28:31], 0 offen offset:192 sc0 sc1\n"
    "v_accvgpr_read_b32 v96, a128\n"
    "v_accvgpr_read_b32 v97, a129\n"
    "v_accvgpr_read_b32 v98, a130\n"
    "v_accvgpr_read_b32 v99, a131\n"
    "v_pk_fma_f32 v[96:97], v[96:97], v[222:223], s[94:95]\n"
    "v_pk_fma_f32 v[98:99], v[98:99], v[222:223], s[94:95]\n"
    "v_cvt_pk_bf16_f32 v48, v96, v97\n"
    "v_cvt_pk_bf16_f32 v49, v98, v99\n"
    "v_accvgpr_read_b32 v100, a144\n"
    "v_accvgpr_read_b32 v101, a145\n"
    "v_accvgpr_read_b32 v102, a146\n"
    "v_accvgpr_read_b32 v103, a147\n"
    "v_pk_fma_f32 v[100:101], v[100:101], v[222:223], s[94:95]\n"
    "v_pk_fma_f32 v[102:103], v[102:103], v[222:223], s[94:95]\n"
    "v_cvt_pk_bf16_f32 v50, v100, v101\n"
    "v_cvt_pk_bf16_f32 v51, v102, v103\n"
    "buffer_store_dwordx4 v[48:51], v205, s[28:31], 0 offen offset:256 sc0 sc1\n"
    "v_accvgpr_read_b32 v104, a160\n"
    "v_accvgpr_read_b32 v105, a161\n"
    "v_accvgpr_read_b32 v106, a162\n"
    "v_accvgpr_read_b32 v107, a163\n"
    "v_pk_fma_f32 v[104:105], v[104:105], v[222:223], s[94:95]\n"
    "v_pk_fma_f32 v[106:107], v[106:107], v[222:223], s[94:95]\n"
    "v_cvt_pk_bf16_f32 v52, v104, v105\n"
    "v_cvt_pk_bf16_f32 v53, v106, v107\n"
    "v_accvgpr_read_b32 v108, a176\n"
    "v_accvgpr_read_b32 v109, a177\n"
    "v_accvgpr_read_b32 v110, a178\n"
    "v_accvgpr_read_b32 v111, a179\n"
    "v_pk_fma_f32 v[108:109], v[108:109], v[222:223], s[94:95]\n"
    "v_pk_fma_f32 v[110:111], v[110:111], v[222:223], s[94:95]\n"
    "v_cvt_pk_bf16_f32 v54, v108, v109\n"
    "v_cvt_pk_bf16_f32 v55, v110, v111\n"
    "buffer_store_dwordx4 v[52:55], v205, s[28:31], 0 offen offset:320 sc0 sc1\n"
    "v_accvgpr_read_b32 v96, a4\n"
    "v_accvgpr_read_b32 v97, a5\n"
    "v_accvgpr_read_b32 v98, a6\n"
    "v_accvgpr_read_b32 v99, a7\n"
    "v_pk_fma_f32 v[96:97], v[96:97], v[224:225], s[94:95]\n"
    "v_pk_fma_f32 v[98:99], v[98:99], v[224:225], s[94:95]\n"
    "v_cvt_pk_bf16_f32 v56, v96, v97\n"
    "v_cvt_pk_bf16_f32 v57, v98, v99\n"
    "v_accvgpr_read_b32 v100, a20\n"
    "v_accvgpr_read_b32 v101, a21\n"
    "v_accvgpr_read_b32 v102, a22\n"
    "v_accvgpr_read_b32 v103, a23\n"
    "v_pk_fma_f32 v[100:101], v[100:101], v[224:225], s[94:95]\n"
    "v_pk_fma_f32 v[102:103], v[102:103], v[224:225], s[94:95]\n"
    "v_cvt_pk_bf16_f32 v58, v100, v101\n"
    "v_cvt_pk_bf16_f32 v59, v102, v103\n"
    "buffer_store_dwordx4 v[56:59], v205, s[28:31], s62 offen sc0 sc1\n"
    "v_accvgpr_read_b32 v104, a36\n"
    "v_accvgpr_read_b32 v105, a37\n"
    "v_accvgpr_read_b32 v106, a38\n"
    "v_accvgpr_read_b32 v107, a39\n"
    "v_pk_fma_f32 v[104:105], v[104:105], v[224:225], s[94:95]\n"
    "v_pk_fma_f32 v[106:107], v[106:107], v[224:225], s[94:95]\n"
    "v_cvt_pk_bf16_f32 v60, v104, v105\n"
    "v_cvt_pk_bf16_f32 v61, v106, v107\n"
    "v_accvgpr_read_b32 v108, a52\n"
    "v_accvgpr_read_b32 v109, a53\n"
    "v_accvgpr_read_b32 v110, a54\n"
    "v_accvgpr_read_b32 v111, a55\n"
    "v_pk_fma_f32 v[108:109], v[108:109], v[224:225], s[94:95]\n"
    "v_pk_fma_f32 v[110:111], v[110:111], v[224:225], s[94:95]\n"
    "v_cvt_pk_bf16_f32 v62, v108, v109\n"
    "v_cvt_pk_bf16_f32 v63, v110, v111\n"
    "buffer_store_dwordx4 v[60:63], v205, s[28:31], s62 offen offset:64 sc0 sc1\n"
    "v_accvgpr_read_b32 v96, a68\n"
    "v_accvgpr_read_b32 v97, a69\n"
    "v_accvgpr_read_b32 v98, a70\n"
    "v_accvgpr_read_b32 v99, a71\n"
    "v_pk_fma_f32 v[96:97], v[96:97], v[224:225], s[94:95]\n"
    "v_pk_fma_f32 v[98:99], v[98:99], v[224:225], s[94:95]\n"
    "v_cvt_pk_bf16_f32 v32, v96, v97\n"
    "v_cvt_pk_bf16_f32 v33, v98, v99\n"
    "v_accvgpr_read_b32 v100, a84\n"
    "v_accvgpr_read_b32 v101, a85\n"
    "v_accvgpr_read_b32 v102, a86\n"
    "v_accvgpr_read_b32 v103, a87\n"
    "v_pk_fma_f32 v[100:101], v[100:101], v[224:225], s[94:95]\n"
    "v_pk_fma_f32 v[102:103], v[102:103], v[224:225], s[94:95]\n"
    "v_cvt_pk_bf16_f32 v34, v100, v101\n"
    "v_cvt_pk_bf16_f32 v35, v102, v103\n"
    "buffer_store_dwordx4 v[32:35], v205, s[28:31], s62 offen offset:128 sc0 sc1\n"
    "v_accvgpr_read_b32 v104, a100\n"
    "v_accvgpr_read_b32 v105, a101\n"
    "v_accvgpr_read_b32 v106, a102\n"
    "v_accvgpr_read_b32 v107, a103\n"
    "v_pk_fma_f32 v[104:105], v[104:105], v[224:225], s[94:95]\n"
    "v_pk_fma_f32 v[106:107], v[106:107], v[224:225], s[94:95]\n"
    "v_cvt_pk_bf16_f32 v36, v104, v105\n"
    "v_cvt_pk_bf16_f32 v37, v106, v107\n"
    "v_accvgpr_read_b32 v108, a116\n"
    "v_accvgpr_read_b32 v109, a117\n"
    "v_accvgpr_read_b32 v110, a118\n"
    "v_accvgpr_read_b32 v111, a119\n"
    "v_pk_fma_f32 v[108:109], v[108:109], v[224:225], s[94:95]\n"
    "v_pk_fma_f32 v[110:111], v[110:111], v[224:225], s[94:95]\n"
    "v_cvt_pk_bf16_f32 v38, v108, v109\n"
    "v_cvt_pk_bf16_f32 v39, v110, v111\n"
    "buffer_store_dwordx4 v[36:39], v205, s[28:31], s62 offen offset:192 sc0 sc1\n"
    "v_accvgpr_read_b32 v96, a132\n"
    "v_accvgpr_read_b32 v97, a133\n"
    "v_accvgpr_read_b32 v98, a134\n"
    "v_accvgpr_read_b32 v99, a135\n"
    "v_pk_fma_f32 v[96:97], v[96:97], v[224:225], s[94:95]\n"
    "v_pk_fma_f32 v[98:99], v[98:99], v[224:225], s[94:95]\n"
    "v_cvt_pk_bf16_f32 v40, v96, v97\n"
    "v_cvt_pk_bf16_f32 v41, v98, v99\n"
    "v_accvgpr_read_b32 v100, a148\n"
    "v_accvgpr_read_b32 v101, a149\n"
    "v_accvgpr_read_b32 v102, a150\n"
    "v_accvgpr_read_b32 v103, a151\n"
    "v_pk_fma_f32 v[100:101], v[100:101], v[224:225], s[94:95]\n"
    "v_pk_fma_f32 v[102:103], v[102:103], v[224:225], s[94:95]\n"
    "v_cvt_pk_bf16_f32 v42, v100, v101\n"
    "v_cvt_pk_bf16_f32 v43, v102, v103\n"
    "buffer_store_dwordx4 v[40:43], v205, s[28:31], s62 offen offset:256 sc0 sc1\n"
    "v_accvgpr_read_b32 v104, a164\n"
    "v_accvgpr_read_b32 v105, a165\n"
    "v_accvgpr_read_b32 v106, a166\n"
    "v_accvgpr_read_b32 v107, a167\n"
    "v_pk_fma_f32 v[104:105], v[104:105], v[224:225], s[94:95]\n"
    "v_pk_fma_f32 v[106:107], v[106:107], v[224:225], s[94:95]\n"
    "v_cvt_pk_bf16_f32 v44, v104, v105\n"
    "v_cvt_pk_bf16_f32 v45, v106, v107\n"
    "v_accvgpr_read_b32 v108, a180\n"
    "v_accvgpr_read_b32 v109, a181\n"
    "v_accvgpr_read_b32 v110, a182\n"
    "v_accvgpr_read_b32 v111, a183\n"
    "v_pk_fma_f32 v[108:109], v[108:109], v[224:225], s[94:95]\n"
    "v_pk_fma_f32 v[110:111], v[110:111], v[224:225], s[94:95]\n"
    "v_cvt_pk_bf16_f32 v46, v108, v109\n"
    "v_cvt_pk_bf16_f32 v47, v110, v111\n"
    "buffer_store_dwordx4 v[44:47], v205, s[28:31], s62 offen offset:320 sc0 sc1\n"
    "v_accvgpr_read_b32 v96, a8\n"
    "v_accvgpr_read_b32 v97, a9\n"
    "v_accvgpr_read_b32 v98, a10\n"
    "v_accvgpr_read_b32 v99, a11\n"
    "v_pk_fma_f32 v[96:97], v[96:97], v[226:227], s[94:95]\n"
    "v_pk_fma_f32 v[98:99], v[98:99], v[226:227], s[94:95]\n"
    "v_cvt_pk_bf16_f32 v48, v96, v97\n"
    "v_cvt_pk_bf16_f32 v49, v98, v99\n"
    "v_accvgpr_read_b32 v100, a24\n"
    "v_accvgpr_read_b32 v101, a25\n"
    "v_accvgpr_read_b32 v102, a26\n"
    "v_accvgpr_read_b32 v103, a27\n"
    "v_pk_fma_f32 v[100:101], v[100:101], v[226:227], s[94:95]\n"
    "v_pk_fma_f32 v[102:103], v[102:103], v[226:227], s[94:95]\n"
    "v_cvt_pk_bf16_f32 v50, v100, v101\n"
    "v_cvt_pk_bf16_f32 v51, v102, v103\n"
    "buffer_store_dwordx4 v[48:51], v205, s[28:31], s63 offen sc0 sc1\n"
    "v_accvgpr_read_b32 v104, a40\n"
    "v_accvgpr_read_b32 v105, a41\n"
    "v_accvgpr_read_b32 v106, a42\n"
    "v_accvgpr_read_b32 v107, a43\n"
    "v_pk_fma_f32 v[104:105], v[104:105], v[226:227], s[94:95]\n"
    "v_pk_fma_f32 v[106:107], v[106:107], v[226:227], s[94:95]\n"
    "v_cvt_pk_bf16_f32 v52, v104, v105\n"
    "v_cvt_pk_bf16_f32 v53, v106, v107\n"
    "v_accvgpr_read_b32 v108, a56\n"
    "v_accvgpr_read_b32 v109, a57\n"
    "v_accvgpr_read_b32 v110, a58\n"
    "v_accvgpr_read_b32 v111, a59\n"
    "v_pk_fma_f32 v[108:109], v[108:109], v[226:227], s[94:95]\n"
    "v_pk_fma_f32 v[110:111], v[110:111], v[226:227], s[94:95]\n"
    "v_cvt_pk_bf16_f32 v54, v108, v109\n"
    "v_cvt_pk_bf16_f32 v55, v110, v111\n"
    "buffer_store_dwordx4 v[52:55], v205, s[28:31], s63 offen offset:64 sc0 sc1\n"
    "v_accvgpr_read_b32 v96, a72\n"
    "v_accvgpr_read_b32 v97, a73\n"
    "v_accvgpr_read_b32 v98, a74\n"
    "v_accvgpr_read_b32 v99, a75\n"
    "v_pk_fma_f32 v[96:97], v[96:97], v[226:227], s[94:95]\n"
    "v_pk_fma_f32 v[98:99], v[98:99], v[226:227], s[94:95]\n"
    "v_cvt_pk_bf16_f32 v56, v96, v97\n"
    "v_cvt_pk_bf16_f32 v57, v98, v99\n"
    "v_accvgpr_read_b32 v100, a88\n"
    "v_accvgpr_read_b32 v101, a89\n"
    "v_accvgpr_read_b32 v102, a90\n"
    "v_accvgpr_read_b32 v103, a91\n"
    "v_pk_fma_f32 v[100:101], v[100:101], v[226:227], s[94:95]\n"
    "v_pk_fma_f32 v[102:103], v[102:103], v[226:227], s[94:95]\n"
    "v_cvt_pk_bf16_f32 v58, v100, v101\n"
    "v_cvt_pk_bf16_f32 v59, v102, v103\n"
    "buffer_store_dwordx4 v[56:59], v205, s[28:31], s63 offen offset:128 sc0 sc1\n"
    "v_accvgpr_read_b32 v104, a104\n"
    "v_accvgpr_read_b32 v105, a105\n"
    "v_accvgpr_read_b32 v106, a106\n"
    "v_accvgpr_read_b32 v107, a107\n"
    "v_pk_fma_f32 v[104:105], v[104:105], v[226:227], s[94:95]\n"
    "v_pk_fma_f32 v[106:107], v[106:107], v[226:227], s[94:95]\n"
    "v_cvt_pk_bf16_f32 v60, v104, v105\n"
    "v_cvt_pk_bf16_f32 v61, v106, v107\n"
    "v_accvgpr_read_b32 v108, a120\n"
    "v_accvgpr_read_b32 v109, a121\n"
    "v_accvgpr_read_b32 v110, a122\n"
    "v_accvgpr_read_b32 v111, a123\n"
    "v_pk_fma_f32 v[108:109], v[108:109], v[226:227], s[94:95]\n"
    "v_pk_fma_f32 v[110:111], v[110:111], v[226:227], s[94:95]\n"
    "v_cvt_pk_bf16_f32 v62, v108, v109\n"
    "v_cvt_pk_bf16_f32 v63, v110, v111\n"
    "buffer_store_dwordx4 v[60:63], v205, s[28:31], s63 offen offset:192 sc0 sc1\n"
    "v_accvgpr_read_b32 v96, a136\n"
    "v_accvgpr_read_b32 v97, a137\n"
    "v_accvgpr_read_b32 v98, a138\n"
    "v_accvgpr_read_b32 v99, a139\n"
    "v_pk_fma_f32 v[96:97], v[96:97], v[226:227], s[94:95]\n"
    "v_pk_fma_f32 v[98:99], v[98:99], v[226:227], s[94:95]\n"
    "v_cvt_pk_bf16_f32 v32, v96, v97\n"
    "v_cvt_pk_bf16_f32 v33, v98, v99\n"
    "v_accvgpr_read_b32 v100, a152\n"
    "v_accvgpr_read_b32 v101, a153\n"
    "v_accvgpr_read_b32 v102, a154\n"
    "v_accvgpr_read_b32 v103, a155\n"
    "v_pk_fma_f32 v[100:101], v[100:101], v[226:227], s[94:95]\n"
    "v_pk_fma_f32 v[102:103], v[102:103], v[226:227], s[94:95]\n"
    "v_cvt_pk_bf16_f32 v34, v100, v101\n"
    "v_cvt_pk_bf16_f32 v35, v102, v103\n"
    "buffer_store_dwordx4 v[32:35], v205, s[28:31], s63 offen offset:256 sc0 sc1\n"
    "v_accvgpr_read_b32 v104, a168\n"
    "v_accvgpr_read_b32 v105, a169\n"
    "v_accvgpr_read_b32 v106, a170\n"
    "v_accvgpr_read_b32 v107, a171\n"
    "v_pk_fma_f32 v[104:105], v[104:105], v[226:227], s[94:95]\n"
    "v_pk_fma_f32 v[106:107], v[106:107], v[226:227], s[94:95]\n"
    "v_cvt_pk_bf16_f32 v36, v104, v105\n"
    "v_cvt_pk_bf16_f32 v37, v106, v107\n"
    "v_accvgpr_read_b32 v108, a184\n"
    "v_accvgpr_read_b32 v109, a185\n"
    "v_accvgpr_read_b32 v110, a186\n"
    "v_accvgpr_read_b32 v111, a187\n"
    "v_pk_fma_f32 v[108:109], v[108:109], v[226:227], s[94:95]\n"
    "v_pk_fma_f32 v[110:111], v[110:111], v[226:227], s[94:95]\n"
    "v_cvt_pk_bf16_f32 v38, v108, v109\n"
    "v_cvt_pk_bf16_f32 v39, v110, v111\n"
    "buffer_store_dwordx4 v[36:39], v205, s[28:31], s63 offen offset:320 sc0 sc1\n"
    "v_accvgpr_read_b32 v96, a12\n"
    "v_accvgpr_read_b32 v97, a13\n"
    "v_accvgpr_read_b32 v98, a14\n"
    "v_accvgpr_read_b32 v99, a15\n"
    "v_pk_fma_f32 v[96:97], v[96:97], v[228:229], s[94:95]\n"
    "v_pk_fma_f32 v[98:99], v[98:99], v[228:229], s[94:95]\n"
    "v_cvt_pk_bf16_f32 v40, v96, v97\n"
    "v_cvt_pk_bf16_f32 v41, v98, v99\n"
    "v_accvgpr_read_b32 v100, a28\n"
    "v_accvgpr_read_b32 v101, a29\n"
    "v_accvgpr_read_b32 v102, a30\n"
    "v_accvgpr_read_b32 v103, a31\n"
    "v_pk_fma_f32 v[100:101], v[100:101], v[228:229], s[94:95]\n"
    "v_pk_fma_f32 v[102:103], v[102:103], v[228:229], s[94:95]\n"
    "v_cvt_pk_bf16_f32 v42, v100, v101\n"
    "v_cvt_pk_bf16_f32 v43, v102, v103\n"
    "buffer_store_dwordx4 v[40:43], v205, s[28:31], s64 offen sc0 sc1\n"
    "v_accvgpr_read_b32 v104, a44\n"
    "v_accvgpr_read_b32 v105, a45\n"
    "v_accvgpr_read_b32 v106, a46\n"
    "v_accvgpr_read_b32 v107, a47\n"
    "v_pk_fma_f32 v[104:105], v[104:105], v[228:229], s[94:95]\n"
    "v_pk_fma_f32 v[106:107], v[106:107], v[228:229], s[94:95]\n"
    "v_cvt_pk_bf16_f32 v44, v104, v105\n"
    "v_cvt_pk_bf16_f32 v45, v106, v107\n"
    "v_accvgpr_read_b32 v108, a60\n"
    "v_accvgpr_read_b32 v109, a61\n"
    "v_accvgpr_read_b32 v110, a62\n"
    "v_accvgpr_read_b32 v111, a63\n"
    "v_pk_fma_f32 v[108:109], v[108:109], v[228:229], s[94:95]\n"
    "v_pk_fma_f32 v[110:111], v[110:111], v[228:229], s[94:95]\n"
    "v_cvt_pk_bf16_f32 v46, v108, v109\n"
    "v_cvt_pk_bf16_f32 v47, v110, v111\n"
    "buffer_store_dwordx4 v[44:47], v205, s[28:31], s64 offen offset:64 sc0 sc1\n"
    "v_accvgpr_read_b32 v96, a76\n"
    "v_accvgpr_read_b32 v97, a77\n"
    "v_accvgpr_read_b32 v98, a78\n"
    "v_accvgpr_read_b32 v99, a79\n"
    "v_pk_fma_f32 v[96:97], v[96:97], v[228:229], s[94:95]\n"
    "v_pk_fma_f32 v[98:99], v[98:99], v[228:229], s[94:95]\n"
    "v_cvt_pk_bf16_f32 v48, v96, v97\n"
    "v_cvt_pk_bf16_f32 v49, v98, v99\n"
    "v_accvgpr_read_b32 v100, a92\n"
    "v_accvgpr_read_b32 v101, a93\n"
    "v_accvgpr_read_b32 v102, a94\n"
    "v_accvgpr_read_b32 v103, a95\n"
    "v_pk_fma_f32 v[100:101], v[100:101], v[228:229], s[94:95]\n"
    "v_pk_fma_f32 v[102:103], v[102:103], v[228:229], s[94:95]\n"
    "v_cvt_pk_bf16_f32 v50, v100, v101\n"
    "v_cvt_pk_bf16_f32 v51, v102, v103\n"
    "buffer_store_dwordx4 v[48:51], v205, s[28:31], s64 offen offset:128 sc0 sc1\n"
    "v_accvgpr_read_b32 v104, a108\n"
    "v_accvgpr_read_b32 v105, a109\n"
    "v_accvgpr_read_b32 v106, a110\n"
    "v_accvgpr_read_b32 v107, a111\n"
    "v_pk_fma_f32 v[104:105], v[104:105], v[228:229], s[94:95]\n"
    "v_pk_fma_f32 v[106:107], v[106:107], v[228:229], s[94:95]\n"
    "v_cvt_pk_bf16_f32 v52, v104, v105\n"
    "v_cvt_pk_bf16_f32 v53, v106, v107\n"
    "v_accvgpr_read_b32 v108, a124\n"
    "v_accvgpr_read_b32 v109, a125\n"
    "v_accvgpr_read_b32 v110, a126\n"
    "v_accvgpr_read_b32 v111, a127\n"
    "v_pk_fma_f32 v[108:109], v[108:109], v[228:229], s[94:95]\n"
    "v_pk_fma_f32 v[110:111], v[110:111], v[228:229], s[94:95]\n"
    "v_cvt_pk_bf16_f32 v54, v108, v109\n"
    "v_cvt_pk_bf16_f32 v55, v110, v111\n"
    "buffer_store_dwordx4 v[52:55], v205, s[28:31], s64 offen offset:192 sc0 sc1\n"
    "v_accvgpr_read_b32 v96, a140\n"
    "v_accvgpr_read_b32 v97, a141\n"
    "v_accvgpr_read_b32 v98, a142\n"
    "v_accvgpr_read_b32 v99, a143\n"
    "v_pk_fma_f32 v[96:97], v[96:97], v[228:229], s[94:95]\n"
    "v_pk_fma_f32 v[98:99], v[98:99], v[228:229], s[94:95]\n"
    "v_cvt_pk_bf16_f32 v56, v96, v97\n"
    "v_cvt_pk_bf16_f32 v57, v98, v99\n"
    "v_accvgpr_read_b32 v100, a156\n"
    "v_accvgpr_read_b32 v101, a157\n"
    "v_accvgpr_read_b32 v102, a158\n"
    "v_accvgpr_read_b32 v103, a159\n"
    "v_pk_fma_f32 v[100:101], v[100:101], v[228:229], s[94:95]\n"
    "v_pk_fma_f32 v[102:103], v[102:103], v[228:229], s[94:95]\n"
    "v_cvt_pk_bf16_f32 v58, v100, v101\n"
    "v_cvt_pk_bf16_f32 v59, v102, v103\n"
    "buffer_store_dwordx4 v[56:59], v205, s[28:31], s64 offen offset:256 sc0 sc1\n"
    "v_accvgpr_read_b32 v104, a172\n"
    "v_accvgpr_read_b32 v105, a173\n"
    "v_accvgpr_read_b32 v106, a174\n"
    "v_accvgpr_read_b32 v107, a175\n"
    "v_pk_fma_f32 v[104:105], v[104:105], v[228:229], s[94:95]\n"
    "v_pk_fma_f32 v[106:107], v[106:107], v[228:229], s[94:95]\n"
    "v_cvt_pk_bf16_f32 v60, v104, v105\n"
    "v_cvt_pk_bf16_f32 v61, v106, v107\n"
    "v_accvgpr_read_b32 v108, a188\n"
    "v_accvgpr_read_b32 v109, a189\n"
    "v_accvgpr_read_b32 v110, a190\n"
    "v_accvgpr_read_b32 v111, a191\n"
    "v_pk_fma_f32 v[108:109], v[108:109], v[228:229], s[94:95]\n"
    "v_pk_fma_f32 v[110:111], v[110:111], v[228:229], s[94:95]\n"
    "v_cvt_pk_bf16_f32 v62, v108, v109\n"
    "v_cvt_pk_bf16_f32 v63, v110, v111\n"
    "buffer_store_dwordx4 v[60:63], v205, s[28:31], s64 offen offset:320 sc0 sc1\n"
    "s_branch LFIN\n"
    "LEND:\n"
    :
    : "s"(__builtin_amdgcn_kernarg_segment_ptr()), "s"(blockIdx.x), "s"(wid)
    : "v0", "v1", "v2", "v3", "v4", "v5", "v6", "v7", "v8", "v9", "v10", "v11", "v12", "v13", "v14", "v15", "v16", "v17", "v18", "v19", "v20", "v21", "v22", "v23", "v24", "v25", "v26", "v27", "v28", "v29", "v30", "v31", "v32", "v33", "v34", "v35", "v36", "v37", "v38", "v39", "v40", "v41", "v42", "v43", "v44", "v45", "v46", "v47", "v48", "v49", "v50", "v51", "v52", "v53", "v54", "v55", "v56", "v57", "v58", "v59", "v60", "v61", "v62", "v63", "v64", "v65", "v66", "v67", "v68", "v69", "v70", "v71", "v72", "v73", "v74", "v75", "v76", "v77", "v78", "v79", "v80", "v81", "v82", "v83", "v84", "v85", "v86", "v87", "v88", "v89", "v90", "v91", "v92", "v93", "v94", "v95", "v96", "v97", "v98", "v99", "v100", "v101", "v102", "v103", "v104", "v105", "v106", "v107", "v108", "v109", "v110", "v111", "v112", "v113", "v114", "v115", "v116", "v117", "v118", "v119", "v120", "v121", "v122", "v123", "v124", "v125", "v126", "v127", "v128", "v129", "v130", "v131", "v132", "v133", "v134", "v135", "v136", "v137", "v138", "v139", "v140", "v141", "v142", "v143", "v144", "v145", "v146", "v147", "v148", "v149", "v150", "v151", "v152", "v153", "v154", "v155", "v156", "v157", "v158", "v159", "v160", "v161", "v162", "v163", "v164", "v165", "v166", "v167", "v168", "v169", "v170", "v171", "v172", "v173", "v174", "v175", "v176", "v177", "v178", "v179", "v180", "v181", "v182", "v183", "v184", "v185", "v186", "v187", "v188", "v189", "v190", "v191", "v192", "v193", "v194", "v195", "v196", "v197", "v198", "v199", "v200", "v201", "v202", "v203", "v204", "v205", "v206", "v207", "v208", "v209", "v210", "v211", "v212", "v213", "v214", "v215", "v216", "v217", "v218", "v219", "v220", "v221", "v222", "v223", "v224", "v225", "v226", "v227", "v228", "v229", "v230", "v231", "v232", "v233", "v234", "v235", "v236", "v237", "v238", "v239", "v240", "v241", "v242", "v243", "v244", "v245", "v246", "v247", "v248", "v249", "v250", "v251", "v252", "v253", "v254", "v255", "a0", "a1", "a2", "a3", "a4", "a5", "a6", "a7", "a8", "a9", "a10", "a11", "a12", "a13", "a14", "a15", "a16", "a17", "a18", "a19", "a20", "a21", "a22", "a23", "a24", "a25", "a26", "a27", "a28", "a29", "a30", "a31", "a32", "a33", "a34", "a35", "a36", "a37", "a38", "a39", "a40", "a41", "a42", "a43", "a44", "a45", "a46", "a47", "a48", "a49", "a50", "a51", "a52", "a53", "a54", "a55", "a56", "a57", "a58", "a59", "a60", "a61", "a62", "a63", "a64", "a65", "a66", "a67", "a68", "a69", "a70", "a71", "a72", "a73", "a74", "a75", "a76", "a77", "a78", "a79", "a80", "a81", "a82", "a83", "a84", "a85", "a86", "a87", "a88", "a89", "a90", "a91", "a92", "a93", "a94", "a95", "a96", "a97", "a98", "a99", "a100", "a101", "a102", "a103", "a104", "a105", "a106", "a107", "a108", "a109", "a110", "a111", "a112", "a113", "a114", "a115", "a116", "a117", "a118", "a119", "a120", "a121", "a122", "a123", "a124", "a125", "a126", "a127", "a128", "a129", "a130", "a131", "a132", "a133", "a134", "a135", "a136", "a137", "a138", "a139", "a140", "a141", "a142", "a143", "a144", "a145", "a146", "a147", "a148", "a149", "a150", "a151", "a152", "a153", "a154", "a155", "a156", "a157", "a158", "a159", "a160", "a161", "a162", "a163", "a164", "a165", "a166", "a167", "a168", "a169", "a170", "a171", "a172", "a173", "a174", "a175", "a176", "a177", "a178", "a179", "a180", "a181", "a182", "a183", "a184", "a185", "a186", "a187", "a188", "a189", "a190", "a191", "a192", "a193", "a194", "a195", "a196", "a197", "a198", "a199", "a200", "a201", "a202", "a203", "a204", "a205", "a206", "a207", "a208", "a209", "a210", "a211", "a212", "a213", "a214", "a215", "a216", "a217", "a218", "a219", "a220", "a221", "a222", "a223", "a224", "a225", "a226", "a227", "a228", "a229", "a230", "a231", "a232", "a233", "a234", "a235", "a236", "a237", "a238", "a239", "a240", "a241", "a242", "a243", "a244", "a245", "a246", "a247", "a248", "a249", "a250", "a251", "a252", "a253", "a254", "a255", "s8", "s9", "s10", "s11", "s12", "s13", "s14", "s15", "s16", "s17", "s18", "s19", "s20", "s21", "s22", "s23", "s24", "s25", "s26", "s27", "s28", "s29", "s30", "s31", "s32", "s33", "s34", "s35", "s36", "s37", "s38", "s39", "s40", "s41", "s42", "s43", "s44", "s45", "s46", "s47", "s48", "s49", "s50", "s51", "s52", "s53", "s54", "s55", "s56", "s57", "s58", "s59", "s60", "s61", "s62", "s63", "s64", "s65", "s66", "s67", "s68", "s69", "s70", "s71", "s72", "s73", "s74", "s75", "s76", "s77", "s78", "s79", "s80", "s81", "s82", "s83", "s84", "s85", "s86", "s87", "s88", "s89", "s90", "s91", "s92", "s93", "s94", "s95", "s96", "s97", "s98", "s99", "s100", "s101", "vcc", "scc", "memory");
}

extern "C" void kernel_launch(void* const* d_in, const int* in_sizes, int n_in, void* d_out, int out_size, void* d_ws, size_t ws_size,
                              hipStream_t stream) {
  (void)hipFuncSetAttribute((const void*)attn_fused, hipFuncAttributeMaxDynamicSharedMemorySize, 153088);
  attn_fused<<<256, 256, 153088, stream>>>(d_in[0], d_in[1], d_in[2], d_in[3], d_in[4], d_in[5], d_in[6], d_ws, d_out);
}
```

```cpp
#include <hip/hip_runtime.h>

__global__ __launch_bounds__(256, 1) void attn_fused(const void* q, const void* qk, const void* qv, const void* ks, const void* kz,
                                                     const void* vs, const void* vz, void* ws, void* out) {
  const int wid = __builtin_amdgcn_readfirstlane(threadIdx.x >> 6);
  asm volatile(
    "s_load_dwordx16 s[8:23], %0, 0x0\n"
    "s_mov_b32 s58, %2\n"
    "s_and_b32 s76, %1, 7\n"
    "s_lshr_b32 s77, %1, 3\n"
    "s_and_b32 s78, s77, 3\n"
    "s_lshl_b32 s56, s76, 2\n"
    "s_add_u32 s56, s56, s78\n"
    "s_lshr_b32 s57, s77, 2\n"
    "v_mbcnt_lo_u32_b32 v231, -1, 0\n"
    "v_mbcnt_hi_u32_b32 v231, -1, v231\n"
    "s_getpc_b64 s[44:45]\n"
    "LPC:\n"
    "s_mov_b32 s46, LSLOW-LPC\n"
    "s_mov_b32 s47, 0x20000\n"
    "s_lshr_b32 s76, %1, 3\n"
    "s_lshl_b32 s76, s76, 2\n"
    "s_add_u32 s76, s76, s58\n"
    "s_lshl_b32 s76, s76, 9\n"
    "v_lshlrev_b32 v196, 3, v231\n"
    "buffer_load_dword v251, v196, s[44:47], s76 offen\n"
    "s_mul_i32 s72, s58, 0xc00\n"
    "s_lshr_b32 s86, s57, 2\n"
    "s_mul_i32 s64, s86, 0xc000\n"
    "s_lshl_b32 s86, s86, 3\n"
    "s_and_b32 s79, s57, 3\n"
    "s_add_u32 s86, s86, s79\n"
    "s_lshl_b32 s86, s86, 2\n"
    "s_xor_b32 s76, s57, 4\n"
    "s_and_b32 s77, s76, 6\n"
    "s_mul_i32 s77, s77, 15\n"
    "s_add_u32 s76, s76, s77\n"
    "s_and_b32 s77, s58, 1\n"
    "s_mul_i32 s78, s77, 12\n"
    "s_add_u32 s78, s78, s86\n"
    "s_lshl_b32 s78, s78, 1\n"
    "s_mul_i32 s77, s77, 0xc0\n"
    "s_movk_i32 s90, 0x556\n"
    "s_lshl_b32 s60, s56, 12\n"
    "s_cmp_lt_u32 s58, 2\n"
    "s_cselect_b32 s76, s76, s78\n"
    "s_cmovk_i32 s90, 0xaab\n"
    "s_cselect_b32 s91, 24, 0\n"
    "s_cselect_b32 s92, 1, 10\n"
    "s_cselect_b32 s89, 7, 0\n"
    "s_cselect_b32 s64, s64, 0\n"
    "s_add_u32 s60, s60, s76\n"
    "s_mul_i32 s60, s60, 0x180\n"
    "s_mul_i32 s76, s76, 0x180\n"
    "s_sub_u32 s34, 0x180000, s76\n"
    "s_mov_b32 s35, 0x20000\n"
    "v_add_u32 v144, s77, v231\n"
    "v_add_u32 v145, 64, v144\n"
    "v_add_u32 v146, 64, v145\n"
    "v_mul_u32_u24 v197, s90, v144\n"
    "v_lshrrev_b32 v197, 16, v197\n"
    "v_mul_u32_u24 v198, s92, v197\n"
    "v_lshrrev_b32 v198, 1, v198\n"
    "v_and_b32 v198, s89, v198\n"
    "v_xor_b32 v198, v198, v144\n"
    "v_mul_u32_u24 v197, s91, v197\n"
    "v_add_lshl_u32 v202, v197, v198, 4\n"
    "v_mul_u32_u24 v197, s90, v145\n"
    "v_lshrrev_b32 v197, 16, v197\n"
    "v_mul_u32_u24 v198, s92, v197\n"
    "v_lshrrev_b32 v198, 1, v198\n"
    "v_and_b32 v198, s89, v198\n"
    "v_xor_b32 v198, v198, v144\n"
    "v_mul_u32_u24 v197, s91, v197\n"
    "v_add_lshl_u32 v203, v197, v198, 4\n"
    "v_mul_u32_u24 v197, s90, v146\n"
    "v_lshrrev_b32 v197, 16, v197\n"
    "v_mul_u32_u24 v198, s92, v197\n"
    "v_lshrrev_b32 v198, 1, v198\n"
    "v_and_b32 v198, s89, v198\n"
    "v_xor_b32 v198, v198, v144\n"
    "v_mul_u32_u24 v197, s91, v197\n"
    "v_add_lshl_u32 v204, v197, v198, 4\n"
    "s_waitcnt lgkmcnt(0)\n"
    "s_cmp_lt_u32 s58, 2\n"
    "s_cselect_b64 s[80:81], s[10:11], s[12:13]\n"
    "s_cselect_b64 s[82:83], s[16:17], s[14:15]\n"
    "s_add_u32 s32, s80, s60\n"
    "s_addc_u32 s33, s81, 0\n"
    "s_cmp_eq_u32 s64, 0\n"
    "s_cbranch_scc1 LNOR\n"
    "s_add_u32 m0, s72, 0x12000\n"
    "s_nop 0\n"
    "buffer_load_dwordx4 v202, s[32:35], 0 offen nt lds\n"
    "buffer_load_dwordx4 v203, s[32:35], 0 offen offset:1024 nt lds\n"
    "buffer_load_dwordx4 v204, s[32:35], 0 offen offset:2048 nt lds\n"
    "LNOR:\n"
    "s_add_u32 m0, s72, 0x15000\n"
    "s_nop 0\n"
    "buffer_load_dwordx4 v202, s[32:35], s64 offen nt lds\n"
    "buffer_load_dwordx4 v203, s[32:35], s64 offen offset:1024 nt lds\n"
    "buffer_load_dwordx4 v204, s[32:35], s64 offen offset:2048 nt lds\n"
    "LR0:\n"
    "s_load_dwordx2 s[24:25], %0, 0x40\n"
    "s_mul_i32 s71, s58, 0x1800\n"
    "s_lshr_b32 s87, s57, 2\n"
    "s_mul_i32 s87, s87, 0x6000\n"
    "s_mul_i32 s61, s56, 0x3000\n"
    "s_lshl_b32 s62, s56, 12\n"
    "s_lshl_b32 s59, s56, 12\n"
    "s_mov_b32 s50, 0x3000\n"
    "s_mov_b32 s51, 0x20000\n"
    "s_and_b32 s77, s57, 1\n"
    "s_mul_i32 s77, s77, 0xc0\n"
    "v_mul_u32_u24 v197, 0x1556, v144\n"
    "v_lshrrev_b32 v197, 16, v197\n"
    "v_mul_u32_u24 v196, 12, v197\n"
    "v_sub_u32 v196, v144, v196\n"
    "v_mul_u32_u24 v248, 0x180, v197\n"
    "v_lshl_add_u32 v248, v196, 4, v248\n"
    "v_add_u32 v248, s77, v248\n"
    "s_add_u32 s48, s82, s61\n"
    "s_addc_u32 s49, s83, 0\n"
    "s_add_u32 m0, s72, 0x21000\n"
    "s_nop 0\n"
    "buffer_load_dwordx4 v248, s[48:51], 0 offen lds\n"
    "s_add_u32 s40, s20, s62\n"
    "s_addc_u32 s41, s21, 0\n"
    "s_add_u32 s52, s18, s62\n"
    "s_addc_u32 s53, s19, 0\n"
    "s_mov_b32 s42, 0x1000\n"
    "s_mov_b32 s43, 0x20000\n"
    "s_mov_b32 s54, 0x1000\n"
    "s_mov_b32 s55, 0x20000\n"
    "v_lshlrev_b32 v196, 1, v231\n"
    "buffer_load_ushort v44, v196, s[40:43], 0 offen\n"
    "buffer_load_ushort v45, v196, s[52:55], 0 offen\n"
    "s_lshl_b32 s77, s58, 6\n"
    "v_add_u32 v41, s77, v231\n"
    "v_bfe_u32 v197, v231, 2, 1\n"
    "v_mul_u32_u24 v197, 12, v197\n"
    "v_and_b32 v198, 7, v231\n"
    "v_add3_u32 v197, v197, v198, s86\n"
    "v_lshrrev_b32 v40, 3, v41\n"
    "v_lshlrev_b32 v40, 7, v40\n"
    "v_lshl_add_u32 v40, v197, 1, v40\n"
    "buffer_load_ushort v42, v40, s[40:43], 0 offen\n"
    "buffer_load_ushort v43, v40, s[52:55], 0 offen\n"
    "s_add_u32 s44, s22, s59\n"
    "s_addc_u32 s45, s23, 0\n"
    "s_mov_b32 s46, 0x900\n"
    "s_mov_b32 s47, 0x20000\n"
    "v_mov_b32 v196, 0x40000000\n"
    "s_cmp_eq_u32 s58, 0\n"
    "s_cselect_b32 s77, 64, 0\n"
    "s_cselect_b32 s74, 1, 0\n"
    "v_and_b32 v213, 7, v231\n"
    "v_mul_u32_u24 v213, 0x100, v213\n"
    "v_cmp_gt_u32 vcc, s77, v231\n"
    "v_mov_b32 v197, 0x800\n"
    "s_mul_i32 s77, s57, 0x100\n"
    "v_cndmask_b32 v213, v196, v213, vcc\n"
    "v_cndmask_b32 v197, v196, v197, vcc\n"
    "buffer_load_dword v221, v197, s[44:47], 0 offen sc1\n"
    "v_cmp_gt_u32 vcc, s74, v231\n"
    "v_mov_b32 v214, s77\n"
    "s_mul_i32 s66, s74, 3\n"
    "v_cndmask_b32 v214, v196, v214, vcc\n"
    "s_lshl_b32 s76, s56, 3\n"
    "s_add_u32 s76, s76, s57\n"
    "s_mul_i32 s76, s76, 0x18000\n"
    "s_add_u32 s28, s8, s76\n"
    "s_addc_u32 s29, s9, 0\n"
    "s_mov_b32 s30, 0x18000\n"
    "s_mov_b32 s31, 0x20000\n"
    "s_mul_i32 s88, s58, 0x6000\n"
    "v_mul_u32_u24 v197, 0xaab, v231\n"
    "v_lshrrev_b32 v197, 16, v197\n"
    "v_mul_u32_u24 v196, 24, v197\n"
    "v_sub_u32 v196, v231, v196\n"
    "v_mul_u32_u24 v152, 0x180, v197\n"
    "v_lshrrev_b32 v197, 1, v197\n"
    "v_xor_b32 v196, v196, v197\n"
    "v_lshl_add_u32 v152, v196, 4, v152\n"
    "v_add_u32 v198, 64, v231\n"
    "v_mul_u32_u24 v197, 0xaab, v198\n"
    "v_lshrrev_b32 v197, 16, v197\n"
    "v_mul_u32_u24 v196, 24, v197\n"
    "v_sub_u32 v196, v198, v196\n"
    "v_mul_u32_u24 v153, 0x180, v197\n"
    "v_lshrrev_b32 v197, 1, v197\n"
    "v_xor_b32 v196, v196, v197\n"
    "v_lshl_add_u32 v153, v196, 4, v153\n"
    "v_add_u32 v198, 0x80, v231\n"
    "v_mul_u32_u24 v197, 0xaab, v198\n"
    "v_lshrrev_b32 v197, 16, v197\n"
    "v_mul_u32_u24 v196, 24, v197\n"
    "v_sub_u32 v196, v198, v196\n"
    "v_mul_u32_u24 v154, 0x180, v197\n"
    "v_lshrrev_b32 v197, 1, v197\n"
    "v_xor_b32 v196, v196, v197\n"
    "v_lshl_add_u32 v154, v196, 4, v154\n"
    "s_mov_b32 s76, 0xc00\n"
    "v_xad_u32 v155, v152, 64, s76\n"
    "v_xad_u32 v156, v153, 64, s76\n"
    "v_xad_u32 v157, v154, 64, s76\n"
    "s_add_u32 s78, s88, 0\n"
    "s_add_u32 m0, s71, 0\n"
    "s_nop 0\n"
    "buffer_load_dwordx4 v152, s[28:31], s78 offen nt lds\n"
    "s_add_u32 s78, s88, 0\n"
    "s_add_u32 m0, s71, 0x400\n"
    "s_nop 0\n"
    "buffer_load_dwordx4 v153, s[28:31], s78 offen nt lds\n"
    "s_add_u32 s78, s64, 0xc000\n"
    "s_add_u32 m0, s72, 0x1e000\n"
    "s_nop 0\n"
    "buffer_load_dwordx4 v202, s[32:35], s78 offen nt lds\n"
    "buffer_load_dwordx4 v203, s[32:35], s78 offen offset:1024 nt lds\n"
    "buffer_load_dwordx4 v204, s[32:35], s78 offen offset:2048 nt lds\n"
    "v_mov_b32 v196, 0x40000000\n"
    "v_and_b32 v46, 15, v231\n"
    "v_lshrrev_b32 v47, 4, v231\n"
    "s_mov_b32 s75, 0x3dd53b95\n"
    "v_min_u32 v199, 2, v47\n"
    "v_lshlrev_b32 v197, 3, v199\n"
    "s_lshl_b32 s76, s58, 1\n"
    "v_add_u32 v197, s76, v197\n"
    "v_lshrrev_b32 v198, 1, v46\n"
    "v_xor_b32 v197, v197, v198\n"
    "v_mul_u32_u24 v205, 0x180, v46\n"
    "v_lshl_add_u32 v205, v197, 4, v205\n"
    "v_add_u32 v205, 0x12000, v205\n"
    "v_xor_b32 v206, 16, v205\n"
    "v_and_b32 v197, 7, v231\n"
    "v_lshl_add_u32 v209, v199, 5, v197\n"
    "s_lshl_b32 s76, s58, 3\n"
    "v_add_lshl_u32 v209, v209, s76, 1\n"
    "v_add_u32 v217, 0x21000, v209\n"
    "s_lshr_b32 s76, s57, 2\n"
    "s_mul_i32 s76, s76, 0xc0\n"
    "s_add_u32 s76, s76, 0x20f40\n"
    "v_add_u32 v209, s76, v209\n"
    "v_lshrrev_b32 v198, 3, v231\n"
    "s_mul_i32 s76, s58, 12\n"
    "v_lshl_add_u32 v199, v198, 1, s76\n"
    "v_mul_u32_u24 v207, 5, v197\n"
    "v_and_b32 v207, 9, v207\n"
    "v_xor_b32 v199, v199, v207\n"
    "v_mul_u32_u24 v207, 0x300, v197\n"
    "v_lshl_add_u32 v207, v199, 4, v207\n"
    "v_add_u32 v207, 0x13800, v207\n"
    "v_xor_b32 v208, 16, v207\n"
    "s_mul_i32 s76, s58, 0xc0\n"
    "s_add_u32 s76, s76, 0x13800\n"
    "v_lshl_add_u32 v207, v231, 2, s76\n"
    "v_lshlrev_b32 v210, 3, v197\n"
    "v_add_u32 v210, 0x24400, v210\n"
    "s_mul_i32 s76, s58, 0x480\n"
    "s_add_u32 s76, s76, 0x24400\n"
    "v_mul_u32_u24 v215, 0x90, v197\n"
    "v_lshl_add_u32 v215, v198, 4, v215\n"
    "v_add_u32 v215, s76, v215\n"
    "v_and_b32 v197, 1, v197\n"
    "v_lshlrev_b32 v197, 3, v197\n"
    "v_sub_u32 v215, v215, v197\n"
    "v_bfe_u32 v197, v231, 2, 2\n"
    "v_mul_u32_u24 v216, 0x90, v197\n"
    "v_lshl_add_u32 v216, v47, 5, v216\n"
    "v_and_b32 v197, 1, v197\n"
    "v_lshlrev_b32 v197, 3, v197\n"
    "v_sub_u32 v216, v216, v197\n"
    "v_and_b32 v197, 3, v231\n"
    "v_lshl_add_u32 v216, v197, 3, v216\n"
    "v_add_u32 v216, s76, v216\n"
    "s_lshr_b32 s76, s57, 1\n"
    "s_xor_b32 s76, s76, 2\n"
    "s_mul_i32 s76, s76, 0x1800\n"
    "s_and_b32 s77, s57, 1\n"
    "s_mul_i32 s77, s77, 0xc00\n"
    "s_add_u32 s76, s76, s77\n"
    "s_lshl_b32 s77, s58, 8\n"
    "s_add_u32 s76, s76, s77\n"
    "v_lshlrev_b32 v197, 4, v46\n"
    "v_lshl_add_u32 v211, v47, 10, v197\n"
    "v_add_u32 v211, s76, v211\n"
    "s_lshr_b32 s76, s57, 2\n"
    "s_lshl_b32 s76, s76, 10\n"
    "s_lshl_b32 s77, s79, 8\n"
    "s_add_u32 s76, s76, s77\n"
    "s_mul_i32 s77, s58, 48\n"
    "v_add_u32 v198, s77, v231\n"
    "v_lshrrev_b32 v197, 5, v198\n"
    "v_lshl_add_u32 v212, v197, 12, s76\n"
    "v_bfe_u32 v197, v198, 2, 1\n"
    "v_lshl_add_u32 v212, v197, 11, v212\n"
    "v_bfe_u32 v197, v198, 3, 2\n"
    "v_lshl_add_u32 v212, v197, 6, v212\n"
    "v_cmp_gt_u32 vcc, 48, v231\n"
    "v_and_b32 v197, 3, v198\n"
    "v_lshl_add_u32 v212, v197, 4, v212\n"
    "v_cndmask_b32 v211, v196, v211, vcc\n"
    "v_cndmask_b32 v212, v196, v212, vcc\n"
    "v_mov_b32 v222, 0\n"
    "v_mov_b32 v223, 0\n"
    "v_mov_b32 v224, 0\n"
    "v_mov_b32 v225, 0\n"
    "v_mov_b32 v160, 0\n"
    "v_mov_b32 v161, 0\n"
    "v_mov_b32 v162, 0\n"
    "v_mov_b32 v163, 0\n"
    "v_mov_b32 v164, 0\n"
    "v_mov_b32 v165, 0\n"
    "v_mov_b32 v172, 0\n"
    "v_mov_b32 v173, 0\n"
    "v_mov_b32 v174, 0\n"
    "v_mov_b32 v175, 0\n"
    "v_lshlrev_b32 v41, 3, v41\n"
    "v_add_u32 v41, 0x24400, v41\n"
    "s_mul_i32 s76, s56, 0xc0000\n"
    "s_add_u32 s77, s76, 0x40000\n"
    "s_add_u32 s36, s22, s77\n"
    "s_addc_u32 s37, s23, 0\n"
    "s_mov_b32 s38, 0xc0000\n"
    "s_mov_b32 s39, 0x20000\n"
    "s_cmp_eq_u32 s87, 0\n"
    "s_cselect_b32 s62, 0x40000000, 0\n"
    "v_lshlrev_b32 v200, 4, v231\n"
    "v_add_u32 v201, 0xc000, v200\n"
    "v_mul_u32_u24 v198, 0x180, v46\n"
    "v_add_u32 v198, s71, v198\n"
    "v_lshrrev_b32 v197, 1, v46\n"
    "v_xor_b32 v197, v197, v47\n"
    "v_lshl_add_u32 v158, v197, 4, v198\n"
    "v_xor_b32 v159, 64, v158\n"
    "v_and_b32 v197, 3, v231\n"
    "v_cmp_eq_u32 vcc, 0, v197\n"
    "v_mov_b32 v198, 0x3f803f80\n"
    "s_nop 0\n"
    "v_cndmask_b32 v192, 0, v198, vcc\n"
    "v_cndmask_b32 v193, 0, v198, vcc\n"
    "v_cndmask_b32 v194, 0, v198, vcc\n"
    "v_cndmask_b32 v195, 0, v198, vcc\n"
    "s_and_b32 s77, s57, 1\n"
    "s_mul_i32 s77, s77, 0xc0\n"
    "s_sub_u32 s77, s77, 0x400\n"
    "v_mul_u32_u24 v197, 0x1556, v145\n"
    "v_lshrrev_b32 v197, 16, v197\n"
    "v_mul_u32_u24 v196, 12, v197\n"
    "v_sub_u32 v196, v145, v196\n"
    "v_mul_u32_u24 v249, 0x180, v197\n"
    "v_lshl_add_u32 v249, v196, 4, v249\n"
    "v_add_u32 v249, s77, v249\n"
    "s_and_b32 s77, s57, 1\n"
    "s_mul_i32 s77, s77, 0xc0\n"
    "s_sub_u32 s77, s77, 0x800\n"
    "v_mul_u32_u24 v197, 0x1556, v146\n"
    "v_lshrrev_b32 v197, 16, v197\n"
    "v_mul_u32_u24 v196, 12, v197\n"
    "v_sub_u32 v196, v146, v196\n"
    "v_mul_u32_u24 v250, 0x180, v197\n"
    "v_lshl_add_u32 v250, v196, 4, v250\n"
    "v_add_u32 v250, s77, v250\n"
    "s_waitcnt vmcnt(9)\n"
    "v_lshlrev_b32 v44, 16, v44\n"
    "s_waitcnt vmcnt(8)\n"
    "v_lshlrev_b32 v45, 16, v45\n"
    "v_mul_f32 v197, v44, v45\n"
    "s_nop 1\n"
    "v_min_f32_dpp v197, v197, v197 row_ror:8 row_mask:0xf bank_mask:0xf\n"
    "s_mul_i32 s76, s56, 0xc0000\n"
    "s_add_u32 s77, s76, 0x1840000\n"
    "v_min_f32_dpp v197, v197, v197 row_ror:4 row_mask:0xf bank_mask:0xf\n"
    "s_add_u32 s40, s22, s77\n"
    "s_addc_u32 s41, s23, 0\n"
    "v_min_f32_dpp v197, v197, v197 row_ror:2 row_mask:0xf bank_mask:0xf\n"
    "s_mov_b32 s42, 0xc0000\n"
    "s_mov_b32 s43, 0x20000\n"
    "v_min_f32_dpp v197, v197, v197 row_ror:1 row_mask:0xf bank_mask:0xf\n"
    "v_mov_b32 v198, v197\n"
    "s_nop 1\n"
    "v_permlane32_swap_b32 v197, v198\n"
    "v_min_f32 v197, v197, v198\n"
    "v_mov_b32 v198, v197\n"
    "s_nop 1\n"
    "v_permlane16_swap_b32 v197, v198\n"
    "v_min_f32 v197, v197, v198\n"
    "v_rcp_f32 v198, v197\n"
    "v_subrev_u32 v199, 0x37800000, v197\n"
    "v_mul_f32 v197, 0x3fcccccd, v197\n"
    "v_cmp_gt_u32 vcc, 0x10000000, v199\n"
    "v_mul_f32 v198, 0x3f3425ed, v198\n"
    "v_mov_b32 v199, 0x3f800000\n"
    "v_cndmask_b32 v197, 0, v197, vcc\n"
    "v_cndmask_b32 v198, v199, v198, vcc\n"
    "v_readfirstlane_b32 s70, v197\n"
    "v_mul_f32 v197, v197, v198\n"
    "v_readfirstlane_b32 s68, v198\n"
    "v_readfirstlane_b32 s69, v197\n"
    "s_waitcnt vmcnt(7)\n"
    "v_lshlrev_b32 v42, 16, v42\n"
    "s_waitcnt vmcnt(6)\n"
    "v_lshlrev_b32 v43, 16, v43\n"
    "v_mul_f32 v44, s68, v43\n"
    "v_fma_f32 v45, -v42, v44, s69\n"
    "ds_write_b64 v41, v[44:45]\n"
    "s_add_u32 s78, s88, 0\n"
    "s_add_u32 m0, s71, 0x800\n"
    "s_nop 0\n"
    "buffer_load_dwordx4 v154, s[28:31], s78 offen nt lds\n"
    "s_add_u32 s78, s88, 0\n"
    "s_add_u32 m0, s71, 0xc00\n"
    "s_nop 0\n"
    "buffer_load_dwordx4 v155, s[28:31], s78 offen nt lds\n"
    "s_waitcnt lgkmcnt(0)\n"
    "s_barrier\n"
    "ds_read_u16_d16_hi v172, v217 offset:6144\n"
    "ds_read_u16_d16_hi v173, v217\n"
    "ds_read_u16_d16_hi v174, v217 offset:6336\n"
    "ds_read_u16_d16_hi v175, v217 offset:192\n"
    "ds_read_b64 v[228:229], v210\n"
    "ds_read2st64_b32 v[240:241], v207 offset0:48 offset1:51\n"
    "ds_read2st64_b32 v[242:243], v207 offset0:54 offset1:57\n"
    "ds_read2st64_b32 v[244:245], v207 offset0:60 offset1:63\n"
    "ds_read2st64_b32 v[246:247], v207 offset0:66 offset1:69\n"
    "ds_read_u16_d16_hi v223, v209 offset:6144\n"
    "ds_read_u16_d16_hi v222, v209\n"
    "ds_read_b128 v[232:235], v205\n"
    "ds_read_b128 v[236:239], v206\n"
    "ds_read_u16_d16_hi v161, v209 offset:6336\n"
    "ds_read_u16_d16_hi v160, v209 offset:192\n"
    "s_waitcnt lgkmcnt(11)\n"
    "ds_read_b128 v[64:67], v205 offset:12288\n"
    "ds_read_b128 v[68:71], v206 offset:12288\n"
    "v_mul_f32 v197, v172, v173\n"
    "v_mul_f32 v198, v174, v175\n"
    "v_min_f32 v197, v197, v198\n"
    "v_max_f32 v197, 0, v197\n"
    "v_mul_f32 v217, 0x3e3fe8d2, v197\n"
    "s_waitcnt lgkmcnt(7)\n"
    "v_mul_f32 v226, s75, v223\n"
    "s_waitcnt lgkmcnt(6)\n"
    "v_fma_f32 v227, -v222, v226, v217\n"
    "s_waitcnt lgkmcnt(5)\n"
    "v_cvt_f32_i32 v232, v232\n"
    "v_cvt_f32_i32 v233, v233\n"
    "v_cvt_f32_i32 v234, v234\n"
    "v_cvt_f32_i32 v235, v235\n"
    "s_waitcnt lgkmcnt(4)\n"
    "v_cvt_f32_i32 v236, v236\n"
    "v_cvt_f32_i32 v237, v237\n"
    "v_cvt_f32_i32 v238, v238\n"
    "v_cvt_f32_i32 v239, v239\n"
    "v_mul_f32_dpp v232, v226, v232 row_newbcast:0 row_mask:0xf bank_mask:0xf\n"
    "v_mul_f32_dpp v233, v226, v233 row_newbcast:1 row_mask:0xf bank_mask:0xf\n"
    "v_mul_f32_dpp v234, v226, v234 row_newbcast:2 row_mask:0xf bank_mask:0xf\n"
    "v_mul_f32_dpp v235, v226, v235 row_newbcast:3 row_mask:0xf bank_mask:0xf\n"
    "v_mul_f32_dpp v236, v226, v236 row_newbcast:4 row_mask:0xf bank_mask:0xf\n"
    "v_mul_f32_dpp v237, v226, v237 row_newbcast:5 row_mask:0xf bank_mask:0xf\n"
    "v_mul_f32_dpp v238, v226, v238 row_newbcast:6 row_mask:0xf bank_mask:0xf\n"
    "v_mul_f32_dpp v239, v226, v239 row_newbcast:7 row_mask:0xf bank_mask:0xf\n"
    "v_add_f32_dpp v232, v227, v232 row_newbcast:0 row_mask:0xf bank_mask:0xf\n"
    "v_add_f32_dpp v233, v227, v233 row_newbcast:1 row_mask:0xf bank_mask:0xf\n"
    "v_add_f32_dpp v234, v227, v234 row_newbcast:2 row_mask:0xf bank_mask:0xf\n"
    "v_add_f32_dpp v235, v227, v235 row_newbcast:3 row_mask:0xf bank_mask:0xf\n"
    "v_add_f32_dpp v236, v227, v236 row_newbcast:4 row_mask:0xf bank_mask:0xf\n"
    "v_add_f32_dpp v237, v227, v237 row_newbcast:5 row_mask:0xf bank_mask:0xf\n"
    "v_add_f32_dpp v238, v227, v238 row_newbcast:6 row_mask:0xf bank_mask:0xf\n"
    "v_add_f32_dpp v239, v227, v239 row_newbcast:7 row_mask:0xf bank_mask:0xf\n"
    "v_cvt_pk_bf16_f32 v232, v232, v233\n"
    "v_cvt_pk_bf16_f32 v233, v234, v235\n"
    "v_cvt_pk_bf16_f32 v234, v236, v237\n"
    "v_cvt_pk_bf16_f32 v235, v238, v239\n"
    "buffer_store_dwordx4 v[232:235], v211, s[36:39], s62 offen sc1\n"
    "v_cvt_f32_i32 v240, v240\n"
    "v_cvt_f32_i32 v241, v241\n"
    "v_cvt_f32_i32 v242, v242\n"
    "v_cvt_f32_i32 v243, v243\n"
    "v_cvt_f32_i32 v244, v244\n"
    "v_cvt_f32_i32 v245, v245\n"
    "v_cvt_f32_i32 v246, v246\n"
    "v_cvt_f32_i32 v247, v247\n"
    "v_mul_f32_dpp v240, v228, v240 row_newbcast:0 row_mask:0xf bank_mask:0xf\n"
    "v_mul_f32_dpp v241, v228, v241 row_newbcast:1 row_mask:0xf bank_mask:0xf\n"
    "v_mul_f32_dpp v242, v228, v242 row_newbcast:2 row_mask:0xf bank_mask:0xf\n"
    "v_mul_f32_dpp v243, v228, v243 row_newbcast:3 row_mask:0xf bank_mask:0xf\n"
    "v_mul_f32_dpp v244, v228, v244 row_newbcast:4 row_mask:0xf bank_mask:0xf\n"
    "v_mul_f32_dpp v245, v228, v245 row_newbcast:5 row_mask:0xf bank_mask:0xf\n"
    "v_mul_f32_dpp v246, v228, v246 row_newbcast:6 row_mask:0xf bank_mask:0xf\n"
    "v_mul_f32_dpp v247, v228, v247 row_newbcast:7 row_mask:0xf bank_mask:0xf\n"
    "v_add_f32_dpp v240, v229, v240 row_newbcast:0 row_mask:0xf bank_mask:0xf\n"
    "v_add_f32_dpp v241, v229, v241 row_newbcast:1 row_mask:0xf bank_mask:0xf\n"
    "v_add_f32_dpp v242, v229, v242 row_newbcast:2 row_mask:0xf bank_mask:0xf\n"
    "v_add_f32_dpp v243, v229, v243 row_newbcast:3 row_mask:0xf bank_mask:0xf\n"
    "v_add_f32_dpp v244, v229, v244 row_newbcast:4 row_mask:0xf bank_mask:0xf\n"
    "v_add_f32_dpp v245, v229, v245 row_newbcast:5 row_mask:0xf bank_mask:0xf\n"
    "v_add_f32_dpp v246, v229, v246 row_newbcast:6 row_mask:0xf bank_mask:0xf\n"
    "v_add_f32_dpp v247, v229, v247 row_newbcast:7 row_mask:0xf bank_mask:0xf\n"
    "v_cvt_pk_bf16_f32 v240, v240, v241\n"
    "v_cvt_pk_bf16_f32 v241, v242, v243\n"
    "v_cvt_pk_bf16_f32 v242, v244, v245\n"
    "v_cvt_pk_bf16_f32 v243, v246, v247\n"
    "s_mov_b32 s78, 0\n"
    "buffer_store_dwordx4 v[240:243], v212, s[40:43], s78 offen sc1\n"
    "s_waitcnt lgkmcnt(3)\n"
    "v_mul_f32 v166, s75, v161\n"
    "s_waitcnt lgkmcnt(2)\n"
    "v_fma_f32 v167, -v160, v166, v217\n"
    "s_waitcnt lgkmcnt(1)\n"
    "v_cvt_f32_i32 v64, v64\n"
    "v_cvt_f32_i32 v65, v65\n"
    "v_cvt_f32_i32 v66, v66\n"
    "v_cvt_f32_i32 v67, v67\n"
    "s_waitcnt lgkmcnt(0)\n"
    "v_cvt_f32_i32 v68, v68\n"
    "v_cvt_f32_i32 v69, v69\n"
    "v_cvt_f32_i32 v70, v70\n"
    "v_cvt_f32_i32 v71, v71\n"
    "v_mul_f32_dpp v64, v166, v64 row_newbcast:0 row_mask:0xf bank_mask:0xf\n"
    "v_mul_f32_dpp v65, v166, v65 row_newbcast:1 row_mask:0xf bank_mask:0xf\n"
    "v_mul_f32_dpp v66, v166, v66 row_newbcast:2 row_mask:0xf bank_mask:0xf\n"
    "v_mul_f32_dpp v67, v166, v67 row_newbcast:3 row_mask:0xf bank_mask:0xf\n"
    "v_mul_f32_dpp v68, v166, v68 row_newbcast:4 row_mask:0xf bank_mask:0xf\n"
    "v_mul_f32_dpp v69, v166, v69 row_newbcast:5 row_mask:0xf bank_mask:0xf\n"
    "v_mul_f32_dpp v70, v166, v70 row_newbcast:6 row_mask:0xf bank_mask:0xf\n"
    "v_mul_f32_dpp v71, v166, v71 row_newbcast:7 row_mask:0xf bank_mask:0xf\n"
    "v_add_f32_dpp v64, v167, v64 row_newbcast:0 row_mask:0xf bank_mask:0xf\n"
    "v_add_f32_dpp v65, v167, v65 row_newbcast:1 row_mask:0xf bank_mask:0xf\n"
    "v_add_f32_dpp v66, v167, v66 row_newbcast:2 row_mask:0xf bank_mask:0xf\n"
    "v_add_f32_dpp v67, v167, v67 row_newbcast:3 row_mask:0xf bank_mask:0xf\n"
    "v_add_f32_dpp v68, v167, v68 row_newbcast:4 row_mask:0xf bank_mask:0xf\n"
    "v_add_f32_dpp v69, v167, v69 row_newbcast:5 row_mask:0xf bank_mask:0xf\n"
    "v_add_f32_dpp v70, v167, v70 row_newbcast:6 row_mask:0xf bank_mask:0xf\n"
    "v_add_f32_dpp v71, v167, v71 row_newbcast:7 row_mask:0xf bank_mask:0xf\n"
    "v_cvt_pk_bf16_f32 v64, v64, v65\n"
    "v_cvt_pk_bf16_f32 v65, v66, v67\n"
    "v_cvt_pk_bf16_f32 v66, v68, v69\n"
    "v_cvt_pk_bf16_f32 v67, v70, v71\n"
    "s_add_u32 s78, s87, 0\n"
    "buffer_store_dwordx4 v[64:67], v211, s[36:39], s78 offen sc1\n"
    "s_waitcnt vmcnt(10)\n"
    "v_add_u32 v221, s74, v221\n"
    "s_mov_b32 s73, 0\n"
    "v_readfirstlane_b32 s67, v221\n"
    "s_mul_i32 s66, s74, 2\n"
    "s_waitcnt vmcnt(0)\n"
    "s_barrier\n"
    "v_mov_b32 v220, 2\n"
    "buffer_store_dwordx2 v[220:221], v214, s[44:47], 0 offen sc1\n"
    "s_add_u32 s78, s64, 0x18000\n"
    "s_add_u32 m0, s72, 0x1b000\n"
    "s_nop 0\n"
    "buffer_load_dwordx4 v202, s[32:35], s78 offen nt lds\n"
    "buffer_load_dwordx4 v203, s[32:35], s78 offen offset:1024 nt lds\n"
    "buffer_load_dwordx4 v204, s[32:35], s78 offen offset:2048 nt lds\n"
    "s_add_u32 s78, s88, 0\n"
    "s_add_u32 m0, s71, 0x1000\n"
    "s_nop 0\n"
    "buffer_load_dwordx4 v156, s[28:31], s78 offen nt lds\n"
    "s_add_u32 s78, s88, 0\n"
    "s_add_u32 m0, s71, 0x1400\n"
    "s_nop 0\n"
    "buffer_load_dwordx4 v157, s[28:31], s78 offen nt lds\n"
    "s_add_u32 s78, s88, 0x1800\n"
    "s_add_u32 m0, s71, 0x6000\n"
    "s_nop 0\n"
    "buffer_load_dwordx4 v152, s[28:31], s78 offen nt lds\n"
    "s_add_u32 s78, s88, 0x1800\n"
    "s_add_u32 m0, s71, 0x6400\n"
    "s_nop 0\n"
    "buffer_load_dwordx4 v153, s[28:31], s78 offen nt lds\n"
    "s_barrier\n"
    "ds_read_u16_d16_hi v163, v209 offset:6528\n"
    "ds_read_u16_d16_hi v162, v209 offset:384\n"
    "ds_read_b128 v[72:75], v205 offset:49152\n"
    "ds_read_b128 v[76:79], v206 offset:49152\n"
    "ds_read_b64 v[170:171], v210 offset:64\n"
    "ds_read2st64_b32 v[80:81], v207 offset0:192 offset1:195\n"
    "ds_read2st64_b32 v[82:83], v207 offset0:198 offset1:201\n"
    "ds_read2st64_b32 v[84:85], v207 offset0:204 offset1:207\n"
    "ds_read2st64_b32 v[86:87], v207 offset0:210 offset1:213\n"
    "s_waitcnt lgkmcnt(8)\n"
    "v_mul_f32 v168, s75, v163\n"
    "s_waitcnt lgkmcnt(7)\n"
    "v_fma_f32 v169, -v162, v168, v217\n"
    "s_waitcnt lgkmcnt(6)\n"
    "v_cvt_f32_i32 v72, v72\n"
    "v_cvt_f32_i32 v73, v73\n"
    "v_cvt_f32_i32 v74, v74\n"
    "v_cvt_f32_i32 v75, v75\n"
    "s_waitcnt lgkmcnt(5)\n"
    "v_cvt_f32_i32 v76, v76\n"
    "v_cvt_f32_i32 v77, v77\n"
    "v_cvt_f32_i32 v78, v78\n"
    "v_cvt_f32_i32 v79, v79\n"
    "v_mul_f32_dpp v72, v168, v72 row_newbcast:0 row_mask:0xf bank_mask:0xf\n"
    "v_mul_f32_dpp v73, v168, v73 row_newbcast:1 row_mask:0xf bank_mask:0xf\n"
    "v_mul_f32_dpp v74, v168, v74 row_newbcast:2 row_mask:0xf bank_mask:0xf\n"
    "v_mul_f32_dpp v75, v168, v75 row_newbcast:3 row_mask:0xf bank_mask:0xf\n"
    "v_mul_f32_dpp v76, v168, v76 row_newbcast:4 row_mask:0xf bank_mask:0xf\n"
    "v_mul_f32_dpp v77, v168, v77 row_newbcast:5 row_mask:0xf bank_mask:0xf\n"
    "v_mul_f32_dpp v78, v168, v78 row_newbcast:6 row_mask:0xf bank_mask:0xf\n"
    "v_mul_f32_dpp v79, v168, v79 row_newbcast:7 row_mask:0xf bank_mask:0xf\n"
    "v_add_f32_dpp v72, v169, v72 row_newbcast:0 row_mask:0xf bank_mask:0xf\n"
    "v_add_f32_dpp v73, v169, v73 row_newbcast:1 row_mask:0xf bank_mask:0xf\n"
    "v_add_f32_dpp v74, v169, v74 row_newbcast:2 row_mask:0xf bank_mask:0xf\n"
    "v_add_f32_dpp v75, v169, v75 row_newbcast:3 row_mask:0xf bank_mask:0xf\n"
    "v_add_f32_dpp v76, v169, v76 row_newbcast:4 row_mask:0xf bank_mask:0xf\n"
    "v_add_f32_dpp v77, v169, v77 row_newbcast:5 row_mask:0xf bank_mask:0xf\n"
    "v_add_f32_dpp v78, v169, v78 row_newbcast:6 row_mask:0xf bank_mask:0xf\n"
    "v_add_f32_dpp v79, v169, v79 row_newbcast:7 row_mask:0xf bank_mask:0xf\n"
    "v_cvt_pk_bf16_f32 v72, v72, v73\n"
    "v_cvt_pk_bf16_f32 v73, v74, v75\n"
    "v_cvt_pk_bf16_f32 v74, v76, v77\n"
    "v_cvt_pk_bf16_f32 v75, v78, v79\n"
    "s_add_u32 s78, s87, 0x6000\n"
    "buffer_store_dwordx4 v[72:75], v211, s[36:39], s78 offen sc1\n"
    "s_waitcnt lgkmcnt(3)\n"
    "v_cvt_f32_i32 v80, v80\n"
    "v_cvt_f32_i32 v81, v81\n"
    "s_waitcnt lgkmcnt(2)\n"
    "v_cvt_f32_i32 v82, v82\n"
    "v_cvt_f32_i32 v83, v83\n"
    "s_waitcnt lgkmcnt(1)\n"
    "v_cvt_f32_i32 v84, v84\n"
    "v_cvt_f32_i32 v85, v85\n"
    "s_waitcnt lgkmcnt(0)\n"
    "v_cvt_f32_i32 v86, v86\n"
    "v_cvt_f32_i32 v87, v87\n"
    "v_mul_f32_dpp v80, v170, v80 row_newbcast:0 row_mask:0xf bank_mask:0xf\n"
    "v_mul_f32_dpp v81, v170, v81 row_newbcast:1 row_mask:0xf bank_mask:0xf\n"
    "v_mul_f32_dpp v82, v170, v82 row_newbcast:2 row_mask:0xf bank_mask:0xf\n"
    "v_mul_f32_dpp v83, v170, v83 row_newbcast:3 row_mask:0xf bank_mask:0xf\n"
    "v_mul_f32_dpp v84, v170, v84 row_newbcast:4 row_mask:0xf bank_mask:0xf\n"
    "v_mul_f32_dpp v85, v170, v85 row_newbcast:5 row_mask:0xf bank_mask:0xf\n"
    "v_mul_f32_dpp v86, v170, v86 row_newbcast:6 row_mask:0xf bank_mask:0xf\n"
    "v_mul_f32_dpp v87, v170, v87 row_newbcast:7 row_mask:0xf bank_mask:0xf\n"
    "v_add_f32_dpp v80, v171, v80 row_newbcast:0 row_mask:0xf bank_mask:0xf\n"
    "v_add_f32_dpp v81, v171, v81 row_newbcast:1 row_mask:0xf bank_mask:0xf\n"
    "v_add_f32_dpp v82, v171, v82 row_newbcast:2 row_mask:0xf bank_mask:0xf\n"
    "v_add_f32_dpp v83, v171, v83 row_newbcast:3 row_mask:0xf bank_mask:0xf\n"
    "v_add_f32_dpp v84, v171, v84 row_newbcast:4 row_mask:0xf bank_mask:0xf\n"
    "v_add_f32_dpp v85, v171, v85 row_newbcast:5 row_mask:0xf bank_mask:0xf\n"
    "v_add_f32_dpp v86, v171, v86 row_newbcast:6 row_mask:0xf bank_mask:0xf\n"
    "v_add_f32_dpp v87, v171, v87 row_newbcast:7 row_mask:0xf bank_mask:0xf\n"
    "v_cvt_pk_bf16_f32 v80, v80, v81\n"
    "v_cvt_pk_bf16_f32 v81, v82, v83\n"
    "v_cvt_pk_bf16_f32 v82, v84, v85\n"
    "v_cvt_pk_bf16_f32 v83, v86, v87\n"
    "s_mov_b32 s78, 0x6000\n"
    "buffer_store_dwordx4 v[80:83], v212, s[40:43], s78 offen sc1\n"
    "buffer_load_dwordx2 v[218:219], v213, s[44:47], 0 offen sc1\n"
    "s_add_u32 s78, s88, 0x1800\n"
    "s_add_u32 m0, s71, 0x6800\n"
    "s_nop 0\n"
    "buffer_load_dwordx4 v154, s[28:31], s78 offen nt lds\n"
    "s_add_u32 s78, s88, 0x1800\n"
    "s_add_u32 m0, s71, 0x6c00\n"
    "s_nop 0\n"
    "buffer_load_dwordx4 v155, s[28:31], s78 offen nt lds\n"
    "s_add_u32 s78, s88, 0x1800\n"
    "s_add_u32 m0, s71, 0x7000\n"
    "s_nop 0\n"
    "buffer_load_dwordx4 v156, s[28:31], s78 offen nt lds\n"
    "s_add_u32 s78, s88, 0x1800\n"
    "s_add_u32 m0, s71, 0x7400\n"
    "s_nop 0\n"
    "buffer_load_dwordx4 v157, s[28:31], s78 offen nt lds\n"
    "s_waitcnt vmcnt(9)\n"
    "ds_read_b128 a[192:195], v158\n"
    "ds_read_b128 a[196:199], v159\n"
    "ds_read_b128 a[200:203], v158 offset:128\n"
    "ds_read_b128 a[204:207], v159 offset:128\n"
    "ds_read_b128 a[208:211], v158 offset:256\n"
    "ds_read_b128 a[212:215], v159 offset:256\n"
    "s_waitcnt vmcnt(4)\n"
    "v_cmp_ne_u32 s[80:81], s67, v219\n"
    "v_cmp_gt_u32 vcc, s66, v218\n"
    "s_or_b64 vcc, vcc, s[80:81]\n"
    "s_cbranch_vccnz LSPRE0\n"
    "LSPB0:\n"
    "s_waitcnt lgkmcnt(0)\n"
    "s_barrier\n"
    "v_mov_b32 v220, 3\n"
    "buffer_store_dwordx2 v[220:221], v214, s[44:47], 0 offen sc1\n"
    "s_add_u32 m0, s72, 0x1e000\n"
    "s_nop 0\n"
    "buffer_load_dwordx4 v200, s[36:39], s72 offen sc1 lds\n"
    "s_add_u32 m0, s72, 0x1e000\n"
    "s_nop 0\n"
    "buffer_load_dwordx4 v200, s[36:39], s72 offen offset:1024 sc1 lds\n"
    "s_add_u32 m0, s72, 0x1e000\n"
    "s_nop 0\n"
    "buffer_load_dwordx4 v200, s[36:39], s72 offen offset:2048 sc1 lds\n"
    "s_add_u32 s65, s71, 0x3000\n"
    "s_add_u32 m0, s71, 0\n"
    "s_nop 0\n"
    "buffer_load_dwordx4 v200, s[36:39], s65 offen sc1 lds\n"
    "s_add_u32 s65, s71, 0x3400\n"
    "s_add_u32 m0, s71, 0x400\n"
    "s_nop 0\n"
    "buffer_load_dwordx4 v200, s[36:39], s65 offen sc1 lds\n"
    "s_add_u32 s65, s71, 0x3800\n"
    "s_add_u32 m0, s71, 0x800\n"
    "s_nop 0\n"
    "buffer_load_dwordx4 v200, s[36:39], s65 offen sc1 lds\n"
    "s_add_u32 s65, s71, 0x3c00\n"
    "s_add_u32 m0, s71, 0xc00\n"
    "s_nop 0\n"
    "buffer_load_dwordx4 v200, s[36:39], s65 offen sc1 lds\n"
    "s_add_u32 s65, s71, 0x4000\n"
    "s_add_u32 m0, s71, 0x1000\n"
    "s_nop 0\n"
    "buffer_load_dwordx4 v200, s[36:39], s65 offen sc1 lds\n"
    "s_add_u32 s65, s71, 0x4400\n"
    "s_add_u32 m0, s71, 0x1400\n"
    "s_nop 0\n"
    "buffer_load_dwordx4 v200, s[36:39], s65 offen sc1 lds\n"
    "s_add_u32 s65, s71, 0\n"
    "s_add_u32 m0, s71, 0x12000\n"
    "s_nop 0\n"
    "buffer_load_dwordx4 v200, s[40:43], s65 offen sc1 lds\n"
    "s_add_u32 s65, s71, 0x400\n"
    "s_add_u32 m0, s71, 0x12400\n"
    "s_nop 0\n"
    "buffer_load_dwordx4 v200, s[40:43], s65 offen sc1 lds\n"
    "s_add_u32 s65, s71, 0x800\n"
    "s_add_u32 m0, s71, 0x12800\n"
    "s_nop 0\n"
    "buffer_load_dwordx4 v200, s[40:43], s65 offen sc1 lds\n"
    "s_add_u32 s65, s71, 0xc00\n"
    "s_add_u32 m0, s71, 0x12c00\n"
    "s_nop 0\n"
    "buffer_load_dwordx4 v200, s[40:43], s65 offen sc1 lds\n"
    "s_add_u32 s65, s71, 0x1000\n"
    "s_add_u32 m0, s71, 0x13000\n"
    "s_nop 0\n"
    "buffer_load_dwordx4 v200, s[40:43], s65 offen sc1 lds\n"
    "s_add_u32 s65, s71, 0x1400\n"
    "s_add_u32 m0, s71, 0x13400\n"
    "s_nop 0\n"
    "buffer_load_dwordx4 v200, s[40:43], s65 offen sc1 lds\n"
    "s_add_u32 s78, s64, 0x24000\n"
    "s_add_u32 m0, s72, 0x18000\n"
    "s_nop 0\n"
    "buffer_load_dwordx4 v202, s[32:35], s78 offen nt lds\n"
    "buffer_load_dwordx4 v203, s[32:35], s78 offen offset:1024 nt lds\n"
    "buffer_load_dwordx4 v204, s[32:35], s78 offen offset:2048 nt lds\n"
    "s_add_u32 s78, s88, 0x3000\n"
    "s_add_u32 m0, s71, 0xc000\n"
    "s_nop 0\n"
    "buffer_load_dwordx4 v152, s[28:31], s78 offen nt lds\n"
    "s_mul_i32 s66, s74, 3\n"
    "s_waitcnt vmcnt(20)\n"
    "ds_read_b128 a[216:219], v158 offset:24576\n"
    "ds_read_b128 a[220:223], v159 offset:24576\n"
    "ds_read_b128 a[224:227], v158 offset:24704\n"
    "ds_read_b128 a[228:231], v159 offset:24704\n"
    "ds_read_b128 a[232:235], v158 offset:24832\n"
    "ds_read_b128 a[236:239], v159 offset:24832\n"
    "s_add_u32 m0, s72, 0x21000\n"
    "s_nop 0\n"
    "buffer_load_dwordx4 v249, s[48:51], 0 offen offset:1024 lds\n"
    "buffer_load_dwordx2 v[218:219], v213, s[44:47], 0 offen sc1\n"
    "v_add_u32 v255, 0x1e000, v200\n"
    "s_waitcnt vmcnt(12) lgkmcnt(0)\n"
    "s_add_u32 s60, s71, 0x9000\n"
    "s_add_u32 s61, s71, 0x6000\n"
    "s_add_u32 s62, s87, 0x12000\n"
    "s_mov_b32 s63, 0x12000\n"
    "s_mov_b32 s59, 0\n"
    "s_mul_i32 s66, s74, 3\n"
    "s_mul_i32 s90, s58, 0x1c00\n"
    "s_sub_u32 s90, s90, 0x1000\n"
    "s_sub_u32 s92, s90, s71\n"
    "s_barrier\n"
    "ds_read_b128 v[160:163], v255\n"
    "ds_read_b128 v[164:167], v255 offset:1024\n"
    "ds_read_b128 v[168:171], v255 offset:2048\n"
    "ds_read_b128 v[172:175], v255 offset:3072\n"
    "ds_read_b128 v[176:179], v255 offset:4096\n"
    "ds_read_b128 v[180:183], v255 offset:5120\n"
    "s_add_u32 s76, s87, 0xc000\n"
    "s_mov_b32 s77, 0xc000\n"
    "s_waitcnt lgkmcnt(4)\n"
    "v_mfma_f32_16x16x32_bf16 v[40:43], v[160:163], a[192:195], 0\n"
    "ds_read_b128 v[184:187], v255 offset:6144\n"
    "ds_read_b64 v[228:229], v210 offset:128\n"
    "v_mfma_f32_16x16x32_bf16 v[40:43], v[164:167], a[196:199], v[40:43]\n"
    "s_add_u32 s78, s88, 0x3000\n"
    "ds_read2st64_b32 v[240:241], v207 offset0:144 offset1:147\n"
    "ds_read2st64_b32 v[242:243], v207 offset0:150 offset1:153\n"
    "v_mfma_f32_16x16x32_bf16 v[44:47], v[160:163], a[216:219], 0\n"
    "s_add_u32 m0, s71, 0xc400\n"
    "ds_read_b128 v[188:191], v255 offset:7168\n"
    "ds_read2st64_b32 v[244:245], v207 offset0:156 offset1:159\n"
    "v_mfma_f32_16x16x32_bf16 v[44:47], v[164:167], a[220:223], v[44:47]\n"
    "s_waitcnt lgkmcnt(8)\n"
    "buffer_load_dwordx4 v153, s[28:31], s78 offen nt lds\n"
    "v_mfma_f32_16x16x32_bf16 v[40:43], v[168:171], a[200:203], v[40:43]\n"
    "ds_read_b128 v[160:163], v255 offset:8192\n"
    "ds_read2st64_b32 v[246:247], v207 offset0:162 offset1:165\n"
    "v_mfma_f32_16x16x32_bf16 v[40:43], v[172:175], a[204:207], v[40:43]\n"
    "ds_read_u16_d16_hi v223, v209 offset:6720\n"
    "ds_read_u16_d16_hi v222, v209 offset:576\n"
    "v_mfma_f32_16x16x32_bf16 v[44:47], v[168:171], a[224:227], v[44:47]\n"
    "ds_read_b128 v[164:167], v255 offset:9216\n"
    "ds_read_b128 v[232:235], v205 offset:36864\n"
    "v_mfma_f32_16x16x32_bf16 v[44:47], v[172:175], a[228:231], v[44:47]\n"
    "s_waitcnt lgkmcnt(12)\n"
    "ds_read_b128 v[236:239], v206 offset:36864\n"
    "v_mfma_f32_16x16x32_bf16 v[40:43], v[176:179], a[208:211], v[40:43]\n"
    "ds_read_b128 v[168:171], v255 offset:10240\n"
    "v_mfma_f32_16x16x32_bf16 v[40:43], v[180:183], a[212:215], v[40:43]\n"
    "v_mfma_f32_16x16x32_bf16 v[44:47], v[176:179], a[232:235], v[44:47]\n"
    "ds_read_b128 v[172:175], v255 offset:11264\n"
    "v_mfma_f32_16x16x32_bf16 v[44:47], v[180:183], a[236:239], v[44:47]\n"
    "s_waitcnt lgkmcnt(10)\n"
    "s_add_u32 s78, s88, 0x3000\n"
    "v_cvt_f32_i32 v240, v240\n"
    "v_mfma_f32_16x16x32_bf16 v[56:59], v[184:187], a[192:195], 0\n"
    "s_add_u32 m0, s71, 0xc800\n"
    "ds_read_b128 v[176:179], v200\n"
    "v_cvt_f32_i32 v241, v241\n"
    "v_mfma_f32_16x16x32_bf16 v[56:59], v[188:191], a[196:199], v[56:59]\n"
    "buffer_load_dwordx4 v154, s[28:31], s78 offen nt lds\n"
    "v_mfma_f32_16x16x32_bf16 v[60:63], v[184:187], a[216:219], 0\n"
    "ds_read_b128 v[180:183], v200 offset:1024\n"
    "v_cvt_f32_i32 v242, v242\n"
    "v_mfma_f32_16x16x32_bf16 v[60:63], v[188:191], a[220:223], v[60:63]\n"
    "s_waitcnt lgkmcnt(6)\n"
    "v_cvt_f32_i32 v243, v243\n"
    "v_cvt_f32_i32 v244, v244\n"
    "v_mfma_f32_16x16x32_bf16 v[56:59], v[160:163], a[200:203], v[56:59]\n"
    "ds_read_b128 v[184:187], v200 offset:2048\n"
    "v_cvt_f32_i32 v245, v245\n"
    "v_mfma_f32_16x16x32_bf16 v[56:59], v[164:167], a[204:207], v[56:59]\n"
    "v_cvt_f32_i32 v246, v246\n"
    "v_mul_f32 v226, s75, v223\n"
    "v_mfma_f32_16x16x32_bf16 v[60:63], v[160:163], a[224:227], v[60:63]\n"
    "ds_read_b128 v[188:191], v200 offset:3072\n"
    "v_cvt_f32_i32 v247, v247\n"
    "v_mfma_f32_16x16x32_bf16 v[60:63], v[164:167], a[228:231], v[60:63]\n"
    "s_waitcnt lgkmcnt(4)\n"
    "v_fma_f32 v227, -v222, v226, v217\n"
    "v_mul_f32_dpp v240, v228, v240 row_newbcast:0 row_mask:0xf bank_mask:0xf\n"
    "v_mfma_f32_16x16x32_bf16 v[56:59], v[168:171], a[208:211], v[56:59]\n"
    "ds_read_b128 v[160:163], v200 offset:4096\n"
    "v_cvt_f32_i32 v232, v232\n"
    "v_mfma_f32_16x16x32_bf16 v[56:59], v[172:175], a[212:215], v[56:59]\n"
    "s_add_u32 s78, s88, 0x3000\n"
    "v_mul_f32_dpp v241, v228, v241 row_newbcast:1 row_mask:0xf bank_mask:0xf\n"
    "v_cvt_f32_i32 v233, v233\n"
    "v_mfma_f32_16x16x32_bf16 v[60:63], v[168:171], a[232:235], v[60:63]\n"
    "s_add_u32 m0, s71, 0xcc00\n"
    "ds_read_b128 v[164:167], v200 offset:5120\n"
    "v_mul_f32_dpp v242, v228, v242 row_newbcast:2 row_mask:0xf bank_mask:0xf\n"
    "v_mfma_f32_16x16x32_bf16 v[60:63], v[172:175], a[236:239], v[60:63]\n"
    "s_waitcnt lgkmcnt(4)\n"
    "buffer_load_dwordx4 v155, s[28:31], s78 offen nt lds\n"
    "v_mfma_f32_16x16x32_bf16 v[64:67], v[176:179], a[192:195], 0\n"
    "ds_read_b128 v[168:171], v200 offset:6144\n"
    "v_cvt_f32_i32 v234, v234\n"
    "v_mul_f32_dpp v243, v228, v243 row_newbcast:3 row_mask:0xf bank_mask:0xf\n"
    "v_cvt_f32_i32 v235, v235\n"
    "v_mfma_f32_16x16x32_bf16 v[64:67], v[180:183], a[196:199], v[64:67]\n"
    "v_mul_f32_dpp v244, v228, v244 row_newbcast:4 row_mask:0xf bank_mask:0xf\n"
    "v_cvt_f32_i32 v236, v236\n"
    "v_mul_f32_dpp v245, v228, v245 row_newbcast:5 row_mask:0xf bank_mask:0xf\n"
    "v_cvt_f32_i32 v237, v237\n"
    "v_mfma_f32_16x16x32_bf16 v[68:71], v[176:179], a[216:219], 0\n"
    "ds_read_b128 v[172:175], v200 offset:7168\n"
    "v_mul_f32_dpp v246, v228, v246 row_newbcast:6 row_mask:0xf bank_mask:0xf\n"
    "v_cvt_f32_i32 v238, v238\n"
    "v_mul_f32_dpp v247, v228, v247 row_newbcast:7 row_mask:0xf bank_mask:0xf\n"
    "v_mfma_f32_16x16x32_bf16 v[68:71], v[180:183], a[220:223], v[68:71]\n"
    "s_waitcnt lgkmcnt(4)\n"
    "v_cvt_f32_i32 v239, v239\n"
    "v_add_f32_dpp v240, v229, v240 row_newbcast:0 row_mask:0xf bank_mask:0xf\n"
    "v_mul_f32_dpp v232, v226, v232 row_newbcast:0 row_mask:0xf bank_mask:0xf\n"
    "v_add_f32_dpp v241, v229, v241 row_newbcast:1 row_mask:0xf bank_mask:0xf\n"
    "v_mfma_f32_16x16x32_bf16 v[64:67], v[184:187], a[200:203], v[64:67]\n"
    "ds_read_b128 v[176:179], v200 offset:8192\n"
    "v_mul_f32_dpp v233, v226, v233 row_newbcast:1 row_mask:0xf bank_mask:0xf\n"
    "v_add_f32_dpp v242, v229, v242 row_newbcast:2 row_mask:0xf bank_mask:0xf\n"
    "v_mul_f32_dpp v234, v226, v234 row_newbcast:2 row_mask:0xf bank_mask:0xf\n"
    "v_mfma_f32_16x16x32_bf16 v[64:67], v[188:191], a[204:207], v[64:67]\n"
    "v_add_f32_dpp v243, v229, v243 row_newbcast:3 row_mask:0xf bank_mask:0xf\n"
    "v_mul_f32_dpp v235, v226, v235 row_newbcast:3 row_mask:0xf bank_mask:0xf\n"
    "v_add_f32_dpp v244, v229, v244 row_newbcast:4 row_mask:0xf bank_mask:0xf\n"
    "v_mul_f32_dpp v236, v226, v236 row_newbcast:4 row_mask:0xf bank_mask:0xf\n"
    "v_mfma_f32_16x16x32_bf16 v[68:71], v[184:187], a[224:227], v[68:71]\n"
    "ds_read_b128 v[180:183], v200 offset:9216\n"
    "v_add_f32_dpp v245, v229, v245 row_newbcast:5 row_mask:0xf bank_mask:0xf\n"
    "v_mul_f32_dpp v237, v226, v237 row_newbcast:5 row_mask:0xf bank_mask:0xf\n"
    "v_add_f32_dpp v246, v229, v246 row_newbcast:6 row_mask:0xf bank_mask:0xf\n"
    "v_mul_f32_dpp v238, v226, v238 row_newbcast:6 row_mask:0xf bank_mask:0xf\n"
    "v_add_f32_dpp v247, v229, v247 row_newbcast:7 row_mask:0xf bank_mask:0xf\n"
    "v_mul_f32_dpp v239, v226, v239 row_newbcast:7 row_mask:0xf bank_mask:0xf\n"
    "v_cvt_pk_bf16_f32 v240, v240, v241\n"
    "v_add_f32_dpp v232, v227, v232 row_newbcast:0 row_mask:0xf bank_mask:0xf\n"
    "v_cvt_pk_bf16_f32 v241, v242, v243\n"
    "v_add_f32_dpp v233, v227, v233 row_newbcast:1 row_mask:0xf bank_mask:0xf\n"
    "v_cvt_pk_bf16_f32 v242, v244, v245\n"
    "v_add_f32_dpp v234, v227, v234 row_newbcast:2 row_mask:0xf bank_mask:0xf\n"
    "v_cvt_pk_bf16_f32 v243, v246, v247\n"
    "v_add_f32_dpp v235, v227, v235 row_newbcast:3 row_mask:0xf bank_mask:0xf\n"
    "v_add_f32_dpp v236, v227, v236 row_newbcast:4 row_mask:0xf bank_mask:0xf\n"
    "v_add_f32_dpp v237, v227, v237 row_newbcast:5 row_mask:0xf bank_mask:0xf\n"
    "v_add_f32_dpp v238, v227, v238 row_newbcast:6 row_mask:0xf bank_mask:0xf\n"
    "v_add_f32_dpp v239, v227, v239 row_newbcast:7 row_mask:0xf bank_mask:0xf\n"
    "v_cvt_pk_bf16_f32 v232, v232, v233\n"
    "v_cvt_pk_bf16_f32 v233, v234, v235\n"
    "v_cvt_pk_bf16_f32 v234, v236, v237\n"
    "v_cvt_pk_bf16_f32 v235, v238, v239\n"
    "v_mfma_f32_16x16x32_bf16 v[68:71], v[188:191], a[228:231], v[68:71]\n"
    "s_waitcnt lgkmcnt(4)\n"
    "s_add_u32 s78, s88, 0x3000\n"
    "v_max3_f32 v196, v40, v41, v42\n"
    "v_max3_f32 v196, v196, v43, v56\n"
    "v_max3_f32 v196, v196, v57, v58\n"
    "buffer_store_dwordx4 v[232:235], v211, s[36:39], s76 offen sc1\n"
    "v_mfma_f32_16x16x32_bf16 v[64:67], v[160:163], a[208:211], v[64:67]\n"
    "s_add_u32 m0, s71, 0xd000\n"
    "ds_read_b128 v[184:187], v200 offset:10240\n"
    "v_max_f32 v196, v196, v59\n"
    "v_max3_f32 v198, v44, v45, v46\n"
    "v_mov_b32 v197, v196\n"
    "v_mfma_f32_16x16x32_bf16 v[64:67], v[164:167], a[212:215], v[64:67]\n"
    "buffer_load_dwordx4 v156, s[28:31], s78 offen nt lds\n"
    "v_max3_f32 v198, v198, v47, v60\n"
    "v_permlane32_swap_b32 v196, v197\n"
    "v_mfma_f32_16x16x32_bf16 v[68:71], v[160:163], a[232:235], v[68:71]\n"
    "ds_read_b128 v[188:191], v200 offset:11264\n"
    "v_max3_f32 v198, v198, v61, v62\n"
    "v_max_f32 v196, v196, v197\n"
    "v_max_f32 v198, v198, v63\n"
    "v_mfma_f32_16x16x32_bf16 v[68:71], v[164:167], a[236:239], v[68:71]\n"
    "s_waitcnt lgkmcnt(4)\n"
    "v_mov_b32 v197, v196\n"
    "v_mov_b32 v199, v198\n"
    "s_nop 0\n"
    "v_permlane16_swap_b32 v196, v197\n"
    "v_permlane32_swap_b32 v198, v199\n"
    "v_mfma_f32_16x16x32_bf16 v[80:83], v[168:171], a[192:195], 0\n"
    "ds_read_b128 v[160:163], v200 offset:12288\n"
    "v_max_f32 v196, v196, v197\n"
    "v_max_f32 v198, v198, v199\n"
    "v_add_f32 v196, 0x42640000, v196\n"
    "v_mfma_f32_16x16x32_bf16 v[80:83], v[172:175], a[196:199], v[80:83]\n"
    "buffer_store_dwordx4 v[240:243], v212, s[40:43], s77 offen sc1\n"
    "v_mov_b32 v199, v198\n"
    "v_sub_f32 v96, 0, v196\n"
    "s_nop 0\n"
    "v_permlane16_swap_b32 v198, v199\n"
    "v_sub_f32 v97, 0, v196\n"
    "v_max_f32 v198, v198, v199\n"
    "v_mfma_f32_16x16x32_bf16 v[84:87], v[168:171], a[216:219], 0\n"
    "ds_read_b128 v[164:167], v200 offset:13312\n"
    "v_sub_f32 v98, 0, v196\n"
    "v_add_f32 v198, 0x42640000, v198\n"
    "v_sub_f32 v99, 0, v196\n"
    "v_mfma_f32_16x16x32_bf16 v[84:87], v[172:175], a[220:223], v[84:87]\n"
    "s_waitcnt lgkmcnt(4)\n"
    "v_sub_f32 v100, 0, v198\n"
    "v_add_f32 v40, v40, v96\n"
    "v_sub_f32 v101, 0, v198\n"
    "v_add_f32 v41, v41, v96\n"
    "v_mfma_f32_16x16x32_bf16 v[80:83], v[176:179], a[200:203], v[80:83]\n"
    "ds_read_b128 v[168:171], v200 offset:14336\n"
    "v_sub_f32 v102, 0, v198\n"
    "v_add_f32 v42, v42, v96\n"
    "v_sub_f32 v103, 0, v198\n"
    "v_mfma_f32_16x16x32_bf16 v[80:83], v[180:183], a[204:207], v[80:83]\n"
    "s_add_u32 s78, s88, 0x3000\n"
    "v_add_f32 v43, v43, v96\n"
    "v_add_f32 v44, v44, v100\n"
    "v_add_f32 v56, v56, v96\n"
    "v_add_f32 v45, v45, v100\n"
    "v_mfma_f32_16x16x32_bf16 v[84:87], v[176:179], a[224:227], v[84:87]\n"
    "s_add_u32 m0, s71, 0xd400\n"
    "ds_read_b128 v[172:175], v200 offset:15360\n"
    "v_add_f32 v57, v57, v96\n"
    "v_add_f32 v46, v46, v100\n"
    "v_add_f32 v58, v58, v96\n"
    "v_mfma_f32_16x16x32_bf16 v[84:87], v[180:183], a[228:231], v[84:87]\n"
    "s_waitcnt lgkmcnt(4)\n"
    "buffer_load_dwordx4 v157, s[28:31], s78 offen nt lds\n"
    "v_add_f32 v47, v47, v100\n"
    "v_add_f32 v59, v59, v96\n"
    "v_mfma_f32_16x16x32_bf16 v[80:83], v[184:187], a[208:211], v[80:83]\n"
    "ds_read_b128 v[176:179], v200 offset:16384\n"
    "v_add_f32 v60, v60, v100\n"
    "v_exp_f32 v40, v40\n"
    "v_mfma_f32_16x16x32_bf16 v[80:83], v[188:191], a[212:215], v[80:83]\n"
    "v_add_f32 v61, v61, v100\n"
    "v_add_f32 v62, v62, v100\n"
    "v_exp_f32 v41, v41\n"
    "v_mfma_f32_16x16x32_bf16 v[84:87], v[184:187], a[232:235], v[84:87]\n"
    "ds_read_b128 v[180:183], v200 offset:17408\n"
    "v_add_f32 v63, v63, v100\n"
    "v_exp_f32 v44, v44\n"
    "v_mfma_f32_16x16x32_bf16 v[84:87], v[188:191], a[236:239], v[84:87]\n"
    "s_waitcnt lgkmcnt(4)\n"
    "v_exp_f32 v42, v42\n"
    "v_exp_f32 v45, v45\n"
    "v_mfma_f32_16x16x32_bf16 v[32:35], v[160:163], a[192:195], v[96:99]\n"
    "ds_read_b128 v[184:187], v200 offset:18432\n"
    "v_exp_f32 v43, v43\n"
    "v_mfma_f32_16x16x32_bf16 v[32:35], v[164:167], a[196:199], v[32:35]\n"
    "v_exp_f32 v46, v46\n"
    "v_exp_f32 v56, v56\n"
    "v_mfma_f32_16x16x32_bf16 v[36:39], v[160:163], a[216:219], v[100:103]\n"
    "ds_read_b128 v[188:191], v200 offset:19456\n"
    "v_exp_f32 v47, v47\n"
    "v_mfma_f32_16x16x32_bf16 v[36:39], v[164:167], a[220:223], v[36:39]\n"
    "s_waitcnt lgkmcnt(4)\n"
    "s_add_u32 s78, s88, 0x4800\n"
    "v_exp_f32 v57, v57\n"
    "v_mfma_f32_16x16x32_bf16 v[32:35], v[168:171], a[200:203], v[32:35]\n"
    "s_add_u32 m0, s71, 0x6000\n"
    "ds_read_b128 v[160:163], v200 offset:20480\n"
    "v_exp_f32 v60, v60\n"
    "v_mfma_f32_16x16x32_bf16 v[32:35], v[172:175], a[204:207], v[32:35]\n"
    "buffer_load_dwordx4 v152, s[28:31], s78 offen nt lds\n"
    "v_exp_f32 v58, v58\n"
    "v_mfma_f32_16x16x32_bf16 v[36:39], v[168:171], a[224:227], v[36:39]\n"
    "ds_read_b128 v[164:167], v200 offset:21504\n"
    "v_exp_f32 v61, v61\n"
    "v_mfma_f32_16x16x32_bf16 v[36:39], v[172:175], a[228:231], v[36:39]\n"
    "s_waitcnt lgkmcnt(4)\n"
    "v_exp_f32 v59, v59\n"
    "v_exp_f32 v62, v62\n"
    "v_mfma_f32_16x16x32_bf16 v[32:35], v[176:179], a[208:211], v[32:35]\n"
    "ds_read_b128 v[168:171], v200 offset:22528\n"
    "v_cvt_pk_bf16_f32 v128, v40, v41\n"
    "v_cvt_pk_bf16_f32 v129, v42, v43\n"
    "v_cvt_pk_bf16_f32 v130, v56, v57\n"
    "v_mfma_f32_16x16x32_bf16 v[32:35], v[180:183], a[212:215], v[32:35]\n"
    "v_exp_f32 v63, v63\n"
    "v_cvt_pk_bf16_f32 v131, v58, v59\n"
    "v_cvt_pk_bf16_f32 v132, v44, v45\n"
    "v_mfma_f32_16x16x32_bf16 v[36:39], v[176:179], a[232:235], v[36:39]\n"
    "ds_read_b128 v[172:175], v200 offset:23552\n"
    "v_add_f32 v64, v64, v96\n"
    "v_cvt_pk_bf16_f32 v133, v46, v47\n"
    "v_add_f32 v65, v65, v96\n"
    "v_mfma_f32_16x16x32_bf16 v[36:39], v[180:183], a[236:239], v[36:39]\n"
    "s_waitcnt lgkmcnt(4)\n"
    "v_cvt_pk_bf16_f32 v134, v60, v61\n"
    "v_add_f32 v66, v66, v96\n"
    "s_waitcnt vmcnt(11)\n"
    "s_barrier\n"
    "v_mfma_f32_16x16x32_bf16 v[48:51], v[184:187], a[192:195], v[96:99]\n"
    "ds_read_b128 v[176:179], v201 offset:24576\n"
    "v_cvt_pk_bf16_f32 v135, v62, v63\n"
    "v_add_f32 v67, v67, v96\n"
    "v_add_f32 v68, v68, v100\n"
    "v_mfma_f32_16x16x32_bf16 v[48:51], v[188:191], a[196:199], v[48:51]\n"
    "s_add_u32 s78, s88, 0x4800\n"
    "v_add_f32 v80, v80, v96\n"
    "v_add_f32 v69, v69, v100\n"
    "v_add_f32 v81, v81, v96\n"
    "v_add_f32 v70, v70, v100\n"
    "v_mfma_f32_16x16x32_bf16 v[52:55], v[184:187], a[216:219], v[100:103]\n"
    "s_add_u32 m0, s71, 0x6400\n"
    "ds_read_b128 v[180:183], v201 offset:25600\n"
    "v_add_f32 v82, v82, v96\n"
    "v_add_f32 v71, v71, v100\n"
    "v_add_f32 v83, v83, v96\n"
    "v_mfma_f32_16x16x32_bf16 v[52:55], v[188:191], a[220:223], v[52:55]\n"
    "s_waitcnt lgkmcnt(4)\n"
    "buffer_load_dwordx4 v153, s[28:31], s78 offen nt lds\n"
    "v_add_f32 v84, v84, v100\n"
    "v_add_f32 v85, v85, v100\n"
    "v_mfma_f32_16x16x32_bf16 v[48:51], v[160:163], a[200:203], v[48:51]\n"
    "ds_read_b128 v[184:187], v201 offset:26624\n"
    "v_exp_f32 v64, v64\n"
    "v_add_f32 v86, v86, v100\n"
    "v_mfma_f32_16x16x32_bf16 v[48:51], v[164:167], a[204:207], v[48:51]\n"
    "v_exp_f32 v65, v65\n"
    "v_add_f32 v87, v87, v100\n"
    "ds_read_b64 v[228:229], v210 offset:192\n"
    "v_mfma_f32_16x16x32_bf16 v[52:55], v[160:163], a[224:227], v[52:55]\n"
    "ds_read_b128 v[188:191], v201 offset:27648\n"
    "v_exp_f32 v68, v68\n"
    "ds_read2st64_b32 v[240:241], v207 offset0:96 offset1:99\n"
    "v_mfma_f32_16x16x32_bf16 v[52:55], v[164:167], a[228:231], v[52:55]\n"
    "s_waitcnt lgkmcnt(6)\n"
    "v_exp_f32 v66, v66\n"
    "v_exp_f32 v69, v69\n"
    "v_mfma_f32_16x16x32_bf16 v[48:51], v[168:171], a[208:211], v[48:51]\n"
    "ds_read_b128 v[160:163], v201 offset:28672\n"
    "v_exp_f32 v67, v67\n"
    "ds_read2st64_b32 v[242:243], v207 offset0:102 offset1:105\n"
    "v_mfma_f32_16x16x32_bf16 v[48:51], v[172:175], a[212:215], v[48:51]\n"
    "v_exp_f32 v70, v70\n"
    "v_exp_f32 v80, v80\n"
    "v_mfma_f32_16x16x32_bf16 v[52:55], v[168:171], a[232:235], v[52:55]\n"
    "ds_read_b128 v[164:167], v201 offset:29696\n"
    "v_exp_f32 v71, v71\n"
    "ds_read2st64_b32 v[244:245], v207 offset0:108 offset1:111\n"
    "v_exp_f32 v81, v81\n"
    "v_exp_f32 v82, v82\n"
    "v_exp_f32 v83, v83\n"
    "v_cvt_pk_bf16_f32 v144, v64, v65\n"
    "v_cvt_pk_bf16_f32 v145, v66, v67\n"
    "v_cvt_pk_bf16_f32 v146, v80, v81\n"
    "v_cvt_pk_bf16_f32 v147, v82, v83\n"
    "v_mfma_f32_16x16x32_bf16 v[52:55], v[172:175], a[236:239], v[52:55]\n"
    "s_waitcnt lgkmcnt(8)\n"
    "s_add_u32 s78, s88, 0x4800\n"
    "v_exp_f32 v84, v84\n"
    "ds_read2st64_b32 v[246:247], v207 offset0:114 offset1:117\n"
    "v_mfma_f32_16x16x32_bf16 a[0:3], v[176:179], v[128:131], 0\n"
    "s_add_u32 m0, s71, 0x6800\n"
    "ds_read_b128 v[168:171], v201 offset:30720\n"
    "ds_read_u16_d16_hi v223, v209 offset:6912\n"
    "v_exp_f32 v85, v85\n"
    "v_exp_f32 v86, v86\n"
    "v_exp_f32 v87, v87\n"
    "v_cvt_pk_bf16_f32 v148, v68, v69\n"
    "v_cvt_pk_bf16_f32 v149, v70, v71\n"
    "v_cvt_pk_bf16_f32 v150, v84, v85\n"
    "v_cvt_pk_bf16_f32 v151, v86, v87\n"
    "v_mfma_f32_16x16x32_bf16 a[0:3], v[180:183], v[144:147], a[0:3]\n"
    "buffer_load_dwordx4 v154, s[28:31], s78 offen nt lds\n"
    "v_mfma_f32_16x16x32_bf16 a[4:7], v[176:179], v[132:135], 0\n"
    "ds_read_b128 v[172:175], v201 offset:31744\n"
    "ds_read_u16_d16_hi v222, v209 offset:768\n"
    "v_mfma_f32_16x16x32_bf16 a[4:7], v[180:183], v[148:151], a[4:7]\n"
    "s_waitcnt lgkmcnt(10)\n"
    "v_exp_f32 v32, v32\n"
    "v_mfma_f32_16x16x32_bf16 a[16:19], v[184:187], v[128:131], 0\n"
    "ds_read_b128 v[176:179], v201 offset:32768\n"
    "ds_read_b128 v[232:235], v205 offset:24576\n"
    "v_mfma_f32_16x16x32_bf16 a[16:19], v[188:191], v[144:147], a[16:19]\n"
    "ds_read_b128 v[236:239], v206 offset:24576\n"
    "v_mfma_f32_16x16x32_bf16 a[20:23], v[184:187], v[132:135], 0\n"
    "ds_read_b128 v[180:183], v201 offset:33792\n"
    "s_waitcnt lgkmcnt(13)\n"
    "v_cvt_f32_i32 v240, v240\n"
    "v_mfma_f32_16x16x32_bf16 a[20:23], v[188:191], v[148:151], a[20:23]\n"
    "s_waitcnt lgkmcnt(10)\n"
    "v_exp_f32 v33, v33\n"
    "v_mfma_f32_16x16x32_bf16 a[32:35], v[160:163], v[128:131], 0\n"
    "ds_read_b128 v[184:187], v201 offset:34816\n"
    "v_cvt_f32_i32 v241, v241\n"
    "v_mfma_f32_16x16x32_bf16 a[32:35], v[164:167], v[144:147], a[32:35]\n"
    "s_add_u32 s78, s88, 0x4800\n"
    "v_exp_f32 v34, v34\n"
    "v_mfma_f32_16x16x32_bf16 a[36:39], v[160:163], v[132:135], 0\n"
    "s_add_u32 m0, s71, 0x6c00\n"
    "ds_read_b128 v[188:191], v201 offset:35840\n"
    "v_cvt_f32_i32 v242, v242\n"
    "v_mfma_f32_16x16x32_bf16 a[36:39], v[164:167], v[148:151], a[36:39]\n"
    "buffer_load_dwordx4 v155, s[28:31], s78 offen nt lds\n"
    "v_mfma_f32_16x16x32_bf16 v[112:115], v[192:195], v[128:131], 0\n"
    "v_exp_f32 v35, v35\n"
    "v_mfma_f32_16x16x32_bf16 v[112:115], v[192:195], v[144:147], v[112:115]\n"
    "s_waitcnt lgkmcnt(7)\n"
    "v_exp_f32 v36, v36\n"
    "v_mfma_f32_16x16x32_bf16 a[48:51], v[168:171], v[128:131], 0\n"
    "ds_read_b128 v[160:163], v201 offset:36864\n"
    "v_cvt_f32_i32 v243, v243\n"
    "v_mfma_f32_16x16x32_bf16 a[48:51], v[172:175], v[144:147], a[48:51]\n"
    "v_cvt_f32_i32 v244, v244\n"
    "v_cvt_f32_i32 v245, v245\n"
    "v_mfma_f32_16x16x32_bf16 a[52:55], v[168:171], v[132:135], 0\n"
    "ds_read_b128 v[164:167], v201 offset:37888\n"
    "v_cvt_f32_i32 v246, v246\n"
    "v_mfma_f32_16x16x32_bf16 a[52:55], v[172:175], v[148:151], a[52:55]\n"
    "v_exp_f32 v37, v37\n"
    "v_mfma_f32_16x16x32_bf16 v[116:119], v[192:195], v[132:135], 0\n"
    "v_exp_f32 v38, v38\n"
    "v_mfma_f32_16x16x32_bf16 v[116:119], v[192:195], v[148:151], v[116:119]\n"
    "s_waitcnt lgkmcnt(4)\n"
    "s_add_u32 s78, s88, 0x4800\n"
    "v_mul_f32 v226, s75, v223\n"
    "v_mfma_f32_16x16x32_bf16 a[64:67], v[176:179], v[128:131], 0\n"
    "s_add_u32 m0, s71, 0x7000\n"
    "ds_read_b128 v[168:171], v201 offset:38912\n"
    "v_cvt_f32_i32 v247, v247\n"
    "v_mfma_f32_16x16x32_bf16 a[64:67], v[180:183], v[144:147], a[64:67]\n"
    "buffer_load_dwordx4 v156, s[28:31], s78 offen nt lds\n"
    "v_mfma_f32_16x16x32_bf16 a[68:71], v[176:179], v[132:135], 0\n"
    "ds_read_b128 v[172:175], v201 offset:39936\n"
    "v_fma_f32 v227, -v222, v226, v217\n"
    "v_mfma_f32_16x16x32_bf16 a[68:71], v[180:183], v[148:151], a[68:71]\n"
    "s_waitcnt lgkmcnt(4)\n"
    "v_mul_f32_dpp v240, v228, v240 row_newbcast:0 row_mask:0xf bank_mask:0xf\n"
    "v_cvt_f32_i32 v232, v232\n"
    "v_mfma_f32_16x16x32_bf16 a[80:83], v[184:187], v[128:131], 0\n"
    "ds_read_b128 v[176:179], v201 offset:40960\n"
    "v_mul_f32_dpp v241, v228, v241 row_newbcast:1 row_mask:0xf bank_mask:0xf\n"
    "v_mfma_f32_16x16x32_bf16 a[80:83], v[188:191], v[144:147], a[80:83]\n"
    "v_exp_f32 v39, v39\n"
    "v_mfma_f32_16x16x32_bf16 a[84:87], v[184:187], v[132:135], 0\n"
    "ds_read_b128 v[180:183], v201 offset:41984\n"
    "v_cvt_f32_i32 v233, v233\n"
    "v_mfma_f32_16x16x32_bf16 a[84:87], v[188:191], v[148:151], a[84:87]\n"
    "s_waitcnt lgkmcnt(4)\n"
    "v_mul_f32_dpp v242, v228, v242 row_newbcast:2 row_mask:0xf bank_mask:0xf\n"
    "v_cvt_f32_i32 v234, v234\n"
    "v_mfma_f32_16x16x32_bf16 a[96:99], v[160:163], v[128:131], 0\n"
    "ds_read_b128 v[184:187], v201 offset:43008\n"
    "v_mul_f32_dpp v243, v228, v243 row_newbcast:3 row_mask:0xf bank_mask:0xf\n"
    "v_mfma_f32_16x16x32_bf16 a[96:99], v[164:167], v[144:147], a[96:99]\n"
    "s_add_u32 s78, s88, 0x4800\n"
    "v_exp_f32 v48, v48\n"
    "v_mfma_f32_16x16x32_bf16 a[100:103], v[160:163], v[132:135], 0\n"
    "s_add_u32 m0, s71, 0x7400\n"
    "ds_read_b128 v[188:191], v201 offset:44032\n"
    "v_cvt_f32_i32 v235, v235\n"
    "v_mfma_f32_16x16x32_bf16 a[100:103], v[164:167], v[148:151], a[100:103]\n"
    "s_waitcnt lgkmcnt(4)\n"
    "buffer_load_dwordx4 v157, s[28:31], s78 offen nt lds\n"
    "v_mfma_f32_16x16x32_bf16 a[112:115], v[168:171], v[128:131], 0\n"
    "ds_read_b128 v[160:163], v201 offset:45056\n"
    "v_mul_f32_dpp v244, v228, v244 row_newbcast:4 row_mask:0xf bank_mask:0xf\n"
    "v_mfma_f32_16x16x32_bf16 a[112:115], v[172:175], v[144:147], a[112:115]\n"
    "v_exp_f32 v49, v49\n"
    "v_mfma_f32_16x16x32_bf16 a[116:119], v[168:171], v[132:135], 0\n"
    "ds_read_b128 v[164:167], v201 offset:46080\n"
    "v_cvt_f32_i32 v236, v236\n"
    "v_mfma_f32_16x16x32_bf16 a[116:119], v[172:175], v[148:151], a[116:119]\n"
    "s_waitcnt lgkmcnt(4)\n"
    "v_mul_f32_dpp v245, v228, v245 row_newbcast:5 row_mask:0xf bank_mask:0xf\n"
    "v_cvt_f32_i32 v237, v237\n"
    "v_mfma_f32_16x16x32_bf16 a[128:131], v[176:179], v[128:131], 0\n"
    "ds_read_b128 v[168:171], v201 offset:47104\n"
    "v_mul_f32_dpp v246, v228, v246 row_newbcast:6 row_mask:0xf bank_mask:0xf\n"
    "v_mfma_f32_16x16x32_bf16 a[128:131], v[180:183], v[144:147], a[128:131]\n"
    "v_exp_f32 v50, v50\n"
    "v_mfma_f32_16x16x32_bf16 a[132:135], v[176:179], v[132:135], 0\n"
    "ds_read_b128 v[172:175], v201 offset:48128\n"
    "v_cvt_f32_i32 v238, v238\n"
    "v_mfma_f32_16x16x32_bf16 a[132:135], v[180:183], v[148:151], a[132:135]\n"
    "s_waitcnt lgkmcnt(4)\n"
    "v_mul_f32_dpp v247, v228, v247 row_newbcast:7 row_mask:0xf bank_mask:0xf\n"
    "v_cvt_f32_i32 v239, v239\n"
    "v_mfma_f32_16x16x32_bf16 a[144:147], v[184:187], v[128:131], 0\n"
    "v_exp_f32 v51, v51\n"
    "v_mfma_f32_16x16x32_bf16 a[144:147], v[188:191], v[144:147], a[144:147]\n"
    "v_add_f32_dpp v240, v229, v240 row_newbcast:0 row_mask:0xf bank_mask:0xf\n"
    "v_mul_f32_dpp v232, v226, v232 row_newbcast:0 row_mask:0xf bank_mask:0xf\n"
    "v_mfma_f32_16x16x32_bf16 a[148:151], v[184:187], v[132:135], 0\n"
    "v_add_f32_dpp v241, v229, v241 row_newbcast:1 row_mask:0xf bank_mask:0xf\n"
    "v_mul_f32_dpp v233, v226, v233 row_newbcast:1 row_mask:0xf bank_mask:0xf\n"
    "v_mfma_f32_16x16x32_bf16 a[148:151], v[188:191], v[148:151], a[148:151]\n"
    "s_waitcnt lgkmcnt(2)\n"
    "v_exp_f32 v52, v52\n"
    "v_mfma_f32_16x16x32_bf16 a[160:163], v[160:163], v[128:131], 0\n"
    "v_add_f32_dpp v242, v229, v242 row_newbcast:2 row_mask:0xf bank_mask:0xf\n"
    "v_mul_f32_dpp v234, v226, v234 row_newbcast:2 row_mask:0xf bank_mask:0xf\n"
    "v_mfma_f32_16x16x32_bf16 a[160:163], v[164:167], v[144:147], a[160:163]\n"
    "v_add_f32_dpp v243, v229, v243 row_newbcast:3 row_mask:0xf bank_mask:0xf\n"
    "v_mul_f32_dpp v235, v226, v235 row_newbcast:3 row_mask:0xf bank_mask:0xf\n"
    "v_mfma_f32_16x16x32_bf16 a[164:167], v[160:163], v[132:135], 0\n"
    "v_exp_f32 v53, v53\n"
    "v_mfma_f32_16x16x32_bf16 a[164:167], v[164:167], v[148:151], a[164:167]\n"
    "s_waitcnt lgkmcnt(0)\n"
    "v_add_f32_dpp v244, v229, v244 row_newbcast:4 row_mask:0xf bank_mask:0xf\n"
    "v_mul_f32_dpp v236, v226, v236 row_newbcast:4 row_mask:0xf bank_mask:0xf\n"
    "v_mfma_f32_16x16x32_bf16 a[176:179], v[168:171], v[128:131], 0\n"
    "v_add_f32_dpp v245, v229, v245 row_newbcast:5 row_mask:0xf bank_mask:0xf\n"
    "v_mul_f32_dpp v237, v226, v237 row_newbcast:5 row_mask:0xf bank_mask:0xf\n"
    "v_mfma_f32_16x16x32_bf16 a[176:179], v[172:175], v[144:147], a[176:179]\n"
    "v_exp_f32 v54, v54\n"
    "v_mfma_f32_16x16x32_bf16 a[180:183], v[168:171], v[132:135], 0\n"
    "v_add_f32_dpp v246, v229, v246 row_newbcast:6 row_mask:0xf bank_mask:0xf\n"
    "v_mul_f32_dpp v238, v226, v238 row_newbcast:6 row_mask:0xf bank_mask:0xf\n"
    "v_add_f32_dpp v247, v229, v247 row_newbcast:7 row_mask:0xf bank_mask:0xf\n"
    "v_exp_f32 v55, v55\n"
    "v_mul_f32_dpp v239, v226, v239 row_newbcast:7 row_mask:0xf bank_mask:0xf\n"
    "v_cvt_pk_bf16_f32 v240, v240, v241\n"
    "v_add_f32_dpp v232, v227, v232 row_newbcast:0 row_mask:0xf bank_mask:0xf\n"
    "v_cvt_pk_bf16_f32 v241, v242, v243\n"
    "v_add_f32_dpp v233, v227, v233 row_newbcast:1 row_mask:0xf bank_mask:0xf\n"
    "v_cvt_pk_bf16_f32 v242, v244, v245\n"
    "v_add_f32_dpp v234, v227, v234 row_newbcast:2 row_mask:0xf bank_mask:0xf\n"
    "v_cvt_pk_bf16_f32 v243, v246, v247\n"
    "v_add_f32_dpp v235, v227, v235 row_newbcast:3 row_mask:0xf bank_mask:0xf\n"
    "v_add_f32_dpp v236, v227, v236 row_newbcast:4 row_mask:0xf bank_mask:0xf\n"
    "v_add_f32_dpp v237, v227, v237 row_newbcast:5 row_mask:0xf bank_mask:0xf\n"
    "v_add_f32_dpp v238, v227, v238 row_newbcast:6 row_mask:0xf bank_mask:0xf\n"
    "v_add_f32_dpp v239, v227, v239 row_newbcast:7 row_mask:0xf bank_mask:0xf\n"
    "v_cvt_pk_bf16_f32 v232, v232, v233\n"
    "v_cvt_pk_bf16_f32 v233, v234, v235\n"
    "v_cvt_pk_bf16_f32 v234, v236, v237\n"
    "v_cvt_pk_bf16_f32 v235, v238, v239\n"
    "v_mfma_f32_16x16x32_bf16 a[180:183], v[172:175], v[148:151], a[180:183]\n"
    "v_add_f32 v113, v113, v32\n"
    "v_add_f32 v114, v114, v33\n"
    "v_add_f32 v113, v113, v34\n"
    "v_add_f32 v114, v114, v35\n"
    "v_add_f32 v117, v117, v36\n"
    "v_add_f32 v118, v118, v37\n"
    "v_add_f32 v117, v117, v38\n"
    "v_add_f32 v118, v118, v39\n"
    "v_add_f32 v113, v113, v48\n"
    "v_add_f32 v114, v114, v49\n"
    "v_add_f32 v113, v113, v50\n"
    "v_add_f32 v114, v114, v51\n"
    "v_add_f32 v117, v117, v52\n"
    "buffer_store_dwordx4 v[232:235], v211, s[36:39], s62 offen sc1\n"
    "buffer_store_dwordx4 v[240:243], v212, s[40:43], s63 offen sc1\n"
    "v_add_f32 v118, v118, v53\n"
    "v_add_f32 v117, v117, v54\n"
    "v_add_f32 v118, v118, v55\n"
    "s_waitcnt vmcnt(8)\n"
    "ds_read_b128 a[240:243], v158 offset:49152\n"
    "ds_read_b128 a[244:247], v159 offset:49152\n"
    "ds_read_b128 a[248:251], v158 offset:49280\n"
    "ds_read_b128 a[252:255], v159 offset:49280\n"
    "ds_read_b128 v[0:3], v158 offset:49408\n"
    "ds_read_b128 v[4:7], v159 offset:49408\n"
    "v_cmp_ne_u32 s[80:81], s67, v219\n"
    "v_cmp_gt_u32 vcc, s66, v218\n"
    "s_or_b64 vcc, vcc, s[80:81]\n"
    "s_cbranch_vccnz LSPRE1\n"
    "LSPB1:\n"
    "v_mov_b32 v220, 4\n"
    "s_add_u32 s66, s66, s74\n"
    "s_waitcnt lgkmcnt(0)\n"
    "s_barrier\n"
    "buffer_store_dwordx2 v[220:221], v214, s[44:47], 0 offen sc1\n"
    "ds_read_b128 v[160:163], v255\n"
    "ds_read_b128 v[164:167], v255 offset:1024\n"
    "ds_read_b128 v[168:171], v255 offset:2048\n"
    "ds_read_b128 v[172:175], v255 offset:3072\n"
    "ds_read_b128 v[176:179], v255 offset:4096\n"
    "ds_read_b128 v[180:183], v255 offset:5120\n"
    "s_waitcnt lgkmcnt(4)\n"
    "v_mfma_f32_16x16x32_bf16 v[44:47], v[160:163], a[240:243], 0\n"
    "ds_read_b128 v[184:187], v255 offset:6144\n"
    "v_mfma_f32_16x16x32_bf16 v[44:47], v[164:167], a[244:247], v[44:47]\n"
    "s_waitcnt lgkmcnt(3)\n"
    "ds_read_b128 v[188:191], v255 offset:7168\n"
    "v_mfma_f32_16x16x32_bf16 v[44:47], v[168:171], a[248:251], v[44:47]\n"
    "s_add_u32 m0, s71, 0xc000\n"
    "ds_read_b128 v[160:163], v255 offset:8192\n"
    "v_mfma_f32_16x16x32_bf16 v[44:47], v[172:175], a[252:255], v[44:47]\n"
    "s_waitcnt lgkmcnt(3)\n"
    "ds_read_b128 v[164:167], v255 offset:9216\n"
    "buffer_load_dwordx4 v200, s[40:43], s61 offen sc1 lds\n"
    "v_mfma_f32_16x16x32_bf16 v[44:47], v[176:179], v[0:3], v[44:47]\n"
    "ds_read_b128 v[168:171], v255 offset:10240\n"
    "v_mfma_f32_16x16x32_bf16 v[44:47], v[180:183], v[4:7], v[44:47]\n"
    "s_waitcnt lgkmcnt(3)\n"
    "ds_read_b128 v[172:175], v255 offset:11264\n"
    "v_mfma_f32_16x16x32_bf16 v[60:63], v[184:187], a[240:243], 0\n"
    "ds_read_b128 v[176:179], v200\n"
    "v_mfma_f32_16x16x32_bf16 v[60:63], v[188:191], a[244:247], v[60:63]\n"
    "s_waitcnt lgkmcnt(3)\n"
    "ds_read_b128 v[180:183], v200 offset:1024\n"
    "v_mfma_f32_16x16x32_bf16 v[60:63], v[160:163], a[248:251], v[60:63]\n"
    "ds_read_b128 v[184:187], v200 offset:2048\n"
    "buffer_load_dwordx4 v200, s[40:43], s61 offen offset:1024 sc1 lds\n"
    "v_mfma_f32_16x16x32_bf16 v[60:63], v[164:167], a[252:255], v[60:63]\n"
    "s_waitcnt lgkmcnt(3)\n"
    "ds_read_b128 v[188:191], v200 offset:3072\n"
    "v_mfma_f32_16x16x32_bf16 v[60:63], v[168:171], v[0:3], v[60:63]\n"
    "ds_read_b128 v[160:163], v200 offset:4096\n"
    "v_mfma_f32_16x16x32_bf16 v[60:63], v[172:175], v[4:7], v[60:63]\n"
    "s_waitcnt lgkmcnt(3)\n"
    "ds_read_b128 v[164:167], v200 offset:5120\n"
    "v_mfma_f32_16x16x32_bf16 v[72:75], v[176:179], a[240:243], 0\n"
    "ds_read_b128 v[168:171], v200 offset:6144\n"
    "v_mfma_f32_16x16x32_bf16 v[72:75], v[180:183], a[244:247], v[72:75]\n"
    "s_waitcnt lgkmcnt(3)\n"
    "ds_read_b128 v[172:175], v200 offset:7168\n"
    "buffer_load_dwordx4 v200, s[40:43], s61 offen offset:2048 sc1 lds\n"
    "v_mfma_f32_16x16x32_bf16 v[72:75], v[184:187], a[248:251], v[72:75]\n"
    "ds_read_b128 v[176:179], v200 offset:8192\n"
    "v_mfma_f32_16x16x32_bf16 v[72:75], v[188:191], a[252:255], v[72:75]\n"
    "s_waitcnt lgkmcnt(3)\n"
    "ds_read_b128 v[180:183], v200 offset:9216\n"
    "v_mfma_f32_16x16x32_bf16 v[72:75], v[160:163], v[0:3], v[72:75]\n"
    "ds_read_b128 v[184:187], v200 offset:10240\n"
    "v_mfma_f32_16x16x32_bf16 v[72:75], v[164:167], v[4:7], v[72:75]\n"
    "s_waitcnt lgkmcnt(3)\n"
    "ds_read_b128 v[188:191], v200 offset:11264\n"
    "v_max3_f32 v196, v44, v45, v46\n"
    "v_max3_f32 v196, v196, v47, v60\n"
    "v_max3_f32 v196, v196, v61, v62\n"
    "v_mfma_f32_16x16x32_bf16 v[88:91], v[168:171], a[240:243], 0\n"
    "ds_read_b128 v[160:163], v200 offset:12288\n"
    "buffer_load_dwordx4 v200, s[40:43], s61 offen offset:3072 sc1 lds\n"
    "v_max_f32 v196, v196, v63\n"
    "v_mfma_f32_16x16x32_bf16 v[88:91], v[172:175], a[244:247], v[88:91]\n"
    "s_waitcnt lgkmcnt(3)\n"
    "ds_read_b128 v[164:167], v200 offset:13312\n"
    "v_mov_b32 v197, v196\n"
    "s_nop 1\n"
    "v_permlane32_swap_b32 v196, v197\n"
    "v_max_f32 v196, v196, v197\n"
    "v_mfma_f32_16x16x32_bf16 v[88:91], v[176:179], a[248:251], v[88:91]\n"
    "ds_read_b128 v[168:171], v200 offset:14336\n"
    "v_mov_b32 v197, v196\n"
    "s_nop 1\n"
    "v_permlane16_swap_b32 v196, v197\n"
    "v_max_f32 v196, v196, v197\n"
    "v_mfma_f32_16x16x32_bf16 v[88:91], v[180:183], a[252:255], v[88:91]\n"
    "s_waitcnt lgkmcnt(3)\n"
    "s_add_u32 s65, s61, 0x1000\n"
    "ds_read_b128 v[172:175], v200 offset:15360\n"
    "v_add_f32 v196, 0x42640000, v196\n"
    "v_sub_f32 v104, 0, v196\n"
    "v_mfma_f32_16x16x32_bf16 v[88:91], v[184:187], v[0:3], v[88:91]\n"
    "s_add_u32 m0, s71, 0xd000\n"
    "ds_read_b128 v[176:179], v200 offset:16384\n"
    "v_sub_f32 v105, 0, v196\n"
    "v_sub_f32 v106, 0, v196\n"
    "v_sub_f32 v107, 0, v196\n"
    "v_mfma_f32_16x16x32_bf16 v[88:91], v[188:191], v[4:7], v[88:91]\n"
    "s_waitcnt lgkmcnt(3)\n"
    "ds_read_b128 v[180:183], v200 offset:17408\n"
    "buffer_load_dwordx4 v200, s[40:43], s65 offen sc1 lds\n"
    "v_add_f32 v44, v44, v104\n"
    "v_mfma_f32_16x16x32_bf16 v[40:43], v[160:163], a[240:243], v[104:107]\n"
    "ds_read_b128 v[184:187], v200 offset:18432\n"
    "v_add_f32 v45, v45, v104\n"
    "v_add_f32 v46, v46, v104\n"
    "v_add_f32 v47, v47, v104\n"
    "v_mfma_f32_16x16x32_bf16 v[40:43], v[164:167], a[244:247], v[40:43]\n"
    "s_waitcnt lgkmcnt(3)\n"
    "ds_read_b128 v[188:191], v200 offset:19456\n"
    "v_add_f32 v60, v60, v104\n"
    "v_add_f32 v61, v61, v104\n"
    "v_add_f32 v62, v62, v104\n"
    "v_mfma_f32_16x16x32_bf16 v[40:43], v[168:171], a[248:251], v[40:43]\n"
    "ds_read_b128 v[160:163], v200 offset:20480\n"
    "v_add_f32 v63, v63, v104\n"
    "v_exp_f32 v44, v44\n"
    "v_mfma_f32_16x16x32_bf16 v[40:43], v[172:175], a[252:255], v[40:43]\n"
    "s_waitcnt lgkmcnt(3)\n"
    "ds_read_b128 v[164:167], v200 offset:21504\n"
    "v_exp_f32 v45, v45\n"
    "v_mfma_f32_16x16x32_bf16 v[40:43], v[176:179], v[0:3], v[40:43]\n"
    "ds_read_b128 v[168:171], v200 offset:22528\n"
    "buffer_load_dwordx4 v200, s[40:43], s65 offen offset:1024 sc1 lds\n"
    "v_mfma_f32_16x16x32_bf16 v[40:43], v[180:183], v[4:7], v[40:43]\n"
    "s_waitcnt lgkmcnt(3)\n"
    "ds_read_b128 v[172:175], v200 offset:23552\n"
    "v_exp_f32 v46, v46\n"
    "v_mfma_f32_16x16x32_bf16 v[56:59], v[184:187], a[240:243], v[104:107]\n"
    "ds_read_b128 v[176:179], v201 offset:24576\n"
    "v_exp_f32 v47, v47\n"
    "v_mfma_f32_16x16x32_bf16 v[56:59], v[188:191], a[244:247], v[56:59]\n"
    "s_waitcnt lgkmcnt(3)\n"
    "s_add_u32 s78, s64, 0x30000\n"
    "ds_read_b128 v[180:183], v201 offset:25600\n"
    "v_exp_f32 v60, v60\n"
    "v_mfma_f32_16x16x32_bf16 v[56:59], v[160:163], a[248:251], v[56:59]\n"
    "s_add_u32 m0, s72, 0x1b000\n"
    "ds_read_b128 v[184:187], v201 offset:26624\n"
    "v_exp_f32 v61, v61\n"
    "v_mfma_f32_16x16x32_bf16 v[56:59], v[164:167], a[252:255], v[56:59]\n"
    "s_waitcnt lgkmcnt(3)\n"
    "ds_read_b128 v[188:191], v201 offset:27648\n"
    "buffer_load_dwordx4 v202, s[32:35], s78 offen nt lds\n"
    "v_mfma_f32_16x16x32_bf16 v[56:59], v[168:171], v[0:3], v[56:59]\n"
    "ds_read_b128 v[160:163], v201 offset:28672\n"
    "v_exp_f32 v62, v62\n"
    "v_exp_f32 v63, v63\n"
    "v_cvt_pk_bf16_f32 v136, v44, v45\n"
    "v_cvt_pk_bf16_f32 v137, v46, v47\n"
    "v_cvt_pk_bf16_f32 v138, v60, v61\n"
    "v_cvt_pk_bf16_f32 v139, v62, v63\n"
    "v_add_f32 v72, v72, v104\n"
    "v_add_f32 v73, v73, v104\n"
    "v_add_f32 v74, v74, v104\n"
    "v_add_f32 v75, v75, v104\n"
    "v_add_f32 v88, v88, v104\n"
    "v_add_f32 v89, v89, v104\n"
    "v_add_f32 v90, v90, v104\n"
    "v_add_f32 v91, v91, v104\n"
    "v_exp_f32 v72, v72\n"
    "v_exp_f32 v73, v73\n"
    "v_exp_f32 v74, v74\n"
    "v_exp_f32 v75, v75\n"
    "v_exp_f32 v88, v88\n"
    "v_exp_f32 v89, v89\n"
    "v_exp_f32 v90, v90\n"
    "v_exp_f32 v91, v91\n"
    "v_cvt_pk_bf16_f32 v152, v72, v73\n"
    "v_cvt_pk_bf16_f32 v153, v74, v75\n"
    "v_cvt_pk_bf16_f32 v154, v88, v89\n"
    "v_cvt_pk_bf16_f32 v155, v90, v91\n"
    "v_mfma_f32_16x16x32_bf16 v[56:59], v[172:175], v[4:7], v[56:59]\n"
    "s_waitcnt lgkmcnt(3)\n"
    "ds_read_b128 v[164:167], v201 offset:29696\n"
    "v_exp_f32 v40, v40\n"
    "v_mfma_f32_16x16x32_bf16 a[8:11], v[176:179], v[136:139], 0\n"
    "ds_read_b128 v[168:171], v201 offset:30720\n"
    "v_mfma_f32_16x16x32_bf16 a[8:11], v[180:183], v[152:155], a[8:11]\n"
    "s_waitcnt lgkmcnt(3)\n"
    "ds_read_b128 v[172:175], v201 offset:31744\n"
    "v_mfma_f32_16x16x32_bf16 a[24:27], v[184:187], v[136:139], 0\n"
    "ds_read_b128 v[176:179], v201 offset:32768\n"
    "buffer_load_dwordx4 v203, s[32:35], s78 offen offset:1024 nt lds\n"
    "v_mfma_f32_16x16x32_bf16 a[24:27], v[188:191], v[152:155], a[24:27]\n"
    "s_waitcnt lgkmcnt(3)\n"
    "ds_read_b128 v[180:183], v201 offset:33792\n"
    "v_mfma_f32_16x16x32_bf16 a[40:43], v[160:163], v[136:139], 0\n"
    "ds_read_b128 v[184:187], v201 offset:34816\n"
    "v_mfma_f32_16x16x32_bf16 a[40:43], v[164:167], v[152:155], a[40:43]\n"
    "ds_read_b128 v[188:191], v201 offset:35840\n"
    "v_mfma_f32_16x16x32_bf16 v[120:123], v[192:195], v[136:139], 0\n"
    "v_exp_f32 v41, v41\n"
    "v_mfma_f32_16x16x32_bf16 v[120:123], v[192:195], v[152:155], v[120:123]\n"
    "s_waitcnt lgkmcnt(4)\n"
    "buffer_load_dwordx4 v204, s[32:35], s78 offen offset:2048 nt lds\n"
    "v_mfma_f32_16x16x32_bf16 a[56:59], v[168:171], v[136:139], 0\n"
    "ds_read_b128 v[160:163], v201 offset:36864\n"
    "v_mfma_f32_16x16x32_bf16 a[56:59], v[172:175], v[152:155], a[56:59]\n"
    "s_waitcnt lgkmcnt(3)\n"
    "ds_read_b128 v[164:167], v201 offset:37888\n"
    "v_mfma_f32_16x16x32_bf16 a[72:75], v[176:179], v[136:139], 0\n"
    "ds_read_b128 v[168:171], v201 offset:38912\n"
    "v_mfma_f32_16x16x32_bf16 a[72:75], v[180:183], v[152:155], a[72:75]\n"
    "s_waitcnt lgkmcnt(3)\n"
    "ds_read_b128 v[172:175], v201 offset:39936\n"
    "v_mfma_f32_16x16x32_bf16 a[88:91], v[184:187], v[136:139], 0\n"
    "ds_read_b128 v[176:179], v201 offset:40960\n"
    "v_mfma_f32_16x16x32_bf16 a[88:91], v[188:191], v[152:155], a[88:91]\n"
    "s_waitcnt lgkmcnt(3)\n"
    "ds_read_b128 v[180:183], v201 offset:41984\n"
    "v_mfma_f32_16x16x32_bf16 a[104:107], v[160:163], v[136:139], 0\n"
    "ds_read_b128 v[184:187], v201 offset:43008\n"
    "v_mfma_f32_16x16x32_bf16 a[104:107], v[164:167], v[152:155], a[104:107]\n"
    "s_waitcnt lgkmcnt(3)\n"
    "ds_read_b128 v[188:191], v201 offset:44032\n"
    "v_mfma_f32_16x16x32_bf16 a[120:123], v[168:171], v[136:139], 0\n"
    "ds_read_b128 v[160:163], v201 offset:45056\n"
    "v_mfma_f32_16x16x32_bf16 a[120:123], v[172:175], v[152:155], a[120:123]\n"
    "s_waitcnt lgkmcnt(3)\n"
    "ds_read_b128 v[164:167], v201 offset:46080\n"
    "v_mfma_f32_16x16x32_bf16 a[136:139], v[176:179], v[136:139], 0\n"
    "ds_read_b128 v[168:171], v201 offset:47104\n"
    "v_mfma_f32_16x16x32_bf16 a[136:139], v[180:183], v[152:155], a[136:139]\n"
    "s_waitcnt lgkmcnt(3)\n"
    "ds_read_b128 v[172:175], v201 offset:48128\n"
    "v_mfma_f32_16x16x32_bf16 a[152:155], v[184:187], v[136:139], 0\n"
    "v_exp_f32 v42, v42\n"
    "v_mfma_f32_16x16x32_bf16 a[152:155], v[188:191], v[152:155], a[152:155]\n"
    "s_waitcnt lgkmcnt(2)\n"
    "v_exp_f32 v43, v43\n"
    "v_mfma_f32_16x16x32_bf16 a[168:171], v[160:163], v[136:139], 0\n"
    "v_exp_f32 v56, v56\n"
    "v_mfma_f32_16x16x32_bf16 a[168:171], v[164:167], v[152:155], a[168:171]\n"
    "s_waitcnt lgkmcnt(0)\n"
    "v_exp_f32 v57, v57\n"
    "v_mfma_f32_16x16x32_bf16 a[184:187], v[168:171], v[136:139], 0\n"
    "v_exp_f32 v58, v58\n"
    "v_exp_f32 v59, v59\n"
    "v_mfma_f32_16x16x32_bf16 a[184:187], v[172:175], v[152:155], a[184:187]\n"
    "v_add_f32 v121, v121, v40\n"
    "v_add_f32 v122, v122, v41\n"
    "v_add_f32 v121, v121, v42\n"
    "v_add_f32 v122, v122, v43\n"
    "v_add_f32 v121, v121, v56\n"
    "v_add_f32 v122, v122, v57\n"
    "s_waitcnt vmcnt(12)\n"
    "ds_read_b128 v[8:11], v158 offset:24576\n"
    "ds_read_b128 v[12:15], v159 offset:24576\n"
    "ds_read_b128 v[16:19], v158 offset:24704\n"
    "ds_read_b128 v[20:23], v159 offset:24704\n"
    "ds_read_b128 v[24:27], v158 offset:24832\n"
    "ds_read_b128 v[28:31], v159 offset:24832\n"
    "s_waitcnt lgkmcnt(0)\n"
    "ds_read_b128 v[160:163], v255\n"
    "ds_read_b128 v[164:167], v255 offset:1024\n"
    "ds_read_b128 v[168:171], v255 offset:2048\n"
    "ds_read_b128 v[172:175], v255 offset:3072\n"
    "ds_read_b128 v[176:179], v255 offset:4096\n"
    "ds_read_b128 v[180:183], v255 offset:5120\n"
    "s_waitcnt lgkmcnt(4)\n"
    "v_mfma_f32_16x16x32_bf16 v[64:67], v[160:163], v[8:11], 0\n"
    "ds_read_b128 v[184:187], v255 offset:6144\n"
    "v_mfma_f32_16x16x32_bf16 v[64:67], v[164:167], v[12:15], v[64:67]\n"
    "s_waitcnt lgkmcnt(3)\n"
    "s_add_u32 m0, s71, 0x6000\n"
    "ds_read_b128 v[188:191], v255 offset:7168\n"
    "v_mfma_f32_16x16x32_bf16 v[64:67], v[168:171], v[16:19], v[64:67]\n"
    "ds_read_b128 v[160:163], v255 offset:8192\n"
    "buffer_load_dwordx4 v200, s[36:39], s60 offen sc1 lds\n"
    "v_mfma_f32_16x16x32_bf16 v[64:67], v[172:175], v[20:23], v[64:67]\n"
    "s_waitcnt lgkmcnt(3)\n"
    "ds_read_b128 v[164:167], v255 offset:9216\n"
    "v_mfma_f32_16x16x32_bf16 v[64:67], v[176:179], v[24:27], v[64:67]\n"
    "ds_read_b128 v[168:171], v255 offset:10240\n"
    "v_mfma_f32_16x16x32_bf16 v[64:67], v[180:183], v[28:31], v[64:67]\n"
    "s_waitcnt lgkmcnt(3)\n"
    "ds_read_b128 v[172:175], v255 offset:11264\n"
    "buffer_load_dwordx4 v200, s[36:39], s60 offen offset:1024 sc1 lds\n"
    "v_mfma_f32_16x16x32_bf16 v[80:83], v[184:187], v[8:11], 0\n"
    "ds_read_b128 v[176:179], v200\n"
    "v_mfma_f32_16x16x32_bf16 v[80:83], v[188:191], v[12:15], v[80:83]\n"
    "s_waitcnt lgkmcnt(3)\n"
    "ds_read_b128 v[180:183], v200 offset:1024\n"
    "v_mfma_f32_16x16x32_bf16 v[80:83], v[160:163], v[16:19], v[80:83]\n"
    "ds_read_b128 v[184:187], v200 offset:2048\n"
    "buffer_load_dwordx4 v200, s[36:39], s60 offen offset:2048 sc1 lds\n"
    "v_mfma_f32_16x16x32_bf16 v[80:83], v[164:167], v[20:23], v[80:83]\n"
    "s_waitcnt lgkmcnt(3)\n"
    "ds_read_b128 v[188:191], v200 offset:3072\n"
    "v_mfma_f32_16x16x32_bf16 v[80:83], v[168:171], v[24:27], v[80:83]\n"
    "ds_read_b128 v[160:163], v200 offset:4096\n"
    "v_mfma_f32_16x16x32_bf16 v[80:83], v[172:175], v[28:31], v[80:83]\n"
    "s_waitcnt lgkmcnt(3)\n"
    "ds_read_b128 v[164:167], v200 offset:5120\n"
    "buffer_load_dwordx4 v200, s[36:39], s60 offen offset:3072 sc1 lds\n"
    "v_mfma_f32_16x16x32_bf16 v[76:79], v[176:179], v[8:11], 0\n"
    "s_add_u32 s65, s60, 0x1000\n"
    "ds_read_b128 v[168:171], v200 offset:6144\n"
    "v_mfma_f32_16x16x32_bf16 v[76:79], v[180:183], v[12:15], v[76:79]\n"
    "s_waitcnt lgkmcnt(3)\n"
    "s_add_u32 m0, s71, 0x7000\n"
    "ds_read_b128 v[172:175], v200 offset:7168\n"
    "v_mfma_f32_16x16x32_bf16 v[76:79], v[184:187], v[16:19], v[76:79]\n"
    "ds_read_b128 v[176:179], v200 offset:8192\n"
    "buffer_load_dwordx4 v200, s[36:39], s65 offen sc1 lds\n"
    "v_mfma_f32_16x16x32_bf16 v[76:79], v[188:191], v[20:23], v[76:79]\n"
    "s_waitcnt lgkmcnt(3)\n"
    "ds_read_b128 v[180:183], v200 offset:9216\n"
    "v_mfma_f32_16x16x32_bf16 v[76:79], v[160:163], v[24:27], v[76:79]\n"
    "ds_read_b128 v[184:187], v200 offset:10240\n"
    "v_mfma_f32_16x16x32_bf16 v[76:79], v[164:167], v[28:31], v[76:79]\n"
    "s_waitcnt lgkmcnt(3)\n"
    "ds_read_b128 v[188:191], v200 offset:11264\n"
    "buffer_load_dwordx4 v200, s[36:39], s65 offen offset:1024 sc1 lds\n"
    "v_max3_f32 v196, v64, v65, v66\n"
    "v_mfma_f32_16x16x32_bf16 v[92:95], v[168:171], v[8:11], 0\n"
    "ds_read_b128 v[160:163], v200 offset:12288\n"
    "v_max3_f32 v196, v196, v67, v80\n"
    "v_max3_f32 v196, v196, v81, v82\n"
    "v_max_f32 v196, v196, v83\n"
    "v_mfma_f32_16x16x32_bf16 v[92:95], v[172:175], v[12:15], v[92:95]\n"
    "s_waitcnt lgkmcnt(3)\n"
    "ds_read_b128 v[164:167], v200 offset:13312\n"
    "v_mov_b32 v197, v196\n"
    "s_nop 1\n"
    "v_permlane32_swap_b32 v196, v197\n"
    "v_max_f32 v196, v196, v197\n"
    "v_mfma_f32_16x16x32_bf16 v[92:95], v[176:179], v[16:19], v[92:95]\n"
    "ds_read_b128 v[168:171], v200 offset:14336\n"
    "v_mov_b32 v197, v196\n"
    "s_nop 1\n"
    "v_permlane16_swap_b32 v196, v197\n"
    "v_max_f32 v196, v196, v197\n"
    "v_mfma_f32_16x16x32_bf16 v[92:95], v[180:183], v[20:23], v[92:95]\n"
    "s_waitcnt lgkmcnt(3)\n"
    "ds_read_b128 v[172:175], v200 offset:15360\n"
    "v_add_f32 v196, 0x42640000, v196\n"
    "v_sub_f32 v108, 0, v196\n"
    "v_sub_f32 v109, 0, v196\n"
    "v_mfma_f32_16x16x32_bf16 v[92:95], v[184:187], v[24:27], v[92:95]\n"
    "ds_read_b128 v[176:179], v200 offset:16384\n"
    "v_sub_f32 v110, 0, v196\n"
    "v_sub_f32 v111, 0, v196\n"
    "v_add_f32 v64, v64, v108\n"
    "v_mfma_f32_16x16x32_bf16 v[92:95], v[188:191], v[28:31], v[92:95]\n"
    "s_waitcnt lgkmcnt(3)\n"
    "ds_read_b128 v[180:183], v200 offset:17408\n"
    "v_add_f32 v65, v65, v108\n"
    "v_add_f32 v66, v66, v108\n"
    "v_add_f32 v67, v67, v108\n"
    "v_mfma_f32_16x16x32_bf16 v[44:47], v[160:163], v[8:11], v[108:111]\n"
    "ds_read_b128 v[184:187], v200 offset:18432\n"
    "v_add_f32 v80, v80, v108\n"
    "v_add_f32 v81, v81, v108\n"
    "v_add_f32 v82, v82, v108\n"
    "v_mfma_f32_16x16x32_bf16 v[44:47], v[164:167], v[12:15], v[44:47]\n"
    "s_waitcnt lgkmcnt(3)\n"
    "ds_read_b128 v[188:191], v200 offset:19456\n"
    "v_add_f32 v83, v83, v108\n"
    "v_exp_f32 v64, v64\n"
    "v_mfma_f32_16x16x32_bf16 v[44:47], v[168:171], v[16:19], v[44:47]\n"
    "ds_read_b128 v[160:163], v200 offset:20480\n"
    "buffer_load_dwordx2 v[218:219], v213, s[44:47], 0 offen sc1\n"
    "v_mfma_f32_16x16x32_bf16 v[44:47], v[172:175], v[20:23], v[44:47]\n"
    "s_waitcnt lgkmcnt(3)\n"
    "ds_read_b128 v[164:167], v200 offset:21504\n"
    "v_exp_f32 v65, v65\n"
    "v_mfma_f32_16x16x32_bf16 v[44:47], v[176:179], v[24:27], v[44:47]\n"
    "ds_read_b128 v[168:171], v200 offset:22528\n"
    "v_exp_f32 v66, v66\n"
    "v_mfma_f32_16x16x32_bf16 v[44:47], v[180:183], v[28:31], v[44:47]\n"
    "s_waitcnt lgkmcnt(3)\n"
    "ds_read_b128 v[172:175], v200 offset:23552\n"
    "v_exp_f32 v67, v67\n"
    "v_mfma_f32_16x16x32_bf16 v[60:63], v[184:187], v[8:11], v[108:111]\n"
    "ds_read_b128 v[176:179], v201 offset:24576\n"
    "v_exp_f32 v80, v80\n"
    "v_mfma_f32_16x16x32_bf16 v[60:63], v[188:191], v[12:15], v[60:63]\n"
    "s_waitcnt lgkmcnt(3)\n"
    "ds_read_b128 v[180:183], v201 offset:25600\n"
    "v_exp_f32 v81, v81\n"
    "v_mfma_f32_16x16x32_bf16 v[60:63], v[160:163], v[16:19], v[60:63]\n"
    "ds_read_b128 v[184:187], v201 offset:26624\n"
    "v_exp_f32 v82, v82\n"
    "v_mfma_f32_16x16x32_bf16 v[60:63], v[164:167], v[20:23], v[60:63]\n"
    "s_waitcnt lgkmcnt(3)\n"
    "ds_read_b128 v[188:191], v201 offset:27648\n"
    "v_exp_f32 v83, v83\n"
    "v_cvt_pk_bf16_f32 v140, v64, v65\n"
    "v_mfma_f32_16x16x32_bf16 v[60:63], v[168:171], v[24:27], v[60:63]\n"
    "ds_read_b128 v[160:163], v201 offset:28672\n"
    "v_cvt_pk_bf16_f32 v141, v66, v67\n"
    "v_cvt_pk_bf16_f32 v142, v80, v81\n"
    "v_cvt_pk_bf16_f32 v143, v82, v83\n"
    "v_add_f32 v76, v76, v108\n"
    "v_add_f32 v77, v77, v108\n"
    "v_add_f32 v78, v78, v108\n"
    "v_add_f32 v79, v79, v108\n"
    "v_add_f32 v92, v92, v108\n"
    "v_add_f32 v93, v93, v108\n"
    "v_add_f32 v94, v94, v108\n"
    "v_add_f32 v95, v95, v108\n"
    "v_exp_f32 v76, v76\n"
    "v_exp_f32 v77, v77\n"
    "v_exp_f32 v78, v78\n"
    "v_exp_f32 v79, v79\n"
    "v_exp_f32 v92, v92\n"
    "v_exp_f32 v93, v93\n"
    "v_exp_f32 v94, v94\n"
    "v_exp_f32 v95, v95\n"
    "v_cvt_pk_bf16_f32 v156, v76, v77\n"
    "v_cvt_pk_bf16_f32 v157, v78, v79\n"
    "v_cvt_pk_bf16_f32 v158, v92, v93\n"
    "v_cvt_pk_bf16_f32 v159, v94, v95\n"
    "v_mfma_f32_16x16x32_bf16 v[60:63], v[172:175], v[28:31], v[60:63]\n"
    "s_waitcnt lgkmcnt(3)\n"
    "ds_read_b128 v[164:167], v201 offset:29696\n"
    "v_exp_f32 v44, v44\n"
    "v_mfma_f32_16x16x32_bf16 a[12:15], v[176:179], v[140:143], 0\n"
    "ds_read_b128 v[168:171], v201 offset:30720\n"
    "v_mfma_f32_16x16x32_bf16 a[12:15], v[180:183], v[156:159], a[12:15]\n"
    "s_waitcnt lgkmcnt(3)\n"
    "ds_read_b128 v[172:175], v201 offset:31744\n"
    "v_mfma_f32_16x16x32_bf16 a[28:31], v[184:187], v[140:143], 0\n"
    "ds_read_b128 v[176:179], v201 offset:32768\n"
    "v_mfma_f32_16x16x32_bf16 a[28:31], v[188:191], v[156:159], a[28:31]\n"
    "s_waitcnt lgkmcnt(3)\n"
    "ds_read_b128 v[180:183], v201 offset:33792\n"
    "v_mfma_f32_16x16x32_bf16 a[44:47], v[160:163], v[140:143], 0\n"
    "ds_read_b128 v[184:187], v201 offset:34816\n"
    "v_mfma_f32_16x16x32_bf16 a[44:47], v[164:167], v[156:159], a[44:47]\n"
    "ds_read_b128 v[188:191], v201 offset:35840\n"
    "v_mfma_f32_16x16x32_bf16 v[124:127], v[192:195], v[140:143], 0\n"
    "v_exp_f32 v45, v45\n"
    "v_mfma_f32_16x16x32_bf16 v[124:127], v[192:195], v[156:159], v[124:127]\n"
    "s_waitcnt lgkmcnt(4)\n"
    "v_exp_f32 v46, v46\n"
    "v_mfma_f32_16x16x32_bf16 a[60:63], v[168:171], v[140:143], 0\n"
    "ds_read_b128 v[160:163], v201 offset:36864\n"
    "v_mfma_f32_16x16x32_bf16 a[60:63], v[172:175], v[156:159], a[60:63]\n"
    "s_waitcnt lgkmcnt(3)\n"
    "ds_read_b128 v[164:167], v201 offset:37888\n"
    "v_mfma_f32_16x16x32_bf16 a[76:79], v[176:179], v[140:143], 0\n"
    "ds_read_b128 v[168:171], v201 offset:38912\n"
    "v_mfma_f32_16x16x32_bf16 a[76:79], v[180:183], v[156:159], a[76:79]\n"
    "s_waitcnt lgkmcnt(3)\n"
    "ds_read_b128 v[172:175], v201 offset:39936\n"
    "v_mfma_f32_16x16x32_bf16 a[92:95], v[184:187], v[140:143], 0\n"
    "ds_read_b128 v[176:179], v201 offset:40960\n"
    "v_mfma_f32_16x16x32_bf16 a[92:95], v[188:191], v[156:159], a[92:95]\n"
    "s_waitcnt lgkmcnt(3)\n"
    "ds_read_b128 v[180:183], v201 offset:41984\n"
    "v_mfma_f32_16x16x32_bf16 a[108:111], v[160:163], v[140:143], 0\n"
    "ds_read_b128 v[184:187], v201 offset:43008\n"
    "v_mfma_f32_16x16x32_bf16 a[108:111], v[164:167], v[156:159], a[108:111]\n"
    "s_waitcnt lgkmcnt(3)\n"
    "ds_read_b128 v[188:191], v201 offset:44032\n"
    "v_mfma_f32_16x16x32_bf16 a[124:127], v[168:171], v[140:143], 0\n"
    "ds_read_b128 v[160:163], v201 offset:45056\n"
    "v_mfma_f32_16x16x32_bf16 a[124:127], v[172:175], v[156:159], a[124:127]\n"
    "s_waitcnt lgkmcnt(3)\n"
    "ds_read_b128 v[164:167], v201 offset:46080\n"
    "v_mfma_f32_16x16x32_bf16 a[140:143], v[176:179], v[140:143], 0\n"
    "ds_read_b128 v[168:171], v201 offset:47104\n"
    "v_mfma_f32_16x16x32_bf16 a[140:143], v[180:183], v[156:159], a[140:143]\n"
    "s_waitcnt lgkmcnt(3)\n"
    "ds_read_b128 v[172:175], v201 offset:48128\n"
    "v_mfma_f32_16x16x32_bf16 a[156:159], v[184:187], v[140:143], 0\n"
    "v_exp_f32 v47, v47\n"
    "v_mfma_f32_16x16x32_bf16 a[156:159], v[188:191], v[156:159], a[156:159]\n"
    "s_waitcnt lgkmcnt(2)\n"
    "v_exp_f32 v60, v60\n"
    "v_mfma_f32_16x16x32_bf16 a[172:175], v[160:163], v[140:143], 0\n"
    "v_exp_f32 v61, v61\n"
    "v_mfma_f32_16x16x32_bf16 a[172:175], v[164:167], v[156:159], a[172:175]\n"
    "s_waitcnt lgkmcnt(0)\n"
    "v_exp_f32 v62, v62\n"
    "v_mfma_f32_16x16x32_bf16 a[188:191], v[168:171], v[140:143], 0\n"
    "v_exp_f32 v63, v63\n"
    "v_mfma_f32_16x16x32_bf16 a[188:191], v[172:175], v[156:159], a[188:191]\n"
    "v_add_f32 v125, v125, v44\n"
    "v_add_f32 v126, v126, v45\n"
    "v_add_f32 v125, v125, v46\n"
    "v_add_f32 v126, v126, v47\n"
    "s_add_u32 s64, s64, 0x3c000\n"
    "s_add_u32 s62, s62, 0x6000\n"
    "s_add_u32 s63, s63, 0x6000\n"
    "s_add_u32 s60, s60, 0x6000\n"
    "s_add_u32 s61, s61, 0x6000\n"
    "v_add_u32 v209, 0x300, v209\n"
    "v_add_u32 v210, 0xc0, v210\n"
    "v_mov_b32 v220, 5\n"
    "s_waitcnt vmcnt(0)\n"
    "v_cmp_ne_u32 s[80:81], s67, v219\n"
    "v_cmp_gt_u32 vcc, s66, v218\n"
    "s_or_b64 vcc, vcc, s[80:81]\n"
    "s_cbranch_vccnz LSPRE2\n"
    "LSPB2:\n"
    "s_barrier\n"
    "s_cmp_eq_u32 s58, 0\n"
    "s_cbranch_scc1 LENT0\n"
    "LENT1:\n"
    "s_mov_b32 s59, 1\n"
    "s_add_u32 m0, s72, 0x21000\n"
    "s_nop 0\n"
    "buffer_load_dwordx4 v250, s[48:51], 0 offen offset:2048 lds\n"
    "s_add_u32 m0, s72, 0x18000\n"
    "s_nop 0\n"
    "buffer_load_dwordx4 v202, s[32:35], s64 offen lds\n"
    "buffer_load_dwordx4 v203, s[32:35], s64 offen offset:1024 lds\n"
    "buffer_load_dwordx4 v204, s[32:35], s64 offen offset:2048 lds\n"
    "s_add_u32 s64, s64, 0xc000\n"
    "s_mov_b32 s94, 0x3000\n"
    "s_mov_b32 s95, 0x6000\n"
    "s_add_u32 s51, s72, 0x18000\n"
    "v_add_u32 v205, 0x3000, v205\n"
    "v_add_u32 v206, 0x3000, v206\n"
    "v_add_u32 v207, 0x3000, v207\n"
    "ds_read_u16_d16_hi v223, v209 offset:6336\n"
    "ds_read_u16_d16_hi v222, v209 offset:192\n"
    "ds_read_b128 v[160:163], v200 offset:24576\n"
    "v_add_f32 v121, v121, v58\n"
    "ds_read_b128 v[232:235], v205 offset:24576\n"
    "v_add_f32 v122, v122, v59\n"
    "ds_read_b128 v[164:167], v200 offset:25600\n"
    "ds_read_b128 v[236:239], v206 offset:24576\n"
    "v_add_f32 v125, v125, v60\n"
    "v_add_f32 v126, v126, v61\n"
    "v_add_f32 v125, v125, v62\n"
    "v_add_f32 v126, v126, v63\n"
    "ds_read_b64 v[228:229], v210 offset:64\n"
    "ds_read2st64_b32 v[240:241], v207 offset0:96 offset1:99\n"
    "ds_read_b128 v[168:171], v200 offset:26624\n"
    "ds_read2st64_b32 v[242:243], v207 offset0:102 offset1:105\n"
    "ds_read2st64_b32 v[244:245], v207 offset0:108 offset1:111\n"
    "ds_read2st64_b32 v[246:247], v207 offset0:114 offset1:117\n"
    "v_cvt_pk_bf16_f32 v140, v44, v45\n"
    "v_cvt_pk_bf16_f32 v141, v46, v47\n"
    "s_waitcnt lgkmcnt(6)\n"
    "v_mul_f32 v226, s75, v223\n"
    "ds_read_b128 v[172:175], v200 offset:27648\n"
    "v_fma_f32 v227, -v222, v226, v217\n"
    "v_cvt_f32_i32 v232, v232\n"
    "v_cvt_f32_i32 v233, v233\n"
    "v_cvt_f32_i32 v234, v234\n"
    "v_cvt_f32_i32 v235, v235\n"
    "s_nop 7\n"
    ".p2align 6\n"
    "LLOOP1:\n"
    "v_mfma_f32_16x16x32_bf16 v[64:67], v[160:163], a[192:195], v[96:99]\n"
    "ds_read_b128 v[176:179], v200 offset:28672\n"
    "v_cvt_f32_i32 v236, v236\n"
    "v_mfma_f32_16x16x32_bf16 v[64:67], v[164:167], a[196:199], v[64:67]\n"
    "s_waitcnt lgkmcnt(1)\n"
    "v_cvt_f32_i32 v237, v237\n"
    "v_cvt_f32_i32 v238, v238\n"
    "v_mfma_f32_16x16x32_bf16 v[64:67], v[168:171], a[200:203], v[64:67]\n"
    "v_cvt_f32_i32 v239, v239\n"
    "v_mul_f32_dpp v232, v226, v232 row_newbcast:0 row_mask:0xf bank_mask:0xf\n"
    "v_mfma_f32_16x16x32_bf16 v[68:71], v[168:171], a[224:227], v[100:103]\n"
    "v_mul_f32_dpp v233, v226, v233 row_newbcast:1 row_mask:0xf bank_mask:0xf\n"
    "v_mul_f32_dpp v234, v226, v234 row_newbcast:2 row_mask:0xf bank_mask:0xf\n"
    "v_mfma_f32_16x16x32_bf16 v[68:71], v[164:167], a[220:223], v[68:71]\n"
    "ds_read_b128 v[180:183], v200 offset:29696\n"
    "v_mul_f32_dpp v235, v226, v235 row_newbcast:3 row_mask:0xf bank_mask:0xf\n"
    "v_mfma_f32_16x16x32_bf16 v[68:71], v[160:163], a[216:219], v[68:71]\n"
    "v_mul_f32_dpp v236, v226, v236 row_newbcast:4 row_mask:0xf bank_mask:0xf\n"
    "v_mul_f32_dpp v237, v226, v237 row_newbcast:5 row_mask:0xf bank_mask:0xf\n"
    "v_mfma_f32_16x16x32_bf16 v[72:75], v[160:163], a[240:243], v[104:107]\n"
    "v_mul_f32_dpp v238, v226, v238 row_newbcast:6 row_mask:0xf bank_mask:0xf\n"
    "v_mul_f32_dpp v239, v226, v239 row_newbcast:7 row_mask:0xf bank_mask:0xf\n"
    "v_mfma_f32_16x16x32_bf16 v[72:75], v[164:167], a[244:247], v[72:75]\n"
    "v_add_f32_dpp v232, v227, v232 row_newbcast:0 row_mask:0xf bank_mask:0xf\n"
    "v_add_f32_dpp v233, v227, v233 row_newbcast:1 row_mask:0xf bank_mask:0xf\n"
    "v_mfma_f32_16x16x32_bf16 v[72:75], v[168:171], a[248:251], v[72:75]\n"
    "v_add_f32_dpp v234, v227, v234 row_newbcast:2 row_mask:0xf bank_mask:0xf\n"
    "v_add_f32_dpp v235, v227, v235 row_newbcast:3 row_mask:0xf bank_mask:0xf\n"
    "v_mfma_f32_16x16x32_bf16 v[76:79], v[168:171], v[16:19], v[108:111]\n"
    "v_add_f32_dpp v236, v227, v236 row_newbcast:4 row_mask:0xf bank_mask:0xf\n"
    "v_add_f32_dpp v237, v227, v237 row_newbcast:5 row_mask:0xf bank_mask:0xf\n"
    "v_mfma_f32_16x16x32_bf16 v[76:79], v[164:167], v[12:15], v[76:79]\n"
    "v_add_f32_dpp v238, v227, v238 row_newbcast:6 row_mask:0xf bank_mask:0xf\n"
    "v_add_f32_dpp v239, v227, v239 row_newbcast:7 row_mask:0xf bank_mask:0xf\n"
    "v_mfma_f32_16x16x32_bf16 v[76:79], v[160:163], v[8:11], v[76:79]\n"
    "s_waitcnt lgkmcnt(1)\n"
    "v_cvt_pk_bf16_f32 v232, v232, v233\n"
    "v_cvt_f32_i32 v240, v240\n"
    "v_mfma_f32_16x16x32_bf16 v[76:79], v[172:175], v[20:23], v[76:79]\n"
    "ds_read_b128 v[160:163], v200 offset:33792\n"
    "v_cvt_pk_bf16_f32 v233, v234, v235\n"
    "v_mfma_f32_16x16x32_bf16 v[76:79], v[176:179], v[24:27], v[76:79]\n"
    "s_waitcnt lgkmcnt(1)\n"
    "v_cvt_f32_i32 v241, v241\n"
    "v_cvt_pk_bf16_f32 v234, v236, v237\n"
    "v_mfma_f32_16x16x32_bf16 v[76:79], v[180:183], v[28:31], v[76:79]\n"
    "v_cvt_f32_i32 v242, v242\n"
    "v_cvt_pk_bf16_f32 v235, v238, v239\n"
    "v_mfma_f32_16x16x32_bf16 v[72:75], v[180:183], v[4:7], v[72:75]\n"
    "ds_read_b128 v[164:167], v200 offset:34816\n"
    "v_cvt_f32_i32 v243, v243\n"
    "v_mfma_f32_16x16x32_bf16 v[72:75], v[176:179], v[0:3], v[72:75]\n"
    "v_cvt_f32_i32 v244, v244\n"
    "v_cvt_f32_i32 v245, v245\n"
    "v_mfma_f32_16x16x32_bf16 v[72:75], v[172:175], a[252:255], v[72:75]\n"
    "buffer_store_dwordx4 v[232:235], v211, s[36:39], s62 offen sc1\n"
    "v_cvt_f32_i32 v246, v246\n"
    "v_cvt_f32_i32 v247, v247\n"
    "v_mul_f32_dpp v240, v228, v240 row_newbcast:0 row_mask:0xf bank_mask:0xf\n"
    "v_mfma_f32_16x16x32_bf16 v[68:71], v[172:175], a[228:231], v[68:71]\n"
    "ds_read_b128 v[168:171], v200 offset:35840\n"
    "v_mul_f32_dpp v241, v228, v241 row_newbcast:1 row_mask:0xf bank_mask:0xf\n"
    "v_mfma_f32_16x16x32_bf16 v[68:71], v[176:179], a[232:235], v[68:71]\n"
    "v_mul_f32_dpp v242, v228, v242 row_newbcast:2 row_mask:0xf bank_mask:0xf\n"
    "v_mul_f32_dpp v243, v228, v243 row_newbcast:3 row_mask:0xf bank_mask:0xf\n"
    "v_mfma_f32_16x16x32_bf16 v[68:71], v[180:183], a[236:239], v[68:71]\n"
    "v_mul_f32_dpp v244, v228, v244 row_newbcast:4 row_mask:0xf bank_mask:0xf\n"
    "v_mul_f32_dpp v245, v228, v245 row_newbcast:5 row_mask:0xf bank_mask:0xf\n"
    "v_mfma_f32_16x16x32_bf16 v[64:67], v[180:183], a[212:215], v[64:67]\n"
    "v_mul_f32_dpp v246, v228, v246 row_newbcast:6 row_mask:0xf bank_mask:0xf\n"
    "v_mul_f32_dpp v247, v228, v247 row_newbcast:7 row_mask:0xf bank_mask:0xf\n"
    "v_mfma_f32_16x16x32_bf16 v[64:67], v[176:179], a[208:211], v[64:67]\n"
    "v_add_f32_dpp v240, v229, v240 row_newbcast:0 row_mask:0xf bank_mask:0xf\n"
    "v_add_f32_dpp v241, v229, v241 row_newbcast:1 row_mask:0xf bank_mask:0xf\n"
    "v_mfma_f32_16x16x32_bf16 v[64:67], v[172:175], a[204:207], v[64:67]\n"
    "s_waitcnt lgkmcnt(1)\n"
    "v_add_f32_dpp v242, v229, v242 row_newbcast:2 row_mask:0xf bank_mask:0xf\n"
    "v_add_f32_dpp v243, v229, v243 row_newbcast:3 row_mask:0xf bank_mask:0xf\n"
    "v_mfma_f32_16x16x32_bf16 v[80:83], v[160:163], a[204:207], v[96:99]\n"
    "ds_read_b128 v[172:175], v200 offset:30720\n"
    "v_add_f32_dpp v244, v229, v244 row_newbcast:4 row_mask:0xf bank_mask:0xf\n"
    "v_mfma_f32_16x16x32_bf16 v[80:83], v[164:167], a[208:211], v[80:83]\n"
    "s_waitcnt lgkmcnt(1)\n"
    "v_add_f32_dpp v245, v229, v245 row_newbcast:5 row_mask:0xf bank_mask:0xf\n"
    "v_add_f32_dpp v246, v229, v246 row_newbcast:6 row_mask:0xf bank_mask:0xf\n"
    "v_mfma_f32_16x16x32_bf16 v[80:83], v[168:171], a[212:215], v[80:83]\n"
    "v_add_f32_dpp v247, v229, v247 row_newbcast:7 row_mask:0xf bank_mask:0xf\n"
    "v_cvt_pk_bf16_f32 v240, v240, v241\n"
    "v_mfma_f32_16x16x32_bf16 v[84:87], v[168:171], a[236:239], v[100:103]\n"
    "ds_read_b128 v[176:179], v200 offset:31744\n"
    "v_cvt_pk_bf16_f32 v241, v242, v243\n"
    "v_mfma_f32_16x16x32_bf16 v[84:87], v[164:167], a[232:235], v[84:87]\n"
    "v_cvt_pk_bf16_f32 v242, v244, v245\n"
    "v_cvt_pk_bf16_f32 v243, v246, v247\n"
    "v_mfma_f32_16x16x32_bf16 v[84:87], v[160:163], a[228:231], v[84:87]\n"
    "buffer_store_dwordx4 v[240:243], v212, s[40:43], s63 offen sc1\n"
    "v_cvt_pk_bf16_f32 v136, v40, v41\n"
    "v_cvt_pk_bf16_f32 v137, v42, v43\n"
    "v_cvt_pk_bf16_f32 v132, v36, v37\n"
    "v_mfma_f32_16x16x32_bf16 v[88:91], v[160:163], a[252:255], v[104:107]\n"
    "ds_read_b128 v[180:183], v200 offset:32768\n"
    "v_cvt_pk_bf16_f32 v133, v38, v39\n"
    "v_mfma_f32_16x16x32_bf16 v[88:91], v[164:167], v[0:3], v[88:91]\n"
    "v_cvt_pk_bf16_f32 v128, v32, v33\n"
    "v_cvt_pk_bf16_f32 v129, v34, v35\n"
    "v_mfma_f32_16x16x32_bf16 v[88:91], v[168:171], v[4:7], v[88:91]\n"
    "v_exp_f32 v64, v64\n"
    "v_mfma_f32_16x16x32_bf16 v[92:95], v[168:171], v[28:31], v[108:111]\n"
    "v_exp_f32 v65, v65\n"
    "v_mfma_f32_16x16x32_bf16 v[92:95], v[164:167], v[24:27], v[92:95]\n"
    "v_exp_f32 v66, v66\n"
    "v_mfma_f32_16x16x32_bf16 v[92:95], v[160:163], v[20:23], v[92:95]\n"
    "s_waitcnt lgkmcnt(1)\n"
    "v_exp_f32 v67, v67\n"
    "v_mfma_f32_16x16x32_bf16 v[92:95], v[172:175], v[8:11], v[92:95]\n"
    "ds_read_b128 v[160:163], v200 offset:36864\n"
    "v_cvt_pk_bf16_f32 v142, v60, v61\n"
    "v_mfma_f32_16x16x32_bf16 v[92:95], v[176:179], v[12:15], v[92:95]\n"
    "s_waitcnt lgkmcnt(1)\n"
    "s_add_u32 s65, s60, s92\n"
    "v_cvt_pk_bf16_f32 v143, v62, v63\n"
    "v_mfma_f32_16x16x32_bf16 v[92:95], v[180:183], v[16:19], v[92:95]\n"
    "s_add_u32 m0, s90, 0\n"
    "v_exp_f32 v68, v68\n"
    "v_mfma_f32_16x16x32_bf16 v[88:91], v[180:183], a[248:251], v[88:91]\n"
    "ds_read_b128 v[164:167], v200 offset:37888\n"
    "v_cvt_pk_bf16_f32 v138, v56, v57\n"
    "v_mfma_f32_16x16x32_bf16 v[88:91], v[176:179], a[244:247], v[88:91]\n"
    "buffer_load_dwordx4 v200, s[36:39], s65 offen sc1 lds\n"
    "v_mfma_f32_16x16x32_bf16 v[88:91], v[172:175], a[240:243], v[88:91]\n"
    "v_exp_f32 v69, v69\n"
    "v_mfma_f32_16x16x32_bf16 v[84:87], v[172:175], a[216:219], v[84:87]\n"
    "ds_read_b128 v[168:171], v200 offset:38912\n"
    "v_cvt_pk_bf16_f32 v139, v58, v59\n"
    "v_mfma_f32_16x16x32_bf16 v[84:87], v[176:179], a[220:223], v[84:87]\n"
    "v_exp_f32 v70, v70\n"
    "v_mfma_f32_16x16x32_bf16 v[84:87], v[180:183], a[224:227], v[84:87]\n"
    "buffer_load_dwordx4 v200, s[36:39], s65 offen offset:1024 sc1 lds\n"
    "v_mfma_f32_16x16x32_bf16 v[80:83], v[180:183], a[200:203], v[80:83]\n"
    "v_exp_f32 v71, v71\n"
    "v_mfma_f32_16x16x32_bf16 v[80:83], v[176:179], a[196:199], v[80:83]\n"
    "v_exp_f32 v72, v72\n"
    "v_mfma_f32_16x16x32_bf16 v[80:83], v[172:175], a[192:195], v[80:83]\n"
    "s_waitcnt lgkmcnt(1)\n"
    "v_cvt_pk_bf16_f32 v134, v52, v53\n"
    "v_cvt_pk_bf16_f32 v135, v54, v55\n"
    "v_mfma_f32_16x16x32_bf16 v[32:35], v[160:163], a[192:195], v[96:99]\n"
    "ds_read_b128 v[172:175], v200 offset:39936\n"
    "v_cvt_pk_bf16_f32 v130, v48, v49\n"
    "v_mfma_f32_16x16x32_bf16 v[32:35], v[164:167], a[196:199], v[32:35]\n"
    "s_waitcnt lgkmcnt(1)\n"
    "buffer_load_dwordx4 v200, s[36:39], s65 offen offset:2048 sc1 lds\n"
    "v_mfma_f32_16x16x32_bf16 v[32:35], v[168:171], a[200:203], v[32:35]\n"
    "v_exp_f32 v73, v73\n"
    "v_mfma_f32_16x16x32_bf16 v[36:39], v[168:171], a[224:227], v[100:103]\n"
    "ds_read_b128 v[176:179], v200 offset:40960\n"
    "v_cvt_pk_bf16_f32 v131, v50, v51\n"
    "v_mfma_f32_16x16x32_bf16 v[36:39], v[164:167], a[220:223], v[36:39]\n"
    "v_exp_f32 v74, v74\n"
    "v_mfma_f32_16x16x32_bf16 v[36:39], v[160:163], a[216:219], v[36:39]\n"
    "buffer_load_dwordx4 v200, s[36:39], s65 offen offset:3072 sc1 lds\n"
    "v_mfma_f32_16x16x32_bf16 v[40:43], v[160:163], a[240:243], v[104:107]\n"
    "s_add_u32 s65, s65, 0x1000\n"
    "ds_read_b128 v[180:183], v200 offset:41984\n"
    "v_cvt_pk_bf16_f32 v144, v64, v65\n"
    "v_mfma_f32_16x16x32_bf16 v[40:43], v[164:167], a[244:247], v[40:43]\n"
    "s_add_u32 m0, s90, 0x1000\n"
    "v_exp_f32 v75, v75\n"
    "v_mfma_f32_16x16x32_bf16 v[40:43], v[168:171], a[248:251], v[40:43]\n"
    "v_exp_f32 v76, v76\n"
    "v_mfma_f32_16x16x32_bf16 v[44:47], v[168:171], v[16:19], v[108:111]\n"
    "buffer_load_dwordx4 v200, s[36:39], s65 offen sc1 lds\n"
    "v_mfma_f32_16x16x32_bf16 v[44:47], v[164:167], v[12:15], v[44:47]\n"
    "v_exp_f32 v77, v77\n"
    "v_mfma_f32_16x16x32_bf16 v[44:47], v[160:163], v[8:11], v[44:47]\n"
    "s_waitcnt lgkmcnt(1)\n"
    "v_exp_f32 v78, v78\n"
    "v_mfma_f32_16x16x32_bf16 v[44:47], v[172:175], v[20:23], v[44:47]\n"
    "ds_read_b128 v[160:163], v200 offset:46080\n"
    "v_cvt_pk_bf16_f32 v145, v66, v67\n"
    "v_mfma_f32_16x16x32_bf16 v[44:47], v[176:179], v[24:27], v[44:47]\n"
    "s_waitcnt lgkmcnt(1)\n"
    "buffer_load_dwordx4 v200, s[36:39], s65 offen offset:1024 sc1 lds\n"
    "v_mfma_f32_16x16x32_bf16 v[44:47], v[180:183], v[28:31], v[44:47]\n"
    "v_exp_f32 v79, v79\n"
    "v_mfma_f32_16x16x32_bf16 v[40:43], v[180:183], v[4:7], v[40:43]\n"
    "ds_read_b128 v[164:167], v200 offset:47104\n"
    "v_cvt_pk_bf16_f32 v148, v68, v69\n"
    "v_mfma_f32_16x16x32_bf16 v[40:43], v[176:179], v[0:3], v[40:43]\n"
    "v_exp_f32 v80, v80\n"
    "v_mfma_f32_16x16x32_bf16 v[40:43], v[172:175], a[252:255], v[40:43]\n"
    "buffer_load_dwordx4 v200, s[36:39], s65 offen offset:2048 sc1 lds\n"
    "v_mfma_f32_16x16x32_bf16 v[36:39], v[172:175], a[228:231], v[36:39]\n"
    "ds_read_b128 v[168:171], v200 offset:48128\n"
    "v_cvt_pk_bf16_f32 v149, v70, v71\n"
    "v_mfma_f32_16x16x32_bf16 v[36:39], v[176:179], a[232:235], v[36:39]\n"
    "s_add_u32 m0, s71, 0x12000\n"
    "v_exp_f32 v81, v81\n"
    "v_mfma_f32_16x16x32_bf16 v[36:39], v[180:183], a[236:239], v[36:39]\n"
    "v_exp_f32 v82, v82\n"
    "v_mfma_f32_16x16x32_bf16 v[32:35], v[180:183], a[212:215], v[32:35]\n"
    "buffer_load_dwordx4 v200, s[40:43], s61 offen sc1 lds\n"
    "v_mfma_f32_16x16x32_bf16 v[32:35], v[176:179], a[208:211], v[32:35]\n"
    "v_exp_f32 v83, v83\n"
    "v_mfma_f32_16x16x32_bf16 v[32:35], v[172:175], a[204:207], v[32:35]\n"
    "s_waitcnt lgkmcnt(1)\n"
    "v_exp_f32 v84, v84\n"
    "v_mfma_f32_16x16x32_bf16 v[48:51], v[160:163], a[204:207], v[96:99]\n"
    "ds_read_b128 v[172:175], v200 offset:43008\n"
    "v_cvt_pk_bf16_f32 v152, v72, v73\n"
    "v_mfma_f32_16x16x32_bf16 v[48:51], v[164:167], a[208:211], v[48:51]\n"
    "s_waitcnt lgkmcnt(1)\n"
    "buffer_load_dwordx4 v200, s[40:43], s61 offen offset:1024 sc1 lds\n"
    "v_mfma_f32_16x16x32_bf16 v[48:51], v[168:171], a[212:215], v[48:51]\n"
    "v_exp_f32 v85, v85\n"
    "v_mfma_f32_16x16x32_bf16 v[52:55], v[168:171], a[236:239], v[100:103]\n"
    "ds_read_b128 v[176:179], v200 offset:44032\n"
    "v_cvt_pk_bf16_f32 v153, v74, v75\n"
    "v_mfma_f32_16x16x32_bf16 v[52:55], v[164:167], a[232:235], v[52:55]\n"
    "v_exp_f32 v86, v86\n"
    "v_mfma_f32_16x16x32_bf16 v[52:55], v[160:163], a[228:231], v[52:55]\n"
    "buffer_load_dwordx4 v200, s[40:43], s61 offen offset:2048 sc1 lds\n"
    "v_mfma_f32_16x16x32_bf16 v[56:59], v[160:163], a[252:255], v[104:107]\n"
    "ds_read_b128 v[180:183], v200 offset:45056\n"
    "v_cvt_pk_bf16_f32 v156, v76, v77\n"
    "v_mfma_f32_16x16x32_bf16 v[56:59], v[164:167], v[0:3], v[56:59]\n"
    "v_exp_f32 v87, v87\n"
    "v_mfma_f32_16x16x32_bf16 v[56:59], v[168:171], v[4:7], v[56:59]\n"
    "v_exp_f32 v88, v88\n"
    "v_mfma_f32_16x16x32_bf16 v[60:63], v[168:171], v[28:31], v[108:111]\n"
    "buffer_load_dwordx4 v200, s[40:43], s61 offen offset:3072 sc1 lds\n"
    "v_mfma_f32_16x16x32_bf16 v[60:63], v[164:167], v[24:27], v[60:63]\n"
    "s_add_u32 s65, s61, 0x1000\n"
    "v_exp_f32 v89, v89\n"
    "v_mfma_f32_16x16x32_bf16 v[60:63], v[160:163], v[20:23], v[60:63]\n"
    "s_waitcnt lgkmcnt(1)\n"
    "s_add_u32 m0, s71, 0x13000\n"
    "v_cvt_pk_bf16_f32 v157, v78, v79\n"
    "v_mfma_f32_16x16x32_bf16 v[60:63], v[172:175], v[8:11], v[60:63]\n"
    "ds_read_b128 v[160:163], v201\n"
    "v_cvt_pk_bf16_f32 v146, v80, v81\n"
    "v_mfma_f32_16x16x32_bf16 v[60:63], v[176:179], v[12:15], v[60:63]\n"
    "s_waitcnt lgkmcnt(1)\n"
    "buffer_load_dwordx4 v200, s[40:43], s65 offen sc1 lds\n"
    "v_mfma_f32_16x16x32_bf16 v[60:63], v[180:183], v[16:19], v[60:63]\n"
    "v_exp_f32 v90, v90\n"
    "v_mfma_f32_16x16x32_bf16 v[56:59], v[180:183], a[248:251], v[56:59]\n"
    "ds_read_b128 v[164:167], v201 offset:1024\n"
    "v_cvt_pk_bf16_f32 v147, v82, v83\n"
    "v_mfma_f32_16x16x32_bf16 v[56:59], v[176:179], a[244:247], v[56:59]\n"
    "v_exp_f32 v91, v91\n"
    "v_mfma_f32_16x16x32_bf16 v[56:59], v[172:175], a[240:243], v[56:59]\n"
    "buffer_load_dwordx4 v200, s[40:43], s65 offen offset:1024 sc1 lds\n"
    "v_mfma_f32_16x16x32_bf16 v[52:55], v[172:175], a[216:219], v[52:55]\n"
    "ds_read_b128 v[168:171], v201 offset:2048\n"
    "v_cvt_pk_bf16_f32 v150, v84, v85\n"
    "v_mfma_f32_16x16x32_bf16 v[52:55], v[176:179], a[220:223], v[52:55]\n"
    "v_exp_f32 v92, v92\n"
    "v_mfma_f32_16x16x32_bf16 v[52:55], v[180:183], a[224:227], v[52:55]\n"
    "v_exp_f32 v93, v93\n"
    "v_mfma_f32_16x16x32_bf16 v[48:51], v[180:183], a[200:203], v[48:51]\n"
    "v_cvt_pk_bf16_f32 v151, v86, v87\n"
    "v_cvt_pk_bf16_f32 v154, v88, v89\n"
    "v_mfma_f32_16x16x32_bf16 v[48:51], v[176:179], a[196:199], v[48:51]\n"
    "v_exp_f32 v94, v94\n"
    "v_mfma_f32_16x16x32_bf16 v[48:51], v[172:175], a[192:195], v[48:51]\n"
    "s_waitcnt lgkmcnt(1)\n"
    "v_cvt_pk_bf16_f32 v155, v90, v91\n"
    "v_cvt_pk_bf16_f32 v158, v92, v93\n"
    "v_mfma_f32_16x16x32_bf16 a[0:3], v[160:163], v[128:131], a[0:3]\n"
    "ds_read_b128 v[172:175], v201 offset:3072\n"
    "ds_read_b128 v[176:179], v201 offset:4096\n"
    "v_mfma_f32_16x16x32_bf16 a[0:3], v[164:167], v[144:147], a[0:3]\n"
    "v_exp_f32 v95, v95\n"
    "v_mfma_f32_16x16x32_bf16 a[4:7], v[160:163], v[132:135], a[4:7]\n"
    "v_cvt_pk_bf16_f32 v159, v94, v95\n"
    "v_add_f32 v113, v113, v64\n"
    "v_mfma_f32_16x16x32_bf16 a[4:7], v[164:167], v[148:151], a[4:7]\n"
    "v_exp_f32 v32, v32\n"
    "v_mfma_f32_16x16x32_bf16 a[8:11], v[160:163], v[136:139], a[8:11]\n"
    "ds_read_b128 v[180:183], v201 offset:5120\n"
    "v_add_f32 v114, v114, v65\n"
    "v_mfma_f32_16x16x32_bf16 a[8:11], v[164:167], v[152:155], a[8:11]\n"
    "v_exp_f32 v33, v33\n"
    "v_mfma_f32_16x16x32_bf16 a[12:15], v[160:163], v[140:143], a[12:15]\n"
    "v_exp_f32 v34, v34\n"
    "v_mfma_f32_16x16x32_bf16 a[12:15], v[164:167], v[156:159], a[12:15]\n"
    "s_waitcnt lgkmcnt(2)\n"
    "v_exp_f32 v35, v35\n"
    "v_mfma_f32_16x16x32_bf16 a[28:31], v[168:171], v[140:143], a[28:31]\n"
    "ds_read_b128 v[160:163], v201 offset:6144\n"
    "v_add_f32 v113, v113, v66\n"
    "v_mfma_f32_16x16x32_bf16 a[28:31], v[172:175], v[156:159], a[28:31]\n"
    "v_exp_f32 v36, v36\n"
    "v_mfma_f32_16x16x32_bf16 a[24:27], v[172:175], v[152:155], a[24:27]\n"
    "v_exp_f32 v37, v37\n"
    "v_mfma_f32_16x16x32_bf16 a[24:27], v[168:171], v[136:139], a[24:27]\n"
    "v_exp_f32 v38, v38\n"
    "v_mfma_f32_16x16x32_bf16 a[20:23], v[168:171], v[132:135], a[20:23]\n"
    "ds_read_b128 v[164:167], v201 offset:7168\n"
    "v_add_f32 v114, v114, v67\n"
    "v_mfma_f32_16x16x32_bf16 a[20:23], v[172:175], v[148:151], a[20:23]\n"
    "v_exp_f32 v39, v39\n"
    "v_mfma_f32_16x16x32_bf16 a[16:19], v[172:175], v[144:147], a[16:19]\n"
    "v_exp_f32 v40, v40\n"
    "v_mfma_f32_16x16x32_bf16 a[16:19], v[168:171], v[128:131], a[16:19]\n"
    "s_waitcnt lgkmcnt(2)\n"
    "v_exp_f32 v41, v41\n"
    "v_mfma_f32_16x16x32_bf16 a[32:35], v[176:179], v[128:131], a[32:35]\n"
    "ds_read_b128 v[168:171], v201 offset:8192\n"
    "v_add_f32 v117, v117, v68\n"
    "v_mfma_f32_16x16x32_bf16 a[32:35], v[180:183], v[144:147], a[32:35]\n"
    "v_exp_f32 v42, v42\n"
    "v_mfma_f32_16x16x32_bf16 a[36:39], v[180:183], v[148:151], a[36:39]\n"
    "v_exp_f32 v43, v43\n"
    "v_mfma_f32_16x16x32_bf16 a[36:39], v[176:179], v[132:135], a[36:39]\n"
    "v_exp_f32 v44, v44\n"
    "v_mfma_f32_16x16x32_bf16 a[40:43], v[176:179], v[136:139], a[40:43]\n"
    "ds_read_b128 v[172:175], v201 offset:9216\n"
    "v_add_f32 v118, v118, v69\n"
    "v_mfma_f32_16x16x32_bf16 a[40:43], v[180:183], v[152:155], a[40:43]\n"
    "v_exp_f32 v45, v45\n"
    "v_mfma_f32_16x16x32_bf16 a[44:47], v[180:183], v[156:159], a[44:47]\n"
    "v_exp_f32 v46, v46\n"
    "v_mfma_f32_16x16x32_bf16 a[44:47], v[176:179], v[140:143], a[44:47]\n"
    "s_waitcnt lgkmcnt(2)\n"
    "v_exp_f32 v47, v47\n"
    "v_mfma_f32_16x16x32_bf16 a[60:63], v[160:163], v[140:143], a[60:63]\n"
    "ds_read_b128 v[176:179], v201 offset:10240\n"
    "v_add_f32 v117, v117, v70\n"
    "v_mfma_f32_16x16x32_bf16 a[60:63], v[164:167], v[156:159], a[60:63]\n"
    "v_exp_f32 v48, v48\n"
    "v_mfma_f32_16x16x32_bf16 a[56:59], v[164:167], v[152:155], a[56:59]\n"
    "v_exp_f32 v49, v49\n"
    "v_mfma_f32_16x16x32_bf16 a[56:59], v[160:163], v[136:139], a[56:59]\n"
    "v_exp_f32 v50, v50\n"
    "v_mfma_f32_16x16x32_bf16 a[52:55], v[160:163], v[132:135], a[52:55]\n"
    "ds_read_b128 v[180:183], v201 offset:11264\n"
    "v_add_f32 v113, v113, v32\n"
    "v_mfma_f32_16x16x32_bf16 a[52:55], v[164:167], v[148:151], a[52:55]\n"
    "v_exp_f32 v51, v51\n"
    "v_mfma_f32_16x16x32_bf16 a[48:51], v[164:167], v[144:147], a[48:51]\n"
    "v_exp_f32 v52, v52\n"
    "v_mfma_f32_16x16x32_bf16 a[48:51], v[160:163], v[128:131], a[48:51]\n"
    "s_waitcnt lgkmcnt(2)\n"
    "v_add_f32 v118, v118, v71\n"
    "v_add_f32 v114, v114, v33\n"
    "v_mfma_f32_16x16x32_bf16 a[64:67], v[168:171], v[128:131], a[64:67]\n"
    "ds_read_b128 v[160:163], v201 offset:12288\n"
    "v_add_f32 v121, v121, v72\n"
    "v_mfma_f32_16x16x32_bf16 a[64:67], v[172:175], v[144:147], a[64:67]\n"
    "v_add_f32 v113, v113, v34\n"
    "v_add_f32 v122, v122, v73\n"
    "v_mfma_f32_16x16x32_bf16 a[68:71], v[172:175], v[148:151], a[68:71]\n"
    "v_exp_f32 v53, v53\n"
    "v_mfma_f32_16x16x32_bf16 a[68:71], v[168:171], v[132:135], a[68:71]\n"
    "s_add_u32 m0, s51, s95\n"
    "v_add_f32 v114, v114, v35\n"
    "v_add_f32 v121, v121, v74\n"
    "v_mfma_f32_16x16x32_bf16 a[72:75], v[168:171], v[136:139], a[72:75]\n"
    "ds_read_b128 v[164:167], v201 offset:13312\n"
    "v_add_f32 v117, v117, v36\n"
    "v_mfma_f32_16x16x32_bf16 a[72:75], v[172:175], v[152:155], a[72:75]\n"
    "buffer_load_dwordx4 v202, s[32:35], s64 offen lds\n"
    "v_mfma_f32_16x16x32_bf16 a[76:79], v[172:175], v[156:159], a[76:79]\n"
    "v_exp_f32 v54, v54\n"
    "v_mfma_f32_16x16x32_bf16 a[76:79], v[168:171], v[140:143], a[76:79]\n"
    "s_waitcnt lgkmcnt(2)\n"
    "v_add_f32 v122, v122, v75\n"
    "v_add_f32 v118, v118, v37\n"
    "v_mfma_f32_16x16x32_bf16 a[92:95], v[176:179], v[140:143], a[92:95]\n"
    "ds_read_b128 v[168:171], v201 offset:14336\n"
    "v_add_f32 v125, v125, v76\n"
    "v_mfma_f32_16x16x32_bf16 a[92:95], v[180:183], v[156:159], a[92:95]\n"
    "buffer_load_dwordx4 v203, s[32:35], s64 offen offset:1024 lds\n"
    "v_mfma_f32_16x16x32_bf16 a[88:91], v[180:183], v[152:155], a[88:91]\n"
    "v_add_f32 v117, v117, v38\n"
    "v_add_f32 v126, v126, v77\n"
    "v_mfma_f32_16x16x32_bf16 a[88:91], v[176:179], v[136:139], a[88:91]\n"
    "v_exp_f32 v55, v55\n"
    "v_mfma_f32_16x16x32_bf16 a[84:87], v[176:179], v[132:135], a[84:87]\n"
    "ds_read_b128 v[172:175], v201 offset:15360\n"
    "v_add_f32 v118, v118, v39\n"
    "v_mfma_f32_16x16x32_bf16 a[84:87], v[180:183], v[148:151], a[84:87]\n"
    "buffer_load_dwordx4 v204, s[32:35], s64 offen offset:2048 lds\n"
    "v_mfma_f32_16x16x32_bf16 a[80:83], v[180:183], v[144:147], a[80:83]\n"
    "v_add_f32 v125, v125, v78\n"
    "v_add_f32 v121, v121, v40\n"
    "v_mfma_f32_16x16x32_bf16 a[80:83], v[176:179], v[128:131], a[80:83]\n"
    "s_waitcnt lgkmcnt(2)\n"
    "v_exp_f32 v56, v56\n"
    "v_mfma_f32_16x16x32_bf16 a[96:99], v[160:163], v[128:131], a[96:99]\n"
    "s_mov_b32 s95, s94\n"
    "ds_read_b128 v[176:179], v201 offset:16384\n"
    "v_add_f32 v126, v126, v79\n"
    "v_mfma_f32_16x16x32_bf16 a[96:99], v[164:167], v[144:147], a[96:99]\n"
    "s_sub_u32 s94, s94, 0x3000\n"
    "v_add_f32 v122, v122, v41\n"
    "v_add_f32 v113, v113, v80\n"
    "v_mfma_f32_16x16x32_bf16 a[100:103], v[164:167], v[148:151], a[100:103]\n"
    "s_min_u32 s94, s94, 0x6000\n"
    "v_add_f32 v121, v121, v42\n"
    "v_add_f32 v114, v114, v81\n"
    "v_mfma_f32_16x16x32_bf16 a[100:103], v[160:163], v[132:135], a[100:103]\n"
    "s_sub_u32 s93, s94, s95\n"
    "v_exp_f32 v57, v57\n"
    "v_mfma_f32_16x16x32_bf16 a[104:107], v[160:163], v[136:139], a[104:107]\n"
    "ds_read_b128 v[180:183], v201 offset:17408\n"
    "v_add_f32 v122, v122, v43\n"
    "v_mfma_f32_16x16x32_bf16 a[104:107], v[164:167], v[152:155], a[104:107]\n"
    "v_add_f32 v113, v113, v82\n"
    "v_add_f32 v125, v125, v44\n"
    "v_mfma_f32_16x16x32_bf16 a[108:111], v[164:167], v[156:159], a[108:111]\n"
    "v_exp_f32 v58, v58\n"
    "v_mfma_f32_16x16x32_bf16 a[108:111], v[160:163], v[140:143], a[108:111]\n"
    "s_waitcnt lgkmcnt(2)\n"
    "v_add_f32 v114, v114, v83\n"
    "v_add_f32 v126, v126, v45\n"
    "v_mfma_f32_16x16x32_bf16 a[124:127], v[168:171], v[140:143], a[124:127]\n"
    "ds_read_b128 v[160:163], v201 offset:18432\n"
    "v_add_f32 v117, v117, v84\n"
    "v_mfma_f32_16x16x32_bf16 a[124:127], v[172:175], v[156:159], a[124:127]\n"
    "v_add_f32 v125, v125, v46\n"
    "v_add_f32 v118, v118, v85\n"
    "v_mfma_f32_16x16x32_bf16 a[120:123], v[172:175], v[152:155], a[120:123]\n"
    "v_exp_f32 v59, v59\n"
    "v_mfma_f32_16x16x32_bf16 a[120:123], v[168:171], v[136:139], a[120:123]\n"
    "v_add_f32 v126, v126, v47\n"
    "v_add_f32 v117, v117, v86\n"
    "v_mfma_f32_16x16x32_bf16 a[116:119], v[168:171], v[132:135], a[116:119]\n"
    "ds_read_b128 v[164:167], v201 offset:19456\n"
    "v_add_f32 v113, v113, v48\n"
    "v_mfma_f32_16x16x32_bf16 a[116:119], v[172:175], v[148:151], a[116:119]\n"
    "v_exp_f32 v60, v60\n"
    "v_mfma_f32_16x16x32_bf16 a[112:115], v[172:175], v[144:147], a[112:115]\n"
    "v_add_u32 v205, s93, v205\n"
    "v_add_f32 v118, v118, v87\n"
    "v_mfma_f32_16x16x32_bf16 a[112:115], v[168:171], v[128:131], a[112:115]\n"
    "s_waitcnt lgkmcnt(2)\n"
    "v_add_f32 v114, v114, v49\n"
    "v_add_u32 v209, 0x180, v209\n"
    "v_mfma_f32_16x16x32_bf16 a[128:131], v[176:179], v[128:131], a[128:131]\n"
    "ds_read_b128 v[168:171], v201 offset:20480\n"
    "v_add_u32 v206, s93, v206\n"
    "v_mfma_f32_16x16x32_bf16 a[128:131], v[180:183], v[144:147], a[128:131]\n"
    "v_add_f32 v121, v121, v88\n"
    "v_add_f32 v113, v113, v50\n"
    "v_mfma_f32_16x16x32_bf16 a[132:135], v[180:183], v[148:151], a[132:135]\n"
    "s_add_u32 s64, s64, 0xc000\n"
    "v_exp_f32 v61, v61\n"
    "v_mfma_f32_16x16x32_bf16 a[132:135], v[176:179], v[132:135], a[132:135]\n"
    "s_add_u32 s62, s62, 0x6000\n"
    "v_add_u32 v210, 0x80, v210\n"
    "v_add_u32 v207, s93, v207\n"
    "v_mfma_f32_16x16x32_bf16 a[136:139], v[176:179], v[136:139], a[136:139]\n"
    "s_add_u32 s63, s63, 0x6000\n"
    "ds_read_b128 v[172:175], v201 offset:21504\n"
    "v_add_f32 v122, v122, v89\n"
    "v_mfma_f32_16x16x32_bf16 a[136:139], v[180:183], v[152:155], a[136:139]\n"
    "s_add_u32 s60, s60, 0x6000\n"
    "v_add_f32 v114, v114, v51\n"
    "v_add_f32 v121, v121, v90\n"
    "v_mfma_f32_16x16x32_bf16 a[140:143], v[180:183], v[156:159], a[140:143]\n"
    "s_add_u32 s61, s61, 0x6000\n"
    "v_add_f32 v117, v117, v52\n"
    "v_add_f32 v122, v122, v91\n"
    "v_mfma_f32_16x16x32_bf16 a[140:143], v[176:179], v[140:143], a[140:143]\n"
    "s_waitcnt lgkmcnt(2)\n"
    "v_exp_f32 v62, v62\n"
    "v_mfma_f32_16x16x32_bf16 a[156:159], v[160:163], v[140:143], a[156:159]\n"
    "ds_read_b128 v[176:179], v201 offset:22528\n"
    "v_add_f32 v118, v118, v53\n"
    "v_mfma_f32_16x16x32_bf16 a[156:159], v[164:167], v[156:159], a[156:159]\n"
    "s_waitcnt vmcnt(3)\n"
    "ds_read_b128 v[180:183], v201 offset:23552\n"
    "v_add_f32 v125, v125, v92\n"
    "v_mfma_f32_16x16x32_bf16 a[152:155], v[164:167], v[152:155], a[152:155]\n"
    "v_add_f32 v117, v117, v54\n"
    "v_add_f32 v126, v126, v93\n"
    "v_mfma_f32_16x16x32_bf16 a[152:155], v[160:163], v[136:139], a[152:155]\n"
    "v_exp_f32 v63, v63\n"
    "v_mfma_f32_16x16x32_bf16 a[148:151], v[160:163], v[132:135], a[148:151]\n"
    "v_add_f32 v118, v118, v55\n"
    "v_add_f32 v125, v125, v94\n"
    "v_mfma_f32_16x16x32_bf16 a[148:151], v[164:167], v[148:151], a[148:151]\n"
    "v_add_f32 v121, v121, v56\n"
    "v_add_f32 v126, v126, v95\n"
    "v_mfma_f32_16x16x32_bf16 a[144:147], v[164:167], v[144:147], a[144:147]\n"
    "v_add_f32 v122, v122, v57\n"
    "v_mfma_f32_16x16x32_bf16 a[144:147], v[160:163], v[128:131], a[144:147]\n"
    "s_waitcnt lgkmcnt(0)\n"
    "v_mfma_f32_16x16x32_bf16 a[160:163], v[168:171], v[128:131], a[160:163]\n"
    "s_barrier\n"
    "v_mfma_f32_16x16x32_bf16 a[160:163], v[172:175], v[144:147], a[160:163]\n"
    "ds_read_u16_d16_hi v223, v209 offset:6144\n"
    "ds_read_u16_d16_hi v222, v209\n"
    "v_mfma_f32_16x16x32_bf16 a[164:167], v[172:175], v[148:151], a[164:167]\n"
    "ds_read_b128 v[160:163], v200\n"
    "v_add_f32 v121, v121, v58\n"
    "v_mfma_f32_16x16x32_bf16 a[164:167], v[168:171], v[132:135], a[164:167]\n"
    "ds_read_b128 v[232:235], v205 offset:24576\n"
    "v_add_f32 v122, v122, v59\n"
    "v_mfma_f32_16x16x32_bf16 a[168:171], v[168:171], v[136:139], a[168:171]\n"
    "ds_read_b128 v[164:167], v200 offset:1024\n"
    "ds_read_b128 v[236:239], v206 offset:24576\n"
    "v_mfma_f32_16x16x32_bf16 a[168:171], v[172:175], v[152:155], a[168:171]\n"
    "v_add_f32 v125, v125, v60\n"
    "v_add_f32 v126, v126, v61\n"
    "v_mfma_f32_16x16x32_bf16 a[172:175], v[172:175], v[156:159], a[172:175]\n"
    "v_add_f32 v125, v125, v62\n"
    "v_add_f32 v126, v126, v63\n"
    "v_mfma_f32_16x16x32_bf16 a[172:175], v[168:171], v[140:143], a[172:175]\n"
    "ds_read_b64 v[228:229], v210\n"
    "ds_read2st64_b32 v[240:241], v207 offset0:96 offset1:99\n"
    "v_mfma_f32_16x16x32_bf16 a[188:191], v[176:179], v[140:143], a[188:191]\n"
    "ds_read_b128 v[168:171], v200 offset:2048\n"
    "ds_read2st64_b32 v[242:243], v207 offset0:102 offset1:105\n"
    "v_mfma_f32_16x16x32_bf16 a[188:191], v[180:183], v[156:159], a[188:191]\n"
    "ds_read2st64_b32 v[244:245], v207 offset0:108 offset1:111\n"
    "ds_read2st64_b32 v[246:247], v207 offset0:114 offset1:117\n"
    "v_mfma_f32_16x16x32_bf16 a[184:187], v[176:179], v[136:139], a[184:187]\n"
    "v_cvt_pk_bf16_f32 v140, v44, v45\n"
    "v_cvt_pk_bf16_f32 v141, v46, v47\n"
    "v_mfma_f32_16x16x32_bf16 a[184:187], v[180:183], v[152:155], a[184:187]\n"
    "s_waitcnt lgkmcnt(6)\n"
    "v_mul_f32 v226, s75, v223\n"
    "v_mfma_f32_16x16x32_bf16 a[180:183], v[176:179], v[132:135], a[180:183]\n"
    "ds_read_b128 v[172:175], v200 offset:3072\n"
    "v_fma_f32 v227, -v222, v226, v217\n"
    "v_mfma_f32_16x16x32_bf16 a[180:183], v[180:183], v[148:151], a[180:183]\n"
    "v_cvt_f32_i32 v232, v232\n"
    "v_cvt_f32_i32 v233, v233\n"
    "v_mfma_f32_16x16x32_bf16 a[176:179], v[176:179], v[128:131], a[176:179]\n"
    "v_cvt_f32_i32 v234, v234\n"
    "v_cvt_f32_i32 v235, v235\n"
    "v_mfma_f32_16x16x32_bf16 a[176:179], v[180:183], v[144:147], a[176:179]\n"
    "v_cvt_f32_i32 v236, v236\n"
    "v_cvt_f32_i32 v237, v237\n"
    "LM01:\n"
    "v_mfma_f32_16x16x32_bf16 v[64:67], v[160:163], a[192:195], v[96:99]\n"
    "ds_read_b128 v[176:179], v200 offset:4096\n"
    "v_cvt_f32_i32 v238, v238\n"
    "v_mfma_f32_16x16x32_bf16 v[64:67], v[164:167], a[196:199], v[64:67]\n"
    "s_waitcnt lgkmcnt(1)\n"
    "v_cvt_f32_i32 v239, v239\n"
    "v_mul_f32_dpp v232, v226, v232 row_newbcast:0 row_mask:0xf bank_mask:0xf\n"
    "v_mfma_f32_16x16x32_bf16 v[64:67], v[168:171], a[200:203], v[64:67]\n"
    "v_mul_f32_dpp v233, v226, v233 row_newbcast:1 row_mask:0xf bank_mask:0xf\n"
    "v_mul_f32_dpp v234, v226, v234 row_newbcast:2 row_mask:0xf bank_mask:0xf\n"
    "v_mfma_f32_16x16x32_bf16 v[68:71], v[168:171], a[224:227], v[100:103]\n"
    "v_mul_f32_dpp v235, v226, v235 row_newbcast:3 row_mask:0xf bank_mask:0xf\n"
    "v_mul_f32_dpp v236, v226, v236 row_newbcast:4 row_mask:0xf bank_mask:0xf\n"
    "v_mfma_f32_16x16x32_bf16 v[68:71], v[164:167], a[220:223], v[68:71]\n"
    "ds_read_b128 v[180:183], v200 offset:5120\n"
    "v_mul_f32_dpp v237, v226, v237 row_newbcast:5 row_mask:0xf bank_mask:0xf\n"
    "v_mfma_f32_16x16x32_bf16 v[68:71], v[160:163], a[216:219], v[68:71]\n"
    "v_mul_f32_dpp v238, v226, v238 row_newbcast:6 row_mask:0xf bank_mask:0xf\n"
    "v_mul_f32_dpp v239, v226, v239 row_newbcast:7 row_mask:0xf bank_mask:0xf\n"
    "v_mfma_f32_16x16x32_bf16 v[72:75], v[160:163], a[240:243], v[104:107]\n"
    "v_add_f32_dpp v232, v227, v232 row_newbcast:0 row_mask:0xf bank_mask:0xf\n"
    "v_add_f32_dpp v233, v227, v233 row_newbcast:1 row_mask:0xf bank_mask:0xf\n"
    "v_mfma_f32_16x16x32_bf16 v[72:75], v[164:167], a[244:247], v[72:75]\n"
    "v_add_f32_dpp v234, v227, v234 row_newbcast:2 row_mask:0xf bank_mask:0xf\n"
    "v_add_f32_dpp v235, v227, v235 row_newbcast:3 row_mask:0xf bank_mask:0xf\n"
    "v_mfma_f32_16x16x32_bf16 v[72:75], v[168:171], a[248:251], v[72:75]\n"
    "v_add_f32_dpp v236, v227, v236 row_newbcast:4 row_mask:0xf bank_mask:0xf\n"
    "v_add_f32_dpp v237, v227, v237 row_newbcast:5 row_mask:0xf bank_mask:0xf\n"
    "v_mfma_f32_16x16x32_bf16 v[76:79], v[168:171], v[16:19], v[108:111]\n"
    "v_add_f32_dpp v238, v227, v238 row_newbcast:6 row_mask:0xf bank_mask:0xf\n"
    "v_add_f32_dpp v239, v227, v239 row_newbcast:7 row_mask:0xf bank_mask:0xf\n"
    "v_mfma_f32_16x16x32_bf16 v[76:79], v[164:167], v[12:15], v[76:79]\n"
    "v_cvt_pk_bf16_f32 v232, v232, v233\n"
    "v_cvt_f32_i32 v240, v240\n"
    "v_mfma_f32_16x16x32_bf16 v[76:79], v[160:163], v[8:11], v[76:79]\n"
    "s_waitcnt lgkmcnt(1)\n"
    "v_cvt_pk_bf16_f32 v233, v234, v235\n"
    "v_cvt_f32_i32 v241, v241\n"
    "v_mfma_f32_16x16x32_bf16 v[76:79], v[172:175], v[20:23], v[76:79]\n"
    "ds_read_b128 v[160:163], v200 offset:9216\n"
    "v_cvt_pk_bf16_f32 v234, v236, v237\n"
    "v_mfma_f32_16x16x32_bf16 v[76:79], v[176:179], v[24:27], v[76:79]\n"
    "s_waitcnt lgkmcnt(1)\n"
    "v_cvt_f32_i32 v242, v242\n"
    "v_cvt_pk_bf16_f32 v235, v238, v239\n"
    "v_mfma_f32_16x16x32_bf16 v[76:79], v[180:183], v[28:31], v[76:79]\n"
    "v_cvt_f32_i32 v243, v243\n"
    "v_cvt_f32_i32 v244, v244\n"
    "v_mfma_f32_16x16x32_bf16 v[72:75], v[180:183], v[4:7], v[72:75]\n"
    "ds_read_b128 v[164:167], v200 offset:10240\n"
    "v_cvt_f32_i32 v245, v245\n"
    "v_mfma_f32_16x16x32_bf16 v[72:75], v[176:179], v[0:3], v[72:75]\n"
    "buffer_store_dwordx4 v[232:235], v211, s[36:39], s62 offen sc1\n"
    "v_cvt_f32_i32 v246, v246\n"
    "v_cvt_f32_i32 v247, v247\n"
    "v_mul_f32_dpp v240, v228, v240 row_newbcast:0 row_mask:0xf bank_mask:0xf\n"
    "v_mfma_f32_16x16x32_bf16 v[72:75], v[172:175], a[252:255], v[72:75]\n"
    "v_mul_f32_dpp v241, v228, v241 row_newbcast:1 row_mask:0xf bank_mask:0xf\n"
    "v_mul_f32_dpp v242, v228, v242 row_newbcast:2 row_mask:0xf bank_mask:0xf\n"
    "v_mfma_f32_16x16x32_bf16 v[68:71], v[172:175], a[228:231], v[68:71]\n"
    "ds_read_b128 v[168:171], v200 offset:11264\n"
    "v_mul_f32_dpp v243, v228, v243 row_newbcast:3 row_mask:0xf bank_mask:0xf\n"
    "v_mfma_f32_16x16x32_bf16 v[68:71], v[176:179], a[232:235], v[68:71]\n"
    "v_mul_f32_dpp v244, v228, v244 row_newbcast:4 row_mask:0xf bank_mask:0xf\n"
    "v_mul_f32_dpp v245, v228, v245 row_newbcast:5 row_mask:0xf bank_mask:0xf\n"
    "v_mfma_f32_16x16x32_bf16 v[68:71], v[180:183], a[236:239], v[68:71]\n"
    "v_mul_f32_dpp v246, v228, v246 row_newbcast:6 row_mask:0xf bank_mask:0xf\n"
    "v_mul_f32_dpp v247, v228, v247 row_newbcast:7 row_mask:0xf bank_mask:0xf\n"
    "v_mfma_f32_16x16x32_bf16 v[64:67], v[180:183], a[212:215], v[64:67]\n"
    "v_add_f32_dpp v240, v229, v240 row_newbcast:0 row_mask:0xf bank_mask:0xf\n"
    "v_add_f32_dpp v241, v229, v241 row_newbcast:1 row_mask:0xf bank_mask:0xf\n"
    "v_mfma_f32_16x16x32_bf16 v[64:67], v[176:179], a[208:211], v[64:67]\n"
    "v_add_f32_dpp v242, v229, v242 row_newbcast:2 row_mask:0xf bank_mask:0xf\n"
    "v_add_f32_dpp v243, v229, v243 row_newbcast:3 row_mask:0xf bank_mask:0xf\n"
    "v_mfma_f32_16x16x32_bf16 v[64:67], v[172:175], a[204:207], v[64:67]\n"
    "s_waitcnt lgkmcnt(1)\n"
    "v_add_f32_dpp v244, v229, v244 row_newbcast:4 row_mask:0xf bank_mask:0xf\n"
    "v_add_f32_dpp v245, v229, v245 row_newbcast:5 row_mask:0xf bank_mask:0xf\n"
    "v_mfma_f32_16x16x32_bf16 v[80:83], v[160:163], a[204:207], v[96:99]\n"
    "ds_read_b128 v[172:175], v200 offset:6144\n"
    "v_add_f32_dpp v246, v229, v246 row_newbcast:6 row_mask:0xf bank_mask:0xf\n"
    "v_mfma_f32_16x16x32_bf16 v[80:83], v[164:167], a[208:211], v[80:83]\n"
    "s_waitcnt lgkmcnt(1)\n"
    "v_add_f32_dpp v247, v229, v247 row_newbcast:7 row_mask:0xf bank_mask:0xf\n"
    "v_cvt_pk_bf16_f32 v240, v240, v241\n"
    "v_mfma_f32_16x16x32_bf16 v[80:83], v[168:171], a[212:215], v[80:83]\n"
    "v_cvt_pk_bf16_f32 v241, v242, v243\n"
    "v_cvt_pk_bf16_f32 v242, v244, v245\n"
    "v_mfma_f32_16x16x32_bf16 v[84:87], v[168:171], a[236:239], v[100:103]\n"
    "ds_read_b128 v[176:179], v200 offset:7168\n"
    "v_cvt_pk_bf16_f32 v243, v246, v247\n"
    "v_mfma_f32_16x16x32_bf16 v[84:87], v[164:167], a[232:235], v[84:87]\n"
    "buffer_store_dwordx4 v[240:243], v212, s[40:43], s63 offen sc1\n"
    "v_cvt_pk_bf16_f32 v136, v40, v41\n"
    "v_cvt_pk_bf16_f32 v137, v42, v43\n"
    "v_cvt_pk_bf16_f32 v132, v36, v37\n"
    "v_mfma_f32_16x16x32_bf16 v[84:87], v[160:163], a[228:231], v[84:87]\n"
    "v_cvt_pk_bf16_f32 v133, v38, v39\n"
    "v_cvt_pk_bf16_f32 v128, v32, v33\n"
    "v_mfma_f32_16x16x32_bf16 v[88:91], v[160:163], a[252:255], v[104:107]\n"
    "ds_read_b128 v[180:183], v200 offset:8192\n"
    "v_cvt_pk_bf16_f32 v129, v34, v35\n"
    "v_mfma_f32_16x16x32_bf16 v[88:91], v[164:167], v[0:3], v[88:91]\n"
    "v_exp_f32 v64, v64\n"
    "v_mfma_f32_16x16x32_bf16 v[88:91], v[168:171], v[4:7], v[88:91]\n"
    "v_exp_f32 v65, v65\n"
    "v_mfma_f32_16x16x32_bf16 v[92:95], v[168:171], v[28:31], v[108:111]\n"
    "v_exp_f32 v66, v66\n"
    "v_mfma_f32_16x16x32_bf16 v[92:95], v[164:167], v[24:27], v[92:95]\n"
    "v_exp_f32 v67, v67\n"
    "v_mfma_f32_16x16x32_bf16 v[92:95], v[160:163], v[20:23], v[92:95]\n"
    "s_waitcnt lgkmcnt(1)\n"
    "v_exp_f32 v68, v68\n"
    "v_mfma_f32_16x16x32_bf16 v[92:95], v[172:175], v[8:11], v[92:95]\n"
    "ds_read_b128 v[160:163], v200 offset:12288\n"
    "v_cvt_pk_bf16_f32 v142, v60, v61\n"
    "v_mfma_f32_16x16x32_bf16 v[92:95], v[176:179], v[12:15], v[92:95]\n"
    "s_waitcnt lgkmcnt(1)\n"
    "s_add_u32 s65, s60, s92\n"
    "v_cvt_pk_bf16_f32 v143, v62, v63\n"
    "v_mfma_f32_16x16x32_bf16 v[92:95], v[180:183], v[16:19], v[92:95]\n"
    "s_add_u32 m0, s90, 0x6000\n"
    "v_exp_f32 v69, v69\n"
    "v_mfma_f32_16x16x32_bf16 v[88:91], v[180:183], a[248:251], v[88:91]\n"
    "ds_read_b128 v[164:167], v200 offset:13312\n"
    "v_cvt_pk_bf16_f32 v138, v56, v57\n"
    "v_mfma_f32_16x16x32_bf16 v[88:91], v[176:179], a[244:247], v[88:91]\n"
    "buffer_load_dwordx4 v200, s[36:39], s65 offen sc1 lds\n"
    "v_mfma_f32_16x16x32_bf16 v[88:91], v[172:175], a[240:243], v[88:91]\n"
    "v_exp_f32 v70, v70\n"
    "v_mfma_f32_16x16x32_bf16 v[84:87], v[172:175], a[216:219], v[84:87]\n"
    "ds_read_b128 v[168:171], v200 offset:14336\n"
    "v_cvt_pk_bf16_f32 v139, v58, v59\n"
    "v_mfma_f32_16x16x32_bf16 v[84:87], v[176:179], a[220:223], v[84:87]\n"
    "v_exp_f32 v71, v71\n"
    "v_mfma_f32_16x16x32_bf16 v[84:87], v[180:183], a[224:227], v[84:87]\n"
    "buffer_load_dwordx4 v200, s[36:39], s65 offen offset:1024 sc1 lds\n"
    "v_mfma_f32_16x16x32_bf16 v[80:83], v[180:183], a[200:203], v[80:83]\n"
    "v_exp_f32 v72, v72\n"
    "v_mfma_f32_16x16x32_bf16 v[80:83], v[176:179], a[196:199], v[80:83]\n"
    "v_cvt_pk_bf16_f32 v134, v52, v53\n"
    "v_cvt_pk_bf16_f32 v135, v54, v55\n"
    "v_mfma_f32_16x16x32_bf16 v[80:83], v[172:175], a[192:195], v[80:83]\n"
    "s_waitcnt lgkmcnt(1)\n"
    "v_exp_f32 v73, v73\n"
    "v_mfma_f32_16x16x32_bf16 v[32:35], v[160:163], a[192:195], v[96:99]\n"
    "ds_read_b128 v[172:175], v200 offset:15360\n"
    "v_cvt_pk_bf16_f32 v130, v48, v49\n"
    "v_mfma_f32_16x16x32_bf16 v[32:35], v[164:167], a[196:199], v[32:35]\n"
    "s_waitcnt lgkmcnt(1)\n"
    "buffer_load_dwordx4 v200, s[36:39], s65 offen offset:2048 sc1 lds\n"
    "v_mfma_f32_16x16x32_bf16 v[32:35], v[168:171], a[200:203], v[32:35]\n"
    "v_cvt_pk_bf16_f32 v131, v50, v51\n"
    "v_cvt_pk_bf16_f32 v144, v64, v65\n"
    "v_mfma_f32_16x16x32_bf16 v[36:39], v[168:171], a[224:227], v[100:103]\n"
    "ds_read_b128 v[176:179], v200 offset:16384\n"
    "v_cvt_pk_bf16_f32 v145, v66, v67\n"
    "v_mfma_f32_16x16x32_bf16 v[36:39], v[164:167], a[220:223], v[36:39]\n"
    "v_exp_f32 v74, v74\n"
    "v_mfma_f32_16x16x32_bf16 v[36:39], v[160:163], a[216:219], v[36:39]\n"
    "buffer_load_dwordx4 v200, s[36:39], s65 offen offset:3072 sc1 lds\n"
    "v_mfma_f32_16x16x32_bf16 v[40:43], v[160:163], a[240:243], v[104:107]\n"
    "s_add_u32 s65, s65, 0x1000\n"
    "ds_read_b128 v[180:183], v200 offset:17408\n"
    "v_cvt_pk_bf16_f32 v148, v68, v69\n"
    "v_mfma_f32_16x16x32_bf16 v[40:43], v[164:167], a[244:247], v[40:43]\n"
    "s_add_u32 m0, s90, 0x7000\n"
    "v_exp_f32 v75, v75\n"
    "v_mfma_f32_16x16x32_bf16 v[40:43], v[168:171], a[248:251], v[40:43]\n"
    "v_exp_f32 v76, v76\n"
    "v_mfma_f32_16x16x32_bf16 v[44:47], v[168:171], v[16:19], v[108:111]\n"
    "buffer_load_dwordx4 v200, s[36:39], s65 offen sc1 lds\n"
    "v_mfma_f32_16x16x32_bf16 v[44:47], v[164:167], v[12:15], v[44:47]\n"
    "v_exp_f32 v77, v77\n"
    "v_mfma_f32_16x16x32_bf16 v[44:47], v[160:163], v[8:11], v[44:47]\n"
    "s_waitcnt lgkmcnt(1)\n"
    "v_exp_f32 v78, v78\n"
    "v_mfma_f32_16x16x32_bf16 v[44:47], v[172:175], v[20:23], v[44:47]\n"
    "ds_read_b128 v[160:163], v200 offset:21504\n"
    "v_cvt_pk_bf16_f32 v149, v70, v71\n"
    "v_mfma_f32_16x16x32_bf16 v[44:47], v[176:179], v[24:27], v[44:47]\n"
    "s_waitcnt lgkmcnt(1)\n"
    "buffer_load_dwordx4 v200, s[36:39], s65 offen offset:1024 sc1 lds\n"
    "v_mfma_f32_16x16x32_bf16 v[44:47], v[180:183], v[28:31], v[44:47]\n"
    "v_exp_f32 v79, v79\n"
    "v_mfma_f32_16x16x32_bf16 v[40:43], v[180:183], v[4:7], v[40:43]\n"
    "ds_read_b128 v[164:167], v200 offset:22528\n"
    "v_cvt_pk_bf16_f32 v152, v72, v73\n"
    "v_mfma_f32_16x16x32_bf16 v[40:43], v[176:179], v[0:3], v[40:43]\n"
    "v_exp_f32 v80, v80\n"
    "v_mfma_f32_16x16x32_bf16 v[40:43], v[172:175], a[252:255], v[40:43]\n"
    "buffer_load_dwordx4 v200, s[36:39], s65 offen offset:2048 sc1 lds\n"
    "v_mfma_f32_16x16x32_bf16 v[36:39], v[172:175], a[228:231], v[36:39]\n"
    "ds_read_b128 v[168:171], v200 offset:23552\n"
    "v_cvt_pk_bf16_f32 v153, v74, v75\n"
    "v_mfma_f32_16x16x32_bf16 v[36:39], v[176:179], a[232:235], v[36:39]\n"
    "s_add_u32 m0, s71, 0xc000\n"
    "v_exp_f32 v81, v81\n"
    "v_mfma_f32_16x16x32_bf16 v[36:39], v[180:183], a[236:239], v[36:39]\n"
    "v_exp_f32 v82, v82\n"
    "v_mfma_f32_16x16x32_bf16 v[32:35], v[180:183], a[212:215], v[32:35]\n"
    "buffer_load_dwordx4 v200, s[40:43], s61 offen sc1 lds\n"
    "v_mfma_f32_16x16x32_bf16 v[32:35], v[176:179], a[208:211], v[32:35]\n"
    "v_exp_f32 v83, v83\n"
    "v_mfma_f32_16x16x32_bf16 v[32:35], v[172:175], a[204:207], v[32:35]\n"
    "s_waitcnt lgkmcnt(1)\n"
    "v_exp_f32 v84, v84\n"
    "v_mfma_f32_16x16x32_bf16 v[48:51], v[160:163], a[204:207], v[96:99]\n"
    "ds_read_b128 v[172:175], v200 offset:18432\n"
    "v_cvt_pk_bf16_f32 v156, v76, v77\n"
    "v_mfma_f32_16x16x32_bf16 v[48:51], v[164:167], a[208:211], v[48:51]\n"
    "s_waitcnt lgkmcnt(1)\n"
    "buffer_load_dwordx4 v200, s[40:43], s61 offen offset:1024 sc1 lds\n"
    "v_mfma_f32_16x16x32_bf16 v[48:51], v[168:171], a[212:215], v[48:51]\n"
    "v_exp_f32 v85, v85\n"
    "v_mfma_f32_16x16x32_bf16 v[52:55], v[168:171], a[236:239], v[100:103]\n"
    "ds_read_b128 v[176:179], v200 offset:19456\n"
    "v_cvt_pk_bf16_f32 v157, v78, v79\n"
    "v_mfma_f32_16x16x32_bf16 v[52:55], v[164:167], a[232:235], v[52:55]\n"
    "v_exp_f32 v86, v86\n"
    "v_mfma_f32_16x16x32_bf16 v[52:55], v[160:163], a[228:231], v[52:55]\n"
    "buffer_load_dwordx4 v200, s[40:43], s61 offen offset:2048 sc1 lds\n"
    "v_mfma_f32_16x16x32_bf16 v[56:59], v[160:163], a[252:255], v[104:107]\n"
    "ds_read_b128 v[180:183], v200 offset:20480\n"
    "v_cvt_pk_bf16_f32 v146, v80, v81\n"
    "v_mfma_f32_16x16x32_bf16 v[56:59], v[164:167], v[0:3], v[56:59]\n"
    "v_exp_f32 v87, v87\n"
    "v_mfma_f32_16x16x32_bf16 v[56:59], v[168:171], v[4:7], v[56:59]\n"
    "v_exp_f32 v88, v88\n"
    "v_mfma_f32_16x16x32_bf16 v[60:63], v[168:171], v[28:31], v[108:111]\n"
    "buffer_load_dwordx4 v200, s[40:43], s61 offen offset:3072 sc1 lds\n"
    "v_mfma_f32_16x16x32_bf16 v[60:63], v[164:167], v[24:27], v[60:63]\n"
    "s_add_u32 s65, s61, 0x1000\n"
    "v_exp_f32 v89, v89\n"
    "v_mfma_f32_16x16x32_bf16 v[60:63], v[160:163], v[20:23], v[60:63]\n"
    "s_waitcnt lgkmcnt(1)\n"
    "s_add_u32 m0, s71, 0xd000\n"
    "v_cvt_pk_bf16_f32 v147, v82, v83\n"
    "v_mfma_f32_16x16x32_bf16 v[60:63], v[172:175], v[8:11], v[60:63]\n"
    "ds_read_b128 v[160:163], v201 offset:24576\n"
    "v_cvt_pk_bf16_f32 v150, v84, v85\n"
    "v_mfma_f32_16x16x32_bf16 v[60:63], v[176:179], v[12:15], v[60:63]\n"
    "s_waitcnt lgkmcnt(1)\n"
    "buffer_load_dwordx4 v200, s[40:43], s65 offen sc1 lds\n"
    "v_mfma_f32_16x16x32_bf16 v[60:63], v[180:183], v[16:19], v[60:63]\n"
    "v_exp_f32 v90, v90\n"
    "v_mfma_f32_16x16x32_bf16 v[56:59], v[180:183], a[248:251], v[56:59]\n"
    "ds_read_b128 v[164:167], v201 offset:25600\n"
    "v_cvt_pk_bf16_f32 v151, v86, v87\n"
    "v_mfma_f32_16x16x32_bf16 v[56:59], v[176:179], a[244:247], v[56:59]\n"
    "v_exp_f32 v91, v91\n"
    "v_mfma_f32_16x16x32_bf16 v[56:59], v[172:175], a[240:243], v[56:59]\n"
    "buffer_load_dwordx4 v200, s[40:43], s65 offen offset:1024 sc1 lds\n"
    "v_mfma_f32_16x16x32_bf16 v[52:55], v[172:175], a[216:219], v[52:55]\n"
    "ds_read_b128 v[168:171], v201 offset:26624\n"
    "v_cvt_pk_bf16_f32 v154, v88, v89\n"
    "v_mfma_f32_16x16x32_bf16 v[52:55], v[176:179], a[220:223], v[52:55]\n"
    "v_exp_f32 v92, v92\n"
    "v_mfma_f32_16x16x32_bf16 v[52:55], v[180:183], a[224:227], v[52:55]\n"
    "v_exp_f32 v93, v93\n"
    "v_mfma_f32_16x16x32_bf16 v[48:51], v[180:183], a[200:203], v[48:51]\n"
    "v_exp_f32 v94, v94\n"
    "v_mfma_f32_16x16x32_bf16 v[48:51], v[176:179], a[196:199], v[48:51]\n"
    "v_cvt_pk_bf16_f32 v155, v90, v91\n"
    "v_cvt_pk_bf16_f32 v158, v92, v93\n"
    "v_mfma_f32_16x16x32_bf16 v[48:51], v[172:175], a[192:195], v[48:51]\n"
    "s_waitcnt lgkmcnt(1)\n"
    "v_exp_f32 v95, v95\n"
    "v_mfma_f32_16x16x32_bf16 a[0:3], v[160:163], v[128:131], a[0:3]\n"
    "ds_read_b128 v[172:175], v201 offset:27648\n"
    "ds_read_b128 v[176:179], v201 offset:28672\n"
    "v_mfma_f32_16x16x32_bf16 a[0:3], v[164:167], v[144:147], a[0:3]\n"
    "v_cvt_pk_bf16_f32 v159, v94, v95\n"
    "v_add_f32 v113, v113, v64\n"
    "v_mfma_f32_16x16x32_bf16 a[4:7], v[160:163], v[132:135], a[4:7]\n"
    "v_exp_f32 v32, v32\n"
    "v_mfma_f32_16x16x32_bf16 a[4:7], v[164:167], v[148:151], a[4:7]\n"
    "v_exp_f32 v33, v33\n"
    "v_mfma_f32_16x16x32_bf16 a[8:11], v[160:163], v[136:139], a[8:11]\n"
    "ds_read_b128 v[180:183], v201 offset:29696\n"
    "v_add_f32 v114, v114, v65\n"
    "v_mfma_f32_16x16x32_bf16 a[8:11], v[164:167], v[152:155], a[8:11]\n"
    "v_exp_f32 v34, v34\n"
    "v_mfma_f32_16x16x32_bf16 a[12:15], v[160:163], v[140:143], a[12:15]\n"
    "v_exp_f32 v35, v35\n"
    "v_mfma_f32_16x16x32_bf16 a[12:15], v[164:167], v[156:159], a[12:15]\n"
    "s_waitcnt lgkmcnt(2)\n"
    "v_exp_f32 v36, v36\n"
    "v_mfma_f32_16x16x32_bf16 a[28:31], v[168:171], v[140:143], a[28:31]\n"
    "ds_read_b128 v[160:163], v201 offset:30720\n"
    "v_add_f32 v113, v113, v66\n"
    "v_mfma_f32_16x16x32_bf16 a[28:31], v[172:175], v[156:159], a[28:31]\n"
    "v_exp_f32 v37, v37\n"
    "v_mfma_f32_16x16x32_bf16 a[24:27], v[172:175], v[152:155], a[24:27]\n"
    "v_exp_f32 v38, v38\n"
    "v_mfma_f32_16x16x32_bf16 a[24:27], v[168:171], v[136:139], a[24:27]\n"
    "v_exp_f32 v39, v39\n"
    "v_mfma_f32_16x16x32_bf16 a[20:23], v[168:171], v[132:135], a[20:23]\n"
    "ds_read_b128 v[164:167], v201 offset:31744\n"
    "v_add_f32 v114, v114, v67\n"
    "v_mfma_f32_16x16x32_bf16 a[20:23], v[172:175], v[148:151], a[20:23]\n"
    "v_exp_f32 v40, v40\n"
    "v_mfma_f32_16x16x32_bf16 a[16:19], v[172:175], v[144:147], a[16:19]\n"
    "v_exp_f32 v41, v41\n"
    "v_mfma_f32_16x16x32_bf16 a[16:19], v[168:171], v[128:131], a[16:19]\n"
    "s_waitcnt lgkmcnt(2)\n"
    "v_exp_f32 v42, v42\n"
    "v_mfma_f32_16x16x32_bf16 a[32:35], v[176:179], v[128:131], a[32:35]\n"
    "ds_read_b128 v[168:171], v201 offset:32768\n"
    "v_add_f32 v117, v117, v68\n"
    "v_mfma_f32_16x16x32_bf16 a[32:35], v[180:183], v[144:147], a[32:35]\n"
    "v_exp_f32 v43, v43\n"
    "v_mfma_f32_16x16x32_bf16 a[36:39], v[180:183], v[148:151], a[36:39]\n"
    "v_exp_f32 v44, v44\n"
    "v_mfma_f32_16x16x32_bf16 a[36:39], v[176:179], v[132:135], a[36:39]\n"
    "v_exp_f32 v45, v45\n"
    "v_mfma_f32_16x16x32_bf16 a[40:43], v[176:179], v[136:139], a[40:43]\n"
    "ds_read_b128 v[172:175], v201 offset:33792\n"
    "v_add_f32 v118, v118, v69\n"
    "v_mfma_f32_16x16x32_bf16 a[40:43], v[180:183], v[152:155], a[40:43]\n"
    "v_exp_f32 v46, v46\n"
    "v_mfma_f32_16x16x32_bf16 a[44:47], v[180:183], v[156:159], a[44:47]\n"
    "v_exp_f32 v47, v47\n"
    "v_mfma_f32_16x16x32_bf16 a[44:47], v[176:179], v[140:143], a[44:47]\n"
    "s_waitcnt lgkmcnt(2)\n"
    "v_exp_f32 v48, v48\n"
    "v_mfma_f32_16x16x32_bf16 a[60:63], v[160:163], v[140:143], a[60:63]\n"
    "ds_read_b128 v[176:179], v201 offset:34816\n"
    "v_add_f32 v117, v117, v70\n"
    "v_mfma_f32_16x16x32_bf16 a[60:63], v[164:167], v[156:159], a[60:63]\n"
    "v_exp_f32 v49, v49\n"
    "v_mfma_f32_16x16x32_bf16 a[56:59], v[164:167], v[152:155], a[56:59]\n"
    "v_exp_f32 v50, v50\n"
    "v_mfma_f32_16x16x32_bf16 a[56:59], v[160:163], v[136:139], a[56:59]\n"
    "v_exp_f32 v51, v51\n"
    "v_mfma_f32_16x16x32_bf16 a[52:55], v[160:163], v[132:135], a[52:55]\n"
    "ds_read_b128 v[180:183], v201 offset:35840\n"
    "v_add_f32 v113, v113, v32\n"
    "v_mfma_f32_16x16x32_bf16 a[52:55], v[164:167], v[148:151], a[52:55]\n"
    "v_exp_f32 v52, v52\n"
    "v_mfma_f32_16x16x32_bf16 a[48:51], v[164:167], v[144:147], a[48:51]\n"
    "v_add_f32 v118, v118, v71\n"
    "v_add_f32 v114, v114, v33\n"
    "v_mfma_f32_16x16x32_bf16 a[48:51], v[160:163], v[128:131], a[48:51]\n"
    "s_waitcnt lgkmcnt(2)\n"
    "v_add_f32 v121, v121, v72\n"
    "v_add_f32 v113, v113, v34\n"
    "v_mfma_f32_16x16x32_bf16 a[64:67], v[168:171], v[128:131], a[64:67]\n"
    "ds_read_b128 v[160:163], v201 offset:36864\n"
    "v_add_f32 v122, v122, v73\n"
    "v_mfma_f32_16x16x32_bf16 a[64:67], v[172:175], v[144:147], a[64:67]\n"
    "v_exp_f32 v53, v53\n"
    "v_mfma_f32_16x16x32_bf16 a[68:71], v[172:175], v[148:151], a[68:71]\n"
    "v_add_f32 v114, v114, v35\n"
    "v_add_f32 v121, v121, v74\n"
    "v_mfma_f32_16x16x32_bf16 a[68:71], v[168:171], v[132:135], a[68:71]\n"
    "s_add_u32 m0, s51, s95\n"
    "v_add_f32 v117, v117, v36\n"
    "v_add_f32 v122, v122, v75\n"
    "v_mfma_f32_16x16x32_bf16 a[72:75], v[168:171], v[136:139], a[72:75]\n"
    "ds_read_b128 v[164:167], v201 offset:37888\n"
    "v_add_f32 v118, v118, v37\n"
    "v_mfma_f32_16x16x32_bf16 a[72:75], v[172:175], v[152:155], a[72:75]\n"
    "buffer_load_dwordx4 v202, s[32:35], s64 offen lds\n"
    "v_mfma_f32_16x16x32_bf16 a[76:79], v[172:175], v[156:159], a[76:79]\n"
    "v_exp_f32 v54, v54\n"
    "v_mfma_f32_16x16x32_bf16 a[76:79], v[168:171], v[140:143], a[76:79]\n"
    "s_waitcnt lgkmcnt(2)\n"
    "v_add_f32 v125, v125, v76\n"
    "v_add_f32 v117, v117, v38\n"
    "v_mfma_f32_16x16x32_bf16 a[92:95], v[176:179], v[140:143], a[92:95]\n"
    "ds_read_b128 v[168:171], v201 offset:38912\n"
    "v_add_f32 v126, v126, v77\n"
    "v_mfma_f32_16x16x32_bf16 a[92:95], v[180:183], v[156:159], a[92:95]\n"
    "buffer_load_dwordx4 v203, s[32:35], s64 offen offset:1024 lds\n"
    "v_mfma_f32_16x16x32_bf16 a[88:91], v[180:183], v[152:155], a[88:91]\n"
    "v_exp_f32 v55, v55\n"
    "v_mfma_f32_16x16x32_bf16 a[88:91], v[176:179], v[136:139], a[88:91]\n"
    "v_add_f32 v118, v118, v39\n"
    "v_add_f32 v125, v125, v78\n"
    "v_mfma_f32_16x16x32_bf16 a[84:87], v[176:179], v[132:135], a[84:87]\n"
    "ds_read_b128 v[172:175], v201 offset:39936\n"
    "v_add_f32 v121, v121, v40\n"
    "v_mfma_f32_16x16x32_bf16 a[84:87], v[180:183], v[148:151], a[84:87]\n"
    "buffer_load_dwordx4 v204, s[32:35], s64 offen offset:2048 lds\n"
    "v_mfma_f32_16x16x32_bf16 a[80:83], v[180:183], v[144:147], a[80:83]\n"
    "v_exp_f32 v56, v56\n"
    "v_mfma_f32_16x16x32_bf16 a[80:83], v[176:179], v[128:131], a[80:83]\n"
    "s_waitcnt lgkmcnt(2)\n"
    "v_add_f32 v126, v126, v79\n"
    "v_add_f32 v122, v122, v41\n"
    "v_mfma_f32_16x16x32_bf16 a[96:99], v[160:163], v[128:131], a[96:99]\n"
    "s_mov_b32 s95, s94\n"
    "ds_read_b128 v[176:179], v201 offset:40960\n"
    "v_add_f32 v113, v113, v80\n"
    "v_mfma_f32_16x16x32_bf16 a[96:99], v[164:167], v[144:147], a[96:99]\n"
    "s_sub_u32 s94, s94, 0x3000\n"
    "v_add_f32 v121, v121, v42\n"
    "v_add_f32 v114, v114, v81\n"
    "v_mfma_f32_16x16x32_bf16 a[100:103], v[164:167], v[148:151], a[100:103]\n"
    "s_min_u32 s94, s94, 0x6000\n"
    "v_exp_f32 v57, v57\n"
    "v_mfma_f32_16x16x32_bf16 a[100:103], v[160:163], v[132:135], a[100:103]\n"
    "s_sub_u32 s93, s94, s95\n"
    "v_add_f32 v122, v122, v43\n"
    "v_add_f32 v113, v113, v82\n"
    "v_mfma_f32_16x16x32_bf16 a[104:107], v[160:163], v[136:139], a[104:107]\n"
    "ds_read_b128 v[180:183], v201 offset:41984\n"
    "v_add_f32 v125, v125, v44\n"
    "v_mfma_f32_16x16x32_bf16 a[104:107], v[164:167], v[152:155], a[104:107]\n"
    "v_exp_f32 v58, v58\n"
    "v_mfma_f32_16x16x32_bf16 a[108:111], v[164:167], v[156:159], a[108:111]\n"
    "v_add_f32 v114, v114, v83\n"
    "v_add_f32 v126, v126, v45\n"
    "v_mfma_f32_16x16x32_bf16 a[108:111], v[160:163], v[140:143], a[108:111]\n"
    "s_waitcnt lgkmcnt(2)\n"
    "v_add_f32 v117, v117, v84\n"
    "v_add_f32 v125, v125, v46\n"
    "v_mfma_f32_16x16x32_bf16 a[124:127], v[168:171], v[140:143], a[124:127]\n"
    "ds_read_b128 v[160:163], v201 offset:43008\n"
    "v_add_f32 v118, v118, v85\n"
    "v_mfma_f32_16x16x32_bf16 a[124:127], v[172:175], v[156:159], a[124:127]\n"
    "v_exp_f32 v59, v59\n"
    "v_mfma_f32_16x16x32_bf16 a[120:123], v[172:175], v[152:155], a[120:123]\n"
    "v_add_f32 v126, v126, v47\n"
    "v_add_f32 v117, v117, v86\n"
    "v_mfma_f32_16x16x32_bf16 a[120:123], v[168:171], v[136:139], a[120:123]\n"
    "v_add_f32 v113, v113, v48\n"
    "v_add_u32 v205, s93, v205\n"
    "v_mfma_f32_16x16x32_bf16 a[116:119], v[168:171], v[132:135], a[116:119]\n"
    "ds_read_b128 v[164:167], v201 offset:44032\n"
    "v_add_f32 v118, v118, v87\n"
    "v_mfma_f32_16x16x32_bf16 a[116:119], v[172:175], v[148:151], a[116:119]\n"
    "v_exp_f32 v60, v60\n"
    "v_mfma_f32_16x16x32_bf16 a[112:115], v[172:175], v[144:147], a[112:115]\n"
    "v_add_f32 v114, v114, v49\n"
    "v_add_u32 v206, s93, v206\n"
    "v_mfma_f32_16x16x32_bf16 a[112:115], v[168:171], v[128:131], a[112:115]\n"
    "s_waitcnt lgkmcnt(2)\n"
    "v_add_f32 v121, v121, v88\n"
    "v_add_f32 v113, v113, v50\n"
    "v_mfma_f32_16x16x32_bf16 a[128:131], v[176:179], v[128:131], a[128:131]\n"
    "ds_read_b128 v[168:171], v201 offset:45056\n"
    "v_add_u32 v207, s93, v207\n"
    "v_mfma_f32_16x16x32_bf16 a[128:131], v[180:183], v[144:147], a[128:131]\n"
    "v_exp_f32 v61, v61\n"
    "v_mfma_f32_16x16x32_bf16 a[132:135], v[180:183], v[148:151], a[132:135]\n"
    "s_add_u32 s64, s64, 0xc000\n"
    "v_add_f32 v122, v122, v89\n"
    "v_add_f32 v114, v114, v51\n"
    "v_mfma_f32_16x16x32_bf16 a[132:135], v[176:179], v[132:135], a[132:135]\n"
    "s_add_u32 s62, s62, 0x6000\n"
    "v_add_f32 v121, v121, v90\n"
    "v_add_f32 v117, v117, v52\n"
    "v_mfma_f32_16x16x32_bf16 a[136:139], v[176:179], v[136:139], a[136:139]\n"
    "s_add_u32 s63, s63, 0x6000\n"
    "ds_read_b128 v[172:175], v201 offset:46080\n"
    "v_add_f32 v122, v122, v91\n"
    "v_mfma_f32_16x16x32_bf16 a[136:139], v[180:183], v[152:155], a[136:139]\n"
    "s_add_u32 s60, s60, 0x6000\n"
    "v_exp_f32 v62, v62\n"
    "v_mfma_f32_16x16x32_bf16 a[140:143], v[180:183], v[156:159], a[140:143]\n"
    "s_add_u32 s61, s61, 0x6000\n"
    "v_add_f32 v118, v118, v53\n"
    "v_add_f32 v125, v125, v92\n"
    "v_mfma_f32_16x16x32_bf16 a[140:143], v[176:179], v[140:143], a[140:143]\n"
    "s_waitcnt lgkmcnt(2)\n"
    "v_add_f32 v117, v117, v54\n"
    "v_add_f32 v126, v126, v93\n"
    "v_mfma_f32_16x16x32_bf16 a[156:159], v[160:163], v[140:143], a[156:159]\n"
    "ds_read_b128 v[176:179], v201 offset:47104\n"
    "v_add_f32 v118, v118, v55\n"
    "v_mfma_f32_16x16x32_bf16 a[156:159], v[164:167], v[156:159], a[156:159]\n"
    "s_waitcnt vmcnt(3)\n"
    "ds_read_b128 v[180:183], v201 offset:48128\n"
    "v_add_f32 v125, v125, v94\n"
    "v_mfma_f32_16x16x32_bf16 a[152:155], v[164:167], v[152:155], a[152:155]\n"
    "v_exp_f32 v63, v63\n"
    "v_mfma_f32_16x16x32_bf16 a[152:155], v[160:163], v[136:139], a[152:155]\n"
    "v_add_f32 v121, v121, v56\n"
    "v_add_f32 v126, v126, v95\n"
    "v_mfma_f32_16x16x32_bf16 a[148:151], v[160:163], v[132:135], a[148:151]\n"
    "v_add_f32 v122, v122, v57\n"
    "v_mfma_f32_16x16x32_bf16 a[148:151], v[164:167], v[148:151], a[148:151]\n"
    "v_mfma_f32_16x16x32_bf16 a[144:147], v[164:167], v[144:147], a[144:147]\n"
    "v_mfma_f32_16x16x32_bf16 a[144:147], v[160:163], v[128:131], a[144:147]\n"
    "s_waitcnt lgkmcnt(0)\n"
    "v_mfma_f32_16x16x32_bf16 a[160:163], v[168:171], v[128:131], a[160:163]\n"
    "s_barrier\n"
    "LMID1:\n"
    "v_mfma_f32_16x16x32_bf16 a[160:163], v[172:175], v[144:147], a[160:163]\n"
    "ds_read_u16_d16_hi v223, v209 offset:6336\n"
    "ds_read_u16_d16_hi v222, v209 offset:192\n"
    "v_mfma_f32_16x16x32_bf16 a[164:167], v[172:175], v[148:151], a[164:167]\n"
    "ds_read_b128 v[160:163], v200 offset:24576\n"
    "v_add_f32 v121, v121, v58\n"
    "v_mfma_f32_16x16x32_bf16 a[164:167], v[168:171], v[132:135], a[164:167]\n"
    "ds_read_b128 v[232:235], v205 offset:24576\n"
    "v_add_f32 v122, v122, v59\n"
    "v_mfma_f32_16x16x32_bf16 a[168:171], v[168:171], v[136:139], a[168:171]\n"
    "ds_read_b128 v[164:167], v200 offset:25600\n"
    "ds_read_b128 v[236:239], v206 offset:24576\n"
    "v_mfma_f32_16x16x32_bf16 a[168:171], v[172:175], v[152:155], a[168:171]\n"
    "v_add_f32 v125, v125, v60\n"
    "v_add_f32 v126, v126, v61\n"
    "v_mfma_f32_16x16x32_bf16 a[172:175], v[172:175], v[156:159], a[172:175]\n"
    "v_add_f32 v125, v125, v62\n"
    "v_add_f32 v126, v126, v63\n"
    "v_mfma_f32_16x16x32_bf16 a[172:175], v[168:171], v[140:143], a[172:175]\n"
    "ds_read_b64 v[228:229], v210 offset:64\n"
    "ds_read2st64_b32 v[240:241], v207 offset0:96 offset1:99\n"
    "v_mfma_f32_16x16x32_bf16 a[188:191], v[176:179], v[140:143], a[188:191]\n"
    "ds_read_b128 v[168:171], v200 offset:26624\n"
    "ds_read2st64_b32 v[242:243], v207 offset0:102 offset1:105\n"
    "v_mfma_f32_16x16x32_bf16 a[188:191], v[180:183], v[156:159], a[188:191]\n"
    "ds_read2st64_b32 v[244:245], v207 offset0:108 offset1:111\n"
    "ds_read2st64_b32 v[246:247], v207 offset0:114 offset1:117\n"
    "v_mfma_f32_16x16x32_bf16 a[184:187], v[176:179], v[136:139], a[184:187]\n"
    "v_cvt_pk_bf16_f32 v140, v44, v45\n"
    "v_cvt_pk_bf16_f32 v141, v46, v47\n"
    "v_mfma_f32_16x16x32_bf16 a[184:187], v[180:183], v[152:155], a[184:187]\n"
    "s_waitcnt lgkmcnt(6)\n"
    "v_mul_f32 v226, s75, v223\n"
    "v_mfma_f32_16x16x32_bf16 a[180:183], v[176:179], v[132:135], a[180:183]\n"
    "s_add_u32 s59, s59, 1\n"
    "ds_read_b128 v[172:175], v200 offset:27648\n"
    "v_fma_f32 v227, -v222, v226, v217\n"
    "v_mfma_f32_16x16x32_bf16 a[180:183], v[180:183], v[148:151], a[180:183]\n"
    "v_cvt_f32_i32 v232, v232\n"
    "v_cvt_f32_i32 v233, v233\n"
    "v_mfma_f32_16x16x32_bf16 a[176:179], v[176:179], v[128:131], a[176:179]\n"
    "s_cmp_lt_u32 s59, 15\n"
    "v_cvt_f32_i32 v234, v234\n"
    "v_cvt_f32_i32 v235, v235\n"
    "v_mfma_f32_16x16x32_bf16 a[176:179], v[180:183], v[144:147], a[176:179]\n"
    "s_cbranch_scc1 LLOOP1\n"
    "s_branch LTAIL\n"
    "LENT0:\n"
    "s_mov_b32 s59, 1\n"
    "s_add_u32 m0, s72, 0x21000\n"
    "s_nop 0\n"
    "buffer_load_dwordx4 v250, s[48:51], 0 offen offset:2048 lds\n"
    "s_add_u32 m0, s72, 0x18000\n"
    "s_nop 0\n"
    "buffer_load_dwordx4 v202, s[32:35], s64 offen lds\n"
    "buffer_load_dwordx4 v203, s[32:35], s64 offen offset:1024 lds\n"
    "buffer_load_dwordx4 v204, s[32:35], s64 offen offset:2048 lds\n"
    "s_add_u32 s64, s64, 0xc000\n"
    "s_mov_b32 s94, 0x3000\n"
    "s_mov_b32 s95, 0x6000\n"
    "s_add_u32 s51, s72, 0x18000\n"
    "v_add_u32 v205, 0x3000, v205\n"
    "v_add_u32 v206, 0x3000, v206\n"
    "v_add_u32 v207, 0x3000, v207\n"
    "buffer_store_dwordx2 v[220:221], v214, s[44:47], 0 offen sc1\n"
    "ds_read_b128 v[160:163], v200 offset:24576\n"
    "ds_read_u16_d16_hi v223, v209 offset:6336\n"
    "ds_read_u16_d16_hi v222, v209 offset:192\n"
    "v_add_f32 v121, v121, v58\n"
    "ds_read_b128 v[164:167], v200 offset:25600\n"
    "ds_read_b128 v[232:235], v205 offset:24576\n"
    "v_add_f32 v122, v122, v59\n"
    "ds_read_b128 v[236:239], v206 offset:24576\n"
    "v_add_f32 v125, v125, v60\n"
    "v_add_f32 v126, v126, v61\n"
    "v_add_f32 v125, v125, v62\n"
    "v_add_f32 v126, v126, v63\n"
    "ds_read_b128 v[168:171], v200 offset:26624\n"
    "ds_read_b64 v[228:229], v210 offset:64\n"
    "ds_read2st64_b32 v[240:241], v207 offset0:96 offset1:99\n"
    "ds_read2st64_b32 v[242:243], v207 offset0:102 offset1:105\n"
    "ds_read2st64_b32 v[244:245], v207 offset0:108 offset1:111\n"
    "ds_read2st64_b32 v[246:247], v207 offset0:114 offset1:117\n"
    "v_cvt_pk_bf16_f32 v140, v44, v45\n"
    "v_cvt_pk_bf16_f32 v141, v46, v47\n"
    "ds_read_b128 v[172:175], v200 offset:27648\n"
    "s_waitcnt lgkmcnt(7)\n"
    "v_mul_f32 v226, s75, v223\n"
    "v_fma_f32 v227, -v222, v226, v217\n"
    "v_cvt_f32_i32 v232, v232\n"
    "v_cvt_f32_i32 v233, v233\n"
    "s_nop 7\n"
    ".p2align 6\n"
    "LLOOP:\n"
    "v_mfma_f32_16x16x32_bf16 v[64:67], v[160:163], a[192:195], v[96:99]\n"
    "ds_read_b128 v[176:179], v200 offset:28672\n"
    "v_cvt_f32_i32 v234, v234\n"
    "v_mfma_f32_16x16x32_bf16 v[64:67], v[164:167], a[196:199], v[64:67]\n"
    "s_waitcnt lgkmcnt(1)\n"
    "v_cvt_f32_i32 v235, v235\n"
    "v_cvt_f32_i32 v236, v236\n"
    "v_mfma_f32_16x16x32_bf16 v[64:67], v[168:171], a[200:203], v[64:67]\n"
    "v_cvt_f32_i32 v237, v237\n"
    "v_cvt_f32_i32 v238, v238\n"
    "v_mfma_f32_16x16x32_bf16 v[68:71], v[168:171], a[224:227], v[100:103]\n"
    "v_cvt_f32_i32 v239, v239\n"
    "v_mul_f32_dpp v232, v226, v232 row_newbcast:0 row_mask:0xf bank_mask:0xf\n"
    "v_mfma_f32_16x16x32_bf16 v[68:71], v[164:167], a[220:223], v[68:71]\n"
    "ds_read_b128 v[180:183], v200 offset:29696\n"
    "v_mul_f32_dpp v233, v226, v233 row_newbcast:1 row_mask:0xf bank_mask:0xf\n"
    "v_mfma_f32_16x16x32_bf16 v[68:71], v[160:163], a[216:219], v[68:71]\n"
    "v_mul_f32_dpp v234, v226, v234 row_newbcast:2 row_mask:0xf bank_mask:0xf\n"
    "v_mul_f32_dpp v235, v226, v235 row_newbcast:3 row_mask:0xf bank_mask:0xf\n"
    "v_mfma_f32_16x16x32_bf16 v[72:75], v[160:163], a[240:243], v[104:107]\n"
    "v_mul_f32_dpp v236, v226, v236 row_newbcast:4 row_mask:0xf bank_mask:0xf\n"
    "v_mul_f32_dpp v237, v226, v237 row_newbcast:5 row_mask:0xf bank_mask:0xf\n"
    "v_mfma_f32_16x16x32_bf16 v[72:75], v[164:167], a[244:247], v[72:75]\n"
    "v_mul_f32_dpp v238, v226, v238 row_newbcast:6 row_mask:0xf bank_mask:0xf\n"
    "v_mul_f32_dpp v239, v226, v239 row_newbcast:7 row_mask:0xf bank_mask:0xf\n"
    "v_mfma_f32_16x16x32_bf16 v[72:75], v[168:171], a[248:251], v[72:75]\n"
    "v_add_f32_dpp v232, v227, v232 row_newbcast:0 row_mask:0xf bank_mask:0xf\n"
    "v_add_f32_dpp v233, v227, v233 row_newbcast:1 row_mask:0xf bank_mask:0xf\n"
    "v_mfma_f32_16x16x32_bf16 v[76:79], v[168:171], v[16:19], v[108:111]\n"
    "v_add_f32_dpp v234, v227, v234 row_newbcast:2 row_mask:0xf bank_mask:0xf\n"
    "v_add_f32_dpp v235, v227, v235 row_newbcast:3 row_mask:0xf bank_mask:0xf\n"
    "v_mfma_f32_16x16x32_bf16 v[76:79], v[164:167], v[12:15], v[76:79]\n"
    "v_add_f32_dpp v236, v227, v236 row_newbcast:4 row_mask:0xf bank_mask:0xf\n"
    "v_add_f32_dpp v237, v227, v237 row_newbcast:5 row_mask:0xf bank_mask:0xf\n"
    "v_mfma_f32_16x16x32_bf16 v[76:79], v[160:163], v[8:11], v[76:79]\n"
    "s_waitcnt lgkmcnt(1)\n"
    "v_add_f32_dpp v238, v227, v238 row_newbcast:6 row_mask:0xf bank_mask:0xf\n"
    "v_add_f32_dpp v239, v227, v239 row_newbcast:7 row_mask:0xf bank_mask:0xf\n"
    "v_mfma_f32_16x16x32_bf16 v[76:79], v[172:175], v[20:23], v[76:79]\n"
    "ds_read_b128 v[160:163], v200 offset:33792\n"
    "v_cvt_pk_bf16_f32 v232, v232, v233\n"
    "v_mfma_f32_16x16x32_bf16 v[76:79], v[176:179], v[24:27], v[76:79]\n"
    "s_waitcnt lgkmcnt(1)\n"
    "v_cvt_f32_i32 v240, v240\n"
    "v_cvt_pk_bf16_f32 v233, v234, v235\n"
    "v_mfma_f32_16x16x32_bf16 v[76:79], v[180:183], v[28:31], v[76:79]\n"
    "v_cvt_f32_i32 v241, v241\n"
    "v_cvt_pk_bf16_f32 v234, v236, v237\n"
    "v_mfma_f32_16x16x32_bf16 v[72:75], v[180:183], v[4:7], v[72:75]\n"
    "ds_read_b128 v[164:167], v200 offset:34816\n"
    "v_cvt_f32_i32 v242, v242\n"
    "v_mfma_f32_16x16x32_bf16 v[72:75], v[176:179], v[0:3], v[72:75]\n"
    "v_cvt_pk_bf16_f32 v235, v238, v239\n"
    "v_cvt_f32_i32 v243, v243\n"
    "v_mfma_f32_16x16x32_bf16 v[72:75], v[172:175], a[252:255], v[72:75]\n"
    "v_cvt_f32_i32 v244, v244\n"
    "v_cvt_f32_i32 v245, v245\n"
    "v_mfma_f32_16x16x32_bf16 v[68:71], v[172:175], a[228:231], v[68:71]\n"
    "ds_read_b128 v[168:171], v200 offset:35840\n"
    "v_cvt_f32_i32 v246, v246\n"
    "v_mfma_f32_16x16x32_bf16 v[68:71], v[176:179], a[232:235], v[68:71]\n"
    "v_cvt_f32_i32 v247, v247\n"
    "v_mul_f32_dpp v240, v228, v240 row_newbcast:0 row_mask:0xf bank_mask:0xf\n"
    "v_mfma_f32_16x16x32_bf16 v[68:71], v[180:183], a[236:239], v[68:71]\n"
    "v_mul_f32_dpp v241, v228, v241 row_newbcast:1 row_mask:0xf bank_mask:0xf\n"
    "v_mul_f32_dpp v242, v228, v242 row_newbcast:2 row_mask:0xf bank_mask:0xf\n"
    "v_mfma_f32_16x16x32_bf16 v[64:67], v[180:183], a[212:215], v[64:67]\n"
    "v_mul_f32_dpp v243, v228, v243 row_newbcast:3 row_mask:0xf bank_mask:0xf\n"
    "v_mul_f32_dpp v244, v228, v244 row_newbcast:4 row_mask:0xf bank_mask:0xf\n"
    "v_mfma_f32_16x16x32_bf16 v[64:67], v[176:179], a[208:211], v[64:67]\n"
    "buffer_store_dwordx4 v[232:235], v211, s[36:39], s62 offen sc1\n"
    "v_mul_f32_dpp v245, v228, v245 row_newbcast:5 row_mask:0xf bank_mask:0xf\n"
    "v_mul_f32_dpp v246, v228, v246 row_newbcast:6 row_mask:0xf bank_mask:0xf\n"
    "v_mul_f32_dpp v247, v228, v247 row_newbcast:7 row_mask:0xf bank_mask:0xf\n"
    "v_mfma_f32_16x16x32_bf16 v[64:67], v[172:175], a[204:207], v[64:67]\n"
    "s_waitcnt lgkmcnt(1)\n"
    "v_add_f32_dpp v240, v229, v240 row_newbcast:0 row_mask:0xf bank_mask:0xf\n"
    "v_add_f32_dpp v241, v229, v241 row_newbcast:1 row_mask:0xf bank_mask:0xf\n"
    "v_mfma_f32_16x16x32_bf16 v[80:83], v[160:163], a[204:207], v[96:99]\n"
    "ds_read_b128 v[172:175], v200 offset:30720\n"
    "v_add_f32_dpp v242, v229, v242 row_newbcast:2 row_mask:0xf bank_mask:0xf\n"
    "v_mfma_f32_16x16x32_bf16 v[80:83], v[164:167], a[208:211], v[80:83]\n"
    "s_waitcnt lgkmcnt(1)\n"
    "v_add_f32_dpp v243, v229, v243 row_newbcast:3 row_mask:0xf bank_mask:0xf\n"
    "v_add_f32_dpp v244, v229, v244 row_newbcast:4 row_mask:0xf bank_mask:0xf\n"
    "v_mfma_f32_16x16x32_bf16 v[80:83], v[168:171], a[212:215], v[80:83]\n"
    "v_add_f32_dpp v245, v229, v245 row_newbcast:5 row_mask:0xf bank_mask:0xf\n"
    "v_add_f32_dpp v246, v229, v246 row_newbcast:6 row_mask:0xf bank_mask:0xf\n"
    "v_mfma_f32_16x16x32_bf16 v[84:87], v[168:171], a[236:239], v[100:103]\n"
    "ds_read_b128 v[176:179], v200 offset:31744\n"
    "v_add_f32_dpp v247, v229, v247 row_newbcast:7 row_mask:0xf bank_mask:0xf\n"
    "v_mfma_f32_16x16x32_bf16 v[84:87], v[164:167], a[232:235], v[84:87]\n"
    "v_cvt_pk_bf16_f32 v240, v240, v241\n"
    "v_cvt_pk_bf16_f32 v241, v242, v243\n"
    "v_mfma_f32_16x16x32_bf16 v[84:87], v[160:163], a[228:231], v[84:87]\n"
    "v_cvt_pk_bf16_f32 v242, v244, v245\n"
    "v_cvt_pk_bf16_f32 v243, v246, v247\n"
    "v_mfma_f32_16x16x32_bf16 v[88:91], v[160:163], a[252:255], v[104:107]\n"
    "ds_read_b128 v[180:183], v200 offset:32768\n"
    "v_cvt_pk_bf16_f32 v136, v40, v41\n"
    "v_mfma_f32_16x16x32_bf16 v[88:91], v[164:167], v[0:3], v[88:91]\n"
    "v_cvt_pk_bf16_f32 v137, v42, v43\n"
    "v_cvt_pk_bf16_f32 v132, v36, v37\n"
    "v_mfma_f32_16x16x32_bf16 v[88:91], v[168:171], v[4:7], v[88:91]\n"
    "v_cvt_pk_bf16_f32 v133, v38, v39\n"
    "v_cvt_pk_bf16_f32 v128, v32, v33\n"
    "v_mfma_f32_16x16x32_bf16 v[92:95], v[168:171], v[28:31], v[108:111]\n"
    "v_cvt_pk_bf16_f32 v129, v34, v35\n"
    "v_cvt_pk_bf16_f32 v142, v60, v61\n"
    "v_mfma_f32_16x16x32_bf16 v[92:95], v[164:167], v[24:27], v[92:95]\n"
    "v_exp_f32 v64, v64\n"
    "v_mfma_f32_16x16x32_bf16 v[92:95], v[160:163], v[20:23], v[92:95]\n"
    "s_waitcnt lgkmcnt(1)\n"
    "v_exp_f32 v65, v65\n"
    "v_mfma_f32_16x16x32_bf16 v[92:95], v[172:175], v[8:11], v[92:95]\n"
    "ds_read_b128 v[160:163], v200 offset:36864\n"
    "v_cvt_pk_bf16_f32 v143, v62, v63\n"
    "v_mfma_f32_16x16x32_bf16 v[92:95], v[176:179], v[12:15], v[92:95]\n"
    "s_waitcnt lgkmcnt(1)\n"
    "v_exp_f32 v66, v66\n"
    "v_mfma_f32_16x16x32_bf16 v[92:95], v[180:183], v[16:19], v[92:95]\n"
    "buffer_store_dwordx4 v[240:243], v212, s[40:43], s63 offen sc1\n"
    "v_exp_f32 v67, v67\n"
    "v_cvt_pk_bf16_f32 v138, v56, v57\n"
    "v_mfma_f32_16x16x32_bf16 v[88:91], v[180:183], a[248:251], v[88:91]\n"
    "ds_read_b128 v[164:167], v200 offset:37888\n"
    "v_cvt_pk_bf16_f32 v139, v58, v59\n"
    "v_mfma_f32_16x16x32_bf16 v[88:91], v[176:179], a[244:247], v[88:91]\n"
    "v_exp_f32 v68, v68\n"
    "v_mfma_f32_16x16x32_bf16 v[88:91], v[172:175], a[240:243], v[88:91]\n"
    "v_exp_f32 v69, v69\n"
    "v_mfma_f32_16x16x32_bf16 v[84:87], v[172:175], a[216:219], v[84:87]\n"
    "ds_read_b128 v[168:171], v200 offset:38912\n"
    "v_cvt_pk_bf16_f32 v134, v52, v53\n"
    "v_mfma_f32_16x16x32_bf16 v[84:87], v[176:179], a[220:223], v[84:87]\n"
    "v_exp_f32 v70, v70\n"
    "v_mfma_f32_16x16x32_bf16 v[84:87], v[180:183], a[224:227], v[84:87]\n"
    "v_exp_f32 v71, v71\n"
    "v_mfma_f32_16x16x32_bf16 v[80:83], v[180:183], a[200:203], v[80:83]\n"
    "v_exp_f32 v72, v72\n"
    "v_mfma_f32_16x16x32_bf16 v[80:83], v[176:179], a[196:199], v[80:83]\n"
    "v_cvt_pk_bf16_f32 v135, v54, v55\n"
    "v_cvt_pk_bf16_f32 v130, v48, v49\n"
    "v_mfma_f32_16x16x32_bf16 v[80:83], v[172:175], a[192:195], v[80:83]\n"
    "s_waitcnt lgkmcnt(1)\n"
    "v_exp_f32 v73, v73\n"
    "v_mfma_f32_16x16x32_bf16 v[32:35], v[160:163], a[192:195], v[96:99]\n"
    "ds_read_b128 v[172:175], v200 offset:39936\n"
    "v_cvt_pk_bf16_f32 v131, v50, v51\n"
    "v_mfma_f32_16x16x32_bf16 v[32:35], v[164:167], a[196:199], v[32:35]\n"
    "s_waitcnt lgkmcnt(1)\n"
    "v_exp_f32 v74, v74\n"
    "v_mfma_f32_16x16x32_bf16 v[32:35], v[168:171], a[200:203], v[32:35]\n"
    "v_exp_f32 v75, v75\n"
    "v_mfma_f32_16x16x32_bf16 v[36:39], v[168:171], a[224:227], v[100:103]\n"
    "ds_read_b128 v[176:179], v200 offset:40960\n"
    "v_cvt_pk_bf16_f32 v144, v64, v65\n"
    "v_mfma_f32_16x16x32_bf16 v[36:39], v[164:167], a[220:223], v[36:39]\n"
    "v_exp_f32 v76, v76\n"
    "v_mfma_f32_16x16x32_bf16 v[36:39], v[160:163], a[216:219], v[36:39]\n"
    "v_exp_f32 v77, v77\n"
    "v_mfma_f32_16x16x32_bf16 v[40:43], v[160:163], a[240:243], v[104:107]\n"
    "ds_read_b128 v[180:183], v200 offset:41984\n"
    "v_cvt_pk_bf16_f32 v145, v66, v67\n"
    "v_mfma_f32_16x16x32_bf16 v[40:43], v[164:167], a[244:247], v[40:43]\n"
    "v_exp_f32 v78, v78\n"
    "v_mfma_f32_16x16x32_bf16 v[40:43], v[168:171], a[248:251], v[40:43]\n"
    "v_exp_f32 v79, v79\n"
    "v_mfma_f32_16x16x32_bf16 v[44:47], v[168:171], v[16:19], v[108:111]\n"
    "v_exp_f32 v80, v80\n"
    "v_mfma_f32_16x16x32_bf16 v[44:47], v[164:167], v[12:15], v[44:47]\n"
    "v_exp_f32 v81, v81\n"
    "v_mfma_f32_16x16x32_bf16 v[44:47], v[160:163], v[8:11], v[44:47]\n"
    "s_waitcnt lgkmcnt(1)\n"
    "v_exp_f32 v82, v82\n"
    "v_mfma_f32_16x16x32_bf16 v[44:47], v[172:175], v[20:23], v[44:47]\n"
    "ds_read_b128 v[160:163], v200 offset:46080\n"
    "v_cvt_pk_bf16_f32 v148, v68, v69\n"
    "v_mfma_f32_16x16x32_bf16 v[44:47], v[176:179], v[24:27], v[44:47]\n"
    "s_waitcnt lgkmcnt(1)\n"
    "v_exp_f32 v83, v83\n"
    "v_mfma_f32_16x16x32_bf16 v[44:47], v[180:183], v[28:31], v[44:47]\n"
    "v_exp_f32 v84, v84\n"
    "v_mfma_f32_16x16x32_bf16 v[40:43], v[180:183], v[4:7], v[40:43]\n"
    "ds_read_b128 v[164:167], v200 offset:47104\n"
    "v_cvt_pk_bf16_f32 v149, v70, v71\n"
    "v_mfma_f32_16x16x32_bf16 v[40:43], v[176:179], v[0:3], v[40:43]\n"
    "v_exp_f32 v85, v85\n"
    "v_mfma_f32_16x16x32_bf16 v[40:43], v[172:175], a[252:255], v[40:43]\n"
    "v_exp_f32 v86, v86\n"
    "v_mfma_f32_16x16x32_bf16 v[36:39], v[172:175], a[228:231], v[36:39]\n"
    "ds_read_b128 v[168:171], v200 offset:48128\n"
    "v_cvt_pk_bf16_f32 v152, v72, v73\n"
    "v_mfma_f32_16x16x32_bf16 v[36:39], v[176:179], a[232:235], v[36:39]\n"
    "v_exp_f32 v87, v87\n"
    "v_mfma_f32_16x16x32_bf16 v[36:39], v[180:183], a[236:239], v[36:39]\n"
    "v_exp_f32 v88, v88\n"
    "v_mfma_f32_16x16x32_bf16 v[32:35], v[180:183], a[212:215], v[32:35]\n"
    "v_exp_f32 v89, v89\n"
    "v_mfma_f32_16x16x32_bf16 v[32:35], v[176:179], a[208:211], v[32:35]\n"
    "v_cvt_pk_bf16_f32 v153, v74, v75\n"
    "v_cvt_pk_bf16_f32 v156, v76, v77\n"
    "v_mfma_f32_16x16x32_bf16 v[32:35], v[172:175], a[204:207], v[32:35]\n"
    "s_waitcnt lgkmcnt(1)\n"
    "s_mov_b32 m0, 0\n"
    "v_cvt_pk_bf16_f32 v157, v78, v79\n"
    "v_mfma_f32_16x16x32_bf16 v[48:51], v[160:163], a[204:207], v[96:99]\n"
    "ds_read_b128 v[172:175], v200 offset:43008\n"
    "v_cvt_pk_bf16_f32 v146, v80, v81\n"
    "v_mfma_f32_16x16x32_bf16 v[48:51], v[164:167], a[208:211], v[48:51]\n"
    "s_waitcnt lgkmcnt(1)\n"
    "buffer_load_dwordx4 v200, s[36:39], s60 offen sc1 lds\n"
    "v_mfma_f32_16x16x32_bf16 v[48:51], v[168:171], a[212:215], v[48:51]\n"
    "v_exp_f32 v90, v90\n"
    "v_mfma_f32_16x16x32_bf16 v[52:55], v[168:171], a[236:239], v[100:103]\n"
    "ds_read_b128 v[176:179], v200 offset:44032\n"
    "v_cvt_pk_bf16_f32 v147, v82, v83\n"
    "v_mfma_f32_16x16x32_bf16 v[52:55], v[164:167], a[232:235], v[52:55]\n"
    "v_exp_f32 v91, v91\n"
    "v_mfma_f32_16x16x32_bf16 v[52:55], v[160:163], a[228:231], v[52:55]\n"
    "buffer_load_dwordx4 v200, s[36:39], s60 offen offset:1024 sc1 lds\n"
    "v_mfma_f32_16x16x32_bf16 v[56:59], v[160:163], a[252:255], v[104:107]\n"
    "ds_read_b128 v[180:183], v200 offset:45056\n"
    "v_cvt_pk_bf16_f32 v150, v84, v85\n"
    "v_mfma_f32_16x16x32_bf16 v[56:59], v[164:167], v[0:3], v[56:59]\n"
    "v_exp_f32 v92, v92\n"
    "v_mfma_f32_16x16x32_bf16 v[56:59], v[168:171], v[4:7], v[56:59]\n"
    "v_exp_f32 v93, v93\n"
    "v_mfma_f32_16x16x32_bf16 v[60:63], v[168:171], v[28:31], v[108:111]\n"
    "buffer_load_dwordx4 v200, s[36:39], s60 offen offset:2048 sc1 lds\n"
    "v_mfma_f32_16x16x32_bf16 v[60:63], v[164:167], v[24:27], v[60:63]\n"
    "v_cvt_pk_bf16_f32 v151, v86, v87\n"
    "v_cvt_pk_bf16_f32 v154, v88, v89\n"
    "v_mfma_f32_16x16x32_bf16 v[60:63], v[160:163], v[20:23], v[60:63]\n"
    "s_waitcnt lgkmcnt(1)\n"
    "s_add_u32 m0, s71, 0x12000\n"
    "v_cvt_pk_bf16_f32 v155, v90, v91\n"
    "v_mfma_f32_16x16x32_bf16 v[60:63], v[172:175], v[8:11], v[60:63]\n"
    "ds_read_b128 v[160:163], v201\n"
    "v_cvt_pk_bf16_f32 v158, v92, v93\n"
    "v_mfma_f32_16x16x32_bf16 v[60:63], v[176:179], v[12:15], v[60:63]\n"
    "s_waitcnt lgkmcnt(1)\n"
    "buffer_load_dwordx4 v200, s[40:43], s61 offen sc1 lds\n"
    "v_mfma_f32_16x16x32_bf16 v[60:63], v[180:183], v[16:19], v[60:63]\n"
    "v_exp_f32 v94, v94\n"
    "v_mfma_f32_16x16x32_bf16 v[56:59], v[180:183], a[248:251], v[56:59]\n"
    "ds_read_b128 v[164:167], v201 offset:1024\n"
    "v_add_f32 v113, v113, v64\n"
    "v_mfma_f32_16x16x32_bf16 v[56:59], v[176:179], a[244:247], v[56:59]\n"
    "v_exp_f32 v95, v95\n"
    "v_mfma_f32_16x16x32_bf16 v[56:59], v[172:175], a[240:243], v[56:59]\n"
    "buffer_load_dwordx4 v200, s[40:43], s61 offen offset:1024 sc1 lds\n"
    "v_mfma_f32_16x16x32_bf16 v[52:55], v[172:175], a[216:219], v[52:55]\n"
    "ds_read_b128 v[168:171], v201 offset:2048\n"
    "v_cvt_pk_bf16_f32 v159, v94, v95\n"
    "v_mfma_f32_16x16x32_bf16 v[52:55], v[176:179], a[220:223], v[52:55]\n"
    "v_exp_f32 v32, v32\n"
    "v_mfma_f32_16x16x32_bf16 v[52:55], v[180:183], a[224:227], v[52:55]\n"
    "v_exp_f32 v33, v33\n"
    "v_mfma_f32_16x16x32_bf16 v[48:51], v[180:183], a[200:203], v[48:51]\n"
    "buffer_load_dwordx4 v200, s[40:43], s61 offen offset:2048 sc1 lds\n"
    "v_mfma_f32_16x16x32_bf16 v[48:51], v[176:179], a[196:199], v[48:51]\n"
    "v_exp_f32 v34, v34\n"
    "v_mfma_f32_16x16x32_bf16 v[48:51], v[172:175], a[192:195], v[48:51]\n"
    "s_waitcnt lgkmcnt(1)\n"
    "v_exp_f32 v35, v35\n"
    "v_mfma_f32_16x16x32_bf16 a[0:3], v[160:163], v[128:131], a[0:3]\n"
    "ds_read_b128 v[172:175], v201 offset:3072\n"
    "ds_read_b128 v[176:179], v201 offset:4096\n"
    "v_mfma_f32_16x16x32_bf16 a[0:3], v[164:167], v[144:147], a[0:3]\n"
    "buffer_load_dwordx4 v200, s[40:43], s61 offen offset:3072 sc1 lds\n"
    "v_mfma_f32_16x16x32_bf16 a[4:7], v[160:163], v[132:135], a[4:7]\n"
    "s_add_u32 s65, s61, 0x1000\n"
    "v_exp_f32 v36, v36\n"
    "v_mfma_f32_16x16x32_bf16 a[4:7], v[164:167], v[148:151], a[4:7]\n"
    "s_add_u32 m0, s71, 0x13000\n"
    "v_exp_f32 v37, v37\n"
    "v_mfma_f32_16x16x32_bf16 a[8:11], v[160:163], v[136:139], a[8:11]\n"
    "ds_read_b128 v[180:183], v201 offset:5120\n"
    "v_add_f32 v114, v114, v65\n"
    "v_mfma_f32_16x16x32_bf16 a[8:11], v[164:167], v[152:155], a[8:11]\n"
    "buffer_load_dwordx4 v200, s[40:43], s65 offen sc1 lds\n"
    "v_mfma_f32_16x16x32_bf16 a[12:15], v[160:163], v[140:143], a[12:15]\n"
    "v_exp_f32 v38, v38\n"
    "v_mfma_f32_16x16x32_bf16 a[12:15], v[164:167], v[156:159], a[12:15]\n"
    "s_waitcnt lgkmcnt(2)\n"
    "v_exp_f32 v39, v39\n"
    "v_mfma_f32_16x16x32_bf16 a[28:31], v[168:171], v[140:143], a[28:31]\n"
    "ds_read_b128 v[160:163], v201 offset:6144\n"
    "v_add_f32 v113, v113, v66\n"
    "v_mfma_f32_16x16x32_bf16 a[28:31], v[172:175], v[156:159], a[28:31]\n"
    "buffer_load_dwordx4 v200, s[40:43], s65 offen offset:1024 sc1 lds\n"
    "v_mfma_f32_16x16x32_bf16 a[24:27], v[172:175], v[152:155], a[24:27]\n"
    "v_exp_f32 v40, v40\n"
    "v_mfma_f32_16x16x32_bf16 a[24:27], v[168:171], v[136:139], a[24:27]\n"
    "v_exp_f32 v41, v41\n"
    "v_mfma_f32_16x16x32_bf16 a[20:23], v[168:171], v[132:135], a[20:23]\n"
    "ds_read_b128 v[164:167], v201 offset:7168\n"
    "v_add_f32 v114, v114, v67\n"
    "v_mfma_f32_16x16x32_bf16 a[20:23], v[172:175], v[148:151], a[20:23]\n"
    "v_exp_f32 v42, v42\n"
    "v_mfma_f32_16x16x32_bf16 a[16:19], v[172:175], v[144:147], a[16:19]\n"
    "v_exp_f32 v43, v43\n"
    "v_mfma_f32_16x16x32_bf16 a[16:19], v[168:171], v[128:131], a[16:19]\n"
    "s_waitcnt lgkmcnt(2)\n"
    "v_exp_f32 v44, v44\n"
    "v_mfma_f32_16x16x32_bf16 a[32:35], v[176:179], v[128:131], a[32:35]\n"
    "ds_read_b128 v[168:171], v201 offset:8192\n"
    "v_add_f32 v117, v117, v68\n"
    "v_mfma_f32_16x16x32_bf16 a[32:35], v[180:183], v[144:147], a[32:35]\n"
    "buffer_load_dwordx2 v[218:219], v213, s[44:47], 0 offen sc1\n"
    "v_mfma_f32_16x16x32_bf16 a[36:39], v[180:183], v[148:151], a[36:39]\n"
    "v_exp_f32 v45, v45\n"
    "v_mfma_f32_16x16x32_bf16 a[36:39], v[176:179], v[132:135], a[36:39]\n"
    "v_exp_f32 v46, v46\n"
    "v_mfma_f32_16x16x32_bf16 a[40:43], v[176:179], v[136:139], a[40:43]\n"
    "ds_read_b128 v[172:175], v201 offset:9216\n"
    "v_add_f32 v118, v118, v69\n"
    "v_mfma_f32_16x16x32_bf16 a[40:43], v[180:183], v[152:155], a[40:43]\n"
    "v_exp_f32 v47, v47\n"
    "v_mfma_f32_16x16x32_bf16 a[44:47], v[180:183], v[156:159], a[44:47]\n"
    "v_exp_f32 v48, v48\n"
    "v_mfma_f32_16x16x32_bf16 a[44:47], v[176:179], v[140:143], a[44:47]\n"
    "s_waitcnt lgkmcnt(2)\n"
    "v_exp_f32 v49, v49\n"
    "v_mfma_f32_16x16x32_bf16 a[60:63], v[160:163], v[140:143], a[60:63]\n"
    "ds_read_b128 v[176:179], v201 offset:10240\n"
    "v_add_f32 v117, v117, v70\n"
    "v_mfma_f32_16x16x32_bf16 a[60:63], v[164:167], v[156:159], a[60:63]\n"
    "v_exp_f32 v50, v50\n"
    "v_mfma_f32_16x16x32_bf16 a[56:59], v[164:167], v[152:155], a[56:59]\n"
    "v_exp_f32 v51, v51\n"
    "v_mfma_f32_16x16x32_bf16 a[56:59], v[160:163], v[136:139], a[56:59]\n"
    "v_add_f32 v113, v113, v32\n"
    "v_add_f32 v118, v118, v71\n"
    "v_mfma_f32_16x16x32_bf16 a[52:55], v[160:163], v[132:135], a[52:55]\n"
    "ds_read_b128 v[180:183], v201 offset:11264\n"
    "v_add_f32 v114, v114, v33\n"
    "v_mfma_f32_16x16x32_bf16 a[52:55], v[164:167], v[148:151], a[52:55]\n"
    "v_exp_f32 v52, v52\n"
    "v_mfma_f32_16x16x32_bf16 a[48:51], v[164:167], v[144:147], a[48:51]\n"
    "v_add_f32 v121, v121, v72\n"
    "v_add_f32 v113, v113, v34\n"
    "v_mfma_f32_16x16x32_bf16 a[48:51], v[160:163], v[128:131], a[48:51]\n"
    "s_waitcnt lgkmcnt(2)\n"
    "v_exp_f32 v53, v53\n"
    "v_mfma_f32_16x16x32_bf16 a[64:67], v[168:171], v[128:131], a[64:67]\n"
    "ds_read_b128 v[160:163], v201 offset:12288\n"
    "v_add_f32 v122, v122, v73\n"
    "v_mfma_f32_16x16x32_bf16 a[64:67], v[172:175], v[144:147], a[64:67]\n"
    "v_add_f32 v114, v114, v35\n"
    "v_add_f32 v121, v121, v74\n"
    "v_mfma_f32_16x16x32_bf16 a[68:71], v[172:175], v[148:151], a[68:71]\n"
    "v_add_f32 v117, v117, v36\n"
    "v_add_f32 v122, v122, v75\n"
    "v_mfma_f32_16x16x32_bf16 a[68:71], v[168:171], v[132:135], a[68:71]\n"
    "s_add_u32 m0, s51, s95\n"
    "v_exp_f32 v54, v54\n"
    "v_mfma_f32_16x16x32_bf16 a[72:75], v[168:171], v[136:139], a[72:75]\n"
    "ds_read_b128 v[164:167], v201 offset:13312\n"
    "v_add_f32 v118, v118, v37\n"
    "v_mfma_f32_16x16x32_bf16 a[72:75], v[172:175], v[152:155], a[72:75]\n"
    "buffer_load_dwordx4 v202, s[32:35], s64 offen lds\n"
    "v_mfma_f32_16x16x32_bf16 a[76:79], v[172:175], v[156:159], a[76:79]\n"
    "v_add_f32 v125, v125, v76\n"
    "v_add_f32 v117, v117, v38\n"
    "v_mfma_f32_16x16x32_bf16 a[76:79], v[168:171], v[140:143], a[76:79]\n"
    "s_waitcnt lgkmcnt(2)\n"
    "v_exp_f32 v55, v55\n"
    "v_mfma_f32_16x16x32_bf16 a[92:95], v[176:179], v[140:143], a[92:95]\n"
    "ds_read_b128 v[168:171], v201 offset:14336\n"
    "v_add_f32 v126, v126, v77\n"
    "v_mfma_f32_16x16x32_bf16 a[92:95], v[180:183], v[156:159], a[92:95]\n"
    "buffer_load_dwordx4 v203, s[32:35], s64 offen offset:1024 lds\n"
    "v_mfma_f32_16x16x32_bf16 a[88:91], v[180:183], v[152:155], a[88:91]\n"
    "v_add_f32 v118, v118, v39\n"
    "v_add_f32 v125, v125, v78\n"
    "v_mfma_f32_16x16x32_bf16 a[88:91], v[176:179], v[136:139], a[88:91]\n"
    "v_add_f32 v121, v121, v40\n"
    "v_add_f32 v126, v126, v79\n"
    "v_mfma_f32_16x16x32_bf16 a[84:87], v[176:179], v[132:135], a[84:87]\n"
    "ds_read_b128 v[172:175], v201 offset:15360\n"
    "v_add_f32 v122, v122, v41\n"
    "v_mfma_f32_16x16x32_bf16 a[84:87], v[180:183], v[148:151], a[84:87]\n"
    "buffer_load_dwordx4 v204, s[32:35], s64 offen offset:2048 lds\n"
    "v_mfma_f32_16x16x32_bf16 a[80:83], v[180:183], v[144:147], a[80:83]\n"
    "v_exp_f32 v56, v56\n"
    "v_mfma_f32_16x16x32_bf16 a[80:83], v[176:179], v[128:131], a[80:83]\n"
    "s_waitcnt lgkmcnt(2)\n"
    "v_add_f32 v113, v113, v80\n"
    "v_add_f32 v121, v121, v42\n"
    "v_mfma_f32_16x16x32_bf16 a[96:99], v[160:163], v[128:131], a[96:99]\n"
    "s_mov_b32 s95, s94\n"
    "ds_read_b128 v[176:179], v201 offset:16384\n"
    "v_add_f32 v114, v114, v81\n"
    "v_mfma_f32_16x16x32_bf16 a[96:99], v[164:167], v[144:147], a[96:99]\n"
    "s_sub_u32 s94, s94, 0x3000\n"
    "v_exp_f32 v57, v57\n"
    "v_mfma_f32_16x16x32_bf16 a[100:103], v[164:167], v[148:151], a[100:103]\n"
    "s_min_u32 s94, s94, 0x6000\n"
    "v_add_f32 v122, v122, v43\n"
    "v_add_f32 v113, v113, v82\n"
    "v_mfma_f32_16x16x32_bf16 a[100:103], v[160:163], v[132:135], a[100:103]\n"
    "s_sub_u32 s93, s94, s95\n"
    "v_add_f32 v125, v125, v44\n"
    "v_add_f32 v114, v114, v83\n"
    "v_mfma_f32_16x16x32_bf16 a[104:107], v[160:163], v[136:139], a[104:107]\n"
    "ds_read_b128 v[180:183], v201 offset:17408\n"
    "v_add_f32 v126, v126, v45\n"
    "v_mfma_f32_16x16x32_bf16 a[104:107], v[164:167], v[152:155], a[104:107]\n"
    "v_exp_f32 v58, v58\n"
    "v_mfma_f32_16x16x32_bf16 a[108:111], v[164:167], v[156:159], a[108:111]\n"
    "v_add_f32 v117, v117, v84\n"
    "v_add_f32 v125, v125, v46\n"
    "v_mfma_f32_16x16x32_bf16 a[108:111], v[160:163], v[140:143], a[108:111]\n"
    "s_waitcnt lgkmcnt(2)\n"
    "v_exp_f32 v59, v59\n"
    "v_mfma_f32_16x16x32_bf16 a[124:127], v[168:171], v[140:143], a[124:127]\n"
    "ds_read_b128 v[160:163], v201 offset:18432\n"
    "v_add_f32 v118, v118, v85\n"
    "v_mfma_f32_16x16x32_bf16 a[124:127], v[172:175], v[156:159], a[124:127]\n"
    "v_add_f32 v126, v126, v47\n"
    "v_add_f32 v117, v117, v86\n"
    "v_mfma_f32_16x16x32_bf16 a[120:123], v[172:175], v[152:155], a[120:123]\n"
    "v_add_f32 v113, v113, v48\n"
    "v_add_u32 v220, 1, v220\n"
    "v_mfma_f32_16x16x32_bf16 a[120:123], v[168:171], v[136:139], a[120:123]\n"
    "v_exp_f32 v60, v60\n"
    "v_mfma_f32_16x16x32_bf16 a[116:119], v[168:171], v[132:135], a[116:119]\n"
    "ds_read_b128 v[164:167], v201 offset:19456\n"
    "v_add_u32 v205, s93, v205\n"
    "v_mfma_f32_16x16x32_bf16 a[116:119], v[172:175], v[148:151], a[116:119]\n"
    "v_add_f32 v118, v118, v87\n"
    "v_add_f32 v114, v114, v49\n"
    "v_mfma_f32_16x16x32_bf16 a[112:115], v[172:175], v[144:147], a[112:115]\n"
    "v_add_u32 v209, 0x180, v209\n"
    "v_add_u32 v206, s93, v206\n"
    "v_mfma_f32_16x16x32_bf16 a[112:115], v[168:171], v[128:131], a[112:115]\n"
    "s_waitcnt lgkmcnt(2)\n"
    "v_add_f32 v121, v121, v88\n"
    "v_add_f32 v113, v113, v50\n"
    "v_mfma_f32_16x16x32_bf16 a[128:131], v[176:179], v[128:131], a[128:131]\n"
    "s_add_u32 s64, s64, 0xc000\n"
    "ds_read_b128 v[168:171], v201 offset:20480\n"
    "v_add_u32 v210, 0x80, v210\n"
    "v_mfma_f32_16x16x32_bf16 a[128:131], v[180:183], v[144:147], a[128:131]\n"
    "s_add_u32 s62, s62, 0x6000\n"
    "v_exp_f32 v61, v61\n"
    "v_mfma_f32_16x16x32_bf16 a[132:135], v[180:183], v[148:151], a[132:135]\n"
    "s_add_u32 s63, s63, 0x6000\n"
    "v_add_u32 v207, s93, v207\n"
    "v_add_f32 v122, v122, v89\n"
    "v_mfma_f32_16x16x32_bf16 a[132:135], v[176:179], v[132:135], a[132:135]\n"
    "s_add_u32 s60, s60, 0x6000\n"
    "v_add_f32 v114, v114, v51\n"
    "v_add_f32 v121, v121, v90\n"
    "v_mfma_f32_16x16x32_bf16 a[136:139], v[176:179], v[136:139], a[136:139]\n"
    "s_add_u32 s61, s61, 0x6000\n"
    "ds_read_b128 v[172:175], v201 offset:21504\n"
    "v_add_f32 v117, v117, v52\n"
    "v_mfma_f32_16x16x32_bf16 a[136:139], v[180:183], v[152:155], a[136:139]\n"
    "s_add_u32 s66, s66, s74\n"
    "v_exp_f32 v62, v62\n"
    "v_mfma_f32_16x16x32_bf16 a[140:143], v[180:183], v[156:159], a[140:143]\n"
    "s_min_u32 s66, s66, 33\n"
    "v_add_f32 v122, v122, v91\n"
    "v_add_f32 v118, v118, v53\n"
    "v_mfma_f32_16x16x32_bf16 a[140:143], v[176:179], v[140:143], a[140:143]\n"
    "s_waitcnt lgkmcnt(2)\n"
    "v_add_f32 v125, v125, v92\n"
    "v_add_f32 v117, v117, v54\n"
    "v_mfma_f32_16x16x32_bf16 a[156:159], v[160:163], v[140:143], a[156:159]\n"
    "ds_read_b128 v[176:179], v201 offset:22528\n"
    "v_add_f32 v126, v126, v93\n"
    "v_mfma_f32_16x16x32_bf16 a[156:159], v[164:167], v[156:159], a[156:159]\n"
    "s_waitcnt vmcnt(3)\n"
    "ds_read_b128 v[180:183], v201 offset:23552\n"
    "v_add_f32 v118, v118, v55\n"
    "v_mfma_f32_16x16x32_bf16 a[152:155], v[164:167], v[152:155], a[152:155]\n"
    "v_cmp_ne_u32 s[80:81], s67, v219\n"
    "v_add_f32 v125, v125, v94\n"
    "v_mfma_f32_16x16x32_bf16 a[152:155], v[160:163], v[136:139], a[152:155]\n"
    "v_cmp_gt_u32 vcc, s66, v218\n"
    "v_add_f32 v121, v121, v56\n"
    "v_mfma_f32_16x16x32_bf16 a[148:151], v[160:163], v[132:135], a[148:151]\n"
    "v_exp_f32 v63, v63\n"
    "v_mfma_f32_16x16x32_bf16 a[148:151], v[164:167], v[148:151], a[148:151]\n"
    "s_or_b64 vcc, vcc, s[80:81]\n"
    "s_cbranch_vccnz LSPRE4\n"
    "LSPB4:\n"
    "v_add_f32 v126, v126, v95\n"
    "v_mfma_f32_16x16x32_bf16 a[144:147], v[164:167], v[144:147], a[144:147]\n"
    "v_add_f32 v122, v122, v57\n"
    "v_mfma_f32_16x16x32_bf16 a[144:147], v[160:163], v[128:131], a[144:147]\n"
    "s_waitcnt lgkmcnt(0)\n"
    "v_mfma_f32_16x16x32_bf16 a[160:163], v[168:171], v[128:131], a[160:163]\n"
    "s_barrier\n"
    "v_mfma_f32_16x16x32_bf16 a[160:163], v[172:175], v[144:147], a[160:163]\n"
    "buffer_store_dwordx2 v[220:221], v214, s[44:47], 0 offen sc1\n"
    "v_mfma_f32_16x16x32_bf16 a[164:167], v[172:175], v[148:151], a[164:167]\n"
    "ds_read_b128 v[160:163], v200\n"
    "ds_read_u16_d16_hi v223, v209 offset:6144\n"
    "v_mfma_f32_16x16x32_bf16 a[164:167], v[168:171], v[132:135], a[164:167]\n"
    "ds_read_u16_d16_hi v222, v209\n"
    "v_add_f32 v121, v121, v58\n"
    "v_mfma_f32_16x16x32_bf16 a[168:171], v[168:171], v[136:139], a[168:171]\n"
    "ds_read_b128 v[164:167], v200 offset:1024\n"
    "ds_read_b128 v[232:235], v205 offset:24576\n"
    "v_mfma_f32_16x16x32_bf16 a[168:171], v[172:175], v[152:155], a[168:171]\n"
    "v_add_f32 v122, v122, v59\n"
    "ds_read_b128 v[236:239], v206 offset:24576\n"
    "v_mfma_f32_16x16x32_bf16 a[172:175], v[172:175], v[156:159], a[172:175]\n"
    "v_add_f32 v125, v125, v60\n"
    "v_add_f32 v126, v126, v61\n"
    "v_mfma_f32_16x16x32_bf16 a[172:175], v[168:171], v[140:143], a[172:175]\n"
    "v_add_f32 v125, v125, v62\n"
    "v_add_f32 v126, v126, v63\n"
    "v_mfma_f32_16x16x32_bf16 a[188:191], v[176:179], v[140:143], a[188:191]\n"
    "ds_read_b128 v[168:171], v200 offset:2048\n"
    "ds_read_b64 v[228:229], v210\n"
    "v_mfma_f32_16x16x32_bf16 a[188:191], v[180:183], v[156:159], a[188:191]\n"
    "ds_read2st64_b32 v[240:241], v207 offset0:96 offset1:99\n"
    "ds_read2st64_b32 v[242:243], v207 offset0:102 offset1:105\n"
    "v_mfma_f32_16x16x32_bf16 a[184:187], v[176:179], v[136:139], a[184:187]\n"
    "ds_read2st64_b32 v[244:245], v207 offset0:108 offset1:111\n"
    "ds_read2st64_b32 v[246:247], v207 offset0:114 offset1:117\n"
    "v_mfma_f32_16x16x32_bf16 a[184:187], v[180:183], v[152:155], a[184:187]\n"
    "v_cvt_pk_bf16_f32 v140, v44, v45\n"
    "v_cvt_pk_bf16_f32 v141, v46, v47\n"
    "v_mfma_f32_16x16x32_bf16 a[180:183], v[176:179], v[132:135], a[180:183]\n"
    "ds_read_b128 v[172:175], v200 offset:3072\n"
    "s_waitcnt lgkmcnt(7)\n"
    "v_mfma_f32_16x16x32_bf16 a[180:183], v[180:183], v[148:151], a[180:183]\n"
    "v_mul_f32 v226, s75, v223\n"
    "v_fma_f32 v227, -v222, v226, v217\n"
    "v_mfma_f32_16x16x32_bf16 a[176:179], v[176:179], v[128:131], a[176:179]\n"
    "v_cvt_f32_i32 v232, v232\n"
    "v_cvt_f32_i32 v233, v233\n"
    "v_mfma_f32_16x16x32_bf16 a[176:179], v[180:183], v[144:147], a[176:179]\n"
    "v_cvt_f32_i32 v234, v234\n"
    "v_cvt_f32_i32 v235, v235\n"
    "LM0:\n"
    "v_mfma_f32_16x16x32_bf16 v[64:67], v[160:163], a[192:195], v[96:99]\n"
    "ds_read_b128 v[176:179], v200 offset:4096\n"
    "v_cvt_f32_i32 v236, v236\n"
    "v_mfma_f32_16x16x32_bf16 v[64:67], v[164:167], a[196:199], v[64:67]\n"
    "s_waitcnt lgkmcnt(1)\n"
    "v_cvt_f32_i32 v237, v237\n"
    "v_cvt_f32_i32 v238, v238\n"
    "v_mfma_f32_16x16x32_bf16 v[64:67], v[168:171], a[200:203], v[64:67]\n"
    "v_cvt_f32_i32 v239, v239\n"
    "v_mul_f32_dpp v232, v226, v232 row_newbcast:0 row_mask:0xf bank_mask:0xf\n"
    "v_mfma_f32_16x16x32_bf16 v[68:71], v[168:171], a[224:227], v[100:103]\n"
    "v_mul_f32_dpp v233, v226, v233 row_newbcast:1 row_mask:0xf bank_mask:0xf\n"
    "v_mul_f32_dpp v234, v226, v234 row_newbcast:2 row_mask:0xf bank_mask:0xf\n"
    "v_mfma_f32_16x16x32_bf16 v[68:71], v[164:167], a[220:223], v[68:71]\n"
    "ds_read_b128 v[180:183], v200 offset:5120\n"
    "v_mul_f32_dpp v235, v226, v235 row_newbcast:3 row_mask:0xf bank_mask:0xf\n"
    "v_mfma_f32_16x16x32_bf16 v[68:71], v[160:163], a[216:219], v[68:71]\n"
    "v_mul_f32_dpp v236, v226, v236 row_newbcast:4 row_mask:0xf bank_mask:0xf\n"
    "v_mul_f32_dpp v237, v226, v237 row_newbcast:5 row_mask:0xf bank_mask:0xf\n"
    "v_mfma_f32_16x16x32_bf16 v[72:75], v[160:163], a[240:243], v[104:107]\n"
    "v_mul_f32_dpp v238, v226, v238 row_newbcast:6 row_mask:0xf bank_mask:0xf\n"
    "v_mul_f32_dpp v239, v226, v239 row_newbcast:7 row_mask:0xf bank_mask:0xf\n"
    "v_mfma_f32_16x16x32_bf16 v[72:75], v[164:167], a[244:247], v[72:75]\n"
    "v_add_f32_dpp v232, v227, v232 row_newbcast:0 row_mask:0xf bank_mask:0xf\n"
    "v_add_f32_dpp v233, v227, v233 row_newbcast:1 row_mask:0xf bank_mask:0xf\n"
    "v_mfma_f32_16x16x32_bf16 v[72:75], v[168:171], a[248:251], v[72:75]\n"
    "v_add_f32_dpp v234, v227, v234 row_newbcast:2 row_mask:0xf bank_mask:0xf\n"
    "v_add_f32_dpp v235, v227, v235 row_newbcast:3 row_mask:0xf bank_mask:0xf\n"
    "v_mfma_f32_16x16x32_bf16 v[76:79], v[168:171], v[16:19], v[108:111]\n"
    "v_add_f32_dpp v236, v227, v236 row_newbcast:4 row_mask:0xf bank_mask:0xf\n"
    "v_add_f32_dpp v237, v227, v237 row_newbcast:5 row_mask:0xf bank_mask:0xf\n"
    "v_mfma_f32_16x16x32_bf16 v[76:79], v[164:167], v[12:15], v[76:79]\n"
    "v_add_f32_dpp v238, v227, v238 row_newbcast:6 row_mask:0xf bank_mask:0xf\n"
    "v_add_f32_dpp v239, v227, v239 row_newbcast:7 row_mask:0xf bank_mask:0xf\n"
    "v_mfma_f32_16x16x32_bf16 v[76:79], v[160:163], v[8:11], v[76:79]\n"
    "s_waitcnt lgkmcnt(1)\n"
    "v_cvt_pk_bf16_f32 v232, v232, v233\n"
    "v_cvt_f32_i32 v240, v240\n"
    "v_mfma_f32_16x16x32_bf16 v[76:79], v[172:175], v[20:23], v[76:79]\n"
    "ds_read_b128 v[160:163], v200 offset:9216\n"
    "v_cvt_pk_bf16_f32 v233, v234, v235\n"
    "v_mfma_f32_16x16x32_bf16 v[76:79], v[176:179], v[24:27], v[76:79]\n"
    "s_waitcnt lgkmcnt(1)\n"
    "v_cvt_f32_i32 v241, v241\n"
    "v_cvt_pk_bf16_f32 v234, v236, v237\n"
    "v_mfma_f32_16x16x32_bf16 v[76:79], v[180:183], v[28:31], v[76:79]\n"
    "v_cvt_f32_i32 v242, v242\n"
    "v_cvt_pk_bf16_f32 v235, v238, v239\n"
    "v_mfma_f32_16x16x32_bf16 v[72:75], v[180:183], v[4:7], v[72:75]\n"
    "ds_read_b128 v[164:167], v200 offset:10240\n"
    "v_cvt_f32_i32 v243, v243\n"
    "v_mfma_f32_16x16x32_bf16 v[72:75], v[176:179], v[0:3], v[72:75]\n"
    "v_cvt_f32_i32 v244, v244\n"
    "v_cvt_f32_i32 v245, v245\n"
    "v_mfma_f32_16x16x32_bf16 v[72:75], v[172:175], a[252:255], v[72:75]\n"
    "v_cvt_f32_i32 v246, v246\n"
    "v_cvt_f32_i32 v247, v247\n"
    "v_mfma_f32_16x16x32_bf16 v[68:71], v[172:175], a[228:231], v[68:71]\n"
    "ds_read_b128 v[168:171], v200 offset:11264\n"
    "v_mul_f32_dpp v240, v228, v240 row_newbcast:0 row_mask:0xf bank_mask:0xf\n"
    "v_mfma_f32_16x16x32_bf16 v[68:71], v[176:179], a[232:235], v[68:71]\n"
    "v_mul_f32_dpp v241, v228, v241 row_newbcast:1 row_mask:0xf bank_mask:0xf\n"
    "v_mul_f32_dpp v242, v228, v242 row_newbcast:2 row_mask:0xf bank_mask:0xf\n"
    "v_mfma_f32_16x16x32_bf16 v[68:71], v[180:183], a[236:239], v[68:71]\n"
    "v_mul_f32_dpp v243, v228, v243 row_newbcast:3 row_mask:0xf bank_mask:0xf\n"
    "v_mul_f32_dpp v244, v228, v244 row_newbcast:4 row_mask:0xf bank_mask:0xf\n"
    "v_mfma_f32_16x16x32_bf16 v[64:67], v[180:183], a[212:215], v[64:67]\n"
    "v_mul_f32_dpp v245, v228, v245 row_newbcast:5 row_mask:0xf bank_mask:0xf\n"
    "v_mul_f32_dpp v246, v228, v246 row_newbcast:6 row_mask:0xf bank_mask:0xf\n"
    "v_mfma_f32_16x16x32_bf16 v[64:67], v[176:179], a[208:211], v[64:67]\n"
    "buffer_store_dwordx4 v[232:235], v211, s[36:39], s62 offen sc1\n"
    "v_mul_f32_dpp v247, v228, v247 row_newbcast:7 row_mask:0xf bank_mask:0xf\n"
    "v_add_f32_dpp v240, v229, v240 row_newbcast:0 row_mask:0xf bank_mask:0xf\n"
    "v_add_f32_dpp v241, v229, v241 row_newbcast:1 row_mask:0xf bank_mask:0xf\n"
    "v_mfma_f32_16x16x32_bf16 v[64:67], v[172:175], a[204:207], v[64:67]\n"
    "s_waitcnt lgkmcnt(1)\n"
    "v_add_f32_dpp v242, v229, v242 row_newbcast:2 row_mask:0xf bank_mask:0xf\n"
    "v_add_f32_dpp v243, v229, v243 row_newbcast:3 row_mask:0xf bank_mask:0xf\n"
    "v_mfma_f32_16x16x32_bf16 v[80:83], v[160:163], a[204:207], v[96:99]\n"
    "ds_read_b128 v[172:175], v200 offset:6144\n"
    "v_add_f32_dpp v244, v229, v244 row_newbcast:4 row_mask:0xf bank_mask:0xf\n"
    "v_mfma_f32_16x16x32_bf16 v[80:83], v[164:167], a[208:211], v[80:83]\n"
    "s_waitcnt lgkmcnt(1)\n"
    "v_add_f32_dpp v245, v229, v245 row_newbcast:5 row_mask:0xf bank_mask:0xf\n"
    "v_add_f32_dpp v246, v229, v246 row_newbcast:6 row_mask:0xf bank_mask:0xf\n"
    "v_mfma_f32_16x16x32_bf16 v[80:83], v[168:171], a[212:215], v[80:83]\n"
    "v_add_f32_dpp v247, v229, v247 row_newbcast:7 row_mask:0xf bank_mask:0xf\n"
    "v_cvt_pk_bf16_f32 v240, v240, v241\n"
    "v_mfma_f32_16x16x32_bf16 v[84:87], v[168:171], a[236:239], v[100:103]\n"
    "ds_read_b128 v[176:179], v200 offset:7168\n"
    "v_cvt_pk_bf16_f32 v241, v242, v243\n"
    "v_mfma_f32_16x16x32_bf16 v[84:87], v[164:167], a[232:235], v[84:87]\n"
    "v_cvt_pk_bf16_f32 v242, v244, v245\n"
    "v_cvt_pk_bf16_f32 v243, v246, v247\n"
    "v_mfma_f32_16x16x32_bf16 v[84:87], v[160:163], a[228:231], v[84:87]\n"
    "v_cvt_pk_bf16_f32 v136, v40, v41\n"
    "v_cvt_pk_bf16_f32 v137, v42, v43\n"
    "v_mfma_f32_16x16x32_bf16 v[88:91], v[160:163], a[252:255], v[104:107]\n"
    "ds_read_b128 v[180:183], v200 offset:8192\n"
    "v_cvt_pk_bf16_f32 v132, v36, v37\n"
    "v_mfma_f32_16x16x32_bf16 v[88:91], v[164:167], v[0:3], v[88:91]\n"
    "v_cvt_pk_bf16_f32 v133, v38, v39\n"
    "v_cvt_pk_bf16_f32 v128, v32, v33\n"
    "v_mfma_f32_16x16x32_bf16 v[88:91], v[168:171], v[4:7], v[88:91]\n"
    "v_cvt_pk_bf16_f32 v129, v34, v35\n"
    "v_cvt_pk_bf16_f32 v142, v60, v61\n"
    "v_mfma_f32_16x16x32_bf16 v[92:95], v[168:171], v[28:31], v[108:111]\n"
    "v_exp_f32 v64, v64\n"
    "v_mfma_f32_16x16x32_bf16 v[92:95], v[164:167], v[24:27], v[92:95]\n"
    "v_exp_f32 v65, v65\n"
    "v_mfma_f32_16x16x32_bf16 v[92:95], v[160:163], v[20:23], v[92:95]\n"
    "s_waitcnt lgkmcnt(1)\n"
    "v_exp_f32 v66, v66\n"
    "v_mfma_f32_16x16x32_bf16 v[92:95], v[172:175], v[8:11], v[92:95]\n"
    "ds_read_b128 v[160:163], v200 offset:12288\n"
    "v_cvt_pk_bf16_f32 v143, v62, v63\n"
    "v_mfma_f32_16x16x32_bf16 v[92:95], v[176:179], v[12:15], v[92:95]\n"
    "s_waitcnt lgkmcnt(1)\n"
    "v_exp_f32 v67, v67\n"
    "v_mfma_f32_16x16x32_bf16 v[92:95], v[180:183], v[16:19], v[92:95]\n"
    "buffer_store_dwordx4 v[240:243], v212, s[40:43], s63 offen sc1\n"
    "v_exp_f32 v68, v68\n"
    "v_cvt_pk_bf16_f32 v138, v56, v57\n"
    "v_mfma_f32_16x16x32_bf16 v[88:91], v[180:183], a[248:251], v[88:91]\n"
    "ds_read_b128 v[164:167], v200 offset:13312\n"
    "v_cvt_pk_bf16_f32 v139, v58, v59\n"
    "v_mfma_f32_16x16x32_bf16 v[88:91], v[176:179], a[244:247], v[88:91]\n"
    "v_exp_f32 v69, v69\n"
    "v_mfma_f32_16x16x32_bf16 v[88:91], v[172:175], a[240:243], v[88:91]\n"
    "v_exp_f32 v70, v70\n"
    "v_mfma_f32_16x16x32_bf16 v[84:87], v[172:175], a[216:219], v[84:87]\n"
    "ds_read_b128 v[168:171], v200 offset:14336\n"
    "v_cvt_pk_bf16_f32 v134, v52, v53\n"
    "v_mfma_f32_16x16x32_bf16 v[84:87], v[176:179], a[220:223], v[84:87]\n"
    "v_exp_f32 v71, v71\n"
    "v_mfma_f32_16x16x32_bf16 v[84:87], v[180:183], a[224:227], v[84:87]\n"
    "v_exp_f32 v72, v72\n"
    "v_mfma_f32_16x16x32_bf16 v[80:83], v[180:183], a[200:203], v[80:83]\n"
    "v_cvt_pk_bf16_f32 v135, v54, v55\n"
    "v_cvt_pk_bf16_f32 v130, v48, v49\n"
    "v_mfma_f32_16x16x32_bf16 v[80:83], v[176:179], a[196:199], v[80:83]\n"
    "v_exp_f32 v73, v73\n"
    "v_mfma_f32_16x16x32_bf16 v[80:83], v[172:175], a[192:195], v[80:83]\n"
    "s_waitcnt lgkmcnt(1)\n"
    "v_cvt_pk_bf16_f32 v131, v50, v51\n"
    "v_cvt_pk_bf16_f32 v144, v64, v65\n"
    "v_mfma_f32_16x16x32_bf16 v[32:35], v[160:163], a[192:195], v[96:99]\n"
    "ds_read_b128 v[172:175], v200 offset:15360\n"
    "v_cvt_pk_bf16_f32 v145, v66, v67\n"
    "v_mfma_f32_16x16x32_bf16 v[32:35], v[164:167], a[196:199], v[32:35]\n"
    "s_waitcnt lgkmcnt(1)\n"
    "v_exp_f32 v74, v74\n"
    "v_mfma_f32_16x16x32_bf16 v[32:35], v[168:171], a[200:203], v[32:35]\n"
    "v_exp_f32 v75, v75\n"
    "v_mfma_f32_16x16x32_bf16 v[36:39], v[168:171], a[224:227], v[100:103]\n"
    "ds_read_b128 v[176:179], v200 offset:16384\n"
    "v_cvt_pk_bf16_f32 v148, v68, v69\n"
    "v_mfma_f32_16x16x32_bf16 v[36:39], v[164:167], a[220:223], v[36:39]\n"
    "v_exp_f32 v76, v76\n"
    "v_mfma_f32_16x16x32_bf16 v[36:39], v[160:163], a[216:219], v[36:39]\n"
    "v_exp_f32 v77, v77\n"
    "v_mfma_f32_16x16x32_bf16 v[40:43], v[160:163], a[240:243], v[104:107]\n"
    "ds_read_b128 v[180:183], v200 offset:17408\n"
    "v_cvt_pk_bf16_f32 v149, v70, v71\n"
    "v_mfma_f32_16x16x32_bf16 v[40:43], v[164:167], a[244:247], v[40:43]\n"
    "v_exp_f32 v78, v78\n"
    "v_mfma_f32_16x16x32_bf16 v[40:43], v[168:171], a[248:251], v[40:43]\n"
    "v_exp_f32 v79, v79\n"
    "v_mfma_f32_16x16x32_bf16 v[44:47], v[168:171], v[16:19], v[108:111]\n"
    "v_exp_f32 v80, v80\n"
    "v_mfma_f32_16x16x32_bf16 v[44:47], v[164:167], v[12:15], v[44:47]\n"
    "v_exp_f32 v81, v81\n"
    "v_mfma_f32_16x16x32_bf16 v[44:47], v[160:163], v[8:11], v[44:47]\n"
    "s_waitcnt lgkmcnt(1)\n"
    "v_exp_f32 v82, v82\n"
    "v_mfma_f32_16x16x32_bf16 v[44:47], v[172:175], v[20:23], v[44:47]\n"
    "ds_read_b128 v[160:163], v200 offset:21504\n"
    "v_cvt_pk_bf16_f32 v152, v72, v73\n"
    "v_mfma_f32_16x16x32_bf16 v[44:47], v[176:179], v[24:27], v[44:47]\n"
    "s_waitcnt lgkmcnt(1)\n"
    "v_exp_f32 v83, v83\n"
    "v_mfma_f32_16x16x32_bf16 v[44:47], v[180:183], v[28:31], v[44:47]\n"
    "v_exp_f32 v84, v84\n"
    "v_mfma_f32_16x16x32_bf16 v[40:43], v[180:183], v[4:7], v[40:43]\n"
    "ds_read_b128 v[164:167], v200 offset:22528\n"
    "v_cvt_pk_bf16_f32 v153, v74, v75\n"
    "v_mfma_f32_16x16x32_bf16 v[40:43], v[176:179], v[0:3], v[40:43]\n"
    "v_exp_f32 v85, v85\n"
    "v_mfma_f32_16x16x32_bf16 v[40:43], v[172:175], a[252:255], v[40:43]\n"
    "v_exp_f32 v86, v86\n"
    "v_mfma_f32_16x16x32_bf16 v[36:39], v[172:175], a[228:231], v[36:39]\n"
    "ds_read_b128 v[168:171], v200 offset:23552\n"
    "v_cvt_pk_bf16_f32 v156, v76, v77\n"
    "v_mfma_f32_16x16x32_bf16 v[36:39], v[176:179], a[232:235], v[36:39]\n"
    "v_exp_f32 v87, v87\n"
    "v_mfma_f32_16x16x32_bf16 v[36:39], v[180:183], a[236:239], v[36:39]\n"
    "v_exp_f32 v88, v88\n"
    "v_mfma_f32_16x16x32_bf16 v[32:35], v[180:183], a[212:215], v[32:35]\n"
    "v_exp_f32 v89, v89\n"
    "v_mfma_f32_16x16x32_bf16 v[32:35], v[176:179], a[208:211], v[32:35]\n"
    "v_exp_f32 v90, v90\n"
    "v_mfma_f32_16x16x32_bf16 v[32:35], v[172:175], a[204:207], v[32:35]\n"
    "s_waitcnt lgkmcnt(1)\n"
    "s_mov_b32 m0, 0x6000\n"
    "v_cvt_pk_bf16_f32 v157, v78, v79\n"
    "v_mfma_f32_16x16x32_bf16 v[48:51], v[160:163], a[204:207], v[96:99]\n"
    "ds_read_b128 v[172:175], v200 offset:18432\n"
    "v_cvt_pk_bf16_f32 v146, v80, v81\n"
    "v_mfma_f32_16x16x32_bf16 v[48:51], v[164:167], a[208:211], v[48:51]\n"
    "s_waitcnt lgkmcnt(1)\n"
    "buffer_load_dwordx4 v200, s[36:39], s60 offen sc1 lds\n"
    "v_mfma_f32_16x16x32_bf16 v[48:51], v[168:171], a[212:215], v[48:51]\n"
    "v_exp_f32 v91, v91\n"
    "v_mfma_f32_16x16x32_bf16 v[52:55], v[168:171], a[236:239], v[100:103]\n"
    "ds_read_b128 v[176:179], v200 offset:19456\n"
    "v_cvt_pk_bf16_f32 v147, v82, v83\n"
    "v_mfma_f32_16x16x32_bf16 v[52:55], v[164:167], a[232:235], v[52:55]\n"
    "v_exp_f32 v92, v92\n"
    "v_mfma_f32_16x16x32_bf16 v[52:55], v[160:163], a[228:231], v[52:55]\n"
    "buffer_load_dwordx4 v200, s[36:39], s60 offen offset:1024 sc1 lds\n"
    "v_mfma_f32_16x16x32_bf16 v[56:59], v[160:163], a[252:255], v[104:107]\n"
    "ds_read_b128 v[180:183], v200 offset:20480\n"
    "v_cvt_pk_bf16_f32 v150, v84, v85\n"
    "v_mfma_f32_16x16x32_bf16 v[56:59], v[164:167], v[0:3], v[56:59]\n"
    "v_exp_f32 v93, v93\n"
    "v_mfma_f32_16x16x32_bf16 v[56:59], v[168:171], v[4:7], v[56:59]\n"
    "v_cvt_pk_bf16_f32 v151, v86, v87\n"
    "v_cvt_pk_bf16_f32 v154, v88, v89\n"
    "v_mfma_f32_16x16x32_bf16 v[60:63], v[168:171], v[28:31], v[108:111]\n"
    "buffer_load_dwordx4 v200, s[36:39], s60 offen offset:2048 sc1 lds\n"
    "v_mfma_f32_16x16x32_bf16 v[60:63], v[164:167], v[24:27], v[60:63]\n"
    "v_exp_f32 v94, v94\n"
    "v_mfma_f32_16x16x32_bf16 v[60:63], v[160:163], v[20:23], v[60:63]\n"
    "s_waitcnt lgkmcnt(1)\n"
    "s_add_u32 m0, s71, 0xc000\n"
    "v_cvt_pk_bf16_f32 v155, v90, v91\n"
    "v_mfma_f32_16x16x32_bf16 v[60:63], v[172:175], v[8:11], v[60:63]\n"
    "ds_read_b128 v[160:163], v201 offset:24576\n"
    "v_cvt_pk_bf16_f32 v158, v92, v93\n"
    "v_mfma_f32_16x16x32_bf16 v[60:63], v[176:179], v[12:15], v[60:63]\n"
    "s_waitcnt lgkmcnt(1)\n"
    "buffer_load_dwordx4 v200, s[40:43], s61 offen sc1 lds\n"
    "v_mfma_f32_16x16x32_bf16 v[60:63], v[180:183], v[16:19], v[60:63]\n"
    "v_exp_f32 v95, v95\n"
    "v_mfma_f32_16x16x32_bf16 v[56:59], v[180:183], a[248:251], v[56:59]\n"
    "ds_read_b128 v[164:167], v201 offset:25600\n"
    "v_cvt_pk_bf16_f32 v159, v94, v95\n"
    "v_mfma_f32_16x16x32_bf16 v[56:59], v[176:179], a[244:247], v[56:59]\n"
    "v_exp_f32 v32, v32\n"
    "v_mfma_f32_16x16x32_bf16 v[56:59], v[172:175], a[240:243], v[56:59]\n"
    "buffer_load_dwordx4 v200, s[40:43], s61 offen offset:1024 sc1 lds\n"
    "v_mfma_f32_16x16x32_bf16 v[52:55], v[172:175], a[216:219], v[52:55]\n"
    "ds_read_b128 v[168:171], v201 offset:26624\n"
    "v_add_f32 v113, v113, v64\n"
    "v_mfma_f32_16x16x32_bf16 v[52:55], v[176:179], a[220:223], v[52:55]\n"
    "v_exp_f32 v33, v33\n"
    "v_mfma_f32_16x16x32_bf16 v[52:55], v[180:183], a[224:227], v[52:55]\n"
    "v_exp_f32 v34, v34\n"
    "v_mfma_f32_16x16x32_bf16 v[48:51], v[180:183], a[200:203], v[48:51]\n"
    "buffer_load_dwordx4 v200, s[40:43], s61 offen offset:2048 sc1 lds\n"
    "v_mfma_f32_16x16x32_bf16 v[48:51], v[176:179], a[196:199], v[48:51]\n"
    "v_exp_f32 v35, v35\n"
    "v_mfma_f32_16x16x32_bf16 v[48:51], v[172:175], a[192:195], v[48:51]\n"
    "s_waitcnt lgkmcnt(1)\n"
    "v_exp_f32 v36, v36\n"
    "v_mfma_f32_16x16x32_bf16 a[0:3], v[160:163], v[128:131], a[0:3]\n"
    "ds_read_b128 v[172:175], v201 offset:27648\n"
    "ds_read_b128 v[176:179], v201 offset:28672\n"
    "v_mfma_f32_16x16x32_bf16 a[0:3], v[164:167], v[144:147], a[0:3]\n"
    "buffer_load_dwordx4 v200, s[40:43], s61 offen offset:3072 sc1 lds\n"
    "v_mfma_f32_16x16x32_bf16 a[4:7], v[160:163], v[132:135], a[4:7]\n"
    "s_add_u32 s65, s61, 0x1000\n"
    "v_exp_f32 v37, v37\n"
    "v_mfma_f32_16x16x32_bf16 a[4:7], v[164:167], v[148:151], a[4:7]\n"
    "s_add_u32 m0, s71, 0xd000\n"
    "v_exp_f32 v38, v38\n"
    "v_mfma_f32_16x16x32_bf16 a[8:11], v[160:163], v[136:139], a[8:11]\n"
    "ds_read_b128 v[180:183], v201 offset:29696\n"
    "v_add_f32 v114, v114, v65\n"
    "v_mfma_f32_16x16x32_bf16 a[8:11], v[164:167], v[152:155], a[8:11]\n"
    "buffer_load_dwordx4 v200, s[40:43], s65 offen sc1 lds\n"
    "v_mfma_f32_16x16x32_bf16 a[12:15], v[160:163], v[140:143], a[12:15]\n"
    "v_exp_f32 v39, v39\n"
    "v_mfma_f32_16x16x32_bf16 a[12:15], v[164:167], v[156:159], a[12:15]\n"
    "s_waitcnt lgkmcnt(2)\n"
    "v_exp_f32 v40, v40\n"
    "v_mfma_f32_16x16x32_bf16 a[28:31], v[168:171], v[140:143], a[28:31]\n"
    "ds_read_b128 v[160:163], v201 offset:30720\n"
    "v_add_f32 v113, v113, v66\n"
    "v_mfma_f32_16x16x32_bf16 a[28:31], v[172:175], v[156:159], a[28:31]\n"
    "buffer_load_dwordx4 v200, s[40:43], s65 offen offset:1024 sc1 lds\n"
    "v_mfma_f32_16x16x32_bf16 a[24:27], v[172:175], v[152:155], a[24:27]\n"
    "v_exp_f32 v41, v41\n"
    "v_mfma_f32_16x16x32_bf16 a[24:27], v[168:171], v[136:139], a[24:27]\n"
    "v_exp_f32 v42, v42\n"
    "v_mfma_f32_16x16x32_bf16 a[20:23], v[168:171], v[132:135], a[20:23]\n"
    "ds_read_b128 v[164:167], v201 offset:31744\n"
    "v_add_f32 v114, v114, v67\n"
    "v_mfma_f32_16x16x32_bf16 a[20:23], v[172:175], v[148:151], a[20:23]\n"
    "v_exp_f32 v43, v43\n"
    "v_mfma_f32_16x16x32_bf16 a[16:19], v[172:175], v[144:147], a[16:19]\n"
    "v_exp_f32 v44, v44\n"
    "v_mfma_f32_16x16x32_bf16 a[16:19], v[168:171], v[128:131], a[16:19]\n"
    "s_waitcnt lgkmcnt(2)\n"
    "v_exp_f32 v45, v45\n"
    "v_mfma_f32_16x16x32_bf16 a[32:35], v[176:179], v[128:131], a[32:35]\n"
    "ds_read_b128 v[168:171], v201 offset:32768\n"
    "v_add_f32 v117, v117, v68\n"
    "v_mfma_f32_16x16x32_bf16 a[32:35], v[180:183], v[144:147], a[32:35]\n"
    "buffer_load_dwordx2 v[218:219], v213, s[44:47], 0 offen sc1\n"
    "v_mfma_f32_16x16x32_bf16 a[36:39], v[180:183], v[148:151], a[36:39]\n"
    "v_exp_f32 v46, v46\n"
    "v_mfma_f32_16x16x32_bf16 a[36:39], v[176:179], v[132:135], a[36:39]\n"
    "v_exp_f32 v47, v47\n"
    "v_mfma_f32_16x16x32_bf16 a[40:43], v[176:179], v[136:139], a[40:43]\n"
    "ds_read_b128 v[172:175], v201 offset:33792\n"
    "v_add_f32 v118, v118, v69\n"
    "v_mfma_f32_16x16x32_bf16 a[40:43], v[180:183], v[152:155], a[40:43]\n"
    "v_exp_f32 v48, v48\n"
    "v_mfma_f32_16x16x32_bf16 a[44:47], v[180:183], v[156:159], a[44:47]\n"
    "v_exp_f32 v49, v49\n"
    "v_mfma_f32_16x16x32_bf16 a[44:47], v[176:179], v[140:143], a[44:47]\n"
    "s_waitcnt lgkmcnt(2)\n"
    "v_exp_f32 v50, v50\n"
    "v_mfma_f32_16x16x32_bf16 a[60:63], v[160:163], v[140:143], a[60:63]\n"
    "ds_read_b128 v[176:179], v201 offset:34816\n"
    "v_add_f32 v117, v117, v70\n"
    "v_mfma_f32_16x16x32_bf16 a[60:63], v[164:167], v[156:159], a[60:63]\n"
    "v_exp_f32 v51, v51\n"
    "v_mfma_f32_16x16x32_bf16 a[56:59], v[164:167], v[152:155], a[56:59]\n"
    "v_add_f32 v113, v113, v32\n"
    "v_add_f32 v118, v118, v71\n"
    "v_mfma_f32_16x16x32_bf16 a[56:59], v[160:163], v[136:139], a[56:59]\n"
    "v_exp_f32 v52, v52\n"
    "v_mfma_f32_16x16x32_bf16 a[52:55], v[160:163], v[132:135], a[52:55]\n"
    "ds_read_b128 v[180:183], v201 offset:35840\n"
    "v_add_f32 v114, v114, v33\n"
    "v_mfma_f32_16x16x32_bf16 a[52:55], v[164:167], v[148:151], a[52:55]\n"
    "v_add_f32 v121, v121, v72\n"
    "v_add_f32 v113, v113, v34\n"
    "v_mfma_f32_16x16x32_bf16 a[48:51], v[164:167], v[144:147], a[48:51]\n"
    "v_exp_f32 v53, v53\n"
    "v_mfma_f32_16x16x32_bf16 a[48:51], v[160:163], v[128:131], a[48:51]\n"
    "s_waitcnt lgkmcnt(2)\n"
    "v_add_f32 v122, v122, v73\n"
    "v_add_f32 v114, v114, v35\n"
    "v_mfma_f32_16x16x32_bf16 a[64:67], v[168:171], v[128:131], a[64:67]\n"
    "ds_read_b128 v[160:163], v201 offset:36864\n"
    "v_add_f32 v121, v121, v74\n"
    "v_mfma_f32_16x16x32_bf16 a[64:67], v[172:175], v[144:147], a[64:67]\n"
    "v_add_f32 v117, v117, v36\n"
    "v_add_f32 v122, v122, v75\n"
    "v_mfma_f32_16x16x32_bf16 a[68:71], v[172:175], v[148:151], a[68:71]\n"
    "v_exp_f32 v54, v54\n"
    "v_mfma_f32_16x16x32_bf16 a[68:71], v[168:171], v[132:135], a[68:71]\n"
    "s_add_u32 m0, s51, s95\n"
    "v_add_f32 v118, v118, v37\n"
    "v_add_f32 v125, v125, v76\n"
    "v_mfma_f32_16x16x32_bf16 a[72:75], v[168:171], v[136:139], a[72:75]\n"
    "ds_read_b128 v[164:167], v201 offset:37888\n"
    "v_add_f32 v117, v117, v38\n"
    "v_mfma_f32_16x16x32_bf16 a[72:75], v[172:175], v[152:155], a[72:75]\n"
    "buffer_load_dwordx4 v202, s[32:35], s64 offen lds\n"
    "v_mfma_f32_16x16x32_bf16 a[76:79], v[172:175], v[156:159], a[76:79]\n"
    "v_exp_f32 v55, v55\n"
    "v_mfma_f32_16x16x32_bf16 a[76:79], v[168:171], v[140:143], a[76:79]\n"
    "s_waitcnt lgkmcnt(2)\n"
    "v_add_f32 v126, v126, v77\n"
    "v_add_f32 v118, v118, v39\n"
    "v_mfma_f32_16x16x32_bf16 a[92:95], v[176:179], v[140:143], a[92:95]\n"
    "ds_read_b128 v[168:171], v201 offset:38912\n"
    "v_add_f32 v125, v125, v78\n"
    "v_mfma_f32_16x16x32_bf16 a[92:95], v[180:183], v[156:159], a[92:95]\n"
    "buffer_load_dwordx4 v203, s[32:35], s64 offen offset:1024 lds\n"
    "v_mfma_f32_16x16x32_bf16 a[88:91], v[180:183], v[152:155], a[88:91]\n"
    "v_add_f32 v121, v121, v40\n"
    "v_add_f32 v126, v126, v79\n"
    "v_mfma_f32_16x16x32_bf16 a[88:91], v[176:179], v[136:139], a[88:91]\n"
    "v_exp_f32 v56, v56\n"
    "v_mfma_f32_16x16x32_bf16 a[84:87], v[176:179], v[132:135], a[84:87]\n"
    "ds_read_b128 v[172:175], v201 offset:39936\n"
    "v_add_f32 v122, v122, v41\n"
    "v_mfma_f32_16x16x32_bf16 a[84:87], v[180:183], v[148:151], a[84:87]\n"
    "buffer_load_dwordx4 v204, s[32:35], s64 offen offset:2048 lds\n"
    "v_mfma_f32_16x16x32_bf16 a[80:83], v[180:183], v[144:147], a[80:83]\n"
    "v_add_f32 v113, v113, v80\n"
    "v_add_f32 v121, v121, v42\n"
    "v_mfma_f32_16x16x32_bf16 a[80:83], v[176:179], v[128:131], a[80:83]\n"
    "s_waitcnt lgkmcnt(2)\n"
    "v_exp_f32 v57, v57\n"
    "v_mfma_f32_16x16x32_bf16 a[96:99], v[160:163], v[128:131], a[96:99]\n"
    "s_mov_b32 s95, s94\n"
    "ds_read_b128 v[176:179], v201 offset:40960\n"
    "v_add_f32 v114, v114, v81\n"
    "v_mfma_f32_16x16x32_bf16 a[96:99], v[164:167], v[144:147], a[96:99]\n"
    "s_sub_u32 s94, s94, 0x3000\n"
    "v_add_f32 v122, v122, v43\n"
    "v_add_f32 v113, v113, v82\n"
    "v_mfma_f32_16x16x32_bf16 a[100:103], v[164:167], v[148:151], a[100:103]\n"
    "s_min_u32 s94, s94, 0x6000\n"
    "v_add_f32 v125, v125, v44\n"
    "v_add_f32 v114, v114, v83\n"
    "v_mfma_f32_16x16x32_bf16 a[100:103], v[160:163], v[132:135], a[100:103]\n"
    "s_sub_u32 s93, s94, s95\n"
    "v_exp_f32 v58, v58\n"
    "v_mfma_f32_16x16x32_bf16 a[104:107], v[160:163], v[136:139], a[104:107]\n"
    "ds_read_b128 v[180:183], v201 offset:41984\n"
    "v_add_f32 v126, v126, v45\n"
    "v_mfma_f32_16x16x32_bf16 a[104:107], v[164:167], v[152:155], a[104:107]\n"
    "v_add_f32 v117, v117, v84\n"
    "v_add_f32 v125, v125, v46\n"
    "v_mfma_f32_16x16x32_bf16 a[108:111], v[164:167], v[156:159], a[108:111]\n"
    "v_exp_f32 v59, v59\n"
    "v_mfma_f32_16x16x32_bf16 a[108:111], v[160:163], v[140:143], a[108:111]\n"
    "s_waitcnt lgkmcnt(2)\n"
    "v_add_f32 v118, v118, v85\n"
    "v_add_f32 v126, v126, v47\n"
    "v_mfma_f32_16x16x32_bf16 a[124:127], v[168:171], v[140:143], a[124:127]\n"
    "ds_read_b128 v[160:163], v201 offset:43008\n"
    "v_add_f32 v117, v117, v86\n"
    "v_mfma_f32_16x16x32_bf16 a[124:127], v[172:175], v[156:159], a[124:127]\n"
    "v_add_f32 v113, v113, v48\n"
    "v_add_u32 v205, s93, v205\n"
    "v_mfma_f32_16x16x32_bf16 a[120:123], v[172:175], v[152:155], a[120:123]\n"
    "v_exp_f32 v60, v60\n"
    "v_mfma_f32_16x16x32_bf16 a[120:123], v[168:171], v[136:139], a[120:123]\n"
    "v_add_f32 v118, v118, v87\n"
    "v_add_f32 v114, v114, v49\n"
    "v_mfma_f32_16x16x32_bf16 a[116:119], v[168:171], v[132:135], a[116:119]\n"
    "ds_read_b128 v[164:167], v201 offset:44032\n"
    "v_add_u32 v206, s93, v206\n"
    "v_mfma_f32_16x16x32_bf16 a[116:119], v[172:175], v[148:151], a[116:119]\n"
    "v_add_f32 v121, v121, v88\n"
    "v_add_f32 v113, v113, v50\n"
    "v_mfma_f32_16x16x32_bf16 a[112:115], v[172:175], v[144:147], a[112:115]\n"
    "v_exp_f32 v61, v61\n"
    "v_mfma_f32_16x16x32_bf16 a[112:115], v[168:171], v[128:131], a[112:115]\n"
    "s_waitcnt lgkmcnt(2)\n"
    "v_add_u32 v220, 1, v220\n"
    "v_add_u32 v207, s93, v207\n"
    "v_mfma_f32_16x16x32_bf16 a[128:131], v[176:179], v[128:131], a[128:131]\n"
    "s_add_u32 s64, s64, 0xc000\n"
    "ds_read_b128 v[168:171], v201 offset:45056\n"
    "v_add_f32 v122, v122, v89\n"
    "v_mfma_f32_16x16x32_bf16 a[128:131], v[180:183], v[144:147], a[128:131]\n"
    "s_add_u32 s62, s62, 0x6000\n"
    "v_add_f32 v114, v114, v51\n"
    "v_add_f32 v121, v121, v90\n"
    "v_mfma_f32_16x16x32_bf16 a[132:135], v[180:183], v[148:151], a[132:135]\n"
    "s_add_u32 s63, s63, 0x6000\n"
    "v_add_f32 v117, v117, v52\n"
    "v_add_f32 v122, v122, v91\n"
    "v_mfma_f32_16x16x32_bf16 a[132:135], v[176:179], v[132:135], a[132:135]\n"
    "s_add_u32 s60, s60, 0x6000\n"
    "v_exp_f32 v62, v62\n"
    "v_mfma_f32_16x16x32_bf16 a[136:139], v[176:179], v[136:139], a[136:139]\n"
    "s_add_u32 s61, s61, 0x6000\n"
    "ds_read_b128 v[172:175], v201 offset:46080\n"
    "v_add_f32 v118, v118, v53\n"
    "v_mfma_f32_16x16x32_bf16 a[136:139], v[180:183], v[152:155], a[136:139]\n"
    "s_add_u32 s66, s66, s74\n"
    "v_add_f32 v125, v125, v92\n"
    "v_add_f32 v117, v117, v54\n"
    "v_mfma_f32_16x16x32_bf16 a[140:143], v[180:183], v[156:159], a[140:143]\n"
    "s_min_u32 s66, s66, 33\n"
    "v_exp_f32 v63, v63\n"
    "v_mfma_f32_16x16x32_bf16 a[140:143], v[176:179], v[140:143], a[140:143]\n"
    "s_waitcnt lgkmcnt(2)\n"
    "v_add_f32 v126, v126, v93\n"
    "v_add_f32 v118, v118, v55\n"
    "v_mfma_f32_16x16x32_bf16 a[156:159], v[160:163], v[140:143], a[156:159]\n"
    "ds_read_b128 v[176:179], v201 offset:47104\n"
    "v_add_f32 v125, v125, v94\n"
    "v_mfma_f32_16x16x32_bf16 a[156:159], v[164:167], v[156:159], a[156:159]\n"
    "s_waitcnt vmcnt(3)\n"
    "ds_read_b128 v[180:183], v201 offset:48128\n"
    "v_add_f32 v121, v121, v56\n"
    "v_mfma_f32_16x16x32_bf16 a[152:155], v[164:167], v[152:155], a[152:155]\n"
    "v_cmp_ne_u32 s[80:81], s67, v219\n"
    "v_add_f32 v126, v126, v95\n"
    "v_mfma_f32_16x16x32_bf16 a[152:155], v[160:163], v[136:139], a[152:155]\n"
    "v_cmp_gt_u32 vcc, s66, v218\n"
    "v_add_f32 v122, v122, v57\n"
    "v_mfma_f32_16x16x32_bf16 a[148:151], v[160:163], v[132:135], a[148:151]\n"
    "v_mfma_f32_16x16x32_bf16 a[148:151], v[164:167], v[148:151], a[148:151]\n"
    "s_or_b64 vcc, vcc, s[80:81]\n"
    "s_cbranch_vccnz LSPRE3\n"
    "LSPB3:\n"
    "v_mfma_f32_16x16x32_bf16 a[144:147], v[164:167], v[144:147], a[144:147]\n"
    "v_mfma_f32_16x16x32_bf16 a[144:147], v[160:163], v[128:131], a[144:147]\n"
    "s_waitcnt lgkmcnt(0)\n"
    "v_mfma_f32_16x16x32_bf16 a[160:163], v[168:171], v[128:131], a[160:163]\n"
    "s_barrier\n"
    "LMID:\n"
    "v_mfma_f32_16x16x32_bf16 a[160:163], v[172:175], v[144:147], a[160:163]\n"
    "buffer_store_dwordx2 v[220:221], v214, s[44:47], 0 offen sc1\n"
    "v_mfma_f32_16x16x32_bf16 a[164:167], v[172:175], v[148:151], a[164:167]\n"
    "ds_read_b128 v[160:163], v200 offset:24576\n"
    "ds_read_u16_d16_hi v223, v209 offset:6336\n"
    "v_mfma_f32_16x16x32_bf16 a[164:167], v[168:171], v[132:135], a[164:167]\n"
    "ds_read_u16_d16_hi v222, v209 offset:192\n"
    "v_add_f32 v121, v121, v58\n"
    "v_mfma_f32_16x16x32_bf16 a[168:171], v[168:171], v[136:139], a[168:171]\n"
    "ds_read_b128 v[164:167], v200 offset:25600\n"
    "ds_read_b128 v[232:235], v205 offset:24576\n"
    "v_mfma_f32_16x16x32_bf16 a[168:171], v[172:175], v[152:155], a[168:171]\n"
    "v_add_f32 v122, v122, v59\n"
    "ds_read_b128 v[236:239], v206 offset:24576\n"
    "v_mfma_f32_16x16x32_bf16 a[172:175], v[172:175], v[156:159], a[172:175]\n"
    "v_add_f32 v125, v125, v60\n"
    "v_add_f32 v126, v126, v61\n"
    "v_mfma_f32_16x16x32_bf16 a[172:175], v[168:171], v[140:143], a[172:175]\n"
    "v_add_f32 v125, v125, v62\n"
    "v_add_f32 v126, v126, v63\n"
    "v_mfma_f32_16x16x32_bf16 a[188:191], v[176:179], v[140:143], a[188:191]\n"
    "ds_read_b128 v[168:171], v200 offset:26624\n"
    "ds_read_b64 v[228:229], v210 offset:64\n"
    "v_mfma_f32_16x16x32_bf16 a[188:191], v[180:183], v[156:159], a[188:191]\n"
    "ds_read2st64_b32 v[240:241], v207 offset0:96 offset1:99\n"
    "ds_read2st64_b32 v[242:243], v207 offset0:102 offset1:105\n"
    "v_mfma_f32_16x16x32_bf16 a[184:187], v[176:179], v[136:139], a[184:187]\n"
    "ds_read2st64_b32 v[244:245], v207 offset0:108 offset1:111\n"
    "ds_read2st64_b32 v[246:247], v207 offset0:114 offset1:117\n"
    "v_mfma_f32_16x16x32_bf16 a[184:187], v[180:183], v[152:155], a[184:187]\n"
    "v_cvt_pk_bf16_f32 v140, v44, v45\n"
    "v_cvt_pk_bf16_f32 v141, v46, v47\n"
    "v_mfma_f32_16x16x32_bf16 a[180:183], v[176:179], v[132:135], a[180:183]\n"
    "s_add_u32 s59, s59, 1\n"
    "ds_read_b128 v[172:175], v200 offset:27648\n"
    "s_waitcnt lgkmcnt(7)\n"
    "v_mfma_f32_16x16x32_bf16 a[180:183], v[180:183], v[148:151], a[180:183]\n"
    "v_mul_f32 v226, s75, v223\n"
    "v_fma_f32 v227, -v222, v226, v217\n"
    "v_mfma_f32_16x16x32_bf16 a[176:179], v[176:179], v[128:131], a[176:179]\n"
    "s_cmp_lt_u32 s59, 15\n"
    "v_cvt_f32_i32 v232, v232\n"
    "v_cvt_f32_i32 v233, v233\n"
    "v_mfma_f32_16x16x32_bf16 a[176:179], v[180:183], v[144:147], a[176:179]\n"
    "s_cbranch_scc1 LLOOP\n"
    "LTAIL:\n"
    "s_waitcnt lgkmcnt(0)\n"
    "s_nop 1\n"
    "v_mfma_f32_16x16x32_bf16 v[64:67], v[160:163], a[192:195], v[96:99]\n"
    "ds_read_b128 v[176:179], v200 offset:28672\n"
    "v_cvt_pk_bf16_f32 v128, v32, v33\n"
    "v_mfma_f32_16x16x32_bf16 v[64:67], v[164:167], a[196:199], v[64:67]\n"
    "s_add_u32 m0, s71, 0\n"
    "v_cvt_pk_bf16_f32 v129, v34, v35\n"
    "v_cvt_pk_bf16_f32 v132, v36, v37\n"
    "v_mfma_f32_16x16x32_bf16 v[68:71], v[164:167], a[220:223], v[100:103]\n"
    "buffer_load_dwordx4 v200, s[36:39], s60 offen sc1 lds\n"
    "v_mfma_f32_16x16x32_bf16 v[68:71], v[160:163], a[216:219], v[68:71]\n"
    "v_cvt_pk_bf16_f32 v133, v38, v39\n"
    "v_cvt_pk_bf16_f32 v136, v40, v41\n"
    "v_mfma_f32_16x16x32_bf16 v[72:75], v[160:163], a[240:243], v[104:107]\n"
    "ds_read_b128 v[180:183], v200 offset:29696\n"
    "v_cvt_pk_bf16_f32 v137, v42, v43\n"
    "v_mfma_f32_16x16x32_bf16 v[72:75], v[164:167], a[244:247], v[72:75]\n"
    "v_cvt_pk_bf16_f32 v130, v48, v49\n"
    "v_cvt_pk_bf16_f32 v131, v50, v51\n"
    "v_mfma_f32_16x16x32_bf16 v[76:79], v[164:167], v[12:15], v[108:111]\n"
    "buffer_load_dwordx4 v200, s[36:39], s60 offen offset:1024 sc1 lds\n"
    "v_mfma_f32_16x16x32_bf16 v[76:79], v[160:163], v[8:11], v[76:79]\n"
    "v_cvt_pk_bf16_f32 v134, v52, v53\n"
    "v_cvt_pk_bf16_f32 v135, v54, v55\n"
    "v_mfma_f32_16x16x32_bf16 v[76:79], v[168:171], v[16:19], v[76:79]\n"
    "ds_read_b128 v[160:163], v200 offset:34816\n"
    "v_cvt_pk_bf16_f32 v138, v56, v57\n"
    "v_mfma_f32_16x16x32_bf16 v[76:79], v[172:175], v[20:23], v[76:79]\n"
    "v_cvt_pk_bf16_f32 v139, v58, v59\n"
    "v_cvt_pk_bf16_f32 v142, v60, v61\n"
    "v_mfma_f32_16x16x32_bf16 v[72:75], v[172:175], a[252:255], v[72:75]\n"
    "buffer_load_dwordx4 v200, s[36:39], s60 offen offset:2048 sc1 lds\n"
    "v_mfma_f32_16x16x32_bf16 v[72:75], v[168:171], a[248:251], v[72:75]\n"
    "v_cvt_pk_bf16_f32 v143, v62, v63\n"
    "v_add_f32 v113, v113, v114\n"
    "v_mfma_f32_16x16x32_bf16 v[68:71], v[168:171], a[224:227], v[68:71]\n"
    "ds_read_b128 v[164:167], v200 offset:35840\n"
    "v_add_f32 v117, v117, v118\n"
    "v_mfma_f32_16x16x32_bf16 v[68:71], v[172:175], a[228:231], v[68:71]\n"
    "v_add_f32 v121, v121, v122\n"
    "v_add_f32 v125, v125, v126\n"
    "v_mfma_f32_16x16x32_bf16 v[64:67], v[172:175], a[204:207], v[64:67]\n"
    "buffer_load_dwordx4 v200, s[36:39], s60 offen offset:3072 sc1 lds\n"
    "v_mfma_f32_16x16x32_bf16 v[64:67], v[168:171], a[200:203], v[64:67]\n"
    "s_waitcnt lgkmcnt(2)\n"
    "v_mov_b32 v115, v113\n"
    "v_mov_b32 v119, v117\n"
    "v_mfma_f32_16x16x32_bf16 v[64:67], v[176:179], a[208:211], v[64:67]\n"
    "s_add_u32 s65, s60, 0x1000\n"
    "ds_read_b128 v[168:171], v200 offset:32768\n"
    "v_mov_b32 v123, v121\n"
    "v_mfma_f32_16x16x32_bf16 v[64:67], v[180:183], a[212:215], v[64:67]\n"
    "s_add_u32 m0, s71, 0x1000\n"
    "v_mov_b32 v127, v125\n"
    "v_permlane32_swap_b32 v113, v115\n"
    "v_mfma_f32_16x16x32_bf16 v[68:71], v[180:183], a[236:239], v[68:71]\n"
    "buffer_load_dwordx4 v200, s[36:39], s65 offen sc1 lds\n"
    "v_mfma_f32_16x16x32_bf16 v[68:71], v[176:179], a[232:235], v[68:71]\n"
    "v_permlane32_swap_b32 v117, v119\n"
    "v_permlane32_swap_b32 v121, v123\n"
    "v_mfma_f32_16x16x32_bf16 v[72:75], v[176:179], v[0:3], v[72:75]\n"
    "ds_read_b128 v[172:175], v200 offset:33792\n"
    "v_permlane32_swap_b32 v125, v127\n"
    "v_mfma_f32_16x16x32_bf16 v[72:75], v[180:183], v[4:7], v[72:75]\n"
    "v_add_f32 v113, v113, v115\n"
    "v_add_f32 v117, v117, v119\n"
    "v_mfma_f32_16x16x32_bf16 v[76:79], v[180:183], v[28:31], v[76:79]\n"
    "buffer_load_dwordx4 v200, s[36:39], s65 offen offset:1024 sc1 lds\n"
    "v_mfma_f32_16x16x32_bf16 v[76:79], v[176:179], v[24:27], v[76:79]\n"
    "s_waitcnt lgkmcnt(2)\n"
    "v_add_f32 v121, v121, v123\n"
    "v_add_f32 v125, v125, v127\n"
    "v_mfma_f32_16x16x32_bf16 v[92:95], v[160:163], v[24:27], v[108:111]\n"
    "ds_read_b128 v[176:179], v200 offset:30720\n"
    "v_mov_b32 v115, v113\n"
    "v_mfma_f32_16x16x32_bf16 v[92:95], v[164:167], v[28:31], v[92:95]\n"
    "s_add_u32 m0, s71, 0x12000\n"
    "v_exp_f32 v64, v64\n"
    "v_mfma_f32_16x16x32_bf16 v[88:91], v[164:167], v[4:7], v[104:107]\n"
    "buffer_load_dwordx4 v200, s[40:43], s61 offen sc1 lds\n"
    "v_mfma_f32_16x16x32_bf16 v[88:91], v[160:163], v[0:3], v[88:91]\n"
    "v_exp_f32 v65, v65\n"
    "v_mfma_f32_16x16x32_bf16 v[84:87], v[160:163], a[232:235], v[100:103]\n"
    "ds_read_b128 v[180:183], v200 offset:31744\n"
    "v_cvt_pk_bf16_f32 v144, v64, v65\n"
    "v_mfma_f32_16x16x32_bf16 v[84:87], v[164:167], a[236:239], v[84:87]\n"
    "v_exp_f32 v66, v66\n"
    "v_mfma_f32_16x16x32_bf16 v[80:83], v[164:167], a[212:215], v[96:99]\n"
    "buffer_load_dwordx4 v200, s[40:43], s61 offen offset:1024 sc1 lds\n"
    "v_mfma_f32_16x16x32_bf16 v[80:83], v[160:163], a[208:211], v[80:83]\n"
    "s_waitcnt lgkmcnt(2)\n"
    "v_exp_f32 v67, v67\n"
    "v_mfma_f32_16x16x32_bf16 v[80:83], v[168:171], a[200:203], v[80:83]\n"
    "ds_read_b128 v[160:163], v200 offset:36864\n"
    "v_cvt_pk_bf16_f32 v145, v66, v67\n"
    "v_mfma_f32_16x16x32_bf16 v[80:83], v[172:175], a[204:207], v[80:83]\n"
    "v_exp_f32 v68, v68\n"
    "v_mfma_f32_16x16x32_bf16 v[84:87], v[172:175], a[228:231], v[84:87]\n"
    "buffer_load_dwordx4 v200, s[40:43], s61 offen offset:2048 sc1 lds\n"
    "v_mfma_f32_16x16x32_bf16 v[84:87], v[168:171], a[224:227], v[84:87]\n"
    "v_exp_f32 v69, v69\n"
    "v_mfma_f32_16x16x32_bf16 v[88:91], v[168:171], a[248:251], v[88:91]\n"
    "ds_read_b128 v[164:167], v200 offset:37888\n"
    "v_cvt_pk_bf16_f32 v148, v68, v69\n"
    "v_mfma_f32_16x16x32_bf16 v[88:91], v[172:175], a[252:255], v[88:91]\n"
    "v_exp_f32 v70, v70\n"
    "v_mfma_f32_16x16x32_bf16 v[92:95], v[172:175], v[20:23], v[92:95]\n"
    "buffer_load_dwordx4 v200, s[40:43], s61 offen offset:3072 sc1 lds\n"
    "v_mfma_f32_16x16x32_bf16 v[92:95], v[168:171], v[16:19], v[92:95]\n"
    "s_waitcnt lgkmcnt(2)\n"
    "v_exp_f32 v71, v71\n"
    "v_mfma_f32_16x16x32_bf16 v[92:95], v[176:179], v[8:11], v[92:95]\n"
    "s_add_u32 s65, s61, 0x1000\n"
    "ds_read_b128 v[168:171], v200 offset:38912\n"
    "v_cvt_pk_bf16_f32 v149, v70, v71\n"
    "v_mfma_f32_16x16x32_bf16 v[92:95], v[180:183], v[12:15], v[92:95]\n"
    "s_add_u32 m0, s71, 0x13000\n"
    "v_exp_f32 v72, v72\n"
    "v_mfma_f32_16x16x32_bf16 v[88:91], v[180:183], a[244:247], v[88:91]\n"
    "buffer_load_dwordx4 v200, s[40:43], s65 offen sc1 lds\n"
    "v_mfma_f32_16x16x32_bf16 v[88:91], v[176:179], a[240:243], v[88:91]\n"
    "v_exp_f32 v73, v73\n"
    "v_mfma_f32_16x16x32_bf16 v[84:87], v[176:179], a[216:219], v[84:87]\n"
    "ds_read_b128 v[172:175], v200 offset:39936\n"
    "v_cvt_pk_bf16_f32 v152, v72, v73\n"
    "v_mfma_f32_16x16x32_bf16 v[84:87], v[180:183], a[220:223], v[84:87]\n"
    "v_exp_f32 v74, v74\n"
    "v_mfma_f32_16x16x32_bf16 v[80:83], v[180:183], a[196:199], v[80:83]\n"
    "buffer_load_dwordx4 v200, s[40:43], s65 offen offset:1024 sc1 lds\n"
    "v_mfma_f32_16x16x32_bf16 v[80:83], v[176:179], a[192:195], v[80:83]\n"
    "s_waitcnt lgkmcnt(2)\n"
    "v_exp_f32 v75, v75\n"
    "v_mfma_f32_16x16x32_bf16 v[32:35], v[160:163], a[192:195], v[96:99]\n"
    "ds_read_b128 v[176:179], v200 offset:40960\n"
    "v_cvt_pk_bf16_f32 v153, v74, v75\n"
    "v_mfma_f32_16x16x32_bf16 v[32:35], v[164:167], a[196:199], v[32:35]\n"
    "v_exp_f32 v76, v76\n"
    "v_mfma_f32_16x16x32_bf16 v[36:39], v[164:167], a[220:223], v[100:103]\n"
    "v_exp_f32 v77, v77\n"
    "v_mfma_f32_16x16x32_bf16 v[36:39], v[160:163], a[216:219], v[36:39]\n"
    "v_exp_f32 v78, v78\n"
    "v_mfma_f32_16x16x32_bf16 v[40:43], v[160:163], a[240:243], v[104:107]\n"
    "ds_read_b128 v[180:183], v200 offset:41984\n"
    "v_cvt_pk_bf16_f32 v156, v76, v77\n"
    "v_mfma_f32_16x16x32_bf16 v[40:43], v[164:167], a[244:247], v[40:43]\n"
    "v_exp_f32 v79, v79\n"
    "v_mfma_f32_16x16x32_bf16 v[44:47], v[164:167], v[12:15], v[108:111]\n"
    "v_exp_f32 v80, v80\n"
    "v_mfma_f32_16x16x32_bf16 v[44:47], v[160:163], v[8:11], v[44:47]\n"
    "s_waitcnt lgkmcnt(2)\n"
    "v_exp_f32 v81, v81\n"
    "v_mfma_f32_16x16x32_bf16 v[44:47], v[168:171], v[16:19], v[44:47]\n"
    "ds_read_b128 v[160:163], v200 offset:47104\n"
    "v_cvt_pk_bf16_f32 v157, v78, v79\n"
    "v_mfma_f32_16x16x32_bf16 v[44:47], v[172:175], v[20:23], v[44:47]\n"
    "v_exp_f32 v82, v82\n"
    "v_mfma_f32_16x16x32_bf16 v[40:43], v[172:175], a[252:255], v[40:43]\n"
    "v_exp_f32 v83, v83\n"
    "v_mfma_f32_16x16x32_bf16 v[40:43], v[168:171], a[248:251], v[40:43]\n"
    "v_exp_f32 v84, v84\n"
    "v_mfma_f32_16x16x32_bf16 v[36:39], v[168:171], a[224:227], v[36:39]\n"
    "ds_read_b128 v[164:167], v200 offset:48128\n"
    "v_cvt_pk_bf16_f32 v146, v80, v81\n"
    "v_mfma_f32_16x16x32_bf16 v[36:39], v[172:175], a[228:231], v[36:39]\n"
    "v_exp_f32 v85, v85\n"
    "v_mfma_f32_16x16x32_bf16 v[32:35], v[172:175], a[204:207], v[32:35]\n"
    "v_exp_f32 v86, v86\n"
    "v_mfma_f32_16x16x32_bf16 v[32:35], v[168:171], a[200:203], v[32:35]\n"
    "s_waitcnt lgkmcnt(2)\n"
    "v_exp_f32 v87, v87\n"
    "v_mfma_f32_16x16x32_bf16 v[32:35], v[176:179], a[208:211], v[32:35]\n"
    "ds_read_b128 v[168:171], v200 offset:45056\n"
    "v_cvt_pk_bf16_f32 v147, v82, v83\n"
    "v_mfma_f32_16x16x32_bf16 v[32:35], v[180:183], a[212:215], v[32:35]\n"
    "v_exp_f32 v88, v88\n"
    "v_mfma_f32_16x16x32_bf16 v[36:39], v[180:183], a[236:239], v[36:39]\n"
    "v_exp_f32 v89, v89\n"
    "v_mfma_f32_16x16x32_bf16 v[36:39], v[176:179], a[232:235], v[36:39]\n"
    "v_exp_f32 v90, v90\n"
    "v_mfma_f32_16x16x32_bf16 v[40:43], v[176:179], v[0:3], v[40:43]\n"
    "ds_read_b128 v[172:175], v200 offset:46080\n"
    "v_cvt_pk_bf16_f32 v150, v84, v85\n"
    "v_mfma_f32_16x16x32_bf16 v[40:43], v[180:183], v[4:7], v[40:43]\n"
    "v_exp_f32 v91, v91\n"
    "v_mfma_f32_16x16x32_bf16 v[44:47], v[180:183], v[28:31], v[44:47]\n"
    "v_exp_f32 v92, v92\n"
    "v_mfma_f32_16x16x32_bf16 v[44:47], v[176:179], v[24:27], v[44:47]\n"
    "s_waitcnt lgkmcnt(2)\n"
    "v_exp_f32 v93, v93\n"
    "v_mfma_f32_16x16x32_bf16 v[60:63], v[160:163], v[24:27], v[108:111]\n"
    "ds_read_b128 v[176:179], v200 offset:43008\n"
    "v_cvt_pk_bf16_f32 v151, v86, v87\n"
    "v_mfma_f32_16x16x32_bf16 v[60:63], v[164:167], v[28:31], v[60:63]\n"
    "v_exp_f32 v94, v94\n"
    "v_mfma_f32_16x16x32_bf16 v[56:59], v[164:167], v[4:7], v[104:107]\n"
    "v_exp_f32 v95, v95\n"
    "v_mfma_f32_16x16x32_bf16 v[56:59], v[160:163], v[0:3], v[56:59]\n"
    "v_exp_f32 v32, v32\n"
    "v_mfma_f32_16x16x32_bf16 v[52:55], v[160:163], a[232:235], v[100:103]\n"
    "ds_read_b128 v[180:183], v200 offset:44032\n"
    "v_cvt_pk_bf16_f32 v154, v88, v89\n"
    "v_mfma_f32_16x16x32_bf16 v[52:55], v[164:167], a[236:239], v[52:55]\n"
    "v_exp_f32 v33, v33\n"
    "v_mfma_f32_16x16x32_bf16 v[48:51], v[164:167], a[212:215], v[96:99]\n"
    "s_waitcnt vmcnt(6)\n"
    "v_exp_f32 v34, v34\n"
    "v_mfma_f32_16x16x32_bf16 v[48:51], v[160:163], a[208:211], v[48:51]\n"
    "s_waitcnt lgkmcnt(2)\n"
    "v_exp_f32 v35, v35\n"
    "v_mfma_f32_16x16x32_bf16 v[48:51], v[168:171], a[200:203], v[48:51]\n"
    "s_barrier\n"
    "v_exp_f32 v36, v36\n"
    "v_mfma_f32_16x16x32_bf16 v[48:51], v[172:175], a[204:207], v[48:51]\n"
    "ds_read_b128 v[160:163], v200\n"
    "v_cvt_pk_bf16_f32 v155, v90, v91\n"
    "v_mfma_f32_16x16x32_bf16 v[52:55], v[172:175], a[228:231], v[52:55]\n"
    "v_exp_f32 v37, v37\n"
    "v_mfma_f32_16x16x32_bf16 v[52:55], v[168:171], a[224:227], v[52:55]\n"
    "v_exp_f32 v38, v38\n"
    "v_mfma_f32_16x16x32_bf16 v[56:59], v[168:171], a[248:251], v[56:59]\n"
    "ds_read_b128 v[164:167], v200 offset:1024\n"
    "v_cvt_pk_bf16_f32 v158, v92, v93\n"
    "v_mfma_f32_16x16x32_bf16 v[56:59], v[172:175], a[252:255], v[56:59]\n"
    "buffer_load_dwordx2 v[218:219], v213, s[44:47], 0 offen sc1\n"
    "v_mfma_f32_16x16x32_bf16 v[60:63], v[172:175], v[20:23], v[60:63]\n"
    "v_exp_f32 v39, v39\n"
    "v_mfma_f32_16x16x32_bf16 v[60:63], v[168:171], v[16:19], v[60:63]\n"
    "s_waitcnt lgkmcnt(2)\n"
    "v_exp_f32 v40, v40\n"
    "v_mfma_f32_16x16x32_bf16 v[60:63], v[176:179], v[8:11], v[60:63]\n"
    "ds_read_b128 v[168:171], v200 offset:2048\n"
    "v_cvt_pk_bf16_f32 v159, v94, v95\n"
    "v_mfma_f32_16x16x32_bf16 v[60:63], v[180:183], v[12:15], v[60:63]\n"
    "v_exp_f32 v41, v41\n"
    "v_mfma_f32_16x16x32_bf16 v[56:59], v[180:183], a[244:247], v[56:59]\n"
    "v_exp_f32 v42, v42\n"
    "v_mfma_f32_16x16x32_bf16 v[56:59], v[176:179], a[240:243], v[56:59]\n"
    "v_exp_f32 v43, v43\n"
    "v_mfma_f32_16x16x32_bf16 v[52:55], v[176:179], a[216:219], v[52:55]\n"
    "ds_read_b128 v[172:175], v200 offset:3072\n"
    "v_cvt_pk_bf16_f32 v32, v32, v33\n"
    "v_mfma_f32_16x16x32_bf16 v[52:55], v[180:183], a[220:223], v[52:55]\n"
    "v_exp_f32 v44, v44\n"
    "v_mfma_f32_16x16x32_bf16 v[48:51], v[180:183], a[196:199], v[48:51]\n"
    "v_exp_f32 v45, v45\n"
    "v_mfma_f32_16x16x32_bf16 v[48:51], v[176:179], a[192:195], v[48:51]\n"
    "s_waitcnt lgkmcnt(2)\n"
    "v_exp_f32 v46, v46\n"
    "v_mfma_f32_16x16x32_bf16 v[64:67], v[160:163], a[192:195], v[96:99]\n"
    "ds_read_b128 v[176:179], v200 offset:4096\n"
    "v_cvt_pk_bf16_f32 v33, v34, v35\n"
    "v_mfma_f32_16x16x32_bf16 v[64:67], v[164:167], a[196:199], v[64:67]\n"
    "v_exp_f32 v47, v47\n"
    "v_mfma_f32_16x16x32_bf16 v[68:71], v[164:167], a[220:223], v[100:103]\n"
    "v_cvt_pk_bf16_f32 v36, v36, v37\n"
    "v_cvt_pk_bf16_f32 v37, v38, v39\n"
    "v_mfma_f32_16x16x32_bf16 v[68:71], v[160:163], a[216:219], v[68:71]\n"
    "v_cvt_pk_bf16_f32 v40, v40, v41\n"
    "v_cvt_pk_bf16_f32 v41, v42, v43\n"
    "v_mfma_f32_16x16x32_bf16 v[72:75], v[160:163], a[240:243], v[104:107]\n"
    "ds_read_b128 v[180:183], v200 offset:5120\n"
    "v_cvt_pk_bf16_f32 v44, v44, v45\n"
    "v_mfma_f32_16x16x32_bf16 v[72:75], v[164:167], a[244:247], v[72:75]\n"
    "v_cvt_pk_bf16_f32 v45, v46, v47\n"
    "v_mov_b32 v119, v117\n"
    "v_mfma_f32_16x16x32_bf16 v[76:79], v[164:167], v[12:15], v[108:111]\n"
    "v_exp_f32 v48, v48\n"
    "v_mfma_f32_16x16x32_bf16 v[76:79], v[160:163], v[8:11], v[76:79]\n"
    "s_waitcnt lgkmcnt(2)\n"
    "v_exp_f32 v49, v49\n"
    "v_mfma_f32_16x16x32_bf16 v[76:79], v[168:171], v[16:19], v[76:79]\n"
    "ds_read_b128 v[160:163], v200 offset:10240\n"
    "v_cvt_pk_bf16_f32 v34, v48, v49\n"
    "v_mfma_f32_16x16x32_bf16 v[76:79], v[172:175], v[20:23], v[76:79]\n"
    "v_exp_f32 v50, v50\n"
    "v_mfma_f32_16x16x32_bf16 v[72:75], v[172:175], a[252:255], v[72:75]\n"
    "v_exp_f32 v51, v51\n"
    "v_mfma_f32_16x16x32_bf16 v[72:75], v[168:171], a[248:251], v[72:75]\n"
    "v_exp_f32 v52, v52\n"
    "v_mfma_f32_16x16x32_bf16 v[68:71], v[168:171], a[224:227], v[68:71]\n"
    "ds_read_b128 v[164:167], v200 offset:11264\n"
    "v_cvt_pk_bf16_f32 v35, v50, v51\n"
    "v_mfma_f32_16x16x32_bf16 v[68:71], v[172:175], a[228:231], v[68:71]\n"
    "v_exp_f32 v53, v53\n"
    "v_mfma_f32_16x16x32_bf16 v[64:67], v[172:175], a[204:207], v[64:67]\n"
    "v_exp_f32 v54, v54\n"
    "v_mfma_f32_16x16x32_bf16 v[64:67], v[168:171], a[200:203], v[64:67]\n"
    "s_waitcnt lgkmcnt(2)\n"
    "v_exp_f32 v55, v55\n"
    "v_mfma_f32_16x16x32_bf16 v[64:67], v[176:179], a[208:211], v[64:67]\n"
    "s_add_u32 s66, s66, s74\n"
    "ds_read_b128 v[168:171], v200 offset:8192\n"
    "v_cvt_pk_bf16_f32 v38, v52, v53\n"
    "v_mfma_f32_16x16x32_bf16 v[64:67], v[180:183], a[212:215], v[64:67]\n"
    "s_add_u32 s60, s72, 0xbd000\n"
    "v_exp_f32 v56, v56\n"
    "v_mfma_f32_16x16x32_bf16 v[68:71], v[180:183], a[236:239], v[68:71]\n"
    "s_add_u32 s61, s71, 0xba000\n"
    "v_exp_f32 v57, v57\n"
    "v_mfma_f32_16x16x32_bf16 v[68:71], v[176:179], a[232:235], v[68:71]\n"
    "v_exp_f32 v58, v58\n"
    "v_mfma_f32_16x16x32_bf16 v[72:75], v[176:179], v[0:3], v[72:75]\n"
    "ds_read_b128 v[172:175], v200 offset:9216\n"
    "v_cvt_pk_bf16_f32 v39, v54, v55\n"
    "v_mfma_f32_16x16x32_bf16 v[72:75], v[180:183], v[4:7], v[72:75]\n"
    "v_exp_f32 v59, v59\n"
    "v_mfma_f32_16x16x32_bf16 v[76:79], v[180:183], v[28:31], v[76:79]\n"
    "s_waitcnt vmcnt(0)\n"
    "v_cmp_ne_u32 s[80:81], s67, v219\n"
    "v_cvt_pk_bf16_f32 v42, v56, v57\n"
    "v_mfma_f32_16x16x32_bf16 v[76:79], v[176:179], v[24:27], v[76:79]\n"
    "s_waitcnt lgkmcnt(2)\n"
    "v_cmp_gt_u32 vcc, s66, v218\n"
    "v_cvt_pk_bf16_f32 v43, v58, v59\n"
    "v_mfma_f32_16x16x32_bf16 v[92:95], v[160:163], v[24:27], v[108:111]\n"
    "ds_read_b128 v[176:179], v200 offset:6144\n"
    "v_mov_b32 v123, v121\n"
    "v_mfma_f32_16x16x32_bf16 v[92:95], v[164:167], v[28:31], v[92:95]\n"
    "s_or_b64 vcc, vcc, s[80:81]\n"
    "v_exp_f32 v64, v64\n"
    "v_mfma_f32_16x16x32_bf16 v[88:91], v[164:167], v[4:7], v[104:107]\n"
    "s_cbranch_vccnz LSPRE5\n"
    "LSPB5:\n"
    "v_exp_f32 v65, v65\n"
    "v_mfma_f32_16x16x32_bf16 v[88:91], v[160:163], v[0:3], v[88:91]\n"
    "v_exp_f32 v66, v66\n"
    "v_mfma_f32_16x16x32_bf16 v[84:87], v[160:163], a[232:235], v[100:103]\n"
    "s_barrier\n"
    "ds_read_b128 v[180:183], v200 offset:7168\n"
    "v_cvt_pk_bf16_f32 v64, v64, v65\n"
    "v_mfma_f32_16x16x32_bf16 v[84:87], v[164:167], a[236:239], v[84:87]\n"
    "s_add_u32 m0, s72, 0x6000\n"
    "v_exp_f32 v67, v67\n"
    "v_mfma_f32_16x16x32_bf16 v[80:83], v[164:167], a[212:215], v[96:99]\n"
    "buffer_load_dwordx4 v200, s[36:39], s60 offen sc1 lds\n"
    "v_mfma_f32_16x16x32_bf16 v[80:83], v[160:163], a[208:211], v[80:83]\n"
    "s_waitcnt lgkmcnt(2)\n"
    "v_exp_f32 v68, v68\n"
    "v_mfma_f32_16x16x32_bf16 v[80:83], v[168:171], a[200:203], v[80:83]\n"
    "ds_read_b128 v[160:163], v200 offset:12288\n"
    "v_cvt_pk_bf16_f32 v65, v66, v67\n"
    "v_mfma_f32_16x16x32_bf16 v[80:83], v[172:175], a[204:207], v[80:83]\n"
    "buffer_load_dwordx4 v200, s[36:39], s60 offen offset:1024 sc1 lds\n"
    "v_mfma_f32_16x16x32_bf16 v[84:87], v[172:175], a[228:231], v[84:87]\n"
    "v_exp_f32 v69, v69\n"
    "v_mfma_f32_16x16x32_bf16 v[84:87], v[168:171], a[224:227], v[84:87]\n"
    "buffer_load_dwordx4 v200, s[36:39], s60 offen offset:2048 sc1 lds\n"
    "v_mfma_f32_16x16x32_bf16 v[88:91], v[168:171], a[248:251], v[88:91]\n"
    "ds_read_b128 v[164:167], v200 offset:13312\n"
    "v_cvt_pk_bf16_f32 v68, v68, v69\n"
    "v_mfma_f32_16x16x32_bf16 v[88:91], v[172:175], a[252:255], v[88:91]\n"
    "v_exp_f32 v60, v60\n"
    "v_mfma_f32_16x16x32_bf16 v[92:95], v[172:175], v[20:23], v[92:95]\n"
    "s_add_u32 m0, s71, 0x18000\n"
    "v_exp_f32 v70, v70\n"
    "v_mfma_f32_16x16x32_bf16 v[92:95], v[168:171], v[16:19], v[92:95]\n"
    "s_waitcnt lgkmcnt(2)\n"
    "buffer_load_dwordx4 v200, s[40:43], s61 offen sc1 lds\n"
    "v_mfma_f32_16x16x32_bf16 v[92:95], v[176:179], v[8:11], v[92:95]\n"
    "ds_read_b128 v[168:171], v200 offset:14336\n"
    "v_mov_b32 v127, v125\n"
    "v_mfma_f32_16x16x32_bf16 v[92:95], v[180:183], v[12:15], v[92:95]\n"
    "v_exp_f32 v61, v61\n"
    "v_mfma_f32_16x16x32_bf16 v[88:91], v[180:183], a[244:247], v[88:91]\n"
    "v_exp_f32 v71, v71\n"
    "v_mfma_f32_16x16x32_bf16 v[88:91], v[176:179], a[240:243], v[88:91]\n"
    "v_exp_f32 v62, v62\n"
    "v_mfma_f32_16x16x32_bf16 v[84:87], v[176:179], a[216:219], v[84:87]\n"
    "ds_read_b128 v[172:175], v200 offset:15360\n"
    "v_cvt_pk_bf16_f32 v46, v60, v61\n"
    "v_mfma_f32_16x16x32_bf16 v[84:87], v[180:183], a[220:223], v[84:87]\n"
    "v_exp_f32 v72, v72\n"
    "v_mfma_f32_16x16x32_bf16 v[80:83], v[180:183], a[196:199], v[80:83]\n"
    "v_exp_f32 v63, v63\n"
    "v_mfma_f32_16x16x32_bf16 v[80:83], v[176:179], a[192:195], v[80:83]\n"
    "s_waitcnt lgkmcnt(2)\n"
    "buffer_load_dwordx4 v200, s[40:43], s61 offen offset:1024 sc1 lds\n"
    "v_mfma_f32_16x16x32_bf16 v[232:235], v[160:163], a[192:195], v[96:99]\n"
    "ds_read_b128 v[176:179], v200 offset:16384\n"
    "v_cvt_pk_bf16_f32 v47, v62, v63\n"
    "v_mfma_f32_16x16x32_bf16 v[232:235], v[164:167], a[196:199], v[232:235]\n"
    "v_exp_f32 v73, v73\n"
    "v_mfma_f32_16x16x32_bf16 v[236:239], v[164:167], a[220:223], v[100:103]\n"
    "v_exp_f32 v74, v74\n"
    "v_mfma_f32_16x16x32_bf16 v[236:239], v[160:163], a[216:219], v[236:239]\n"
    "v_exp_f32 v75, v75\n"
    "v_mfma_f32_16x16x32_bf16 v[240:243], v[160:163], a[240:243], v[104:107]\n"
    "ds_read_b128 v[180:183], v200 offset:17408\n"
    "v_cvt_pk_bf16_f32 v69, v70, v71\n"
    "v_mfma_f32_16x16x32_bf16 v[240:243], v[164:167], a[244:247], v[240:243]\n"
    "v_exp_f32 v76, v76\n"
    "v_mfma_f32_16x16x32_bf16 v[244:247], v[164:167], v[12:15], v[108:111]\n"
    "v_exp_f32 v77, v77\n"
    "v_mfma_f32_16x16x32_bf16 v[244:247], v[160:163], v[8:11], v[244:247]\n"
    "s_waitcnt lgkmcnt(2)\n"
    "buffer_load_dwordx4 v200, s[40:43], s61 offen offset:2048 sc1 lds\n"
    "v_mfma_f32_16x16x32_bf16 v[244:247], v[168:171], v[16:19], v[244:247]\n"
    "ds_read_b128 v[160:163], v200 offset:22528\n"
    "v_cvt_pk_bf16_f32 v72, v72, v73\n"
    "v_mfma_f32_16x16x32_bf16 v[244:247], v[172:175], v[20:23], v[244:247]\n"
    "v_exp_f32 v78, v78\n"
    "v_mfma_f32_16x16x32_bf16 v[240:243], v[172:175], a[252:255], v[240:243]\n"
    "v_exp_f32 v79, v79\n"
    "v_mfma_f32_16x16x32_bf16 v[240:243], v[168:171], a[248:251], v[240:243]\n"
    "v_exp_f32 v80, v80\n"
    "v_mfma_f32_16x16x32_bf16 v[236:239], v[168:171], a[224:227], v[236:239]\n"
    "ds_read_b128 v[164:167], v200 offset:23552\n"
    "v_cvt_pk_bf16_f32 v73, v74, v75\n"
    "v_mfma_f32_16x16x32_bf16 v[236:239], v[172:175], a[228:231], v[236:239]\n"
    "v_exp_f32 v81, v81\n"
    "v_mfma_f32_16x16x32_bf16 v[232:235], v[172:175], a[204:207], v[232:235]\n"
    "v_exp_f32 v82, v82\n"
    "v_mfma_f32_16x16x32_bf16 v[232:235], v[168:171], a[200:203], v[232:235]\n"
    "s_waitcnt lgkmcnt(2)\n"
    "buffer_load_dwordx4 v200, s[40:43], s61 offen offset:3072 sc1 lds\n"
    "v_mfma_f32_16x16x32_bf16 v[232:235], v[176:179], a[208:211], v[232:235]\n"
    "ds_read_b128 v[168:171], v200 offset:20480\n"
    "v_cvt_pk_bf16_f32 v76, v76, v77\n"
    "v_mfma_f32_16x16x32_bf16 v[232:235], v[180:183], a[212:215], v[232:235]\n"
    "v_exp_f32 v83, v83\n"
    "v_mfma_f32_16x16x32_bf16 v[236:239], v[180:183], a[236:239], v[236:239]\n"
    "v_exp_f32 v84, v84\n"
    "v_mfma_f32_16x16x32_bf16 v[236:239], v[176:179], a[232:235], v[236:239]\n"
    "v_exp_f32 v85, v85\n"
    "v_mfma_f32_16x16x32_bf16 v[240:243], v[176:179], v[0:3], v[240:243]\n"
    "ds_read_b128 v[172:175], v200 offset:21504\n"
    "v_cvt_pk_bf16_f32 v77, v78, v79\n"
    "v_mfma_f32_16x16x32_bf16 v[240:243], v[180:183], v[4:7], v[240:243]\n"
    "s_add_u32 s65, s61, 0x1000\n"
    "v_exp_f32 v86, v86\n"
    "v_mfma_f32_16x16x32_bf16 v[244:247], v[180:183], v[28:31], v[244:247]\n"
    "s_add_u32 m0, s71, 0x19000\n"
    "v_exp_f32 v87, v87\n"
    "v_mfma_f32_16x16x32_bf16 v[244:247], v[176:179], v[24:27], v[244:247]\n"
    "s_waitcnt lgkmcnt(2)\n"
    "buffer_load_dwordx4 v200, s[40:43], s65 offen sc1 lds\n"
    "v_mfma_f32_16x16x32_bf16 v[60:63], v[160:163], v[24:27], v[108:111]\n"
    "ds_read_b128 v[176:179], v200 offset:18432\n"
    "v_cvt_pk_bf16_f32 v66, v80, v81\n"
    "v_mfma_f32_16x16x32_bf16 v[60:63], v[164:167], v[28:31], v[60:63]\n"
    "v_exp_f32 v88, v88\n"
    "v_mfma_f32_16x16x32_bf16 v[56:59], v[164:167], v[4:7], v[104:107]\n"
    "v_exp_f32 v89, v89\n"
    "v_mfma_f32_16x16x32_bf16 v[56:59], v[160:163], v[0:3], v[56:59]\n"
    "v_exp_f32 v90, v90\n"
    "v_mfma_f32_16x16x32_bf16 v[52:55], v[160:163], a[232:235], v[100:103]\n"
    "ds_read_b128 v[180:183], v200 offset:19456\n"
    "v_cvt_pk_bf16_f32 v67, v82, v83\n"
    "v_mfma_f32_16x16x32_bf16 v[52:55], v[164:167], a[236:239], v[52:55]\n"
    "v_exp_f32 v91, v91\n"
    "v_mfma_f32_16x16x32_bf16 v[48:51], v[164:167], a[212:215], v[96:99]\n"
    "s_waitcnt vmcnt(5)\n"
    "v_exp_f32 v92, v92\n"
    "v_mfma_f32_16x16x32_bf16 v[48:51], v[160:163], a[208:211], v[48:51]\n"
    "s_waitcnt lgkmcnt(2)\n"
    "buffer_load_dwordx4 v200, s[40:43], s65 offen offset:1024 sc1 lds\n"
    "v_mfma_f32_16x16x32_bf16 v[48:51], v[168:171], a[200:203], v[48:51]\n"
    "s_barrier\n"
    "v_exp_f32 v93, v93\n"
    "v_mfma_f32_16x16x32_bf16 v[48:51], v[172:175], a[204:207], v[48:51]\n"
    "ds_read_b128 v[160:163], v200 offset:24576\n"
    "v_cvt_pk_bf16_f32 v70, v84, v85\n"
    "v_mfma_f32_16x16x32_bf16 v[52:55], v[172:175], a[228:231], v[52:55]\n"
    "v_exp_f32 v94, v94\n"
    "v_mfma_f32_16x16x32_bf16 v[52:55], v[168:171], a[224:227], v[52:55]\n"
    "v_exp_f32 v95, v95\n"
    "v_mfma_f32_16x16x32_bf16 v[56:59], v[168:171], a[248:251], v[56:59]\n"
    "ds_read_b128 v[164:167], v200 offset:25600\n"
    "v_cvt_pk_bf16_f32 v71, v86, v87\n"
    "v_mfma_f32_16x16x32_bf16 v[56:59], v[172:175], a[252:255], v[56:59]\n"
    "v_exp_f32 v232, v232\n"
    "v_mfma_f32_16x16x32_bf16 v[60:63], v[172:175], v[20:23], v[60:63]\n"
    "v_exp_f32 v233, v233\n"
    "v_mfma_f32_16x16x32_bf16 v[60:63], v[168:171], v[16:19], v[60:63]\n"
    "s_waitcnt lgkmcnt(2)\n"
    "v_exp_f32 v234, v234\n"
    "v_mfma_f32_16x16x32_bf16 v[60:63], v[176:179], v[8:11], v[60:63]\n"
    "s_lshl_b32 s76, s56, 3\n"
    "ds_read_b128 v[168:171], v200 offset:26624\n"
    "v_cvt_pk_bf16_f32 v74, v88, v89\n"
    "v_mfma_f32_16x16x32_bf16 v[60:63], v[180:183], v[12:15], v[60:63]\n"
    "s_add_u32 s76, s76, s57\n"
    "v_exp_f32 v235, v235\n"
    "v_mfma_f32_16x16x32_bf16 v[56:59], v[180:183], a[244:247], v[56:59]\n"
    "s_mul_i32 s76, s76, 0x18000\n"
    "v_exp_f32 v236, v236\n"
    "v_mfma_f32_16x16x32_bf16 v[56:59], v[176:179], a[240:243], v[56:59]\n"
    "s_add_u32 s76, s76, s88\n"
    "v_exp_f32 v237, v237\n"
    "v_mfma_f32_16x16x32_bf16 v[52:55], v[176:179], a[216:219], v[52:55]\n"
    "s_add_u32 s28, s24, s76\n"
    "ds_read_b128 v[172:175], v200 offset:27648\n"
    "v_cvt_pk_bf16_f32 v75, v90, v91\n"
    "v_mfma_f32_16x16x32_bf16 v[52:55], v[180:183], a[220:223], v[52:55]\n"
    "s_addc_u32 s29, s25, 0\n"
    "v_exp_f32 v238, v238\n"
    "v_mfma_f32_16x16x32_bf16 v[48:51], v[180:183], a[196:199], v[48:51]\n"
    "s_and_b32 s29, s29, 0xffff\n"
    "v_exp_f32 v239, v239\n"
    "v_mfma_f32_16x16x32_bf16 v[48:51], v[176:179], a[192:195], v[48:51]\n"
    "s_waitcnt lgkmcnt(2)\n"
    "v_exp_f32 v240, v240\n"
    "v_mfma_f32_16x16x32_bf16 v[80:83], v[160:163], a[192:195], v[96:99]\n"
    "s_mov_b32 s30, 0x6000\n"
    "ds_read_b128 v[176:179], v200 offset:28672\n"
    "v_cvt_pk_bf16_f32 v78, v92, v93\n"
    "v_mfma_f32_16x16x32_bf16 v[80:83], v[164:167], a[196:199], v[80:83]\n"
    "s_mov_b32 s31, 0x20000\n"
    "v_cvt_pk_bf16_f32 v79, v94, v95\n"
    "v_permlane16_swap_b32 v113, v115\n"
    "v_mfma_f32_16x16x32_bf16 v[84:87], v[164:167], a[220:223], v[100:103]\n"
    "s_xor_b32 s94, s70, 0x80000000\n"
    "v_exp_f32 v241, v241\n"
    "v_mfma_f32_16x16x32_bf16 v[84:87], v[160:163], a[216:219], v[84:87]\n"
    "s_xor_b32 s95, s70, 0x80000000\n"
    "v_exp_f32 v242, v242\n"
    "v_mfma_f32_16x16x32_bf16 v[88:91], v[160:163], a[240:243], v[104:107]\n"
    "s_mov_b32 s62, 0x1800\n"
    "ds_read_b128 v[180:183], v200 offset:29696\n"
    "v_permlane16_swap_b32 v117, v119\n"
    "v_mfma_f32_16x16x32_bf16 v[88:91], v[164:167], a[244:247], v[88:91]\n"
    "s_mov_b32 s63, 0x3000\n"
    "v_exp_f32 v243, v243\n"
    "v_mfma_f32_16x16x32_bf16 v[92:95], v[164:167], v[12:15], v[108:111]\n"
    "s_mov_b32 s64, 0x4800\n"
    "v_exp_f32 v244, v244\n"
    "v_mfma_f32_16x16x32_bf16 v[92:95], v[160:163], v[8:11], v[92:95]\n"
    "s_waitcnt lgkmcnt(2)\n"
    "v_exp_f32 v245, v245\n"
    "v_mfma_f32_16x16x32_bf16 v[92:95], v[168:171], v[16:19], v[92:95]\n"
    "ds_read_b128 v[160:163], v200 offset:34816\n"
    "v_permlane16_swap_b32 v121, v123\n"
    "v_mfma_f32_16x16x32_bf16 v[92:95], v[172:175], v[20:23], v[92:95]\n"
    "v_exp_f32 v246, v246\n"
    "v_mfma_f32_16x16x32_bf16 v[88:91], v[172:175], a[252:255], v[88:91]\n"
    "v_exp_f32 v247, v247\n"
    "v_mfma_f32_16x16x32_bf16 v[88:91], v[168:171], a[248:251], v[88:91]\n"
    "v_exp_f32 v48, v48\n"
    "v_mfma_f32_16x16x32_bf16 v[84:87], v[168:171], a[224:227], v[84:87]\n"
    "ds_read_b128 v[164:167], v200 offset:35840\n"
    "v_permlane16_swap_b32 v125, v127\n"
    "v_mfma_f32_16x16x32_bf16 v[84:87], v[172:175], a[228:231], v[84:87]\n"
    "v_exp_f32 v49, v49\n"
    "v_mfma_f32_16x16x32_bf16 v[80:83], v[172:175], a[204:207], v[80:83]\n"
    "v_exp_f32 v50, v50\n"
    "v_mfma_f32_16x16x32_bf16 v[80:83], v[168:171], a[200:203], v[80:83]\n"
    "s_waitcnt lgkmcnt(2)\n"
    "v_exp_f32 v51, v51\n"
    "v_mfma_f32_16x16x32_bf16 v[80:83], v[176:179], a[208:211], v[80:83]\n"
    "ds_read_b128 v[168:171], v200 offset:32768\n"
    "v_add_f32 v113, v113, v115\n"
    "v_mfma_f32_16x16x32_bf16 v[80:83], v[180:183], a[212:215], v[80:83]\n"
    "v_exp_f32 v52, v52\n"
    "v_mfma_f32_16x16x32_bf16 v[84:87], v[180:183], a[236:239], v[84:87]\n"
    "v_exp_f32 v53, v53\n"
    "v_mfma_f32_16x16x32_bf16 v[84:87], v[176:179], a[232:235], v[84:87]\n"
    "v_exp_f32 v54, v54\n"
    "v_mfma_f32_16x16x32_bf16 v[88:91], v[176:179], v[0:3], v[88:91]\n"
    "ds_read_b128 v[172:175], v200 offset:33792\n"
    "v_add_f32 v117, v117, v119\n"
    "v_mfma_f32_16x16x32_bf16 v[88:91], v[180:183], v[4:7], v[88:91]\n"
    "v_add_f32 v121, v121, v123\n"
    "v_add_f32 v125, v125, v127\n"
    "v_mfma_f32_16x16x32_bf16 v[92:95], v[180:183], v[28:31], v[92:95]\n"
    "v_exp_f32 v55, v55\n"
    "v_mfma_f32_16x16x32_bf16 v[92:95], v[176:179], v[24:27], v[92:95]\n"
    "s_waitcnt lgkmcnt(2)\n"
    "v_add_f32 v112, v112, v113\n"
    "v_add_f32 v116, v116, v117\n"
    "v_mfma_f32_16x16x32_bf16 v[252:255], v[160:163], v[24:27], v[108:111]\n"
    "ds_read_b128 v[176:179], v200 offset:30720\n"
    "v_add_f32 v120, v120, v121\n"
    "v_mfma_f32_16x16x32_bf16 v[252:255], v[164:167], v[28:31], v[252:255]\n"
    "v_add_f32 v124, v124, v125\n"
    "v_and_b32 v198, 15, v231\n"
    "v_mfma_f32_16x16x32_bf16 v[248:251], v[164:167], v[4:7], v[104:107]\n"
    "v_mul_u32_u24 v205, 0x180, v198\n"
    "v_lshrrev_b32 v199, 4, v231\n"
    "v_mfma_f32_16x16x32_bf16 v[248:251], v[160:163], v[0:3], v[248:251]\n"
    "v_lshl_add_u32 v205, v199, 4, v205\n"
    "v_add_u32 v207, 0x18000, v200\n"
    "v_mfma_f32_16x16x32_bf16 v[188:191], v[160:163], a[232:235], v[100:103]\n"
    "ds_read_b128 v[180:183], v200 offset:31744\n"
    "v_cvt_pk_bf16_f32 v232, v232, v233\n"
    "v_mfma_f32_16x16x32_bf16 v[188:191], v[164:167], a[236:239], v[188:191]\n"
    "v_exp_f32 v56, v56\n"
    "v_mfma_f32_16x16x32_bf16 v[184:187], v[164:167], a[212:215], v[96:99]\n"
    "v_exp_f32 v57, v57\n"
    "v_mfma_f32_16x16x32_bf16 v[184:187], v[160:163], a[208:211], v[184:187]\n"
    "s_waitcnt lgkmcnt(2)\n"
    "v_exp_f32 v58, v58\n"
    "v_mfma_f32_16x16x32_bf16 v[184:187], v[168:171], a[200:203], v[184:187]\n"
    "ds_read_b128 v[160:163], v201\n"
    "v_cvt_pk_bf16_f32 v233, v234, v235\n"
    "v_mfma_f32_16x16x32_bf16 v[184:187], v[172:175], a[204:207], v[184:187]\n"
    "v_exp_f32 v59, v59\n"
    "v_mfma_f32_16x16x32_bf16 v[188:191], v[172:175], a[228:231], v[188:191]\n"
    "v_exp_f32 v60, v60\n"
    "v_mfma_f32_16x16x32_bf16 v[188:191], v[168:171], a[224:227], v[188:191]\n"
    "v_exp_f32 v61, v61\n"
    "v_mfma_f32_16x16x32_bf16 v[248:251], v[168:171], a[248:251], v[248:251]\n"
    "ds_read_b128 v[164:167], v201 offset:1024\n"
    "v_cvt_pk_bf16_f32 v236, v236, v237\n"
    "v_mfma_f32_16x16x32_bf16 v[248:251], v[172:175], a[252:255], v[248:251]\n"
    "v_exp_f32 v62, v62\n"
    "v_mfma_f32_16x16x32_bf16 v[252:255], v[172:175], v[20:23], v[252:255]\n"
    "v_exp_f32 v63, v63\n"
    "v_mfma_f32_16x16x32_bf16 v[252:255], v[168:171], v[16:19], v[252:255]\n"
    "s_waitcnt lgkmcnt(2)\n"
    "v_exp_f32 v80, v80\n"
    "v_mfma_f32_16x16x32_bf16 v[252:255], v[176:179], v[8:11], v[252:255]\n"
    "ds_read_b128 v[168:171], v201 offset:24576\n"
    "v_cvt_pk_bf16_f32 v237, v238, v239\n"
    "v_mfma_f32_16x16x32_bf16 v[252:255], v[180:183], v[12:15], v[252:255]\n"
    "v_exp_f32 v81, v81\n"
    "v_mfma_f32_16x16x32_bf16 v[248:251], v[180:183], a[244:247], v[248:251]\n"
    "v_exp_f32 v82, v82\n"
    "v_mfma_f32_16x16x32_bf16 v[248:251], v[176:179], a[240:243], v[248:251]\n"
    "v_exp_f32 v83, v83\n"
    "v_mfma_f32_16x16x32_bf16 v[188:191], v[176:179], a[216:219], v[188:191]\n"
    "ds_read_b128 v[172:175], v201 offset:25600\n"
    "v_cvt_pk_bf16_f32 v240, v240, v241\n"
    "v_mfma_f32_16x16x32_bf16 v[188:191], v[180:183], a[220:223], v[188:191]\n"
    "v_exp_f32 v84, v84\n"
    "v_mfma_f32_16x16x32_bf16 v[184:187], v[180:183], a[196:199], v[184:187]\n"
    "v_exp_f32 v85, v85\n"
    "v_mfma_f32_16x16x32_bf16 v[184:187], v[176:179], a[192:195], v[184:187]\n"
    "s_waitcnt lgkmcnt(2)\n"
    "v_exp_f32 v86, v86\n"
    "v_mfma_f32_16x16x32_bf16 a[0:3], v[160:163], v[128:131], a[0:3]\n"
    "ds_read_b128 v[176:179], v201 offset:2048\n"
    "v_cvt_pk_bf16_f32 v241, v242, v243\n"
    "v_mfma_f32_16x16x32_bf16 a[0:3], v[164:167], v[144:147], a[0:3]\n"
    "v_exp_f32 v87, v87\n"
    "v_mfma_f32_16x16x32_bf16 a[4:7], v[160:163], v[132:135], a[4:7]\n"
    "v_exp_f32 v88, v88\n"
    "v_mfma_f32_16x16x32_bf16 a[4:7], v[164:167], v[148:151], a[4:7]\n"
    "v_exp_f32 v89, v89\n"
    "v_mfma_f32_16x16x32_bf16 a[8:11], v[160:163], v[136:139], a[8:11]\n"
    "ds_read_b128 v[180:183], v201 offset:3072\n"
    "v_cvt_pk_bf16_f32 v244, v244, v245\n"
    "v_mfma_f32_16x16x32_bf16 a[8:11], v[164:167], v[152:155], a[8:11]\n"
    "v_cvt_pk_bf16_f32 v245, v246, v247\n"
    "v_cvt_pk_bf16_f32 v234, v48, v49\n"
    "v_mfma_f32_16x16x32_bf16 a[12:15], v[160:163], v[140:143], a[12:15]\n"
    "v_cvt_pk_bf16_f32 v235, v50, v51\n"
    "v_cvt_pk_bf16_f32 v238, v52, v53\n"
    "v_mfma_f32_16x16x32_bf16 a[12:15], v[164:167], v[156:159], a[12:15]\n"
    "v_cvt_pk_bf16_f32 v239, v54, v55\n"
    "v_cvt_pk_bf16_f32 v242, v56, v57\n"
    "v_mfma_f32_16x16x32_bf16 v[112:115], v[192:195], v[144:147], v[112:115]\n"
    "v_exp_f32 v90, v90\n"
    "v_mfma_f32_16x16x32_bf16 v[112:115], v[192:195], v[32:35], v[112:115]\n"
    "v_cvt_pk_bf16_f32 v243, v58, v59\n"
    "v_cvt_pk_bf16_f32 v246, v60, v61\n"
    "v_mfma_f32_16x16x32_bf16 v[112:115], v[192:195], v[64:67], v[112:115]\n"
    "v_cvt_pk_bf16_f32 v247, v62, v63\n"
    "v_cvt_pk_bf16_f32 v80, v80, v81\n"
    "v_mfma_f32_16x16x32_bf16 v[116:119], v[192:195], v[148:151], v[116:119]\n"
    "v_exp_f32 v91, v91\n"
    "v_mfma_f32_16x16x32_bf16 v[116:119], v[192:195], v[36:39], v[116:119]\n"
    "v_exp_f32 v92, v92\n"
    "v_mfma_f32_16x16x32_bf16 v[116:119], v[192:195], v[68:71], v[116:119]\n"
    "s_waitcnt lgkmcnt(2)\n"
    "v_exp_f32 v93, v93\n"
    "v_mfma_f32_16x16x32_bf16 a[0:3], v[168:171], v[32:35], a[0:3]\n"
    "ds_read_b128 v[160:163], v201 offset:26624\n"
    "v_cvt_pk_bf16_f32 v81, v82, v83\n"
    "v_mfma_f32_16x16x32_bf16 a[0:3], v[172:175], v[64:67], a[0:3]\n"
    "v_exp_f32 v94, v94\n"
    "v_mfma_f32_16x16x32_bf16 a[4:7], v[168:171], v[36:39], a[4:7]\n"
    "v_exp_f32 v95, v95\n"
    "v_mfma_f32_16x16x32_bf16 a[4:7], v[172:175], v[68:71], a[4:7]\n"
    "v_exp_f32 v184, v184\n"
    "v_mfma_f32_16x16x32_bf16 a[8:11], v[168:171], v[40:43], a[8:11]\n"
    "ds_read_b128 v[164:167], v201 offset:27648\n"
    "v_cvt_pk_bf16_f32 v84, v84, v85\n"
    "v_mfma_f32_16x16x32_bf16 a[8:11], v[172:175], v[72:75], a[8:11]\n"
    "v_exp_f32 v185, v185\n"
    "v_mfma_f32_16x16x32_bf16 a[12:15], v[168:171], v[44:47], a[12:15]\n"
    "v_exp_f32 v186, v186\n"
    "v_mfma_f32_16x16x32_bf16 a[12:15], v[172:175], v[76:79], a[12:15]\n"
    "v_exp_f32 v187, v187\n"
    "v_mfma_f32_16x16x32_bf16 v[120:123], v[192:195], v[152:155], v[120:123]\n"
    "v_exp_f32 v188, v188\n"
    "v_mfma_f32_16x16x32_bf16 v[120:123], v[192:195], v[40:43], v[120:123]\n"
    "v_exp_f32 v189, v189\n"
    "v_mfma_f32_16x16x32_bf16 v[120:123], v[192:195], v[72:75], v[120:123]\n"
    "v_exp_f32 v190, v190\n"
    "v_mfma_f32_16x16x32_bf16 v[124:127], v[192:195], v[156:159], v[124:127]\n"
    "v_exp_f32 v191, v191\n"
    "v_mfma_f32_16x16x32_bf16 v[124:127], v[192:195], v[44:47], v[124:127]\n"
    "s_waitcnt vmcnt(0)\n"
    "v_exp_f32 v248, v248\n"
    "v_mfma_f32_16x16x32_bf16 v[124:127], v[192:195], v[76:79], v[124:127]\n"
    "s_waitcnt lgkmcnt(2)\n"
    "v_exp_f32 v249, v249\n"
    "v_mfma_f32_16x16x32_bf16 a[16:19], v[176:179], v[128:131], a[16:19]\n"
    "s_barrier\n"
    "v_exp_f32 v250, v250\n"
    "v_mfma_f32_16x16x32_bf16 a[16:19], v[180:183], v[144:147], a[16:19]\n"
    "ds_read_b128 v[168:171], v207\n"
    "v_cvt_pk_bf16_f32 v85, v86, v87\n"
    "v_mfma_f32_16x16x32_bf16 a[20:23], v[176:179], v[132:135], a[20:23]\n"
    "v_exp_f32 v251, v251\n"
    "v_mfma_f32_16x16x32_bf16 a[20:23], v[180:183], v[148:151], a[20:23]\n"
    "v_exp_f32 v252, v252\n"
    "v_mfma_f32_16x16x32_bf16 a[24:27], v[176:179], v[136:139], a[24:27]\n"
    "ds_read_b128 v[172:175], v207 offset:1024\n"
    "v_cvt_pk_bf16_f32 v88, v88, v89\n"
    "v_mfma_f32_16x16x32_bf16 a[24:27], v[180:183], v[152:155], a[24:27]\n"
    "v_exp_f32 v253, v253\n"
    "v_mfma_f32_16x16x32_bf16 a[28:31], v[176:179], v[140:143], a[28:31]\n"
    "v_exp_f32 v254, v254\n"
    "v_mfma_f32_16x16x32_bf16 a[28:31], v[180:183], v[156:159], a[28:31]\n"
    "s_waitcnt lgkmcnt(2)\n"
    "v_cvt_pk_bf16_f32 v89, v90, v91\n"
    "v_cvt_pk_bf16_f32 v92, v92, v93\n"
    "v_mfma_f32_16x16x32_bf16 a[16:19], v[160:163], v[32:35], a[16:19]\n"
    "ds_read_b128 v[176:179], v207 offset:2048\n"
    "v_cvt_pk_bf16_f32 v93, v94, v95\n"
    "v_mfma_f32_16x16x32_bf16 a[16:19], v[164:167], v[64:67], a[16:19]\n"
    "ds_read_b128 v[180:183], v207 offset:3072\n"
    "v_cvt_pk_bf16_f32 v82, v184, v185\n"
    "v_mfma_f32_16x16x32_bf16 a[20:23], v[160:163], v[36:39], a[20:23]\n"
    "v_cvt_pk_bf16_f32 v83, v186, v187\n"
    "v_cvt_pk_bf16_f32 v86, v188, v189\n"
    "v_mfma_f32_16x16x32_bf16 a[20:23], v[164:167], v[68:71], a[20:23]\n"
    "v_exp_f32 v255, v255\n"
    "v_mfma_f32_16x16x32_bf16 a[24:27], v[160:163], v[40:43], a[24:27]\n"
    "v_cvt_pk_bf16_f32 v87, v190, v191\n"
    "v_cvt_pk_bf16_f32 v90, v248, v249\n"
    "v_mfma_f32_16x16x32_bf16 a[24:27], v[164:167], v[72:75], a[24:27]\n"
    "v_cvt_pk_bf16_f32 v91, v250, v251\n"
    "v_cvt_pk_bf16_f32 v94, v252, v253\n"
    "v_mfma_f32_16x16x32_bf16 a[28:31], v[160:163], v[44:47], a[28:31]\n"
    "v_cvt_pk_bf16_f32 v95, v254, v255\n"
    "v_mfma_f32_16x16x32_bf16 a[28:31], v[164:167], v[76:79], a[28:31]\n"
    "v_mfma_f32_16x16x32_bf16 v[112:115], v[192:195], v[232:235], v[112:115]\n"
    "ds_read_b128 v[160:163], v201 offset:4096\n"
    "v_mfma_f32_16x16x32_bf16 v[112:115], v[192:195], v[80:83], v[112:115]\n"
    "s_waitcnt lgkmcnt(3)\n"
    "v_mfma_f32_16x16x32_bf16 a[0:3], v[168:171], v[232:235], a[0:3]\n"
    "v_mfma_f32_16x16x32_bf16 a[0:3], v[172:175], v[80:83], a[0:3]\n"
    "s_waitcnt lgkmcnt(1)\n"
    "v_mfma_f32_16x16x32_bf16 a[16:19], v[176:179], v[232:235], a[16:19]\n"
    "ds_read_b128 v[164:167], v201 offset:5120\n"
    "v_mfma_f32_16x16x32_bf16 a[16:19], v[180:183], v[80:83], a[16:19]\n"
    "v_mfma_f32_16x16x32_bf16 v[116:119], v[192:195], v[236:239], v[116:119]\n"
    "v_mul_f32 v222, s68, v112\n"
    "v_rcp_f32 v222, v222\n"
    "v_mfma_f32_16x16x32_bf16 v[116:119], v[192:195], v[84:87], v[116:119]\n"
    "v_accvgpr_read_b32 v96, a0\n"
    "v_accvgpr_read_b32 v97, a1\n"
    "v_accvgpr_read_b32 v98, a2\n"
    "v_mfma_f32_16x16x32_bf16 a[4:7], v[168:171], v[236:239], a[4:7]\n"
    "v_accvgpr_read_b32 v99, a3\n"
    "v_fma_f32 v96, v96, v222, s94\n"
    "v_fma_f32 v97, v97, v222, s94\n"
    "v_mfma_f32_16x16x32_bf16 a[4:7], v[172:175], v[84:87], a[4:7]\n"
    "v_fma_f32 v98, v98, v222, s94\n"
    "v_fma_f32 v99, v99, v222, s94\n"
    "v_cvt_pk_bf16_f32 v48, v96, v97\n"
    "v_mfma_f32_16x16x32_bf16 a[20:23], v[176:179], v[236:239], a[20:23]\n"
    "v_cvt_pk_bf16_f32 v49, v98, v99\n"
    "v_accvgpr_read_b32 v100, a16\n"
    "v_accvgpr_read_b32 v101, a17\n"
    "v_mfma_f32_16x16x32_bf16 a[20:23], v[180:183], v[84:87], a[20:23]\n"
    "v_accvgpr_read_b32 v102, a18\n"
    "v_accvgpr_read_b32 v103, a19\n"
    "v_fma_f32 v100, v100, v222, s94\n"
    "v_mfma_f32_16x16x32_bf16 v[120:123], v[192:195], v[240:243], v[120:123]\n"
    "v_fma_f32 v101, v101, v222, s94\n"
    "v_fma_f32 v102, v102, v222, s94\n"
    "v_fma_f32 v103, v103, v222, s94\n"
    "v_mfma_f32_16x16x32_bf16 v[120:123], v[192:195], v[88:91], v[120:123]\n"
    "v_cvt_pk_bf16_f32 v50, v100, v101\n"
    "v_cvt_pk_bf16_f32 v51, v102, v103\n"
    "v_mfma_f32_16x16x32_bf16 a[8:11], v[168:171], v[240:243], a[8:11]\n"
    "buffer_store_dwordx4 v[48:51], v205, s[28:31], 0 offen sc0 sc1\n"
    "v_mul_f32 v224, s68, v116\n"
    "v_rcp_f32 v224, v224\n"
    "v_accvgpr_read_b32 v104, a4\n"
    "v_mfma_f32_16x16x32_bf16 a[8:11], v[172:175], v[88:91], a[8:11]\n"
    "v_accvgpr_read_b32 v105, a5\n"
    "v_accvgpr_read_b32 v106, a6\n"
    "v_accvgpr_read_b32 v107, a7\n"
    "v_mfma_f32_16x16x32_bf16 a[24:27], v[176:179], v[240:243], a[24:27]\n"
    "v_fma_f32 v104, v104, v224, s94\n"
    "v_fma_f32 v105, v105, v224, s94\n"
    "v_fma_f32 v106, v106, v224, s94\n"
    "v_mfma_f32_16x16x32_bf16 a[24:27], v[180:183], v[88:91], a[24:27]\n"
    "v_fma_f32 v107, v107, v224, s94\n"
    "v_cvt_pk_bf16_f32 v52, v104, v105\n"
    "v_cvt_pk_bf16_f32 v53, v106, v107\n"
    "v_mfma_f32_16x16x32_bf16 v[124:127], v[192:195], v[244:247], v[124:127]\n"
    "v_accvgpr_read_b32 v108, a20\n"
    "v_accvgpr_read_b32 v109, a21\n"
    "v_accvgpr_read_b32 v110, a22\n"
    "v_mfma_f32_16x16x32_bf16 v[124:127], v[192:195], v[92:95], v[124:127]\n"
    "v_accvgpr_read_b32 v111, a23\n"
    "v_fma_f32 v108, v108, v224, s94\n"
    "v_fma_f32 v109, v109, v224, s94\n"
    "v_mfma_f32_16x16x32_bf16 a[12:15], v[168:171], v[244:247], a[12:15]\n"
    "v_fma_f32 v110, v110, v224, s94\n"
    "v_fma_f32 v111, v111, v224, s94\n"
    "v_mfma_f32_16x16x32_bf16 a[12:15], v[172:175], v[92:95], a[12:15]\n"
    "v_cvt_pk_bf16_f32 v54, v108, v109\n"
    "v_cvt_pk_bf16_f32 v55, v110, v111\n"
    "v_mfma_f32_16x16x32_bf16 a[28:31], v[176:179], v[244:247], a[28:31]\n"
    "ds_read_b128 v[168:171], v201 offset:28672\n"
    "v_mfma_f32_16x16x32_bf16 a[28:31], v[180:183], v[92:95], a[28:31]\n"
    "s_waitcnt lgkmcnt(1)\n"
    "ds_read_b128 v[172:175], v201 offset:29696\n"
    "v_mfma_f32_16x16x32_bf16 a[32:35], v[160:163], v[128:131], a[32:35]\n"
    "ds_read_b128 v[176:179], v207 offset:4096\n"
    "v_mfma_f32_16x16x32_bf16 a[32:35], v[164:167], v[144:147], a[32:35]\n"
    "buffer_store_dwordx4 v[52:55], v205, s[28:31], s62 offen sc0 sc1\n"
    "v_mul_f32 v226, s68, v120\n"
    "v_rcp_f32 v226, v226\n"
    "v_mfma_f32_16x16x32_bf16 a[36:39], v[160:163], v[132:135], a[36:39]\n"
    "v_accvgpr_read_b32 v96, a8\n"
    "v_accvgpr_read_b32 v97, a9\n"
    "v_mfma_f32_16x16x32_bf16 a[36:39], v[164:167], v[148:151], a[36:39]\n"
    "v_accvgpr_read_b32 v98, a10\n"
    "v_accvgpr_read_b32 v99, a11\n"
    "v_mfma_f32_16x16x32_bf16 a[40:43], v[160:163], v[136:139], a[40:43]\n"
    "ds_read_b128 v[180:183], v207 offset:5120\n"
    "v_fma_f32 v96, v96, v226, s94\n"
    "v_mfma_f32_16x16x32_bf16 a[40:43], v[164:167], v[152:155], a[40:43]\n"
    "v_fma_f32 v97, v97, v226, s94\n"
    "v_fma_f32 v98, v98, v226, s94\n"
    "v_mfma_f32_16x16x32_bf16 a[44:47], v[160:163], v[140:143], a[44:47]\n"
    "v_fma_f32 v99, v99, v226, s94\n"
    "v_cvt_pk_bf16_f32 v56, v96, v97\n"
    "v_mfma_f32_16x16x32_bf16 a[44:47], v[164:167], v[156:159], a[44:47]\n"
    "s_waitcnt lgkmcnt(2)\n"
    "v_cvt_pk_bf16_f32 v57, v98, v99\n"
    "v_accvgpr_read_b32 v100, a24\n"
    "v_mfma_f32_16x16x32_bf16 a[44:47], v[168:171], v[44:47], a[44:47]\n"
    "ds_read_b128 v[160:163], v201 offset:6144\n"
    "v_accvgpr_read_b32 v101, a25\n"
    "v_mfma_f32_16x16x32_bf16 a[44:47], v[172:175], v[76:79], a[44:47]\n"
    "v_accvgpr_read_b32 v102, a26\n"
    "v_accvgpr_read_b32 v103, a27\n"
    "v_mfma_f32_16x16x32_bf16 a[40:43], v[168:171], v[40:43], a[40:43]\n"
    "v_fma_f32 v100, v100, v226, s94\n"
    "v_fma_f32 v101, v101, v226, s94\n"
    "v_mfma_f32_16x16x32_bf16 a[40:43], v[172:175], v[72:75], a[40:43]\n"
    "v_fma_f32 v102, v102, v226, s94\n"
    "v_fma_f32 v103, v103, v226, s94\n"
    "v_mfma_f32_16x16x32_bf16 a[36:39], v[168:171], v[36:39], a[36:39]\n"
    "ds_read_b128 v[164:167], v201 offset:7168\n"
    "v_cvt_pk_bf16_f32 v58, v100, v101\n"
    "v_mfma_f32_16x16x32_bf16 a[36:39], v[172:175], v[68:71], a[36:39]\n"
    "v_cvt_pk_bf16_f32 v59, v102, v103\n"
    "v_mfma_f32_16x16x32_bf16 a[32:35], v[168:171], v[32:35], a[32:35]\n"
    "buffer_store_dwordx4 v[56:59], v205, s[28:31], s63 offen sc0 sc1\n"
    "v_mul_f32 v228, s68, v124\n"
    "v_rcp_f32 v228, v228\n"
    "v_mfma_f32_16x16x32_bf16 a[32:35], v[172:175], v[64:67], a[32:35]\n"
    "s_waitcnt lgkmcnt(2)\n"
    "v_accvgpr_read_b32 v104, a12\n"
    "v_accvgpr_read_b32 v105, a13\n"
    "v_mfma_f32_16x16x32_bf16 a[32:35], v[176:179], v[232:235], a[32:35]\n"
    "ds_read_b128 v[168:171], v201 offset:30720\n"
    "v_accvgpr_read_b32 v106, a14\n"
    "v_mfma_f32_16x16x32_bf16 a[32:35], v[180:183], v[80:83], a[32:35]\n"
    "v_accvgpr_read_b32 v107, a15\n"
    "v_fma_f32 v104, v104, v228, s94\n"
    "v_mfma_f32_16x16x32_bf16 a[36:39], v[176:179], v[236:239], a[36:39]\n"
    "v_fma_f32 v105, v105, v228, s94\n"
    "v_fma_f32 v106, v106, v228, s94\n"
    "v_mfma_f32_16x16x32_bf16 a[36:39], v[180:183], v[84:87], a[36:39]\n"
    "v_fma_f32 v107, v107, v228, s94\n"
    "v_cvt_pk_bf16_f32 v60, v104, v105\n"
    "v_mfma_f32_16x16x32_bf16 a[40:43], v[176:179], v[240:243], a[40:43]\n"
    "ds_read_b128 v[172:175], v201 offset:31744\n"
    "v_cvt_pk_bf16_f32 v61, v106, v107\n"
    "v_mfma_f32_16x16x32_bf16 a[40:43], v[180:183], v[88:91], a[40:43]\n"
    "v_accvgpr_read_b32 v108, a28\n"
    "v_accvgpr_read_b32 v109, a29\n"
    "v_mfma_f32_16x16x32_bf16 a[44:47], v[176:179], v[244:247], a[44:47]\n"
    "v_accvgpr_read_b32 v110, a30\n"
    "v_accvgpr_read_b32 v111, a31\n"
    "v_mfma_f32_16x16x32_bf16 a[44:47], v[180:183], v[92:95], a[44:47]\n"
    "s_waitcnt lgkmcnt(2)\n"
    "v_fma_f32 v108, v108, v228, s94\n"
    "v_fma_f32 v109, v109, v228, s94\n"
    "v_mfma_f32_16x16x32_bf16 a[48:51], v[160:163], v[128:131], a[48:51]\n"
    "ds_read_b128 v[176:179], v207 offset:6144\n"
    "v_fma_f32 v110, v110, v228, s94\n"
    "v_mfma_f32_16x16x32_bf16 a[48:51], v[164:167], v[144:147], a[48:51]\n"
    "v_fma_f32 v111, v111, v228, s94\n"
    "v_cvt_pk_bf16_f32 v62, v108, v109\n"
    "v_mfma_f32_16x16x32_bf16 a[52:55], v[160:163], v[132:135], a[52:55]\n"
    "v_cvt_pk_bf16_f32 v63, v110, v111\n"
    "v_mfma_f32_16x16x32_bf16 a[52:55], v[164:167], v[148:151], a[52:55]\n"
    "buffer_store_dwordx4 v[60:63], v205, s[28:31], s64 offen sc0 sc1\n"
    "v_accvgpr_read_b32 v96, a32\n"
    "v_accvgpr_read_b32 v97, a33\n"
    "v_accvgpr_read_b32 v98, a34\n"
    "v_mfma_f32_16x16x32_bf16 a[56:59], v[160:163], v[136:139], a[56:59]\n"
    "ds_read_b128 v[180:183], v207 offset:7168\n"
    "v_accvgpr_read_b32 v99, a35\n"
    "v_mfma_f32_16x16x32_bf16 a[56:59], v[164:167], v[152:155], a[56:59]\n"
    "v_fma_f32 v96, v96, v222, s94\n"
    "v_fma_f32 v97, v97, v222, s94\n"
    "v_mfma_f32_16x16x32_bf16 a[60:63], v[160:163], v[140:143], a[60:63]\n"
    "v_fma_f32 v98, v98, v222, s94\n"
    "v_fma_f32 v99, v99, v222, s94\n"
    "v_mfma_f32_16x16x32_bf16 a[60:63], v[164:167], v[156:159], a[60:63]\n"
    "s_waitcnt lgkmcnt(2)\n"
    "v_cvt_pk_bf16_f32 v184, v96, v97\n"
    "v_cvt_pk_bf16_f32 v185, v98, v99\n"
    "v_mfma_f32_16x16x32_bf16 a[60:63], v[168:171], v[44:47], a[60:63]\n"
    "ds_read_b128 v[160:163], v201 offset:8192\n"
    "v_accvgpr_read_b32 v100, a36\n"
    "v_mfma_f32_16x16x32_bf16 a[60:63], v[172:175], v[76:79], a[60:63]\n"
    "v_accvgpr_read_b32 v101, a37\n"
    "v_accvgpr_read_b32 v102, a38\n"
    "v_mfma_f32_16x16x32_bf16 a[56:59], v[168:171], v[40:43], a[56:59]\n"
    "v_accvgpr_read_b32 v103, a39\n"
    "v_fma_f32 v100, v100, v224, s94\n"
    "v_mfma_f32_16x16x32_bf16 a[56:59], v[172:175], v[72:75], a[56:59]\n"
    "v_fma_f32 v101, v101, v224, s94\n"
    "v_fma_f32 v102, v102, v224, s94\n"
    "v_mfma_f32_16x16x32_bf16 a[52:55], v[168:171], v[36:39], a[52:55]\n"
    "ds_read_b128 v[164:167], v201 offset:9216\n"
    "v_fma_f32 v103, v103, v224, s94\n"
    "v_mfma_f32_16x16x32_bf16 a[52:55], v[172:175], v[68:71], a[52:55]\n"
    "v_cvt_pk_bf16_f32 v188, v100, v101\n"
    "v_cvt_pk_bf16_f32 v189, v102, v103\n"
    "v_mfma_f32_16x16x32_bf16 a[48:51], v[168:171], v[32:35], a[48:51]\n"
    "v_accvgpr_read_b32 v104, a40\n"
    "v_accvgpr_read_b32 v105, a41\n"
    "v_mfma_f32_16x16x32_bf16 a[48:51], v[172:175], v[64:67], a[48:51]\n"
    "s_waitcnt lgkmcnt(2)\n"
    "v_accvgpr_read_b32 v106, a42\n"
    "v_accvgpr_read_b32 v107, a43\n"
    "v_mfma_f32_16x16x32_bf16 a[48:51], v[176:179], v[232:235], a[48:51]\n"
    "ds_read_b128 v[168:171], v201 offset:32768\n"
    "v_fma_f32 v104, v104, v226, s94\n"
    "v_mfma_f32_16x16x32_bf16 a[48:51], v[180:183], v[80:83], a[48:51]\n"
    "v_fma_f32 v105, v105, v226, s94\n"
    "v_fma_f32 v106, v106, v226, s94\n"
    "v_mfma_f32_16x16x32_bf16 a[52:55], v[176:179], v[236:239], a[52:55]\n"
    "v_fma_f32 v107, v107, v226, s94\n"
    "v_cvt_pk_bf16_f32 v248, v104, v105\n"
    "v_mfma_f32_16x16x32_bf16 a[52:55], v[180:183], v[84:87], a[52:55]\n"
    "v_cvt_pk_bf16_f32 v249, v106, v107\n"
    "v_accvgpr_read_b32 v108, a44\n"
    "v_mfma_f32_16x16x32_bf16 a[56:59], v[176:179], v[240:243], a[56:59]\n"
    "ds_read_b128 v[172:175], v201 offset:33792\n"
    "v_accvgpr_read_b32 v109, a45\n"
    "v_mfma_f32_16x16x32_bf16 a[56:59], v[180:183], v[88:91], a[56:59]\n"
    "v_accvgpr_read_b32 v110, a46\n"
    "v_accvgpr_read_b32 v111, a47\n"
    "v_mfma_f32_16x16x32_bf16 a[60:63], v[176:179], v[244:247], a[60:63]\n"
    "v_fma_f32 v108, v108, v228, s94\n"
    "v_fma_f32 v109, v109, v228, s94\n"
    "v_mfma_f32_16x16x32_bf16 a[60:63], v[180:183], v[92:95], a[60:63]\n"
    "s_waitcnt lgkmcnt(2)\n"
    "v_fma_f32 v110, v110, v228, s94\n"
    "v_fma_f32 v111, v111, v228, s94\n"
    "v_mfma_f32_16x16x32_bf16 a[64:67], v[160:163], v[128:131], a[64:67]\n"
    "ds_read_b128 v[176:179], v207 offset:8192\n"
    "v_cvt_pk_bf16_f32 v252, v108, v109\n"
    "v_mfma_f32_16x16x32_bf16 a[64:67], v[164:167], v[144:147], a[64:67]\n"
    "v_cvt_pk_bf16_f32 v253, v110, v111\n"
    "v_accvgpr_read_b32 v96, a48\n"
    "v_mfma_f32_16x16x32_bf16 a[68:71], v[160:163], v[132:135], a[68:71]\n"
    "v_accvgpr_read_b32 v97, a49\n"
    "v_accvgpr_read_b32 v98, a50\n"
    "v_mfma_f32_16x16x32_bf16 a[68:71], v[164:167], v[148:151], a[68:71]\n"
    "v_accvgpr_read_b32 v99, a51\n"
    "v_fma_f32 v96, v96, v222, s94\n"
    "v_mfma_f32_16x16x32_bf16 a[72:75], v[160:163], v[136:139], a[72:75]\n"
    "ds_read_b128 v[180:183], v207 offset:9216\n"
    "v_fma_f32 v97, v97, v222, s94\n"
    "v_mfma_f32_16x16x32_bf16 a[72:75], v[164:167], v[152:155], a[72:75]\n"
    "v_fma_f32 v98, v98, v222, s94\n"
    "v_fma_f32 v99, v99, v222, s94\n"
    "v_mfma_f32_16x16x32_bf16 a[76:79], v[160:163], v[140:143], a[76:79]\n"
    "v_cvt_pk_bf16_f32 v186, v96, v97\n"
    "v_cvt_pk_bf16_f32 v187, v98, v99\n"
    "v_mfma_f32_16x16x32_bf16 a[76:79], v[164:167], v[156:159], a[76:79]\n"
    "s_waitcnt lgkmcnt(2)\n"
    "buffer_store_dwordx4 v[184:187], v205, s[28:31], 0 offen offset:64 sc0 sc1\n"
    "v_accvgpr_read_b32 v100, a52\n"
    "v_accvgpr_read_b32 v101, a53\n"
    "v_accvgpr_read_b32 v102, a54\n"
    "v_mfma_f32_16x16x32_bf16 a[76:79], v[168:171], v[44:47], a[76:79]\n"
    "ds_read_b128 v[160:163], v201 offset:10240\n"
    "v_accvgpr_read_b32 v103, a55\n"
    "v_mfma_f32_16x16x32_bf16 a[76:79], v[172:175], v[76:79], a[76:79]\n"
    "v_fma_f32 v100, v100, v224, s94\n"
    "v_fma_f32 v101, v101, v224, s94\n"
    "v_mfma_f32_16x16x32_bf16 a[72:75], v[168:171], v[40:43], a[72:75]\n"
    "v_fma_f32 v102, v102, v224, s94\n"
    "v_fma_f32 v103, v103, v224, s94\n"
    "v_mfma_f32_16x16x32_bf16 a[72:75], v[172:175], v[72:75], a[72:75]\n"
    "v_cvt_pk_bf16_f32 v190, v100, v101\n"
    "v_cvt_pk_bf16_f32 v191, v102, v103\n"
    "v_mfma_f32_16x16x32_bf16 a[68:71], v[168:171], v[36:39], a[68:71]\n"
    "ds_read_b128 v[164:167], v201 offset:11264\n"
    "v_mfma_f32_16x16x32_bf16 a[68:71], v[172:175], v[68:71], a[68:71]\n"
    "buffer_store_dwordx4 v[188:191], v205, s[28:31], s62 offen offset:64 sc0 sc1\n"
    "v_accvgpr_read_b32 v104, a56\n"
    "v_accvgpr_read_b32 v105, a57\n"
    "v_accvgpr_read_b32 v106, a58\n"
    "v_mfma_f32_16x16x32_bf16 a[64:67], v[168:171], v[32:35], a[64:67]\n"
    "v_accvgpr_read_b32 v107, a59\n"
    "v_fma_f32 v104, v104, v226, s94\n"
    "v_mfma_f32_16x16x32_bf16 a[64:67], v[172:175], v[64:67], a[64:67]\n"
    "s_waitcnt lgkmcnt(2)\n"
    "v_fma_f32 v105, v105, v226, s94\n"
    "v_fma_f32 v106, v106, v226, s94\n"
    "v_mfma_f32_16x16x32_bf16 a[64:67], v[176:179], v[232:235], a[64:67]\n"
    "ds_read_b128 v[168:171], v201 offset:34816\n"
    "v_fma_f32 v107, v107, v226, s94\n"
    "v_mfma_f32_16x16x32_bf16 a[64:67], v[180:183], v[80:83], a[64:67]\n"
    "v_cvt_pk_bf16_f32 v250, v104, v105\n"
    "v_cvt_pk_bf16_f32 v251, v106, v107\n"
    "v_mfma_f32_16x16x32_bf16 a[68:71], v[176:179], v[236:239], a[68:71]\n"
    "buffer_store_dwordx4 v[248:251], v205, s[28:31], s63 offen offset:64 sc0 sc1\n"
    "v_accvgpr_read_b32 v108, a60\n"
    "v_accvgpr_read_b32 v109, a61\n"
    "v_accvgpr_read_b32 v110, a62\n"
    "v_mfma_f32_16x16x32_bf16 a[68:71], v[180:183], v[84:87], a[68:71]\n"
    "v_accvgpr_read_b32 v111, a63\n"
    "v_fma_f32 v108, v108, v228, s94\n"
    "v_mfma_f32_16x16x32_bf16 a[72:75], v[176:179], v[240:243], a[72:75]\n"
    "ds_read_b128 v[172:175], v201 offset:35840\n"
    "v_fma_f32 v109, v109, v228, s94\n"
    "v_mfma_f32_16x16x32_bf16 a[72:75], v[180:183], v[88:91], a[72:75]\n"
    "v_fma_f32 v110, v110, v228, s94\n"
    "v_fma_f32 v111, v111, v228, s94\n"
    "v_mfma_f32_16x16x32_bf16 a[76:79], v[176:179], v[244:247], a[76:79]\n"
    "v_cvt_pk_bf16_f32 v254, v108, v109\n"
    "v_cvt_pk_bf16_f32 v255, v110, v111\n"
    "v_mfma_f32_16x16x32_bf16 a[76:79], v[180:183], v[92:95], a[76:79]\n"
    "s_waitcnt lgkmcnt(2)\n"
    "buffer_store_dwordx4 v[252:255], v205, s[28:31], s64 offen offset:64 sc0 sc1\n"
    "v_accvgpr_read_b32 v96, a64\n"
    "v_accvgpr_read_b32 v97, a65\n"
    "v_accvgpr_read_b32 v98, a66\n"
    "v_mfma_f32_16x16x32_bf16 a[80:83], v[160:163], v[128:131], a[80:83]\n"
    "ds_read_b128 v[176:179], v207 offset:10240\n"
    "v_accvgpr_read_b32 v99, a67\n"
    "v_mfma_f32_16x16x32_bf16 a[80:83], v[164:167], v[144:147], a[80:83]\n"
    "v_fma_f32 v96, v96, v222, s94\n"
    "v_fma_f32 v97, v97, v222, s94\n"
    "v_mfma_f32_16x16x32_bf16 a[84:87], v[160:163], v[132:135], a[84:87]\n"
    "v_fma_f32 v98, v98, v222, s94\n"
    "v_fma_f32 v99, v99, v222, s94\n"
    "v_mfma_f32_16x16x32_bf16 a[84:87], v[164:167], v[148:151], a[84:87]\n"
    "v_cvt_pk_bf16_f32 v48, v96, v97\n"
    "v_cvt_pk_bf16_f32 v49, v98, v99\n"
    "v_mfma_f32_16x16x32_bf16 a[88:91], v[160:163], v[136:139], a[88:91]\n"
    "ds_read_b128 v[180:183], v207 offset:11264\n"
    "v_accvgpr_read_b32 v100, a68\n"
    "v_mfma_f32_16x16x32_bf16 a[88:91], v[164:167], v[152:155], a[88:91]\n"
    "v_accvgpr_read_b32 v101, a69\n"
    "v_accvgpr_read_b32 v102, a70\n"
    "v_mfma_f32_16x16x32_bf16 a[92:95], v[160:163], v[140:143], a[92:95]\n"
    "v_accvgpr_read_b32 v103, a71\n"
    "v_fma_f32 v100, v100, v224, s94\n"
    "v_mfma_f32_16x16x32_bf16 a[92:95], v[164:167], v[156:159], a[92:95]\n"
    "s_waitcnt lgkmcnt(2)\n"
    "v_fma_f32 v101, v101, v224, s94\n"
    "v_fma_f32 v102, v102, v224, s94\n"
    "v_mfma_f32_16x16x32_bf16 a[92:95], v[168:171], v[44:47], a[92:95]\n"
    "ds_read_b128 v[160:163], v201 offset:12288\n"
    "v_fma_f32 v103, v103, v224, s94\n"
    "v_mfma_f32_16x16x32_bf16 a[92:95], v[172:175], v[76:79], a[92:95]\n"
    "v_cvt_pk_bf16_f32 v52, v100, v101\n"
    "v_cvt_pk_bf16_f32 v53, v102, v103\n"
    "v_mfma_f32_16x16x32_bf16 a[88:91], v[168:171], v[40:43], a[88:91]\n"
    "v_accvgpr_read_b32 v104, a72\n"
    "v_accvgpr_read_b32 v105, a73\n"
    "v_mfma_f32_16x16x32_bf16 a[88:91], v[172:175], v[72:75], a[88:91]\n"
    "v_accvgpr_read_b32 v106, a74\n"
    "v_accvgpr_read_b32 v107, a75\n"
    "v_mfma_f32_16x16x32_bf16 a[84:87], v[168:171], v[36:39], a[84:87]\n"
    "ds_read_b128 v[164:167], v201 offset:13312\n"
    "v_fma_f32 v104, v104, v226, s94\n"
    "v_mfma_f32_16x16x32_bf16 a[84:87], v[172:175], v[68:71], a[84:87]\n"
    "v_fma_f32 v105, v105, v226, s94\n"
    "v_fma_f32 v106, v106, v226, s94\n"
    "v_mfma_f32_16x16x32_bf16 a[80:83], v[168:171], v[32:35], a[80:83]\n"
    "v_fma_f32 v107, v107, v226, s94\n"
    "v_cvt_pk_bf16_f32 v56, v104, v105\n"
    "v_mfma_f32_16x16x32_bf16 a[80:83], v[172:175], v[64:67], a[80:83]\n"
    "s_waitcnt lgkmcnt(2)\n"
    "v_cvt_pk_bf16_f32 v57, v106, v107\n"
    "v_accvgpr_read_b32 v108, a76\n"
    "v_mfma_f32_16x16x32_bf16 a[80:83], v[176:179], v[232:235], a[80:83]\n"
    "ds_read_b128 v[168:171], v201 offset:36864\n"
    "v_accvgpr_read_b32 v109, a77\n"
    "v_mfma_f32_16x16x32_bf16 a[80:83], v[180:183], v[80:83], a[80:83]\n"
    "v_accvgpr_read_b32 v110, a78\n"
    "v_accvgpr_read_b32 v111, a79\n"
    "v_mfma_f32_16x16x32_bf16 a[84:87], v[176:179], v[236:239], a[84:87]\n"
    "v_fma_f32 v108, v108, v228, s94\n"
    "v_fma_f32 v109, v109, v228, s94\n"
    "v_mfma_f32_16x16x32_bf16 a[84:87], v[180:183], v[84:87], a[84:87]\n"
    "v_fma_f32 v110, v110, v228, s94\n"
    "v_fma_f32 v111, v111, v228, s94\n"
    "v_mfma_f32_16x16x32_bf16 a[88:91], v[176:179], v[240:243], a[88:91]\n"
    "ds_read_b128 v[172:175], v201 offset:37888\n"
    "v_cvt_pk_bf16_f32 v60, v108, v109\n"
    "v_mfma_f32_16x16x32_bf16 a[88:91], v[180:183], v[88:91], a[88:91]\n"
    "v_cvt_pk_bf16_f32 v61, v110, v111\n"
    "v_mfma_f32_16x16x32_bf16 a[92:95], v[176:179], v[244:247], a[92:95]\n"
    "v_mfma_f32_16x16x32_bf16 a[92:95], v[180:183], v[92:95], a[92:95]\n"
    "s_waitcnt lgkmcnt(2)\n"
    "v_accvgpr_read_b32 v96, a80\n"
    "v_accvgpr_read_b32 v97, a81\n"
    "v_mfma_f32_16x16x32_bf16 a[96:99], v[160:163], v[128:131], a[96:99]\n"
    "ds_read_b128 v[176:179], v207 offset:12288\n"
    "v_accvgpr_read_b32 v98, a82\n"
    "v_mfma_f32_16x16x32_bf16 a[96:99], v[164:167], v[144:147], a[96:99]\n"
    "v_accvgpr_read_b32 v99, a83\n"
    "v_fma_f32 v96, v96, v222, s94\n"
    "v_mfma_f32_16x16x32_bf16 a[100:103], v[160:163], v[132:135], a[100:103]\n"
    "v_fma_f32 v97, v97, v222, s94\n"
    "v_fma_f32 v98, v98, v222, s94\n"
    "v_mfma_f32_16x16x32_bf16 a[100:103], v[164:167], v[148:151], a[100:103]\n"
    "v_fma_f32 v99, v99, v222, s94\n"
    "v_cvt_pk_bf16_f32 v50, v96, v97\n"
    "v_mfma_f32_16x16x32_bf16 a[104:107], v[160:163], v[136:139], a[104:107]\n"
    "ds_read_b128 v[180:183], v207 offset:13312\n"
    "v_cvt_pk_bf16_f32 v51, v98, v99\n"
    "v_mfma_f32_16x16x32_bf16 a[104:107], v[164:167], v[152:155], a[104:107]\n"
    "buffer_store_dwordx4 v[48:51], v205, s[28:31], 0 offen offset:128 sc0 sc1\n"
    "v_accvgpr_read_b32 v100, a84\n"
    "v_accvgpr_read_b32 v101, a85\n"
    "v_accvgpr_read_b32 v102, a86\n"
    "v_mfma_f32_16x16x32_bf16 a[108:111], v[160:163], v[140:143], a[108:111]\n"
    "v_accvgpr_read_b32 v103, a87\n"
    "v_fma_f32 v100, v100, v224, s94\n"
    "v_mfma_f32_16x16x32_bf16 a[108:111], v[164:167], v[156:159], a[108:111]\n"
    "s_waitcnt lgkmcnt(2)\n"
    "s_cmp_eq_u32 s57, 0\n"
    "s_cselect_b32 s77, s74, 0\n"
    "v_mov_b32 v196, 0x40000000\n"
    "v_mov_b32 v197, 0x800\n"
    "v_cmp_gt_u32 vcc, s77, v231\n"
    "v_mov_b32 v198, 0\n"
    "v_mov_b32 v198, 0\n"
    "v_cndmask_b32 v197, v196, v197, vcc\n"
    "buffer_store_dword v221, v197, s[44:47], 0 offen sc1\n"
    "v_mfma_f32_16x16x32_bf16 a[108:111], v[168:171], v[44:47], a[108:111]\n"
    "ds_read_b128 v[160:163], v201 offset:14336\n"
    "v_fma_f32 v101, v101, v224, s94\n"
    "v_mfma_f32_16x16x32_bf16 a[108:111], v[172:175], v[76:79], a[108:111]\n"
    "v_fma_f32 v102, v102, v224, s94\n"
    "v_fma_f32 v103, v103, v224, s94\n"
    "v_mfma_f32_16x16x32_bf16 a[104:107], v[168:171], v[40:43], a[104:107]\n"
    "v_cvt_pk_bf16_f32 v54, v100, v101\n"
    "v_cvt_pk_bf16_f32 v55, v102, v103\n"
    "v_mfma_f32_16x16x32_bf16 a[104:107], v[172:175], v[72:75], a[104:107]\n"
    "buffer_store_dwordx4 v[52:55], v205, s[28:31], s62 offen offset:128 sc0 sc1\n"
    "v_accvgpr_read_b32 v104, a88\n"
    "v_accvgpr_read_b32 v105, a89\n"
    "v_accvgpr_read_b32 v106, a90\n"
    "v_mfma_f32_16x16x32_bf16 a[100:103], v[168:171], v[36:39], a[100:103]\n"
    "ds_read_b128 v[164:167], v201 offset:15360\n"
    "v_accvgpr_read_b32 v107, a91\n"
    "v_mfma_f32_16x16x32_bf16 a[100:103], v[172:175], v[68:71], a[100:103]\n"
    "v_fma_f32 v104, v104, v226, s94\n"
    "v_fma_f32 v105, v105, v226, s94\n"
    "v_mfma_f32_16x16x32_bf16 a[96:99], v[168:171], v[32:35], a[96:99]\n"
    "v_fma_f32 v106, v106, v226, s94\n"
    "v_fma_f32 v107, v107, v226, s94\n"
    "v_mfma_f32_16x16x32_bf16 a[96:99], v[172:175], v[64:67], a[96:99]\n"
    "s_waitcnt lgkmcnt(2)\n"
    "v_cvt_pk_bf16_f32 v58, v104, v105\n"
    "v_cvt_pk_bf16_f32 v59, v106, v107\n"
    "v_mfma_f32_16x16x32_bf16 a[96:99], v[176:179], v[232:235], a[96:99]\n"
    "ds_read_b128 v[168:171], v201 offset:38912\n"
    "v_mfma_f32_16x16x32_bf16 a[96:99], v[180:183], v[80:83], a[96:99]\n"
    "buffer_store_dwordx4 v[56:59], v205, s[28:31], s63 offen offset:128 sc0 sc1\n"
    "v_accvgpr_read_b32 v108, a92\n"
    "v_accvgpr_read_b32 v109, a93\n"
    "v_accvgpr_read_b32 v110, a94\n"
    "v_mfma_f32_16x16x32_bf16 a[100:103], v[176:179], v[236:239], a[100:103]\n"
    "v_accvgpr_read_b32 v111, a95\n"
    "v_fma_f32 v108, v108, v228, s94\n"
    "v_mfma_f32_16x16x32_bf16 a[100:103], v[180:183], v[84:87], a[100:103]\n"
    "v_fma_f32 v109, v109, v228, s94\n"
    "v_fma_f32 v110, v110, v228, s94\n"
    "v_mfma_f32_16x16x32_bf16 a[104:107], v[176:179], v[240:243], a[104:107]\n"
    "ds_read_b128 v[172:175], v201 offset:39936\n"
    "v_fma_f32 v111, v111, v228, s94\n"
    "v_mfma_f32_16x16x32_bf16 a[104:107], v[180:183], v[88:91], a[104:107]\n"
    "v_cvt_pk_bf16_f32 v62, v108, v109\n"
    "v_cvt_pk_bf16_f32 v63, v110, v111\n"
    "v_mfma_f32_16x16x32_bf16 a[108:111], v[176:179], v[244:247], a[108:111]\n"
    "buffer_store_dwordx4 v[60:63], v205, s[28:31], s64 offen offset:128 sc0 sc1\n"
    "v_mfma_f32_16x16x32_bf16 a[108:111], v[180:183], v[92:95], a[108:111]\n"
    "s_waitcnt lgkmcnt(2)\n"
    "v_accvgpr_read_b32 v96, a96\n"
    "v_accvgpr_read_b32 v97, a97\n"
    "v_mfma_f32_16x16x32_bf16 a[112:115], v[160:163], v[128:131], a[112:115]\n"
    "ds_read_b128 v[176:179], v207 offset:14336\n"
    "v_accvgpr_read_b32 v98, a98\n"
    "v_mfma_f32_16x16x32_bf16 a[112:115], v[164:167], v[144:147], a[112:115]\n"
    "v_accvgpr_read_b32 v99, a99\n"
    "v_fma_f32 v96, v96, v222, s94\n"
    "v_mfma_f32_16x16x32_bf16 a[116:119], v[160:163], v[132:135], a[116:119]\n"
    "v_fma_f32 v97, v97, v222, s94\n"
    "v_fma_f32 v98, v98, v222, s94\n"
    "v_mfma_f32_16x16x32_bf16 a[116:119], v[164:167], v[148:151], a[116:119]\n"
    "v_fma_f32 v99, v99, v222, s94\n"
    "v_cvt_pk_bf16_f32 v184, v96, v97\n"
    "v_mfma_f32_16x16x32_bf16 a[120:123], v[160:163], v[136:139], a[120:123]\n"
    "ds_read_b128 v[180:183], v207 offset:15360\n"
    "v_cvt_pk_bf16_f32 v185, v98, v99\n"
    "v_mfma_f32_16x16x32_bf16 a[120:123], v[164:167], v[152:155], a[120:123]\n"
    "v_accvgpr_read_b32 v100, a100\n"
    "v_accvgpr_read_b32 v101, a101\n"
    "v_mfma_f32_16x16x32_bf16 a[124:127], v[160:163], v[140:143], a[124:127]\n"
    "v_accvgpr_read_b32 v102, a102\n"
    "v_accvgpr_read_b32 v103, a103\n"
    "v_mfma_f32_16x16x32_bf16 a[124:127], v[164:167], v[156:159], a[124:127]\n"
    "s_waitcnt lgkmcnt(2)\n"
    "v_fma_f32 v100, v100, v224, s94\n"
    "v_fma_f32 v101, v101, v224, s94\n"
    "v_mfma_f32_16x16x32_bf16 a[124:127], v[168:171], v[44:47], a[124:127]\n"
    "ds_read_b128 v[160:163], v201 offset:16384\n"
    "v_fma_f32 v102, v102, v224, s94\n"
    "v_mfma_f32_16x16x32_bf16 a[124:127], v[172:175], v[76:79], a[124:127]\n"
    "v_fma_f32 v103, v103, v224, s94\n"
    "v_cvt_pk_bf16_f32 v188, v100, v101\n"
    "v_mfma_f32_16x16x32_bf16 a[120:123], v[168:171], v[40:43], a[120:123]\n"
    "v_cvt_pk_bf16_f32 v189, v102, v103\n"
    "v_accvgpr_read_b32 v104, a104\n"
    "v_mfma_f32_16x16x32_bf16 a[120:123], v[172:175], v[72:75], a[120:123]\n"
    "v_accvgpr_read_b32 v105, a105\n"
    "v_accvgpr_read_b32 v106, a106\n"
    "v_mfma_f32_16x16x32_bf16 a[116:119], v[168:171], v[36:39], a[116:119]\n"
    "ds_read_b128 v[164:167], v201 offset:17408\n"
    "v_accvgpr_read_b32 v107, a107\n"
    "v_mfma_f32_16x16x32_bf16 a[116:119], v[172:175], v[68:71], a[116:119]\n"
    "v_fma_f32 v104, v104, v226, s94\n"
    "v_fma_f32 v105, v105, v226, s94\n"
    "v_mfma_f32_16x16x32_bf16 a[112:115], v[168:171], v[32:35], a[112:115]\n"
    "v_fma_f32 v106, v106, v226, s94\n"
    "v_fma_f32 v107, v107, v226, s94\n"
    "v_mfma_f32_16x16x32_bf16 a[112:115], v[172:175], v[64:67], a[112:115]\n"
    "s_waitcnt lgkmcnt(2)\n"
    "v_cvt_pk_bf16_f32 v248, v104, v105\n"
    "v_cvt_pk_bf16_f32 v249, v106, v107\n"
    "v_mfma_f32_16x16x32_bf16 a[112:115], v[176:179], v[232:235], a[112:115]\n"
    "ds_read_b128 v[168:171], v201 offset:40960\n"
    "v_accvgpr_read_b32 v108, a108\n"
    "v_mfma_f32_16x16x32_bf16 a[112:115], v[180:183], v[80:83], a[112:115]\n"
    "v_accvgpr_read_b32 v109, a109\n"
    "v_accvgpr_read_b32 v110, a110\n"
    "v_mfma_f32_16x16x32_bf16 a[116:119], v[176:179], v[236:239], a[116:119]\n"
    "v_accvgpr_read_b32 v111, a111\n"
    "v_fma_f32 v108, v108, v228, s94\n"
    "v_mfma_f32_16x16x32_bf16 a[116:119], v[180:183], v[84:87], a[116:119]\n"
    "v_fma_f32 v109, v109, v228, s94\n"
    "v_fma_f32 v110, v110, v228, s94\n"
    "v_mfma_f32_16x16x32_bf16 a[120:123], v[176:179], v[240:243], a[120:123]\n"
    "ds_read_b128 v[172:175], v201 offset:41984\n"
    "v_fma_f32 v111, v111, v228, s94\n"
    "v_mfma_f32_16x16x32_bf16 a[120:123], v[180:183], v[88:91], a[120:123]\n"
    "v_cvt_pk_bf16_f32 v252, v108, v109\n"
    "v_cvt_pk_bf16_f32 v253, v110, v111\n"
    "v_mfma_f32_16x16x32_bf16 a[124:127], v[176:179], v[244:247], a[124:127]\n"
    "v_mfma_f32_16x16x32_bf16 a[124:127], v[180:183], v[92:95], a[124:127]\n"
    "s_waitcnt lgkmcnt(2)\n"
    "v_accvgpr_read_b32 v96, a112\n"
    "v_accvgpr_read_b32 v97, a113\n"
    "v_mfma_f32_16x16x32_bf16 a[128:131], v[160:163], v[128:131], a[128:131]\n"
    "ds_read_b128 v[176:179], v207 offset:16384\n"
    "v_accvgpr_read_b32 v98, a114\n"
    "v_mfma_f32_16x16x32_bf16 a[128:131], v[164:167], v[144:147], a[128:131]\n"
    "v_accvgpr_read_b32 v99, a115\n"
    "v_fma_f32 v96, v96, v222, s94\n"
    "v_mfma_f32_16x16x32_bf16 a[132:135], v[160:163], v[132:135], a[132:135]\n"
    "v_fma_f32 v97, v97, v222, s94\n"
    "v_fma_f32 v98, v98, v222, s94\n"
    "v_mfma_f32_16x16x32_bf16 a[132:135], v[164:167], v[148:151], a[132:135]\n"
    "v_fma_f32 v99, v99, v222, s94\n"
    "v_cvt_pk_bf16_f32 v186, v96, v97\n"
    "v_mfma_f32_16x16x32_bf16 a[136:139], v[160:163], v[136:139], a[136:139]\n"
    "ds_read_b128 v[180:183], v207 offset:17408\n"
    "v_cvt_pk_bf16_f32 v187, v98, v99\n"
    "v_mfma_f32_16x16x32_bf16 a[136:139], v[164:167], v[152:155], a[136:139]\n"
    "buffer_store_dwordx4 v[184:187], v205, s[28:31], 0 offen offset:192 sc0 sc1\n"
    "v_accvgpr_read_b32 v100, a116\n"
    "v_accvgpr_read_b32 v101, a117\n"
    "v_accvgpr_read_b32 v102, a118\n"
    "v_mfma_f32_16x16x32_bf16 a[140:143], v[160:163], v[140:143], a[140:143]\n"
    "v_accvgpr_read_b32 v103, a119\n"
    "v_fma_f32 v100, v100, v224, s94\n"
    "v_mfma_f32_16x16x32_bf16 a[140:143], v[164:167], v[156:159], a[140:143]\n"
    "s_waitcnt lgkmcnt(2)\n"
    "v_fma_f32 v101, v101, v224, s94\n"
    "v_fma_f32 v102, v102, v224, s94\n"
    "v_mfma_f32_16x16x32_bf16 a[140:143], v[168:171], v[44:47], a[140:143]\n"
    "ds_read_b128 v[160:163], v201 offset:18432\n"
    "v_fma_f32 v103, v103, v224, s94\n"
    "v_mfma_f32_16x16x32_bf16 a[140:143], v[172:175], v[76:79], a[140:143]\n"
    "v_cvt_pk_bf16_f32 v190, v100, v101\n"
    "v_cvt_pk_bf16_f32 v191, v102, v103\n"
    "v_mfma_f32_16x16x32_bf16 a[136:139], v[168:171], v[40:43], a[136:139]\n"
    "buffer_store_dwordx4 v[188:191], v205, s[28:31], s62 offen offset:192 sc0 sc1\n"
    "v_accvgpr_read_b32 v104, a120\n"
    "v_accvgpr_read_b32 v105, a121\n"
    "v_accvgpr_read_b32 v106, a122\n"
    "v_mfma_f32_16x16x32_bf16 a[136:139], v[172:175], v[72:75], a[136:139]\n"
    "v_accvgpr_read_b32 v107, a123\n"
    "v_fma_f32 v104, v104, v226, s94\n"
    "v_mfma_f32_16x16x32_bf16 a[132:135], v[168:171], v[36:39], a[132:135]\n"
    "ds_read_b128 v[164:167], v201 offset:19456\n"
    "v_fma_f32 v105, v105, v226, s94\n"
    "v_mfma_f32_16x16x32_bf16 a[132:135], v[172:175], v[68:71], a[132:135]\n"
    "v_fma_f32 v106, v106, v226, s94\n"
    "v_fma_f32 v107, v107, v226, s94\n"
    "v_mfma_f32_16x16x32_bf16 a[128:131], v[168:171], v[32:35], a[128:131]\n"
    "v_cvt_pk_bf16_f32 v250, v104, v105\n"
    "v_cvt_pk_bf16_f32 v251, v106, v107\n"
    "v_mfma_f32_16x16x32_bf16 a[128:131], v[172:175], v[64:67], a[128:131]\n"
    "s_waitcnt lgkmcnt(2)\n"
    "buffer_store_dwordx4 v[248:251], v205, s[28:31], s63 offen offset:192 sc0 sc1\n"
    "v_accvgpr_read_b32 v108, a124\n"
    "v_accvgpr_read_b32 v109, a125\n"
    "v_accvgpr_read_b32 v110, a126\n"
    "v_mfma_f32_16x16x32_bf16 a[128:131], v[176:179], v[232:235], a[128:131]\n"
    "ds_read_b128 v[168:171], v201 offset:43008\n"
    "v_accvgpr_read_b32 v111, a127\n"
    "v_mfma_f32_16x16x32_bf16 a[128:131], v[180:183], v[80:83], a[128:131]\n"
    "v_fma_f32 v108, v108, v228, s94\n"
    "v_fma_f32 v109, v109, v228, s94\n"
    "v_mfma_f32_16x16x32_bf16 a[132:135], v[176:179], v[236:239], a[132:135]\n"
    "v_fma_f32 v110, v110, v228, s94\n"
    "v_fma_f32 v111, v111, v228, s94\n"
    "v_mfma_f32_16x16x32_bf16 a[132:135], v[180:183], v[84:87], a[132:135]\n"
    "v_cvt_pk_bf16_f32 v254, v108, v109\n"
    "v_cvt_pk_bf16_f32 v255, v110, v111\n"
    "v_mfma_f32_16x16x32_bf16 a[136:139], v[176:179], v[240:243], a[136:139]\n"
    "ds_read_b128 v[172:175], v201 offset:44032\n"
    "v_mfma_f32_16x16x32_bf16 a[136:139], v[180:183], v[88:91], a[136:139]\n"
    "buffer_store_dwordx4 v[252:255], v205, s[28:31], s64 offen offset:192 sc0 sc1\n"
    "v_mfma_f32_16x16x32_bf16 a[140:143], v[176:179], v[244:247], a[140:143]\n"
    "v_mfma_f32_16x16x32_bf16 a[140:143], v[180:183], v[92:95], a[140:143]\n"
    "s_waitcnt lgkmcnt(2)\n"
    "v_accvgpr_read_b32 v96, a128\n"
    "v_accvgpr_read_b32 v97, a129\n"
    "v_mfma_f32_16x16x32_bf16 a[144:147], v[160:163], v[128:131], a[144:147]\n"
    "ds_read_b128 v[176:179], v207 offset:18432\n"
    "v_accvgpr_read_b32 v98, a130\n"
    "v_mfma_f32_16x16x32_bf16 a[144:147], v[164:167], v[144:147], a[144:147]\n"
    "v_accvgpr_read_b32 v99, a131\n"
    "v_fma_f32 v96, v96, v222, s94\n"
    "v_mfma_f32_16x16x32_bf16 a[148:151], v[160:163], v[132:135], a[148:151]\n"
    "v_fma_f32 v97, v97, v222, s94\n"
    "v_fma_f32 v98, v98, v222, s94\n"
    "v_mfma_f32_16x16x32_bf16 a[148:151], v[164:167], v[148:151], a[148:151]\n"
    "v_fma_f32 v99, v99, v222, s94\n"
    "v_cvt_pk_bf16_f32 v48, v96, v97\n"
    "v_mfma_f32_16x16x32_bf16 a[152:155], v[160:163], v[136:139], a[152:155]\n"
    "ds_read_b128 v[180:183], v207 offset:19456\n"
    "v_cvt_pk_bf16_f32 v49, v98, v99\n"
    "v_mfma_f32_16x16x32_bf16 a[152:155], v[164:167], v[152:155], a[152:155]\n"
    "v_accvgpr_read_b32 v100, a132\n"
    "v_accvgpr_read_b32 v101, a133\n"
    "v_mfma_f32_16x16x32_bf16 a[156:159], v[160:163], v[140:143], a[156:159]\n"
    "v_accvgpr_read_b32 v102, a134\n"
    "v_accvgpr_read_b32 v103, a135\n"
    "v_mfma_f32_16x16x32_bf16 a[156:159], v[164:167], v[156:159], a[156:159]\n"
    "s_waitcnt lgkmcnt(2)\n"
    "v_fma_f32 v100, v100, v224, s94\n"
    "v_fma_f32 v101, v101, v224, s94\n"
    "v_mfma_f32_16x16x32_bf16 a[156:159], v[168:171], v[44:47], a[156:159]\n"
    "ds_read_b128 v[160:163], v201 offset:20480\n"
    "v_fma_f32 v102, v102, v224, s94\n"
    "v_mfma_f32_16x16x32_bf16 a[156:159], v[172:175], v[76:79], a[156:159]\n"
    "v_fma_f32 v103, v103, v224, s94\n"
    "v_cvt_pk_bf16_f32 v52, v100, v101\n"
    "v_mfma_f32_16x16x32_bf16 a[152:155], v[168:171], v[40:43], a[152:155]\n"
    "v_cvt_pk_bf16_f32 v53, v102, v103\n"
    "v_accvgpr_read_b32 v104, a136\n"
    "v_mfma_f32_16x16x32_bf16 a[152:155], v[172:175], v[72:75], a[152:155]\n"
    "v_accvgpr_read_b32 v105, a137\n"
    "v_accvgpr_read_b32 v106, a138\n"
    "v_mfma_f32_16x16x32_bf16 a[148:151], v[168:171], v[36:39], a[148:151]\n"
    "ds_read_b128 v[164:167], v201 offset:21504\n"
    "v_accvgpr_read_b32 v107, a139\n"
    "v_mfma_f32_16x16x32_bf16 a[148:151], v[172:175], v[68:71], a[148:151]\n"
    "v_fma_f32 v104, v104, v226, s94\n"
    "v_fma_f32 v105, v105, v226, s94\n"
    "v_mfma_f32_16x16x32_bf16 a[144:147], v[168:171], v[32:35], a[144:147]\n"
    "v_fma_f32 v106, v106, v226, s94\n"
    "v_fma_f32 v107, v107, v226, s94\n"
    "v_mfma_f32_16x16x32_bf16 a[144:147], v[172:175], v[64:67], a[144:147]\n"
    "s_waitcnt lgkmcnt(2)\n"
    "v_cvt_pk_bf16_f32 v56, v104, v105\n"
    "v_cvt_pk_bf16_f32 v57, v106, v107\n"
    "v_mfma_f32_16x16x32_bf16 a[144:147], v[176:179], v[232:235], a[144:147]\n"
    "ds_read_b128 v[168:171], v201 offset:45056\n"
    "v_accvgpr_read_b32 v108, a140\n"
    "v_mfma_f32_16x16x32_bf16 a[144:147], v[180:183], v[80:83], a[144:147]\n"
    "v_accvgpr_read_b32 v109, a141\n"
    "v_accvgpr_read_b32 v110, a142\n"
    "v_mfma_f32_16x16x32_bf16 a[148:151], v[176:179], v[236:239], a[148:151]\n"
    "v_accvgpr_read_b32 v111, a143\n"
    "v_fma_f32 v108, v108, v228, s94\n"
    "v_mfma_f32_16x16x32_bf16 a[148:151], v[180:183], v[84:87], a[148:151]\n"
    "v_fma_f32 v109, v109, v228, s94\n"
    "v_fma_f32 v110, v110, v228, s94\n"
    "v_mfma_f32_16x16x32_bf16 a[152:155], v[176:179], v[240:243], a[152:155]\n"
    "ds_read_b128 v[172:175], v201 offset:46080\n"
    "v_fma_f32 v111, v111, v228, s94\n"
    "v_mfma_f32_16x16x32_bf16 a[152:155], v[180:183], v[88:91], a[152:155]\n"
    "v_cvt_pk_bf16_f32 v60, v108, v109\n"
    "v_cvt_pk_bf16_f32 v61, v110, v111\n"
    "v_mfma_f32_16x16x32_bf16 a[156:159], v[176:179], v[244:247], a[156:159]\n"
    "v_mfma_f32_16x16x32_bf16 a[156:159], v[180:183], v[92:95], a[156:159]\n"
    "s_waitcnt lgkmcnt(2)\n"
    "v_accvgpr_read_b32 v96, a144\n"
    "v_accvgpr_read_b32 v97, a145\n"
    "v_mfma_f32_16x16x32_bf16 a[160:163], v[160:163], v[128:131], a[160:163]\n"
    "ds_read_b128 v[176:179], v207 offset:20480\n"
    "v_accvgpr_read_b32 v98, a146\n"
    "v_mfma_f32_16x16x32_bf16 a[160:163], v[164:167], v[144:147], a[160:163]\n"
    "v_accvgpr_read_b32 v99, a147\n"
    "v_fma_f32 v96, v96, v222, s94\n"
    "v_mfma_f32_16x16x32_bf16 a[164:167], v[160:163], v[132:135], a[164:167]\n"
    "v_fma_f32 v97, v97, v222, s94\n"
    "v_fma_f32 v98, v98, v222, s94\n"
    "v_mfma_f32_16x16x32_bf16 a[164:167], v[164:167], v[148:151], a[164:167]\n"
    "v_fma_f32 v99, v99, v222, s94\n"
    "v_cvt_pk_bf16_f32 v50, v96, v97\n"
    "v_mfma_f32_16x16x32_bf16 a[168:171], v[160:163], v[136:139], a[168:171]\n"
    "ds_read_b128 v[180:183], v207 offset:21504\n"
    "v_cvt_pk_bf16_f32 v51, v98, v99\n"
    "v_mfma_f32_16x16x32_bf16 a[168:171], v[164:167], v[152:155], a[168:171]\n"
    "buffer_store_dwordx4 v[48:51], v205, s[28:31], 0 offen offset:256 sc0 sc1\n"
    "v_accvgpr_read_b32 v100, a148\n"
    "v_accvgpr_read_b32 v101, a149\n"
    "v_accvgpr_read_b32 v102, a150\n"
    "v_mfma_f32_16x16x32_bf16 a[172:175], v[160:163], v[140:143], a[172:175]\n"
    "v_accvgpr_read_b32 v103, a151\n"
    "v_fma_f32 v100, v100, v224, s94\n"
    "v_mfma_f32_16x16x32_bf16 a[172:175], v[164:167], v[156:159], a[172:175]\n"
    "s_waitcnt lgkmcnt(2)\n"
    "v_fma_f32 v101, v101, v224, s94\n"
    "v_fma_f32 v102, v102, v224, s94\n"
    "v_mfma_f32_16x16x32_bf16 a[172:175], v[168:171], v[44:47], a[172:175]\n"
    "ds_read_b128 v[160:163], v201 offset:22528\n"
    "v_fma_f32 v103, v103, v224, s94\n"
    "v_mfma_f32_16x16x32_bf16 a[172:175], v[172:175], v[76:79], a[172:175]\n"
    "v_cvt_pk_bf16_f32 v54, v100, v101\n"
    "v_cvt_pk_bf16_f32 v55, v102, v103\n"
    "v_mfma_f32_16x16x32_bf16 a[168:171], v[168:171], v[40:43], a[168:171]\n"
    "buffer_store_dwordx4 v[52:55], v205, s[28:31], s62 offen offset:256 sc0 sc1\n"
    "v_accvgpr_read_b32 v104, a152\n"
    "v_accvgpr_read_b32 v105, a153\n"
    "v_accvgpr_read_b32 v106, a154\n"
    "v_mfma_f32_16x16x32_bf16 a[168:171], v[172:175], v[72:75], a[168:171]\n"
    "v_accvgpr_read_b32 v107, a155\n"
    "v_fma_f32 v104, v104, v226, s94\n"
    "v_mfma_f32_16x16x32_bf16 a[164:167], v[168:171], v[36:39], a[164:167]\n"
    "ds_read_b128 v[164:167], v201 offset:23552\n"
    "v_fma_f32 v105, v105, v226, s94\n"
    "v_mfma_f32_16x16x32_bf16 a[164:167], v[172:175], v[68:71], a[164:167]\n"
    "v_fma_f32 v106, v106, v226, s94\n"
    "v_fma_f32 v107, v107, v226, s94\n"
    "v_mfma_f32_16x16x32_bf16 a[160:163], v[168:171], v[32:35], a[160:163]\n"
    "v_cvt_pk_bf16_f32 v58, v104, v105\n"
    "v_cvt_pk_bf16_f32 v59, v106, v107\n"
    "v_mfma_f32_16x16x32_bf16 a[160:163], v[172:175], v[64:67], a[160:163]\n"
    "s_waitcnt lgkmcnt(2)\n"
    "buffer_store_dwordx4 v[56:59], v205, s[28:31], s63 offen offset:256 sc0 sc1\n"
    "v_accvgpr_read_b32 v108, a156\n"
    "v_accvgpr_read_b32 v109, a157\n"
    "v_accvgpr_read_b32 v110, a158\n"
    "v_mfma_f32_16x16x32_bf16 a[160:163], v[176:179], v[232:235], a[160:163]\n"
    "ds_read_b128 v[168:171], v201 offset:47104\n"
    "v_accvgpr_read_b32 v111, a159\n"
    "v_mfma_f32_16x16x32_bf16 a[160:163], v[180:183], v[80:83], a[160:163]\n"
    "v_fma_f32 v108, v108, v228, s94\n"
    "v_fma_f32 v109, v109, v228, s94\n"
    "v_mfma_f32_16x16x32_bf16 a[164:167], v[176:179], v[236:239], a[164:167]\n"
    "v_fma_f32 v110, v110, v228, s94\n"
    "v_fma_f32 v111, v111, v228, s94\n"
    "v_mfma_f32_16x16x32_bf16 a[164:167], v[180:183], v[84:87], a[164:167]\n"
    "v_cvt_pk_bf16_f32 v62, v108, v109\n"
    "v_cvt_pk_bf16_f32 v63, v110, v111\n"
    "v_mfma_f32_16x16x32_bf16 a[168:171], v[176:179], v[240:243], a[168:171]\n"
    "ds_read_b128 v[172:175], v201 offset:48128\n"
    "v_mfma_f32_16x16x32_bf16 a[168:171], v[180:183], v[88:91], a[168:171]\n"
    "buffer_store_dwordx4 v[60:63], v205, s[28:31], s64 offen offset:256 sc0 sc1\n"
    "v_mfma_f32_16x16x32_bf16 a[172:175], v[176:179], v[244:247], a[172:175]\n"
    "v_mfma_f32_16x16x32_bf16 a[172:175], v[180:183], v[92:95], a[172:175]\n"
    "s_waitcnt lgkmcnt(2)\n"
    "v_accvgpr_read_b32 v96, a160\n"
    "v_accvgpr_read_b32 v97, a161\n"
    "v_mfma_f32_16x16x32_bf16 a[176:179], v[160:163], v[128:131], a[176:179]\n"
    "ds_read_b128 v[176:179], v207 offset:22528\n"
    "v_accvgpr_read_b32 v98, a162\n"
    "v_mfma_f32_16x16x32_bf16 a[176:179], v[164:167], v[144:147], a[176:179]\n"
    "v_accvgpr_read_b32 v99, a163\n"
    "v_fma_f32 v96, v96, v222, s94\n"
    "v_mfma_f32_16x16x32_bf16 a[180:183], v[160:163], v[132:135], a[180:183]\n"
    "v_fma_f32 v97, v97, v222, s94\n"
    "v_fma_f32 v98, v98, v222, s94\n"
    "v_mfma_f32_16x16x32_bf16 a[180:183], v[164:167], v[148:151], a[180:183]\n"
    "v_fma_f32 v99, v99, v222, s94\n"
    "v_cvt_pk_bf16_f32 v184, v96, v97\n"
    "v_mfma_f32_16x16x32_bf16 a[184:187], v[160:163], v[136:139], a[184:187]\n"
    "ds_read_b128 v[180:183], v207 offset:23552\n"
    "v_cvt_pk_bf16_f32 v185, v98, v99\n"
    "v_mfma_f32_16x16x32_bf16 a[184:187], v[164:167], v[152:155], a[184:187]\n"
    "v_accvgpr_read_b32 v100, a164\n"
    "v_accvgpr_read_b32 v101, a165\n"
    "v_mfma_f32_16x16x32_bf16 a[188:191], v[160:163], v[140:143], a[188:191]\n"
    "v_accvgpr_read_b32 v102, a166\n"
    "v_accvgpr_read_b32 v103, a167\n"
    "v_mfma_f32_16x16x32_bf16 a[188:191], v[164:167], v[156:159], a[188:191]\n"
    "s_waitcnt lgkmcnt(2)\n"
    "v_fma_f32 v100, v100, v224, s94\n"
    "v_fma_f32 v101, v101, v224, s94\n"
    "v_mfma_f32_16x16x32_bf16 a[188:191], v[168:171], v[44:47], a[188:191]\n"
    "v_fma_f32 v102, v102, v224, s94\n"
    "v_fma_f32 v103, v103, v224, s94\n"
    "v_mfma_f32_16x16x32_bf16 a[188:191], v[172:175], v[76:79], a[188:191]\n"
    "v_cvt_pk_bf16_f32 v188, v100, v101\n"
    "v_cvt_pk_bf16_f32 v189, v102, v103\n"
    "v_mfma_f32_16x16x32_bf16 a[184:187], v[168:171], v[40:43], a[184:187]\n"
    "v_accvgpr_read_b32 v104, a168\n"
    "v_accvgpr_read_b32 v105, a169\n"
    "v_mfma_f32_16x16x32_bf16 a[184:187], v[172:175], v[72:75], a[184:187]\n"
    "v_accvgpr_read_b32 v106, a170\n"
    "v_accvgpr_read_b32 v107, a171\n"
    "v_mfma_f32_16x16x32_bf16 a[180:183], v[168:171], v[36:39], a[180:183]\n"
    "v_fma_f32 v104, v104, v226, s94\n"
    "v_fma_f32 v105, v105, v226, s94\n"
    "v_mfma_f32_16x16x32_bf16 a[180:183], v[172:175], v[68:71], a[180:183]\n"
    "v_fma_f32 v106, v106, v226, s94\n"
    "v_fma_f32 v107, v107, v226, s94\n"
    "v_mfma_f32_16x16x32_bf16 a[176:179], v[168:171], v[32:35], a[176:179]\n"
    "v_cvt_pk_bf16_f32 v248, v104, v105\n"
    "v_cvt_pk_bf16_f32 v249, v106, v107\n"
    "v_mfma_f32_16x16x32_bf16 a[176:179], v[172:175], v[64:67], a[176:179]\n"
    "s_waitcnt lgkmcnt(0)\n"
    "v_accvgpr_read_b32 v108, a172\n"
    "v_accvgpr_read_b32 v109, a173\n"
    "v_mfma_f32_16x16x32_bf16 a[176:179], v[176:179], v[232:235], a[176:179]\n"
    "v_accvgpr_read_b32 v110, a174\n"
    "v_accvgpr_read_b32 v111, a175\n"
    "v_mfma_f32_16x16x32_bf16 a[176:179], v[180:183], v[80:83], a[176:179]\n"
    "v_fma_f32 v108, v108, v228, s94\n"
    "v_fma_f32 v109, v109, v228, s94\n"
    "v_mfma_f32_16x16x32_bf16 a[180:183], v[176:179], v[236:239], a[180:183]\n"
    "v_fma_f32 v110, v110, v228, s94\n"
    "v_fma_f32 v111, v111, v228, s94\n"
    "v_mfma_f32_16x16x32_bf16 a[180:183], v[180:183], v[84:87], a[180:183]\n"
    "v_cvt_pk_bf16_f32 v252, v108, v109\n"
    "v_cvt_pk_bf16_f32 v253, v110, v111\n"
    "v_mfma_f32_16x16x32_bf16 a[184:187], v[176:179], v[240:243], a[184:187]\n"
    "v_mfma_f32_16x16x32_bf16 a[184:187], v[180:183], v[88:91], a[184:187]\n"
    "v_mfma_f32_16x16x32_bf16 a[188:191], v[176:179], v[244:247], a[188:191]\n"
    "v_mfma_f32_16x16x32_bf16 a[188:191], v[180:183], v[92:95], a[188:191]\n"
    "v_accvgpr_read_b32 v96, a176\n"
    "v_accvgpr_read_b32 v97, a177\n"
    "v_accvgpr_read_b32 v98, a178\n"
    "v_accvgpr_read_b32 v99, a179\n"
    "v_fma_f32 v96, v96, v222, s94\n"
    "v_fma_f32 v97, v97, v222, s94\n"
    "v_fma_f32 v98, v98, v222, s94\n"
    "v_fma_f32 v99, v99, v222, s94\n"
    "v_cvt_pk_bf16_f32 v186, v96, v97\n"
    "v_cvt_pk_bf16_f32 v187, v98, v99\n"
    "buffer_store_dwordx4 v[184:187], v205, s[28:31], 0 offen offset:320 sc0 sc1\n"
    "v_accvgpr_read_b32 v100, a180\n"
    "v_accvgpr_read_b32 v101, a181\n"
    "v_accvgpr_read_b32 v102, a182\n"
    "v_accvgpr_read_b32 v103, a183\n"
    "v_fma_f32 v100, v100, v224, s94\n"
    "v_fma_f32 v101, v101, v224, s94\n"
    "v_fma_f32 v102, v102, v224, s94\n"
    "v_fma_f32 v103, v103, v224, s94\n"
    "v_cvt_pk_bf16_f32 v190, v100, v101\n"
    "v_cvt_pk_bf16_f32 v191, v102, v103\n"
    "buffer_store_dwordx4 v[188:191], v205, s[28:31], s62 offen offset:320 sc0 sc1\n"
    "v_accvgpr_read_b32 v104, a184\n"
    "v_accvgpr_read_b32 v105, a185\n"
    "v_accvgpr_read_b32 v106, a186\n"
    "v_accvgpr_read_b32 v107, a187\n"
    "v_fma_f32 v104, v104, v226, s94\n"
    "v_fma_f32 v105, v105, v226, s94\n"
    "v_fma_f32 v106, v106, v226, s94\n"
    "v_fma_f32 v107, v107, v226, s94\n"
    "v_cvt_pk_bf16_f32 v250, v104, v105\n"
    "v_cvt_pk_bf16_f32 v251, v106, v107\n"
    "buffer_store_dwordx4 v[248:251], v205, s[28:31], s63 offen offset:320 sc0 sc1\n"
    "v_accvgpr_read_b32 v108, a188\n"
    "v_accvgpr_read_b32 v109, a189\n"
    "v_accvgpr_read_b32 v110, a190\n"
    "v_accvgpr_read_b32 v111, a191\n"
    "v_fma_f32 v108, v108, v228, s94\n"
    "v_fma_f32 v109, v109, v228, s94\n"
    "v_fma_f32 v110, v110, v228, s94\n"
    "v_fma_f32 v111, v111, v228, s94\n"
    "v_cvt_pk_bf16_f32 v254, v108, v109\n"
    "v_cvt_pk_bf16_f32 v255, v110, v111\n"
    "buffer_store_dwordx4 v[252:255], v205, s[28:31], s64 offen offset:320 sc0 sc1\n"
    "v_add_f32 v196, v112, v116\n"
    "v_add_f32 v197, v120, v124\n"
    "v_add_f32 v196, v196, v197\n"
    "v_cmp_ngt_f32 vcc, 0x45800000, v196\n"
    "s_barrier\n"
    "s_cbranch_vccnz LSLOW\n"
    "LFIN:\n"
    "s_branch LEND\n"
    "LSPRE0:\n"
    "LSPIN0:\n"
    "s_sleep 1\n"
    "buffer_load_dwordx2 v[218:219], v213, s[44:47], 0 offen sc1\n"
    "s_waitcnt vmcnt(0)\n"
    "s_add_u32 s73, s73, 1\n"
    "s_cmp_gt_u32 s73, 200000\n"
    "s_cbranch_scc1 LSPB0\n"
    "v_cmp_ne_u32 s[80:81], s67, v219\n"
    "v_cmp_gt_u32 vcc, s66, v218\n"
    "s_or_b64 vcc, vcc, s[80:81]\n"
    "s_cbranch_vccnz LSPIN0\n"
    "s_branch LSPB0\n"
    "LSPRE1:\n"
    "LSPIN1:\n"
    "s_sleep 1\n"
    "buffer_load_dwordx2 v[218:219], v213, s[44:47], 0 offen sc1\n"
    "s_waitcnt vmcnt(0)\n"
    "s_add_u32 s73, s73, 1\n"
    "s_cmp_gt_u32 s73, 200000\n"
    "s_cbranch_scc1 LSPB1\n"
    "v_cmp_ne_u32 s[80:81], s67, v219\n"
    "v_cmp_gt_u32 vcc, s66, v218\n"
    "s_or_b64 vcc, vcc, s[80:81]\n"
    "s_cbranch_vccnz LSPIN1\n"
    "s_branch LSPB1\n"
    "LSPRE2:\n"
    "LSPIN2:\n"
    "s_sleep 1\n"
    "buffer_load_dwordx2 v[218:219], v213, s[44:47], 0 offen sc1\n"
    "s_waitcnt vmcnt(0)\n"
    "s_add_u32 s73, s73, 1\n"
    "s_cmp_gt_u32 s73, 200000\n"
    "s_cbranch_scc1 LSPB2\n"
    "v_cmp_ne_u32 s[80:81], s67, v219\n"
    "v_cmp_gt_u32 vcc, s66, v218\n"
    "s_or_b64 vcc, vcc, s[80:81]\n"
    "s_cbranch_vccnz LSPIN2\n"
    "s_branch LSPB2\n"
    "LSPRE3:\n"
    "LSPIN3:\n"
    "s_sleep 1\n"
    "buffer_load_dwordx2 v[218:219], v213, s[44:47], 0 offen sc1\n"
    "s_waitcnt vmcnt(0)\n"
    "s_add_u32 s73, s73, 1\n"
    "s_cmp_gt_u32 s73, 200000\n"
    "s_cbranch_scc1 LSPB3\n"
    "v_cmp_ne_u32 s[80:81], s67, v219\n"
    "v_cmp_gt_u32 vcc, s66, v218\n"
    "s_or_b64 vcc, vcc, s[80:81]\n"
    "s_cbranch_vccnz LSPIN3\n"
    "s_branch LSPB3\n"
    "LSPRE4:\n"
    "LSPIN4:\n"
    "s_sleep 1\n"
    "buffer_load_dwordx2 v[218:219], v213, s[44:47], 0 offen sc1\n"
    "s_waitcnt vmcnt(0)\n"
    "s_add_u32 s73, s73, 1\n"
    "s_cmp_gt_u32 s73, 200000\n"
    "s_cbranch_scc1 LSPB4\n"
    "v_cmp_ne_u32 s[80:81], s67, v219\n"
    "v_cmp_gt_u32 vcc, s66, v218\n"
    "s_or_b64 vcc, vcc, s[80:81]\n"
    "s_cbranch_vccnz LSPIN4\n"
    "s_branch LSPB4\n"
    "LSPRE5:\n"
    "LSPIN5:\n"
    "s_sleep 1\n"
    "buffer_load_dwordx2 v[218:219], v213, s[44:47], 0 offen sc1\n"
    "s_waitcnt vmcnt(0)\n"
    "s_add_u32 s73, s73, 1\n"
    "s_cmp_gt_u32 s73, 200000\n"
    "s_cbranch_scc1 LSPB5\n"
    "v_cmp_ne_u32 s[80:81], s67, v219\n"
    "v_cmp_gt_u32 vcc, s66, v218\n"
    "s_or_b64 vcc, vcc, s[80:81]\n"
    "s_cbranch_vccnz LSPIN5\n"
    "s_branch LSPB5\n"
    "LSLOW:\n"
    "s_waitcnt vmcnt(0)\n"
    "s_nop 15\n"
    "s_mul_i32 s62, s58, 0x9000\n"
    "v_add_u32 v205, s62, v200\n"
    "s_mov_b32 s63, 0\n"
    "v_mov_b32 v128, 0\n"
    "v_mov_b32 v129, 0\n"
    "v_mov_b32 v130, 0\n"
    "v_mov_b32 v131, 0\n"
    "v_mov_b32 v96, 0xff61b1e6\n"
    "v_mov_b32 v100, 0xff61b1e6\n"
    "v_mov_b32 v104, 0xff61b1e6\n"
    "v_mov_b32 v108, 0xff61b1e6\n"
    "v_mfma_f32_16x16x32_bf16 a[0:3], v[128:131], v[128:131], 0\n"
    "v_mfma_f32_16x16x32_bf16 a[4:7], v[128:131], v[128:131], 0\n"
    "v_mfma_f32_16x16x32_bf16 a[8:11], v[128:131], v[128:131], 0\n"
    "v_mfma_f32_16x16x32_bf16 a[12:15], v[128:131], v[128:131], 0\n"
    "v_mfma_f32_16x16x32_bf16 a[16:19], v[128:131], v[128:131], 0\n"
    "v_mfma_f32_16x16x32_bf16 a[20:23], v[128:131], v[128:131], 0\n"
    "v_mfma_f32_16x16x32_bf16 a[24:27], v[128:131], v[128:131], 0\n"
    "v_mfma_f32_16x16x32_bf16 a[28:31], v[128:131], v[128:131], 0\n"
    "v_mfma_f32_16x16x32_bf16 a[32:35], v[128:131], v[128:131], 0\n"
    "v_mfma_f32_16x16x32_bf16 a[36:39], v[128:131], v[128:131], 0\n"
    "v_mfma_f32_16x16x32_bf16 a[40:43], v[128:131], v[128:131], 0\n"
    "v_mfma_f32_16x16x32_bf16 a[44:47], v[128:131], v[128:131], 0\n"
    "v_mfma_f32_16x16x32_bf16 a[48:51], v[128:131], v[128:131], 0\n"
    "v_mfma_f32_16x16x32_bf16 a[52:55], v[128:131], v[128:131], 0\n"
    "v_mfma_f32_16x16x32_bf16 a[56:59], v[128:131], v[128:131], 0\n"
    "v_mfma_f32_16x16x32_bf16 a[60:63], v[128:131], v[128:131], 0\n"
    "v_mfma_f32_16x16x32_bf16 a[64:67], v[128:131], v[128:131], 0\n"
    "v_mfma_f32_16x16x32_bf16 a[68:71], v[128:131], v[128:131], 0\n"
    "v_mfma_f32_16x16x32_bf16 a[72:75], v[128:131], v[128:131], 0\n"
    "v_mfma_f32_16x16x32_bf16 a[76:79], v[128:131], v[128:131], 0\n"
    "v_mfma_f32_16x16x32_bf16 a[80:83], v[128:131], v[128:131], 0\n"
    "v_mfma_f32_16x16x32_bf16 a[84:87], v[128:131], v[128:131], 0\n"
    "v_mfma_f32_16x16x32_bf16 a[88:91], v[128:131], v[128:131], 0\n"
    "v_mfma_f32_16x16x32_bf16 a[92:95], v[128:131], v[128:131], 0\n"
    "v_mfma_f32_16x16x32_bf16 a[96:99], v[128:131], v[128:131], 0\n"
    "v_mfma_f32_16x16x32_bf16 a[100:103], v[128:131], v[128:131], 0\n"
    "v_mfma_f32_16x16x32_bf16 a[104:107], v[128:131], v[128:131], 0\n"
    "v_mfma_f32_16x16x32_bf16 a[108:111], v[128:131], v[128:131], 0\n"
    "v_mfma_f32_16x16x32_bf16 a[112:115], v[128:131], v[128:131], 0\n"
    "v_mfma_f32_16x16x32_bf16 a[116:119], v[128:131], v[128:131], 0\n"
    "v_mfma_f32_16x16x32_bf16 a[120:123], v[128:131], v[128:131], 0\n"
    "v_mfma_f32_16x16x32_bf16 a[124:127], v[128:131], v[128:131], 0\n"
    "v_mfma_f32_16x16x32_bf16 a[128:131], v[128:131], v[128:131], 0\n"
    "v_mfma_f32_16x16x32_bf16 a[132:135], v[128:131], v[128:131], 0\n"
    "v_mfma_f32_16x16x32_bf16 a[136:139], v[128:131], v[128:131], 0\n"
    "v_mfma_f32_16x16x32_bf16 a[140:143], v[128:131], v[128:131], 0\n"
    "v_mfma_f32_16x16x32_bf16 a[144:147], v[128:131], v[128:131], 0\n"
    "v_mfma_f32_16x16x32_bf16 a[148:151], v[128:131], v[128:131], 0\n"
    "v_mfma_f32_16x16x32_bf16 a[152:155], v[128:131], v[128:131], 0\n"
    "v_mfma_f32_16x16x32_bf16 a[156:159], v[128:131], v[128:131], 0\n"
    "v_mfma_f32_16x16x32_bf16 a[160:163], v[128:131], v[128:131], 0\n"
    "v_mfma_f32_16x16x32_bf16 a[164:167], v[128:131], v[128:131], 0\n"
    "v_mfma_f32_16x16x32_bf16 a[168:171], v[128:131], v[128:131], 0\n"
    "v_mfma_f32_16x16x32_bf16 a[172:175], v[128:131], v[128:131], 0\n"
    "v_mfma_f32_16x16x32_bf16 a[176:179], v[128:131], v[128:131], 0\n"
    "v_mfma_f32_16x16x32_bf16 a[180:183], v[128:131], v[128:131], 0\n"
    "v_mfma_f32_16x16x32_bf16 a[184:187], v[128:131], v[128:131], 0\n"
    "v_mfma_f32_16x16x32_bf16 a[188:191], v[128:131], v[128:131], 0\n"
    "v_mfma_f32_16x16x32_bf16 v[112:115], v[128:131], v[128:131], 0\n"
    "v_mfma_f32_16x16x32_bf16 v[116:119], v[128:131], v[128:131], 0\n"
    "v_mfma_f32_16x16x32_bf16 v[120:123], v[128:131], v[128:131], 0\n"
    "v_mfma_f32_16x16x32_bf16 v[124:127], v[128:131], v[128:131], 0\n"
    "s_nop 15\n"
    "LSLOOP:\n"
    "s_add_u32 s78, s63, 0\n"
    "s_add_u32 m0, s62, 0\n"
    "s_nop 0\n"
    "buffer_load_dwordx4 v200, s[36:39], s78 offen sc1 lds\n"
    "buffer_load_dwordx4 v200, s[36:39], s78 offen offset:1024 sc1 lds\n"
    "buffer_load_dwordx4 v200, s[36:39], s78 offen offset:2048 sc1 lds\n"
    "buffer_load_dwordx4 v200, s[36:39], s78 offen offset:3072 sc1 lds\n"
    "s_add_u32 s78, s63, 0x1000\n"
    "s_add_u32 m0, s62, 0x1000\n"
    "s_nop 0\n"
    "buffer_load_dwordx4 v200, s[36:39], s78 offen sc1 lds\n"
    "buffer_load_dwordx4 v200, s[36:39], s78 offen offset:1024 sc1 lds\n"
    "buffer_load_dwordx4 v200, s[36:39], s78 offen offset:2048 sc1 lds\n"
    "buffer_load_dwordx4 v200, s[36:39], s78 offen offset:3072 sc1 lds\n"
    "s_add_u32 s78, s63, 0x2000\n"
    "s_add_u32 m0, s62, 0x2000\n"
    "s_nop 0\n"
    "buffer_load_dwordx4 v200, s[36:39], s78 offen sc1 lds\n"
    "buffer_load_dwordx4 v200, s[36:39], s78 offen offset:1024 sc1 lds\n"
    "buffer_load_dwordx4 v200, s[36:39], s78 offen offset:2048 sc1 lds\n"
    "buffer_load_dwordx4 v200, s[36:39], s78 offen offset:3072 sc1 lds\n"
    "s_add_u32 s78, s63, 0\n"
    "s_add_u32 m0, s62, 0x3000\n"
    "s_nop 0\n"
    "buffer_load_dwordx4 v200, s[40:43], s78 offen sc1 lds\n"
    "buffer_load_dwordx4 v200, s[40:43], s78 offen offset:1024 sc1 lds\n"
    "buffer_load_dwordx4 v200, s[40:43], s78 offen offset:2048 sc1 lds\n"
    "buffer_load_dwordx4 v200, s[40:43], s78 offen offset:3072 sc1 lds\n"
    "s_add_u32 s78, s63, 0x1000\n"
    "s_add_u32 m0, s62, 0x4000\n"
    "s_nop 0\n"
    "buffer_load_dwordx4 v200, s[40:43], s78 offen sc1 lds\n"
    "buffer_load_dwordx4 v200, s[40:43], s78 offen offset:1024 sc1 lds\n"
    "buffer_load_dwordx4 v200, s[40:43], s78 offen offset:2048 sc1 lds\n"
    "buffer_load_dwordx4 v200, s[40:43], s78 offen offset:3072 sc1 lds\n"
    "s_add_u32 s78, s63, 0x2000\n"
    "s_add_u32 m0, s62, 0x5000\n"
    "s_nop 0\n"
    "buffer_load_dwordx4 v200, s[40:43], s78 offen sc1 lds\n"
    "buffer_load_dwordx4 v200, s[40:43], s78 offen offset:1024 sc1 lds\n"
    "buffer_load_dwordx4 v200, s[40:43], s78 offen offset:2048 sc1 lds\n"
    "buffer_load_dwordx4 v200, s[40:43], s78 offen offset:3072 sc1 lds\n"
    "s_add_u32 s78, s63, 0x3000\n"
    "s_add_u32 m0, s62, 0x6000\n"
    "s_nop 0\n"
    "buffer_load_dwordx4 v200, s[40:43], s78 offen sc1 lds\n"
    "buffer_load_dwordx4 v200, s[40:43], s78 offen offset:1024 sc1 lds\n"
    "buffer_load_dwordx4 v200, s[40:43], s78 offen offset:2048 sc1 lds\n"
    "buffer_load_dwordx4 v200, s[40:43], s78 offen offset:3072 sc1 lds\n"
    "s_add_u32 s78, s63, 0x4000\n"
    "s_add_u32 m0, s62, 0x7000\n"
    "s_nop 0\n"
    "buffer_load_dwordx4 v200, s[40:43], s78 offen sc1 lds\n"
    "buffer_load_dwordx4 v200, s[40:43], s78 offen offset:1024 sc1 lds\n"
    "buffer_load_dwordx4 v200, s[40:43], s78 offen offset:2048 sc1 lds\n"
    "buffer_load_dwordx4 v200, s[40:43], s78 offen offset:3072 sc1 lds\n"
    "s_add_u32 s78, s63, 0x5000\n"
    "s_add_u32 m0, s62, 0x8000\n"
    "s_nop 0\n"
    "buffer_load_dwordx4 v200, s[40:43], s78 offen sc1 lds\n"
    "buffer_load_dwordx4 v200, s[40:43], s78 offen offset:1024 sc1 lds\n"
    "buffer_load_dwordx4 v200, s[40:43], s78 offen offset:2048 sc1 lds\n"
    "buffer_load_dwordx4 v200, s[40:43], s78 offen offset:3072 sc1 lds\n"
    "s_waitcnt vmcnt(0)\n"
    "ds_read_b128 v[160:163], v205\n"
    "ds_read_b128 v[164:167], v205 offset:1024\n"
    "s_waitcnt lgkmcnt(1)\n"
    "v_mfma_f32_16x16x32_bf16 v[32:35], v[160:163], a[192:195], 0\n"
    "s_waitcnt lgkmcnt(0)\n"
    "v_mfma_f32_16x16x32_bf16 v[32:35], v[164:167], a[196:199], v[32:35]\n"
    "v_mfma_f32_16x16x32_bf16 v[36:39], v[164:167], a[220:223], 0\n"
    "v_mfma_f32_16x16x32_bf16 v[36:39], v[160:163], a[216:219], v[36:39]\n"
    "v_mfma_f32_16x16x32_bf16 v[40:43], v[160:163], a[240:243], 0\n"
    "v_mfma_f32_16x16x32_bf16 v[40:43], v[164:167], a[244:247], v[40:43]\n"
    "v_mfma_f32_16x16x32_bf16 v[44:47], v[164:167], v[12:15], 0\n"
    "v_mfma_f32_16x16x32_bf16 v[44:47], v[160:163], v[8:11], v[44:47]\n"
    "ds_read_b128 v[168:171], v205 offset:2048\n"
    "ds_read_b128 v[172:175], v205 offset:3072\n"
    "s_waitcnt lgkmcnt(1)\n"
    "v_mfma_f32_16x16x32_bf16 v[44:47], v[168:171], v[16:19], v[44:47]\n"
    "s_waitcnt lgkmcnt(0)\n"
    "v_mfma_f32_16x16x32_bf16 v[44:47], v[172:175], v[20:23], v[44:47]\n"
    "v_mfma_f32_16x16x32_bf16 v[40:43], v[172:175], a[252:255], v[40:43]\n"
    "v_mfma_f32_16x16x32_bf16 v[40:43], v[168:171], a[248:251], v[40:43]\n"
    "v_mfma_f32_16x16x32_bf16 v[36:39], v[168:171], a[224:227], v[36:39]\n"
    "v_mfma_f32_16x16x32_bf16 v[36:39], v[172:175], a[228:231], v[36:39]\n"
    "v_mfma_f32_16x16x32_bf16 v[32:35], v[172:175], a[204:207], v[32:35]\n"
    "v_mfma_f32_16x16x32_bf16 v[32:35], v[168:171], a[200:203], v[32:35]\n"
    "ds_read_b128 v[176:179], v205 offset:4096\n"
    "ds_read_b128 v[180:183], v205 offset:5120\n"
    "s_waitcnt lgkmcnt(1)\n"
    "v_mfma_f32_16x16x32_bf16 v[32:35], v[176:179], a[208:211], v[32:35]\n"
    "s_waitcnt lgkmcnt(0)\n"
    "v_mfma_f32_16x16x32_bf16 v[32:35], v[180:183], a[212:215], v[32:35]\n"
    "v_mfma_f32_16x16x32_bf16 v[36:39], v[180:183], a[236:239], v[36:39]\n"
    "v_mfma_f32_16x16x32_bf16 v[36:39], v[176:179], a[232:235], v[36:39]\n"
    "v_mfma_f32_16x16x32_bf16 v[40:43], v[176:179], v[0:3], v[40:43]\n"
    "v_mfma_f32_16x16x32_bf16 v[40:43], v[180:183], v[4:7], v[40:43]\n"
    "v_mfma_f32_16x16x32_bf16 v[44:47], v[180:183], v[28:31], v[44:47]\n"
    "v_mfma_f32_16x16x32_bf16 v[44:47], v[176:179], v[24:27], v[44:47]\n"
    "ds_read_b128 v[160:163], v205 offset:10240\n"
    "ds_read_b128 v[164:167], v205 offset:11264\n"
    "s_waitcnt lgkmcnt(1)\n"
    "v_mfma_f32_16x16x32_bf16 v[60:63], v[160:163], v[24:27], 0\n"
    "s_waitcnt lgkmcnt(0)\n"
    "v_mfma_f32_16x16x32_bf16 v[60:63], v[164:167], v[28:31], v[60:63]\n"
    "v_mfma_f32_16x16x32_bf16 v[56:59], v[164:167], v[4:7], 0\n"
    "v_mfma_f32_16x16x32_bf16 v[56:59], v[160:163], v[0:3], v[56:59]\n"
    "v_mfma_f32_16x16x32_bf16 v[52:55], v[160:163], a[232:235], 0\n"
    "v_mfma_f32_16x16x32_bf16 v[52:55], v[164:167], a[236:239], v[52:55]\n"
    "v_mfma_f32_16x16x32_bf16 v[48:51], v[164:167], a[212:215], 0\n"
    "v_mfma_f32_16x16x32_bf16 v[48:51], v[160:163], a[208:211], v[48:51]\n"
    "ds_read_b128 v[168:171], v205 offset:8192\n"
    "ds_read_b128 v[172:175], v205 offset:9216\n"
    "s_waitcnt lgkmcnt(1)\n"
    "v_mfma_f32_16x16x32_bf16 v[48:51], v[168:171], a[200:203], v[48:51]\n"
    "s_waitcnt lgkmcnt(0)\n"
    "v_mfma_f32_16x16x32_bf16 v[48:51], v[172:175], a[204:207], v[48:51]\n"
    "v_mfma_f32_16x16x32_bf16 v[52:55], v[172:175], a[228:231], v[52:55]\n"
    "v_mfma_f32_16x16x32_bf16 v[52:55], v[168:171], a[224:227], v[52:55]\n"
    "v_mfma_f32_16x16x32_bf16 v[56:59], v[168:171], a[248:251], v[56:59]\n"
    "v_mfma_f32_16x16x32_bf16 v[56:59], v[172:175], a[252:255], v[56:59]\n"
    "v_mfma_f32_16x16x32_bf16 v[60:63], v[172:175], v[20:23], v[60:63]\n"
    "v_mfma_f32_16x16x32_bf16 v[60:63], v[168:171], v[16:19], v[60:63]\n"
    "ds_read_b128 v[176:179], v205 offset:6144\n"
    "ds_read_b128 v[180:183], v205 offset:7168\n"
    "s_waitcnt lgkmcnt(1)\n"
    "v_mfma_f32_16x16x32_bf16 v[60:63], v[176:179], v[8:11], v[60:63]\n"
    "s_waitcnt lgkmcnt(0)\n"
    "v_mfma_f32_16x16x32_bf16 v[60:63], v[180:183], v[12:15], v[60:63]\n"
    "v_mfma_f32_16x16x32_bf16 v[56:59], v[180:183], a[244:247], v[56:59]\n"
    "v_mfma_f32_16x16x32_bf16 v[56:59], v[176:179], a[240:243], v[56:59]\n"
    "v_mfma_f32_16x16x32_bf16 v[52:55], v[176:179], a[216:219], v[52:55]\n"
    "v_mfma_f32_16x16x32_bf16 v[52:55], v[180:183], a[220:223], v[52:55]\n"
    "v_mfma_f32_16x16x32_bf16 v[48:51], v[180:183], a[196:199], v[48:51]\n"
    "v_mfma_f32_16x16x32_bf16 v[48:51], v[176:179], a[192:195], v[48:51]\n"
    "s_add_u32 s78, s63, 0x3000\n"
    "s_add_u32 m0, s62, 0\n"
    "s_nop 0\n"
    "buffer_load_dwordx4 v200, s[36:39], s78 offen sc1 lds\n"
    "buffer_load_dwordx4 v200, s[36:39], s78 offen offset:1024 sc1 lds\n"
    "buffer_load_dwordx4 v200, s[36:39], s78 offen offset:2048 sc1 lds\n"
    "buffer_load_dwordx4 v200, s[36:39], s78 offen offset:3072 sc1 lds\n"
    "s_add_u32 s78, s63, 0x4000\n"
    "s_add_u32 m0, s62, 0x1000\n"
    "s_nop 0\n"
    "buffer_load_dwordx4 v200, s[36:39], s78 offen sc1 lds\n"
    "buffer_load_dwordx4 v200, s[36:39], s78 offen offset:1024 sc1 lds\n"
    "buffer_load_dwordx4 v200, s[36:39], s78 offen offset:2048 sc1 lds\n"
    "buffer_load_dwordx4 v200, s[36:39], s78 offen offset:3072 sc1 lds\n"
    "s_add_u32 s78, s63, 0x5000\n"
    "s_add_u32 m0, s62, 0x2000\n"
    "s_nop 0\n"
    "buffer_load_dwordx4 v200, s[36:39], s78 offen sc1 lds\n"
    "buffer_load_dwordx4 v200, s[36:39], s78 offen offset:1024 sc1 lds\n"
    "buffer_load_dwordx4 v200, s[36:39], s78 offen offset:2048 sc1 lds\n"
    "buffer_load_dwordx4 v200, s[36:39], s78 offen offset:3072 sc1 lds\n"
    "s_waitcnt vmcnt(0)\n"
    "ds_read_b128 v[160:163], v205\n"
    "ds_read_b128 v[164:167], v205 offset:1024\n"
    "s_waitcnt lgkmcnt(1)\n"
    "v_mfma_f32_16x16x32_bf16 v[64:67], v[160:163], a[192:195], 0\n"
    "s_waitcnt lgkmcnt(0)\n"
    "v_mfma_f32_16x16x32_bf16 v[64:67], v[164:167], a[196:199], v[64:67]\n"
    "v_mfma_f32_16x16x32_bf16 v[68:71], v[164:167], a[220:223], 0\n"
    "v_mfma_f32_16x16x32_bf16 v[68:71], v[160:163], a[216:219], v[68:71]\n"
    "v_mfma_f32_16x16x32_bf16 v[72:75], v[160:163], a[240:243], 0\n"
    "v_mfma_f32_16x16x32_bf16 v[72:75], v[164:167], a[244:247], v[72:75]\n"
    "v_mfma_f32_16x16x32_bf16 v[76:79], v[164:167], v[12:15], 0\n"
    "v_mfma_f32_16x16x32_bf16 v[76:79], v[160:163], v[8:11], v[76:79]\n"
    "ds_read_b128 v[168:171], v205 offset:2048\n"
    "ds_read_b128 v[172:175], v205 offset:3072\n"
    "s_waitcnt lgkmcnt(1)\n"
    "v_mfma_f32_16x16x32_bf16 v[76:79], v[168:171], v[16:19], v[76:79]\n"
    "s_waitcnt lgkmcnt(0)\n"
    "v_mfma_f32_16x16x32_bf16 v[76:79], v[172:175], v[20:23], v[76:79]\n"
    "v_mfma_f32_16x16x32_bf16 v[72:75], v[172:175], a[252:255], v[72:75]\n"
    "v_mfma_f32_16x16x32_bf16 v[72:75], v[168:171], a[248:251], v[72:75]\n"
    "v_mfma_f32_16x16x32_bf16 v[68:71], v[168:171], a[224:227], v[68:71]\n"
    "v_mfma_f32_16x16x32_bf16 v[68:71], v[172:175], a[228:231], v[68:71]\n"
    "v_mfma_f32_16x16x32_bf16 v[64:67], v[172:175], a[204:207], v[64:67]\n"
    "v_mfma_f32_16x16x32_bf16 v[64:67], v[168:171], a[200:203], v[64:67]\n"
    "ds_read_b128 v[176:179], v205 offset:4096\n"
    "ds_read_b128 v[180:183], v205 offset:5120\n"
    "s_waitcnt lgkmcnt(1)\n"
    "v_mfma_f32_16x16x32_bf16 v[64:67], v[176:179], a[208:211], v[64:67]\n"
    "s_waitcnt lgkmcnt(0)\n"
    "v_mfma_f32_16x16x32_bf16 v[64:67], v[180:183], a[212:215], v[64:67]\n"
    "v_mfma_f32_16x16x32_bf16 v[68:71], v[180:183], a[236:239], v[68:71]\n"
    "v_mfma_f32_16x16x32_bf16 v[68:71], v[176:179], a[232:235], v[68:71]\n"
    "v_mfma_f32_16x16x32_bf16 v[72:75], v[176:179], v[0:3], v[72:75]\n"
    "v_mfma_f32_16x16x32_bf16 v[72:75], v[180:183], v[4:7], v[72:75]\n"
    "v_mfma_f32_16x16x32_bf16 v[76:79], v[180:183], v[28:31], v[76:79]\n"
    "v_mfma_f32_16x16x32_bf16 v[76:79], v[176:179], v[24:27], v[76:79]\n"
    "ds_read_b128 v[160:163], v205 offset:10240\n"
    "ds_read_b128 v[164:167], v205 offset:11264\n"
    "s_waitcnt lgkmcnt(1)\n"
    "v_mfma_f32_16x16x32_bf16 v[92:95], v[160:163], v[24:27], 0\n"
    "s_waitcnt lgkmcnt(0)\n"
    "v_mfma_f32_16x16x32_bf16 v[92:95], v[164:167], v[28:31], v[92:95]\n"
    "v_mfma_f32_16x16x32_bf16 v[88:91], v[164:167], v[4:7], 0\n"
    "v_mfma_f32_16x16x32_bf16 v[88:91], v[160:163], v[0:3], v[88:91]\n"
    "v_mfma_f32_16x16x32_bf16 v[84:87], v[160:163], a[232:235], 0\n"
    "v_mfma_f32_16x16x32_bf16 v[84:87], v[164:167], a[236:239], v[84:87]\n"
    "v_mfma_f32_16x16x32_bf16 v[80:83], v[164:167], a[212:215], 0\n"
    "v_mfma_f32_16x16x32_bf16 v[80:83], v[160:163], a[208:211], v[80:83]\n"
    "ds_read_b128 v[168:171], v205 offset:8192\n"
    "ds_read_b128 v[172:175], v205 offset:9216\n"
    "s_waitcnt lgkmcnt(1)\n"
    "v_mfma_f32_16x16x32_bf16 v[80:83], v[168:171], a[200:203], v[80:83]\n"
    "s_waitcnt lgkmcnt(0)\n"
    "v_mfma_f32_16x16x32_bf16 v[80:83], v[172:175], a[204:207], v[80:83]\n"
    "v_mfma_f32_16x16x32_bf16 v[84:87], v[172:175], a[228:231], v[84:87]\n"
    "v_mfma_f32_16x16x32_bf16 v[84:87], v[168:171], a[224:227], v[84:87]\n"
    "v_mfma_f32_16x16x32_bf16 v[88:91], v[168:171], a[248:251], v[88:91]\n"
    "v_mfma_f32_16x16x32_bf16 v[88:91], v[172:175], a[252:255], v[88:91]\n"
    "v_mfma_f32_16x16x32_bf16 v[92:95], v[172:175], v[20:23], v[92:95]\n"
    "v_mfma_f32_16x16x32_bf16 v[92:95], v[168:171], v[16:19], v[92:95]\n"
    "ds_read_b128 v[176:179], v205 offset:6144\n"
    "ds_read_b128 v[180:183], v205 offset:7168\n"
    "s_waitcnt lgkmcnt(1)\n"
    "v_mfma_f32_16x16x32_bf16 v[92:95], v[176:179], v[8:11], v[92:95]\n"
    "s_waitcnt lgkmcnt(0)\n"
    "v_mfma_f32_16x16x32_bf16 v[92:95], v[180:183], v[12:15], v[92:95]\n"
    "v_mfma_f32_16x16x32_bf16 v[88:91], v[180:183], a[244:247], v[88:91]\n"
    "v_mfma_f32_16x16x32_bf16 v[88:91], v[176:179], a[240:243], v[88:91]\n"
    "v_mfma_f32_16x16x32_bf16 v[84:87], v[176:179], a[216:219], v[84:87]\n"
    "v_mfma_f32_16x16x32_bf16 v[84:87], v[180:183], a[220:223], v[84:87]\n"
    "v_mfma_f32_16x16x32_bf16 v[80:83], v[180:183], a[196:199], v[80:83]\n"
    "v_mfma_f32_16x16x32_bf16 v[80:83], v[176:179], a[192:195], v[80:83]\n"
    "v_max3_f32 v196, v32, v33, v34\n"
    "v_max3_f32 v196, v196, v35, v48\n"
    "v_max3_f32 v196, v196, v49, v50\n"
    "v_max3_f32 v196, v196, v51, v64\n"
    "v_max3_f32 v196, v196, v65, v66\n"
    "s_nop 2\n"
    "v_max3_f32 v196, v196, v67, v80\n"
    "v_max3_f32 v196, v196, v81, v82\n"
    "v_max_f32 v196, v196, v83\n"
    "v_mov_b32 v197, v196\n"
    "s_nop 1\n"
    "v_permlane32_swap_b32 v196, v197\n"
    "v_max_f32 v196, v196, v197\n"
    "v_mov_b32 v197, v196\n"
    "s_nop 1\n"
    "v_permlane16_swap_b32 v196, v197\n"
    "v_max_f32 v196, v196, v197\n"
    "v_max_f32 v196, v196, v96\n"
    "v_sub_f32 v197, v96, v196\n"
    "v_mov_b32 v96, v196\n"
    "v_exp_f32 v197, v197\n"
    "v_sub_f32 v32, v32, v196\n"
    "v_sub_f32 v33, v33, v196\n"
    "v_sub_f32 v34, v34, v196\n"
    "v_sub_f32 v35, v35, v196\n"
    "v_sub_f32 v48, v48, v196\n"
    "v_sub_f32 v49, v49, v196\n"
    "v_sub_f32 v50, v50, v196\n"
    "v_sub_f32 v51, v51, v196\n"
    "v_sub_f32 v64, v64, v196\n"
    "v_sub_f32 v65, v65, v196\n"
    "v_sub_f32 v66, v66, v196\n"
    "v_sub_f32 v67, v67, v196\n"
    "v_sub_f32 v80, v80, v196\n"
    "v_sub_f32 v81, v81, v196\n"
    "v_sub_f32 v82, v82, v196\n"
    "v_sub_f32 v83, v83, v196\n"
    "v_mul_f32 v112, v112, v197\n"
    "v_mul_f32 v113, v113, v197\n"
    "v_mul_f32 v114, v114, v197\n"
    "v_mul_f32 v115, v115, v197\n"
    "v_accvgpr_read_b32 v198, a0\n"
    "v_accvgpr_read_b32 v199, a1\n"
    "v_mul_f32 v198, v198, v197\n"
    "v_mul_f32 v199, v199, v197\n"
    "v_accvgpr_write_b32 a0, v198\n"
    "v_accvgpr_write_b32 a1, v199\n"
    "v_accvgpr_read_b32 v198, a2\n"
    "v_accvgpr_read_b32 v199, a3\n"
    "v_mul_f32 v198, v198, v197\n"
    "v_mul_f32 v199, v199, v197\n"
    "v_accvgpr_write_b32 a2, v198\n"
    "v_accvgpr_write_b32 a3, v199\n"
    "v_accvgpr_read_b32 v198, a16\n"
    "v_accvgpr_read_b32 v199, a17\n"
    "v_mul_f32 v198, v198, v197\n"
    "v_mul_f32 v199, v199, v197\n"
    "v_accvgpr_write_b32 a16, v198\n"
    "v_accvgpr_write_b32 a17, v199\n"
    "v_accvgpr_read_b32 v198, a18\n"
    "v_accvgpr_read_b32 v199, a19\n"
    "v_mul_f32 v198, v198, v197\n"
    "v_mul_f32 v199, v199, v197\n"
    "v_accvgpr_write_b32 a18, v198\n"
    "v_accvgpr_write_b32 a19, v199\n"
    "v_accvgpr_read_b32 v198, a32\n"
    "v_accvgpr_read_b32 v199, a33\n"
    "v_mul_f32 v198, v198, v197\n"
    "v_mul_f32 v199, v199, v197\n"
    "v_accvgpr_write_b32 a32, v198\n"
    "v_accvgpr_write_b32 a33, v199\n"
    "v_accvgpr_read_b32 v198, a34\n"
    "v_accvgpr_read_b32 v199, a35\n"
    "v_mul_f32 v198, v198, v197\n"
    "v_mul_f32 v199, v199, v197\n"
    "v_accvgpr_write_b32 a34, v198\n"
    "v_accvgpr_write_b32 a35, v199\n"
    "v_accvgpr_read_b32 v198, a48\n"
    "v_accvgpr_read_b32 v199, a49\n"
    "v_mul_f32 v198, v198, v197\n"
    "v_mul_f32 v199, v199, v197\n"
    "v_accvgpr_write_b32 a48, v198\n"
    "v_accvgpr_write_b32 a49, v199\n"
    "v_accvgpr_read_b32 v198, a50\n"
    "v_accvgpr_read_b32 v199, a51\n"
    "v_mul_f32 v198, v198, v197\n"
    "v_mul_f32 v199, v199, v197\n"
    "v_accvgpr_write_b32 a50, v198\n"
    "v_accvgpr_write_b32 a51, v199\n"
    "v_accvgpr_read_b32 v198, a64\n"
    "v_accvgpr_read_b32 v199, a65\n"
    "v_mul_f32 v198, v198, v197\n"
    "v_mul_f32 v199, v199, v197\n"
    "v_accvgpr_write_b32 a64, v198\n"
    "v_accvgpr_write_b32 a65, v199\n"
    "v_accvgpr_read_b32 v198, a66\n"
    "v_accvgpr_read_b32 v199, a67\n"
    "v_mul_f32 v198, v198, v197\n"
    "v_mul_f32 v199, v199, v197\n"
    "v_accvgpr_write_b32 a66, v198\n"
    "v_accvgpr_write_b32 a67, v199\n"
    "v_accvgpr_read_b32 v198, a80\n"
    "v_accvgpr_read_b32 v199, a81\n"
    "v_mul_f32 v198, v198, v197\n"
    "v_mul_f32 v199, v199, v197\n"
    "v_accvgpr_write_b32 a80, v198\n"
    "v_accvgpr_write_b32 a81, v199\n"
    "v_accvgpr_read_b32 v198, a82\n"
    "v_accvgpr_read_b32 v199, a83\n"
    "v_mul_f32 v198, v198, v197\n"
    "v_mul_f32 v199, v199, v197\n"
    "v_accvgpr_write_b32 a82, v198\n"
    "v_accvgpr_write_b32 a83, v199\n"
    "v_accvgpr_read_b32 v198, a96\n"
    "v_accvgpr_read_b32 v199, a97\n"
    "v_mul_f32 v198, v198, v197\n"
    "v_mul_f32 v199, v199, v197\n"
    "v_accvgpr_write_b32 a96, v198\n"
    "v_accvgpr_write_b32 a97, v199\n"
    "v_accvgpr_read_b32 v198, a98\n"
    "v_accvgpr_read_b32 v199, a99\n"
    "v_mul_f32 v198, v198, v197\n"
    "v_mul_f32 v199, v199, v197\n"
    "v_accvgpr_write_b32 a98, v198\n"
    "v_accvgpr_write_b32 a99, v199\n"
    "v_accvgpr_read_b32 v198, a112\n"
    "v_accvgpr_read_b32 v199, a113\n"
    "v_mul_f32 v198, v198, v197\n"
    "v_mul_f32 v199, v199, v197\n"
    "v_accvgpr_write_b32 a112, v198\n"
    "v_accvgpr_write_b32 a113, v199\n"
    "v_accvgpr_read_b32 v198, a114\n"
    "v_accvgpr_read_b32 v199, a115\n"
    "v_mul_f32 v198, v198, v197\n"
    "v_mul_f32 v199, v199, v197\n"
    "v_accvgpr_write_b32 a114, v198\n"
    "v_accvgpr_write_b32 a115, v199\n"
    "v_accvgpr_read_b32 v198, a128\n"
    "v_accvgpr_read_b32 v199, a129\n"
    "v_mul_f32 v198, v198, v197\n"
    "v_mul_f32 v199, v199, v197\n"
    "v_accvgpr_write_b32 a128, v198\n"
    "v_accvgpr_write_b32 a129, v199\n"
    "v_accvgpr_read_b32 v198, a130\n"
    "v_accvgpr_read_b32 v199, a131\n"
    "v_mul_f32 v198, v198, v197\n"
    "v_mul_f32 v199, v199, v197\n"
    "v_accvgpr_write_b32 a130, v198\n"
    "v_accvgpr_write_b32 a131, v199\n"
    "v_accvgpr_read_b32 v198, a144\n"
    "v_accvgpr_read_b32 v199, a145\n"
    "v_mul_f32 v198, v198, v197\n"
    "v_mul_f32 v199, v199, v197\n"
    "v_accvgpr_write_b32 a144, v198\n"
    "v_accvgpr_write_b32 a145, v199\n"
    "v_accvgpr_read_b32 v198, a146\n"
    "v_accvgpr_read_b32 v199, a147\n"
    "v_mul_f32 v198, v198, v197\n"
    "v_mul_f32 v199, v199, v197\n"
    "v_accvgpr_write_b32 a146, v198\n"
    "v_accvgpr_write_b32 a147, v199\n"
    "v_accvgpr_read_b32 v198, a160\n"
    "v_accvgpr_read_b32 v199, a161\n"
    "v_mul_f32 v198, v198, v197\n"
    "v_mul_f32 v199, v199, v197\n"
    "v_accvgpr_write_b32 a160, v198\n"
    "v_accvgpr_write_b32 a161, v199\n"
    "v_accvgpr_read_b32 v198, a162\n"
    "v_accvgpr_read_b32 v199, a163\n"
    "v_mul_f32 v198, v198, v197\n"
    "v_mul_f32 v199, v199, v197\n"
    "v_accvgpr_write_b32 a162, v198\n"
    "v_accvgpr_write_b32 a163, v199\n"
    "v_accvgpr_read_b32 v198, a176\n"
    "v_accvgpr_read_b32 v199, a177\n"
    "v_mul_f32 v198, v198, v197\n"
    "v_mul_f32 v199, v199, v197\n"
    "v_accvgpr_write_b32 a176, v198\n"
    "v_accvgpr_write_b32 a177, v199\n"
    "v_accvgpr_read_b32 v198, a178\n"
    "v_accvgpr_read_b32 v199, a179\n"
    "v_mul_f32 v198, v198, v197\n"
    "v_mul_f32 v199, v199, v197\n"
    "v_accvgpr_write_b32 a178, v198\n"
    "v_accvgpr_write_b32 a179, v199\n"
    "v_max3_f32 v196, v36, v37, v38\n"
    "v_max3_f32 v196, v196, v39, v52\n"
    "v_max3_f32 v196, v196, v53, v54\n"
    "v_max3_f32 v196, v196, v55, v68\n"
    "v_max3_f32 v196, v196, v69, v70\n"
    "v_max3_f32 v196, v196, v71, v84\n"
    "v_max3_f32 v196, v196, v85, v86\n"
    "v_max_f32 v196, v196, v87\n"
    "v_mov_b32 v197, v196\n"
    "s_nop 1\n"
    "v_permlane32_swap_b32 v196, v197\n"
    "v_max_f32 v196, v196, v197\n"
    "v_mov_b32 v197, v196\n"
    "s_nop 1\n"
    "v_permlane16_swap_b32 v196, v197\n"
    "v_max_f32 v196, v196, v197\n"
    "v_max_f32 v196, v196, v100\n"
    "v_sub_f32 v197, v100, v196\n"
    "v_mov_b32 v100, v196\n"
    "v_exp_f32 v197, v197\n"
    "v_sub_f32 v36, v36, v196\n"
    "v_sub_f32 v37, v37, v196\n"
    "v_sub_f32 v38, v38, v196\n"
    "v_sub_f32 v39, v39, v196\n"
    "v_sub_f32 v52, v52, v196\n"
    "v_sub_f32 v53, v53, v196\n"
    "v_sub_f32 v54, v54, v196\n"
    "v_sub_f32 v55, v55, v196\n"
    "v_sub_f32 v68, v68, v196\n"
    "v_sub_f32 v69, v69, v196\n"
    "v_sub_f32 v70, v70, v196\n"
    "v_sub_f32 v71, v71, v196\n"
    "v_sub_f32 v84, v84, v196\n"
    "v_sub_f32 v85, v85, v196\n"
    "v_sub_f32 v86, v86, v196\n"
    "v_sub_f32 v87, v87, v196\n"
    "v_mul_f32 v116, v116, v197\n"
    "v_mul_f32 v117, v117, v197\n"
    "v_mul_f32 v118, v118, v197\n"
    "v_mul_f32 v119, v119, v197\n"
    "v_accvgpr_read_b32 v198, a4\n"
    "v_accvgpr_read_b32 v199, a5\n"
    "v_mul_f32 v198, v198, v197\n"
    "v_mul_f32 v199, v199, v197\n"
    "v_accvgpr_write_b32 a4, v198\n"
    "v_accvgpr_write_b32 a5, v199\n"
    "v_accvgpr_read_b32 v198, a6\n"
    "v_accvgpr_read_b32 v199, a7\n"
    "v_mul_f32 v198, v198, v197\n"
    "v_mul_f32 v199, v199, v197\n"
    "v_accvgpr_write_b32 a6, v198\n"
    "v_accvgpr_write_b32 a7, v199\n"
    "v_accvgpr_read_b32 v198, a20\n"
    "v_accvgpr_read_b32 v199, a21\n"
    "v_mul_f32 v198, v198, v197\n"
    "v_mul_f32 v199, v199, v197\n"
    "v_accvgpr_write_b32 a20, v198\n"
    "v_accvgpr_write_b32 a21, v199\n"
    "v_accvgpr_read_b32 v198, a22\n"
    "v_accvgpr_read_b32 v199, a23\n"
    "v_mul_f32 v198, v198, v197\n"
    "v_mul_f32 v199, v199, v197\n"
    "v_accvgpr_write_b32 a22, v198\n"
    "v_accvgpr_write_b32 a23, v199\n"
    "v_accvgpr_read_b32 v198, a36\n"
    "v_accvgpr_read_b32 v199, a37\n"
    "v_mul_f32 v198, v198, v197\n"
    "v_mul_f32 v199, v199, v197\n"
    "v_accvgpr_write_b32 a36, v198\n"
    "v_accvgpr_write_b32 a37, v199\n"
    "v_accvgpr_read_b32 v198, a38\n"
    "v_accvgpr_read_b32 v199, a39\n"
    "v_mul_f32 v198, v198, v197\n"
    "v_mul_f32 v199, v199, v197\n"
    "v_accvgpr_write_b32 a38, v198\n"
    "v_accvgpr_write_b32 a39, v199\n"
    "v_accvgpr_read_b32 v198, a52\n"
    "v_accvgpr_read_b32 v199, a53\n"
    "v_mul_f32 v198, v198, v197\n"
    "v_mul_f32 v199, v199, v197\n"
    "v_accvgpr_write_b32 a52, v198\n"
    "v_accvgpr_write_b32 a53, v199\n"
    "v_accvgpr_read_b32 v198, a54\n"
    "v_accvgpr_read_b32 v199, a55\n"
    "v_mul_f32 v198, v198, v197\n"
    "v_mul_f32 v199, v199, v197\n"
    "v_accvgpr_write_b32 a54, v198\n"
    "v_accvgpr_write_b32 a55, v199\n"
    "v_accvgpr_read_b32 v198, a68\n"
    "v_accvgpr_read_b32 v199, a69\n"
    "v_mul_f32 v198, v198, v197\n"
    "v_mul_f32 v199, v199, v197\n"
    "v_accvgpr_write_b32 a68, v198\n"
    "v_accvgpr_write_b32 a69, v199\n"
    "v_accvgpr_read_b32 v198, a70\n"
    "v_accvgpr_read_b32 v199, a71\n"
    "v_mul_f32 v198, v198, v197\n"
    "v_mul_f32 v199, v199, v197\n"
    "v_accvgpr_write_b32 a70, v198\n"
    "v_accvgpr_write_b32 a71, v199\n"
    "v_accvgpr_read_b32 v198, a84\n"
    "v_accvgpr_read_b32 v199, a85\n"
    "v_mul_f32 v198, v198, v197\n"
    "v_mul_f32 v199, v199, v197\n"
    "v_accvgpr_write_b32 a84, v198\n"
    "v_accvgpr_write_b32 a85, v199\n"
    "v_accvgpr_read_b32 v198, a86\n"
    "v_accvgpr_read_b32 v199, a87\n"
    "v_mul_f32 v198, v198, v197\n"
    "v_mul_f32 v199, v199, v197\n"
    "v_accvgpr_write_b32 a86, v198\n"
    "v_accvgpr_write_b32 a87, v199\n"
    "v_accvgpr_read_b32 v198, a100\n"
    "v_accvgpr_read_b32 v199, a101\n"
    "v_mul_f32 v198, v198, v197\n"
    "v_mul_f32 v199, v199, v197\n"
    "v_accvgpr_write_b32 a100, v198\n"
    "v_accvgpr_write_b32 a101, v199\n"
    "v_accvgpr_read_b32 v198, a102\n"
    "v_accvgpr_read_b32 v199, a103\n"
    "v_mul_f32 v198, v198, v197\n"
    "v_mul_f32 v199, v199, v197\n"
    "v_accvgpr_write_b32 a102, v198\n"
    "v_accvgpr_write_b32 a103, v199\n"
    "v_accvgpr_read_b32 v198, a116\n"
    "v_accvgpr_read_b32 v199, a117\n"
    "v_mul_f32 v198, v198, v197\n"
    "v_mul_f32 v199, v199, v197\n"
    "v_accvgpr_write_b32 a116, v198\n"
    "v_accvgpr_write_b32 a117, v199\n"
    "v_accvgpr_read_b32 v198, a118\n"
    "v_accvgpr_read_b32 v199, a119\n"
    "v_mul_f32 v198, v198, v197\n"
    "v_mul_f32 v199, v199, v197\n"
    "v_accvgpr_write_b32 a118, v198\n"
    "v_accvgpr_write_b32 a119, v199\n"
    "v_accvgpr_read_b32 v198, a132\n"
    "v_accvgpr_read_b32 v199, a133\n"
    "v_mul_f32 v198, v198, v197\n"
    "v_mul_f32 v199, v199, v197\n"
    "v_accvgpr_write_b32 a132, v198\n"
    "v_accvgpr_write_b32 a133, v199\n"
    "v_accvgpr_read_b32 v198, a134\n"
    "v_accvgpr_read_b32 v199, a135\n"
    "v_mul_f32 v198, v198, v197\n"
    "v_mul_f32 v199, v199, v197\n"
    "v_accvgpr_write_b32 a134, v198\n"
    "v_accvgpr_write_b32 a135, v199\n"
    "v_accvgpr_read_b32 v198, a148\n"
    "v_accvgpr_read_b32 v199, a149\n"
    "v_mul_f32 v198, v198, v197\n"
    "v_mul_f32 v199, v199, v197\n"
    "v_accvgpr_write_b32 a148, v198\n"
    "v_accvgpr_write_b32 a149, v199\n"
    "v_accvgpr_read_b32 v198, a150\n"
    "v_accvgpr_read_b32 v199, a151\n"
    "v_mul_f32 v198, v198, v197\n"
    "v_mul_f32 v199, v199, v197\n"
    "v_accvgpr_write_b32 a150, v198\n"
    "v_accvgpr_write_b32 a151, v199\n"
    "v_accvgpr_read_b32 v198, a164\n"
    "v_accvgpr_read_b32 v199, a165\n"
    "v_mul_f32 v198, v198, v197\n"
    "v_mul_f32 v199, v199, v197\n"
    "v_accvgpr_write_b32 a164, v198\n"
    "v_accvgpr_write_b32 a165, v199\n"
    "v_accvgpr_read_b32 v198, a166\n"
    "v_accvgpr_read_b32 v199, a167\n"
    "v_mul_f32 v198, v198, v197\n"
    "v_mul_f32 v199, v199, v197\n"
    "v_accvgpr_write_b32 a166, v198\n"
    "v_accvgpr_write_b32 a167, v199\n"
    "v_accvgpr_read_b32 v198, a180\n"
    "v_accvgpr_read_b32 v199, a181\n"
    "v_mul_f32 v198, v198, v197\n"
    "v_mul_f32 v199, v199, v197\n"
    "v_accvgpr_write_b32 a180, v198\n"
    "v_accvgpr_write_b32 a181, v199\n"
    "v_accvgpr_read_b32 v198, a182\n"
    "v_accvgpr_read_b32 v199, a183\n"
    "v_mul_f32 v198, v198, v197\n"
    "v_mul_f32 v199, v199, v197\n"
    "v_accvgpr_write_b32 a182, v198\n"
    "v_accvgpr_write_b32 a183, v199\n"
    "v_max3_f32 v196, v40, v41, v42\n"
    "v_max3_f32 v196, v196, v43, v56\n"
    "v_max3_f32 v196, v196, v57, v58\n"
    "v_max3_f32 v196, v196, v59, v72\n"
    "v_max3_f32 v196, v196, v73, v74\n"
    "v_max3_f32 v196, v196, v75, v88\n"
    "v_max3_f32 v196, v196, v89, v90\n"
    "v_max_f32 v196, v196, v91\n"
    "v_mov_b32 v197, v196\n"
    "s_nop 1\n"
    "v_permlane32_swap_b32 v196, v197\n"
    "v_max_f32 v196, v196, v197\n"
    "v_mov_b32 v197, v196\n"
    "s_nop 1\n"
    "v_permlane16_swap_b32 v196, v197\n"
    "v_max_f32 v196, v196, v197\n"
    "v_max_f32 v196, v196, v104\n"
    "v_sub_f32 v197, v104, v196\n"
    "v_mov_b32 v104, v196\n"
    "v_exp_f32 v197, v197\n"
    "v_sub_f32 v40, v40, v196\n"
    "v_sub_f32 v41, v41, v196\n"
    "v_sub_f32 v42, v42, v196\n"
    "v_sub_f32 v43, v43, v196\n"
    "v_sub_f32 v56, v56, v196\n"
    "v_sub_f32 v57, v57, v196\n"
    "v_sub_f32 v58, v58, v196\n"
    "v_sub_f32 v59, v59, v196\n"
    "v_sub_f32 v72, v72, v196\n"
    "v_sub_f32 v73, v73, v196\n"
    "v_sub_f32 v74, v74, v196\n"
    "v_sub_f32 v75, v75, v196\n"
    "v_sub_f32 v88, v88, v196\n"
    "v_sub_f32 v89, v89, v196\n"
    "v_sub_f32 v90, v90, v196\n"
    "v_sub_f32 v91, v91, v196\n"
    "v_mul_f32 v120, v120, v197\n"
    "v_mul_f32 v121, v121, v197\n"
    "v_mul_f32 v122, v122, v197\n"
    "v_mul_f32 v123, v123, v197\n"
    "v_accvgpr_read_b32 v198, a8\n"
    "v_accvgpr_read_b32 v199, a9\n"
    "v_mul_f32 v198, v198, v197\n"
    "v_mul_f32 v199, v199, v197\n"
    "v_accvgpr_write_b32 a8, v198\n"
    "v_accvgpr_write_b32 a9, v199\n"
    "v_accvgpr_read_b32 v198, a10\n"
    "v_accvgpr_read_b32 v199, a11\n"
    "v_mul_f32 v198, v198, v197\n"
    "v_mul_f32 v199, v199, v197\n"
    "v_accvgpr_write_b32 a10, v198\n"
    "v_accvgpr_write_b32 a11, v199\n"
    "v_accvgpr_read_b32 v198, a24\n"
    "v_accvgpr_read_b32 v199, a25\n"
    "v_mul_f32 v198, v198, v197\n"
    "v_mul_f32 v199, v199, v197\n"
    "v_accvgpr_write_b32 a24, v198\n"
    "v_accvgpr_write_b32 a25, v199\n"
    "v_accvgpr_read_b32 v198, a26\n"
    "v_accvgpr_read_b32 v199, a27\n"
    "v_mul_f32 v198, v198, v197\n"
    "v_mul_f32 v199, v199, v197\n"
    "v_accvgpr_write_b32 a26, v198\n"
    "v_accvgpr_write_b32 a27, v199\n"
    "v_accvgpr_read_b32 v198, a40\n"
    "v_accvgpr_read_b32 v199, a41\n"
    "v_mul_f32 v198, v198, v197\n"
    "v_mul_f32 v199, v199, v197\n"
    "v_accvgpr_write_b32 a40, v198\n"
    "v_accvgpr_write_b32 a41, v199\n"
    "v_accvgpr_read_b32 v198, a42\n"
    "v_accvgpr_read_b32 v199, a43\n"
    "v_mul_f32 v198, v198, v197\n"
    "v_mul_f32 v199, v199, v197\n"
    "v_accvgpr_write_b32 a42, v198\n"
    "v_accvgpr_write_b32 a43, v199\n"
    "v_accvgpr_read_b32 v198, a56\n"
    "v_accvgpr_read_b32 v199, a57\n"
    "v_mul_f32 v198, v198, v197\n"
    "v_mul_f32 v199, v199, v197\n"
    "v_accvgpr_write_b32 a56, v198\n"
    "v_accvgpr_write_b32 a57, v199\n"
    "v_accvgpr_read_b32 v198, a58\n"
    "v_accvgpr_read_b32 v199, a59\n"
    "v_mul_f32 v198, v198, v197\n"
    "v_mul_f32 v199, v199, v197\n"
    "v_accvgpr_write_b32 a58, v198\n"
    "v_accvgpr_write_b32 a59, v199\n"
    "v_accvgpr_read_b32 v198, a72\n"
    "v_accvgpr_read_b32 v199, a73\n"
    "v_mul_f32 v198, v198, v197\n"
    "v_mul_f32 v199, v199, v197\n"
    "v_accvgpr_write_b32 a72, v198\n"
    "v_accvgpr_write_b32 a73, v199\n"
    "v_accvgpr_read_b32 v198, a74\n"
    "v_accvgpr_read_b32 v199, a75\n"
    "v_mul_f32 v198, v198, v197\n"
    "v_mul_f32 v199, v199, v197\n"
    "v_accvgpr_write_b32 a74, v198\n"
    "v_accvgpr_write_b32 a75, v199\n"
    "v_accvgpr_read_b32 v198, a88\n"
    "v_accvgpr_read_b32 v199, a89\n"
    "v_mul_f32 v198, v198, v197\n"
    "v_mul_f32 v199, v199, v197\n"
    "v_accvgpr_write_b32 a88, v198\n"
    "v_accvgpr_write_b32 a89, v199\n"
    "v_accvgpr_read_b32 v198, a90\n"
    "v_accvgpr_read_b32 v199, a91\n"
    "v_mul_f32 v198, v198, v197\n"
    "v_mul_f32 v199, v199, v197\n"
    "v_accvgpr_write_b32 a90, v198\n"
    "v_accvgpr_write_b32 a91, v199\n"
    "v_accvgpr_read_b32 v198, a104\n"
    "v_accvgpr_read_b32 v199, a105\n"
    "v_mul_f32 v198, v198, v197\n"
    "v_mul_f32 v199, v199, v197\n"
    "v_accvgpr_write_b32 a104, v198\n"
    "v_accvgpr_write_b32 a105, v199\n"
    "v_accvgpr_read_b32 v198, a106\n"
    "v_accvgpr_read_b32 v199, a107\n"
    "v_mul_f32 v198, v198, v197\n"
    "v_mul_f32 v199, v199, v197\n"
    "v_accvgpr_write_b32 a106, v198\n"
    "v_accvgpr_write_b32 a107, v199\n"
    "v_accvgpr_read_b32 v198, a120\n"
    "v_accvgpr_read_b32 v199, a121\n"
    "v_mul_f32 v198, v198, v197\n"
    "v_mul_f32 v199, v199, v197\n"
    "v_accvgpr_write_b32 a120, v198\n"
    "v_accvgpr_write_b32 a121, v199\n"
    "v_accvgpr_read_b32 v198, a122\n"
    "v_accvgpr_read_b32 v199, a123\n"
    "v_mul_f32 v198, v198, v197\n"
    "v_mul_f32 v199, v199, v197\n"
    "v_accvgpr_write_b32 a122, v198\n"
    "v_accvgpr_write_b32 a123, v199\n"
    "v_accvgpr_read_b32 v198, a136\n"
    "v_accvgpr_read_b32 v199, a137\n"
    "v_mul_f32 v198, v198, v197\n"
    "v_mul_f32 v199, v199, v197\n"
    "v_accvgpr_write_b32 a136, v198\n"
    "v_accvgpr_write_b32 a137, v199\n"
    "v_accvgpr_read_b32 v198, a138\n"
    "v_accvgpr_read_b32 v199, a139\n"
    "v_mul_f32 v198, v198, v197\n"
    "v_mul_f32 v199, v199, v197\n"
    "v_accvgpr_write_b32 a138, v198\n"
    "v_accvgpr_write_b32 a139, v199\n"
    "v_accvgpr_read_b32 v198, a152\n"
    "v_accvgpr_read_b32 v199, a153\n"
    "v_mul_f32 v198, v198, v197\n"
    "v_mul_f32 v199, v199, v197\n"
    "v_accvgpr_write_b32 a152, v198\n"
    "v_accvgpr_write_b32 a153, v199\n"
    "v_accvgpr_read_b32 v198, a154\n"
    "v_accvgpr_read_b32 v199, a155\n"
    "v_mul_f32 v198, v198, v197\n"
    "v_mul_f32 v199, v199, v197\n"
    "v_accvgpr_write_b32 a154, v198\n"
    "v_accvgpr_write_b32 a155, v199\n"
    "v_accvgpr_read_b32 v198, a168\n"
    "v_accvgpr_read_b32 v199, a169\n"
    "v_mul_f32 v198, v198, v197\n"
    "v_mul_f32 v199, v199, v197\n"
    "v_accvgpr_write_b32 a168, v198\n"
    "v_accvgpr_write_b32 a169, v199\n"
    "v_accvgpr_read_b32 v198, a170\n"
    "v_accvgpr_read_b32 v199, a171\n"
    "v_mul_f32 v198, v198, v197\n"
    "v_mul_f32 v199, v199, v197\n"
    "v_accvgpr_write_b32 a170, v198\n"
    "v_accvgpr_write_b32 a171, v199\n"
    "v_accvgpr_read_b32 v198, a184\n"
    "v_accvgpr_read_b32 v199, a185\n"
    "v_mul_f32 v198, v198, v197\n"
    "v_mul_f32 v199, v199, v197\n"
    "v_accvgpr_write_b32 a184, v198\n"
    "v_accvgpr_write_b32 a185, v199\n"
    "v_accvgpr_read_b32 v198, a186\n"
    "v_accvgpr_read_b32 v199, a187\n"
    "v_mul_f32 v198, v198, v197\n"
    "v_mul_f32 v199, v199, v197\n"
    "v_accvgpr_write_b32 a186, v198\n"
    "v_accvgpr_write_b32 a187, v199\n"
    "v_max3_f32 v196, v44, v45, v46\n"
    "v_max3_f32 v196, v196, v47, v60\n"
    "v_max3_f32 v196, v196, v61, v62\n"
    "v_max3_f32 v196, v196, v63, v76\n"
    "v_max3_f32 v196, v196, v77, v78\n"
    "v_max3_f32 v196, v196, v79, v92\n"
    "v_max3_f32 v196, v196, v93, v94\n"
    "v_max_f32 v196, v196, v95\n"
    "v_mov_b32 v197, v196\n"
    "s_nop 1\n"
    "v_permlane32_swap_b32 v196, v197\n"
    "v_max_f32 v196, v196, v197\n"
    "v_mov_b32 v197, v196\n"
    "s_nop 1\n"
    "v_permlane16_swap_b32 v196, v197\n"
    "v_max_f32 v196, v196, v197\n"
    "v_max_f32 v196, v196, v108\n"
    "v_sub_f32 v197, v108, v196\n"
    "v_mov_b32 v108, v196\n"
    "v_exp_f32 v197, v197\n"
    "v_sub_f32 v44, v44, v196\n"
    "v_sub_f32 v45, v45, v196\n"
    "v_sub_f32 v46, v46, v196\n"
    "v_sub_f32 v47, v47, v196\n"
    "v_sub_f32 v60, v60, v196\n"
    "v_sub_f32 v61, v61, v196\n"
    "v_sub_f32 v62, v62, v196\n"
    "v_sub_f32 v63, v63, v196\n"
    "v_sub_f32 v76, v76, v196\n"
    "v_sub_f32 v77, v77, v196\n"
    "v_sub_f32 v78, v78, v196\n"
    "v_sub_f32 v79, v79, v196\n"
    "v_sub_f32 v92, v92, v196\n"
    "v_sub_f32 v93, v93, v196\n"
    "v_sub_f32 v94, v94, v196\n"
    "v_sub_f32 v95, v95, v196\n"
    "v_mul_f32 v124, v124, v197\n"
    "v_mul_f32 v125, v125, v197\n"
    "v_mul_f32 v126, v126, v197\n"
    "v_mul_f32 v127, v127, v197\n"
    "v_accvgpr_read_b32 v198, a12\n"
    "v_accvgpr_read_b32 v199, a13\n"
    "v_mul_f32 v198, v198, v197\n"
    "v_mul_f32 v199, v199, v197\n"
    "v_accvgpr_write_b32 a12, v198\n"
    "v_accvgpr_write_b32 a13, v199\n"
    "v_accvgpr_read_b32 v198, a14\n"
    "v_accvgpr_read_b32 v199, a15\n"
    "v_mul_f32 v198, v198, v197\n"
    "v_mul_f32 v199, v199, v197\n"
    "v_accvgpr_write_b32 a14, v198\n"
    "v_accvgpr_write_b32 a15, v199\n"
    "v_accvgpr_read_b32 v198, a28\n"
    "v_accvgpr_read_b32 v199, a29\n"
    "v_mul_f32 v198, v198, v197\n"
    "v_mul_f32 v199, v199, v197\n"
    "v_accvgpr_write_b32 a28, v198\n"
    "v_accvgpr_write_b32 a29, v199\n"
    "v_accvgpr_read_b32 v198, a30\n"
    "v_accvgpr_read_b32 v199, a31\n"
    "v_mul_f32 v198, v198, v197\n"
    "v_mul_f32 v199, v199, v197\n"
    "v_accvgpr_write_b32 a30, v198\n"
    "v_accvgpr_write_b32 a31, v199\n"
    "v_accvgpr_read_b32 v198, a44\n"
    "v_accvgpr_read_b32 v199, a45\n"
    "v_mul_f32 v198, v198, v197\n"
    "v_mul_f32 v199, v199, v197\n"
    "v_accvgpr_write_b32 a44, v198\n"
    "v_accvgpr_write_b32 a45, v199\n"
    "v_accvgpr_read_b32 v198, a46\n"
    "v_accvgpr_read_b32 v199, a47\n"
    "v_mul_f32 v198, v198, v197\n"
    "v_mul_f32 v199, v199, v197\n"
    "v_accvgpr_write_b32 a46, v198\n"
    "v_accvgpr_write_b32 a47, v199\n"
    "v_accvgpr_read_b32 v198, a60\n"
    "v_accvgpr_read_b32 v199, a61\n"
    "v_mul_f32 v198, v198, v197\n"
    "v_mul_f32 v199, v199, v197\n"
    "v_accvgpr_write_b32 a60, v198\n"
    "v_accvgpr_write_b32 a61, v199\n"
    "v_accvgpr_read_b32 v198, a62\n"
    "v_accvgpr_read_b32 v199, a63\n"
    "v_mul_f32 v198, v198, v197\n"
    "v_mul_f32 v199, v199, v197\n"
    "v_accvgpr_write_b32 a62, v198\n"
    "v_accvgpr_write_b32 a63, v199\n"
    "v_accvgpr_read_b32 v198, a76\n"
    "v_accvgpr_read_b32 v199, a77\n"
    "v_mul_f32 v198, v198, v197\n"
    "v_mul_f32 v199, v199, v197\n"
    "v_accvgpr_write_b32 a76, v198\n"
    "v_accvgpr_write_b32 a77, v199\n"
    "v_accvgpr_read_b32 v198, a78\n"
    "v_accvgpr_read_b32 v199, a79\n"
    "v_mul_f32 v198, v198, v197\n"
    "v_mul_f32 v199, v199, v197\n"
    "v_accvgpr_write_b32 a78, v198\n"
    "v_accvgpr_write_b32 a79, v199\n"
    "v_accvgpr_read_b32 v198, a92\n"
    "v_accvgpr_read_b32 v199, a93\n"
    "v_mul_f32 v198, v198, v197\n"
    "v_mul_f32 v199, v199, v197\n"
    "v_accvgpr_write_b32 a92, v198\n"
    "v_accvgpr_write_b32 a93, v199\n"
    "v_accvgpr_read_b32 v198, a94\n"
    "v_accvgpr_read_b32 v199, a95\n"
    "v_mul_f32 v198, v198, v197\n"
    "v_mul_f32 v199, v199, v197\n"
    "v_accvgpr_write_b32 a94, v198\n"
    "v_accvgpr_write_b32 a95, v199\n"
    "v_accvgpr_read_b32 v198, a108\n"
    "v_accvgpr_read_b32 v199, a109\n"
    "v_mul_f32 v198, v198, v197\n"
    "v_mul_f32 v199, v199, v197\n"
    "v_accvgpr_write_b32 a108, v198\n"
    "v_accvgpr_write_b32 a109, v199\n"
    "v_accvgpr_read_b32 v198, a110\n"
    "v_accvgpr_read_b32 v199, a111\n"
    "v_mul_f32 v198, v198, v197\n"
    "v_mul_f32 v199, v199, v197\n"
    "v_accvgpr_write_b32 a110, v198\n"
    "v_accvgpr_write_b32 a111, v199\n"
    "v_accvgpr_read_b32 v198, a124\n"
    "v_accvgpr_read_b32 v199, a125\n"
    "v_mul_f32 v198, v198, v197\n"
    "v_mul_f32 v199, v199, v197\n"
    "v_accvgpr_write_b32 a124, v198\n"
    "v_accvgpr_write_b32 a125, v199\n"
    "v_accvgpr_read_b32 v198, a126\n"
    "v_accvgpr_read_b32 v199, a127\n"
    "v_mul_f32 v198, v198, v197\n"
    "v_mul_f32 v199, v199, v197\n"
    "v_accvgpr_write_b32 a126, v198\n"
    "v_accvgpr_write_b32 a127, v199\n"
    "v_accvgpr_read_b32 v198, a140\n"
    "v_accvgpr_read_b32 v199, a141\n"
    "v_mul_f32 v198, v198, v197\n"
    "v_mul_f32 v199, v199, v197\n"
    "v_accvgpr_write_b32 a140, v198\n"
    "v_accvgpr_write_b32 a141, v199\n"
    "v_accvgpr_read_b32 v198, a142\n"
    "v_accvgpr_read_b32 v199, a143\n"
    "v_mul_f32 v198, v198, v197\n"
    "v_mul_f32 v199, v199, v197\n"
    "v_accvgpr_write_b32 a142, v198\n"
    "v_accvgpr_write_b32 a143, v199\n"
    "v_accvgpr_read_b32 v198, a156\n"
    "v_accvgpr_read_b32 v199, a157\n"
    "v_mul_f32 v198, v198, v197\n"
    "v_mul_f32 v199, v199, v197\n"
    "v_accvgpr_write_b32 a156, v198\n"
    "v_accvgpr_write_b32 a157, v199\n"
    "v_accvgpr_read_b32 v198, a158\n"
    "v_accvgpr_read_b32 v199, a159\n"
    "v_mul_f32 v198, v198, v197\n"
    "v_mul_f32 v199, v199, v197\n"
    "v_accvgpr_write_b32 a158, v198\n"
    "v_accvgpr_write_b32 a159, v199\n"
    "v_accvgpr_read_b32 v198, a172\n"
    "v_accvgpr_read_b32 v199, a173\n"
    "v_mul_f32 v198, v198, v197\n"
    "v_mul_f32 v199, v199, v197\n"
    "v_accvgpr_write_b32 a172, v198\n"
    "v_accvgpr_write_b32 a173, v199\n"
    "v_accvgpr_read_b32 v198, a174\n"
    "v_accvgpr_read_b32 v199, a175\n"
    "v_mul_f32 v198, v198, v197\n"
    "v_mul_f32 v199, v199, v197\n"
    "v_accvgpr_write_b32 a174, v198\n"
    "v_accvgpr_write_b32 a175, v199\n"
    "v_accvgpr_read_b32 v198, a188\n"
    "v_accvgpr_read_b32 v199, a189\n"
    "v_mul_f32 v198, v198, v197\n"
    "v_mul_f32 v199, v199, v197\n"
    "v_accvgpr_write_b32 a188, v198\n"
    "v_accvgpr_write_b32 a189, v199\n"
    "v_accvgpr_read_b32 v198, a190\n"
    "v_accvgpr_read_b32 v199, a191\n"
    "v_mul_f32 v198, v198, v197\n"
    "v_mul_f32 v199, v199, v197\n"
    "v_accvgpr_write_b32 a190, v198\n"
    "v_accvgpr_write_b32 a191, v199\n"
    "v_exp_f32 v32, v32\n"
    "v_exp_f32 v33, v33\n"
    "v_exp_f32 v34, v34\n"
    "v_cvt_pk_bf16_f32 v128, v32, v33\n"
    "v_exp_f32 v35, v35\n"
    "v_exp_f32 v36, v36\n"
    "v_cvt_pk_bf16_f32 v129, v34, v35\n"
    "v_exp_f32 v37, v37\n"
    "v_exp_f32 v38, v38\n"
    "v_cvt_pk_bf16_f32 v132, v36, v37\n"
    "v_exp_f32 v39, v39\n"
    "v_exp_f32 v40, v40\n"
    "v_cvt_pk_bf16_f32 v133, v38, v39\n"
    "v_exp_f32 v41, v41\n"
    "v_exp_f32 v42, v42\n"
    "v_cvt_pk_bf16_f32 v136, v40, v41\n"
    "v_exp_f32 v43, v43\n"
    "v_exp_f32 v44, v44\n"
    "v_cvt_pk_bf16_f32 v137, v42, v43\n"
    "v_exp_f32 v45, v45\n"
    "v_exp_f32 v46, v46\n"
    "v_cvt_pk_bf16_f32 v140, v44, v45\n"
    "v_exp_f32 v47, v47\n"
    "v_exp_f32 v48, v48\n"
    "v_cvt_pk_bf16_f32 v141, v46, v47\n"
    "v_exp_f32 v49, v49\n"
    "v_exp_f32 v50, v50\n"
    "v_cvt_pk_bf16_f32 v130, v48, v49\n"
    "v_exp_f32 v51, v51\n"
    "v_exp_f32 v52, v52\n"
    "v_cvt_pk_bf16_f32 v131, v50, v51\n"
    "v_exp_f32 v53, v53\n"
    "v_exp_f32 v54, v54\n"
    "v_cvt_pk_bf16_f32 v134, v52, v53\n"
    "v_exp_f32 v55, v55\n"
    "v_exp_f32 v56, v56\n"
    "v_cvt_pk_bf16_f32 v135, v54, v55\n"
    "v_exp_f32 v57, v57\n"
    "v_exp_f32 v58, v58\n"
    "v_cvt_pk_bf16_f32 v138, v56, v57\n"
    "v_exp_f32 v59, v59\n"
    "v_exp_f32 v60, v60\n"
    "v_cvt_pk_bf16_f32 v139, v58, v59\n"
    "v_exp_f32 v61, v61\n"
    "v_exp_f32 v62, v62\n"
    "v_cvt_pk_bf16_f32 v142, v60, v61\n"
    "v_exp_f32 v63, v63\n"
    "s_nop 0\n"
    "v_cvt_pk_bf16_f32 v143, v62, v63\n"
    "v_exp_f32 v64, v64\n"
    "v_exp_f32 v65, v65\n"
    "v_exp_f32 v66, v66\n"
    "v_cvt_pk_bf16_f32 v144, v64, v65\n"
    "v_exp_f32 v67, v67\n"
    "v_exp_f32 v68, v68\n"
    "v_cvt_pk_bf16_f32 v145, v66, v67\n"
    "v_exp_f32 v69, v69\n"
    "v_exp_f32 v70, v70\n"
    "v_cvt_pk_bf16_f32 v148, v68, v69\n"
    "v_exp_f32 v71, v71\n"
    "v_exp_f32 v72, v72\n"
    "v_cvt_pk_bf16_f32 v149, v70, v71\n"
    "v_exp_f32 v73, v73\n"
    "v_exp_f32 v74, v74\n"
    "v_cvt_pk_bf16_f32 v152, v72, v73\n"
    "v_exp_f32 v75, v75\n"
    "v_exp_f32 v76, v76\n"
    "v_cvt_pk_bf16_f32 v153, v74, v75\n"
    "v_exp_f32 v77, v77\n"
    "v_exp_f32 v78, v78\n"
    "v_cvt_pk_bf16_f32 v156, v76, v77\n"
    "v_exp_f32 v79, v79\n"
    "v_exp_f32 v80, v80\n"
    "v_cvt_pk_bf16_f32 v157, v78, v79\n"
    "v_exp_f32 v81, v81\n"
    "v_exp_f32 v82, v82\n"
    "v_cvt_pk_bf16_f32 v146, v80, v81\n"
    "v_exp_f32 v83, v83\n"
    "v_exp_f32 v84, v84\n"
    "v_cvt_pk_bf16_f32 v147, v82, v83\n"
    "v_exp_f32 v85, v85\n"
    "v_exp_f32 v86, v86\n"
    "v_cvt_pk_bf16_f32 v150, v84, v85\n"
    "v_exp_f32 v87, v87\n"
    "v_exp_f32 v88, v88\n"
    "v_cvt_pk_bf16_f32 v151, v86, v87\n"
    "v_exp_f32 v89, v89\n"
    "v_exp_f32 v90, v90\n"
    "v_cvt_pk_bf16_f32 v154, v88, v89\n"
    "v_exp_f32 v91, v91\n"
    "v_exp_f32 v92, v92\n"
    "v_cvt_pk_bf16_f32 v155, v90, v91\n"
    "v_exp_f32 v93, v93\n"
    "v_exp_f32 v94, v94\n"
    "v_cvt_pk_bf16_f32 v158, v92, v93\n"
    "v_exp_f32 v95, v95\n"
    "s_nop 0\n"
    "v_cvt_pk_bf16_f32 v159, v94, v95\n"
    "ds_read_b128 v[160:163], v205 offset:12288\n"
    "ds_read_b128 v[164:167], v205 offset:13312\n"
    "s_waitcnt lgkmcnt(1)\n"
    "v_mfma_f32_16x16x32_bf16 a[0:3], v[160:163], v[128:131], a[0:3]\n"
    "s_waitcnt lgkmcnt(0)\n"
    "v_mfma_f32_16x16x32_bf16 a[0:3], v[164:167], v[144:147], a[0:3]\n"
    "v_mfma_f32_16x16x32_bf16 a[4:7], v[160:163], v[132:135], a[4:7]\n"
    "v_mfma_f32_16x16x32_bf16 a[4:7], v[164:167], v[148:151], a[4:7]\n"
    "v_mfma_f32_16x16x32_bf16 a[8:11], v[160:163], v[136:139], a[8:11]\n"
    "v_mfma_f32_16x16x32_bf16 a[8:11], v[164:167], v[152:155], a[8:11]\n"
    "v_mfma_f32_16x16x32_bf16 a[12:15], v[160:163], v[140:143], a[12:15]\n"
    "v_mfma_f32_16x16x32_bf16 a[12:15], v[164:167], v[156:159], a[12:15]\n"
    "ds_read_b128 v[168:171], v205 offset:14336\n"
    "ds_read_b128 v[172:175], v205 offset:15360\n"
    "s_waitcnt lgkmcnt(1)\n"
    "v_mfma_f32_16x16x32_bf16 a[28:31], v[168:171], v[140:143], a[28:31]\n"
    "s_waitcnt lgkmcnt(0)\n"
    "v_mfma_f32_16x16x32_bf16 a[28:31], v[172:175], v[156:159], a[28:31]\n"
    "v_mfma_f32_16x16x32_bf16 a[24:27], v[172:175], v[152:155], a[24:27]\n"
    "v_mfma_f32_16x16x32_bf16 a[24:27], v[168:171], v[136:139], a[24:27]\n"
    "v_mfma_f32_16x16x32_bf16 a[20:23], v[168:171], v[132:135], a[20:23]\n"
    "v_mfma_f32_16x16x32_bf16 a[20:23], v[172:175], v[148:151], a[20:23]\n"
    "v_mfma_f32_16x16x32_bf16 a[16:19], v[172:175], v[144:147], a[16:19]\n"
    "v_mfma_f32_16x16x32_bf16 a[16:19], v[168:171], v[128:131], a[16:19]\n"
    "ds_read_b128 v[176:179], v205 offset:16384\n"
    "ds_read_b128 v[180:183], v205 offset:17408\n"
    "s_waitcnt lgkmcnt(1)\n"
    "v_mfma_f32_16x16x32_bf16 a[32:35], v[176:179], v[128:131], a[32:35]\n"
    "s_waitcnt lgkmcnt(0)\n"
    "v_mfma_f32_16x16x32_bf16 a[32:35], v[180:183], v[144:147], a[32:35]\n"
    "v_mfma_f32_16x16x32_bf16 a[36:39], v[180:183], v[148:151], a[36:39]\n"
    "v_mfma_f32_16x16x32_bf16 a[36:39], v[176:179], v[132:135], a[36:39]\n"
    "v_mfma_f32_16x16x32_bf16 a[40:43], v[176:179], v[136:139], a[40:43]\n"
    "v_mfma_f32_16x16x32_bf16 a[40:43], v[180:183], v[152:155], a[40:43]\n"
    "v_mfma_f32_16x16x32_bf16 a[44:47], v[180:183], v[156:159], a[44:47]\n"
    "v_mfma_f32_16x16x32_bf16 a[44:47], v[176:179], v[140:143], a[44:47]\n"
    "ds_read_b128 v[160:163], v205 offset:18432\n"
    "ds_read_b128 v[164:167], v205 offset:19456\n"
    "s_waitcnt lgkmcnt(1)\n"
    "v_mfma_f32_16x16x32_bf16 a[60:63], v[160:163], v[140:143], a[60:63]\n"
    "s_waitcnt lgkmcnt(0)\n"
    "v_mfma_f32_16x16x32_bf16 a[60:63], v[164:167], v[156:159], a[60:63]\n"
    "v_mfma_f32_16x16x32_bf16 a[56:59], v[164:167], v[152:155], a[56:59]\n"
    "v_mfma_f32_16x16x32_bf16 a[56:59], v[160:163], v[136:139], a[56:59]\n"
    "v_mfma_f32_16x16x32_bf16 a[52:55], v[160:163], v[132:135], a[52:55]\n"
    "v_mfma_f32_16x16x32_bf16 a[52:55], v[164:167], v[148:151], a[52:55]\n"
    "v_mfma_f32_16x16x32_bf16 a[48:51], v[164:167], v[144:147], a[48:51]\n"
    "v_mfma_f32_16x16x32_bf16 a[48:51], v[160:163], v[128:131], a[48:51]\n"
    "ds_read_b128 v[168:171], v205 offset:20480\n"
    "ds_read_b128 v[172:175], v205 offset:21504\n"
    "s_waitcnt lgkmcnt(1)\n"
    "v_mfma_f32_16x16x32_bf16 a[64:67], v[168:171], v[128:131], a[64:67]\n"
    "s_waitcnt lgkmcnt(0)\n"
    "v_mfma_f32_16x16x32_bf16 a[64:67], v[172:175], v[144:147], a[64:67]\n"
    "v_mfma_f32_16x16x32_bf16 a[68:71], v[172:175], v[148:151], a[68:71]\n"
    "v_mfma_f32_16x16x32_bf16 a[68:71], v[168:171], v[132:135], a[68:71]\n"
    "v_mfma_f32_16x16x32_bf16 a[72:75], v[168:171], v[136:139], a[72:75]\n"
    "v_mfma_f32_16x16x32_bf16 a[72:75], v[172:175], v[152:155], a[72:75]\n"
    "v_mfma_f32_16x16x32_bf16 a[76:79], v[172:175], v[156:159], a[76:79]\n"
    "v_mfma_f32_16x16x32_bf16 a[76:79], v[168:171], v[140:143], a[76:79]\n"
    "ds_read_b128 v[176:179], v205 offset:22528\n"
    "ds_read_b128 v[180:183], v205 offset:23552\n"
    "s_waitcnt lgkmcnt(1)\n"
    "v_mfma_f32_16x16x32_bf16 a[92:95], v[176:179], v[140:143], a[92:95]\n"
    "s_waitcnt lgkmcnt(0)\n"
    "v_mfma_f32_16x16x32_bf16 a[92:95], v[180:183], v[156:159], a[92:95]\n"
    "v_mfma_f32_16x16x32_bf16 a[88:91], v[180:183], v[152:155], a[88:91]\n"
    "v_mfma_f32_16x16x32_bf16 a[88:91], v[176:179], v[136:139], a[88:91]\n"
    "v_mfma_f32_16x16x32_bf16 a[84:87], v[176:179], v[132:135], a[84:87]\n"
    "v_mfma_f32_16x16x32_bf16 a[84:87], v[180:183], v[148:151], a[84:87]\n"
    "v_mfma_f32_16x16x32_bf16 a[80:83], v[180:183], v[144:147], a[80:83]\n"
    "v_mfma_f32_16x16x32_bf16 a[80:83], v[176:179], v[128:131], a[80:83]\n"
    "ds_read_b128 v[160:163], v205 offset:24576\n"
    "ds_read_b128 v[164:167], v205 offset:25600\n"
    "s_waitcnt lgkmcnt(1)\n"
    "v_mfma_f32_16x16x32_bf16 a[96:99], v[160:163], v[128:131], a[96:99]\n"
    "s_waitcnt lgkmcnt(0)\n"
    "v_mfma_f32_16x16x32_bf16 a[96:99], v[164:167], v[144:147], a[96:99]\n"
    "v_mfma_f32_16x16x32_bf16 a[100:103], v[164:167], v[148:151], a[100:103]\n"
    "v_mfma_f32_16x16x32_bf16 a[100:103], v[160:163], v[132:135], a[100:103]\n"
    "v_mfma_f32_16x16x32_bf16 a[104:107], v[160:163], v[136:139], a[104:107]\n"
    "v_mfma_f32_16x16x32_bf16 a[104:107], v[164:167], v[152:155], a[104:107]\n"
    "v_mfma_f32_16x16x32_bf16 a[108:111], v[164:167], v[156:159], a[108:111]\n"
    "v_mfma_f32_16x16x32_bf16 a[108:111], v[160:163], v[140:143], a[108:111]\n"
    "ds_read_b128 v[168:171], v205 offset:26624\n"
    "ds_read_b128 v[172:175], v205 offset:27648\n"
    "s_waitcnt lgkmcnt(1)\n"
    "v_mfma_f32_16x16x32_bf16 a[124:127], v[168:171], v[140:143], a[124:127]\n"
    "s_waitcnt lgkmcnt(0)\n"
    "v_mfma_f32_16x16x32_bf16 a[124:127], v[172:175], v[156:159], a[124:127]\n"
    "v_mfma_f32_16x16x32_bf16 a[120:123], v[172:175], v[152:155], a[120:123]\n"
    "v_mfma_f32_16x16x32_bf16 a[120:123], v[168:171], v[136:139], a[120:123]\n"
    "v_mfma_f32_16x16x32_bf16 a[116:119], v[168:171], v[132:135], a[116:119]\n"
    "v_mfma_f32_16x16x32_bf16 a[116:119], v[172:175], v[148:151], a[116:119]\n"
    "v_mfma_f32_16x16x32_bf16 a[112:115], v[172:175], v[144:147], a[112:115]\n"
    "v_mfma_f32_16x16x32_bf16 a[112:115], v[168:171], v[128:131], a[112:115]\n"
    "ds_read_b128 v[176:179], v205 offset:28672\n"
    "ds_read_b128 v[180:183], v205 offset:29696\n"
    "s_waitcnt lgkmcnt(1)\n"
    "v_mfma_f32_16x16x32_bf16 a[128:131], v[176:179], v[128:131], a[128:131]\n"
    "s_waitcnt lgkmcnt(0)\n"
    "v_mfma_f32_16x16x32_bf16 a[128:131], v[180:183], v[144:147], a[128:131]\n"
    "v_mfma_f32_16x16x32_bf16 a[132:135], v[180:183], v[148:151], a[132:135]\n"
    "v_mfma_f32_16x16x32_bf16 a[132:135], v[176:179], v[132:135], a[132:135]\n"
    "v_mfma_f32_16x16x32_bf16 a[136:139], v[176:179], v[136:139], a[136:139]\n"
    "v_mfma_f32_16x16x32_bf16 a[136:139], v[180:183], v[152:155], a[136:139]\n"
    "v_mfma_f32_16x16x32_bf16 a[140:143], v[180:183], v[156:159], a[140:143]\n"
    "v_mfma_f32_16x16x32_bf16 a[140:143], v[176:179], v[140:143], a[140:143]\n"
    "ds_read_b128 v[160:163], v205 offset:30720\n"
    "ds_read_b128 v[164:167], v205 offset:31744\n"
    "s_waitcnt lgkmcnt(1)\n"
    "v_mfma_f32_16x16x32_bf16 a[156:159], v[160:163], v[140:143], a[156:159]\n"
    "s_waitcnt lgkmcnt(0)\n"
    "v_mfma_f32_16x16x32_bf16 a[156:159], v[164:167], v[156:159], a[156:159]\n"
    "v_mfma_f32_16x16x32_bf16 a[152:155], v[164:167], v[152:155], a[152:155]\n"
    "v_mfma_f32_16x16x32_bf16 a[152:155], v[160:163], v[136:139], a[152:155]\n"
    "v_mfma_f32_16x16x32_bf16 a[148:151], v[160:163], v[132:135], a[148:151]\n"
    "v_mfma_f32_16x16x32_bf16 a[148:151], v[164:167], v[148:151], a[148:151]\n"
    "v_mfma_f32_16x16x32_bf16 a[144:147], v[164:167], v[144:147], a[144:147]\n"
    "v_mfma_f32_16x16x32_bf16 a[144:147], v[160:163], v[128:131], a[144:147]\n"
    "ds_read_b128 v[168:171], v205 offset:32768\n"
    "ds_read_b128 v[172:175], v205 offset:33792\n"
    "s_waitcnt lgkmcnt(1)\n"
    "v_mfma_f32_16x16x32_bf16 a[160:163], v[168:171], v[128:131], a[160:163]\n"
    "s_waitcnt lgkmcnt(0)\n"
    "v_mfma_f32_16x16x32_bf16 a[160:163], v[172:175], v[144:147], a[160:163]\n"
    "v_mfma_f32_16x16x32_bf16 a[164:167], v[172:175], v[148:151], a[164:167]\n"
    "v_mfma_f32_16x16x32_bf16 a[164:167], v[168:171], v[132:135], a[164:167]\n"
    "v_mfma_f32_16x16x32_bf16 a[168:171], v[168:171], v[136:139], a[168:171]\n"
    "v_mfma_f32_16x16x32_bf16 a[168:171], v[172:175], v[152:155], a[168:171]\n"
    "v_mfma_f32_16x16x32_bf16 a[172:175], v[172:175], v[156:159], a[172:175]\n"
    "v_mfma_f32_16x16x32_bf16 a[172:175], v[168:171], v[140:143], a[172:175]\n"
    "ds_read_b128 v[176:179], v205 offset:34816\n"
    "ds_read_b128 v[180:183], v205 offset:35840\n"
    "s_waitcnt lgkmcnt(1)\n"
    "v_mfma_f32_16x16x32_bf16 a[188:191], v[176:179], v[140:143], a[188:191]\n"
    "s_waitcnt lgkmcnt(0)\n"
    "v_mfma_f32_16x16x32_bf16 a[188:191], v[180:183], v[156:159], a[188:191]\n"
    "v_mfma_f32_16x16x32_bf16 a[184:187], v[176:179], v[136:139], a[184:187]\n"
    "v_mfma_f32_16x16x32_bf16 a[184:187], v[180:183], v[152:155], a[184:187]\n"
    "v_mfma_f32_16x16x32_bf16 a[180:183], v[176:179], v[132:135], a[180:183]\n"
    "v_mfma_f32_16x16x32_bf16 a[180:183], v[180:183], v[148:151], a[180:183]\n"
    "v_mfma_f32_16x16x32_bf16 a[176:179], v[176:179], v[128:131], a[176:179]\n"
    "v_mfma_f32_16x16x32_bf16 a[176:179], v[180:183], v[144:147], a[176:179]\n"
    "v_mfma_f32_16x16x32_bf16 v[112:115], v[192:195], v[128:131], v[112:115]\n"
    "v_mfma_f32_16x16x32_bf16 v[112:115], v[192:195], v[144:147], v[112:115]\n"
    "v_mfma_f32_16x16x32_bf16 v[116:119], v[192:195], v[132:135], v[116:119]\n"
    "v_mfma_f32_16x16x32_bf16 v[116:119], v[192:195], v[148:151], v[116:119]\n"
    "v_mfma_f32_16x16x32_bf16 v[120:123], v[192:195], v[136:139], v[120:123]\n"
    "v_mfma_f32_16x16x32_bf16 v[120:123], v[192:195], v[152:155], v[120:123]\n"
    "v_mfma_f32_16x16x32_bf16 v[124:127], v[192:195], v[140:143], v[124:127]\n"
    "v_mfma_f32_16x16x32_bf16 v[124:127], v[192:195], v[156:159], v[124:127]\n"
    "s_nop 15\n"
    "s_add_u32 s63, s63, 0x6000\n"
    "s_cmp_lt_u32 s63, 0xc0000\n"
    "s_cbranch_scc1 LSLOOP\n"
    "s_branch LEPI\n"
    "LEPI:\n"
    "s_lshl_b32 s76, s56, 3\n"
    "s_add_u32 s76, s76, s57\n"
    "s_mul_i32 s76, s76, 0x18000\n"
    "s_add_u32 s76, s76, s88\n"
    "s_add_u32 s28, s24, s76\n"
    "s_addc_u32 s29, s25, 0\n"
    "s_and_b32 s29, s29, 0xffff\n"
    "s_mov_b32 s30, 0x6000\n"
    "s_mov_b32 s31, 0x20000\n"
    "s_xor_b32 s94, s70, 0x80000000\n"
    "s_xor_b32 s95, s70, 0x80000000\n"
    "s_mov_b32 s62, 0x1800\n"
    "s_mov_b32 s63, 0x3000\n"
    "s_mov_b32 s64, 0x4800\n"
    "v_and_b32 v198, 15, v231\n"
    "v_mul_u32_u24 v205, 0x180, v198\n"
    "v_lshrrev_b32 v199, 4, v231\n"
    "v_lshl_add_u32 v205, v199, 4, v205\n"
    "v_mul_f32 v222, s68, v112\n"
    "v_rcp_f32 v222, v222\n"
    "v_mul_f32 v224, s68, v116\n"
    "v_rcp_f32 v224, v224\n"
    "v_mul_f32 v226, s68, v120\n"
    "v_rcp_f32 v226, v226\n"
    "v_mul_f32 v228, s68, v124\n"
    "v_rcp_f32 v228, v228\n"
    "v_mov_b32 v223, v222\n"
    "v_mov_b32 v225, v224\n"
    "v_mov_b32 v227, v226\n"
    "v_mov_b32 v229, v228\n"
    "v_accvgpr_read_b32 v96, a0\n"
    "v_accvgpr_read_b32 v97, a1\n"
    "v_accvgpr_read_b32 v98, a2\n"
    "v_accvgpr_read_b32 v99, a3\n"
    "v_pk_fma_f32 v[96:97], v[96:97], v[222:223], s[94:95]\n"
    "v_pk_fma_f32 v[98:99], v[98:99], v[222:223], s[94:95]\n"
    "v_cvt_pk_bf16_f32 v32, v96, v97\n"
    "v_cvt_pk_bf16_f32 v33, v98, v99\n"
    "v_accvgpr_read_b32 v100, a16\n"
    "v_accvgpr_read_b32 v101, a17\n"
    "v_accvgpr_read_b32 v102, a18\n"
    "v_accvgpr_read_b32 v103, a19\n"
    "v_pk_fma_f32 v[100:101], v[100:101], v[222:223], s[94:95]\n"
    "v_pk_fma_f32 v[102:103], v[102:103], v[222:223], s[94:95]\n"
    "v_cvt_pk_bf16_f32 v34, v100, v101\n"
    "v_cvt_pk_bf16_f32 v35, v102, v103\n"
    "buffer_store_dwordx4 v[32:35], v205, s[28:31], 0 offen sc0 sc1\n"
    "v_accvgpr_read_b32 v104, a32\n"
    "v_accvgpr_read_b32 v105, a33\n"
    "v_accvgpr_read_b32 v106, a34\n"
    "v_accvgpr_read_b32 v107, a35\n"
    "v_pk_fma_f32 v[104:105], v[104:105], v[222:223], s[94:95]\n"
    "v_pk_fma_f32 v[106:107], v[106:107], v[222:223], s[94:95]\n"
    "v_cvt_pk_bf16_f32 v36, v104, v105\n"
    "v_cvt_pk_bf16_f32 v37, v106, v107\n"
    "v_accvgpr_read_b32 v108, a48\n"
    "v_accvgpr_read_b32 v109, a49\n"
    "v_accvgpr_read_b32 v110, a50\n"
    "v_accvgpr_read_b32 v111, a51\n"
    "v_pk_fma_f32 v[108:109], v[108:109], v[222:223], s[94:95]\n"
    "v_pk_fma_f32 v[110:111], v[110:111], v[222:223], s[94:95]\n"
    "v_cvt_pk_bf16_f32 v38, v108, v109\n"
    "v_cvt_pk_bf16_f32 v39, v110, v111\n"
    "buffer_store_dwordx4 v[36:39], v205, s[28:31], 0 offen offset:64 sc0 sc1\n"
    "v_accvgpr_read_b32 v96, a64\n"
    "v_accvgpr_read_b32 v97, a65\n"
    "v_accvgpr_read_b32 v98, a66\n"
    "v_accvgpr_read_b32 v99, a67\n"
    "v_pk_fma_f32 v[96:97], v[96:97], v[222:223], s[94:95]\n"
    "v_pk_fma_f32 v[98:99], v[98:99], v[222:223], s[94:95]\n"
    "v_cvt_pk_bf16_f32 v40, v96, v97\n"
    "v_cvt_pk_bf16_f32 v41, v98, v99\n"
    "v_accvgpr_read_b32 v100, a80\n"
    "v_accvgpr_read_b32 v101, a81\n"
    "v_accvgpr_read_b32 v102, a82\n"
    "v_accvgpr_read_b32 v103, a83\n"
    "v_pk_fma_f32 v[100:101], v[100:101], v[222:223], s[94:95]\n"
    "v_pk_fma_f32 v[102:103], v[102:103], v[222:223], s[94:95]\n"
    "v_cvt_pk_bf16_f32 v42, v100, v101\n"
    "v_cvt_pk_bf16_f32 v43, v102, v103\n"
    "buffer_store_dwordx4 v[40:43], v205, s[28:31], 0 offen offset:128 sc0 sc1\n"
    "v_accvgpr_read_b32 v104, a96\n"
    "v_accvgpr_read_b32 v105, a97\n"
    "v_accvgpr_read_b32 v106, a98\n"
    "v_accvgpr_read_b32 v107, a99\n"
    "v_pk_fma_f32 v[104:105], v[104:105], v[222:223], s[94:95]\n"
    "v_pk_fma_f32 v[106:107], v[106:107], v[222:223], s[94:95]\n"
    "v_cvt_pk_bf16_f32 v44, v104, v105\n"
    "v_cvt_pk_bf16_f32 v45, v106, v107\n"
    "v_accvgpr_read_b32 v108, a112\n"
    "v_accvgpr_read_b32 v109, a113\n"
    "v_accvgpr_read_b32 v110, a114\n"
    "v_accvgpr_read_b32 v111, a115\n"
    "v_pk_fma_f32 v[108:109], v[108:109], v[222:223], s[94:95]\n"
    "v_pk_fma_f32 v[110:111], v[110:111], v[222:223], s[94:95]\n"
    "v_cvt_pk_bf16_f32 v46, v108, v109\n"
    "v_cvt_pk_bf16_f32 v47, v110, v111\n"
    "buffer_store_dwordx4 v[44:47], v205, s[28:31], 0 offen offset:192 sc0 sc1\n"
    "v_accvgpr_read_b32 v96, a128\n"
    "v_accvgpr_read_b32 v97, a129\n"
    "v_accvgpr_read_b32 v98, a130\n"
    "v_accvgpr_read_b32 v99, a131\n"
    "v_pk_fma_f32 v[96:97], v[96:97], v[222:223], s[94:95]\n"
    "v_pk_fma_f32 v[98:99], v[98:99], v[222:223], s[94:95]\n"
    "v_cvt_pk_bf16_f32 v48, v96, v97\n"
    "v_cvt_pk_bf16_f32 v49, v98, v99\n"
    "v_accvgpr_read_b32 v100, a144\n"
    "v_accvgpr_read_b32 v101, a145\n"
    "v_accvgpr_read_b32 v102, a146\n"
    "v_accvgpr_read_b32 v103, a147\n"
    "v_pk_fma_f32 v[100:101], v[100:101], v[222:223], s[94:95]\n"
    "v_pk_fma_f32 v[102:103], v[102:103], v[222:223], s[94:95]\n"
    "v_cvt_pk_bf16_f32 v50, v100, v101\n"
    "v_cvt_pk_bf16_f32 v51, v102, v103\n"
    "buffer_store_dwordx4 v[48:51], v205, s[28:31], 0 offen offset:256 sc0 sc1\n"
    "v_accvgpr_read_b32 v104, a160\n"
    "v_accvgpr_read_b32 v105, a161\n"
    "v_accvgpr_read_b32 v106, a162\n"
    "v_accvgpr_read_b32 v107, a163\n"
    "v_pk_fma_f32 v[104:105], v[104:105], v[222:223], s[94:95]\n"
    "v_pk_fma_f32 v[106:107], v[106:107], v[222:223], s[94:95]\n"
    "v_cvt_pk_bf16_f32 v52, v104, v105\n"
    "v_cvt_pk_bf16_f32 v53, v106, v107\n"
    "v_accvgpr_read_b32 v108, a176\n"
    "v_accvgpr_read_b32 v109, a177\n"
    "v_accvgpr_read_b32 v110, a178\n"
    "v_accvgpr_read_b32 v111, a179\n"
    "v_pk_fma_f32 v[108:109], v[108:109], v[222:223], s[94:95]\n"
    "v_pk_fma_f32 v[110:111], v[110:111], v[222:223], s[94:95]\n"
    "v_cvt_pk_bf16_f32 v54, v108, v109\n"
    "v_cvt_pk_bf16_f32 v55, v110, v111\n"
    "buffer_store_dwordx4 v[52:55], v205, s[28:31], 0 offen offset:320 sc0 sc1\n"
    "v_accvgpr_read_b32 v96, a4\n"
    "v_accvgpr_read_b32 v97, a5\n"
    "v_accvgpr_read_b32 v98, a6\n"
    "v_accvgpr_read_b32 v99, a7\n"
    "v_pk_fma_f32 v[96:97], v[96:97], v[224:225], s[94:95]\n"
    "v_pk_fma_f32 v[98:99], v[98:99], v[224:225], s[94:95]\n"
    "v_cvt_pk_bf16_f32 v56, v96, v97\n"
    "v_cvt_pk_bf16_f32 v57, v98, v99\n"
    "v_accvgpr_read_b32 v100, a20\n"
    "v_accvgpr_read_b32 v101, a21\n"
    "v_accvgpr_read_b32 v102, a22\n"
    "v_accvgpr_read_b32 v103, a23\n"
    "v_pk_fma_f32 v[100:101], v[100:101], v[224:225], s[94:95]\n"
    "v_pk_fma_f32 v[102:103], v[102:103], v[224:225], s[94:95]\n"
    "v_cvt_pk_bf16_f32 v58, v100, v101\n"
    "v_cvt_pk_bf16_f32 v59, v102, v103\n"
    "buffer_store_dwordx4 v[56:59], v205, s[28:31], s62 offen sc0 sc1\n"
    "v_accvgpr_read_b32 v104, a36\n"
    "v_accvgpr_read_b32 v105, a37\n"
    "v_accvgpr_read_b32 v106, a38\n"
    "v_accvgpr_read_b32 v107, a39\n"
    "v_pk_fma_f32 v[104:105], v[104:105], v[224:225], s[94:95]\n"
    "v_pk_fma_f32 v[106:107], v[106:107], v[224:225], s[94:95]\n"
    "v_cvt_pk_bf16_f32 v60, v104, v105\n"
    "v_cvt_pk_bf16_f32 v61, v106, v107\n"
    "v_accvgpr_read_b32 v108, a52\n"
    "v_accvgpr_read_b32 v109, a53\n"
    "v_accvgpr_read_b32 v110, a54\n"
    "v_accvgpr_read_b32 v111, a55\n"
    "v_pk_fma_f32 v[108:109], v[108:109], v[224:225], s[94:95]\n"
    "v_pk_fma_f32 v[110:111], v[110:111], v[224:225], s[94:95]\n"
    "v_cvt_pk_bf16_f32 v62, v108, v109\n"
    "v_cvt_pk_bf16_f32 v63, v110, v111\n"
    "buffer_store_dwordx4 v[60:63], v205, s[28:31], s62 offen offset:64 sc0 sc1\n"
    "v_accvgpr_read_b32 v96, a68\n"
    "v_accvgpr_read_b32 v97, a69\n"
    "v_accvgpr_read_b32 v98, a70\n"
    "v_accvgpr_read_b32 v99, a71\n"
    "v_pk_fma_f32 v[96:97], v[96:97], v[224:225], s[94:95]\n"
    "v_pk_fma_f32 v[98:99], v[98:99], v[224:225], s[94:95]\n"
    "v_cvt_pk_bf16_f32 v32, v96, v97\n"
    "v_cvt_pk_bf16_f32 v33, v98, v99\n"
    "v_accvgpr_read_b32 v100, a84\n"
    "v_accvgpr_read_b32 v101, a85\n"
    "v_accvgpr_read_b32 v102, a86\n"
    "v_accvgpr_read_b32 v103, a87\n"
    "v_pk_fma_f32 v[100:101], v[100:101], v[224:225], s[94:95]\n"
    "v_pk_fma_f32 v[102:103], v[102:103], v[224:225], s[94:95]\n"
    "v_cvt_pk_bf16_f32 v34, v100, v101\n"
    "v_cvt_pk_bf16_f32 v35, v102, v103\n"
    "buffer_store_dwordx4 v[32:35], v205, s[28:31], s62 offen offset:128 sc0 sc1\n"
    "v_accvgpr_read_b32 v104, a100\n"
    "v_accvgpr_read_b32 v105, a101\n"
    "v_accvgpr_read_b32 v106, a102\n"
    "v_accvgpr_read_b32 v107, a103\n"
    "v_pk_fma_f32 v[104:105], v[104:105], v[224:225], s[94:95]\n"
    "v_pk_fma_f32 v[106:107], v[106:107], v[224:225], s[94:95]\n"
    "v_cvt_pk_bf16_f32 v36, v104, v105\n"
    "v_cvt_pk_bf16_f32 v37, v106, v107\n"
    "v_accvgpr_read_b32 v108, a116\n"
    "v_accvgpr_read_b32 v109, a117\n"
    "v_accvgpr_read_b32 v110, a118\n"
    "v_accvgpr_read_b32 v111, a119\n"
    "v_pk_fma_f32 v[108:109], v[108:109], v[224:225], s[94:95]\n"
    "v_pk_fma_f32 v[110:111], v[110:111], v[224:225], s[94:95]\n"
    "v_cvt_pk_bf16_f32 v38, v108, v109\n"
    "v_cvt_pk_bf16_f32 v39, v110, v111\n"
    "buffer_store_dwordx4 v[36:39], v205, s[28:31], s62 offen offset:192 sc0 sc1\n"
    "v_accvgpr_read_b32 v96, a132\n"
    "v_accvgpr_read_b32 v97, a133\n"
    "v_accvgpr_read_b32 v98, a134\n"
    "v_accvgpr_read_b32 v99, a135\n"
    "v_pk_fma_f32 v[96:97], v[96:97], v[224:225], s[94:95]\n"
    "v_pk_fma_f32 v[98:99], v[98:99], v[224:225], s[94:95]\n"
    "v_cvt_pk_bf16_f32 v40, v96, v97\n"
    "v_cvt_pk_bf16_f32 v41, v98, v99\n"
    "v_accvgpr_read_b32 v100, a148\n"
    "v_accvgpr_read_b32 v101, a149\n"
    "v_accvgpr_read_b32 v102, a150\n"
    "v_accvgpr_read_b32 v103, a151\n"
    "v_pk_fma_f32 v[100:101], v[100:101], v[224:225], s[94:95]\n"
    "v_pk_fma_f32 v[102:103], v[102:103], v[224:225], s[94:95]\n"
    "v_cvt_pk_bf16_f32 v42, v100, v101\n"
    "v_cvt_pk_bf16_f32 v43, v102, v103\n"
    "buffer_store_dwordx4 v[40:43], v205, s[28:31], s62 offen offset:256 sc0 sc1\n"
    "v_accvgpr_read_b32 v104, a164\n"
    "v_accvgpr_read_b32 v105, a165\n"
    "v_accvgpr_read_b32 v106, a166\n"
    "v_accvgpr_read_b32 v107, a167\n"
    "v_pk_fma_f32 v[104:105], v[104:105], v[224:225], s[94:95]\n"
    "v_pk_fma_f32 v[106:107], v[106:107], v[224:225], s[94:95]\n"
    "v_cvt_pk_bf16_f32 v44, v104, v105\n"
    "v_cvt_pk_bf16_f32 v45, v106, v107\n"
    "v_accvgpr_read_b32 v108, a180\n"
    "v_accvgpr_read_b32 v109, a181\n"
    "v_accvgpr_read_b32 v110, a182\n"
    "v_accvgpr_read_b32 v111, a183\n"
    "v_pk_fma_f32 v[108:109], v[108:109], v[224:225], s[94:95]\n"
    "v_pk_fma_f32 v[110:111], v[110:111], v[224:225], s[94:95]\n"
    "v_cvt_pk_bf16_f32 v46, v108, v109\n"
    "v_cvt_pk_bf16_f32 v47, v110, v111\n"
    "buffer_store_dwordx4 v[44:47], v205, s[28:31], s62 offen offset:320 sc0 sc1\n"
    "v_accvgpr_read_b32 v96, a8\n"
    "v_accvgpr_read_b32 v97, a9\n"
    "v_accvgpr_read_b32 v98, a10\n"
    "v_accvgpr_read_b32 v99, a11\n"
    "v_pk_fma_f32 v[96:97], v[96:97], v[226:227], s[94:95]\n"
    "v_pk_fma_f32 v[98:99], v[98:99], v[226:227], s[94:95]\n"
    "v_cvt_pk_bf16_f32 v48, v96, v97\n"
    "v_cvt_pk_bf16_f32 v49, v98, v99\n"
    "v_accvgpr_read_b32 v100, a24\n"
    "v_accvgpr_read_b32 v101, a25\n"
    "v_accvgpr_read_b32 v102, a26\n"
    "v_accvgpr_read_b32 v103, a27\n"
    "v_pk_fma_f32 v[100:101], v[100:101], v[226:227], s[94:95]\n"
    "v_pk_fma_f32 v[102:103], v[102:103], v[226:227], s[94:95]\n"
    "v_cvt_pk_bf16_f32 v50, v100, v101\n"
    "v_cvt_pk_bf16_f32 v51, v102, v103\n"
    "buffer_store_dwordx4 v[48:51], v205, s[28:31], s63 offen sc0 sc1\n"
    "v_accvgpr_read_b32 v104, a40\n"
    "v_accvgpr_read_b32 v105, a41\n"
    "v_accvgpr_read_b32 v106, a42\n"
    "v_accvgpr_read_b32 v107, a43\n"
    "v_pk_fma_f32 v[104:105], v[104:105], v[226:227], s[94:95]\n"
    "v_pk_fma_f32 v[106:107], v[106:107], v[226:227], s[94:95]\n"
    "v_cvt_pk_bf16_f32 v52, v104, v105\n"
    "v_cvt_pk_bf16_f32 v53, v106, v107\n"
    "v_accvgpr_read_b32 v108, a56\n"
    "v_accvgpr_read_b32 v109, a57\n"
    "v_accvgpr_read_b32 v110, a58\n"
    "v_accvgpr_read_b32 v111, a59\n"
    "v_pk_fma_f32 v[108:109], v[108:109], v[226:227], s[94:95]\n"
    "v_pk_fma_f32 v[110:111], v[110:111], v[226:227], s[94:95]\n"
    "v_cvt_pk_bf16_f32 v54, v108, v109\n"
    "v_cvt_pk_bf16_f32 v55, v110, v111\n"
    "buffer_store_dwordx4 v[52:55], v205, s[28:31], s63 offen offset:64 sc0 sc1\n"
    "v_accvgpr_read_b32 v96, a72\n"
    "v_accvgpr_read_b32 v97, a73\n"
    "v_accvgpr_read_b32 v98, a74\n"
    "v_accvgpr_read_b32 v99, a75\n"
    "v_pk_fma_f32 v[96:97], v[96:97], v[226:227], s[94:95]\n"
    "v_pk_fma_f32 v[98:99], v[98:99], v[226:227], s[94:95]\n"
    "v_cvt_pk_bf16_f32 v56, v96, v97\n"
    "v_cvt_pk_bf16_f32 v57, v98, v99\n"
    "v_accvgpr_read_b32 v100, a88\n"
    "v_accvgpr_read_b32 v101, a89\n"
    "v_accvgpr_read_b32 v102, a90\n"
    "v_accvgpr_read_b32 v103, a91\n"
    "v_pk_fma_f32 v[100:101], v[100:101], v[226:227], s[94:95]\n"
    "v_pk_fma_f32 v[102:103], v[102:103], v[226:227], s[94:95]\n"
    "v_cvt_pk_bf16_f32 v58, v100, v101\n"
    "v_cvt_pk_bf16_f32 v59, v102, v103\n"
    "buffer_store_dwordx4 v[56:59], v205, s[28:31], s63 offen offset:128 sc0 sc1\n"
    "v_accvgpr_read_b32 v104, a104\n"
    "v_accvgpr_read_b32 v105, a105\n"
    "v_accvgpr_read_b32 v106, a106\n"
    "v_accvgpr_read_b32 v107, a107\n"
    "v_pk_fma_f32 v[104:105], v[104:105], v[226:227], s[94:95]\n"
    "v_pk_fma_f32 v[106:107], v[106:107], v[226:227], s[94:95]\n"
    "v_cvt_pk_bf16_f32 v60, v104, v105\n"
    "v_cvt_pk_bf16_f32 v61, v106, v107\n"
    "v_accvgpr_read_b32 v108, a120\n"
    "v_accvgpr_read_b32 v109, a121\n"
    "v_accvgpr_read_b32 v110, a122\n"
    "v_accvgpr_read_b32 v111, a123\n"
    "v_pk_fma_f32 v[108:109], v[108:109], v[226:227], s[94:95]\n"
    "v_pk_fma_f32 v[110:111], v[110:111], v[226:227], s[94:95]\n"
    "v_cvt_pk_bf16_f32 v62, v108, v109\n"
    "v_cvt_pk_bf16_f32 v63, v110, v111\n"
    "buffer_store_dwordx4 v[60:63], v205, s[28:31], s63 offen offset:192 sc0 sc1\n"
    "v_accvgpr_read_b32 v96, a136\n"
    "v_accvgpr_read_b32 v97, a137\n"
    "v_accvgpr_read_b32 v98, a138\n"
    "v_accvgpr_read_b32 v99, a139\n"
    "v_pk_fma_f32 v[96:97], v[96:97], v[226:227], s[94:95]\n"
    "v_pk_fma_f32 v[98:99], v[98:99], v[226:227], s[94:95]\n"
    "v_cvt_pk_bf16_f32 v32, v96, v97\n"
    "v_cvt_pk_bf16_f32 v33, v98, v99\n"
    "v_accvgpr_read_b32 v100, a152\n"
    "v_accvgpr_read_b32 v101, a153\n"
    "v_accvgpr_read_b32 v102, a154\n"
    "v_accvgpr_read_b32 v103, a155\n"
    "v_pk_fma_f32 v[100:101], v[100:101], v[226:227], s[94:95]\n"
    "v_pk_fma_f32 v[102:103], v[102:103], v[226:227], s[94:95]\n"
    "v_cvt_pk_bf16_f32 v34, v100, v101\n"
    "v_cvt_pk_bf16_f32 v35, v102, v103\n"
    "buffer_store_dwordx4 v[32:35], v205, s[28:31], s63 offen offset:256 sc0 sc1\n"
    "v_accvgpr_read_b32 v104, a168\n"
    "v_accvgpr_read_b32 v105, a169\n"
    "v_accvgpr_read_b32 v106, a170\n"
    "v_accvgpr_read_b32 v107, a171\n"
    "v_pk_fma_f32 v[104:105], v[104:105], v[226:227], s[94:95]\n"
    "v_pk_fma_f32 v[106:107], v[106:107], v[226:227], s[94:95]\n"
    "v_cvt_pk_bf16_f32 v36, v104, v105\n"
    "v_cvt_pk_bf16_f32 v37, v106, v107\n"
    "v_accvgpr_read_b32 v108, a184\n"
    "v_accvgpr_read_b32 v109, a185\n"
    "v_accvgpr_read_b32 v110, a186\n"
    "v_accvgpr_read_b32 v111, a187\n"
    "v_pk_fma_f32 v[108:109], v[108:109], v[226:227], s[94:95]\n"
    "v_pk_fma_f32 v[110:111], v[110:111], v[226:227], s[94:95]\n"
    "v_cvt_pk_bf16_f32 v38, v108, v109\n"
    "v_cvt_pk_bf16_f32 v39, v110, v111\n"
    "buffer_store_dwordx4 v[36:39], v205, s[28:31], s63 offen offset:320 sc0 sc1\n"
    "v_accvgpr_read_b32 v96, a12\n"
    "v_accvgpr_read_b32 v97, a13\n"
    "v_accvgpr_read_b32 v98, a14\n"
    "v_accvgpr_read_b32 v99, a15\n"
    "v_pk_fma_f32 v[96:97], v[96:97], v[228:229], s[94:95]\n"
    "v_pk_fma_f32 v[98:99], v[98:99], v[228:229], s[94:95]\n"
    "v_cvt_pk_bf16_f32 v40, v96, v97\n"
    "v_cvt_pk_bf16_f32 v41, v98, v99\n"
    "v_accvgpr_read_b32 v100, a28\n"
    "v_accvgpr_read_b32 v101, a29\n"
    "v_accvgpr_read_b32 v102, a30\n"
    "v_accvgpr_read_b32 v103, a31\n"
    "v_pk_fma_f32 v[100:101], v[100:101], v[228:229], s[94:95]\n"
    "v_pk_fma_f32 v[102:103], v[102:103], v[228:229], s[94:95]\n"
    "v_cvt_pk_bf16_f32 v42, v100, v101\n"
    "v_cvt_pk_bf16_f32 v43, v102, v103\n"
    "buffer_store_dwordx4 v[40:43], v205, s[28:31], s64 offen sc0 sc1\n"
    "v_accvgpr_read_b32 v104, a44\n"
    "v_accvgpr_read_b32 v105, a45\n"
    "v_accvgpr_read_b32 v106, a46\n"
    "v_accvgpr_read_b32 v107, a47\n"
    "v_pk_fma_f32 v[104:105], v[104:105], v[228:229], s[94:95]\n"
    "v_pk_fma_f32 v[106:107], v[106:107], v[228:229], s[94:95]\n"
    "v_cvt_pk_bf16_f32 v44, v104, v105\n"
    "v_cvt_pk_bf16_f32 v45, v106, v107\n"
    "v_accvgpr_read_b32 v108, a60\n"
    "v_accvgpr_read_b32 v109, a61\n"
    "v_accvgpr_read_b32 v110, a62\n"
    "v_accvgpr_read_b32 v111, a63\n"
    "v_pk_fma_f32 v[108:109], v[108:109], v[228:229], s[94:95]\n"
    "v_pk_fma_f32 v[110:111], v[110:111], v[228:229], s[94:95]\n"
    "v_cvt_pk_bf16_f32 v46, v108, v109\n"
    "v_cvt_pk_bf16_f32 v47, v110, v111\n"
    "buffer_store_dwordx4 v[44:47], v205, s[28:31], s64 offen offset:64 sc0 sc1\n"
    "v_accvgpr_read_b32 v96, a76\n"
    "v_accvgpr_read_b32 v97, a77\n"
    "v_accvgpr_read_b32 v98, a78\n"
    "v_accvgpr_read_b32 v99, a79\n"
    "v_pk_fma_f32 v[96:97], v[96:97], v[228:229], s[94:95]\n"
    "v_pk_fma_f32 v[98:99], v[98:99], v[228:229], s[94:95]\n"
    "v_cvt_pk_bf16_f32 v48, v96, v97\n"
    "v_cvt_pk_bf16_f32 v49, v98, v99\n"
    "v_accvgpr_read_b32 v100, a92\n"
    "v_accvgpr_read_b32 v101, a93\n"
    "v_accvgpr_read_b32 v102, a94\n"
    "v_accvgpr_read_b32 v103, a95\n"
    "v_pk_fma_f32 v[100:101], v[100:101], v[228:229], s[94:95]\n"
    "v_pk_fma_f32 v[102:103], v[102:103], v[228:229], s[94:95]\n"
    "v_cvt_pk_bf16_f32 v50, v100, v101\n"
    "v_cvt_pk_bf16_f32 v51, v102, v103\n"
    "buffer_store_dwordx4 v[48:51], v205, s[28:31], s64 offen offset:128 sc0 sc1\n"
    "v_accvgpr_read_b32 v104, a108\n"
    "v_accvgpr_read_b32 v105, a109\n"
    "v_accvgpr_read_b32 v106, a110\n"
    "v_accvgpr_read_b32 v107, a111\n"
    "v_pk_fma_f32 v[104:105], v[104:105], v[228:229], s[94:95]\n"
    "v_pk_fma_f32 v[106:107], v[106:107], v[228:229], s[94:95]\n"
    "v_cvt_pk_bf16_f32 v52, v104, v105\n"
    "v_cvt_pk_bf16_f32 v53, v106, v107\n"
    "v_accvgpr_read_b32 v108, a124\n"
    "v_accvgpr_read_b32 v109, a125\n"
    "v_accvgpr_read_b32 v110, a126\n"
    "v_accvgpr_read_b32 v111, a127\n"
    "v_pk_fma_f32 v[108:109], v[108:109], v[228:229], s[94:95]\n"
    "v_pk_fma_f32 v[110:111], v[110:111], v[228:229], s[94:95]\n"
    "v_cvt_pk_bf16_f32 v54, v108, v109\n"
    "v_cvt_pk_bf16_f32 v55, v110, v111\n"
    "buffer_store_dwordx4 v[52:55], v205, s[28:31], s64 offen offset:192 sc0 sc1\n"
    "v_accvgpr_read_b32 v96, a140\n"
    "v_accvgpr_read_b32 v97, a141\n"
    "v_accvgpr_read_b32 v98, a142\n"
    "v_accvgpr_read_b32 v99, a143\n"
    "v_pk_fma_f32 v[96:97], v[96:97], v[228:229], s[94:95]\n"
    "v_pk_fma_f32 v[98:99], v[98:99], v[228:229], s[94:95]\n"
    "v_cvt_pk_bf16_f32 v56, v96, v97\n"
    "v_cvt_pk_bf16_f32 v57, v98, v99\n"
    "v_accvgpr_read_b32 v100, a156\n"
    "v_accvgpr_read_b32 v101, a157\n"
    "v_accvgpr_read_b32 v102, a158\n"
    "v_accvgpr_read_b32 v103, a159\n"
    "v_pk_fma_f32 v[100:101], v[100:101], v[228:229], s[94:95]\n"
    "v_pk_fma_f32 v[102:103], v[102:103], v[228:229], s[94:95]\n"
    "v_cvt_pk_bf16_f32 v58, v100, v101\n"
    "v_cvt_pk_bf16_f32 v59, v102, v103\n"
    "buffer_store_dwordx4 v[56:59], v205, s[28:31], s64 offen offset:256 sc0 sc1\n"
    "v_accvgpr_read_b32 v104, a172\n"
    "v_accvgpr_read_b32 v105, a173\n"
    "v_accvgpr_read_b32 v106, a174\n"
    "v_accvgpr_read_b32 v107, a175\n"
    "v_pk_fma_f32 v[104:105], v[104:105], v[228:229], s[94:95]\n"
    "v_pk_fma_f32 v[106:107], v[106:107], v[228:229], s[94:95]\n"
    "v_cvt_pk_bf16_f32 v60, v104, v105\n"
    "v_cvt_pk_bf16_f32 v61, v106, v107\n"
    "v_accvgpr_read_b32 v108, a188\n"
    "v_accvgpr_read_b32 v109, a189\n"
    "v_accvgpr_read_b32 v110, a190\n"
    "v_accvgpr_read_b32 v111, a191\n"
    "v_pk_fma_f32 v[108:109], v[108:109], v[228:229], s[94:95]\n"
    "v_pk_fma_f32 v[110:111], v[110:111], v[228:229], s[94:95]\n"
    "v_cvt_pk_bf16_f32 v62, v108, v109\n"
    "v_cvt_pk_bf16_f32 v63, v110, v111\n"
    "buffer_store_dwordx4 v[60:63], v205, s[28:31], s64 offen offset:320 sc0 sc1\n"
    "s_branch LFIN\n"
    "LEND:\n"
    :
    : "s"(__builtin_amdgcn_kernarg_segment_ptr()), "s"(blockIdx.x), "s"(wid)
    : "v0", "v1", "v2", "v3", "v4", "v5", "v6", "v7", "v8", "v9", "v10", "v11", "v12", "v13", "v14", "v15", "v16", "v17", "v18", "v19", "v20", "v21", "v22", "v23", "v24", "v25", "v26", "v27", "v28", "v29", "v30", "v31", "v32", "v33", "v34", "v35", "v36", "v37", "v38", "v39", "v40", "v41", "v42", "v43", "v44", "v45", "v46", "v47", "v48", "v49", "v50", "v51", "v52", "v53", "v54", "v55", "v56", "v57", "v58", "v59", "v60", "v61", "v62", "v63", "v64", "v65", "v66", "v67", "v68", "v69", "v70", "v71", "v72", "v73", "v74", "v75", "v76", "v77", "v78", "v79", "v80", "v81", "v82", "v83", "v84", "v85", "v86", "v87", "v88", "v89", "v90", "v91", "v92", "v93", "v94", "v95", "v96", "v97", "v98", "v99", "v100", "v101", "v102", "v103", "v104", "v105", "v106", "v107", "v108", "v109", "v110", "v111", "v112", "v113", "v114", "v115", "v116", "v117", "v118", "v119", "v120", "v121", "v122", "v123", "v124", "v125", "v126", "v127", "v128", "v129", "v130", "v131", "v132", "v133", "v134", "v135", "v136", "v137", "v138", "v139", "v140", "v141", "v142", "v143", "v144", "v145", "v146", "v147", "v148", "v149", "v150", "v151", "v152", "v153", "v154", "v155", "v156", "v157", "v158", "v159", "v160", "v161", "v162", "v163", "v164", "v165", "v166", "v167", "v168", "v169", "v170", "v171", "v172", "v173", "v174", "v175", "v176", "v177", "v178", "v179", "v180", "v181", "v182", "v183", "v184", "v185", "v186", "v187", "v188", "v189", "v190", "v191", "v192", "v193", "v194", "v195", "v196", "v197", "v198", "v199", "v200", "v201", "v202", "v203", "v204", "v205", "v206", "v207", "v208", "v209", "v210", "v211", "v212", "v213", "v214", "v215", "v216", "v217", "v218", "v219", "v220", "v221", "v222", "v223", "v224", "v225", "v226", "v227", "v228", "v229", "v230", "v231", "v232", "v233", "v234", "v235", "v236", "v237", "v238", "v239", "v240", "v241", "v242", "v243", "v244", "v245", "v246", "v247", "v248", "v249", "v250", "v251", "v252", "v253", "v254", "v255", "a0", "a1", "a2", "a3", "a4", "a5", "a6", "a7", "a8", "a9", "a10", "a11", "a12", "a13", "a14", "a15", "a16", "a17", "a18", "a19", "a20", "a21", "a22", "a23", "a24", "a25", "a26", "a27", "a28", "a29", "a30", "a31", "a32", "a33", "a34", "a35", "a36", "a37", "a38", "a39", "a40", "a41", "a42", "a43", "a44", "a45", "a46", "a47", "a48", "a49", "a50", "a51", "a52", "a53", "a54", "a55", "a56", "a57", "a58", "a59", "a60", "a61", "a62", "a63", "a64", "a65", "a66", "a67", "a68", "a69", "a70", "a71", "a72", "a73", "a74", "a75", "a76", "a77", "a78", "a79", "a80", "a81", "a82", "a83", "a84", "a85", "a86", "a87", "a88", "a89", "a90", "a91", "a92", "a93", "a94", "a95", "a96", "a97", "a98", "a99", "a100", "a101", "a102", "a103", "a104", "a105", "a106", "a107", "a108", "a109", "a110", "a111", "a112", "a113", "a114", "a115", "a116", "a117", "a118", "a119", "a120", "a121", "a122", "a123", "a124", "a125", "a126", "a127", "a128", "a129", "a130", "a131", "a132", "a133", "a134", "a135", "a136", "a137", "a138", "a139", "a140", "a141", "a142", "a143", "a144", "a145", "a146", "a147", "a148", "a149", "a150", "a151", "a152", "a153", "a154", "a155", "a156", "a157", "a158", "a159", "a160", "a161", "a162", "a163", "a164", "a165", "a166", "a167", "a168", "a169", "a170", "a171", "a172", "a173", "a174", "a175", "a176", "a177", "a178", "a179", "a180", "a181", "a182", "a183", "a184", "a185", "a186", "a187", "a188", "a189", "a190", "a191", "a192", "a193", "a194", "a195", "a196", "a197", "a198", "a199", "a200", "a201", "a202", "a203", "a204", "a205", "a206", "a207", "a208", "a209", "a210", "a211", "a212", "a213", "a214", "a215", "a216", "a217", "a218", "a219", "a220", "a221", "a222", "a223", "a224", "a225", "a226", "a227", "a228", "a229", "a230", "a231", "a232", "a233", "a234", "a235", "a236", "a237", "a238", "a239", "a240", "a241", "a242", "a243", "a244", "a245", "a246", "a247", "a248", "a249", "a250", "a251", "a252", "a253", "a254", "a255", "s8", "s9", "s10", "s11", "s12", "s13", "s14", "s15", "s16", "s17", "s18", "s19", "s20", "s21", "s22", "s23", "s24", "s25", "s26", "s27", "s28", "s29", "s30", "s31", "s32", "s33", "s34", "s35", "s36", "s37", "s38", "s39", "s40", "s41", "s42", "s43", "s44", "s45", "s46", "s47", "s48", "s49", "s50", "s51", "s52", "s53", "s54", "s55", "s56", "s57", "s58", "s59", "s60", "s61", "s62", "s63", "s64", "s65", "s66", "s67", "s68", "s69", "s70", "s71", "s72", "s73", "s74", "s75", "s76", "s77", "s78", "s79", "s80", "s81", "s82", "s83", "s84", "s85", "s86", "s87", "s88", "s89", "s90", "s91", "s92", "s93", "s94", "s95", "s96", "s97", "s98", "s99", "s100", "s101", "vcc", "scc", "memory");
}

extern "C" void kernel_launch(void* const* d_in, const int* in_sizes, int n_in, void* d_out, int out_size, void* d_ws, size_t ws_size,
                              hipStream_t stream) {
  (void)hipFuncSetAttribute((const void*)attn_fused, hipFuncAttributeMaxDynamicSharedMemorySize, 153088);
  attn_fused<<<256, 256, 153088, stream>>>(d_in[0], d_in[1], d_in[2], d_in[3], d_in[4], d_in[5], d_in[6], d_ws, d_out);
}
```

```cpp
#include <hip/hip_runtime.h>

__global__ __launch_bounds__(256, 1) void attn_fused(const void* q, const void* qk, const void* qv, const void* ks, const void* kz,
                                                     const void* vs, const void* vz, void* ws, void* out) {
  const int wid = __builtin_amdgcn_readfirstlane(threadIdx.x >> 6);
  asm volatile(
    "LS0:\n"
    "s_load_dwordx16 s[8:23], %0, 0x0\n"
    "s_mov_b32 s58, %2\n"
    "s_and_b32 s76, %1, 7\n"
    "s_lshr_b32 s77, %1, 3\n"
    "s_and_b32 s78, s77, 3\n"
    "s_lshl_b32 s56, s76, 2\n"
    "s_add_u32 s56, s56, s78\n"
    "s_lshr_b32 s57, s77, 2\n"
    "v_mbcnt_lo_u32_b32 v231, -1, 0\n"
    "v_mbcnt_hi_u32_b32 v231, -1, v231\n"
    "s_getpc_b64 s[44:45]\n"
    "LPC:\n"
    "s_mov_b32 s46, LSLOW-LPC\n"
    "s_mov_b32 s47, 0x20000\n"
    "s_lshr_b32 s76, %1, 3\n"
    "s_lshl_b32 s76, s76, 2\n"
    "s_add_u32 s76, s76, s58\n"
    "s_lshl_b32 s76, s76, 9\n"
    "v_lshlrev_b32 v196, 3, v231\n"
    "buffer_load_dword v251, v196, s[44:47], s76 offen\n"
    "s_mul_i32 s72, s58, 0xc00\n"
    "s_lshr_b32 s86, s57, 2\n"
    "s_mul_i32 s64, s86, 0xc000\n"
    "s_lshl_b32 s86, s86, 3\n"
    "s_and_b32 s79, s57, 3\n"
    "s_add_u32 s86, s86, s79\n"
    "s_lshl_b32 s86, s86, 2\n"
    "s_xor_b32 s76, s57, 4\n"
    "s_and_b32 s77, s76, 6\n"
    "s_mul_i32 s77, s77, 15\n"
    "s_add_u32 s76, s76, s77\n"
    "s_and_b32 s77, s58, 1\n"
    "s_mul_i32 s78, s77, 12\n"
    "s_add_u32 s78, s78, s86\n"
    "s_lshl_b32 s78, s78, 1\n"
    "s_mul_i32 s77, s77, 0xc0\n"
    "s_movk_i32 s90, 0x556\n"
    "s_lshl_b32 s60, s56, 12\n"
    "s_cmp_lt_u32 s58, 2\n"
    "s_cselect_b32 s76, s76, s78\n"
    "s_cmovk_i32 s90, 0xaab\n"
    "s_cselect_b32 s91, 24, 0\n"
    "s_cselect_b32 s92, 1, 10\n"
    "s_cselect_b32 s89, 7, 0\n"
    "s_cselect_b32 s64, s64, 0\n"
    "s_add_u32 s60, s60, s76\n"
    "s_mul_i32 s60, s60, 0x180\n"
    "s_mul_i32 s76, s76, 0x180\n"
    "s_sub_u32 s34, 0x180000, s76\n"
    "s_mov_b32 s35, 0x20000\n"
    "v_add_u32 v144, s77, v231\n"
    "v_add_u32 v145, 64, v144\n"
    "v_add_u32 v146, 64, v145\n"
    "v_mul_u32_u24 v197, s90, v144\n"
    "v_lshrrev_b32 v197, 16, v197\n"
    "v_mul_u32_u24 v198, s92, v197\n"
    "v_lshrrev_b32 v198, 1, v198\n"
    "v_and_b32 v198, s89, v198\n"
    "v_xor_b32 v198, v198, v144\n"
    "v_mul_u32_u24 v197, s91, v197\n"
    "v_add_lshl_u32 v202, v197, v198, 4\n"
    "v_mul_u32_u24 v197, s90, v145\n"
    "v_lshrrev_b32 v197, 16, v197\n"
    "v_mul_u32_u24 v198, s92, v197\n"
    "v_lshrrev_b32 v198, 1, v198\n"
    "v_and_b32 v198, s89, v198\n"
    "v_xor_b32 v198, v198, v144\n"
    "v_mul_u32_u24 v197, s91, v197\n"
    "v_add_lshl_u32 v203, v197, v198, 4\n"
    "v_mul_u32_u24 v197, s90, v146\n"
    "v_lshrrev_b32 v197, 16, v197\n"
    "v_mul_u32_u24 v198, s92, v197\n"
    "v_lshrrev_b32 v198, 1, v198\n"
    "v_and_b32 v198, s89, v198\n"
    "v_xor_b32 v198, v198, v144\n"
    "v_mul_u32_u24 v197, s91, v197\n"
    "v_add_lshl_u32 v204, v197, v198, 4\n"
    "s_waitcnt lgkmcnt(0)\n"
    "s_cmp_lt_u32 s58, 2\n"
    "s_cselect_b64 s[80:81], s[10:11], s[12:13]\n"
    "s_cselect_b64 s[82:83], s[16:17], s[14:15]\n"
    "s_add_u32 s32, s80, s60\n"
    "s_addc_u32 s33, s81, 0\n"
    "s_cmp_eq_u32 s64, 0\n"
    "s_cbranch_scc1 LNOR\n"
    "s_add_u32 m0, s72, 0x12000\n"
    "s_nop 0\n"
    "buffer_load_dwordx4 v202, s[32:35], 0 offen nt lds\n"
    "buffer_load_dwordx4 v203, s[32:35], 0 offen offset:1024 nt lds\n"
    "buffer_load_dwordx4 v204, s[32:35], 0 offen offset:2048 nt lds\n"
    "LNOR:\n"
    "s_add_u32 m0, s72, 0x15000\n"
    "s_nop 0\n"
    "buffer_load_dwordx4 v202, s[32:35], s64 offen nt lds\n"
    "buffer_load_dwordx4 v203, s[32:35], s64 offen offset:1024 nt lds\n"
    "buffer_load_dwordx4 v204, s[32:35], s64 offen offset:2048 nt lds\n"
    "LR0:\n"
    "s_load_dwordx2 s[24:25], %0, 0x40\n"
    "s_mul_i32 s71, s58, 0x1800\n"
    "s_lshr_b32 s87, s57, 2\n"
    "s_mul_i32 s87, s87, 0x6000\n"
    "s_mul_i32 s61, s56, 0x3000\n"
    "s_lshl_b32 s62, s56, 12\n"
    "s_lshl_b32 s59, s56, 12\n"
    "s_mov_b32 s50, 0x3000\n"
    "s_mov_b32 s51, 0x20000\n"
    "s_and_b32 s77, s57, 1\n"
    "s_mul_i32 s77, s77, 0xc0\n"
    "v_mul_u32_u24 v197, 0x1556, v144\n"
    "v_lshrrev_b32 v197, 16, v197\n"
    "v_mul_u32_u24 v196, 12, v197\n"
    "v_sub_u32 v196, v144, v196\n"
    "v_mul_u32_u24 v248, 0x180, v197\n"
    "v_lshl_add_u32 v248, v196, 4, v248\n"
    "v_add_u32 v248, s77, v248\n"
    "s_add_u32 s48, s82, s61\n"
    "s_addc_u32 s49, s83, 0\n"
    "s_add_u32 m0, s72, 0x21000\n"
    "s_nop 0\n"
    "buffer_load_dwordx4 v248, s[48:51], 0 offen lds\n"
    "s_add_u32 s40, s20, s62\n"
    "s_addc_u32 s41, s21, 0\n"
    "s_add_u32 s52, s18, s62\n"
    "s_addc_u32 s53, s19, 0\n"
    "s_mov_b32 s42, 0x1000\n"
    "s_mov_b32 s43, 0x20000\n"
    "s_mov_b32 s54, 0x1000\n"
    "s_mov_b32 s55, 0x20000\n"
    "v_lshlrev_b32 v196, 1, v231\n"
    "buffer_load_ushort v44, v196, s[40:43], 0 offen\n"
    "buffer_load_ushort v45, v196, s[52:55], 0 offen\n"
    "s_lshl_b32 s77, s58, 6\n"
    "v_add_u32 v41, s77, v231\n"
    "v_bfe_u32 v197, v231, 2, 1\n"
    "v_mul_u32_u24 v197, 12, v197\n"
    "v_and_b32 v198, 7, v231\n"
    "v_add3_u32 v197, v197, v198, s86\n"
    "v_lshrrev_b32 v40, 3, v41\n"
    "v_lshlrev_b32 v40, 7, v40\n"
    "v_lshl_add_u32 v40, v197, 1, v40\n"
    "buffer_load_ushort v42, v40, s[40:43], 0 offen\n"
    "buffer_load_ushort v43, v40, s[52:55], 0 offen\n"
    "s_add_u32 s44, s22, s59\n"
    "s_addc_u32 s45, s23, 0\n"
    "s_mov_b32 s46, 0x900\n"
    "s_mov_b32 s47, 0x20000\n"
    "v_mov_b32 v196, 0x40000000\n"
    "s_cmp_eq_u32 s58, 0\n"
    "s_cselect_b32 s77, 64, 0\n"
    "s_cselect_b32 s74, 1, 0\n"
    "v_and_b32 v213, 7, v231\n"
    "v_mul_u32_u24 v213, 0x100, v213\n"
    "v_cmp_gt_u32 vcc, s77, v231\n"
    "v_mov_b32 v197, 0x800\n"
    "s_mul_i32 s77, s57, 0x100\n"
    "v_cndmask_b32 v213, v196, v213, vcc\n"
    "v_cndmask_b32 v197, v196, v197, vcc\n"
    "buffer_load_dword v221, v197, s[44:47], 0 offen sc1\n"
    "v_cmp_gt_u32 vcc, s74, v231\n"
    "v_mov_b32 v214, s77\n"
    "s_mul_i32 s66, s74, 3\n"
    "v_cndmask_b32 v214, v196, v214, vcc\n"
    "s_lshl_b32 s76, s56, 3\n"
    "s_add_u32 s76, s76, s57\n"
    "s_mul_i32 s76, s76, 0x18000\n"
    "s_add_u32 s28, s8, s76\n"
    "s_addc_u32 s29, s9, 0\n"
    "s_mov_b32 s30, 0x18000\n"
    "s_mov_b32 s31, 0x20000\n"
    "s_mul_i32 s88, s58, 0x6000\n"
    "v_mul_u32_u24 v197, 0xaab, v231\n"
    "v_lshrrev_b32 v197, 16, v197\n"
    "v_mul_u32_u24 v196, 24, v197\n"
    "v_sub_u32 v196, v231, v196\n"
    "v_mul_u32_u24 v152, 0x180, v197\n"
    "v_lshrrev_b32 v197, 1, v197\n"
    "v_xor_b32 v196, v196, v197\n"
    "v_lshl_add_u32 v152, v196, 4, v152\n"
    "v_add_u32 v198, 64, v231\n"
    "v_mul_u32_u24 v197, 0xaab, v198\n"
    "v_lshrrev_b32 v197, 16, v197\n"
    "v_mul_u32_u24 v196, 24, v197\n"
    "v_sub_u32 v196, v198, v196\n"
    "v_mul_u32_u24 v153, 0x180, v197\n"
    "v_lshrrev_b32 v197, 1, v197\n"
    "v_xor_b32 v196, v196, v197\n"
    "v_lshl_add_u32 v153, v196, 4, v153\n"
    "v_add_u32 v198, 0x80, v231\n"
    "v_mul_u32_u24 v197, 0xaab, v198\n"
    "v_lshrrev_b32 v197, 16, v197\n"
    "v_mul_u32_u24 v196, 24, v197\n"
    "v_sub_u32 v196, v198, v196\n"
    "v_mul_u32_u24 v154, 0x180, v197\n"
    "v_lshrrev_b32 v197, 1, v197\n"
    "v_xor_b32 v196, v196, v197\n"
    "v_lshl_add_u32 v154, v196, 4, v154\n"
    "s_mov_b32 s76, 0xc00\n"
    "v_xad_u32 v155, v152, 64, s76\n"
    "v_xad_u32 v156, v153, 64, s76\n"
    "v_xad_u32 v157, v154, 64, s76\n"
    "s_add_u32 s78, s88, 0\n"
    "s_add_u32 m0, s71, 0\n"
    "s_nop 0\n"
    "buffer_load_dwordx4 v152, s[28:31], s78 offen nt lds\n"
    "s_add_u32 s78, s88, 0\n"
    "s_add_u32 m0, s71, 0x400\n"
    "s_nop 0\n"
    "buffer_load_dwordx4 v153, s[28:31], s78 offen nt lds\n"
    "s_add_u32 s78, s64, 0xc000\n"
    "s_add_u32 m0, s72, 0x1e000\n"
    "s_nop 0\n"
    "buffer_load_dwordx4 v202, s[32:35], s78 offen nt lds\n"
    "buffer_load_dwordx4 v203, s[32:35], s78 offen offset:1024 nt lds\n"
    "buffer_load_dwordx4 v204, s[32:35], s78 offen offset:2048 nt lds\n"
    "v_mov_b32 v196, 0x40000000\n"
    "v_and_b32 v46, 15, v231\n"
    "v_lshrrev_b32 v47, 4, v231\n"
    "s_mov_b32 s75, 0x3dd53b95\n"
    "v_min_u32 v199, 2, v47\n"
    "v_lshlrev_b32 v197, 3, v199\n"
    "s_lshl_b32 s76, s58, 1\n"
    "v_add_u32 v197, s76, v197\n"
    "v_lshrrev_b32 v198, 1, v46\n"
    "v_xor_b32 v197, v197, v198\n"
    "v_mul_u32_u24 v205, 0x180, v46\n"
    "v_lshl_add_u32 v205, v197, 4, v205\n"
    "v_add_u32 v205, 0x12000, v205\n"
    "v_xor_b32 v206, 16, v205\n"
    "v_and_b32 v197, 7, v231\n"
    "v_lshl_add_u32 v209, v199, 5, v197\n"
    "s_lshl_b32 s76, s58, 3\n"
    "v_add_lshl_u32 v209, v209, s76, 1\n"
    "v_add_u32 v217, 0x21000, v209\n"
    "s_lshr_b32 s76, s57, 2\n"
    "s_mul_i32 s76, s76, 0xc0\n"
    "s_add_u32 s76, s76, 0x20f40\n"
    "v_add_u32 v209, s76, v209\n"
    "v_lshrrev_b32 v198, 3, v231\n"
    "s_mul_i32 s76, s58, 12\n"
    "v_lshl_add_u32 v199, v198, 1, s76\n"
    "v_mul_u32_u24 v207, 5, v197\n"
    "v_and_b32 v207, 9, v207\n"
    "v_xor_b32 v199, v199, v207\n"
    "v_mul_u32_u24 v207, 0x300, v197\n"
    "v_lshl_add_u32 v207, v199, 4, v207\n"
    "v_add_u32 v207, 0x13800, v207\n"
    "v_xor_b32 v208, 16, v207\n"
    "s_mul_i32 s76, s58, 0xc0\n"
    "s_add_u32 s76, s76, 0x13800\n"
    "v_lshl_add_u32 v207, v231, 2, s76\n"
    "v_lshlrev_b32 v210, 3, v197\n"
    "v_add_u32 v210, 0x24400, v210\n"
    "s_mul_i32 s76, s58, 0x480\n"
    "s_add_u32 s76, s76, 0x24400\n"
    "v_mul_u32_u24 v215, 0x90, v197\n"
    "v_lshl_add_u32 v215, v198, 4, v215\n"
    "v_add_u32 v215, s76, v215\n"
    "v_and_b32 v197, 1, v197\n"
    "v_lshlrev_b32 v197, 3, v197\n"
    "v_sub_u32 v215, v215, v197\n"
    "v_bfe_u32 v197, v231, 2, 2\n"
    "v_mul_u32_u24 v216, 0x90, v197\n"
    "v_lshl_add_u32 v216, v47, 5, v216\n"
    "v_and_b32 v197, 1, v197\n"
    "v_lshlrev_b32 v197, 3, v197\n"
    "v_sub_u32 v216, v216, v197\n"
    "v_and_b32 v197, 3, v231\n"
    "v_lshl_add_u32 v216, v197, 3, v216\n"
    "v_add_u32 v216, s76, v216\n"
    "s_lshr_b32 s76, s57, 1\n"
    "s_xor_b32 s76, s76, 2\n"
    "s_mul_i32 s76, s76, 0x1800\n"
    "s_and_b32 s77, s57, 1\n"
    "s_mul_i32 s77, s77, 0xc00\n"
    "s_add_u32 s76, s76, s77\n"
    "s_lshl_b32 s77, s58, 8\n"
    "s_add_u32 s76, s76, s77\n"
    "v_lshlrev_b32 v197, 4, v46\n"
    "v_lshl_add_u32 v211, v47, 10, v197\n"
    "v_add_u32 v211, s76, v211\n"
    "s_lshr_b32 s76, s57, 2\n"
    "s_lshl_b32 s76, s76, 10\n"
    "s_lshl_b32 s77, s79, 8\n"
    "s_add_u32 s76, s76, s77\n"
    "s_mul_i32 s77, s58, 48\n"
    "v_add_u32 v198, s77, v231\n"
    "v_lshrrev_b32 v197, 5, v198\n"
    "v_lshl_add_u32 v212, v197, 12, s76\n"
    "v_bfe_u32 v197, v198, 2, 1\n"
    "v_lshl_add_u32 v212, v197, 11, v212\n"
    "v_bfe_u32 v197, v198, 3, 2\n"
    "v_lshl_add_u32 v212, v197, 6, v212\n"
    "v_cmp_gt_u32 vcc, 48, v231\n"
    "v_and_b32 v197, 3, v198\n"
    "v_lshl_add_u32 v212, v197, 4, v212\n"
    "v_cndmask_b32 v211, v196, v211, vcc\n"
    "v_cndmask_b32 v212, v196, v212, vcc\n"
    "v_mov_b32 v222, 0\n"
    "v_mov_b32 v223, 0\n"
    "v_mov_b32 v224, 0\n"
    "v_mov_b32 v225, 0\n"
    "v_mov_b32 v160, 0\n"
    "v_mov_b32 v161, 0\n"
    "v_mov_b32 v162, 0\n"
    "v_mov_b32 v163, 0\n"
    "v_mov_b32 v164, 0\n"
    "v_mov_b32 v165, 0\n"
    "v_mov_b32 v172, 0\n"
    "v_mov_b32 v173, 0\n"
    "v_mov_b32 v174, 0\n"
    "v_mov_b32 v175, 0\n"
    "v_lshlrev_b32 v41, 3, v41\n"
    "v_add_u32 v41, 0x24400, v41\n"
    "s_mul_i32 s76, s56, 0xc0000\n"
    "s_add_u32 s77, s76, 0x40000\n"
    "s_add_u32 s36, s22, s77\n"
    "s_addc_u32 s37, s23, 0\n"
    "s_mov_b32 s38, 0xc0000\n"
    "s_mov_b32 s39, 0x20000\n"
    "s_cmp_eq_u32 s87, 0\n"
    "s_cselect_b32 s62, 0x40000000, 0\n"
    "v_lshlrev_b32 v200, 4, v231\n"
    "v_add_u32 v201, 0xc000, v200\n"
    "v_mul_u32_u24 v198, 0x180, v46\n"
    "v_add_u32 v198, s71, v198\n"
    "v_lshrrev_b32 v197, 1, v46\n"
    "v_xor_b32 v197, v197, v47\n"
    "v_lshl_add_u32 v158, v197, 4, v198\n"
    "v_xor_b32 v159, 64, v158\n"
    "v_and_b32 v197, 3, v231\n"
    "v_cmp_eq_u32 vcc, 0, v197\n"
    "v_mov_b32 v198, 0x3f803f80\n"
    "s_nop 0\n"
    "v_cndmask_b32 v192, 0, v198, vcc\n"
    "v_cndmask_b32 v193, 0, v198, vcc\n"
    "v_cndmask_b32 v194, 0, v198, vcc\n"
    "v_cndmask_b32 v195, 0, v198, vcc\n"
    "s_and_b32 s77, s57, 1\n"
    "s_mul_i32 s77, s77, 0xc0\n"
    "s_sub_u32 s77, s77, 0x400\n"
    "v_mul_u32_u24 v197, 0x1556, v145\n"
    "v_lshrrev_b32 v197, 16, v197\n"
    "v_mul_u32_u24 v196, 12, v197\n"
    "v_sub_u32 v196, v145, v196\n"
    "v_mul_u32_u24 v249, 0x180, v197\n"
    "v_lshl_add_u32 v249, v196, 4, v249\n"
    "v_add_u32 v249, s77, v249\n"
    "s_and_b32 s77, s57, 1\n"
    "s_mul_i32 s77, s77, 0xc0\n"
    "s_sub_u32 s77, s77, 0x800\n"
    "v_mul_u32_u24 v197, 0x1556, v146\n"
    "v_lshrrev_b32 v197, 16, v197\n"
    "v_mul_u32_u24 v196, 12, v197\n"
    "v_sub_u32 v196, v146, v196\n"
    "v_mul_u32_u24 v250, 0x180, v197\n"
    "v_lshl_add_u32 v250, v196, 4, v250\n"
    "v_add_u32 v250, s77, v250\n"
    "s_waitcnt vmcnt(9)\n"
    "v_lshlrev_b32 v44, 16, v44\n"
    "s_waitcnt vmcnt(8)\n"
    "v_lshlrev_b32 v45, 16, v45\n"
    "v_mul_f32 v197, v44, v45\n"
    "s_nop 1\n"
    "v_min_f32_dpp v197, v197, v197 row_ror:8 row_mask:0xf bank_mask:0xf\n"
    "s_mul_i32 s76, s56, 0xc0000\n"
    "s_add_u32 s77, s76, 0x1840000\n"
    "v_min_f32_dpp v197, v197, v197 row_ror:4 row_mask:0xf bank_mask:0xf\n"
    "s_add_u32 s40, s22, s77\n"
    "s_addc_u32 s41, s23, 0\n"
    "v_min_f32_dpp v197, v197, v197 row_ror:2 row_mask:0xf bank_mask:0xf\n"
    "s_mov_b32 s42, 0xc0000\n"
    "s_mov_b32 s43, 0x20000\n"
    "v_min_f32_dpp v197, v197, v197 row_ror:1 row_mask:0xf bank_mask:0xf\n"
    "v_mov_b32 v198, v197\n"
    "s_nop 1\n"
    "v_permlane32_swap_b32 v197, v198\n"
    "v_min_f32 v197, v197, v198\n"
    "v_mov_b32 v198, v197\n"
    "s_nop 1\n"
    "v_permlane16_swap_b32 v197, v198\n"
    "v_min_f32 v197, v197, v198\n"
    "v_rcp_f32 v198, v197\n"
    "v_subrev_u32 v199, 0x37800000, v197\n"
    "v_mul_f32 v197, 0x3fcccccd, v197\n"
    "v_cmp_gt_u32 vcc, 0x10000000, v199\n"
    "v_mul_f32 v198, 0x3f3425ed, v198\n"
    "v_mov_b32 v199, 0x3f800000\n"
    "v_cndmask_b32 v197, 0, v197, vcc\n"
    "v_cndmask_b32 v198, v199, v198, vcc\n"
    "v_readfirstlane_b32 s70, v197\n"
    "v_mul_f32 v197, v197, v198\n"
    "v_readfirstlane_b32 s68, v198\n"
    "v_readfirstlane_b32 s69, v197\n"
    "s_waitcnt vmcnt(7)\n"
    "v_lshlrev_b32 v42, 16, v42\n"
    "s_waitcnt vmcnt(6)\n"
    "v_lshlrev_b32 v43, 16, v43\n"
    "v_mul_f32 v44, s68, v43\n"
    "v_fma_f32 v45, -v42, v44, s69\n"
    "ds_write_b64 v41, v[44:45]\n"
    "s_add_u32 s78, s88, 0\n"
    "s_add_u32 m0, s71, 0x800\n"
    "s_nop 0\n"
    "buffer_load_dwordx4 v154, s[28:31], s78 offen nt lds\n"
    "s_add_u32 s78, s88, 0\n"
    "s_add_u32 m0, s71, 0xc00\n"
    "s_nop 0\n"
    "buffer_load_dwordx4 v155, s[28:31], s78 offen nt lds\n"
    "s_waitcnt lgkmcnt(0)\n"
    "s_barrier\n"
    "ds_read_u16_d16_hi v172, v217 offset:6144\n"
    "ds_read_u16_d16_hi v173, v217\n"
    "ds_read_u16_d16_hi v174, v217 offset:6336\n"
    "ds_read_u16_d16_hi v175, v217 offset:192\n"
    "ds_read_b64 v[228:229], v210\n"
    "ds_read2st64_b32 v[240:241], v207 offset0:48 offset1:51\n"
    "ds_read2st64_b32 v[242:243], v207 offset0:54 offset1:57\n"
    "ds_read2st64_b32 v[244:245], v207 offset0:60 offset1:63\n"
    "ds_read2st64_b32 v[246:247], v207 offset0:66 offset1:69\n"
    "ds_read_u16_d16_hi v223, v209 offset:6144\n"
    "ds_read_u16_d16_hi v222, v209\n"
    "ds_read_b128 v[232:235], v205\n"
    "ds_read_b128 v[236:239], v206\n"
    "ds_read_u16_d16_hi v161, v209 offset:6336\n"
    "ds_read_u16_d16_hi v160, v209 offset:192\n"
    "s_waitcnt lgkmcnt(11)\n"
    "ds_read_b128 v[64:67], v205 offset:12288\n"
    "ds_read_b128 v[68:71], v206 offset:12288\n"
    "v_mul_f32 v197, v172, v173\n"
    "v_mul_f32 v198, v174, v175\n"
    "v_min_f32 v197, v197, v198\n"
    "v_max_f32 v197, 0, v197\n"
    "v_mul_f32 v217, 0x3e3fe8d2, v197\n"
    "s_waitcnt lgkmcnt(7)\n"
    "v_mul_f32 v226, s75, v223\n"
    "s_waitcnt lgkmcnt(6)\n"
    "v_fma_f32 v227, -v222, v226, v217\n"
    "s_waitcnt lgkmcnt(5)\n"
    "v_cvt_f32_i32 v232, v232\n"
    "v_cvt_f32_i32 v233, v233\n"
    "v_cvt_f32_i32 v234, v234\n"
    "v_cvt_f32_i32 v235, v235\n"
    "s_waitcnt lgkmcnt(4)\n"
    "v_cvt_f32_i32 v236, v236\n"
    "v_cvt_f32_i32 v237, v237\n"
    "v_cvt_f32_i32 v238, v238\n"
    "v_cvt_f32_i32 v239, v239\n"
    "v_mul_f32_dpp v232, v226, v232 row_newbcast:0 row_mask:0xf bank_mask:0xf\n"
    "v_mul_f32_dpp v233, v226, v233 row_newbcast:1 row_mask:0xf bank_mask:0xf\n"
    "v_mul_f32_dpp v234, v226, v234 row_newbcast:2 row_mask:0xf bank_mask:0xf\n"
    "v_mul_f32_dpp v235, v226, v235 row_newbcast:3 row_mask:0xf bank_mask:0xf\n"
    "v_mul_f32_dpp v236, v226, v236 row_newbcast:4 row_mask:0xf bank_mask:0xf\n"
    "v_mul_f32_dpp v237, v226, v237 row_newbcast:5 row_mask:0xf bank_mask:0xf\n"
    "v_mul_f32_dpp v238, v226, v238 row_newbcast:6 row_mask:0xf bank_mask:0xf\n"
    "v_mul_f32_dpp v239, v226, v239 row_newbcast:7 row_mask:0xf bank_mask:0xf\n"
    "v_add_f32_dpp v232, v227, v232 row_newbcast:0 row_mask:0xf bank_mask:0xf\n"
    "v_add_f32_dpp v233, v227, v233 row_newbcast:1 row_mask:0xf bank_mask:0xf\n"
    "v_add_f32_dpp v234, v227, v234 row_newbcast:2 row_mask:0xf bank_mask:0xf\n"
    "v_add_f32_dpp v235, v227, v235 row_newbcast:3 row_mask:0xf bank_mask:0xf\n"
    "v_add_f32_dpp v236, v227, v236 row_newbcast:4 row_mask:0xf bank_mask:0xf\n"
    "v_add_f32_dpp v237, v227, v237 row_newbcast:5 row_mask:0xf bank_mask:0xf\n"
    "v_add_f32_dpp v238, v227, v238 row_newbcast:6 row_mask:0xf bank_mask:0xf\n"
    "v_add_f32_dpp v239, v227, v239 row_newbcast:7 row_mask:0xf bank_mask:0xf\n"
    "v_cvt_pk_bf16_f32 v232, v232, v233\n"
    "v_cvt_pk_bf16_f32 v233, v234, v235\n"
    "v_cvt_pk_bf16_f32 v234, v236, v237\n"
    "v_cvt_pk_bf16_f32 v235, v238, v239\n"
    "buffer_store_dwordx4 v[232:235], v211, s[36:39], s62 offen sc1\n"
    "v_cvt_f32_i32 v240, v240\n"
    "v_cvt_f32_i32 v241, v241\n"
    "v_cvt_f32_i32 v242, v242\n"
    "v_cvt_f32_i32 v243, v243\n"
    "v_cvt_f32_i32 v244, v244\n"
    "v_cvt_f32_i32 v245, v245\n"
    "v_cvt_f32_i32 v246, v246\n"
    "v_cvt_f32_i32 v247, v247\n"
    "v_mul_f32_dpp v240, v228, v240 row_newbcast:0 row_mask:0xf bank_mask:0xf\n"
    "v_mul_f32_dpp v241, v228, v241 row_newbcast:1 row_mask:0xf bank_mask:0xf\n"
    "v_mul_f32_dpp v242, v228, v242 row_newbcast:2 row_mask:0xf bank_mask:0xf\n"
    "v_mul_f32_dpp v243, v228, v243 row_newbcast:3 row_mask:0xf bank_mask:0xf\n"
    "v_mul_f32_dpp v244, v228, v244 row_newbcast:4 row_mask:0xf bank_mask:0xf\n"
    "v_mul_f32_dpp v245, v228, v245 row_newbcast:5 row_mask:0xf bank_mask:0xf\n"
    "v_mul_f32_dpp v246, v228, v246 row_newbcast:6 row_mask:0xf bank_mask:0xf\n"
    "v_mul_f32_dpp v247, v228, v247 row_newbcast:7 row_mask:0xf bank_mask:0xf\n"
    "v_add_f32_dpp v240, v229, v240 row_newbcast:0 row_mask:0xf bank_mask:0xf\n"
    "v_add_f32_dpp v241, v229, v241 row_newbcast:1 row_mask:0xf bank_mask:0xf\n"
    "v_add_f32_dpp v242, v229, v242 row_newbcast:2 row_mask:0xf bank_mask:0xf\n"
    "v_add_f32_dpp v243, v229, v243 row_newbcast:3 row_mask:0xf bank_mask:0xf\n"
    "v_add_f32_dpp v244, v229, v244 row_newbcast:4 row_mask:0xf bank_mask:0xf\n"
    "v_add_f32_dpp v245, v229, v245 row_newbcast:5 row_mask:0xf bank_mask:0xf\n"
    "v_add_f32_dpp v246, v229, v246 row_newbcast:6 row_mask:0xf bank_mask:0xf\n"
    "v_add_f32_dpp v247, v229, v247 row_newbcast:7 row_mask:0xf bank_mask:0xf\n"
    "v_cvt_pk_bf16_f32 v240, v240, v241\n"
    "v_cvt_pk_bf16_f32 v241, v242, v243\n"
    "v_cvt_pk_bf16_f32 v242, v244, v245\n"
    "v_cvt_pk_bf16_f32 v243, v246, v247\n"
    "s_mov_b32 s78, 0\n"
    "buffer_store_dwordx4 v[240:243], v212, s[40:43], s78 offen sc1\n"
    "s_waitcnt lgkmcnt(3)\n"
    "v_mul_f32 v166, s75, v161\n"
    "s_waitcnt lgkmcnt(2)\n"
    "v_fma_f32 v167, -v160, v166, v217\n"
    "s_waitcnt lgkmcnt(1)\n"
    "v_cvt_f32_i32 v64, v64\n"
    "v_cvt_f32_i32 v65, v65\n"
    "v_cvt_f32_i32 v66, v66\n"
    "v_cvt_f32_i32 v67, v67\n"
    "s_waitcnt lgkmcnt(0)\n"
    "v_cvt_f32_i32 v68, v68\n"
    "v_cvt_f32_i32 v69, v69\n"
    "v_cvt_f32_i32 v70, v70\n"
    "v_cvt_f32_i32 v71, v71\n"
    "v_mul_f32_dpp v64, v166, v64 row_newbcast:0 row_mask:0xf bank_mask:0xf\n"
    "v_mul_f32_dpp v65, v166, v65 row_newbcast:1 row_mask:0xf bank_mask:0xf\n"
    "v_mul_f32_dpp v66, v166, v66 row_newbcast:2 row_mask:0xf bank_mask:0xf\n"
    "v_mul_f32_dpp v67, v166, v67 row_newbcast:3 row_mask:0xf bank_mask:0xf\n"
    "v_mul_f32_dpp v68, v166, v68 row_newbcast:4 row_mask:0xf bank_mask:0xf\n"
    "v_mul_f32_dpp v69, v166, v69 row_newbcast:5 row_mask:0xf bank_mask:0xf\n"
    "v_mul_f32_dpp v70, v166, v70 row_newbcast:6 row_mask:0xf bank_mask:0xf\n"
    "v_mul_f32_dpp v71, v166, v71 row_newbcast:7 row_mask:0xf bank_mask:0xf\n"
    "v_add_f32_dpp v64, v167, v64 row_newbcast:0 row_mask:0xf bank_mask:0xf\n"
    "v_add_f32_dpp v65, v167, v65 row_newbcast:1 row_mask:0xf bank_mask:0xf\n"
    "v_add_f32_dpp v66, v167, v66 row_newbcast:2 row_mask:0xf bank_mask:0xf\n"
    "v_add_f32_dpp v67, v167, v67 row_newbcast:3 row_mask:0xf bank_mask:0xf\n"
    "v_add_f32_dpp v68, v167, v68 row_newbcast:4 row_mask:0xf bank_mask:0xf\n"
    "v_add_f32_dpp v69, v167, v69 row_newbcast:5 row_mask:0xf bank_mask:0xf\n"
    "v_add_f32_dpp v70, v167, v70 row_newbcast:6 row_mask:0xf bank_mask:0xf\n"
    "v_add_f32_dpp v71, v167, v71 row_newbcast:7 row_mask:0xf bank_mask:0xf\n"
    "v_cvt_pk_bf16_f32 v64, v64, v65\n"
    "v_cvt_pk_bf16_f32 v65, v66, v67\n"
    "v_cvt_pk_bf16_f32 v66, v68, v69\n"
    "v_cvt_pk_bf16_f32 v67, v70, v71\n"
    "s_add_u32 s78, s87, 0\n"
    "buffer_store_dwordx4 v[64:67], v211, s[36:39], s78 offen sc1\n"
    "s_waitcnt vmcnt(10)\n"
    "v_add_u32 v221, s74, v221\n"
    "s_mov_b32 s73, 0\n"
    "v_readfirstlane_b32 s67, v221\n"
    "s_mul_i32 s66, s74, 2\n"
    "s_waitcnt vmcnt(0)\n"
    "s_barrier\n"
    "v_mov_b32 v220, 2\n"
    "buffer_store_dwordx2 v[220:221], v214, s[44:47], 0 offen sc1\n"
    "s_add_u32 s78, s64, 0x18000\n"
    "s_add_u32 m0, s72, 0x1b000\n"
    "s_nop 0\n"
    "buffer_load_dwordx4 v202, s[32:35], s78 offen nt lds\n"
    "buffer_load_dwordx4 v203, s[32:35], s78 offen offset:1024 nt lds\n"
    "buffer_load_dwordx4 v204, s[32:35], s78 offen offset:2048 nt lds\n"
    "s_add_u32 s78, s88, 0\n"
    "s_add_u32 m0, s71, 0x1000\n"
    "s_nop 0\n"
    "buffer_load_dwordx4 v156, s[28:31], s78 offen nt lds\n"
    "s_add_u32 s78, s88, 0\n"
    "s_add_u32 m0, s71, 0x1400\n"
    "s_nop 0\n"
    "buffer_load_dwordx4 v157, s[28:31], s78 offen nt lds\n"
    "s_add_u32 s78, s88, 0x1800\n"
    "s_add_u32 m0, s71, 0x6000\n"
    "s_nop 0\n"
    "buffer_load_dwordx4 v152, s[28:31], s78 offen nt lds\n"
    "s_add_u32 s78, s88, 0x1800\n"
    "s_add_u32 m0, s71, 0x6400\n"
    "s_nop 0\n"
    "buffer_load_dwordx4 v153, s[28:31], s78 offen nt lds\n"
    "s_barrier\n"
    "ds_read_u16_d16_hi v163, v209 offset:6528\n"
    "ds_read_u16_d16_hi v162, v209 offset:384\n"
    "ds_read_b128 v[72:75], v205 offset:49152\n"
    "ds_read_b128 v[76:79], v206 offset:49152\n"
    "ds_read_b64 v[170:171], v210 offset:64\n"
    "ds_read2st64_b32 v[80:81], v207 offset0:192 offset1:195\n"
    "ds_read2st64_b32 v[82:83], v207 offset0:198 offset1:201\n"
    "ds_read2st64_b32 v[84:85], v207 offset0:204 offset1:207\n"
    "ds_read2st64_b32 v[86:87], v207 offset0:210 offset1:213\n"
    "s_waitcnt lgkmcnt(8)\n"
    "v_mul_f32 v168, s75, v163\n"
    "s_waitcnt lgkmcnt(7)\n"
    "v_fma_f32 v169, -v162, v168, v217\n"
    "s_waitcnt lgkmcnt(6)\n"
    "v_cvt_f32_i32 v72, v72\n"
    "v_cvt_f32_i32 v73, v73\n"
    "v_cvt_f32_i32 v74, v74\n"
    "v_cvt_f32_i32 v75, v75\n"
    "s_waitcnt lgkmcnt(5)\n"
    "v_cvt_f32_i32 v76, v76\n"
    "v_cvt_f32_i32 v77, v77\n"
    "v_cvt_f32_i32 v78, v78\n"
    "v_cvt_f32_i32 v79, v79\n"
    "v_mul_f32_dpp v72, v168, v72 row_newbcast:0 row_mask:0xf bank_mask:0xf\n"
    "v_mul_f32_dpp v73, v168, v73 row_newbcast:1 row_mask:0xf bank_mask:0xf\n"
    "v_mul_f32_dpp v74, v168, v74 row_newbcast:2 row_mask:0xf bank_mask:0xf\n"
    "v_mul_f32_dpp v75, v168, v75 row_newbcast:3 row_mask:0xf bank_mask:0xf\n"
    "v_mul_f32_dpp v76, v168, v76 row_newbcast:4 row_mask:0xf bank_mask:0xf\n"
    "v_mul_f32_dpp v77, v168, v77 row_newbcast:5 row_mask:0xf bank_mask:0xf\n"
    "v_mul_f32_dpp v78, v168, v78 row_newbcast:6 row_mask:0xf bank_mask:0xf\n"
    "v_mul_f32_dpp v79, v168, v79 row_newbcast:7 row_mask:0xf bank_mask:0xf\n"
    "v_add_f32_dpp v72, v169, v72 row_newbcast:0 row_mask:0xf bank_mask:0xf\n"
    "v_add_f32_dpp v73, v169, v73 row_newbcast:1 row_mask:0xf bank_mask:0xf\n"
    "v_add_f32_dpp v74, v169, v74 row_newbcast:2 row_mask:0xf bank_mask:0xf\n"
    "v_add_f32_dpp v75, v169, v75 row_newbcast:3 row_mask:0xf bank_mask:0xf\n"
    "v_add_f32_dpp v76, v169, v76 row_newbcast:4 row_mask:0xf bank_mask:0xf\n"
    "v_add_f32_dpp v77, v169, v77 row_newbcast:5 row_mask:0xf bank_mask:0xf\n"
    "v_add_f32_dpp v78, v169, v78 row_newbcast:6 row_mask:0xf bank_mask:0xf\n"
    "v_add_f32_dpp v79, v169, v79 row_newbcast:7 row_mask:0xf bank_mask:0xf\n"
    "v_cvt_pk_bf16_f32 v72, v72, v73\n"
    "v_cvt_pk_bf16_f32 v73, v74, v75\n"
    "v_cvt_pk_bf16_f32 v74, v76, v77\n"
    "v_cvt_pk_bf16_f32 v75, v78, v79\n"
    "s_add_u32 s78, s87, 0x6000\n"
    "buffer_store_dwordx4 v[72:75], v211, s[36:39], s78 offen sc1\n"
    "s_waitcnt lgkmcnt(3)\n"
    "v_cvt_f32_i32 v80, v80\n"
    "v_cvt_f32_i32 v81, v81\n"
    "s_waitcnt lgkmcnt(2)\n"
    "v_cvt_f32_i32 v82, v82\n"
    "v_cvt_f32_i32 v83, v83\n"
    "s_waitcnt lgkmcnt(1)\n"
    "v_cvt_f32_i32 v84, v84\n"
    "v_cvt_f32_i32 v85, v85\n"
    "s_waitcnt lgkmcnt(0)\n"
    "v_cvt_f32_i32 v86, v86\n"
    "v_cvt_f32_i32 v87, v87\n"
    "v_mul_f32_dpp v80, v170, v80 row_newbcast:0 row_mask:0xf bank_mask:0xf\n"
    "v_mul_f32_dpp v81, v170, v81 row_newbcast:1 row_mask:0xf bank_mask:0xf\n"
    "v_mul_f32_dpp v82, v170, v82 row_newbcast:2 row_mask:0xf bank_mask:0xf\n"
    "v_mul_f32_dpp v83, v170, v83 row_newbcast:3 row_mask:0xf bank_mask:0xf\n"
    "v_mul_f32_dpp v84, v170, v84 row_newbcast:4 row_mask:0xf bank_mask:0xf\n"
    "v_mul_f32_dpp v85, v170, v85 row_newbcast:5 row_mask:0xf bank_mask:0xf\n"
    "v_mul_f32_dpp v86, v170, v86 row_newbcast:6 row_mask:0xf bank_mask:0xf\n"
    "v_mul_f32_dpp v87, v170, v87 row_newbcast:7 row_mask:0xf bank_mask:0xf\n"
    "v_add_f32_dpp v80, v171, v80 row_newbcast:0 row_mask:0xf bank_mask:0xf\n"
    "v_add_f32_dpp v81, v171, v81 row_newbcast:1 row_mask:0xf bank_mask:0xf\n"
    "v_add_f32_dpp v82, v171, v82 row_newbcast:2 row_mask:0xf bank_mask:0xf\n"
    "v_add_f32_dpp v83, v171, v83 row_newbcast:3 row_mask:0xf bank_mask:0xf\n"
    "v_add_f32_dpp v84, v171, v84 row_newbcast:4 row_mask:0xf bank_mask:0xf\n"
    "v_add_f32_dpp v85, v171, v85 row_newbcast:5 row_mask:0xf bank_mask:0xf\n"
    "v_add_f32_dpp v86, v171, v86 row_newbcast:6 row_mask:0xf bank_mask:0xf\n"
    "v_add_f32_dpp v87, v171, v87 row_newbcast:7 row_mask:0xf bank_mask:0xf\n"
    "v_cvt_pk_bf16_f32 v80, v80, v81\n"
    "v_cvt_pk_bf16_f32 v81, v82, v83\n"
    "v_cvt_pk_bf16_f32 v82, v84, v85\n"
    "v_cvt_pk_bf16_f32 v83, v86, v87\n"
    "s_mov_b32 s78, 0x6000\n"
    "buffer_store_dwordx4 v[80:83], v212, s[40:43], s78 offen sc1\n"
    "buffer_load_dwordx2 v[218:219], v213, s[44:47], 0 offen sc1\n"
    "s_add_u32 s78, s88, 0x1800\n"
    "s_add_u32 m0, s71, 0x6800\n"
    "s_nop 0\n"
    "buffer_load_dwordx4 v154, s[28:31], s78 offen nt lds\n"
    "s_add_u32 s78, s88, 0x1800\n"
    "s_add_u32 m0, s71, 0x6c00\n"
    "s_nop 0\n"
    "buffer_load_dwordx4 v155, s[28:31], s78 offen nt lds\n"
    "s_add_u32 s78, s88, 0x1800\n"
    "s_add_u32 m0, s71, 0x7000\n"
    "s_nop 0\n"
    "buffer_load_dwordx4 v156, s[28:31], s78 offen nt lds\n"
    "s_add_u32 s78, s88, 0x1800\n"
    "s_add_u32 m0, s71, 0x7400\n"
    "s_nop 0\n"
    "buffer_load_dwordx4 v157, s[28:31], s78 offen nt lds\n"
    "s_waitcnt vmcnt(9)\n"
    "ds_read_b128 a[192:195], v158\n"
    "ds_read_b128 a[196:199], v159\n"
    "ds_read_b128 a[200:203], v158 offset:128\n"
    "ds_read_b128 a[204:207], v159 offset:128\n"
    "ds_read_b128 a[208:211], v158 offset:256\n"
    "ds_read_b128 a[212:215], v159 offset:256\n"
    "s_waitcnt vmcnt(4)\n"
    "v_cmp_ne_u32 s[80:81], s67, v219\n"
    "v_cmp_gt_u32 vcc, s66, v218\n"
    "s_or_b64 vcc, vcc, s[80:81]\n"
    "s_cbranch_vccnz LSPRE0\n"
    "LSPB0:\n"
    "s_waitcnt lgkmcnt(0)\n"
    "s_barrier\n"
    "v_mov_b32 v220, 3\n"
    "buffer_store_dwordx2 v[220:221], v214, s[44:47], 0 offen sc1\n"
    "s_add_u32 m0, s72, 0x1e000\n"
    "s_nop 0\n"
    "buffer_load_dwordx4 v200, s[36:39], s72 offen sc1 lds\n"
    "s_add_u32 m0, s72, 0x1e000\n"
    "s_nop 0\n"
    "buffer_load_dwordx4 v200, s[36:39], s72 offen offset:1024 sc1 lds\n"
    "s_add_u32 m0, s72, 0x1e000\n"
    "s_nop 0\n"
    "buffer_load_dwordx4 v200, s[36:39], s72 offen offset:2048 sc1 lds\n"
    "s_add_u32 s65, s71, 0x3000\n"
    "s_add_u32 m0, s71, 0\n"
    "s_nop 0\n"
    "buffer_load_dwordx4 v200, s[36:39], s65 offen sc1 lds\n"
    "s_add_u32 s65, s71, 0x3400\n"
    "s_add_u32 m0, s71, 0x400\n"
    "s_nop 0\n"
    "buffer_load_dwordx4 v200, s[36:39], s65 offen sc1 lds\n"
    "s_add_u32 s65, s71, 0x3800\n"
    "s_add_u32 m0, s71, 0x800\n"
    "s_nop 0\n"
    "buffer_load_dwordx4 v200, s[36:39], s65 offen sc1 lds\n"
    "s_add_u32 s65, s71, 0x3c00\n"
    "s_add_u32 m0, s71, 0xc00\n"
    "s_nop 0\n"
    "buffer_load_dwordx4 v200, s[36:39], s65 offen sc1 lds\n"
    "s_add_u32 s65, s71, 0x4000\n"
    "s_add_u32 m0, s71, 0x1000\n"
    "s_nop 0\n"
    "buffer_load_dwordx4 v200, s[36:39], s65 offen sc1 lds\n"
    "s_add_u32 s65, s71, 0x4400\n"
    "s_add_u32 m0, s71, 0x1400\n"
    "s_nop 0\n"
    "buffer_load_dwordx4 v200, s[36:39], s65 offen sc1 lds\n"
    "s_add_u32 s65, s71, 0\n"
    "s_add_u32 m0, s71, 0x12000\n"
    "s_nop 0\n"
    "buffer_load_dwordx4 v200, s[40:43], s65 offen sc1 lds\n"
    "s_add_u32 s65, s71, 0x400\n"
    "s_add_u32 m0, s71, 0x12400\n"
    "s_nop 0\n"
    "buffer_load_dwordx4 v200, s[40:43], s65 offen sc1 lds\n"
    "s_add_u32 s65, s71, 0x800\n"
    "s_add_u32 m0, s71, 0x12800\n"
    "s_nop 0\n"
    "buffer_load_dwordx4 v200, s[40:43], s65 offen sc1 lds\n"
    "s_add_u32 s65, s71, 0xc00\n"
    "s_add_u32 m0, s71, 0x12c00\n"
    "s_nop 0\n"
    "buffer_load_dwordx4 v200, s[40:43], s65 offen sc1 lds\n"
    "s_add_u32 s65, s71, 0x1000\n"
    "s_add_u32 m0, s71, 0x13000\n"
    "s_nop 0\n"
    "buffer_load_dwordx4 v200, s[40:43], s65 offen sc1 lds\n"
    "s_add_u32 s65, s71, 0x1400\n"
    "s_add_u32 m0, s71, 0x13400\n"
    "s_nop 0\n"
    "buffer_load_dwordx4 v200, s[40:43], s65 offen sc1 lds\n"
    "s_add_u32 s78, s64, 0x24000\n"
    "s_add_u32 m0, s72, 0x18000\n"
    "s_nop 0\n"
    "buffer_load_dwordx4 v202, s[32:35], s78 offen nt lds\n"
    "buffer_load_dwordx4 v203, s[32:35], s78 offen offset:1024 nt lds\n"
    "buffer_load_dwordx4 v204, s[32:35], s78 offen offset:2048 nt lds\n"
    "s_add_u32 s78, s88, 0x3000\n"
    "s_add_u32 m0, s71, 0xc000\n"
    "s_nop 0\n"
    "buffer_load_dwordx4 v152, s[28:31], s78 offen nt lds\n"
    "s_mul_i32 s66, s74, 3\n"
    "s_waitcnt vmcnt(20)\n"
    "ds_read_b128 a[216:219], v158 offset:24576\n"
    "ds_read_b128 a[220:223], v159 offset:24576\n"
    "ds_read_b128 a[224:227], v158 offset:24704\n"
    "ds_read_b128 a[228:231], v159 offset:24704\n"
    "ds_read_b128 a[232:235], v158 offset:24832\n"
    "ds_read_b128 a[236:239], v159 offset:24832\n"
    "s_add_u32 m0, s72, 0x21000\n"
    "s_nop 0\n"
    "buffer_load_dwordx4 v249, s[48:51], 0 offen offset:1024 lds\n"
    "buffer_load_dwordx2 v[218:219], v213, s[44:47], 0 offen sc1\n"
    "v_add_u32 v255, 0x1e000, v200\n"
    "s_add_u32 s60, s71, 0x9000\n"
    "s_add_u32 s61, s71, 0x6000\n"
    "s_add_u32 s62, s87, 0x12000\n"
    "s_mov_b32 s63, 0x12000\n"
    "s_mov_b32 s59, 0\n"
    "s_mul_i32 s66, s74, 3\n"
    "s_getpc_b64 s[52:53]\n"
    "LPC2:\n"
    "s_add_u32 s52, s52, LTAIL-LPC2\n"
    "s_addc_u32 s53, s53, 0\n"
    "s_mov_b32 s54, LSLOW-LTAIL\n"
    "s_lshr_b32 s91, %1, 3\n"
    "s_mul_i32 s91, s91, 4\n"
    "s_add_u32 s91, s91, s58\n"
    "s_sub_u32 s91, s91, 0\n"
    "s_lshl_b32 s91, s91, 9\n"
    "v_lshlrev_b32 v252, 3, v231\n"
    "s_mul_i32 s90, s58, 0x1c00\n"
    "s_sub_u32 s90, s90, 0x1000\n"
    "s_sub_u32 s92, s90, s71\n"
    "s_waitcnt vmcnt(12) lgkmcnt(0)\n"
    "s_barrier\n"
    "ds_read_b128 v[160:163], v255\n"
    "ds_read_b128 v[164:167], v255 offset:1024\n"
    "ds_read_b128 v[168:171], v255 offset:2048\n"
    "ds_read_b128 v[172:175], v255 offset:3072\n"
    "ds_read_b128 v[176:179], v255 offset:4096\n"
    "ds_read_b128 v[180:183], v255 offset:5120\n"
    "s_add_u32 s76, s87, 0xc000\n"
    "s_mov_b32 s77, 0xc000\n"
    "s_waitcnt lgkmcnt(4)\n"
    "v_mfma_f32_16x16x32_bf16 v[40:43], v[160:163], a[192:195], 0\n"
    "ds_read_b128 v[184:187], v255 offset:6144\n"
    "ds_read_b64 v[228:229], v210 offset:128\n"
    "v_mfma_f32_16x16x32_bf16 v[40:43], v[164:167], a[196:199], v[40:43]\n"
    "s_add_u32 s78, s88, 0x3000\n"
    "ds_read2st64_b32 v[240:241], v207 offset0:144 offset1:147\n"
    "ds_read2st64_b32 v[242:243], v207 offset0:150 offset1:153\n"
    "v_mfma_f32_16x16x32_bf16 v[44:47], v[160:163], a[216:219], 0\n"
    "s_add_u32 m0, s71, 0xc400\n"
    "ds_read_b128 v[188:191], v255 offset:7168\n"
    "ds_read2st64_b32 v[244:245], v207 offset0:156 offset1:159\n"
    "v_mfma_f32_16x16x32_bf16 v[44:47], v[164:167], a[220:223], v[44:47]\n"
    "s_waitcnt lgkmcnt(8)\n"
    "buffer_load_dwordx4 v153, s[28:31], s78 offen nt lds\n"
    "v_mfma_f32_16x16x32_bf16 v[40:43], v[168:171], a[200:203], v[40:43]\n"
    "ds_read_b128 v[160:163], v255 offset:8192\n"
    "ds_read2st64_b32 v[246:247], v207 offset0:162 offset1:165\n"
    "v_mfma_f32_16x16x32_bf16 v[40:43], v[172:175], a[204:207], v[40:43]\n"
    "ds_read_u16_d16_hi v223, v209 offset:6720\n"
    "ds_read_u16_d16_hi v222, v209 offset:576\n"
    "v_mfma_f32_16x16x32_bf16 v[44:47], v[168:171], a[224:227], v[44:47]\n"
    "ds_read_b128 v[164:167], v255 offset:9216\n"
    "ds_read_b128 v[232:235], v205 offset:36864\n"
    "v_mfma_f32_16x16x32_bf16 v[44:47], v[172:175], a[228:231], v[44:47]\n"
    "s_waitcnt lgkmcnt(12)\n"
    "ds_read_b128 v[236:239], v206 offset:36864\n"
    "v_mfma_f32_16x16x32_bf16 v[40:43], v[176:179], a[208:211], v[40:43]\n"
    "ds_read_b128 v[168:171], v255 offset:10240\n"
    "v_mfma_f32_16x16x32_bf16 v[40:43], v[180:183], a[212:215], v[40:43]\n"
    "v_mfma_f32_16x16x32_bf16 v[44:47], v[176:179], a[232:235], v[44:47]\n"
    "ds_read_b128 v[172:175], v255 offset:11264\n"
    "v_mfma_f32_16x16x32_bf16 v[44:47], v[180:183], a[236:239], v[44:47]\n"
    "s_waitcnt lgkmcnt(10)\n"
    "s_add_u32 s78, s88, 0x3000\n"
    "v_cvt_f32_i32 v240, v240\n"
    "v_mfma_f32_16x16x32_bf16 v[56:59], v[184:187], a[192:195], 0\n"
    "s_add_u32 m0, s71, 0xc800\n"
    "ds_read_b128 v[176:179], v200\n"
    "v_cvt_f32_i32 v241, v241\n"
    "v_mfma_f32_16x16x32_bf16 v[56:59], v[188:191], a[196:199], v[56:59]\n"
    "buffer_load_dwordx4 v154, s[28:31], s78 offen nt lds\n"
    "v_mfma_f32_16x16x32_bf16 v[60:63], v[184:187], a[216:219], 0\n"
    "ds_read_b128 v[180:183], v200 offset:1024\n"
    "v_cvt_f32_i32 v242, v242\n"
    "v_mfma_f32_16x16x32_bf16 v[60:63], v[188:191], a[220:223], v[60:63]\n"
    "s_waitcnt lgkmcnt(6)\n"
    "v_cvt_f32_i32 v243, v243\n"
    "v_cvt_f32_i32 v244, v244\n"
    "v_mfma_f32_16x16x32_bf16 v[56:59], v[160:163], a[200:203], v[56:59]\n"
    "ds_read_b128 v[184:187], v200 offset:2048\n"
    "v_cvt_f32_i32 v245, v245\n"
    "v_mfma_f32_16x16x32_bf16 v[56:59], v[164:167], a[204:207], v[56:59]\n"
    "v_cvt_f32_i32 v246, v246\n"
    "v_mul_f32 v226, s75, v223\n"
    "v_mfma_f32_16x16x32_bf16 v[60:63], v[160:163], a[224:227], v[60:63]\n"
    "ds_read_b128 v[188:191], v200 offset:3072\n"
    "v_cvt_f32_i32 v247, v247\n"
    "v_mfma_f32_16x16x32_bf16 v[60:63], v[164:167], a[228:231], v[60:63]\n"
    "s_waitcnt lgkmcnt(4)\n"
    "v_fma_f32 v227, -v222, v226, v217\n"
    "v_mul_f32_dpp v240, v228, v240 row_newbcast:0 row_mask:0xf bank_mask:0xf\n"
    "v_mfma_f32_16x16x32_bf16 v[56:59], v[168:171], a[208:211], v[56:59]\n"
    "ds_read_b128 v[160:163], v200 offset:4096\n"
    "v_cvt_f32_i32 v232, v232\n"
    "v_mfma_f32_16x16x32_bf16 v[56:59], v[172:175], a[212:215], v[56:59]\n"
    "s_add_u32 s78, s88, 0x3000\n"
    "v_mul_f32_dpp v241, v228, v241 row_newbcast:1 row_mask:0xf bank_mask:0xf\n"
    "v_cvt_f32_i32 v233, v233\n"
    "v_mfma_f32_16x16x32_bf16 v[60:63], v[168:171], a[232:235], v[60:63]\n"
    "s_add_u32 m0, s71, 0xcc00\n"
    "ds_read_b128 v[164:167], v200 offset:5120\n"
    "v_mul_f32_dpp v242, v228, v242 row_newbcast:2 row_mask:0xf bank_mask:0xf\n"
    "v_mfma_f32_16x16x32_bf16 v[60:63], v[172:175], a[236:239], v[60:63]\n"
    "s_waitcnt lgkmcnt(4)\n"
    "buffer_load_dwordx4 v155, s[28:31], s78 offen nt lds\n"
    "v_mfma_f32_16x16x32_bf16 v[64:67], v[176:179], a[192:195], 0\n"
    "ds_read_b128 v[168:171], v200 offset:6144\n"
    "v_cvt_f32_i32 v234, v234\n"
    "v_mul_f32_dpp v243, v228, v243 row_newbcast:3 row_mask:0xf bank_mask:0xf\n"
    "v_cvt_f32_i32 v235, v235\n"
    "v_mfma_f32_16x16x32_bf16 v[64:67], v[180:183], a[196:199], v[64:67]\n"
    "v_mul_f32_dpp v244, v228, v244 row_newbcast:4 row_mask:0xf bank_mask:0xf\n"
    "v_cvt_f32_i32 v236, v236\n"
    "v_mul_f32_dpp v245, v228, v245 row_newbcast:5 row_mask:0xf bank_mask:0xf\n"
    "v_cvt_f32_i32 v237, v237\n"
    "v_mfma_f32_16x16x32_bf16 v[68:71], v[176:179], a[216:219], 0\n"
    "ds_read_b128 v[172:175], v200 offset:7168\n"
    "v_mul_f32_dpp v246, v228, v246 row_newbcast:6 row_mask:0xf bank_mask:0xf\n"
    "v_cvt_f32_i32 v238, v238\n"
    "v_mul_f32_dpp v247, v228, v247 row_newbcast:7 row_mask:0xf bank_mask:0xf\n"
    "v_mfma_f32_16x16x32_bf16 v[68:71], v[180:183], a[220:223], v[68:71]\n"
    "s_waitcnt lgkmcnt(4)\n"
    "v_cvt_f32_i32 v239, v239\n"
    "v_add_f32_dpp v240, v229, v240 row_newbcast:0 row_mask:0xf bank_mask:0xf\n"
    "v_mul_f32_dpp v232, v226, v232 row_newbcast:0 row_mask:0xf bank_mask:0xf\n"
    "v_add_f32_dpp v241, v229, v241 row_newbcast:1 row_mask:0xf bank_mask:0xf\n"
    "v_mfma_f32_16x16x32_bf16 v[64:67], v[184:187], a[200:203], v[64:67]\n"
    "ds_read_b128 v[176:179], v200 offset:8192\n"
    "v_mul_f32_dpp v233, v226, v233 row_newbcast:1 row_mask:0xf bank_mask:0xf\n"
    "v_add_f32_dpp v242, v229, v242 row_newbcast:2 row_mask:0xf bank_mask:0xf\n"
    "v_mul_f32_dpp v234, v226, v234 row_newbcast:2 row_mask:0xf bank_mask:0xf\n"
    "v_mfma_f32_16x16x32_bf16 v[64:67], v[188:191], a[204:207], v[64:67]\n"
    "v_add_f32_dpp v243, v229, v243 row_newbcast:3 row_mask:0xf bank_mask:0xf\n"
    "v_mul_f32_dpp v235, v226, v235 row_newbcast:3 row_mask:0xf bank_mask:0xf\n"
    "v_add_f32_dpp v244, v229, v244 row_newbcast:4 row_mask:0xf bank_mask:0xf\n"
    "v_mul_f32_dpp v236, v226, v236 row_newbcast:4 row_mask:0xf bank_mask:0xf\n"
    "v_mfma_f32_16x16x32_bf16 v[68:71], v[184:187], a[224:227], v[68:71]\n"
    "ds_read_b128 v[180:183], v200 offset:9216\n"
    "v_add_f32_dpp v245, v229, v245 row_newbcast:5 row_mask:0xf bank_mask:0xf\n"
    "v_mul_f32_dpp v237, v226, v237 row_newbcast:5 row_mask:0xf bank_mask:0xf\n"
    "v_add_f32_dpp v246, v229, v246 row_newbcast:6 row_mask:0xf bank_mask:0xf\n"
    "v_mul_f32_dpp v238, v226, v238 row_newbcast:6 row_mask:0xf bank_mask:0xf\n"
    "v_add_f32_dpp v247, v229, v247 row_newbcast:7 row_mask:0xf bank_mask:0xf\n"
    "v_mul_f32_dpp v239, v226, v239 row_newbcast:7 row_mask:0xf bank_mask:0xf\n"
    "v_cvt_pk_bf16_f32 v240, v240, v241\n"
    "v_add_f32_dpp v232, v227, v232 row_newbcast:0 row_mask:0xf bank_mask:0xf\n"
    "v_cvt_pk_bf16_f32 v241, v242, v243\n"
    "v_add_f32_dpp v233, v227, v233 row_newbcast:1 row_mask:0xf bank_mask:0xf\n"
    "v_cvt_pk_bf16_f32 v242, v244, v245\n"
    "v_add_f32_dpp v234, v227, v234 row_newbcast:2 row_mask:0xf bank_mask:0xf\n"
    "v_cvt_pk_bf16_f32 v243, v246, v247\n"
    "v_add_f32_dpp v235, v227, v235 row_newbcast:3 row_mask:0xf bank_mask:0xf\n"
    "v_add_f32_dpp v236, v227, v236 row_newbcast:4 row_mask:0xf bank_mask:0xf\n"
    "v_add_f32_dpp v237, v227, v237 row_newbcast:5 row_mask:0xf bank_mask:0xf\n"
    "v_add_f32_dpp v238, v227, v238 row_newbcast:6 row_mask:0xf bank_mask:0xf\n"
    "v_add_f32_dpp v239, v227, v239 row_newbcast:7 row_mask:0xf bank_mask:0xf\n"
    "v_cvt_pk_bf16_f32 v232, v232, v233\n"
    "v_cvt_pk_bf16_f32 v233, v234, v235\n"
    "v_cvt_pk_bf16_f32 v234, v236, v237\n"
    "v_cvt_pk_bf16_f32 v235, v238, v239\n"
    "v_mfma_f32_16x16x32_bf16 v[68:71], v[188:191], a[228:231], v[68:71]\n"
    "s_waitcnt lgkmcnt(4)\n"
    "s_add_u32 s78, s88, 0x3000\n"
    "v_max3_f32 v196, v40, v41, v42\n"
    "v_max3_f32 v196, v196, v43, v56\n"
    "v_max3_f32 v196, v196, v57, v58\n"
    "buffer_store_dwordx4 v[232:235], v211, s[36:39], s76 offen sc1\n"
    "v_mfma_f32_16x16x32_bf16 v[64:67], v[160:163], a[208:211], v[64:67]\n"
    "s_add_u32 m0, s71, 0xd000\n"
    "ds_read_b128 v[184:187], v200 offset:10240\n"
    "v_max_f32 v196, v196, v59\n"
    "v_max3_f32 v198, v44, v45, v46\n"
    "v_mov_b32 v197, v196\n"
    "v_mfma_f32_16x16x32_bf16 v[64:67], v[164:167], a[212:215], v[64:67]\n"
    "buffer_load_dwordx4 v156, s[28:31], s78 offen nt lds\n"
    "v_max3_f32 v198, v198, v47, v60\n"
    "v_permlane32_swap_b32 v196, v197\n"
    "v_mfma_f32_16x16x32_bf16 v[68:71], v[160:163], a[232:235], v[68:71]\n"
    "ds_read_b128 v[188:191], v200 offset:11264\n"
    "v_max3_f32 v198, v198, v61, v62\n"
    "v_max_f32 v196, v196, v197\n"
    "v_max_f32 v198, v198, v63\n"
    "v_mfma_f32_16x16x32_bf16 v[68:71], v[164:167], a[236:239], v[68:71]\n"
    "s_waitcnt lgkmcnt(4)\n"
    "v_mov_b32 v197, v196\n"
    "v_mov_b32 v199, v198\n"
    "s_nop 0\n"
    "v_permlane16_swap_b32 v196, v197\n"
    "v_permlane32_swap_b32 v198, v199\n"
    "v_mfma_f32_16x16x32_bf16 v[80:83], v[168:171], a[192:195], 0\n"
    "ds_read_b128 v[160:163], v200 offset:12288\n"
    "v_max_f32 v196, v196, v197\n"
    "v_max_f32 v198, v198, v199\n"
    "v_add_f32 v196, 0x42640000, v196\n"
    "v_mfma_f32_16x16x32_bf16 v[80:83], v[172:175], a[196:199], v[80:83]\n"
    "buffer_store_dwordx4 v[240:243], v212, s[40:43], s77 offen sc1\n"
    "v_mov_b32 v199, v198\n"
    "v_sub_f32 v96, 0, v196\n"
    "s_nop 0\n"
    "v_permlane16_swap_b32 v198, v199\n"
    "v_sub_f32 v97, 0, v196\n"
    "v_max_f32 v198, v198, v199\n"
    "v_mfma_f32_16x16x32_bf16 v[84:87], v[168:171], a[216:219], 0\n"
    "ds_read_b128 v[164:167], v200 offset:13312\n"
    "v_sub_f32 v98, 0, v196\n"
    "v_add_f32 v198, 0x42640000, v198\n"
    "v_sub_f32 v99, 0, v196\n"
    "v_mfma_f32_16x16x32_bf16 v[84:87], v[172:175], a[220:223], v[84:87]\n"
    "s_waitcnt lgkmcnt(4)\n"
    "v_sub_f32 v100, 0, v198\n"
    "v_add_f32 v40, v40, v96\n"
    "v_sub_f32 v101, 0, v198\n"
    "v_add_f32 v41, v41, v96\n"
    "v_mfma_f32_16x16x32_bf16 v[80:83], v[176:179], a[200:203], v[80:83]\n"
    "ds_read_b128 v[168:171], v200 offset:14336\n"
    "v_sub_f32 v102, 0, v198\n"
    "v_add_f32 v42, v42, v96\n"
    "v_sub_f32 v103, 0, v198\n"
    "v_mfma_f32_16x16x32_bf16 v[80:83], v[180:183], a[204:207], v[80:83]\n"
    "s_add_u32 s78, s88, 0x3000\n"
    "v_add_f32 v43, v43, v96\n"
    "v_add_f32 v44, v44, v100\n"
    "v_add_f32 v56, v56, v96\n"
    "v_add_f32 v45, v45, v100\n"
    "v_mfma_f32_16x16x32_bf16 v[84:87], v[176:179], a[224:227], v[84:87]\n"
    "s_add_u32 m0, s71, 0xd400\n"
    "ds_read_b128 v[172:175], v200 offset:15360\n"
    "v_add_f32 v57, v57, v96\n"
    "v_add_f32 v46, v46, v100\n"
    "v_add_f32 v58, v58, v96\n"
    "v_mfma_f32_16x16x32_bf16 v[84:87], v[180:183], a[228:231], v[84:87]\n"
    "s_waitcnt lgkmcnt(4)\n"
    "buffer_load_dwordx4 v157, s[28:31], s78 offen nt lds\n"
    "v_add_f32 v47, v47, v100\n"
    "v_add_f32 v59, v59, v96\n"
    "v_mfma_f32_16x16x32_bf16 v[80:83], v[184:187], a[208:211], v[80:83]\n"
    "ds_read_b128 v[176:179], v200 offset:16384\n"
    "v_add_f32 v60, v60, v100\n"
    "v_exp_f32 v40, v40\n"
    "v_mfma_f32_16x16x32_bf16 v[80:83], v[188:191], a[212:215], v[80:83]\n"
    "v_add_f32 v61, v61, v100\n"
    "v_add_f32 v62, v62, v100\n"
    "v_exp_f32 v41, v41\n"
    "v_mfma_f32_16x16x32_bf16 v[84:87], v[184:187], a[232:235], v[84:87]\n"
    "ds_read_b128 v[180:183], v200 offset:17408\n"
    "v_add_f32 v63, v63, v100\n"
    "v_exp_f32 v44, v44\n"
    "v_mfma_f32_16x16x32_bf16 v[84:87], v[188:191], a[236:239], v[84:87]\n"
    "s_waitcnt lgkmcnt(4)\n"
    "v_exp_f32 v42, v42\n"
    "v_exp_f32 v45, v45\n"
    "v_mfma_f32_16x16x32_bf16 v[32:35], v[160:163], a[192:195], v[96:99]\n"
    "ds_read_b128 v[184:187], v200 offset:18432\n"
    "v_exp_f32 v43, v43\n"
    "v_mfma_f32_16x16x32_bf16 v[32:35], v[164:167], a[196:199], v[32:35]\n"
    "v_exp_f32 v46, v46\n"
    "v_exp_f32 v56, v56\n"
    "v_mfma_f32_16x16x32_bf16 v[36:39], v[160:163], a[216:219], v[100:103]\n"
    "ds_read_b128 v[188:191], v200 offset:19456\n"
    "v_exp_f32 v47, v47\n"
    "v_mfma_f32_16x16x32_bf16 v[36:39], v[164:167], a[220:223], v[36:39]\n"
    "s_waitcnt lgkmcnt(4)\n"
    "s_add_u32 s78, s88, 0x4800\n"
    "v_exp_f32 v57, v57\n"
    "v_mfma_f32_16x16x32_bf16 v[32:35], v[168:171], a[200:203], v[32:35]\n"
    "s_add_u32 m0, s71, 0x6000\n"
    "ds_read_b128 v[160:163], v200 offset:20480\n"
    "v_exp_f32 v60, v60\n"
    "v_mfma_f32_16x16x32_bf16 v[32:35], v[172:175], a[204:207], v[32:35]\n"
    "buffer_load_dwordx4 v152, s[28:31], s78 offen nt lds\n"
    "v_exp_f32 v58, v58\n"
    "v_mfma_f32_16x16x32_bf16 v[36:39], v[168:171], a[224:227], v[36:39]\n"
    "ds_read_b128 v[164:167], v200 offset:21504\n"
    "v_exp_f32 v61, v61\n"
    "v_mfma_f32_16x16x32_bf16 v[36:39], v[172:175], a[228:231], v[36:39]\n"
    "s_waitcnt lgkmcnt(4)\n"
    "v_exp_f32 v59, v59\n"
    "v_exp_f32 v62, v62\n"
    "v_mfma_f32_16x16x32_bf16 v[32:35], v[176:179], a[208:211], v[32:35]\n"
    "ds_read_b128 v[168:171], v200 offset:22528\n"
    "v_cvt_pk_bf16_f32 v128, v40, v41\n"
    "v_cvt_pk_bf16_f32 v129, v42, v43\n"
    "v_cvt_pk_bf16_f32 v130, v56, v57\n"
    "v_mfma_f32_16x16x32_bf16 v[32:35], v[180:183], a[212:215], v[32:35]\n"
    "v_exp_f32 v63, v63\n"
    "v_cvt_pk_bf16_f32 v131, v58, v59\n"
    "v_cvt_pk_bf16_f32 v132, v44, v45\n"
    "v_mfma_f32_16x16x32_bf16 v[36:39], v[176:179], a[232:235], v[36:39]\n"
    "ds_read_b128 v[172:175], v200 offset:23552\n"
    "v_add_f32 v64, v64, v96\n"
    "v_cvt_pk_bf16_f32 v133, v46, v47\n"
    "v_add_f32 v65, v65, v96\n"
    "v_mfma_f32_16x16x32_bf16 v[36:39], v[180:183], a[236:239], v[36:39]\n"
    "s_waitcnt lgkmcnt(4)\n"
    "v_cvt_pk_bf16_f32 v134, v60, v61\n"
    "v_add_f32 v66, v66, v96\n"
    "s_waitcnt vmcnt(11)\n"
    "s_barrier\n"
    "v_mfma_f32_16x16x32_bf16 v[48:51], v[184:187], a[192:195], v[96:99]\n"
    "ds_read_b128 v[176:179], v201 offset:24576\n"
    "v_cvt_pk_bf16_f32 v135, v62, v63\n"
    "v_add_f32 v67, v67, v96\n"
    "v_add_f32 v68, v68, v100\n"
    "v_mfma_f32_16x16x32_bf16 v[48:51], v[188:191], a[196:199], v[48:51]\n"
    "s_add_u32 s78, s88, 0x4800\n"
    "v_add_f32 v80, v80, v96\n"
    "v_add_f32 v69, v69, v100\n"
    "v_add_f32 v81, v81, v96\n"
    "v_add_f32 v70, v70, v100\n"
    "v_mfma_f32_16x16x32_bf16 v[52:55], v[184:187], a[216:219], v[100:103]\n"
    "s_add_u32 m0, s71, 0x6400\n"
    "ds_read_b128 v[180:183], v201 offset:25600\n"
    "v_add_f32 v82, v82, v96\n"
    "v_add_f32 v71, v71, v100\n"
    "v_add_f32 v83, v83, v96\n"
    "v_mfma_f32_16x16x32_bf16 v[52:55], v[188:191], a[220:223], v[52:55]\n"
    "s_waitcnt lgkmcnt(4)\n"
    "buffer_load_dwordx4 v153, s[28:31], s78 offen nt lds\n"
    "v_add_f32 v84, v84, v100\n"
    "v_add_f32 v85, v85, v100\n"
    "v_mfma_f32_16x16x32_bf16 v[48:51], v[160:163], a[200:203], v[48:51]\n"
    "ds_read_b128 v[184:187], v201 offset:26624\n"
    "v_exp_f32 v64, v64\n"
    "v_add_f32 v86, v86, v100\n"
    "v_mfma_f32_16x16x32_bf16 v[48:51], v[164:167], a[204:207], v[48:51]\n"
    "v_exp_f32 v65, v65\n"
    "v_add_f32 v87, v87, v100\n"
    "ds_read_b64 v[228:229], v210 offset:192\n"
    "v_mfma_f32_16x16x32_bf16 v[52:55], v[160:163], a[224:227], v[52:55]\n"
    "ds_read_b128 v[188:191], v201 offset:27648\n"
    "v_exp_f32 v68, v68\n"
    "ds_read2st64_b32 v[240:241], v207 offset0:96 offset1:99\n"
    "v_mfma_f32_16x16x32_bf16 v[52:55], v[164:167], a[228:231], v[52:55]\n"
    "s_waitcnt lgkmcnt(6)\n"
    "v_exp_f32 v66, v66\n"
    "v_exp_f32 v69, v69\n"
    "v_mfma_f32_16x16x32_bf16 v[48:51], v[168:171], a[208:211], v[48:51]\n"
    "ds_read_b128 v[160:163], v201 offset:28672\n"
    "v_exp_f32 v67, v67\n"
    "ds_read2st64_b32 v[242:243], v207 offset0:102 offset1:105\n"
    "v_mfma_f32_16x16x32_bf16 v[48:51], v[172:175], a[212:215], v[48:51]\n"
    "v_exp_f32 v70, v70\n"
    "v_exp_f32 v80, v80\n"
    "v_mfma_f32_16x16x32_bf16 v[52:55], v[168:171], a[232:235], v[52:55]\n"
    "ds_read_b128 v[164:167], v201 offset:29696\n"
    "v_exp_f32 v71, v71\n"
    "ds_read2st64_b32 v[244:245], v207 offset0:108 offset1:111\n"
    "v_exp_f32 v81, v81\n"
    "v_exp_f32 v82, v82\n"
    "v_exp_f32 v83, v83\n"
    "v_cvt_pk_bf16_f32 v144, v64, v65\n"
    "v_cvt_pk_bf16_f32 v145, v66, v67\n"
    "v_cvt_pk_bf16_f32 v146, v80, v81\n"
    "v_cvt_pk_bf16_f32 v147, v82, v83\n"
    "v_mfma_f32_16x16x32_bf16 v[52:55], v[172:175], a[236:239], v[52:55]\n"
    "s_waitcnt lgkmcnt(8)\n"
    "s_add_u32 s78, s88, 0x4800\n"
    "v_exp_f32 v84, v84\n"
    "ds_read2st64_b32 v[246:247], v207 offset0:114 offset1:117\n"
    "v_mfma_f32_16x16x32_bf16 a[0:3], v[176:179], v[128:131], 0\n"
    "s_add_u32 m0, s71, 0x6800\n"
    "ds_read_b128 v[168:171], v201 offset:30720\n"
    "ds_read_u16_d16_hi v223, v209 offset:6912\n"
    "v_exp_f32 v85, v85\n"
    "v_exp_f32 v86, v86\n"
    "v_exp_f32 v87, v87\n"
    "v_cvt_pk_bf16_f32 v148, v68, v69\n"
    "v_cvt_pk_bf16_f32 v149, v70, v71\n"
    "v_cvt_pk_bf16_f32 v150, v84, v85\n"
    "v_cvt_pk_bf16_f32 v151, v86, v87\n"
    "v_mfma_f32_16x16x32_bf16 a[0:3], v[180:183], v[144:147], a[0:3]\n"
    "buffer_load_dwordx4 v154, s[28:31], s78 offen nt lds\n"
    "v_mfma_f32_16x16x32_bf16 a[4:7], v[176:179], v[132:135], 0\n"
    "ds_read_b128 v[172:175], v201 offset:31744\n"
    "ds_read_u16_d16_hi v222, v209 offset:768\n"
    "v_mfma_f32_16x16x32_bf16 a[4:7], v[180:183], v[148:151], a[4:7]\n"
    "s_waitcnt lgkmcnt(10)\n"
    "v_exp_f32 v32, v32\n"
    "v_mfma_f32_16x16x32_bf16 a[16:19], v[184:187], v[128:131], 0\n"
    "ds_read_b128 v[176:179], v201 offset:32768\n"
    "ds_read_b128 v[232:235], v205 offset:24576\n"
    "v_mfma_f32_16x16x32_bf16 a[16:19], v[188:191], v[144:147], a[16:19]\n"
    "ds_read_b128 v[236:239], v206 offset:24576\n"
    "v_mfma_f32_16x16x32_bf16 a[20:23], v[184:187], v[132:135], 0\n"
    "ds_read_b128 v[180:183], v201 offset:33792\n"
    "s_waitcnt lgkmcnt(13)\n"
    "v_cvt_f32_i32 v240, v240\n"
    "v_mfma_f32_16x16x32_bf16 a[20:23], v[188:191], v[148:151], a[20:23]\n"
    "s_waitcnt lgkmcnt(10)\n"
    "v_exp_f32 v33, v33\n"
    "v_mfma_f32_16x16x32_bf16 a[32:35], v[160:163], v[128:131], 0\n"
    "ds_read_b128 v[184:187], v201 offset:34816\n"
    "v_cvt_f32_i32 v241, v241\n"
    "v_mfma_f32_16x16x32_bf16 a[32:35], v[164:167], v[144:147], a[32:35]\n"
    "s_add_u32 s78, s88, 0x4800\n"
    "v_exp_f32 v34, v34\n"
    "v_mfma_f32_16x16x32_bf16 a[36:39], v[160:163], v[132:135], 0\n"
    "s_add_u32 m0, s71, 0x6c00\n"
    "ds_read_b128 v[188:191], v201 offset:35840\n"
    "v_cvt_f32_i32 v242, v242\n"
    "v_mfma_f32_16x16x32_bf16 a[36:39], v[164:167], v[148:151], a[36:39]\n"
    "buffer_load_dwordx4 v155, s[28:31], s78 offen nt lds\n"
    "v_mfma_f32_16x16x32_bf16 v[112:115], v[192:195], v[128:131], 0\n"
    "v_exp_f32 v35, v35\n"
    "v_mfma_f32_16x16x32_bf16 v[112:115], v[192:195], v[144:147], v[112:115]\n"
    "s_waitcnt lgkmcnt(7)\n"
    "v_exp_f32 v36, v36\n"
    "v_mfma_f32_16x16x32_bf16 a[48:51], v[168:171], v[128:131], 0\n"
    "ds_read_b128 v[160:163], v201 offset:36864\n"
    "v_cvt_f32_i32 v243, v243\n"
    "v_mfma_f32_16x16x32_bf16 a[48:51], v[172:175], v[144:147], a[48:51]\n"
    "v_cvt_f32_i32 v244, v244\n"
    "v_cvt_f32_i32 v245, v245\n"
    "v_mfma_f32_16x16x32_bf16 a[52:55], v[168:171], v[132:135], 0\n"
    "ds_read_b128 v[164:167], v201 offset:37888\n"
    "v_cvt_f32_i32 v246, v246\n"
    "v_mfma_f32_16x16x32_bf16 a[52:55], v[172:175], v[148:151], a[52:55]\n"
    "v_exp_f32 v37, v37\n"
    "v_mfma_f32_16x16x32_bf16 v[116:119], v[192:195], v[132:135], 0\n"
    "v_exp_f32 v38, v38\n"
    "v_mfma_f32_16x16x32_bf16 v[116:119], v[192:195], v[148:151], v[116:119]\n"
    "s_waitcnt lgkmcnt(4)\n"
    "s_add_u32 s78, s88, 0x4800\n"
    "v_mul_f32 v226, s75, v223\n"
    "v_mfma_f32_16x16x32_bf16 a[64:67], v[176:179], v[128:131], 0\n"
    "s_add_u32 m0, s71, 0x7000\n"
    "ds_read_b128 v[168:171], v201 offset:38912\n"
    "v_cvt_f32_i32 v247, v247\n"
    "v_mfma_f32_16x16x32_bf16 a[64:67], v[180:183], v[144:147], a[64:67]\n"
    "buffer_load_dwordx4 v156, s[28:31], s78 offen nt lds\n"
    "v_mfma_f32_16x16x32_bf16 a[68:71], v[176:179], v[132:135], 0\n"
    "ds_read_b128 v[172:175], v201 offset:39936\n"
    "v_fma_f32 v227, -v222, v226, v217\n"
    "v_mfma_f32_16x16x32_bf16 a[68:71], v[180:183], v[148:151], a[68:71]\n"
    "s_waitcnt lgkmcnt(4)\n"
    "v_mul_f32_dpp v240, v228, v240 row_newbcast:0 row_mask:0xf bank_mask:0xf\n"
    "v_cvt_f32_i32 v232, v232\n"
    "v_mfma_f32_16x16x32_bf16 a[80:83], v[184:187], v[128:131], 0\n"
    "ds_read_b128 v[176:179], v201 offset:40960\n"
    "v_mul_f32_dpp v241, v228, v241 row_newbcast:1 row_mask:0xf bank_mask:0xf\n"
    "v_mfma_f32_16x16x32_bf16 a[80:83], v[188:191], v[144:147], a[80:83]\n"
    "v_exp_f32 v39, v39\n"
    "v_mfma_f32_16x16x32_bf16 a[84:87], v[184:187], v[132:135], 0\n"
    "ds_read_b128 v[180:183], v201 offset:41984\n"
    "v_cvt_f32_i32 v233, v233\n"
    "v_mfma_f32_16x16x32_bf16 a[84:87], v[188:191], v[148:151], a[84:87]\n"
    "s_waitcnt lgkmcnt(4)\n"
    "v_mul_f32_dpp v242, v228, v242 row_newbcast:2 row_mask:0xf bank_mask:0xf\n"
    "v_cvt_f32_i32 v234, v234\n"
    "v_mfma_f32_16x16x32_bf16 a[96:99], v[160:163], v[128:131], 0\n"
    "ds_read_b128 v[184:187], v201 offset:43008\n"
    "v_mul_f32_dpp v243, v228, v243 row_newbcast:3 row_mask:0xf bank_mask:0xf\n"
    "v_mfma_f32_16x16x32_bf16 a[96:99], v[164:167], v[144:147], a[96:99]\n"
    "s_add_u32 s78, s88, 0x4800\n"
    "v_exp_f32 v48, v48\n"
    "v_mfma_f32_16x16x32_bf16 a[100:103], v[160:163], v[132:135], 0\n"
    "s_add_u32 m0, s71, 0x7400\n"
    "ds_read_b128 v[188:191], v201 offset:44032\n"
    "v_cvt_f32_i32 v235, v235\n"
    "v_mfma_f32_16x16x32_bf16 a[100:103], v[164:167], v[148:151], a[100:103]\n"
    "s_waitcnt lgkmcnt(4)\n"
    "buffer_load_dwordx4 v157, s[28:31], s78 offen nt lds\n"
    "v_mfma_f32_16x16x32_bf16 a[112:115], v[168:171], v[128:131], 0\n"
    "ds_read_b128 v[160:163], v201 offset:45056\n"
    "v_mul_f32_dpp v244, v228, v244 row_newbcast:4 row_mask:0xf bank_mask:0xf\n"
    "v_mfma_f32_16x16x32_bf16 a[112:115], v[172:175], v[144:147], a[112:115]\n"
    "v_exp_f32 v49, v49\n"
    "v_mfma_f32_16x16x32_bf16 a[116:119], v[168:171], v[132:135], 0\n"
    "ds_read_b128 v[164:167], v201 offset:46080\n"
    "v_cvt_f32_i32 v236, v236\n"
    "v_mfma_f32_16x16x32_bf16 a[116:119], v[172:175], v[148:151], a[116:119]\n"
    "s_waitcnt lgkmcnt(4)\n"
    "v_mul_f32_dpp v245, v228, v245 row_newbcast:5 row_mask:0xf bank_mask:0xf\n"
    "v_cvt_f32_i32 v237, v237\n"
    "v_mfma_f32_16x16x32_bf16 a[128:131], v[176:179], v[128:131], 0\n"
    "ds_read_b128 v[168:171], v201 offset:47104\n"
    "v_mul_f32_dpp v246, v228, v246 row_newbcast:6 row_mask:0xf bank_mask:0xf\n"
    "v_mfma_f32_16x16x32_bf16 a[128:131], v[180:183], v[144:147], a[128:131]\n"
    "v_exp_f32 v50, v50\n"
    "v_mfma_f32_16x16x32_bf16 a[132:135], v[176:179], v[132:135], 0\n"
    "ds_read_b128 v[172:175], v201 offset:48128\n"
    "v_cvt_f32_i32 v238, v238\n"
    "v_mfma_f32_16x16x32_bf16 a[132:135], v[180:183], v[148:151], a[132:135]\n"
    "s_waitcnt lgkmcnt(4)\n"
    "v_mul_f32_dpp v247, v228, v247 row_newbcast:7 row_mask:0xf bank_mask:0xf\n"
    "v_cvt_f32_i32 v239, v239\n"
    "v_mfma_f32_16x16x32_bf16 a[144:147], v[184:187], v[128:131], 0\n"
    "v_exp_f32 v51, v51\n"
    "v_mfma_f32_16x16x32_bf16 a[144:147], v[188:191], v[144:147], a[144:147]\n"
    "v_add_f32_dpp v240, v229, v240 row_newbcast:0 row_mask:0xf bank_mask:0xf\n"
    "v_mul_f32_dpp v232, v226, v232 row_newbcast:0 row_mask:0xf bank_mask:0xf\n"
    "v_mfma_f32_16x16x32_bf16 a[148:151], v[184:187], v[132:135], 0\n"
    "v_add_f32_dpp v241, v229, v241 row_newbcast:1 row_mask:0xf bank_mask:0xf\n"
    "v_mul_f32_dpp v233, v226, v233 row_newbcast:1 row_mask:0xf bank_mask:0xf\n"
    "v_mfma_f32_16x16x32_bf16 a[148:151], v[188:191], v[148:151], a[148:151]\n"
    "s_waitcnt lgkmcnt(2)\n"
    "v_exp_f32 v52, v52\n"
    "v_mfma_f32_16x16x32_bf16 a[160:163], v[160:163], v[128:131], 0\n"
    "v_add_f32_dpp v242, v229, v242 row_newbcast:2 row_mask:0xf bank_mask:0xf\n"
    "v_mul_f32_dpp v234, v226, v234 row_newbcast:2 row_mask:0xf bank_mask:0xf\n"
    "v_mfma_f32_16x16x32_bf16 a[160:163], v[164:167], v[144:147], a[160:163]\n"
    "v_add_f32_dpp v243, v229, v243 row_newbcast:3 row_mask:0xf bank_mask:0xf\n"
    "v_mul_f32_dpp v235, v226, v235 row_newbcast:3 row_mask:0xf bank_mask:0xf\n"
    "v_mfma_f32_16x16x32_bf16 a[164:167], v[160:163], v[132:135], 0\n"
    "v_exp_f32 v53, v53\n"
    "v_mfma_f32_16x16x32_bf16 a[164:167], v[164:167], v[148:151], a[164:167]\n"
    "s_waitcnt lgkmcnt(0)\n"
    "v_add_f32_dpp v244, v229, v244 row_newbcast:4 row_mask:0xf bank_mask:0xf\n"
    "v_mul_f32_dpp v236, v226, v236 row_newbcast:4 row_mask:0xf bank_mask:0xf\n"
    "v_mfma_f32_16x16x32_bf16 a[176:179], v[168:171], v[128:131], 0\n"
    "v_add_f32_dpp v245, v229, v245 row_newbcast:5 row_mask:0xf bank_mask:0xf\n"
    "v_mul_f32_dpp v237, v226, v237 row_newbcast:5 row_mask:0xf bank_mask:0xf\n"
    "v_mfma_f32_16x16x32_bf16 a[176:179], v[172:175], v[144:147], a[176:179]\n"
    "v_exp_f32 v54, v54\n"
    "v_mfma_f32_16x16x32_bf16 a[180:183], v[168:171], v[132:135], 0\n"
    "v_add_f32_dpp v246, v229, v246 row_newbcast:6 row_mask:0xf bank_mask:0xf\n"
    "v_mul_f32_dpp v238, v226, v238 row_newbcast:6 row_mask:0xf bank_mask:0xf\n"
    "v_add_f32_dpp v247, v229, v247 row_newbcast:7 row_mask:0xf bank_mask:0xf\n"
    "v_exp_f32 v55, v55\n"
    "v_mul_f32_dpp v239, v226, v239 row_newbcast:7 row_mask:0xf bank_mask:0xf\n"
    "v_cvt_pk_bf16_f32 v240, v240, v241\n"
    "v_add_f32_dpp v232, v227, v232 row_newbcast:0 row_mask:0xf bank_mask:0xf\n"
    "v_cvt_pk_bf16_f32 v241, v242, v243\n"
    "v_add_f32_dpp v233, v227, v233 row_newbcast:1 row_mask:0xf bank_mask:0xf\n"
    "v_cvt_pk_bf16_f32 v242, v244, v245\n"
    "v_add_f32_dpp v234, v227, v234 row_newbcast:2 row_mask:0xf bank_mask:0xf\n"
    "v_cvt_pk_bf16_f32 v243, v246, v247\n"
    "v_add_f32_dpp v235, v227, v235 row_newbcast:3 row_mask:0xf bank_mask:0xf\n"
    "v_add_f32_dpp v236, v227, v236 row_newbcast:4 row_mask:0xf bank_mask:0xf\n"
    "v_add_f32_dpp v237, v227, v237 row_newbcast:5 row_mask:0xf bank_mask:0xf\n"
    "v_add_f32_dpp v238, v227, v238 row_newbcast:6 row_mask:0xf bank_mask:0xf\n"
    "v_add_f32_dpp v239, v227, v239 row_newbcast:7 row_mask:0xf bank_mask:0xf\n"
    "v_cvt_pk_bf16_f32 v232, v232, v233\n"
    "v_cvt_pk_bf16_f32 v233, v234, v235\n"
    "v_cvt_pk_bf16_f32 v234, v236, v237\n"
    "v_cvt_pk_bf16_f32 v235, v238, v239\n"
    "v_mfma_f32_16x16x32_bf16 a[180:183], v[172:175], v[148:151], a[180:183]\n"
    "v_add_f32 v113, v113, v32\n"
    "v_add_f32 v114, v114, v33\n"
    "v_add_f32 v113, v113, v34\n"
    "v_add_f32 v114, v114, v35\n"
    "v_add_f32 v117, v117, v36\n"
    "v_add_f32 v118, v118, v37\n"
    "v_add_f32 v117, v117, v38\n"
    "v_add_f32 v118, v118, v39\n"
    "v_add_f32 v113, v113, v48\n"
    "v_add_f32 v114, v114, v49\n"
    "v_add_f32 v113, v113, v50\n"
    "v_add_f32 v114, v114, v51\n"
    "v_add_f32 v117, v117, v52\n"
    "buffer_store_dwordx4 v[232:235], v211, s[36:39], s62 offen sc1\n"
    "buffer_store_dwordx4 v[240:243], v212, s[40:43], s63 offen sc1\n"
    "v_add_f32 v118, v118, v53\n"
    "v_add_f32 v117, v117, v54\n"
    "v_add_f32 v118, v118, v55\n"
    "s_waitcnt vmcnt(8)\n"
    "ds_read_b128 a[240:243], v158 offset:49152\n"
    "ds_read_b128 a[244:247], v159 offset:49152\n"
    "ds_read_b128 a[248:251], v158 offset:49280\n"
    "ds_read_b128 a[252:255], v159 offset:49280\n"
    "ds_read_b128 v[0:3], v158 offset:49408\n"
    "ds_read_b128 v[4:7], v159 offset:49408\n"
    "v_cmp_ne_u32 s[80:81], s67, v219\n"
    "v_cmp_gt_u32 vcc, s66, v218\n"
    "s_or_b64 vcc, vcc, s[80:81]\n"
    "s_cbranch_vccnz LSPRE1\n"
    "LSPB1:\n"
    "v_mov_b32 v220, 4\n"
    "s_add_u32 s66, s66, s74\n"
    "s_waitcnt lgkmcnt(0)\n"
    "s_barrier\n"
    "buffer_store_dwordx2 v[220:221], v214, s[44:47], 0 offen sc1\n"
    "ds_read_b128 v[160:163], v255\n"
    "ds_read_b128 v[164:167], v255 offset:1024\n"
    "ds_read_b128 v[168:171], v255 offset:2048\n"
    "ds_read_b128 v[172:175], v255 offset:3072\n"
    "ds_read_b128 v[176:179], v255 offset:4096\n"
    "ds_read_b128 v[180:183], v255 offset:5120\n"
    "s_waitcnt lgkmcnt(4)\n"
    "v_mfma_f32_16x16x32_bf16 v[44:47], v[160:163], a[240:243], 0\n"
    "ds_read_b128 v[184:187], v255 offset:6144\n"
    "v_mfma_f32_16x16x32_bf16 v[44:47], v[164:167], a[244:247], v[44:47]\n"
    "s_waitcnt lgkmcnt(3)\n"
    "ds_read_b128 v[188:191], v255 offset:7168\n"
    "v_mfma_f32_16x16x32_bf16 v[44:47], v[168:171], a[248:251], v[44:47]\n"
    "s_add_u32 m0, s71, 0xc000\n"
    "ds_read_b128 v[160:163], v255 offset:8192\n"
    "v_mfma_f32_16x16x32_bf16 v[44:47], v[172:175], a[252:255], v[44:47]\n"
    "s_waitcnt lgkmcnt(3)\n"
    "ds_read_b128 v[164:167], v255 offset:9216\n"
    "buffer_load_dwordx4 v200, s[40:43], s61 offen sc1 lds\n"
    "v_mfma_f32_16x16x32_bf16 v[44:47], v[176:179], v[0:3], v[44:47]\n"
    "ds_read_b128 v[168:171], v255 offset:10240\n"
    "v_mfma_f32_16x16x32_bf16 v[44:47], v[180:183], v[4:7], v[44:47]\n"
    "s_waitcnt lgkmcnt(3)\n"
    "ds_read_b128 v[172:175], v255 offset:11264\n"
    "v_mfma_f32_16x16x32_bf16 v[60:63], v[184:187], a[240:243], 0\n"
    "ds_read_b128 v[176:179], v200\n"
    "v_mfma_f32_16x16x32_bf16 v[60:63], v[188:191], a[244:247], v[60:63]\n"
    "s_waitcnt lgkmcnt(3)\n"
    "ds_read_b128 v[180:183], v200 offset:1024\n"
    "v_mfma_f32_16x16x32_bf16 v[60:63], v[160:163], a[248:251], v[60:63]\n"
    "ds_read_b128 v[184:187], v200 offset:2048\n"
    "buffer_load_dwordx4 v200, s[40:43], s61 offen offset:1024 sc1 lds\n"
    "v_mfma_f32_16x16x32_bf16 v[60:63], v[164:167], a[252:255], v[60:63]\n"
    "s_waitcnt lgkmcnt(3)\n"
    "ds_read_b128 v[188:191], v200 offset:3072\n"
    "v_mfma_f32_16x16x32_bf16 v[60:63], v[168:171], v[0:3], v[60:63]\n"
    "ds_read_b128 v[160:163], v200 offset:4096\n"
    "v_mfma_f32_16x16x32_bf16 v[60:63], v[172:175], v[4:7], v[60:63]\n"
    "s_waitcnt lgkmcnt(3)\n"
    "ds_read_b128 v[164:167], v200 offset:5120\n"
    "v_mfma_f32_16x16x32_bf16 v[72:75], v[176:179], a[240:243], 0\n"
    "ds_read_b128 v[168:171], v200 offset:6144\n"
    "v_mfma_f32_16x16x32_bf16 v[72:75], v[180:183], a[244:247], v[72:75]\n"
    "s_waitcnt lgkmcnt(3)\n"
    "ds_read_b128 v[172:175], v200 offset:7168\n"
    "buffer_load_dwordx4 v200, s[40:43], s61 offen offset:2048 sc1 lds\n"
    "v_mfma_f32_16x16x32_bf16 v[72:75], v[184:187], a[248:251], v[72:75]\n"
    "ds_read_b128 v[176:179], v200 offset:8192\n"
    "v_mfma_f32_16x16x32_bf16 v[72:75], v[188:191], a[252:255], v[72:75]\n"
    "s_waitcnt lgkmcnt(3)\n"
    "ds_read_b128 v[180:183], v200 offset:9216\n"
    "v_mfma_f32_16x16x32_bf16 v[72:75], v[160:163], v[0:3], v[72:75]\n"
    "ds_read_b128 v[184:187], v200 offset:10240\n"
    "v_mfma_f32_16x16x32_bf16 v[72:75], v[164:167], v[4:7], v[72:75]\n"
    "s_waitcnt lgkmcnt(3)\n"
    "ds_read_b128 v[188:191], v200 offset:11264\n"
    "v_max3_f32 v196, v44, v45, v46\n"
    "v_max3_f32 v196, v196, v47, v60\n"
    "v_max3_f32 v196, v196, v61, v62\n"
    "v_mfma_f32_16x16x32_bf16 v[88:91], v[168:171], a[240:243], 0\n"
    "ds_read_b128 v[160:163], v200 offset:12288\n"
    "buffer_load_dwordx4 v200, s[40:43], s61 offen offset:3072 sc1 lds\n"
    "v_max_f32 v196, v196, v63\n"
    "v_mfma_f32_16x16x32_bf16 v[88:91], v[172:175], a[244:247], v[88:91]\n"
    "s_waitcnt lgkmcnt(3)\n"
    "ds_read_b128 v[164:167], v200 offset:13312\n"
    "v_mov_b32 v197, v196\n"
    "s_nop 1\n"
    "v_permlane32_swap_b32 v196, v197\n"
    "v_max_f32 v196, v196, v197\n"
    "v_mfma_f32_16x16x32_bf16 v[88:91], v[176:179], a[248:251], v[88:91]\n"
    "ds_read_b128 v[168:171], v200 offset:14336\n"
    "v_mov_b32 v197, v196\n"
    "s_nop 1\n"
    "v_permlane16_swap_b32 v196, v197\n"
    "v_max_f32 v196, v196, v197\n"
    "v_mfma_f32_16x16x32_bf16 v[88:91], v[180:183], a[252:255], v[88:91]\n"
    "s_waitcnt lgkmcnt(3)\n"
    "s_add_u32 s65, s61, 0x1000\n"
    "ds_read_b128 v[172:175], v200 offset:15360\n"
    "v_add_f32 v196, 0x42640000, v196\n"
    "v_sub_f32 v104, 0, v196\n"
    "v_mfma_f32_16x16x32_bf16 v[88:91], v[184:187], v[0:3], v[88:91]\n"
    "s_add_u32 m0, s71, 0xd000\n"
    "ds_read_b128 v[176:179], v200 offset:16384\n"
    "v_sub_f32 v105, 0, v196\n"
    "v_sub_f32 v106, 0, v196\n"
    "v_sub_f32 v107, 0, v196\n"
    "v_mfma_f32_16x16x32_bf16 v[88:91], v[188:191], v[4:7], v[88:91]\n"
    "s_waitcnt lgkmcnt(3)\n"
    "ds_read_b128 v[180:183], v200 offset:17408\n"
    "buffer_load_dwordx4 v200, s[40:43], s65 offen sc1 lds\n"
    "v_add_f32 v44, v44, v104\n"
    "v_mfma_f32_16x16x32_bf16 v[40:43], v[160:163], a[240:243], v[104:107]\n"
    "ds_read_b128 v[184:187], v200 offset:18432\n"
    "v_add_f32 v45, v45, v104\n"
    "v_add_f32 v46, v46, v104\n"
    "v_add_f32 v47, v47, v104\n"
    "v_mfma_f32_16x16x32_bf16 v[40:43], v[164:167], a[244:247], v[40:43]\n"
    "s_waitcnt lgkmcnt(3)\n"
    "ds_read_b128 v[188:191], v200 offset:19456\n"
    "v_add_f32 v60, v60, v104\n"
    "v_add_f32 v61, v61, v104\n"
    "v_add_f32 v62, v62, v104\n"
    "v_mfma_f32_16x16x32_bf16 v[40:43], v[168:171], a[248:251], v[40:43]\n"
    "ds_read_b128 v[160:163], v200 offset:20480\n"
    "v_add_f32 v63, v63, v104\n"
    "v_exp_f32 v44, v44\n"
    "v_mfma_f32_16x16x32_bf16 v[40:43], v[172:175], a[252:255], v[40:43]\n"
    "s_waitcnt lgkmcnt(3)\n"
    "ds_read_b128 v[164:167], v200 offset:21504\n"
    "v_exp_f32 v45, v45\n"
    "v_mfma_f32_16x16x32_bf16 v[40:43], v[176:179], v[0:3], v[40:43]\n"
    "ds_read_b128 v[168:171], v200 offset:22528\n"
    "buffer_load_dwordx4 v200, s[40:43], s65 offen offset:1024 sc1 lds\n"
    "v_mfma_f32_16x16x32_bf16 v[40:43], v[180:183], v[4:7], v[40:43]\n"
    "s_waitcnt lgkmcnt(3)\n"
    "ds_read_b128 v[172:175], v200 offset:23552\n"
    "v_exp_f32 v46, v46\n"
    "v_mfma_f32_16x16x32_bf16 v[56:59], v[184:187], a[240:243], v[104:107]\n"
    "ds_read_b128 v[176:179], v201 offset:24576\n"
    "v_exp_f32 v47, v47\n"
    "v_mfma_f32_16x16x32_bf16 v[56:59], v[188:191], a[244:247], v[56:59]\n"
    "s_waitcnt lgkmcnt(3)\n"
    "s_add_u32 s78, s64, 0x30000\n"
    "ds_read_b128 v[180:183], v201 offset:25600\n"
    "v_exp_f32 v60, v60\n"
    "v_mfma_f32_16x16x32_bf16 v[56:59], v[160:163], a[248:251], v[56:59]\n"
    "s_add_u32 m0, s72, 0x1b000\n"
    "ds_read_b128 v[184:187], v201 offset:26624\n"
    "v_exp_f32 v61, v61\n"
    "v_mfma_f32_16x16x32_bf16 v[56:59], v[164:167], a[252:255], v[56:59]\n"
    "s_waitcnt lgkmcnt(3)\n"
    "ds_read_b128 v[188:191], v201 offset:27648\n"
    "buffer_load_dwordx4 v202, s[32:35], s78 offen nt lds\n"
    "v_mfma_f32_16x16x32_bf16 v[56:59], v[168:171], v[0:3], v[56:59]\n"
    "ds_read_b128 v[160:163], v201 offset:28672\n"
    "v_exp_f32 v62, v62\n"
    "v_exp_f32 v63, v63\n"
    "v_cvt_pk_bf16_f32 v136, v44, v45\n"
    "v_cvt_pk_bf16_f32 v137, v46, v47\n"
    "v_cvt_pk_bf16_f32 v138, v60, v61\n"
    "v_cvt_pk_bf16_f32 v139, v62, v63\n"
    "v_add_f32 v72, v72, v104\n"
    "v_add_f32 v73, v73, v104\n"
    "v_add_f32 v74, v74, v104\n"
    "v_add_f32 v75, v75, v104\n"
    "v_add_f32 v88, v88, v104\n"
    "v_add_f32 v89, v89, v104\n"
    "v_add_f32 v90, v90, v104\n"
    "v_add_f32 v91, v91, v104\n"
    "v_exp_f32 v72, v72\n"
    "v_exp_f32 v73, v73\n"
    "v_exp_f32 v74, v74\n"
    "v_exp_f32 v75, v75\n"
    "v_exp_f32 v88, v88\n"
    "v_exp_f32 v89, v89\n"
    "v_exp_f32 v90, v90\n"
    "v_exp_f32 v91, v91\n"
    "v_cvt_pk_bf16_f32 v152, v72, v73\n"
    "v_cvt_pk_bf16_f32 v153, v74, v75\n"
    "v_cvt_pk_bf16_f32 v154, v88, v89\n"
    "v_cvt_pk_bf16_f32 v155, v90, v91\n"
    "v_mfma_f32_16x16x32_bf16 v[56:59], v[172:175], v[4:7], v[56:59]\n"
    "s_waitcnt lgkmcnt(3)\n"
    "ds_read_b128 v[164:167], v201 offset:29696\n"
    "v_exp_f32 v40, v40\n"
    "v_mfma_f32_16x16x32_bf16 a[8:11], v[176:179], v[136:139], 0\n"
    "ds_read_b128 v[168:171], v201 offset:30720\n"
    "v_mfma_f32_16x16x32_bf16 a[8:11], v[180:183], v[152:155], a[8:11]\n"
    "s_waitcnt lgkmcnt(3)\n"
    "ds_read_b128 v[172:175], v201 offset:31744\n"
    "v_mfma_f32_16x16x32_bf16 a[24:27], v[184:187], v[136:139], 0\n"
    "ds_read_b128 v[176:179], v201 offset:32768\n"
    "buffer_load_dwordx4 v203, s[32:35], s78 offen offset:1024 nt lds\n"
    "v_mfma_f32_16x16x32_bf16 a[24:27], v[188:191], v[152:155], a[24:27]\n"
    "s_waitcnt lgkmcnt(3)\n"
    "ds_read_b128 v[180:183], v201 offset:33792\n"
    "v_mfma_f32_16x16x32_bf16 a[40:43], v[160:163], v[136:139], 0\n"
    "ds_read_b128 v[184:187], v201 offset:34816\n"
    "v_mfma_f32_16x16x32_bf16 a[40:43], v[164:167], v[152:155], a[40:43]\n"
    "ds_read_b128 v[188:191], v201 offset:35840\n"
    "v_mfma_f32_16x16x32_bf16 v[120:123], v[192:195], v[136:139], 0\n"
    "v_exp_f32 v41, v41\n"
    "v_mfma_f32_16x16x32_bf16 v[120:123], v[192:195], v[152:155], v[120:123]\n"
    "s_waitcnt lgkmcnt(4)\n"
    "buffer_load_dwordx4 v204, s[32:35], s78 offen offset:2048 nt lds\n"
    "v_mfma_f32_16x16x32_bf16 a[56:59], v[168:171], v[136:139], 0\n"
    "ds_read_b128 v[160:163], v201 offset:36864\n"
    "v_mfma_f32_16x16x32_bf16 a[56:59], v[172:175], v[152:155], a[56:59]\n"
    "s_waitcnt lgkmcnt(3)\n"
    "ds_read_b128 v[164:167], v201 offset:37888\n"
    "v_mfma_f32_16x16x32_bf16 a[72:75], v[176:179], v[136:139], 0\n"
    "ds_read_b128 v[168:171], v201 offset:38912\n"
    "v_mfma_f32_16x16x32_bf16 a[72:75], v[180:183], v[152:155], a[72:75]\n"
    "s_waitcnt lgkmcnt(3)\n"
    "ds_read_b128 v[172:175], v201 offset:39936\n"
    "v_mfma_f32_16x16x32_bf16 a[88:91], v[184:187], v[136:139], 0\n"
    "ds_read_b128 v[176:179], v201 offset:40960\n"
    "v_mfma_f32_16x16x32_bf16 a[88:91], v[188:191], v[152:155], a[88:91]\n"
    "s_waitcnt lgkmcnt(3)\n"
    "ds_read_b128 v[180:183], v201 offset:41984\n"
    "v_mfma_f32_16x16x32_bf16 a[104:107], v[160:163], v[136:139], 0\n"
    "ds_read_b128 v[184:187], v201 offset:43008\n"
    "v_mfma_f32_16x16x32_bf16 a[104:107], v[164:167], v[152:155], a[104:107]\n"
    "s_waitcnt lgkmcnt(3)\n"
    "ds_read_b128 v[188:191], v201 offset:44032\n"
    "v_mfma_f32_16x16x32_bf16 a[120:123], v[168:171], v[136:139], 0\n"
    "ds_read_b128 v[160:163], v201 offset:45056\n"
    "v_mfma_f32_16x16x32_bf16 a[120:123], v[172:175], v[152:155], a[120:123]\n"
    "s_waitcnt lgkmcnt(3)\n"
    "ds_read_b128 v[164:167], v201 offset:46080\n"
    "v_mfma_f32_16x16x32_bf16 a[136:139], v[176:179], v[136:139], 0\n"
    "ds_read_b128 v[168:171], v201 offset:47104\n"
    "v_mfma_f32_16x16x32_bf16 a[136:139], v[180:183], v[152:155], a[136:139]\n"
    "s_waitcnt lgkmcnt(3)\n"
    "ds_read_b128 v[172:175], v201 offset:48128\n"
    "v_mfma_f32_16x16x32_bf16 a[152:155], v[184:187], v[136:139], 0\n"
    "v_exp_f32 v42, v42\n"
    "v_mfma_f32_16x16x32_bf16 a[152:155], v[188:191], v[152:155], a[152:155]\n"
    "s_waitcnt lgkmcnt(2)\n"
    "v_exp_f32 v43, v43\n"
    "v_mfma_f32_16x16x32_bf16 a[168:171], v[160:163], v[136:139], 0\n"
    "v_exp_f32 v56, v56\n"
    "v_mfma_f32_16x16x32_bf16 a[168:171], v[164:167], v[152:155], a[168:171]\n"
    "s_waitcnt lgkmcnt(0)\n"
    "v_exp_f32 v57, v57\n"
    "v_mfma_f32_16x16x32_bf16 a[184:187], v[168:171], v[136:139], 0\n"
    "v_exp_f32 v58, v58\n"
    "v_exp_f32 v59, v59\n"
    "v_mfma_f32_16x16x32_bf16 a[184:187], v[172:175], v[152:155], a[184:187]\n"
    "v_add_f32 v121, v121, v40\n"
    "v_add_f32 v122, v122, v41\n"
    "v_add_f32 v121, v121, v42\n"
    "v_add_f32 v122, v122, v43\n"
    "v_add_f32 v121, v121, v56\n"
    "v_add_f32 v122, v122, v57\n"
    "s_waitcnt vmcnt(12)\n"
    "ds_read_b128 v[8:11], v158 offset:24576\n"
    "ds_read_b128 v[12:15], v159 offset:24576\n"
    "ds_read_b128 v[16:19], v158 offset:24704\n"
    "ds_read_b128 v[20:23], v159 offset:24704\n"
    "ds_read_b128 v[24:27], v158 offset:24832\n"
    "ds_read_b128 v[28:31], v159 offset:24832\n"
    "s_waitcnt lgkmcnt(0)\n"
    "ds_read_b128 v[160:163], v255\n"
    "ds_read_b128 v[164:167], v255 offset:1024\n"
    "ds_read_b128 v[168:171], v255 offset:2048\n"
    "ds_read_b128 v[172:175], v255 offset:3072\n"
    "ds_read_b128 v[176:179], v255 offset:4096\n"
    "ds_read_b128 v[180:183], v255 offset:5120\n"
    "s_waitcnt lgkmcnt(4)\n"
    "v_mfma_f32_16x16x32_bf16 v[64:67], v[160:163], v[8:11], 0\n"
    "ds_read_b128 v[184:187], v255 offset:6144\n"
    "v_mfma_f32_16x16x32_bf16 v[64:67], v[164:167], v[12:15], v[64:67]\n"
    "s_waitcnt lgkmcnt(3)\n"
    "s_add_u32 m0, s71, 0x6000\n"
    "ds_read_b128 v[188:191], v255 offset:7168\n"
    "v_mfma_f32_16x16x32_bf16 v[64:67], v[168:171], v[16:19], v[64:67]\n"
    "ds_read_b128 v[160:163], v255 offset:8192\n"
    "buffer_load_dwordx4 v200, s[36:39], s60 offen sc1 lds\n"
    "v_mfma_f32_16x16x32_bf16 v[64:67], v[172:175], v[20:23], v[64:67]\n"
    "s_waitcnt lgkmcnt(3)\n"
    "ds_read_b128 v[164:167], v255 offset:9216\n"
    "v_mfma_f32_16x16x32_bf16 v[64:67], v[176:179], v[24:27], v[64:67]\n"
    "ds_read_b128 v[168:171], v255 offset:10240\n"
    "v_mfma_f32_16x16x32_bf16 v[64:67], v[180:183], v[28:31], v[64:67]\n"
    "s_waitcnt lgkmcnt(3)\n"
    "ds_read_b128 v[172:175], v255 offset:11264\n"
    "buffer_load_dwordx4 v200, s[36:39], s60 offen offset:1024 sc1 lds\n"
    "v_mfma_f32_16x16x32_bf16 v[80:83], v[184:187], v[8:11], 0\n"
    "ds_read_b128 v[176:179], v200\n"
    "v_mfma_f32_16x16x32_bf16 v[80:83], v[188:191], v[12:15], v[80:83]\n"
    "s_waitcnt lgkmcnt(3)\n"
    "ds_read_b128 v[180:183], v200 offset:1024\n"
    "v_mfma_f32_16x16x32_bf16 v[80:83], v[160:163], v[16:19], v[80:83]\n"
    "ds_read_b128 v[184:187], v200 offset:2048\n"
    "buffer_load_dwordx4 v200, s[36:39], s60 offen offset:2048 sc1 lds\n"
    "v_mfma_f32_16x16x32_bf16 v[80:83], v[164:167], v[20:23], v[80:83]\n"
    "s_waitcnt lgkmcnt(3)\n"
    "ds_read_b128 v[188:191], v200 offset:3072\n"
    "v_mfma_f32_16x16x32_bf16 v[80:83], v[168:171], v[24:27], v[80:83]\n"
    "ds_read_b128 v[160:163], v200 offset:4096\n"
    "v_mfma_f32_16x16x32_bf16 v[80:83], v[172:175], v[28:31], v[80:83]\n"
    "s_waitcnt lgkmcnt(3)\n"
    "ds_read_b128 v[164:167], v200 offset:5120\n"
    "buffer_load_dwordx4 v200, s[36:39], s60 offen offset:3072 sc1 lds\n"
    "v_mfma_f32_16x16x32_bf16 v[76:79], v[176:179], v[8:11], 0\n"
    "s_add_u32 s65, s60, 0x1000\n"
    "ds_read_b128 v[168:171], v200 offset:6144\n"
    "v_mfma_f32_16x16x32_bf16 v[76:79], v[180:183], v[12:15], v[76:79]\n"
    "s_waitcnt lgkmcnt(3)\n"
    "s_add_u32 m0, s71, 0x7000\n"
    "ds_read_b128 v[172:175], v200 offset:7168\n"
    "v_mfma_f32_16x16x32_bf16 v[76:79], v[184:187], v[16:19], v[76:79]\n"
    "ds_read_b128 v[176:179], v200 offset:8192\n"
    "buffer_load_dwordx4 v200, s[36:39], s65 offen sc1 lds\n"
    "v_mfma_f32_16x16x32_bf16 v[76:79], v[188:191], v[20:23], v[76:79]\n"
    "s_waitcnt lgkmcnt(3)\n"
    "ds_read_b128 v[180:183], v200 offset:9216\n"
    "v_mfma_f32_16x16x32_bf16 v[76:79], v[160:163], v[24:27], v[76:79]\n"
    "ds_read_b128 v[184:187], v200 offset:10240\n"
    "v_mfma_f32_16x16x32_bf16 v[76:79], v[164:167], v[28:31], v[76:79]\n"
    "s_waitcnt lgkmcnt(3)\n"
    "ds_read_b128 v[188:191], v200 offset:11264\n"
    "buffer_load_dwordx4 v200, s[36:39], s65 offen offset:1024 sc1 lds\n"
    "v_max3_f32 v196, v64, v65, v66\n"
    "v_mfma_f32_16x16x32_bf16 v[92:95], v[168:171], v[8:11], 0\n"
    "ds_read_b128 v[160:163], v200 offset:12288\n"
    "v_max3_f32 v196, v196, v67, v80\n"
    "v_max3_f32 v196, v196, v81, v82\n"
    "v_max_f32 v196, v196, v83\n"
    "v_mfma_f32_16x16x32_bf16 v[92:95], v[172:175], v[12:15], v[92:95]\n"
    "s_waitcnt lgkmcnt(3)\n"
    "ds_read_b128 v[164:167], v200 offset:13312\n"
    "v_mov_b32 v197, v196\n"
    "s_nop 1\n"
    "v_permlane32_swap_b32 v196, v197\n"
    "v_max_f32 v196, v196, v197\n"
    "v_mfma_f32_16x16x32_bf16 v[92:95], v[176:179], v[16:19], v[92:95]\n"
    "ds_read_b128 v[168:171], v200 offset:14336\n"
    "v_mov_b32 v197, v196\n"
    "s_nop 1\n"
    "v_permlane16_swap_b32 v196, v197\n"
    "v_max_f32 v196, v196, v197\n"
    "v_mfma_f32_16x16x32_bf16 v[92:95], v[180:183], v[20:23], v[92:95]\n"
    "s_waitcnt lgkmcnt(3)\n"
    "ds_read_b128 v[172:175], v200 offset:15360\n"
    "v_add_f32 v196, 0x42640000, v196\n"
    "v_sub_f32 v108, 0, v196\n"
    "v_sub_f32 v109, 0, v196\n"
    "v_mfma_f32_16x16x32_bf16 v[92:95], v[184:187], v[24:27], v[92:95]\n"
    "ds_read_b128 v[176:179], v200 offset:16384\n"
    "v_sub_f32 v110, 0, v196\n"
    "v_sub_f32 v111, 0, v196\n"
    "v_add_f32 v64, v64, v108\n"
    "v_mfma_f32_16x16x32_bf16 v[92:95], v[188:191], v[28:31], v[92:95]\n"
    "s_waitcnt lgkmcnt(3)\n"
    "ds_read_b128 v[180:183], v200 offset:17408\n"
    "v_add_f32 v65, v65, v108\n"
    "v_add_f32 v66, v66, v108\n"
    "v_add_f32 v67, v67, v108\n"
    "v_mfma_f32_16x16x32_bf16 v[44:47], v[160:163], v[8:11], v[108:111]\n"
    "ds_read_b128 v[184:187], v200 offset:18432\n"
    "v_add_f32 v80, v80, v108\n"
    "v_add_f32 v81, v81, v108\n"
    "v_add_f32 v82, v82, v108\n"
    "v_mfma_f32_16x16x32_bf16 v[44:47], v[164:167], v[12:15], v[44:47]\n"
    "s_waitcnt lgkmcnt(3)\n"
    "ds_read_b128 v[188:191], v200 offset:19456\n"
    "v_add_f32 v83, v83, v108\n"
    "v_exp_f32 v64, v64\n"
    "v_mfma_f32_16x16x32_bf16 v[44:47], v[168:171], v[16:19], v[44:47]\n"
    "ds_read_b128 v[160:163], v200 offset:20480\n"
    "buffer_load_dwordx2 v[218:219], v213, s[44:47], 0 offen sc1\n"
    "v_mfma_f32_16x16x32_bf16 v[44:47], v[172:175], v[20:23], v[44:47]\n"
    "s_waitcnt lgkmcnt(3)\n"
    "ds_read_b128 v[164:167], v200 offset:21504\n"
    "v_exp_f32 v65, v65\n"
    "v_mfma_f32_16x16x32_bf16 v[44:47], v[176:179], v[24:27], v[44:47]\n"
    "ds_read_b128 v[168:171], v200 offset:22528\n"
    "v_exp_f32 v66, v66\n"
    "v_mfma_f32_16x16x32_bf16 v[44:47], v[180:183], v[28:31], v[44:47]\n"
    "s_waitcnt lgkmcnt(3)\n"
    "ds_read_b128 v[172:175], v200 offset:23552\n"
    "v_exp_f32 v67, v67\n"
    "v_mfma_f32_16x16x32_bf16 v[60:63], v[184:187], v[8:11], v[108:111]\n"
    "ds_read_b128 v[176:179], v201 offset:24576\n"
    "v_exp_f32 v80, v80\n"
    "v_mfma_f32_16x16x32_bf16 v[60:63], v[188:191], v[12:15], v[60:63]\n"
    "s_waitcnt lgkmcnt(3)\n"
    "ds_read_b128 v[180:183], v201 offset:25600\n"
    "v_exp_f32 v81, v81\n"
    "v_mfma_f32_16x16x32_bf16 v[60:63], v[160:163], v[16:19], v[60:63]\n"
    "ds_read_b128 v[184:187], v201 offset:26624\n"
    "v_exp_f32 v82, v82\n"
    "v_mfma_f32_16x16x32_bf16 v[60:63], v[164:167], v[20:23], v[60:63]\n"
    "s_waitcnt lgkmcnt(3)\n"
    "ds_read_b128 v[188:191], v201 offset:27648\n"
    "v_exp_f32 v83, v83\n"
    "v_cvt_pk_bf16_f32 v140, v64, v65\n"
    "v_mfma_f32_16x16x32_bf16 v[60:63], v[168:171], v[24:27], v[60:63]\n"
    "ds_read_b128 v[160:163], v201 offset:28672\n"
    "v_cvt_pk_bf16_f32 v141, v66, v67\n"
    "v_cvt_pk_bf16_f32 v142, v80, v81\n"
    "v_cvt_pk_bf16_f32 v143, v82, v83\n"
    "v_add_f32 v76, v76, v108\n"
    "v_add_f32 v77, v77, v108\n"
    "v_add_f32 v78, v78, v108\n"
    "v_add_f32 v79, v79, v108\n"
    "v_add_f32 v92, v92, v108\n"
    "v_add_f32 v93, v93, v108\n"
    "v_add_f32 v94, v94, v108\n"
    "v_add_f32 v95, v95, v108\n"
    "v_exp_f32 v76, v76\n"
    "v_exp_f32 v77, v77\n"
    "v_exp_f32 v78, v78\n"
    "v_exp_f32 v79, v79\n"
    "v_exp_f32 v92, v92\n"
    "v_exp_f32 v93, v93\n"
    "v_exp_f32 v94, v94\n"
    "v_exp_f32 v95, v95\n"
    "v_cvt_pk_bf16_f32 v156, v76, v77\n"
    "v_cvt_pk_bf16_f32 v157, v78, v79\n"
    "v_cvt_pk_bf16_f32 v158, v92, v93\n"
    "v_cvt_pk_bf16_f32 v159, v94, v95\n"
    "v_mfma_f32_16x16x32_bf16 v[60:63], v[172:175], v[28:31], v[60:63]\n"
    "s_waitcnt lgkmcnt(3)\n"
    "ds_read_b128 v[164:167], v201 offset:29696\n"
    "v_exp_f32 v44, v44\n"
    "v_mfma_f32_16x16x32_bf16 a[12:15], v[176:179], v[140:143], 0\n"
    "ds_read_b128 v[168:171], v201 offset:30720\n"
    "v_mfma_f32_16x16x32_bf16 a[12:15], v[180:183], v[156:159], a[12:15]\n"
    "s_waitcnt lgkmcnt(3)\n"
    "ds_read_b128 v[172:175], v201 offset:31744\n"
    "v_mfma_f32_16x16x32_bf16 a[28:31], v[184:187], v[140:143], 0\n"
    "ds_read_b128 v[176:179], v201 offset:32768\n"
    "v_mfma_f32_16x16x32_bf16 a[28:31], v[188:191], v[156:159], a[28:31]\n"
    "s_waitcnt lgkmcnt(3)\n"
    "ds_read_b128 v[180:183], v201 offset:33792\n"
    "v_mfma_f32_16x16x32_bf16 a[44:47], v[160:163], v[140:143], 0\n"
    "ds_read_b128 v[184:187], v201 offset:34816\n"
    "v_mfma_f32_16x16x32_bf16 a[44:47], v[164:167], v[156:159], a[44:47]\n"
    "ds_read_b128 v[188:191], v201 offset:35840\n"
    "v_mfma_f32_16x16x32_bf16 v[124:127], v[192:195], v[140:143], 0\n"
    "v_exp_f32 v45, v45\n"
    "v_mfma_f32_16x16x32_bf16 v[124:127], v[192:195], v[156:159], v[124:127]\n"
    "s_waitcnt lgkmcnt(4)\n"
    "v_exp_f32 v46, v46\n"
    "v_mfma_f32_16x16x32_bf16 a[60:63], v[168:171], v[140:143], 0\n"
    "ds_read_b128 v[160:163], v201 offset:36864\n"
    "v_mfma_f32_16x16x32_bf16 a[60:63], v[172:175], v[156:159], a[60:63]\n"
    "s_waitcnt lgkmcnt(3)\n"
    "ds_read_b128 v[164:167], v201 offset:37888\n"
    "v_mfma_f32_16x16x32_bf16 a[76:79], v[176:179], v[140:143], 0\n"
    "ds_read_b128 v[168:171], v201 offset:38912\n"
    "v_mfma_f32_16x16x32_bf16 a[76:79], v[180:183], v[156:159], a[76:79]\n"
    "s_waitcnt lgkmcnt(3)\n"
    "ds_read_b128 v[172:175], v201 offset:39936\n"
    "v_mfma_f32_16x16x32_bf16 a[92:95], v[184:187], v[140:143], 0\n"
    "ds_read_b128 v[176:179], v201 offset:40960\n"
    "v_mfma_f32_16x16x32_bf16 a[92:95], v[188:191], v[156:159], a[92:95]\n"
    "s_waitcnt lgkmcnt(3)\n"
    "ds_read_b128 v[180:183], v201 offset:41984\n"
    "v_mfma_f32_16x16x32_bf16 a[108:111], v[160:163], v[140:143], 0\n"
    "ds_read_b128 v[184:187], v201 offset:43008\n"
    "v_mfma_f32_16x16x32_bf16 a[108:111], v[164:167], v[156:159], a[108:111]\n"
    "s_waitcnt lgkmcnt(3)\n"
    "ds_read_b128 v[188:191], v201 offset:44032\n"
    "v_mfma_f32_16x16x32_bf16 a[124:127], v[168:171], v[140:143], 0\n"
    "ds_read_b128 v[160:163], v201 offset:45056\n"
    "v_mfma_f32_16x16x32_bf16 a[124:127], v[172:175], v[156:159], a[124:127]\n"
    "s_waitcnt lgkmcnt(3)\n"
    "ds_read_b128 v[164:167], v201 offset:46080\n"
    "v_mfma_f32_16x16x32_bf16 a[140:143], v[176:179], v[140:143], 0\n"
    "ds_read_b128 v[168:171], v201 offset:47104\n"
    "v_mfma_f32_16x16x32_bf16 a[140:143], v[180:183], v[156:159], a[140:143]\n"
    "s_waitcnt lgkmcnt(3)\n"
    "ds_read_b128 v[172:175], v201 offset:48128\n"
    "v_mfma_f32_16x16x32_bf16 a[156:159], v[184:187], v[140:143], 0\n"
    "v_exp_f32 v47, v47\n"
    "v_mfma_f32_16x16x32_bf16 a[156:159], v[188:191], v[156:159], a[156:159]\n"
    "s_waitcnt lgkmcnt(2)\n"
    "v_exp_f32 v60, v60\n"
    "v_mfma_f32_16x16x32_bf16 a[172:175], v[160:163], v[140:143], 0\n"
    "v_exp_f32 v61, v61\n"
    "v_mfma_f32_16x16x32_bf16 a[172:175], v[164:167], v[156:159], a[172:175]\n"
    "s_waitcnt lgkmcnt(0)\n"
    "v_exp_f32 v62, v62\n"
    "v_mfma_f32_16x16x32_bf16 a[188:191], v[168:171], v[140:143], 0\n"
    "v_exp_f32 v63, v63\n"
    "v_mfma_f32_16x16x32_bf16 a[188:191], v[172:175], v[156:159], a[188:191]\n"
    "v_add_f32 v125, v125, v44\n"
    "v_add_f32 v126, v126, v45\n"
    "v_add_f32 v125, v125, v46\n"
    "v_add_f32 v126, v126, v47\n"
    "s_add_u32 s64, s64, 0x3c000\n"
    "s_add_u32 s62, s62, 0x6000\n"
    "s_add_u32 s63, s63, 0x6000\n"
    "s_add_u32 s60, s60, 0x6000\n"
    "s_add_u32 s61, s61, 0x6000\n"
    "v_add_u32 v209, 0x300, v209\n"
    "v_add_u32 v210, 0xc0, v210\n"
    "v_mov_b32 v220, 5\n"
    "s_waitcnt vmcnt(0)\n"
    "v_cmp_ne_u32 s[80:81], s67, v219\n"
    "v_cmp_gt_u32 vcc, s66, v218\n"
    "s_or_b64 vcc, vcc, s[80:81]\n"
    "s_cbranch_vccnz LSPRE2\n"
    "LSPB2:\n"
    "s_barrier\n"
    "s_cmp_eq_u32 s58, 0\n"
    "s_cbranch_scc1 LENT0\n"
    "LENT1:\n"
    "s_mov_b32 s59, 1\n"
    "s_add_u32 m0, s72, 0x21000\n"
    "s_nop 0\n"
    "buffer_load_dwordx4 v250, s[48:51], 0 offen offset:2048 lds\n"
    "s_add_u32 m0, s72, 0x18000\n"
    "s_nop 0\n"
    "buffer_load_dwordx4 v202, s[32:35], s64 offen lds\n"
    "buffer_load_dwordx4 v203, s[32:35], s64 offen offset:1024 lds\n"
    "buffer_load_dwordx4 v204, s[32:35], s64 offen offset:2048 lds\n"
    "s_add_u32 s64, s64, 0xc000\n"
    "s_mov_b32 s94, 0x3000\n"
    "s_mov_b32 s95, 0x6000\n"
    "s_add_u32 s51, s72, 0x18000\n"
    "v_add_u32 v205, 0x3000, v205\n"
    "v_add_u32 v206, 0x3000, v206\n"
    "v_add_u32 v207, 0x3000, v207\n"
    "ds_read_u16_d16_hi v223, v209 offset:6336\n"
    "ds_read_u16_d16_hi v222, v209 offset:192\n"
    "ds_read_b128 v[160:163], v200 offset:24576\n"
    "v_add_f32 v121, v121, v58\n"
    "ds_read_b128 v[232:235], v205 offset:24576\n"
    "v_add_f32 v122, v122, v59\n"
    "ds_read_b128 v[164:167], v200 offset:25600\n"
    "ds_read_b128 v[236:239], v206 offset:24576\n"
    "v_add_f32 v125, v125, v60\n"
    "v_add_f32 v126, v126, v61\n"
    "v_add_f32 v125, v125, v62\n"
    "v_add_f32 v126, v126, v63\n"
    "ds_read_b64 v[228:229], v210 offset:64\n"
    "ds_read2st64_b32 v[240:241], v207 offset0:96 offset1:99\n"
    "ds_read_b128 v[168:171], v200 offset:26624\n"
    "ds_read2st64_b32 v[242:243], v207 offset0:102 offset1:105\n"
    "ds_read2st64_b32 v[244:245], v207 offset0:108 offset1:111\n"
    "ds_read2st64_b32 v[246:247], v207 offset0:114 offset1:117\n"
    "v_cvt_pk_bf16_f32 v140, v44, v45\n"
    "v_cvt_pk_bf16_f32 v141, v46, v47\n"
    "s_waitcnt lgkmcnt(6)\n"
    "v_mul_f32 v226, s75, v223\n"
    "ds_read_b128 v[172:175], v200 offset:27648\n"
    "v_fma_f32 v227, -v222, v226, v217\n"
    "v_cvt_f32_i32 v232, v232\n"
    "v_cvt_f32_i32 v233, v233\n"
    "v_cvt_f32_i32 v234, v234\n"
    "v_cvt_f32_i32 v235, v235\n"
    "s_nop 7\n"
    ".p2align 6\n"
    "LLOOP1:\n"
    "v_mfma_f32_16x16x32_bf16 v[64:67], v[160:163], a[192:195], v[96:99]\n"
    "ds_read_b128 v[176:179], v200 offset:28672\n"
    "v_cvt_f32_i32 v236, v236\n"
    "v_mfma_f32_16x16x32_bf16 v[64:67], v[164:167], a[196:199], v[64:67]\n"
    "s_waitcnt lgkmcnt(1)\n"
    "v_cvt_f32_i32 v237, v237\n"
    "v_cvt_f32_i32 v238, v238\n"
    "v_mfma_f32_16x16x32_bf16 v[64:67], v[168:171], a[200:203], v[64:67]\n"
    "v_cvt_f32_i32 v239, v239\n"
    "v_mul_f32_dpp v232, v226, v232 row_newbcast:0 row_mask:0xf bank_mask:0xf\n"
    "v_mfma_f32_16x16x32_bf16 v[68:71], v[168:171], a[224:227], v[100:103]\n"
    "v_mul_f32_dpp v233, v226, v233 row_newbcast:1 row_mask:0xf bank_mask:0xf\n"
    "v_mul_f32_dpp v234, v226, v234 row_newbcast:2 row_mask:0xf bank_mask:0xf\n"
    "v_mfma_f32_16x16x32_bf16 v[68:71], v[164:167], a[220:223], v[68:71]\n"
    "ds_read_b128 v[180:183], v200 offset:29696\n"
    "v_mul_f32_dpp v235, v226, v235 row_newbcast:3 row_mask:0xf bank_mask:0xf\n"
    "v_mfma_f32_16x16x32_bf16 v[68:71], v[160:163], a[216:219], v[68:71]\n"
    "v_mul_f32_dpp v236, v226, v236 row_newbcast:4 row_mask:0xf bank_mask:0xf\n"
    "v_mul_f32_dpp v237, v226, v237 row_newbcast:5 row_mask:0xf bank_mask:0xf\n"
    "v_mfma_f32_16x16x32_bf16 v[72:75], v[160:163], a[240:243], v[104:107]\n"
    "v_mul_f32_dpp v238, v226, v238 row_newbcast:6 row_mask:0xf bank_mask:0xf\n"
    "v_mul_f32_dpp v239, v226, v239 row_newbcast:7 row_mask:0xf bank_mask:0xf\n"
    "v_mfma_f32_16x16x32_bf16 v[72:75], v[164:167], a[244:247], v[72:75]\n"
    "v_add_f32_dpp v232, v227, v232 row_newbcast:0 row_mask:0xf bank_mask:0xf\n"
    "v_add_f32_dpp v233, v227, v233 row_newbcast:1 row_mask:0xf bank_mask:0xf\n"
    "v_mfma_f32_16x16x32_bf16 v[72:75], v[168:171], a[248:251], v[72:75]\n"
    "v_add_f32_dpp v234, v227, v234 row_newbcast:2 row_mask:0xf bank_mask:0xf\n"
    "v_add_f32_dpp v235, v227, v235 row_newbcast:3 row_mask:0xf bank_mask:0xf\n"
    "v_mfma_f32_16x16x32_bf16 v[76:79], v[168:171], v[16:19], v[108:111]\n"
    "v_add_f32_dpp v236, v227, v236 row_newbcast:4 row_mask:0xf bank_mask:0xf\n"
    "v_add_f32_dpp v237, v227, v237 row_newbcast:5 row_mask:0xf bank_mask:0xf\n"
    "v_mfma_f32_16x16x32_bf16 v[76:79], v[164:167], v[12:15], v[76:79]\n"
    "v_add_f32_dpp v238, v227, v238 row_newbcast:6 row_mask:0xf bank_mask:0xf\n"
    "v_add_f32_dpp v239, v227, v239 row_newbcast:7 row_mask:0xf bank_mask:0xf\n"
    "v_mfma_f32_16x16x32_bf16 v[76:79], v[160:163], v[8:11], v[76:79]\n"
    "s_waitcnt lgkmcnt(1)\n"
    "v_cvt_pk_bf16_f32 v232, v232, v233\n"
    "v_cvt_f32_i32 v240, v240\n"
    "v_mfma_f32_16x16x32_bf16 v[76:79], v[172:175], v[20:23], v[76:79]\n"
    "ds_read_b128 v[160:163], v200 offset:33792\n"
    "v_cvt_pk_bf16_f32 v233, v234, v235\n"
    "v_mfma_f32_16x16x32_bf16 v[76:79], v[176:179], v[24:27], v[76:79]\n"
    "s_waitcnt lgkmcnt(1)\n"
    "v_cvt_f32_i32 v241, v241\n"
    "v_cvt_pk_bf16_f32 v234, v236, v237\n"
    "v_mfma_f32_16x16x32_bf16 v[76:79], v[180:183], v[28:31], v[76:79]\n"
    "v_cvt_f32_i32 v242, v242\n"
    "v_cvt_pk_bf16_f32 v235, v238, v239\n"
    "v_mfma_f32_16x16x32_bf16 v[72:75], v[180:183], v[4:7], v[72:75]\n"
    "ds_read_b128 v[164:167], v200 offset:34816\n"
    "v_cvt_f32_i32 v243, v243\n"
    "v_mfma_f32_16x16x32_bf16 v[72:75], v[176:179], v[0:3], v[72:75]\n"
    "v_cvt_f32_i32 v244, v244\n"
    "v_cvt_f32_i32 v245, v245\n"
    "v_mfma_f32_16x16x32_bf16 v[72:75], v[172:175], a[252:255], v[72:75]\n"
    "buffer_store_dwordx4 v[232:235], v211, s[36:39], s62 offen sc1\n"
    "v_cvt_f32_i32 v246, v246\n"
    "v_cvt_f32_i32 v247, v247\n"
    "v_mul_f32_dpp v240, v228, v240 row_newbcast:0 row_mask:0xf bank_mask:0xf\n"
    "v_mfma_f32_16x16x32_bf16 v[68:71], v[172:175], a[228:231], v[68:71]\n"
    "ds_read_b128 v[168:171], v200 offset:35840\n"
    "v_mul_f32_dpp v241, v228, v241 row_newbcast:1 row_mask:0xf bank_mask:0xf\n"
    "v_mfma_f32_16x16x32_bf16 v[68:71], v[176:179], a[232:235], v[68:71]\n"
    "v_mul_f32_dpp v242, v228, v242 row_newbcast:2 row_mask:0xf bank_mask:0xf\n"
    "v_mul_f32_dpp v243, v228, v243 row_newbcast:3 row_mask:0xf bank_mask:0xf\n"
    "v_mfma_f32_16x16x32_bf16 v[68:71], v[180:183], a[236:239], v[68:71]\n"
    "v_mul_f32_dpp v244, v228, v244 row_newbcast:4 row_mask:0xf bank_mask:0xf\n"
    "v_mul_f32_dpp v245, v228, v245 row_newbcast:5 row_mask:0xf bank_mask:0xf\n"
    "v_mfma_f32_16x16x32_bf16 v[64:67], v[180:183], a[212:215], v[64:67]\n"
    "v_mul_f32_dpp v246, v228, v246 row_newbcast:6 row_mask:0xf bank_mask:0xf\n"
    "v_mul_f32_dpp v247, v228, v247 row_newbcast:7 row_mask:0xf bank_mask:0xf\n"
    "v_mfma_f32_16x16x32_bf16 v[64:67], v[176:179], a[208:211], v[64:67]\n"
    "v_add_f32_dpp v240, v229, v240 row_newbcast:0 row_mask:0xf bank_mask:0xf\n"
    "v_add_f32_dpp v241, v229, v241 row_newbcast:1 row_mask:0xf bank_mask:0xf\n"
    "v_mfma_f32_16x16x32_bf16 v[64:67], v[172:175], a[204:207], v[64:67]\n"
    "s_waitcnt lgkmcnt(1)\n"
    "v_add_f32_dpp v242, v229, v242 row_newbcast:2 row_mask:0xf bank_mask:0xf\n"
    "v_add_f32_dpp v243, v229, v243 row_newbcast:3 row_mask:0xf bank_mask:0xf\n"
    "v_mfma_f32_16x16x32_bf16 v[80:83], v[160:163], a[204:207], v[96:99]\n"
    "ds_read_b128 v[172:175], v200 offset:30720\n"
    "v_add_f32_dpp v244, v229, v244 row_newbcast:4 row_mask:0xf bank_mask:0xf\n"
    "v_mfma_f32_16x16x32_bf16 v[80:83], v[164:167], a[208:211], v[80:83]\n"
    "s_waitcnt lgkmcnt(1)\n"
    "v_add_f32_dpp v245, v229, v245 row_newbcast:5 row_mask:0xf bank_mask:0xf\n"
    "v_add_f32_dpp v246, v229, v246 row_newbcast:6 row_mask:0xf bank_mask:0xf\n"
    "v_mfma_f32_16x16x32_bf16 v[80:83], v[168:171], a[212:215], v[80:83]\n"
    "v_add_f32_dpp v247, v229, v247 row_newbcast:7 row_mask:0xf bank_mask:0xf\n"
    "v_cvt_pk_bf16_f32 v240, v240, v241\n"
    "v_mfma_f32_16x16x32_bf16 v[84:87], v[168:171], a[236:239], v[100:103]\n"
    "ds_read_b128 v[176:179], v200 offset:31744\n"
    "v_cvt_pk_bf16_f32 v241, v242, v243\n"
    "v_mfma_f32_16x16x32_bf16 v[84:87], v[164:167], a[232:235], v[84:87]\n"
    "v_cvt_pk_bf16_f32 v242, v244, v245\n"
    "v_cvt_pk_bf16_f32 v243, v246, v247\n"
    "v_mfma_f32_16x16x32_bf16 v[84:87], v[160:163], a[228:231], v[84:87]\n"
    "buffer_store_dwordx4 v[240:243], v212, s[40:43], s63 offen sc1\n"
    "v_cvt_pk_bf16_f32 v136, v40, v41\n"
    "v_cvt_pk_bf16_f32 v137, v42, v43\n"
    "v_cvt_pk_bf16_f32 v132, v36, v37\n"
    "v_mfma_f32_16x16x32_bf16 v[88:91], v[160:163], a[252:255], v[104:107]\n"
    "ds_read_b128 v[180:183], v200 offset:32768\n"
    "v_cvt_pk_bf16_f32 v133, v38, v39\n"
    "v_mfma_f32_16x16x32_bf16 v[88:91], v[164:167], v[0:3], v[88:91]\n"
    "v_cvt_pk_bf16_f32 v128, v32, v33\n"
    "v_cvt_pk_bf16_f32 v129, v34, v35\n"
    "v_mfma_f32_16x16x32_bf16 v[88:91], v[168:171], v[4:7], v[88:91]\n"
    "v_exp_f32 v64, v64\n"
    "v_mfma_f32_16x16x32_bf16 v[92:95], v[168:171], v[28:31], v[108:111]\n"
    "v_exp_f32 v65, v65\n"
    "v_mfma_f32_16x16x32_bf16 v[92:95], v[164:167], v[24:27], v[92:95]\n"
    "v_exp_f32 v66, v66\n"
    "v_mfma_f32_16x16x32_bf16 v[92:95], v[160:163], v[20:23], v[92:95]\n"
    "s_waitcnt lgkmcnt(1)\n"
    "v_exp_f32 v67, v67\n"
    "v_mfma_f32_16x16x32_bf16 v[92:95], v[172:175], v[8:11], v[92:95]\n"
    "ds_read_b128 v[160:163], v200 offset:36864\n"
    "v_cvt_pk_bf16_f32 v142, v60, v61\n"
    "v_mfma_f32_16x16x32_bf16 v[92:95], v[176:179], v[12:15], v[92:95]\n"
    "s_waitcnt lgkmcnt(1)\n"
    "s_add_u32 s65, s60, s92\n"
    "v_cvt_pk_bf16_f32 v143, v62, v63\n"
    "v_mfma_f32_16x16x32_bf16 v[92:95], v[180:183], v[16:19], v[92:95]\n"
    "s_add_u32 m0, s90, 0\n"
    "v_exp_f32 v68, v68\n"
    "v_mfma_f32_16x16x32_bf16 v[88:91], v[180:183], a[248:251], v[88:91]\n"
    "ds_read_b128 v[164:167], v200 offset:37888\n"
    "v_cvt_pk_bf16_f32 v138, v56, v57\n"
    "v_mfma_f32_16x16x32_bf16 v[88:91], v[176:179], a[244:247], v[88:91]\n"
    "buffer_load_dwordx4 v200, s[36:39], s65 offen sc1 lds\n"
    "v_mfma_f32_16x16x32_bf16 v[88:91], v[172:175], a[240:243], v[88:91]\n"
    "v_exp_f32 v69, v69\n"
    "v_mfma_f32_16x16x32_bf16 v[84:87], v[172:175], a[216:219], v[84:87]\n"
    "ds_read_b128 v[168:171], v200 offset:38912\n"
    "v_cvt_pk_bf16_f32 v139, v58, v59\n"
    "v_mfma_f32_16x16x32_bf16 v[84:87], v[176:179], a[220:223], v[84:87]\n"
    "v_exp_f32 v70, v70\n"
    "v_mfma_f32_16x16x32_bf16 v[84:87], v[180:183], a[224:227], v[84:87]\n"
    "buffer_load_dwordx4 v200, s[36:39], s65 offen offset:1024 sc1 lds\n"
    "v_mfma_f32_16x16x32_bf16 v[80:83], v[180:183], a[200:203], v[80:83]\n"
    "v_exp_f32 v71, v71\n"
    "v_mfma_f32_16x16x32_bf16 v[80:83], v[176:179], a[196:199], v[80:83]\n"
    "v_exp_f32 v72, v72\n"
    "v_mfma_f32_16x16x32_bf16 v[80:83], v[172:175], a[192:195], v[80:83]\n"
    "s_waitcnt lgkmcnt(1)\n"
    "v_cvt_pk_bf16_f32 v134, v52, v53\n"
    "v_cvt_pk_bf16_f32 v135, v54, v55\n"
    "v_mfma_f32_16x16x32_bf16 v[32:35], v[160:163], a[192:195], v[96:99]\n"
    "ds_read_b128 v[172:175], v200 offset:39936\n"
    "v_cvt_pk_bf16_f32 v130, v48, v49\n"
    "v_mfma_f32_16x16x32_bf16 v[32:35], v[164:167], a[196:199], v[32:35]\n"
    "s_waitcnt lgkmcnt(1)\n"
    "buffer_load_dwordx4 v200, s[36:39], s65 offen offset:2048 sc1 lds\n"
    "v_mfma_f32_16x16x32_bf16 v[32:35], v[168:171], a[200:203], v[32:35]\n"
    "v_exp_f32 v73, v73\n"
    "v_mfma_f32_16x16x32_bf16 v[36:39], v[168:171], a[224:227], v[100:103]\n"
    "ds_read_b128 v[176:179], v200 offset:40960\n"
    "v_cvt_pk_bf16_f32 v131, v50, v51\n"
    "v_mfma_f32_16x16x32_bf16 v[36:39], v[164:167], a[220:223], v[36:39]\n"
    "v_exp_f32 v74, v74\n"
    "v_mfma_f32_16x16x32_bf16 v[36:39], v[160:163], a[216:219], v[36:39]\n"
    "buffer_load_dwordx4 v200, s[36:39], s65 offen offset:3072 sc1 lds\n"
    "v_mfma_f32_16x16x32_bf16 v[40:43], v[160:163], a[240:243], v[104:107]\n"
    "s_add_u32 s65, s65, 0x1000\n"
    "ds_read_b128 v[180:183], v200 offset:41984\n"
    "v_cvt_pk_bf16_f32 v144, v64, v65\n"
    "v_mfma_f32_16x16x32_bf16 v[40:43], v[164:167], a[244:247], v[40:43]\n"
    "s_add_u32 m0, s90, 0x1000\n"
    "v_exp_f32 v75, v75\n"
    "v_mfma_f32_16x16x32_bf16 v[40:43], v[168:171], a[248:251], v[40:43]\n"
    "v_exp_f32 v76, v76\n"
    "v_mfma_f32_16x16x32_bf16 v[44:47], v[168:171], v[16:19], v[108:111]\n"
    "buffer_load_dwordx4 v200, s[36:39], s65 offen sc1 lds\n"
    "v_mfma_f32_16x16x32_bf16 v[44:47], v[164:167], v[12:15], v[44:47]\n"
    "v_exp_f32 v77, v77\n"
    "v_mfma_f32_16x16x32_bf16 v[44:47], v[160:163], v[8:11], v[44:47]\n"
    "s_waitcnt lgkmcnt(1)\n"
    "v_exp_f32 v78, v78\n"
    "v_mfma_f32_16x16x32_bf16 v[44:47], v[172:175], v[20:23], v[44:47]\n"
    "ds_read_b128 v[160:163], v200 offset:46080\n"
    "v_cvt_pk_bf16_f32 v145, v66, v67\n"
    "v_mfma_f32_16x16x32_bf16 v[44:47], v[176:179], v[24:27], v[44:47]\n"
    "s_waitcnt lgkmcnt(1)\n"
    "buffer_load_dwordx4 v200, s[36:39], s65 offen offset:1024 sc1 lds\n"
    "v_mfma_f32_16x16x32_bf16 v[44:47], v[180:183], v[28:31], v[44:47]\n"
    "v_exp_f32 v79, v79\n"
    "v_mfma_f32_16x16x32_bf16 v[40:43], v[180:183], v[4:7], v[40:43]\n"
    "ds_read_b128 v[164:167], v200 offset:47104\n"
    "v_cvt_pk_bf16_f32 v148, v68, v69\n"
    "v_mfma_f32_16x16x32_bf16 v[40:43], v[176:179], v[0:3], v[40:43]\n"
    "v_exp_f32 v80, v80\n"
    "v_mfma_f32_16x16x32_bf16 v[40:43], v[172:175], a[252:255], v[40:43]\n"
    "buffer_load_dwordx4 v200, s[36:39], s65 offen offset:2048 sc1 lds\n"
    "v_mfma_f32_16x16x32_bf16 v[36:39], v[172:175], a[228:231], v[36:39]\n"
    "ds_read_b128 v[168:171], v200 offset:48128\n"
    "v_cvt_pk_bf16_f32 v149, v70, v71\n"
    "v_mfma_f32_16x16x32_bf16 v[36:39], v[176:179], a[232:235], v[36:39]\n"
    "s_add_u32 m0, s71, 0x12000\n"
    "v_exp_f32 v81, v81\n"
    "v_mfma_f32_16x16x32_bf16 v[36:39], v[180:183], a[236:239], v[36:39]\n"
    "v_exp_f32 v82, v82\n"
    "v_mfma_f32_16x16x32_bf16 v[32:35], v[180:183], a[212:215], v[32:35]\n"
    "buffer_load_dwordx4 v200, s[40:43], s61 offen sc1 lds\n"
    "v_mfma_f32_16x16x32_bf16 v[32:35], v[176:179], a[208:211], v[32:35]\n"
    "v_exp_f32 v83, v83\n"
    "v_mfma_f32_16x16x32_bf16 v[32:35], v[172:175], a[204:207], v[32:35]\n"
    "s_waitcnt lgkmcnt(1)\n"
    "v_exp_f32 v84, v84\n"
    "v_mfma_f32_16x16x32_bf16 v[48:51], v[160:163], a[204:207], v[96:99]\n"
    "ds_read_b128 v[172:175], v200 offset:43008\n"
    "v_cvt_pk_bf16_f32 v152, v72, v73\n"
    "v_mfma_f32_16x16x32_bf16 v[48:51], v[164:167], a[208:211], v[48:51]\n"
    "s_waitcnt lgkmcnt(1)\n"
    "buffer_load_dwordx4 v200, s[40:43], s61 offen offset:1024 sc1 lds\n"
    "v_mfma_f32_16x16x32_bf16 v[48:51], v[168:171], a[212:215], v[48:51]\n"
    "v_exp_f32 v85, v85\n"
    "v_mfma_f32_16x16x32_bf16 v[52:55], v[168:171], a[236:239], v[100:103]\n"
    "ds_read_b128 v[176:179], v200 offset:44032\n"
    "v_cvt_pk_bf16_f32 v153, v74, v75\n"
    "v_mfma_f32_16x16x32_bf16 v[52:55], v[164:167], a[232:235], v[52:55]\n"
    "v_exp_f32 v86, v86\n"
    "v_mfma_f32_16x16x32_bf16 v[52:55], v[160:163], a[228:231], v[52:55]\n"
    "buffer_load_dwordx4 v200, s[40:43], s61 offen offset:2048 sc1 lds\n"
    "v_mfma_f32_16x16x32_bf16 v[56:59], v[160:163], a[252:255], v[104:107]\n"
    "ds_read_b128 v[180:183], v200 offset:45056\n"
    "v_cvt_pk_bf16_f32 v156, v76, v77\n"
    "v_mfma_f32_16x16x32_bf16 v[56:59], v[164:167], v[0:3], v[56:59]\n"
    "v_exp_f32 v87, v87\n"
    "v_mfma_f32_16x16x32_bf16 v[56:59], v[168:171], v[4:7], v[56:59]\n"
    "v_exp_f32 v88, v88\n"
    "v_mfma_f32_16x16x32_bf16 v[60:63], v[168:171], v[28:31], v[108:111]\n"
    "buffer_load_dwordx4 v200, s[40:43], s61 offen offset:3072 sc1 lds\n"
    "v_mfma_f32_16x16x32_bf16 v[60:63], v[164:167], v[24:27], v[60:63]\n"
    "s_add_u32 s65, s61, 0x1000\n"
    "v_exp_f32 v89, v89\n"
    "v_mfma_f32_16x16x32_bf16 v[60:63], v[160:163], v[20:23], v[60:63]\n"
    "s_waitcnt lgkmcnt(1)\n"
    "s_add_u32 m0, s71, 0x13000\n"
    "v_cvt_pk_bf16_f32 v157, v78, v79\n"
    "v_mfma_f32_16x16x32_bf16 v[60:63], v[172:175], v[8:11], v[60:63]\n"
    "ds_read_b128 v[160:163], v201\n"
    "v_cvt_pk_bf16_f32 v146, v80, v81\n"
    "v_mfma_f32_16x16x32_bf16 v[60:63], v[176:179], v[12:15], v[60:63]\n"
    "s_waitcnt lgkmcnt(1)\n"
    "buffer_load_dwordx4 v200, s[40:43], s65 offen sc1 lds\n"
    "v_mfma_f32_16x16x32_bf16 v[60:63], v[180:183], v[16:19], v[60:63]\n"
    "v_exp_f32 v90, v90\n"
    "v_mfma_f32_16x16x32_bf16 v[56:59], v[180:183], a[248:251], v[56:59]\n"
    "ds_read_b128 v[164:167], v201 offset:1024\n"
    "v_cvt_pk_bf16_f32 v147, v82, v83\n"
    "v_mfma_f32_16x16x32_bf16 v[56:59], v[176:179], a[244:247], v[56:59]\n"
    "v_exp_f32 v91, v91\n"
    "v_mfma_f32_16x16x32_bf16 v[56:59], v[172:175], a[240:243], v[56:59]\n"
    "buffer_load_dwordx4 v200, s[40:43], s65 offen offset:1024 sc1 lds\n"
    "v_mfma_f32_16x16x32_bf16 v[52:55], v[172:175], a[216:219], v[52:55]\n"
    "ds_read_b128 v[168:171], v201 offset:2048\n"
    "v_cvt_pk_bf16_f32 v150, v84, v85\n"
    "v_mfma_f32_16x16x32_bf16 v[52:55], v[176:179], a[220:223], v[52:55]\n"
    "v_exp_f32 v92, v92\n"
    "v_mfma_f32_16x16x32_bf16 v[52:55], v[180:183], a[224:227], v[52:55]\n"
    "v_exp_f32 v93, v93\n"
    "v_mfma_f32_16x16x32_bf16 v[48:51], v[180:183], a[200:203], v[48:51]\n"
    "v_cvt_pk_bf16_f32 v151, v86, v87\n"
    "v_cvt_pk_bf16_f32 v154, v88, v89\n"
    "v_mfma_f32_16x16x32_bf16 v[48:51], v[176:179], a[196:199], v[48:51]\n"
    "v_exp_f32 v94, v94\n"
    "v_mfma_f32_16x16x32_bf16 v[48:51], v[172:175], a[192:195], v[48:51]\n"
    "s_waitcnt lgkmcnt(1)\n"
    "v_cvt_pk_bf16_f32 v155, v90, v91\n"
    "v_cvt_pk_bf16_f32 v158, v92, v93\n"
    "v_mfma_f32_16x16x32_bf16 a[0:3], v[160:163], v[128:131], a[0:3]\n"
    "ds_read_b128 v[172:175], v201 offset:3072\n"
    "ds_read_b128 v[176:179], v201 offset:4096\n"
    "v_mfma_f32_16x16x32_bf16 a[0:3], v[164:167], v[144:147], a[0:3]\n"
    "v_exp_f32 v95, v95\n"
    "v_mfma_f32_16x16x32_bf16 a[4:7], v[160:163], v[132:135], a[4:7]\n"
    "v_cvt_pk_bf16_f32 v159, v94, v95\n"
    "v_add_f32 v113, v113, v64\n"
    "v_mfma_f32_16x16x32_bf16 a[4:7], v[164:167], v[148:151], a[4:7]\n"
    "v_exp_f32 v32, v32\n"
    "v_mfma_f32_16x16x32_bf16 a[8:11], v[160:163], v[136:139], a[8:11]\n"
    "ds_read_b128 v[180:183], v201 offset:5120\n"
    "v_add_f32 v114, v114, v65\n"
    "v_mfma_f32_16x16x32_bf16 a[8:11], v[164:167], v[152:155], a[8:11]\n"
    "v_exp_f32 v33, v33\n"
    "v_mfma_f32_16x16x32_bf16 a[12:15], v[160:163], v[140:143], a[12:15]\n"
    "v_exp_f32 v34, v34\n"
    "v_mfma_f32_16x16x32_bf16 a[12:15], v[164:167], v[156:159], a[12:15]\n"
    "s_waitcnt lgkmcnt(2)\n"
    "v_exp_f32 v35, v35\n"
    "v_mfma_f32_16x16x32_bf16 a[28:31], v[168:171], v[140:143], a[28:31]\n"
    "ds_read_b128 v[160:163], v201 offset:6144\n"
    "v_add_f32 v113, v113, v66\n"
    "v_mfma_f32_16x16x32_bf16 a[28:31], v[172:175], v[156:159], a[28:31]\n"
    "v_exp_f32 v36, v36\n"
    "v_mfma_f32_16x16x32_bf16 a[24:27], v[172:175], v[152:155], a[24:27]\n"
    "v_exp_f32 v37, v37\n"
    "v_mfma_f32_16x16x32_bf16 a[24:27], v[168:171], v[136:139], a[24:27]\n"
    "v_exp_f32 v38, v38\n"
    "v_mfma_f32_16x16x32_bf16 a[20:23], v[168:171], v[132:135], a[20:23]\n"
    "ds_read_b128 v[164:167], v201 offset:7168\n"
    "v_add_f32 v114, v114, v67\n"
    "v_mfma_f32_16x16x32_bf16 a[20:23], v[172:175], v[148:151], a[20:23]\n"
    "v_exp_f32 v39, v39\n"
    "v_mfma_f32_16x16x32_bf16 a[16:19], v[172:175], v[144:147], a[16:19]\n"
    "v_exp_f32 v40, v40\n"
    "v_mfma_f32_16x16x32_bf16 a[16:19], v[168:171], v[128:131], a[16:19]\n"
    "s_waitcnt lgkmcnt(2)\n"
    "v_exp_f32 v41, v41\n"
    "v_mfma_f32_16x16x32_bf16 a[32:35], v[176:179], v[128:131], a[32:35]\n"
    "ds_read_b128 v[168:171], v201 offset:8192\n"
    "v_add_f32 v117, v117, v68\n"
    "v_mfma_f32_16x16x32_bf16 a[32:35], v[180:183], v[144:147], a[32:35]\n"
    "v_exp_f32 v42, v42\n"
    "v_mfma_f32_16x16x32_bf16 a[36:39], v[180:183], v[148:151], a[36:39]\n"
    "v_exp_f32 v43, v43\n"
    "v_mfma_f32_16x16x32_bf16 a[36:39], v[176:179], v[132:135], a[36:39]\n"
    "v_exp_f32 v44, v44\n"
    "v_mfma_f32_16x16x32_bf16 a[40:43], v[176:179], v[136:139], a[40:43]\n"
    "ds_read_b128 v[172:175], v201 offset:9216\n"
    "v_add_f32 v118, v118, v69\n"
    "v_mfma_f32_16x16x32_bf16 a[40:43], v[180:183], v[152:155], a[40:43]\n"
    "v_exp_f32 v45, v45\n"
    "v_mfma_f32_16x16x32_bf16 a[44:47], v[180:183], v[156:159], a[44:47]\n"
    "v_exp_f32 v46, v46\n"
    "v_mfma_f32_16x16x32_bf16 a[44:47], v[176:179], v[140:143], a[44:47]\n"
    "s_waitcnt lgkmcnt(2)\n"
    "v_exp_f32 v47, v47\n"
    "v_mfma_f32_16x16x32_bf16 a[60:63], v[160:163], v[140:143], a[60:63]\n"
    "ds_read_b128 v[176:179], v201 offset:10240\n"
    "v_add_f32 v117, v117, v70\n"
    "v_mfma_f32_16x16x32_bf16 a[60:63], v[164:167], v[156:159], a[60:63]\n"
    "v_exp_f32 v48, v48\n"
    "v_mfma_f32_16x16x32_bf16 a[56:59], v[164:167], v[152:155], a[56:59]\n"
    "v_exp_f32 v49, v49\n"
    "v_mfma_f32_16x16x32_bf16 a[56:59], v[160:163], v[136:139], a[56:59]\n"
    "v_exp_f32 v50, v50\n"
    "v_mfma_f32_16x16x32_bf16 a[52:55], v[160:163], v[132:135], a[52:55]\n"
    "ds_read_b128 v[180:183], v201 offset:11264\n"
    "v_add_f32 v113, v113, v32\n"
    "v_mfma_f32_16x16x32_bf16 a[52:55], v[164:167], v[148:151], a[52:55]\n"
    "v_exp_f32 v51, v51\n"
    "v_mfma_f32_16x16x32_bf16 a[48:51], v[164:167], v[144:147], a[48:51]\n"
    "v_exp_f32 v52, v52\n"
    "v_mfma_f32_16x16x32_bf16 a[48:51], v[160:163], v[128:131], a[48:51]\n"
    "s_waitcnt lgkmcnt(2)\n"
    "v_add_f32 v118, v118, v71\n"
    "v_add_f32 v114, v114, v33\n"
    "v_mfma_f32_16x16x32_bf16 a[64:67], v[168:171], v[128:131], a[64:67]\n"
    "ds_read_b128 v[160:163], v201 offset:12288\n"
    "v_add_f32 v121, v121, v72\n"
    "v_mfma_f32_16x16x32_bf16 a[64:67], v[172:175], v[144:147], a[64:67]\n"
    "v_add_f32 v113, v113, v34\n"
    "v_add_f32 v122, v122, v73\n"
    "v_mfma_f32_16x16x32_bf16 a[68:71], v[172:175], v[148:151], a[68:71]\n"
    "v_exp_f32 v53, v53\n"
    "v_mfma_f32_16x16x32_bf16 a[68:71], v[168:171], v[132:135], a[68:71]\n"
    "s_add_u32 m0, s51, s95\n"
    "v_add_f32 v114, v114, v35\n"
    "v_add_f32 v121, v121, v74\n"
    "v_mfma_f32_16x16x32_bf16 a[72:75], v[168:171], v[136:139], a[72:75]\n"
    "ds_read_b128 v[164:167], v201 offset:13312\n"
    "v_add_f32 v117, v117, v36\n"
    "v_mfma_f32_16x16x32_bf16 a[72:75], v[172:175], v[152:155], a[72:75]\n"
    "buffer_load_dwordx4 v202, s[32:35], s64 offen lds\n"
    "v_mfma_f32_16x16x32_bf16 a[76:79], v[172:175], v[156:159], a[76:79]\n"
    "v_exp_f32 v54, v54\n"
    "v_mfma_f32_16x16x32_bf16 a[76:79], v[168:171], v[140:143], a[76:79]\n"
    "s_waitcnt lgkmcnt(2)\n"
    "v_add_f32 v122, v122, v75\n"
    "v_add_f32 v118, v118, v37\n"
    "v_mfma_f32_16x16x32_bf16 a[92:95], v[176:179], v[140:143], a[92:95]\n"
    "ds_read_b128 v[168:171], v201 offset:14336\n"
    "v_add_f32 v125, v125, v76\n"
    "v_mfma_f32_16x16x32_bf16 a[92:95], v[180:183], v[156:159], a[92:95]\n"
    "buffer_load_dwordx4 v203, s[32:35], s64 offen offset:1024 lds\n"
    "v_mfma_f32_16x16x32_bf16 a[88:91], v[180:183], v[152:155], a[88:91]\n"
    "v_add_f32 v117, v117, v38\n"
    "v_add_f32 v126, v126, v77\n"
    "v_mfma_f32_16x16x32_bf16 a[88:91], v[176:179], v[136:139], a[88:91]\n"
    "v_exp_f32 v55, v55\n"
    "v_mfma_f32_16x16x32_bf16 a[84:87], v[176:179], v[132:135], a[84:87]\n"
    "ds_read_b128 v[172:175], v201 offset:15360\n"
    "v_add_f32 v118, v118, v39\n"
    "v_mfma_f32_16x16x32_bf16 a[84:87], v[180:183], v[148:151], a[84:87]\n"
    "buffer_load_dwordx4 v204, s[32:35], s64 offen offset:2048 lds\n"
    "v_mfma_f32_16x16x32_bf16 a[80:83], v[180:183], v[144:147], a[80:83]\n"
    "v_add_f32 v125, v125, v78\n"
    "v_add_f32 v121, v121, v40\n"
    "v_mfma_f32_16x16x32_bf16 a[80:83], v[176:179], v[128:131], a[80:83]\n"
    "s_waitcnt lgkmcnt(2)\n"
    "v_exp_f32 v56, v56\n"
    "v_mfma_f32_16x16x32_bf16 a[96:99], v[160:163], v[128:131], a[96:99]\n"
    "s_mov_b32 s95, s94\n"
    "ds_read_b128 v[176:179], v201 offset:16384\n"
    "v_add_f32 v126, v126, v79\n"
    "v_mfma_f32_16x16x32_bf16 a[96:99], v[164:167], v[144:147], a[96:99]\n"
    "s_sub_u32 s94, s94, 0x3000\n"
    "v_add_f32 v122, v122, v41\n"
    "v_add_f32 v113, v113, v80\n"
    "v_mfma_f32_16x16x32_bf16 a[100:103], v[164:167], v[148:151], a[100:103]\n"
    "s_min_u32 s94, s94, 0x6000\n"
    "v_add_f32 v121, v121, v42\n"
    "v_add_f32 v114, v114, v81\n"
    "v_mfma_f32_16x16x32_bf16 a[100:103], v[160:163], v[132:135], a[100:103]\n"
    "s_sub_u32 s93, s94, s95\n"
    "v_exp_f32 v57, v57\n"
    "v_mfma_f32_16x16x32_bf16 a[104:107], v[160:163], v[136:139], a[104:107]\n"
    "ds_read_b128 v[180:183], v201 offset:17408\n"
    "v_add_f32 v122, v122, v43\n"
    "v_mfma_f32_16x16x32_bf16 a[104:107], v[164:167], v[152:155], a[104:107]\n"
    "v_add_f32 v113, v113, v82\n"
    "v_add_f32 v125, v125, v44\n"
    "v_mfma_f32_16x16x32_bf16 a[108:111], v[164:167], v[156:159], a[108:111]\n"
    "v_exp_f32 v58, v58\n"
    "v_mfma_f32_16x16x32_bf16 a[108:111], v[160:163], v[140:143], a[108:111]\n"
    "s_waitcnt lgkmcnt(2)\n"
    "v_add_f32 v114, v114, v83\n"
    "v_add_f32 v126, v126, v45\n"
    "v_mfma_f32_16x16x32_bf16 a[124:127], v[168:171], v[140:143], a[124:127]\n"
    "ds_read_b128 v[160:163], v201 offset:18432\n"
    "v_add_f32 v117, v117, v84\n"
    "v_mfma_f32_16x16x32_bf16 a[124:127], v[172:175], v[156:159], a[124:127]\n"
    "v_add_f32 v125, v125, v46\n"
    "v_add_f32 v118, v118, v85\n"
    "v_mfma_f32_16x16x32_bf16 a[120:123], v[172:175], v[152:155], a[120:123]\n"
    "v_exp_f32 v59, v59\n"
    "v_mfma_f32_16x16x32_bf16 a[120:123], v[168:171], v[136:139], a[120:123]\n"
    "v_add_f32 v126, v126, v47\n"
    "v_add_f32 v117, v117, v86\n"
    "v_mfma_f32_16x16x32_bf16 a[116:119], v[168:171], v[132:135], a[116:119]\n"
    "ds_read_b128 v[164:167], v201 offset:19456\n"
    "v_add_f32 v113, v113, v48\n"
    "v_mfma_f32_16x16x32_bf16 a[116:119], v[172:175], v[148:151], a[116:119]\n"
    "v_exp_f32 v60, v60\n"
    "v_mfma_f32_16x16x32_bf16 a[112:115], v[172:175], v[144:147], a[112:115]\n"
    "v_add_u32 v205, s93, v205\n"
    "v_add_f32 v118, v118, v87\n"
    "v_mfma_f32_16x16x32_bf16 a[112:115], v[168:171], v[128:131], a[112:115]\n"
    "s_waitcnt lgkmcnt(2)\n"
    "v_add_f32 v114, v114, v49\n"
    "v_add_u32 v209, 0x180, v209\n"
    "v_mfma_f32_16x16x32_bf16 a[128:131], v[176:179], v[128:131], a[128:131]\n"
    "ds_read_b128 v[168:171], v201 offset:20480\n"
    "v_add_u32 v206, s93, v206\n"
    "v_mfma_f32_16x16x32_bf16 a[128:131], v[180:183], v[144:147], a[128:131]\n"
    "v_add_f32 v121, v121, v88\n"
    "v_add_f32 v113, v113, v50\n"
    "v_mfma_f32_16x16x32_bf16 a[132:135], v[180:183], v[148:151], a[132:135]\n"
    "s_add_u32 s64, s64, 0xc000\n"
    "v_exp_f32 v61, v61\n"
    "v_mfma_f32_16x16x32_bf16 a[132:135], v[176:179], v[132:135], a[132:135]\n"
    "s_add_u32 s62, s62, 0x6000\n"
    "v_add_u32 v210, 0x80, v210\n"
    "v_add_u32 v207, s93, v207\n"
    "v_mfma_f32_16x16x32_bf16 a[136:139], v[176:179], v[136:139], a[136:139]\n"
    "s_add_u32 s63, s63, 0x6000\n"
    "ds_read_b128 v[172:175], v201 offset:21504\n"
    "v_add_f32 v122, v122, v89\n"
    "v_mfma_f32_16x16x32_bf16 a[136:139], v[180:183], v[152:155], a[136:139]\n"
    "s_add_u32 s60, s60, 0x6000\n"
    "v_add_f32 v114, v114, v51\n"
    "v_add_f32 v121, v121, v90\n"
    "v_mfma_f32_16x16x32_bf16 a[140:143], v[180:183], v[156:159], a[140:143]\n"
    "s_add_u32 s61, s61, 0x6000\n"
    "v_add_f32 v117, v117, v52\n"
    "v_add_f32 v122, v122, v91\n"
    "v_mfma_f32_16x16x32_bf16 a[140:143], v[176:179], v[140:143], a[140:143]\n"
    "s_waitcnt lgkmcnt(2)\n"
    "v_exp_f32 v62, v62\n"
    "v_mfma_f32_16x16x32_bf16 a[156:159], v[160:163], v[140:143], a[156:159]\n"
    "ds_read_b128 v[176:179], v201 offset:22528\n"
    "v_add_f32 v118, v118, v53\n"
    "v_mfma_f32_16x16x32_bf16 a[156:159], v[164:167], v[156:159], a[156:159]\n"
    "s_waitcnt vmcnt(3)\n"
    "ds_read_b128 v[180:183], v201 offset:23552\n"
    "v_add_f32 v125, v125, v92\n"
    "v_mfma_f32_16x16x32_bf16 a[152:155], v[164:167], v[152:155], a[152:155]\n"
    "v_add_f32 v117, v117, v54\n"
    "v_add_f32 v126, v126, v93\n"
    "v_mfma_f32_16x16x32_bf16 a[152:155], v[160:163], v[136:139], a[152:155]\n"
    "v_exp_f32 v63, v63\n"
    "v_mfma_f32_16x16x32_bf16 a[148:151], v[160:163], v[132:135], a[148:151]\n"
    "v_add_f32 v118, v118, v55\n"
    "v_add_f32 v125, v125, v94\n"
    "v_mfma_f32_16x16x32_bf16 a[148:151], v[164:167], v[148:151], a[148:151]\n"
    "v_add_f32 v121, v121, v56\n"
    "v_add_f32 v126, v126, v95\n"
    "v_mfma_f32_16x16x32_bf16 a[144:147], v[164:167], v[144:147], a[144:147]\n"
    "v_add_f32 v122, v122, v57\n"
    "v_mfma_f32_16x16x32_bf16 a[144:147], v[160:163], v[128:131], a[144:147]\n"
    "s_waitcnt lgkmcnt(0)\n"
    "v_mfma_f32_16x16x32_bf16 a[160:163], v[168:171], v[128:131], a[160:163]\n"
    "s_barrier\n"
    "v_mfma_f32_16x16x32_bf16 a[160:163], v[172:175], v[144:147], a[160:163]\n"
    "ds_read_u16_d16_hi v223, v209 offset:6144\n"
    "ds_read_u16_d16_hi v222, v209\n"
    "v_mfma_f32_16x16x32_bf16 a[164:167], v[172:175], v[148:151], a[164:167]\n"
    "ds_read_b128 v[160:163], v200\n"
    "v_add_f32 v121, v121, v58\n"
    "v_mfma_f32_16x16x32_bf16 a[164:167], v[168:171], v[132:135], a[164:167]\n"
    "ds_read_b128 v[232:235], v205 offset:24576\n"
    "v_add_f32 v122, v122, v59\n"
    "v_mfma_f32_16x16x32_bf16 a[168:171], v[168:171], v[136:139], a[168:171]\n"
    "ds_read_b128 v[164:167], v200 offset:1024\n"
    "ds_read_b128 v[236:239], v206 offset:24576\n"
    "v_mfma_f32_16x16x32_bf16 a[168:171], v[172:175], v[152:155], a[168:171]\n"
    "v_add_f32 v125, v125, v60\n"
    "v_add_f32 v126, v126, v61\n"
    "v_mfma_f32_16x16x32_bf16 a[172:175], v[172:175], v[156:159], a[172:175]\n"
    "v_add_f32 v125, v125, v62\n"
    "v_add_f32 v126, v126, v63\n"
    "v_mfma_f32_16x16x32_bf16 a[172:175], v[168:171], v[140:143], a[172:175]\n"
    "ds_read_b64 v[228:229], v210\n"
    "ds_read2st64_b32 v[240:241], v207 offset0:96 offset1:99\n"
    "v_mfma_f32_16x16x32_bf16 a[188:191], v[176:179], v[140:143], a[188:191]\n"
    "ds_read_b128 v[168:171], v200 offset:2048\n"
    "ds_read2st64_b32 v[242:243], v207 offset0:102 offset1:105\n"
    "v_mfma_f32_16x16x32_bf16 a[188:191], v[180:183], v[156:159], a[188:191]\n"
    "ds_read2st64_b32 v[244:245], v207 offset0:108 offset1:111\n"
    "ds_read2st64_b32 v[246:247], v207 offset0:114 offset1:117\n"
    "v_mfma_f32_16x16x32_bf16 a[184:187], v[176:179], v[136:139], a[184:187]\n"
    "v_cvt_pk_bf16_f32 v140, v44, v45\n"
    "v_cvt_pk_bf16_f32 v141, v46, v47\n"
    "v_mfma_f32_16x16x32_bf16 a[184:187], v[180:183], v[152:155], a[184:187]\n"
    "s_waitcnt lgkmcnt(6)\n"
    "v_mul_f32 v226, s75, v223\n"
    "v_mfma_f32_16x16x32_bf16 a[180:183], v[176:179], v[132:135], a[180:183]\n"
    "ds_read_b128 v[172:175], v200 offset:3072\n"
    "v_fma_f32 v227, -v222, v226, v217\n"
    "v_mfma_f32_16x16x32_bf16 a[180:183], v[180:183], v[148:151], a[180:183]\n"
    "v_cvt_f32_i32 v232, v232\n"
    "v_cvt_f32_i32 v233, v233\n"
    "v_mfma_f32_16x16x32_bf16 a[176:179], v[176:179], v[128:131], a[176:179]\n"
    "v_cvt_f32_i32 v234, v234\n"
    "v_cvt_f32_i32 v235, v235\n"
    "v_mfma_f32_16x16x32_bf16 a[176:179], v[180:183], v[144:147], a[176:179]\n"
    "v_cvt_f32_i32 v236, v236\n"
    "v_cvt_f32_i32 v237, v237\n"
    "LM01:\n"
    "v_mfma_f32_16x16x32_bf16 v[64:67], v[160:163], a[192:195], v[96:99]\n"
    "ds_read_b128 v[176:179], v200 offset:4096\n"
    "v_cvt_f32_i32 v238, v238\n"
    "v_mfma_f32_16x16x32_bf16 v[64:67], v[164:167], a[196:199], v[64:67]\n"
    "s_waitcnt lgkmcnt(1)\n"
    "v_cvt_f32_i32 v239, v239\n"
    "v_mul_f32_dpp v232, v226, v232 row_newbcast:0 row_mask:0xf bank_mask:0xf\n"
    "v_mfma_f32_16x16x32_bf16 v[64:67], v[168:171], a[200:203], v[64:67]\n"
    "v_mul_f32_dpp v233, v226, v233 row_newbcast:1 row_mask:0xf bank_mask:0xf\n"
    "v_mul_f32_dpp v234, v226, v234 row_newbcast:2 row_mask:0xf bank_mask:0xf\n"
    "v_mfma_f32_16x16x32_bf16 v[68:71], v[168:171], a[224:227], v[100:103]\n"
    "v_mul_f32_dpp v235, v226, v235 row_newbcast:3 row_mask:0xf bank_mask:0xf\n"
    "v_mul_f32_dpp v236, v226, v236 row_newbcast:4 row_mask:0xf bank_mask:0xf\n"
    "v_mfma_f32_16x16x32_bf16 v[68:71], v[164:167], a[220:223], v[68:71]\n"
    "ds_read_b128 v[180:183], v200 offset:5120\n"
    "v_mul_f32_dpp v237, v226, v237 row_newbcast:5 row_mask:0xf bank_mask:0xf\n"
    "v_mfma_f32_16x16x32_bf16 v[68:71], v[160:163], a[216:219], v[68:71]\n"
    "v_mul_f32_dpp v238, v226, v238 row_newbcast:6 row_mask:0xf bank_mask:0xf\n"
    "v_mul_f32_dpp v239, v226, v239 row_newbcast:7 row_mask:0xf bank_mask:0xf\n"
    "v_mfma_f32_16x16x32_bf16 v[72:75], v[160:163], a[240:243], v[104:107]\n"
    "v_add_f32_dpp v232, v227, v232 row_newbcast:0 row_mask:0xf bank_mask:0xf\n"
    "v_add_f32_dpp v233, v227, v233 row_newbcast:1 row_mask:0xf bank_mask:0xf\n"
    "v_mfma_f32_16x16x32_bf16 v[72:75], v[164:167], a[244:247], v[72:75]\n"
    "v_add_f32_dpp v234, v227, v234 row_newbcast:2 row_mask:0xf bank_mask:0xf\n"
    "v_add_f32_dpp v235, v227, v235 row_newbcast:3 row_mask:0xf bank_mask:0xf\n"
    "v_mfma_f32_16x16x32_bf16 v[72:75], v[168:171], a[248:251], v[72:75]\n"
    "v_add_f32_dpp v236, v227, v236 row_newbcast:4 row_mask:0xf bank_mask:0xf\n"
    "v_add_f32_dpp v237, v227, v237 row_newbcast:5 row_mask:0xf bank_mask:0xf\n"
    "v_mfma_f32_16x16x32_bf16 v[76:79], v[168:171], v[16:19], v[108:111]\n"
    "v_add_f32_dpp v238, v227, v238 row_newbcast:6 row_mask:0xf bank_mask:0xf\n"
    "v_add_f32_dpp v239, v227, v239 row_newbcast:7 row_mask:0xf bank_mask:0xf\n"
    "v_mfma_f32_16x16x32_bf16 v[76:79], v[164:167], v[12:15], v[76:79]\n"
    "v_cvt_pk_bf16_f32 v232, v232, v233\n"
    "v_cvt_f32_i32 v240, v240\n"
    "v_mfma_f32_16x16x32_bf16 v[76:79], v[160:163], v[8:11], v[76:79]\n"
    "s_waitcnt lgkmcnt(1)\n"
    "v_cvt_pk_bf16_f32 v233, v234, v235\n"
    "v_cvt_f32_i32 v241, v241\n"
    "v_mfma_f32_16x16x32_bf16 v[76:79], v[172:175], v[20:23], v[76:79]\n"
    "ds_read_b128 v[160:163], v200 offset:9216\n"
    "v_cvt_pk_bf16_f32 v234, v236, v237\n"
    "v_mfma_f32_16x16x32_bf16 v[76:79], v[176:179], v[24:27], v[76:79]\n"
    "s_waitcnt lgkmcnt(1)\n"
    "v_cvt_f32_i32 v242, v242\n"
    "v_cvt_pk_bf16_f32 v235, v238, v239\n"
    "v_mfma_f32_16x16x32_bf16 v[76:79], v[180:183], v[28:31], v[76:79]\n"
    "v_cvt_f32_i32 v243, v243\n"
    "v_cvt_f32_i32 v244, v244\n"
    "v_mfma_f32_16x16x32_bf16 v[72:75], v[180:183], v[4:7], v[72:75]\n"
    "ds_read_b128 v[164:167], v200 offset:10240\n"
    "v_cvt_f32_i32 v245, v245\n"
    "v_mfma_f32_16x16x32_bf16 v[72:75], v[176:179], v[0:3], v[72:75]\n"
    "buffer_store_dwordx4 v[232:235], v211, s[36:39], s62 offen sc1\n"
    "v_cvt_f32_i32 v246, v246\n"
    "v_cvt_f32_i32 v247, v247\n"
    "v_mul_f32_dpp v240, v228, v240 row_newbcast:0 row_mask:0xf bank_mask:0xf\n"
    "v_mfma_f32_16x16x32_bf16 v[72:75], v[172:175], a[252:255], v[72:75]\n"
    "v_mul_f32_dpp v241, v228, v241 row_newbcast:1 row_mask:0xf bank_mask:0xf\n"
    "v_mul_f32_dpp v242, v228, v242 row_newbcast:2 row_mask:0xf bank_mask:0xf\n"
    "v_mfma_f32_16x16x32_bf16 v[68:71], v[172:175], a[228:231], v[68:71]\n"
    "ds_read_b128 v[168:171], v200 offset:11264\n"
    "v_mul_f32_dpp v243, v228, v243 row_newbcast:3 row_mask:0xf bank_mask:0xf\n"
    "v_mfma_f32_16x16x32_bf16 v[68:71], v[176:179], a[232:235], v[68:71]\n"
    "v_mul_f32_dpp v244, v228, v244 row_newbcast:4 row_mask:0xf bank_mask:0xf\n"
    "v_mul_f32_dpp v245, v228, v245 row_newbcast:5 row_mask:0xf bank_mask:0xf\n"
    "v_mfma_f32_16x16x32_bf16 v[68:71], v[180:183], a[236:239], v[68:71]\n"
    "v_mul_f32_dpp v246, v228, v246 row_newbcast:6 row_mask:0xf bank_mask:0xf\n"
    "v_mul_f32_dpp v247, v228, v247 row_newbcast:7 row_mask:0xf bank_mask:0xf\n"
    "v_mfma_f32_16x16x32_bf16 v[64:67], v[180:183], a[212:215], v[64:67]\n"
    "v_add_f32_dpp v240, v229, v240 row_newbcast:0 row_mask:0xf bank_mask:0xf\n"
    "v_add_f32_dpp v241, v229, v241 row_newbcast:1 row_mask:0xf bank_mask:0xf\n"
    "v_mfma_f32_16x16x32_bf16 v[64:67], v[176:179], a[208:211], v[64:67]\n"
    "v_add_f32_dpp v242, v229, v242 row_newbcast:2 row_mask:0xf bank_mask:0xf\n"
    "v_add_f32_dpp v243, v229, v243 row_newbcast:3 row_mask:0xf bank_mask:0xf\n"
    "v_mfma_f32_16x16x32_bf16 v[64:67], v[172:175], a[204:207], v[64:67]\n"
    "s_waitcnt lgkmcnt(1)\n"
    "v_add_f32_dpp v244, v229, v244 row_newbcast:4 row_mask:0xf bank_mask:0xf\n"
    "v_add_f32_dpp v245, v229, v245 row_newbcast:5 row_mask:0xf bank_mask:0xf\n"
    "v_mfma_f32_16x16x32_bf16 v[80:83], v[160:163], a[204:207], v[96:99]\n"
    "ds_read_b128 v[172:175], v200 offset:6144\n"
    "v_add_f32_dpp v246, v229, v246 row_newbcast:6 row_mask:0xf bank_mask:0xf\n"
    "v_mfma_f32_16x16x32_bf16 v[80:83], v[164:167], a[208:211], v[80:83]\n"
    "s_waitcnt lgkmcnt(1)\n"
    "v_add_f32_dpp v247, v229, v247 row_newbcast:7 row_mask:0xf bank_mask:0xf\n"
    "v_cvt_pk_bf16_f32 v240, v240, v241\n"
    "v_mfma_f32_16x16x32_bf16 v[80:83], v[168:171], a[212:215], v[80:83]\n"
    "v_cvt_pk_bf16_f32 v241, v242, v243\n"
    "v_cvt_pk_bf16_f32 v242, v244, v245\n"
    "v_mfma_f32_16x16x32_bf16 v[84:87], v[168:171], a[236:239], v[100:103]\n"
    "ds_read_b128 v[176:179], v200 offset:7168\n"
    "v_cvt_pk_bf16_f32 v243, v246, v247\n"
    "v_mfma_f32_16x16x32_bf16 v[84:87], v[164:167], a[232:235], v[84:87]\n"
    "buffer_store_dwordx4 v[240:243], v212, s[40:43], s63 offen sc1\n"
    "v_cvt_pk_bf16_f32 v136, v40, v41\n"
    "v_cvt_pk_bf16_f32 v137, v42, v43\n"
    "v_cvt_pk_bf16_f32 v132, v36, v37\n"
    "v_mfma_f32_16x16x32_bf16 v[84:87], v[160:163], a[228:231], v[84:87]\n"
    "v_cvt_pk_bf16_f32 v133, v38, v39\n"
    "v_cvt_pk_bf16_f32 v128, v32, v33\n"
    "v_mfma_f32_16x16x32_bf16 v[88:91], v[160:163], a[252:255], v[104:107]\n"
    "ds_read_b128 v[180:183], v200 offset:8192\n"
    "v_cvt_pk_bf16_f32 v129, v34, v35\n"
    "v_mfma_f32_16x16x32_bf16 v[88:91], v[164:167], v[0:3], v[88:91]\n"
    "v_exp_f32 v64, v64\n"
    "v_mfma_f32_16x16x32_bf16 v[88:91], v[168:171], v[4:7], v[88:91]\n"
    "v_exp_f32 v65, v65\n"
    "v_mfma_f32_16x16x32_bf16 v[92:95], v[168:171], v[28:31], v[108:111]\n"
    "v_exp_f32 v66, v66\n"
    "v_mfma_f32_16x16x32_bf16 v[92:95], v[164:167], v[24:27], v[92:95]\n"
    "v_exp_f32 v67, v67\n"
    "v_mfma_f32_16x16x32_bf16 v[92:95], v[160:163], v[20:23], v[92:95]\n"
    "s_waitcnt lgkmcnt(1)\n"
    "v_exp_f32 v68, v68\n"
    "v_mfma_f32_16x16x32_bf16 v[92:95], v[172:175], v[8:11], v[92:95]\n"
    "ds_read_b128 v[160:163], v200 offset:12288\n"
    "v_cvt_pk_bf16_f32 v142, v60, v61\n"
    "v_mfma_f32_16x16x32_bf16 v[92:95], v[176:179], v[12:15], v[92:95]\n"
    "s_waitcnt lgkmcnt(1)\n"
    "s_add_u32 s65, s60, s92\n"
    "v_cvt_pk_bf16_f32 v143, v62, v63\n"
    "v_mfma_f32_16x16x32_bf16 v[92:95], v[180:183], v[16:19], v[92:95]\n"
    "s_add_u32 m0, s90, 0x6000\n"
    "v_exp_f32 v69, v69\n"
    "v_mfma_f32_16x16x32_bf16 v[88:91], v[180:183], a[248:251], v[88:91]\n"
    "ds_read_b128 v[164:167], v200 offset:13312\n"
    "v_cvt_pk_bf16_f32 v138, v56, v57\n"
    "v_mfma_f32_16x16x32_bf16 v[88:91], v[176:179], a[244:247], v[88:91]\n"
    "buffer_load_dwordx4 v200, s[36:39], s65 offen sc1 lds\n"
    "v_mfma_f32_16x16x32_bf16 v[88:91], v[172:175], a[240:243], v[88:91]\n"
    "v_exp_f32 v70, v70\n"
    "v_mfma_f32_16x16x32_bf16 v[84:87], v[172:175], a[216:219], v[84:87]\n"
    "ds_read_b128 v[168:171], v200 offset:14336\n"
    "v_cvt_pk_bf16_f32 v139, v58, v59\n"
    "v_mfma_f32_16x16x32_bf16 v[84:87], v[176:179], a[220:223], v[84:87]\n"
    "v_exp_f32 v71, v71\n"
    "v_mfma_f32_16x16x32_bf16 v[84:87], v[180:183], a[224:227], v[84:87]\n"
    "buffer_load_dwordx4 v200, s[36:39], s65 offen offset:1024 sc1 lds\n"
    "v_mfma_f32_16x16x32_bf16 v[80:83], v[180:183], a[200:203], v[80:83]\n"
    "v_exp_f32 v72, v72\n"
    "v_mfma_f32_16x16x32_bf16 v[80:83], v[176:179], a[196:199], v[80:83]\n"
    "v_cvt_pk_bf16_f32 v134, v52, v53\n"
    "v_cvt_pk_bf16_f32 v135, v54, v55\n"
    "v_mfma_f32_16x16x32_bf16 v[80:83], v[172:175], a[192:195], v[80:83]\n"
    "s_waitcnt lgkmcnt(1)\n"
    "v_exp_f32 v73, v73\n"
    "v_mfma_f32_16x16x32_bf16 v[32:35], v[160:163], a[192:195], v[96:99]\n"
    "ds_read_b128 v[172:175], v200 offset:15360\n"
    "v_cvt_pk_bf16_f32 v130, v48, v49\n"
    "v_mfma_f32_16x16x32_bf16 v[32:35], v[164:167], a[196:199], v[32:35]\n"
    "s_waitcnt lgkmcnt(1)\n"
    "buffer_load_dwordx4 v200, s[36:39], s65 offen offset:2048 sc1 lds\n"
    "v_mfma_f32_16x16x32_bf16 v[32:35], v[168:171], a[200:203], v[32:35]\n"
    "v_cvt_pk_bf16_f32 v131, v50, v51\n"
    "v_cvt_pk_bf16_f32 v144, v64, v65\n"
    "v_mfma_f32_16x16x32_bf16 v[36:39], v[168:171], a[224:227], v[100:103]\n"
    "ds_read_b128 v[176:179], v200 offset:16384\n"
    "v_cvt_pk_bf16_f32 v145, v66, v67\n"
    "v_mfma_f32_16x16x32_bf16 v[36:39], v[164:167], a[220:223], v[36:39]\n"
    "v_exp_f32 v74, v74\n"
    "v_mfma_f32_16x16x32_bf16 v[36:39], v[160:163], a[216:219], v[36:39]\n"
    "buffer_load_dwordx4 v200, s[36:39], s65 offen offset:3072 sc1 lds\n"
    "v_mfma_f32_16x16x32_bf16 v[40:43], v[160:163], a[240:243], v[104:107]\n"
    "s_add_u32 s65, s65, 0x1000\n"
    "ds_read_b128 v[180:183], v200 offset:17408\n"
    "v_cvt_pk_bf16_f32 v148, v68, v69\n"
    "v_mfma_f32_16x16x32_bf16 v[40:43], v[164:167], a[244:247], v[40:43]\n"
    "s_add_u32 m0, s90, 0x7000\n"
    "v_exp_f32 v75, v75\n"
    "v_mfma_f32_16x16x32_bf16 v[40:43], v[168:171], a[248:251], v[40:43]\n"
    "v_exp_f32 v76, v76\n"
    "v_mfma_f32_16x16x32_bf16 v[44:47], v[168:171], v[16:19], v[108:111]\n"
    "buffer_load_dwordx4 v200, s[36:39], s65 offen sc1 lds\n"
    "v_mfma_f32_16x16x32_bf16 v[44:47], v[164:167], v[12:15], v[44:47]\n"
    "v_exp_f32 v77, v77\n"
    "v_mfma_f32_16x16x32_bf16 v[44:47], v[160:163], v[8:11], v[44:47]\n"
    "s_waitcnt lgkmcnt(1)\n"
    "v_exp_f32 v78, v78\n"
    "v_mfma_f32_16x16x32_bf16 v[44:47], v[172:175], v[20:23], v[44:47]\n"
    "ds_read_b128 v[160:163], v200 offset:21504\n"
    "v_cvt_pk_bf16_f32 v149, v70, v71\n"
    "v_mfma_f32_16x16x32_bf16 v[44:47], v[176:179], v[24:27], v[44:47]\n"
    "s_waitcnt lgkmcnt(1)\n"
    "buffer_load_dwordx4 v200, s[36:39], s65 offen offset:1024 sc1 lds\n"
    "v_mfma_f32_16x16x32_bf16 v[44:47], v[180:183], v[28:31], v[44:47]\n"
    "v_exp_f32 v79, v79\n"
    "v_mfma_f32_16x16x32_bf16 v[40:43], v[180:183], v[4:7], v[40:43]\n"
    "ds_read_b128 v[164:167], v200 offset:22528\n"
    "v_cvt_pk_bf16_f32 v152, v72, v73\n"
    "v_mfma_f32_16x16x32_bf16 v[40:43], v[176:179], v[0:3], v[40:43]\n"
    "v_exp_f32 v80, v80\n"
    "v_mfma_f32_16x16x32_bf16 v[40:43], v[172:175], a[252:255], v[40:43]\n"
    "buffer_load_dwordx4 v200, s[36:39], s65 offen offset:2048 sc1 lds\n"
    "v_mfma_f32_16x16x32_bf16 v[36:39], v[172:175], a[228:231], v[36:39]\n"
    "ds_read_b128 v[168:171], v200 offset:23552\n"
    "v_cvt_pk_bf16_f32 v153, v74, v75\n"
    "v_mfma_f32_16x16x32_bf16 v[36:39], v[176:179], a[232:235], v[36:39]\n"
    "s_add_u32 m0, s71, 0xc000\n"
    "v_exp_f32 v81, v81\n"
    "v_mfma_f32_16x16x32_bf16 v[36:39], v[180:183], a[236:239], v[36:39]\n"
    "v_exp_f32 v82, v82\n"
    "v_mfma_f32_16x16x32_bf16 v[32:35], v[180:183], a[212:215], v[32:35]\n"
    "buffer_load_dwordx4 v200, s[40:43], s61 offen sc1 lds\n"
    "v_mfma_f32_16x16x32_bf16 v[32:35], v[176:179], a[208:211], v[32:35]\n"
    "v_exp_f32 v83, v83\n"
    "v_mfma_f32_16x16x32_bf16 v[32:35], v[172:175], a[204:207], v[32:35]\n"
    "s_waitcnt lgkmcnt(1)\n"
    "v_exp_f32 v84, v84\n"
    "v_mfma_f32_16x16x32_bf16 v[48:51], v[160:163], a[204:207], v[96:99]\n"
    "ds_read_b128 v[172:175], v200 offset:18432\n"
    "v_cvt_pk_bf16_f32 v156, v76, v77\n"
    "v_mfma_f32_16x16x32_bf16 v[48:51], v[164:167], a[208:211], v[48:51]\n"
    "s_waitcnt lgkmcnt(1)\n"
    "buffer_load_dwordx4 v200, s[40:43], s61 offen offset:1024 sc1 lds\n"
    "v_mfma_f32_16x16x32_bf16 v[48:51], v[168:171], a[212:215], v[48:51]\n"
    "v_exp_f32 v85, v85\n"
    "v_mfma_f32_16x16x32_bf16 v[52:55], v[168:171], a[236:239], v[100:103]\n"
    "ds_read_b128 v[176:179], v200 offset:19456\n"
    "v_cvt_pk_bf16_f32 v157, v78, v79\n"
    "v_mfma_f32_16x16x32_bf16 v[52:55], v[164:167], a[232:235], v[52:55]\n"
    "v_exp_f32 v86, v86\n"
    "v_mfma_f32_16x16x32_bf16 v[52:55], v[160:163], a[228:231], v[52:55]\n"
    "buffer_load_dwordx4 v200, s[40:43], s61 offen offset:2048 sc1 lds\n"
    "v_mfma_f32_16x16x32_bf16 v[56:59], v[160:163], a[252:255], v[104:107]\n"
    "ds_read_b128 v[180:183], v200 offset:20480\n"
    "v_cvt_pk_bf16_f32 v146, v80, v81\n"
    "v_mfma_f32_16x16x32_bf16 v[56:59], v[164:167], v[0:3], v[56:59]\n"
    "v_exp_f32 v87, v87\n"
    "v_mfma_f32_16x16x32_bf16 v[56:59], v[168:171], v[4:7], v[56:59]\n"
    "v_exp_f32 v88, v88\n"
    "v_mfma_f32_16x16x32_bf16 v[60:63], v[168:171], v[28:31], v[108:111]\n"
    "buffer_load_dwordx4 v200, s[40:43], s61 offen offset:3072 sc1 lds\n"
    "v_mfma_f32_16x16x32_bf16 v[60:63], v[164:167], v[24:27], v[60:63]\n"
    "s_add_u32 s65, s61, 0x1000\n"
    "v_exp_f32 v89, v89\n"
    "v_mfma_f32_16x16x32_bf16 v[60:63], v[160:163], v[20:23], v[60:63]\n"
    "s_waitcnt lgkmcnt(1)\n"
    "s_add_u32 m0, s71, 0xd000\n"
    "v_cvt_pk_bf16_f32 v147, v82, v83\n"
    "v_mfma_f32_16x16x32_bf16 v[60:63], v[172:175], v[8:11], v[60:63]\n"
    "ds_read_b128 v[160:163], v201 offset:24576\n"
    "v_cvt_pk_bf16_f32 v150, v84, v85\n"
    "v_mfma_f32_16x16x32_bf16 v[60:63], v[176:179], v[12:15], v[60:63]\n"
    "s_waitcnt lgkmcnt(1)\n"
    "buffer_load_dwordx4 v200, s[40:43], s65 offen sc1 lds\n"
    "v_mfma_f32_16x16x32_bf16 v[60:63], v[180:183], v[16:19], v[60:63]\n"
    "v_exp_f32 v90, v90\n"
    "v_mfma_f32_16x16x32_bf16 v[56:59], v[180:183], a[248:251], v[56:59]\n"
    "ds_read_b128 v[164:167], v201 offset:25600\n"
    "v_cvt_pk_bf16_f32 v151, v86, v87\n"
    "v_mfma_f32_16x16x32_bf16 v[56:59], v[176:179], a[244:247], v[56:59]\n"
    "v_exp_f32 v91, v91\n"
    "v_mfma_f32_16x16x32_bf16 v[56:59], v[172:175], a[240:243], v[56:59]\n"
    "buffer_load_dwordx4 v200, s[40:43], s65 offen offset:1024 sc1 lds\n"
    "v_mfma_f32_16x16x32_bf16 v[52:55], v[172:175], a[216:219], v[52:55]\n"
    "ds_read_b128 v[168:171], v201 offset:26624\n"
    "v_cvt_pk_bf16_f32 v154, v88, v89\n"
    "v_mfma_f32_16x16x32_bf16 v[52:55], v[176:179], a[220:223], v[52:55]\n"
    "v_exp_f32 v92, v92\n"
    "v_mfma_f32_16x16x32_bf16 v[52:55], v[180:183], a[224:227], v[52:55]\n"
    "v_exp_f32 v93, v93\n"
    "v_mfma_f32_16x16x32_bf16 v[48:51], v[180:183], a[200:203], v[48:51]\n"
    "v_exp_f32 v94, v94\n"
    "v_mfma_f32_16x16x32_bf16 v[48:51], v[176:179], a[196:199], v[48:51]\n"
    "v_cvt_pk_bf16_f32 v155, v90, v91\n"
    "v_cvt_pk_bf16_f32 v158, v92, v93\n"
    "v_mfma_f32_16x16x32_bf16 v[48:51], v[172:175], a[192:195], v[48:51]\n"
    "s_waitcnt lgkmcnt(1)\n"
    "v_exp_f32 v95, v95\n"
    "v_mfma_f32_16x16x32_bf16 a[0:3], v[160:163], v[128:131], a[0:3]\n"
    "ds_read_b128 v[172:175], v201 offset:27648\n"
    "ds_read_b128 v[176:179], v201 offset:28672\n"
    "v_mfma_f32_16x16x32_bf16 a[0:3], v[164:167], v[144:147], a[0:3]\n"
    "v_cvt_pk_bf16_f32 v159, v94, v95\n"
    "v_add_f32 v113, v113, v64\n"
    "v_mfma_f32_16x16x32_bf16 a[4:7], v[160:163], v[132:135], a[4:7]\n"
    "v_exp_f32 v32, v32\n"
    "v_mfma_f32_16x16x32_bf16 a[4:7], v[164:167], v[148:151], a[4:7]\n"
    "v_exp_f32 v33, v33\n"
    "v_mfma_f32_16x16x32_bf16 a[8:11], v[160:163], v[136:139], a[8:11]\n"
    "ds_read_b128 v[180:183], v201 offset:29696\n"
    "v_add_f32 v114, v114, v65\n"
    "v_mfma_f32_16x16x32_bf16 a[8:11], v[164:167], v[152:155], a[8:11]\n"
    "v_exp_f32 v34, v34\n"
    "v_mfma_f32_16x16x32_bf16 a[12:15], v[160:163], v[140:143], a[12:15]\n"
    "v_exp_f32 v35, v35\n"
    "v_mfma_f32_16x16x32_bf16 a[12:15], v[164:167], v[156:159], a[12:15]\n"
    "s_waitcnt lgkmcnt(2)\n"
    "v_exp_f32 v36, v36\n"
    "v_mfma_f32_16x16x32_bf16 a[28:31], v[168:171], v[140:143], a[28:31]\n"
    "ds_read_b128 v[160:163], v201 offset:30720\n"
    "v_add_f32 v113, v113, v66\n"
    "v_mfma_f32_16x16x32_bf16 a[28:31], v[172:175], v[156:159], a[28:31]\n"
    "v_exp_f32 v37, v37\n"
    "v_mfma_f32_16x16x32_bf16 a[24:27], v[172:175], v[152:155], a[24:27]\n"
    "v_exp_f32 v38, v38\n"
    "v_mfma_f32_16x16x32_bf16 a[24:27], v[168:171], v[136:139], a[24:27]\n"
    "v_exp_f32 v39, v39\n"
    "v_mfma_f32_16x16x32_bf16 a[20:23], v[168:171], v[132:135], a[20:23]\n"
    "ds_read_b128 v[164:167], v201 offset:31744\n"
    "v_add_f32 v114, v114, v67\n"
    "v_mfma_f32_16x16x32_bf16 a[20:23], v[172:175], v[148:151], a[20:23]\n"
    "v_exp_f32 v40, v40\n"
    "v_mfma_f32_16x16x32_bf16 a[16:19], v[172:175], v[144:147], a[16:19]\n"
    "v_exp_f32 v41, v41\n"
    "v_mfma_f32_16x16x32_bf16 a[16:19], v[168:171], v[128:131], a[16:19]\n"
    "s_waitcnt lgkmcnt(2)\n"
    "v_exp_f32 v42, v42\n"
    "v_mfma_f32_16x16x32_bf16 a[32:35], v[176:179], v[128:131], a[32:35]\n"
    "ds_read_b128 v[168:171], v201 offset:32768\n"
    "v_add_f32 v117, v117, v68\n"
    "v_mfma_f32_16x16x32_bf16 a[32:35], v[180:183], v[144:147], a[32:35]\n"
    "v_exp_f32 v43, v43\n"
    "v_mfma_f32_16x16x32_bf16 a[36:39], v[180:183], v[148:151], a[36:39]\n"
    "v_exp_f32 v44, v44\n"
    "v_mfma_f32_16x16x32_bf16 a[36:39], v[176:179], v[132:135], a[36:39]\n"
    "v_exp_f32 v45, v45\n"
    "v_mfma_f32_16x16x32_bf16 a[40:43], v[176:179], v[136:139], a[40:43]\n"
    "ds_read_b128 v[172:175], v201 offset:33792\n"
    "v_add_f32 v118, v118, v69\n"
    "v_mfma_f32_16x16x32_bf16 a[40:43], v[180:183], v[152:155], a[40:43]\n"
    "v_exp_f32 v46, v46\n"
    "v_mfma_f32_16x16x32_bf16 a[44:47], v[180:183], v[156:159], a[44:47]\n"
    "v_exp_f32 v47, v47\n"
    "v_mfma_f32_16x16x32_bf16 a[44:47], v[176:179], v[140:143], a[44:47]\n"
    "s_waitcnt lgkmcnt(2)\n"
    "v_exp_f32 v48, v48\n"
    "v_mfma_f32_16x16x32_bf16 a[60:63], v[160:163], v[140:143], a[60:63]\n"
    "ds_read_b128 v[176:179], v201 offset:34816\n"
    "v_add_f32 v117, v117, v70\n"
    "v_mfma_f32_16x16x32_bf16 a[60:63], v[164:167], v[156:159], a[60:63]\n"
    "v_exp_f32 v49, v49\n"
    "v_mfma_f32_16x16x32_bf16 a[56:59], v[164:167], v[152:155], a[56:59]\n"
    "v_exp_f32 v50, v50\n"
    "v_mfma_f32_16x16x32_bf16 a[56:59], v[160:163], v[136:139], a[56:59]\n"
    "v_exp_f32 v51, v51\n"
    "v_mfma_f32_16x16x32_bf16 a[52:55], v[160:163], v[132:135], a[52:55]\n"
    "ds_read_b128 v[180:183], v201 offset:35840\n"
    "v_add_f32 v113, v113, v32\n"
    "v_mfma_f32_16x16x32_bf16 a[52:55], v[164:167], v[148:151], a[52:55]\n"
    "v_exp_f32 v52, v52\n"
    "v_mfma_f32_16x16x32_bf16 a[48:51], v[164:167], v[144:147], a[48:51]\n"
    "v_add_f32 v118, v118, v71\n"
    "v_add_f32 v114, v114, v33\n"
    "v_mfma_f32_16x16x32_bf16 a[48:51], v[160:163], v[128:131], a[48:51]\n"
    "s_waitcnt lgkmcnt(2)\n"
    "v_add_f32 v121, v121, v72\n"
    "v_add_f32 v113, v113, v34\n"
    "v_mfma_f32_16x16x32_bf16 a[64:67], v[168:171], v[128:131], a[64:67]\n"
    "ds_read_b128 v[160:163], v201 offset:36864\n"
    "v_add_f32 v122, v122, v73\n"
    "v_mfma_f32_16x16x32_bf16 a[64:67], v[172:175], v[144:147], a[64:67]\n"
    "v_exp_f32 v53, v53\n"
    "v_mfma_f32_16x16x32_bf16 a[68:71], v[172:175], v[148:151], a[68:71]\n"
    "v_add_f32 v114, v114, v35\n"
    "v_add_f32 v121, v121, v74\n"
    "v_mfma_f32_16x16x32_bf16 a[68:71], v[168:171], v[132:135], a[68:71]\n"
    "s_add_u32 m0, s51, s95\n"
    "v_add_f32 v117, v117, v36\n"
    "v_add_f32 v122, v122, v75\n"
    "v_mfma_f32_16x16x32_bf16 a[72:75], v[168:171], v[136:139], a[72:75]\n"
    "ds_read_b128 v[164:167], v201 offset:37888\n"
    "v_add_f32 v118, v118, v37\n"
    "v_mfma_f32_16x16x32_bf16 a[72:75], v[172:175], v[152:155], a[72:75]\n"
    "buffer_load_dwordx4 v202, s[32:35], s64 offen lds\n"
    "v_mfma_f32_16x16x32_bf16 a[76:79], v[172:175], v[156:159], a[76:79]\n"
    "v_exp_f32 v54, v54\n"
    "v_mfma_f32_16x16x32_bf16 a[76:79], v[168:171], v[140:143], a[76:79]\n"
    "s_waitcnt lgkmcnt(2)\n"
    "v_add_f32 v125, v125, v76\n"
    "v_add_f32 v117, v117, v38\n"
    "v_mfma_f32_16x16x32_bf16 a[92:95], v[176:179], v[140:143], a[92:95]\n"
    "ds_read_b128 v[168:171], v201 offset:38912\n"
    "v_add_f32 v126, v126, v77\n"
    "v_mfma_f32_16x16x32_bf16 a[92:95], v[180:183], v[156:159], a[92:95]\n"
    "buffer_load_dwordx4 v203, s[32:35], s64 offen offset:1024 lds\n"
    "v_mfma_f32_16x16x32_bf16 a[88:91], v[180:183], v[152:155], a[88:91]\n"
    "v_exp_f32 v55, v55\n"
    "v_mfma_f32_16x16x32_bf16 a[88:91], v[176:179], v[136:139], a[88:91]\n"
    "v_add_f32 v118, v118, v39\n"
    "v_add_f32 v125, v125, v78\n"
    "v_mfma_f32_16x16x32_bf16 a[84:87], v[176:179], v[132:135], a[84:87]\n"
    "ds_read_b128 v[172:175], v201 offset:39936\n"
    "v_add_f32 v121, v121, v40\n"
    "v_mfma_f32_16x16x32_bf16 a[84:87], v[180:183], v[148:151], a[84:87]\n"
    "buffer_load_dwordx4 v204, s[32:35], s64 offen offset:2048 lds\n"
    "v_mfma_f32_16x16x32_bf16 a[80:83], v[180:183], v[144:147], a[80:83]\n"
    "v_exp_f32 v56, v56\n"
    "v_mfma_f32_16x16x32_bf16 a[80:83], v[176:179], v[128:131], a[80:83]\n"
    "s_waitcnt lgkmcnt(2)\n"
    "v_add_f32 v126, v126, v79\n"
    "v_add_f32 v122, v122, v41\n"
    "v_mfma_f32_16x16x32_bf16 a[96:99], v[160:163], v[128:131], a[96:99]\n"
    "s_mov_b32 s95, s94\n"
    "ds_read_b128 v[176:179], v201 offset:40960\n"
    "v_add_f32 v113, v113, v80\n"
    "v_mfma_f32_16x16x32_bf16 a[96:99], v[164:167], v[144:147], a[96:99]\n"
    "s_sub_u32 s94, s94, 0x3000\n"
    "v_add_f32 v121, v121, v42\n"
    "v_add_f32 v114, v114, v81\n"
    "v_mfma_f32_16x16x32_bf16 a[100:103], v[164:167], v[148:151], a[100:103]\n"
    "s_min_u32 s94, s94, 0x6000\n"
    "v_exp_f32 v57, v57\n"
    "v_mfma_f32_16x16x32_bf16 a[100:103], v[160:163], v[132:135], a[100:103]\n"
    "s_sub_u32 s93, s94, s95\n"
    "v_add_f32 v122, v122, v43\n"
    "v_add_f32 v113, v113, v82\n"
    "v_mfma_f32_16x16x32_bf16 a[104:107], v[160:163], v[136:139], a[104:107]\n"
    "ds_read_b128 v[180:183], v201 offset:41984\n"
    "v_add_f32 v125, v125, v44\n"
    "v_mfma_f32_16x16x32_bf16 a[104:107], v[164:167], v[152:155], a[104:107]\n"
    "v_exp_f32 v58, v58\n"
    "v_mfma_f32_16x16x32_bf16 a[108:111], v[164:167], v[156:159], a[108:111]\n"
    "v_add_f32 v114, v114, v83\n"
    "v_add_f32 v126, v126, v45\n"
    "v_mfma_f32_16x16x32_bf16 a[108:111], v[160:163], v[140:143], a[108:111]\n"
    "s_waitcnt lgkmcnt(2)\n"
    "v_add_f32 v117, v117, v84\n"
    "v_add_f32 v125, v125, v46\n"
    "v_mfma_f32_16x16x32_bf16 a[124:127], v[168:171], v[140:143], a[124:127]\n"
    "ds_read_b128 v[160:163], v201 offset:43008\n"
    "v_add_f32 v118, v118, v85\n"
    "v_mfma_f32_16x16x32_bf16 a[124:127], v[172:175], v[156:159], a[124:127]\n"
    "v_exp_f32 v59, v59\n"
    "v_mfma_f32_16x16x32_bf16 a[120:123], v[172:175], v[152:155], a[120:123]\n"
    "v_add_f32 v126, v126, v47\n"
    "v_add_f32 v117, v117, v86\n"
    "v_mfma_f32_16x16x32_bf16 a[120:123], v[168:171], v[136:139], a[120:123]\n"
    "v_add_f32 v113, v113, v48\n"
    "v_add_u32 v205, s93, v205\n"
    "v_mfma_f32_16x16x32_bf16 a[116:119], v[168:171], v[132:135], a[116:119]\n"
    "ds_read_b128 v[164:167], v201 offset:44032\n"
    "v_add_f32 v118, v118, v87\n"
    "v_mfma_f32_16x16x32_bf16 a[116:119], v[172:175], v[148:151], a[116:119]\n"
    "v_exp_f32 v60, v60\n"
    "v_mfma_f32_16x16x32_bf16 a[112:115], v[172:175], v[144:147], a[112:115]\n"
    "v_add_f32 v114, v114, v49\n"
    "v_add_u32 v206, s93, v206\n"
    "v_mfma_f32_16x16x32_bf16 a[112:115], v[168:171], v[128:131], a[112:115]\n"
    "s_waitcnt lgkmcnt(2)\n"
    "v_add_f32 v121, v121, v88\n"
    "v_add_f32 v113, v113, v50\n"
    "v_mfma_f32_16x16x32_bf16 a[128:131], v[176:179], v[128:131], a[128:131]\n"
    "ds_read_b128 v[168:171], v201 offset:45056\n"
    "v_add_u32 v207, s93, v207\n"
    "v_mfma_f32_16x16x32_bf16 a[128:131], v[180:183], v[144:147], a[128:131]\n"
    "v_exp_f32 v61, v61\n"
    "v_mfma_f32_16x16x32_bf16 a[132:135], v[180:183], v[148:151], a[132:135]\n"
    "s_add_u32 s64, s64, 0xc000\n"
    "v_add_f32 v122, v122, v89\n"
    "v_add_f32 v114, v114, v51\n"
    "v_mfma_f32_16x16x32_bf16 a[132:135], v[176:179], v[132:135], a[132:135]\n"
    "s_add_u32 s62, s62, 0x6000\n"
    "v_add_f32 v121, v121, v90\n"
    "v_add_f32 v117, v117, v52\n"
    "v_mfma_f32_16x16x32_bf16 a[136:139], v[176:179], v[136:139], a[136:139]\n"
    "s_add_u32 s63, s63, 0x6000\n"
    "ds_read_b128 v[172:175], v201 offset:46080\n"
    "v_add_f32 v122, v122, v91\n"
    "v_mfma_f32_16x16x32_bf16 a[136:139], v[180:183], v[152:155], a[136:139]\n"
    "s_add_u32 s60, s60, 0x6000\n"
    "v_exp_f32 v62, v62\n"
    "v_mfma_f32_16x16x32_bf16 a[140:143], v[180:183], v[156:159], a[140:143]\n"
    "s_add_u32 s61, s61, 0x6000\n"
    "v_add_f32 v118, v118, v53\n"
    "v_add_f32 v125, v125, v92\n"
    "v_mfma_f32_16x16x32_bf16 a[140:143], v[176:179], v[140:143], a[140:143]\n"
    "s_waitcnt lgkmcnt(2)\n"
    "v_add_f32 v117, v117, v54\n"
    "v_add_f32 v126, v126, v93\n"
    "v_mfma_f32_16x16x32_bf16 a[156:159], v[160:163], v[140:143], a[156:159]\n"
    "ds_read_b128 v[176:179], v201 offset:47104\n"
    "v_add_f32 v118, v118, v55\n"
    "v_mfma_f32_16x16x32_bf16 a[156:159], v[164:167], v[156:159], a[156:159]\n"
    "s_waitcnt vmcnt(3)\n"
    "ds_read_b128 v[180:183], v201 offset:48128\n"
    "v_add_f32 v125, v125, v94\n"
    "v_mfma_f32_16x16x32_bf16 a[152:155], v[164:167], v[152:155], a[152:155]\n"
    "v_exp_f32 v63, v63\n"
    "v_mfma_f32_16x16x32_bf16 a[152:155], v[160:163], v[136:139], a[152:155]\n"
    "v_add_f32 v121, v121, v56\n"
    "v_add_f32 v126, v126, v95\n"
    "v_mfma_f32_16x16x32_bf16 a[148:151], v[160:163], v[132:135], a[148:151]\n"
    "v_add_f32 v122, v122, v57\n"
    "v_mfma_f32_16x16x32_bf16 a[148:151], v[164:167], v[148:151], a[148:151]\n"
    "v_mfma_f32_16x16x32_bf16 a[144:147], v[164:167], v[144:147], a[144:147]\n"
    "v_mfma_f32_16x16x32_bf16 a[144:147], v[160:163], v[128:131], a[144:147]\n"
    "s_waitcnt lgkmcnt(0)\n"
    "v_mfma_f32_16x16x32_bf16 a[160:163], v[168:171], v[128:131], a[160:163]\n"
    "s_barrier\n"
    "LMID1:\n"
    "v_mfma_f32_16x16x32_bf16 a[160:163], v[172:175], v[144:147], a[160:163]\n"
    "ds_read_u16_d16_hi v223, v209 offset:6336\n"
    "ds_read_u16_d16_hi v222, v209 offset:192\n"
    "v_mfma_f32_16x16x32_bf16 a[164:167], v[172:175], v[148:151], a[164:167]\n"
    "ds_read_b128 v[160:163], v200 offset:24576\n"
    "v_add_f32 v121, v121, v58\n"
    "v_mfma_f32_16x16x32_bf16 a[164:167], v[168:171], v[132:135], a[164:167]\n"
    "ds_read_b128 v[232:235], v205 offset:24576\n"
    "v_add_f32 v122, v122, v59\n"
    "v_mfma_f32_16x16x32_bf16 a[168:171], v[168:171], v[136:139], a[168:171]\n"
    "ds_read_b128 v[164:167], v200 offset:25600\n"
    "ds_read_b128 v[236:239], v206 offset:24576\n"
    "v_mfma_f32_16x16x32_bf16 a[168:171], v[172:175], v[152:155], a[168:171]\n"
    "v_add_f32 v125, v125, v60\n"
    "v_add_f32 v126, v126, v61\n"
    "v_mfma_f32_16x16x32_bf16 a[172:175], v[172:175], v[156:159], a[172:175]\n"
    "v_add_f32 v125, v125, v62\n"
    "v_add_f32 v126, v126, v63\n"
    "v_mfma_f32_16x16x32_bf16 a[172:175], v[168:171], v[140:143], a[172:175]\n"
    "ds_read_b64 v[228:229], v210 offset:64\n"
    "ds_read2st64_b32 v[240:241], v207 offset0:96 offset1:99\n"
    "v_mfma_f32_16x16x32_bf16 a[188:191], v[176:179], v[140:143], a[188:191]\n"
    "ds_read_b128 v[168:171], v200 offset:26624\n"
    "ds_read2st64_b32 v[242:243], v207 offset0:102 offset1:105\n"
    "v_mfma_f32_16x16x32_bf16 a[188:191], v[180:183], v[156:159], a[188:191]\n"
    "ds_read2st64_b32 v[244:245], v207 offset0:108 offset1:111\n"
    "ds_read2st64_b32 v[246:247], v207 offset0:114 offset1:117\n"
    "v_mfma_f32_16x16x32_bf16 a[184:187], v[176:179], v[136:139], a[184:187]\n"
    "v_cvt_pk_bf16_f32 v140, v44, v45\n"
    "v_cvt_pk_bf16_f32 v141, v46, v47\n"
    "v_mfma_f32_16x16x32_bf16 a[184:187], v[180:183], v[152:155], a[184:187]\n"
    "s_waitcnt lgkmcnt(6)\n"
    "v_mul_f32 v226, s75, v223\n"
    "v_mfma_f32_16x16x32_bf16 a[180:183], v[176:179], v[132:135], a[180:183]\n"
    "s_add_u32 s59, s59, 1\n"
    "ds_read_b128 v[172:175], v200 offset:27648\n"
    "v_fma_f32 v227, -v222, v226, v217\n"
    "v_mfma_f32_16x16x32_bf16 a[180:183], v[180:183], v[148:151], a[180:183]\n"
    "v_cvt_f32_i32 v232, v232\n"
    "v_cvt_f32_i32 v233, v233\n"
    "v_mfma_f32_16x16x32_bf16 a[176:179], v[176:179], v[128:131], a[176:179]\n"
    "s_cmp_lt_u32 s59, 15\n"
    "v_cvt_f32_i32 v234, v234\n"
    "v_cvt_f32_i32 v235, v235\n"
    "v_mfma_f32_16x16x32_bf16 a[176:179], v[180:183], v[144:147], a[176:179]\n"
    "s_cbranch_scc1 LLOOP1\n"
    "s_branch LTAIL\n"
    "LENT0:\n"
    "s_mov_b32 s59, 1\n"
    "s_add_u32 m0, s72, 0x21000\n"
    "s_nop 0\n"
    "buffer_load_dwordx4 v250, s[48:51], 0 offen offset:2048 lds\n"
    "s_add_u32 m0, s72, 0x18000\n"
    "s_nop 0\n"
    "buffer_load_dwordx4 v202, s[32:35], s64 offen lds\n"
    "buffer_load_dwordx4 v203, s[32:35], s64 offen offset:1024 lds\n"
    "buffer_load_dwordx4 v204, s[32:35], s64 offen offset:2048 lds\n"
    "s_add_u32 s64, s64, 0xc000\n"
    "s_mov_b32 s94, 0x3000\n"
    "s_mov_b32 s95, 0x6000\n"
    "s_add_u32 s51, s72, 0x18000\n"
    "v_add_u32 v205, 0x3000, v205\n"
    "v_add_u32 v206, 0x3000, v206\n"
    "v_add_u32 v207, 0x3000, v207\n"
    "buffer_store_dwordx2 v[220:221], v214, s[44:47], 0 offen sc1\n"
    "ds_read_b128 v[160:163], v200 offset:24576\n"
    "ds_read_u16_d16_hi v223, v209 offset:6336\n"
    "ds_read_u16_d16_hi v222, v209 offset:192\n"
    "v_add_f32 v121, v121, v58\n"
    "ds_read_b128 v[164:167], v200 offset:25600\n"
    "ds_read_b128 v[232:235], v205 offset:24576\n"
    "v_add_f32 v122, v122, v59\n"
    "ds_read_b128 v[236:239], v206 offset:24576\n"
    "v_add_f32 v125, v125, v60\n"
    "v_add_f32 v126, v126, v61\n"
    "v_add_f32 v125, v125, v62\n"
    "v_add_f32 v126, v126, v63\n"
    "ds_read_b128 v[168:171], v200 offset:26624\n"
    "ds_read_b64 v[228:229], v210 offset:64\n"
    "ds_read2st64_b32 v[240:241], v207 offset0:96 offset1:99\n"
    "ds_read2st64_b32 v[242:243], v207 offset0:102 offset1:105\n"
    "ds_read2st64_b32 v[244:245], v207 offset0:108 offset1:111\n"
    "ds_read2st64_b32 v[246:247], v207 offset0:114 offset1:117\n"
    "v_cvt_pk_bf16_f32 v140, v44, v45\n"
    "v_cvt_pk_bf16_f32 v141, v46, v47\n"
    "ds_read_b128 v[172:175], v200 offset:27648\n"
    "s_waitcnt lgkmcnt(7)\n"
    "v_mul_f32 v226, s75, v223\n"
    "v_fma_f32 v227, -v222, v226, v217\n"
    "v_cvt_f32_i32 v232, v232\n"
    "v_cvt_f32_i32 v233, v233\n"
    "s_nop 7\n"
    ".p2align 6\n"
    "LLOOP:\n"
    "v_mfma_f32_16x16x32_bf16 v[64:67], v[160:163], a[192:195], v[96:99]\n"
    "ds_read_b128 v[176:179], v200 offset:28672\n"
    "v_cvt_f32_i32 v234, v234\n"
    "v_mfma_f32_16x16x32_bf16 v[64:67], v[164:167], a[196:199], v[64:67]\n"
    "s_waitcnt lgkmcnt(1)\n"
    "v_cvt_f32_i32 v235, v235\n"
    "v_cvt_f32_i32 v236, v236\n"
    "v_mfma_f32_16x16x32_bf16 v[64:67], v[168:171], a[200:203], v[64:67]\n"
    "v_cvt_f32_i32 v237, v237\n"
    "v_cvt_f32_i32 v238, v238\n"
    "v_mfma_f32_16x16x32_bf16 v[68:71], v[168:171], a[224:227], v[100:103]\n"
    "v_cvt_f32_i32 v239, v239\n"
    "v_mul_f32_dpp v232, v226, v232 row_newbcast:0 row_mask:0xf bank_mask:0xf\n"
    "v_mfma_f32_16x16x32_bf16 v[68:71], v[164:167], a[220:223], v[68:71]\n"
    "ds_read_b128 v[180:183], v200 offset:29696\n"
    "v_mul_f32_dpp v233, v226, v233 row_newbcast:1 row_mask:0xf bank_mask:0xf\n"
    "v_mfma_f32_16x16x32_bf16 v[68:71], v[160:163], a[216:219], v[68:71]\n"
    "v_mul_f32_dpp v234, v226, v234 row_newbcast:2 row_mask:0xf bank_mask:0xf\n"
    "v_mul_f32_dpp v235, v226, v235 row_newbcast:3 row_mask:0xf bank_mask:0xf\n"
    "v_mfma_f32_16x16x32_bf16 v[72:75], v[160:163], a[240:243], v[104:107]\n"
    "v_mul_f32_dpp v236, v226, v236 row_newbcast:4 row_mask:0xf bank_mask:0xf\n"
    "v_mul_f32_dpp v237, v226, v237 row_newbcast:5 row_mask:0xf bank_mask:0xf\n"
    "v_mfma_f32_16x16x32_bf16 v[72:75], v[164:167], a[244:247], v[72:75]\n"
    "v_mul_f32_dpp v238, v226, v238 row_newbcast:6 row_mask:0xf bank_mask:0xf\n"
    "v_mul_f32_dpp v239, v226, v239 row_newbcast:7 row_mask:0xf bank_mask:0xf\n"
    "v_mfma_f32_16x16x32_bf16 v[72:75], v[168:171], a[248:251], v[72:75]\n"
    "v_add_f32_dpp v232, v227, v232 row_newbcast:0 row_mask:0xf bank_mask:0xf\n"
    "v_add_f32_dpp v233, v227, v233 row_newbcast:1 row_mask:0xf bank_mask:0xf\n"
    "v_mfma_f32_16x16x32_bf16 v[76:79], v[168:171], v[16:19], v[108:111]\n"
    "v_add_f32_dpp v234, v227, v234 row_newbcast:2 row_mask:0xf bank_mask:0xf\n"
    "v_add_f32_dpp v235, v227, v235 row_newbcast:3 row_mask:0xf bank_mask:0xf\n"
    "v_mfma_f32_16x16x32_bf16 v[76:79], v[164:167], v[12:15], v[76:79]\n"
    "v_add_f32_dpp v236, v227, v236 row_newbcast:4 row_mask:0xf bank_mask:0xf\n"
    "v_add_f32_dpp v237, v227, v237 row_newbcast:5 row_mask:0xf bank_mask:0xf\n"
    "v_mfma_f32_16x16x32_bf16 v[76:79], v[160:163], v[8:11], v[76:79]\n"
    "s_waitcnt lgkmcnt(1)\n"
    "v_add_f32_dpp v238, v227, v238 row_newbcast:6 row_mask:0xf bank_mask:0xf\n"
    "v_add_f32_dpp v239, v227, v239 row_newbcast:7 row_mask:0xf bank_mask:0xf\n"
    "v_mfma_f32_16x16x32_bf16 v[76:79], v[172:175], v[20:23], v[76:79]\n"
    "ds_read_b128 v[160:163], v200 offset:33792\n"
    "v_cvt_pk_bf16_f32 v232, v232, v233\n"
    "v_mfma_f32_16x16x32_bf16 v[76:79], v[176:179], v[24:27], v[76:79]\n"
    "s_waitcnt lgkmcnt(1)\n"
    "v_cvt_f32_i32 v240, v240\n"
    "v_cvt_pk_bf16_f32 v233, v234, v235\n"
    "v_mfma_f32_16x16x32_bf16 v[76:79], v[180:183], v[28:31], v[76:79]\n"
    "v_cvt_f32_i32 v241, v241\n"
    "v_cvt_pk_bf16_f32 v234, v236, v237\n"
    "v_mfma_f32_16x16x32_bf16 v[72:75], v[180:183], v[4:7], v[72:75]\n"
    "ds_read_b128 v[164:167], v200 offset:34816\n"
    "v_cvt_f32_i32 v242, v242\n"
    "v_mfma_f32_16x16x32_bf16 v[72:75], v[176:179], v[0:3], v[72:75]\n"
    "v_cvt_pk_bf16_f32 v235, v238, v239\n"
    "v_cvt_f32_i32 v243, v243\n"
    "v_mfma_f32_16x16x32_bf16 v[72:75], v[172:175], a[252:255], v[72:75]\n"
    "v_cvt_f32_i32 v244, v244\n"
    "v_cvt_f32_i32 v245, v245\n"
    "v_mfma_f32_16x16x32_bf16 v[68:71], v[172:175], a[228:231], v[68:71]\n"
    "ds_read_b128 v[168:171], v200 offset:35840\n"
    "v_cvt_f32_i32 v246, v246\n"
    "v_mfma_f32_16x16x32_bf16 v[68:71], v[176:179], a[232:235], v[68:71]\n"
    "v_cvt_f32_i32 v247, v247\n"
    "v_mul_f32_dpp v240, v228, v240 row_newbcast:0 row_mask:0xf bank_mask:0xf\n"
    "v_mfma_f32_16x16x32_bf16 v[68:71], v[180:183], a[236:239], v[68:71]\n"
    "v_mul_f32_dpp v241, v228, v241 row_newbcast:1 row_mask:0xf bank_mask:0xf\n"
    "v_mul_f32_dpp v242, v228, v242 row_newbcast:2 row_mask:0xf bank_mask:0xf\n"
    "v_mfma_f32_16x16x32_bf16 v[64:67], v[180:183], a[212:215], v[64:67]\n"
    "v_mul_f32_dpp v243, v228, v243 row_newbcast:3 row_mask:0xf bank_mask:0xf\n"
    "v_mul_f32_dpp v244, v228, v244 row_newbcast:4 row_mask:0xf bank_mask:0xf\n"
    "v_mfma_f32_16x16x32_bf16 v[64:67], v[176:179], a[208:211], v[64:67]\n"
    "buffer_store_dwordx4 v[232:235], v211, s[36:39], s62 offen sc1\n"
    "v_mul_f32_dpp v245, v228, v245 row_newbcast:5 row_mask:0xf bank_mask:0xf\n"
    "v_mul_f32_dpp v246, v228, v246 row_newbcast:6 row_mask:0xf bank_mask:0xf\n"
    "v_mul_f32_dpp v247, v228, v247 row_newbcast:7 row_mask:0xf bank_mask:0xf\n"
    "v_mfma_f32_16x16x32_bf16 v[64:67], v[172:175], a[204:207], v[64:67]\n"
    "s_waitcnt lgkmcnt(1)\n"
    "v_add_f32_dpp v240, v229, v240 row_newbcast:0 row_mask:0xf bank_mask:0xf\n"
    "v_add_f32_dpp v241, v229, v241 row_newbcast:1 row_mask:0xf bank_mask:0xf\n"
    "v_mfma_f32_16x16x32_bf16 v[80:83], v[160:163], a[204:207], v[96:99]\n"
    "ds_read_b128 v[172:175], v200 offset:30720\n"
    "v_add_f32_dpp v242, v229, v242 row_newbcast:2 row_mask:0xf bank_mask:0xf\n"
    "v_mfma_f32_16x16x32_bf16 v[80:83], v[164:167], a[208:211], v[80:83]\n"
    "s_waitcnt lgkmcnt(1)\n"
    "v_add_f32_dpp v243, v229, v243 row_newbcast:3 row_mask:0xf bank_mask:0xf\n"
    "v_add_f32_dpp v244, v229, v244 row_newbcast:4 row_mask:0xf bank_mask:0xf\n"
    "v_mfma_f32_16x16x32_bf16 v[80:83], v[168:171], a[212:215], v[80:83]\n"
    "v_add_f32_dpp v245, v229, v245 row_newbcast:5 row_mask:0xf bank_mask:0xf\n"
    "v_add_f32_dpp v246, v229, v246 row_newbcast:6 row_mask:0xf bank_mask:0xf\n"
    "v_mfma_f32_16x16x32_bf16 v[84:87], v[168:171], a[236:239], v[100:103]\n"
    "ds_read_b128 v[176:179], v200 offset:31744\n"
    "v_add_f32_dpp v247, v229, v247 row_newbcast:7 row_mask:0xf bank_mask:0xf\n"
    "v_mfma_f32_16x16x32_bf16 v[84:87], v[164:167], a[232:235], v[84:87]\n"
    "v_cvt_pk_bf16_f32 v240, v240, v241\n"
    "v_cvt_pk_bf16_f32 v241, v242, v243\n"
    "v_mfma_f32_16x16x32_bf16 v[84:87], v[160:163], a[228:231], v[84:87]\n"
    "v_cvt_pk_bf16_f32 v242, v244, v245\n"
    "v_cvt_pk_bf16_f32 v243, v246, v247\n"
    "v_mfma_f32_16x16x32_bf16 v[88:91], v[160:163], a[252:255], v[104:107]\n"
    "ds_read_b128 v[180:183], v200 offset:32768\n"
    "v_cvt_pk_bf16_f32 v136, v40, v41\n"
    "v_mfma_f32_16x16x32_bf16 v[88:91], v[164:167], v[0:3], v[88:91]\n"
    "v_cvt_pk_bf16_f32 v137, v42, v43\n"
    "v_cvt_pk_bf16_f32 v132, v36, v37\n"
    "v_mfma_f32_16x16x32_bf16 v[88:91], v[168:171], v[4:7], v[88:91]\n"
    "v_cvt_pk_bf16_f32 v133, v38, v39\n"
    "v_cvt_pk_bf16_f32 v128, v32, v33\n"
    "v_mfma_f32_16x16x32_bf16 v[92:95], v[168:171], v[28:31], v[108:111]\n"
    "v_cvt_pk_bf16_f32 v129, v34, v35\n"
    "v_cvt_pk_bf16_f32 v142, v60, v61\n"
    "v_mfma_f32_16x16x32_bf16 v[92:95], v[164:167], v[24:27], v[92:95]\n"
    "v_exp_f32 v64, v64\n"
    "v_mfma_f32_16x16x32_bf16 v[92:95], v[160:163], v[20:23], v[92:95]\n"
    "s_waitcnt lgkmcnt(1)\n"
    "v_exp_f32 v65, v65\n"
    "v_mfma_f32_16x16x32_bf16 v[92:95], v[172:175], v[8:11], v[92:95]\n"
    "ds_read_b128 v[160:163], v200 offset:36864\n"
    "v_cvt_pk_bf16_f32 v143, v62, v63\n"
    "v_mfma_f32_16x16x32_bf16 v[92:95], v[176:179], v[12:15], v[92:95]\n"
    "s_waitcnt lgkmcnt(1)\n"
    "v_exp_f32 v66, v66\n"
    "v_mfma_f32_16x16x32_bf16 v[92:95], v[180:183], v[16:19], v[92:95]\n"
    "buffer_store_dwordx4 v[240:243], v212, s[40:43], s63 offen sc1\n"
    "v_exp_f32 v67, v67\n"
    "v_cvt_pk_bf16_f32 v138, v56, v57\n"
    "v_mfma_f32_16x16x32_bf16 v[88:91], v[180:183], a[248:251], v[88:91]\n"
    "ds_read_b128 v[164:167], v200 offset:37888\n"
    "v_cvt_pk_bf16_f32 v139, v58, v59\n"
    "v_mfma_f32_16x16x32_bf16 v[88:91], v[176:179], a[244:247], v[88:91]\n"
    "v_exp_f32 v68, v68\n"
    "v_mfma_f32_16x16x32_bf16 v[88:91], v[172:175], a[240:243], v[88:91]\n"
    "v_exp_f32 v69, v69\n"
    "v_mfma_f32_16x16x32_bf16 v[84:87], v[172:175], a[216:219], v[84:87]\n"
    "ds_read_b128 v[168:171], v200 offset:38912\n"
    "v_cvt_pk_bf16_f32 v134, v52, v53\n"
    "v_mfma_f32_16x16x32_bf16 v[84:87], v[176:179], a[220:223], v[84:87]\n"
    "v_exp_f32 v70, v70\n"
    "v_mfma_f32_16x16x32_bf16 v[84:87], v[180:183], a[224:227], v[84:87]\n"
    "v_exp_f32 v71, v71\n"
    "v_mfma_f32_16x16x32_bf16 v[80:83], v[180:183], a[200:203], v[80:83]\n"
    "v_exp_f32 v72, v72\n"
    "v_mfma_f32_16x16x32_bf16 v[80:83], v[176:179], a[196:199], v[80:83]\n"
    "v_cvt_pk_bf16_f32 v135, v54, v55\n"
    "v_cvt_pk_bf16_f32 v130, v48, v49\n"
    "v_mfma_f32_16x16x32_bf16 v[80:83], v[172:175], a[192:195], v[80:83]\n"
    "s_waitcnt lgkmcnt(1)\n"
    "v_exp_f32 v73, v73\n"
    "v_mfma_f32_16x16x32_bf16 v[32:35], v[160:163], a[192:195], v[96:99]\n"
    "ds_read_b128 v[172:175], v200 offset:39936\n"
    "v_cvt_pk_bf16_f32 v131, v50, v51\n"
    "v_mfma_f32_16x16x32_bf16 v[32:35], v[164:167], a[196:199], v[32:35]\n"
    "s_waitcnt lgkmcnt(1)\n"
    "v_exp_f32 v74, v74\n"
    "v_mfma_f32_16x16x32_bf16 v[32:35], v[168:171], a[200:203], v[32:35]\n"
    "v_exp_f32 v75, v75\n"
    "v_mfma_f32_16x16x32_bf16 v[36:39], v[168:171], a[224:227], v[100:103]\n"
    "ds_read_b128 v[176:179], v200 offset:40960\n"
    "v_cvt_pk_bf16_f32 v144, v64, v65\n"
    "v_mfma_f32_16x16x32_bf16 v[36:39], v[164:167], a[220:223], v[36:39]\n"
    "v_exp_f32 v76, v76\n"
    "v_mfma_f32_16x16x32_bf16 v[36:39], v[160:163], a[216:219], v[36:39]\n"
    "v_exp_f32 v77, v77\n"
    "v_mfma_f32_16x16x32_bf16 v[40:43], v[160:163], a[240:243], v[104:107]\n"
    "ds_read_b128 v[180:183], v200 offset:41984\n"
    "v_cvt_pk_bf16_f32 v145, v66, v67\n"
    "v_mfma_f32_16x16x32_bf16 v[40:43], v[164:167], a[244:247], v[40:43]\n"
    "v_exp_f32 v78, v78\n"
    "v_mfma_f32_16x16x32_bf16 v[40:43], v[168:171], a[248:251], v[40:43]\n"
    "v_exp_f32 v79, v79\n"
    "v_mfma_f32_16x16x32_bf16 v[44:47], v[168:171], v[16:19], v[108:111]\n"
    "v_exp_f32 v80, v80\n"
    "v_mfma_f32_16x16x32_bf16 v[44:47], v[164:167], v[12:15], v[44:47]\n"
    "v_exp_f32 v81, v81\n"
    "v_mfma_f32_16x16x32_bf16 v[44:47], v[160:163], v[8:11], v[44:47]\n"
    "s_waitcnt lgkmcnt(1)\n"
    "v_exp_f32 v82, v82\n"
    "v_mfma_f32_16x16x32_bf16 v[44:47], v[172:175], v[20:23], v[44:47]\n"
    "ds_read_b128 v[160:163], v200 offset:46080\n"
    "v_cvt_pk_bf16_f32 v148, v68, v69\n"
    "v_mfma_f32_16x16x32_bf16 v[44:47], v[176:179], v[24:27], v[44:47]\n"
    "s_waitcnt lgkmcnt(1)\n"
    "v_exp_f32 v83, v83\n"
    "v_mfma_f32_16x16x32_bf16 v[44:47], v[180:183], v[28:31], v[44:47]\n"
    "v_exp_f32 v84, v84\n"
    "v_mfma_f32_16x16x32_bf16 v[40:43], v[180:183], v[4:7], v[40:43]\n"
    "ds_read_b128 v[164:167], v200 offset:47104\n"
    "v_cvt_pk_bf16_f32 v149, v70, v71\n"
    "v_mfma_f32_16x16x32_bf16 v[40:43], v[176:179], v[0:3], v[40:43]\n"
    "v_exp_f32 v85, v85\n"
    "v_mfma_f32_16x16x32_bf16 v[40:43], v[172:175], a[252:255], v[40:43]\n"
    "v_exp_f32 v86, v86\n"
    "v_mfma_f32_16x16x32_bf16 v[36:39], v[172:175], a[228:231], v[36:39]\n"
    "ds_read_b128 v[168:171], v200 offset:48128\n"
    "v_cvt_pk_bf16_f32 v152, v72, v73\n"
    "v_mfma_f32_16x16x32_bf16 v[36:39], v[176:179], a[232:235], v[36:39]\n"
    "v_exp_f32 v87, v87\n"
    "v_mfma_f32_16x16x32_bf16 v[36:39], v[180:183], a[236:239], v[36:39]\n"
    "v_exp_f32 v88, v88\n"
    "v_mfma_f32_16x16x32_bf16 v[32:35], v[180:183], a[212:215], v[32:35]\n"
    "v_exp_f32 v89, v89\n"
    "v_mfma_f32_16x16x32_bf16 v[32:35], v[176:179], a[208:211], v[32:35]\n"
    "v_cvt_pk_bf16_f32 v153, v74, v75\n"
    "v_cvt_pk_bf16_f32 v156, v76, v77\n"
    "v_mfma_f32_16x16x32_bf16 v[32:35], v[172:175], a[204:207], v[32:35]\n"
    "s_waitcnt lgkmcnt(1)\n"
    "s_mov_b32 m0, 0\n"
    "v_cvt_pk_bf16_f32 v157, v78, v79\n"
    "v_mfma_f32_16x16x32_bf16 v[48:51], v[160:163], a[204:207], v[96:99]\n"
    "ds_read_b128 v[172:175], v200 offset:43008\n"
    "v_cvt_pk_bf16_f32 v146, v80, v81\n"
    "v_mfma_f32_16x16x32_bf16 v[48:51], v[164:167], a[208:211], v[48:51]\n"
    "s_waitcnt lgkmcnt(1)\n"
    "buffer_load_dwordx4 v200, s[36:39], s60 offen sc1 lds\n"
    "v_mfma_f32_16x16x32_bf16 v[48:51], v[168:171], a[212:215], v[48:51]\n"
    "v_exp_f32 v90, v90\n"
    "v_mfma_f32_16x16x32_bf16 v[52:55], v[168:171], a[236:239], v[100:103]\n"
    "ds_read_b128 v[176:179], v200 offset:44032\n"
    "v_cvt_pk_bf16_f32 v147, v82, v83\n"
    "v_mfma_f32_16x16x32_bf16 v[52:55], v[164:167], a[232:235], v[52:55]\n"
    "v_exp_f32 v91, v91\n"
    "v_mfma_f32_16x16x32_bf16 v[52:55], v[160:163], a[228:231], v[52:55]\n"
    "buffer_load_dwordx4 v200, s[36:39], s60 offen offset:1024 sc1 lds\n"
    "v_mfma_f32_16x16x32_bf16 v[56:59], v[160:163], a[252:255], v[104:107]\n"
    "ds_read_b128 v[180:183], v200 offset:45056\n"
    "v_cvt_pk_bf16_f32 v150, v84, v85\n"
    "v_mfma_f32_16x16x32_bf16 v[56:59], v[164:167], v[0:3], v[56:59]\n"
    "v_exp_f32 v92, v92\n"
    "v_mfma_f32_16x16x32_bf16 v[56:59], v[168:171], v[4:7], v[56:59]\n"
    "v_exp_f32 v93, v93\n"
    "v_mfma_f32_16x16x32_bf16 v[60:63], v[168:171], v[28:31], v[108:111]\n"
    "buffer_load_dwordx4 v200, s[36:39], s60 offen offset:2048 sc1 lds\n"
    "v_mfma_f32_16x16x32_bf16 v[60:63], v[164:167], v[24:27], v[60:63]\n"
    "v_cvt_pk_bf16_f32 v151, v86, v87\n"
    "v_cvt_pk_bf16_f32 v154, v88, v89\n"
    "v_mfma_f32_16x16x32_bf16 v[60:63], v[160:163], v[20:23], v[60:63]\n"
    "s_waitcnt lgkmcnt(1)\n"
    "s_add_u32 m0, s71, 0x12000\n"
    "v_cvt_pk_bf16_f32 v155, v90, v91\n"
    "v_mfma_f32_16x16x32_bf16 v[60:63], v[172:175], v[8:11], v[60:63]\n"
    "ds_read_b128 v[160:163], v201\n"
    "v_cvt_pk_bf16_f32 v158, v92, v93\n"
    "v_mfma_f32_16x16x32_bf16 v[60:63], v[176:179], v[12:15], v[60:63]\n"
    "s_waitcnt lgkmcnt(1)\n"
    "buffer_load_dwordx4 v200, s[40:43], s61 offen sc1 lds\n"
    "v_mfma_f32_16x16x32_bf16 v[60:63], v[180:183], v[16:19], v[60:63]\n"
    "v_exp_f32 v94, v94\n"
    "v_mfma_f32_16x16x32_bf16 v[56:59], v[180:183], a[248:251], v[56:59]\n"
    "ds_read_b128 v[164:167], v201 offset:1024\n"
    "v_add_f32 v113, v113, v64\n"
    "v_mfma_f32_16x16x32_bf16 v[56:59], v[176:179], a[244:247], v[56:59]\n"
    "v_exp_f32 v95, v95\n"
    "v_mfma_f32_16x16x32_bf16 v[56:59], v[172:175], a[240:243], v[56:59]\n"
    "buffer_load_dwordx4 v200, s[40:43], s61 offen offset:1024 sc1 lds\n"
    "v_mfma_f32_16x16x32_bf16 v[52:55], v[172:175], a[216:219], v[52:55]\n"
    "ds_read_b128 v[168:171], v201 offset:2048\n"
    "v_cvt_pk_bf16_f32 v159, v94, v95\n"
    "v_mfma_f32_16x16x32_bf16 v[52:55], v[176:179], a[220:223], v[52:55]\n"
    "v_exp_f32 v32, v32\n"
    "v_mfma_f32_16x16x32_bf16 v[52:55], v[180:183], a[224:227], v[52:55]\n"
    "v_exp_f32 v33, v33\n"
    "v_mfma_f32_16x16x32_bf16 v[48:51], v[180:183], a[200:203], v[48:51]\n"
    "buffer_load_dwordx4 v200, s[40:43], s61 offen offset:2048 sc1 lds\n"
    "v_mfma_f32_16x16x32_bf16 v[48:51], v[176:179], a[196:199], v[48:51]\n"
    "v_exp_f32 v34, v34\n"
    "v_mfma_f32_16x16x32_bf16 v[48:51], v[172:175], a[192:195], v[48:51]\n"
    "s_waitcnt lgkmcnt(1)\n"
    "v_exp_f32 v35, v35\n"
    "v_mfma_f32_16x16x32_bf16 a[0:3], v[160:163], v[128:131], a[0:3]\n"
    "ds_read_b128 v[172:175], v201 offset:3072\n"
    "ds_read_b128 v[176:179], v201 offset:4096\n"
    "v_mfma_f32_16x16x32_bf16 a[0:3], v[164:167], v[144:147], a[0:3]\n"
    "buffer_load_dwordx4 v200, s[40:43], s61 offen offset:3072 sc1 lds\n"
    "v_mfma_f32_16x16x32_bf16 a[4:7], v[160:163], v[132:135], a[4:7]\n"
    "s_add_u32 s65, s61, 0x1000\n"
    "v_exp_f32 v36, v36\n"
    "v_mfma_f32_16x16x32_bf16 a[4:7], v[164:167], v[148:151], a[4:7]\n"
    "s_add_u32 m0, s71, 0x13000\n"
    "v_exp_f32 v37, v37\n"
    "v_mfma_f32_16x16x32_bf16 a[8:11], v[160:163], v[136:139], a[8:11]\n"
    "ds_read_b128 v[180:183], v201 offset:5120\n"
    "v_add_f32 v114, v114, v65\n"
    "v_mfma_f32_16x16x32_bf16 a[8:11], v[164:167], v[152:155], a[8:11]\n"
    "buffer_load_dwordx4 v200, s[40:43], s65 offen sc1 lds\n"
    "v_mfma_f32_16x16x32_bf16 a[12:15], v[160:163], v[140:143], a[12:15]\n"
    "v_exp_f32 v38, v38\n"
    "v_mfma_f32_16x16x32_bf16 a[12:15], v[164:167], v[156:159], a[12:15]\n"
    "s_waitcnt lgkmcnt(2)\n"
    "v_exp_f32 v39, v39\n"
    "v_mfma_f32_16x16x32_bf16 a[28:31], v[168:171], v[140:143], a[28:31]\n"
    "ds_read_b128 v[160:163], v201 offset:6144\n"
    "v_add_f32 v113, v113, v66\n"
    "v_mfma_f32_16x16x32_bf16 a[28:31], v[172:175], v[156:159], a[28:31]\n"
    "buffer_load_dwordx4 v200, s[40:43], s65 offen offset:1024 sc1 lds\n"
    "v_mfma_f32_16x16x32_bf16 a[24:27], v[172:175], v[152:155], a[24:27]\n"
    "v_exp_f32 v40, v40\n"
    "v_mfma_f32_16x16x32_bf16 a[24:27], v[168:171], v[136:139], a[24:27]\n"
    "v_exp_f32 v41, v41\n"
    "v_mfma_f32_16x16x32_bf16 a[20:23], v[168:171], v[132:135], a[20:23]\n"
    "ds_read_b128 v[164:167], v201 offset:7168\n"
    "v_add_f32 v114, v114, v67\n"
    "v_mfma_f32_16x16x32_bf16 a[20:23], v[172:175], v[148:151], a[20:23]\n"
    "v_exp_f32 v42, v42\n"
    "v_mfma_f32_16x16x32_bf16 a[16:19], v[172:175], v[144:147], a[16:19]\n"
    "v_exp_f32 v43, v43\n"
    "v_mfma_f32_16x16x32_bf16 a[16:19], v[168:171], v[128:131], a[16:19]\n"
    "s_waitcnt lgkmcnt(2)\n"
    "v_exp_f32 v44, v44\n"
    "v_mfma_f32_16x16x32_bf16 a[32:35], v[176:179], v[128:131], a[32:35]\n"
    "ds_read_b128 v[168:171], v201 offset:8192\n"
    "v_add_f32 v117, v117, v68\n"
    "v_mfma_f32_16x16x32_bf16 a[32:35], v[180:183], v[144:147], a[32:35]\n"
    "buffer_load_dwordx2 v[218:219], v213, s[44:47], 0 offen sc1\n"
    "v_mfma_f32_16x16x32_bf16 a[36:39], v[180:183], v[148:151], a[36:39]\n"
    "v_exp_f32 v45, v45\n"
    "v_mfma_f32_16x16x32_bf16 a[36:39], v[176:179], v[132:135], a[36:39]\n"
    "v_exp_f32 v46, v46\n"
    "v_mfma_f32_16x16x32_bf16 a[40:43], v[176:179], v[136:139], a[40:43]\n"
    "ds_read_b128 v[172:175], v201 offset:9216\n"
    "v_add_f32 v118, v118, v69\n"
    "v_mfma_f32_16x16x32_bf16 a[40:43], v[180:183], v[152:155], a[40:43]\n"
    "v_exp_f32 v47, v47\n"
    "v_mfma_f32_16x16x32_bf16 a[44:47], v[180:183], v[156:159], a[44:47]\n"
    "v_exp_f32 v48, v48\n"
    "v_mfma_f32_16x16x32_bf16 a[44:47], v[176:179], v[140:143], a[44:47]\n"
    "s_waitcnt lgkmcnt(2)\n"
    "v_exp_f32 v49, v49\n"
    "v_mfma_f32_16x16x32_bf16 a[60:63], v[160:163], v[140:143], a[60:63]\n"
    "ds_read_b128 v[176:179], v201 offset:10240\n"
    "v_add_f32 v117, v117, v70\n"
    "v_mfma_f32_16x16x32_bf16 a[60:63], v[164:167], v[156:159], a[60:63]\n"
    "v_exp_f32 v50, v50\n"
    "v_mfma_f32_16x16x32_bf16 a[56:59], v[164:167], v[152:155], a[56:59]\n"
    "v_exp_f32 v51, v51\n"
    "v_mfma_f32_16x16x32_bf16 a[56:59], v[160:163], v[136:139], a[56:59]\n"
    "v_add_f32 v113, v113, v32\n"
    "v_add_f32 v118, v118, v71\n"
    "v_mfma_f32_16x16x32_bf16 a[52:55], v[160:163], v[132:135], a[52:55]\n"
    "ds_read_b128 v[180:183], v201 offset:11264\n"
    "v_add_f32 v114, v114, v33\n"
    "v_mfma_f32_16x16x32_bf16 a[52:55], v[164:167], v[148:151], a[52:55]\n"
    "v_exp_f32 v52, v52\n"
    "v_mfma_f32_16x16x32_bf16 a[48:51], v[164:167], v[144:147], a[48:51]\n"
    "v_add_f32 v121, v121, v72\n"
    "v_add_f32 v113, v113, v34\n"
    "v_mfma_f32_16x16x32_bf16 a[48:51], v[160:163], v[128:131], a[48:51]\n"
    "s_waitcnt lgkmcnt(2)\n"
    "v_exp_f32 v53, v53\n"
    "v_mfma_f32_16x16x32_bf16 a[64:67], v[168:171], v[128:131], a[64:67]\n"
    "ds_read_b128 v[160:163], v201 offset:12288\n"
    "v_add_f32 v122, v122, v73\n"
    "v_mfma_f32_16x16x32_bf16 a[64:67], v[172:175], v[144:147], a[64:67]\n"
    "v_add_f32 v114, v114, v35\n"
    "v_add_f32 v121, v121, v74\n"
    "v_mfma_f32_16x16x32_bf16 a[68:71], v[172:175], v[148:151], a[68:71]\n"
    "v_add_f32 v117, v117, v36\n"
    "v_add_f32 v122, v122, v75\n"
    "v_mfma_f32_16x16x32_bf16 a[68:71], v[168:171], v[132:135], a[68:71]\n"
    "s_add_u32 m0, s51, s95\n"
    "v_exp_f32 v54, v54\n"
    "v_mfma_f32_16x16x32_bf16 a[72:75], v[168:171], v[136:139], a[72:75]\n"
    "ds_read_b128 v[164:167], v201 offset:13312\n"
    "v_add_f32 v118, v118, v37\n"
    "v_mfma_f32_16x16x32_bf16 a[72:75], v[172:175], v[152:155], a[72:75]\n"
    "buffer_load_dwordx4 v202, s[32:35], s64 offen lds\n"
    "v_mfma_f32_16x16x32_bf16 a[76:79], v[172:175], v[156:159], a[76:79]\n"
    "v_add_f32 v125, v125, v76\n"
    "v_add_f32 v117, v117, v38\n"
    "v_mfma_f32_16x16x32_bf16 a[76:79], v[168:171], v[140:143], a[76:79]\n"
    "s_waitcnt lgkmcnt(2)\n"
    "v_exp_f32 v55, v55\n"
    "v_mfma_f32_16x16x32_bf16 a[92:95], v[176:179], v[140:143], a[92:95]\n"
    "ds_read_b128 v[168:171], v201 offset:14336\n"
    "v_add_f32 v126, v126, v77\n"
    "v_mfma_f32_16x16x32_bf16 a[92:95], v[180:183], v[156:159], a[92:95]\n"
    "buffer_load_dwordx4 v203, s[32:35], s64 offen offset:1024 lds\n"
    "v_mfma_f32_16x16x32_bf16 a[88:91], v[180:183], v[152:155], a[88:91]\n"
    "v_add_f32 v118, v118, v39\n"
    "v_add_f32 v125, v125, v78\n"
    "v_mfma_f32_16x16x32_bf16 a[88:91], v[176:179], v[136:139], a[88:91]\n"
    "v_add_f32 v121, v121, v40\n"
    "v_add_f32 v126, v126, v79\n"
    "v_mfma_f32_16x16x32_bf16 a[84:87], v[176:179], v[132:135], a[84:87]\n"
    "ds_read_b128 v[172:175], v201 offset:15360\n"
    "v_add_f32 v122, v122, v41\n"
    "v_mfma_f32_16x16x32_bf16 a[84:87], v[180:183], v[148:151], a[84:87]\n"
    "buffer_load_dwordx4 v204, s[32:35], s64 offen offset:2048 lds\n"
    "v_mfma_f32_16x16x32_bf16 a[80:83], v[180:183], v[144:147], a[80:83]\n"
    "v_exp_f32 v56, v56\n"
    "v_mfma_f32_16x16x32_bf16 a[80:83], v[176:179], v[128:131], a[80:83]\n"
    "s_waitcnt lgkmcnt(2)\n"
    "v_add_f32 v113, v113, v80\n"
    "v_add_f32 v121, v121, v42\n"
    "v_mfma_f32_16x16x32_bf16 a[96:99], v[160:163], v[128:131], a[96:99]\n"
    "s_mov_b32 s95, s94\n"
    "ds_read_b128 v[176:179], v201 offset:16384\n"
    "v_add_f32 v114, v114, v81\n"
    "v_mfma_f32_16x16x32_bf16 a[96:99], v[164:167], v[144:147], a[96:99]\n"
    "s_sub_u32 s94, s94, 0x3000\n"
    "v_exp_f32 v57, v57\n"
    "v_mfma_f32_16x16x32_bf16 a[100:103], v[164:167], v[148:151], a[100:103]\n"
    "s_min_u32 s94, s94, 0x6000\n"
    "v_add_f32 v122, v122, v43\n"
    "v_add_f32 v113, v113, v82\n"
    "v_mfma_f32_16x16x32_bf16 a[100:103], v[160:163], v[132:135], a[100:103]\n"
    "s_sub_u32 s93, s94, s95\n"
    "v_add_f32 v125, v125, v44\n"
    "v_add_f32 v114, v114, v83\n"
    "v_mfma_f32_16x16x32_bf16 a[104:107], v[160:163], v[136:139], a[104:107]\n"
    "ds_read_b128 v[180:183], v201 offset:17408\n"
    "v_add_f32 v126, v126, v45\n"
    "v_mfma_f32_16x16x32_bf16 a[104:107], v[164:167], v[152:155], a[104:107]\n"
    "v_exp_f32 v58, v58\n"
    "v_mfma_f32_16x16x32_bf16 a[108:111], v[164:167], v[156:159], a[108:111]\n"
    "v_add_f32 v117, v117, v84\n"
    "v_add_f32 v125, v125, v46\n"
    "v_mfma_f32_16x16x32_bf16 a[108:111], v[160:163], v[140:143], a[108:111]\n"
    "s_waitcnt lgkmcnt(2)\n"
    "v_exp_f32 v59, v59\n"
    "v_mfma_f32_16x16x32_bf16 a[124:127], v[168:171], v[140:143], a[124:127]\n"
    "ds_read_b128 v[160:163], v201 offset:18432\n"
    "v_add_f32 v118, v118, v85\n"
    "v_mfma_f32_16x16x32_bf16 a[124:127], v[172:175], v[156:159], a[124:127]\n"
    "v_add_f32 v126, v126, v47\n"
    "v_add_f32 v117, v117, v86\n"
    "v_mfma_f32_16x16x32_bf16 a[120:123], v[172:175], v[152:155], a[120:123]\n"
    "v_add_f32 v113, v113, v48\n"
    "v_add_u32 v220, 1, v220\n"
    "v_mfma_f32_16x16x32_bf16 a[120:123], v[168:171], v[136:139], a[120:123]\n"
    "v_exp_f32 v60, v60\n"
    "v_mfma_f32_16x16x32_bf16 a[116:119], v[168:171], v[132:135], a[116:119]\n"
    "ds_read_b128 v[164:167], v201 offset:19456\n"
    "v_add_u32 v205, s93, v205\n"
    "v_mfma_f32_16x16x32_bf16 a[116:119], v[172:175], v[148:151], a[116:119]\n"
    "v_add_f32 v118, v118, v87\n"
    "v_add_f32 v114, v114, v49\n"
    "v_mfma_f32_16x16x32_bf16 a[112:115], v[172:175], v[144:147], a[112:115]\n"
    "v_add_u32 v209, 0x180, v209\n"
    "v_add_u32 v206, s93, v206\n"
    "v_mfma_f32_16x16x32_bf16 a[112:115], v[168:171], v[128:131], a[112:115]\n"
    "s_waitcnt lgkmcnt(2)\n"
    "v_add_f32 v121, v121, v88\n"
    "v_add_f32 v113, v113, v50\n"
    "v_mfma_f32_16x16x32_bf16 a[128:131], v[176:179], v[128:131], a[128:131]\n"
    "s_add_u32 s64, s64, 0xc000\n"
    "ds_read_b128 v[168:171], v201 offset:20480\n"
    "v_add_u32 v210, 0x80, v210\n"
    "v_mfma_f32_16x16x32_bf16 a[128:131], v[180:183], v[144:147], a[128:131]\n"
    "s_add_u32 s62, s62, 0x6000\n"
    "v_exp_f32 v61, v61\n"
    "v_mfma_f32_16x16x32_bf16 a[132:135], v[180:183], v[148:151], a[132:135]\n"
    "s_add_u32 s63, s63, 0x6000\n"
    "v_add_u32 v207, s93, v207\n"
    "v_add_f32 v122, v122, v89\n"
    "v_mfma_f32_16x16x32_bf16 a[132:135], v[176:179], v[132:135], a[132:135]\n"
    "s_add_u32 s60, s60, 0x6000\n"
    "v_add_f32 v114, v114, v51\n"
    "v_add_f32 v121, v121, v90\n"
    "v_mfma_f32_16x16x32_bf16 a[136:139], v[176:179], v[136:139], a[136:139]\n"
    "s_add_u32 s61, s61, 0x6000\n"
    "ds_read_b128 v[172:175], v201 offset:21504\n"
    "v_add_f32 v117, v117, v52\n"
    "v_mfma_f32_16x16x32_bf16 a[136:139], v[180:183], v[152:155], a[136:139]\n"
    "s_add_u32 s66, s66, s74\n"
    "v_exp_f32 v62, v62\n"
    "v_mfma_f32_16x16x32_bf16 a[140:143], v[180:183], v[156:159], a[140:143]\n"
    "s_min_u32 s66, s66, 33\n"
    "v_add_f32 v122, v122, v91\n"
    "v_add_f32 v118, v118, v53\n"
    "v_mfma_f32_16x16x32_bf16 a[140:143], v[176:179], v[140:143], a[140:143]\n"
    "s_waitcnt lgkmcnt(2)\n"
    "v_add_f32 v125, v125, v92\n"
    "v_add_f32 v117, v117, v54\n"
    "v_mfma_f32_16x16x32_bf16 a[156:159], v[160:163], v[140:143], a[156:159]\n"
    "ds_read_b128 v[176:179], v201 offset:22528\n"
    "v_add_f32 v126, v126, v93\n"
    "v_mfma_f32_16x16x32_bf16 a[156:159], v[164:167], v[156:159], a[156:159]\n"
    "s_waitcnt vmcnt(3)\n"
    "ds_read_b128 v[180:183], v201 offset:23552\n"
    "v_add_f32 v118, v118, v55\n"
    "v_mfma_f32_16x16x32_bf16 a[152:155], v[164:167], v[152:155], a[152:155]\n"
    "v_cmp_ne_u32 s[80:81], s67, v219\n"
    "v_add_f32 v125, v125, v94\n"
    "v_mfma_f32_16x16x32_bf16 a[152:155], v[160:163], v[136:139], a[152:155]\n"
    "v_cmp_gt_u32 vcc, s66, v218\n"
    "v_add_f32 v121, v121, v56\n"
    "v_mfma_f32_16x16x32_bf16 a[148:151], v[160:163], v[132:135], a[148:151]\n"
    "v_exp_f32 v63, v63\n"
    "v_mfma_f32_16x16x32_bf16 a[148:151], v[164:167], v[148:151], a[148:151]\n"
    "s_or_b64 vcc, vcc, s[80:81]\n"
    "s_cbranch_vccnz LSPRE4\n"
    "LSPB4:\n"
    "v_add_f32 v126, v126, v95\n"
    "v_mfma_f32_16x16x32_bf16 a[144:147], v[164:167], v[144:147], a[144:147]\n"
    "v_add_f32 v122, v122, v57\n"
    "v_mfma_f32_16x16x32_bf16 a[144:147], v[160:163], v[128:131], a[144:147]\n"
    "s_waitcnt lgkmcnt(0)\n"
    "v_mfma_f32_16x16x32_bf16 a[160:163], v[168:171], v[128:131], a[160:163]\n"
    "s_barrier\n"
    "v_mfma_f32_16x16x32_bf16 a[160:163], v[172:175], v[144:147], a[160:163]\n"
    "buffer_store_dwordx2 v[220:221], v214, s[44:47], 0 offen sc1\n"
    "v_mfma_f32_16x16x32_bf16 a[164:167], v[172:175], v[148:151], a[164:167]\n"
    "ds_read_b128 v[160:163], v200\n"
    "ds_read_u16_d16_hi v223, v209 offset:6144\n"
    "v_mfma_f32_16x16x32_bf16 a[164:167], v[168:171], v[132:135], a[164:167]\n"
    "ds_read_u16_d16_hi v222, v209\n"
    "v_add_f32 v121, v121, v58\n"
    "v_mfma_f32_16x16x32_bf16 a[168:171], v[168:171], v[136:139], a[168:171]\n"
    "ds_read_b128 v[164:167], v200 offset:1024\n"
    "ds_read_b128 v[232:235], v205 offset:24576\n"
    "v_mfma_f32_16x16x32_bf16 a[168:171], v[172:175], v[152:155], a[168:171]\n"
    "v_add_f32 v122, v122, v59\n"
    "ds_read_b128 v[236:239], v206 offset:24576\n"
    "v_mfma_f32_16x16x32_bf16 a[172:175], v[172:175], v[156:159], a[172:175]\n"
    "v_add_f32 v125, v125, v60\n"
    "v_add_f32 v126, v126, v61\n"
    "v_mfma_f32_16x16x32_bf16 a[172:175], v[168:171], v[140:143], a[172:175]\n"
    "v_add_f32 v125, v125, v62\n"
    "v_add_f32 v126, v126, v63\n"
    "v_mfma_f32_16x16x32_bf16 a[188:191], v[176:179], v[140:143], a[188:191]\n"
    "ds_read_b128 v[168:171], v200 offset:2048\n"
    "ds_read_b64 v[228:229], v210\n"
    "v_mfma_f32_16x16x32_bf16 a[188:191], v[180:183], v[156:159], a[188:191]\n"
    "ds_read2st64_b32 v[240:241], v207 offset0:96 offset1:99\n"
    "ds_read2st64_b32 v[242:243], v207 offset0:102 offset1:105\n"
    "v_mfma_f32_16x16x32_bf16 a[184:187], v[176:179], v[136:139], a[184:187]\n"
    "ds_read2st64_b32 v[244:245], v207 offset0:108 offset1:111\n"
    "ds_read2st64_b32 v[246:247], v207 offset0:114 offset1:117\n"
    "v_mfma_f32_16x16x32_bf16 a[184:187], v[180:183], v[152:155], a[184:187]\n"
    "v_cvt_pk_bf16_f32 v140, v44, v45\n"
    "v_cvt_pk_bf16_f32 v141, v46, v47\n"
    "v_mfma_f32_16x16x32_bf16 a[180:183], v[176:179], v[132:135], a[180:183]\n"
    "ds_read_b128 v[172:175], v200 offset:3072\n"
    "s_waitcnt lgkmcnt(7)\n"
    "v_mfma_f32_16x16x32_bf16 a[180:183], v[180:183], v[148:151], a[180:183]\n"
    "v_mul_f32 v226, s75, v223\n"
    "v_fma_f32 v227, -v222, v226, v217\n"
    "v_mfma_f32_16x16x32_bf16 a[176:179], v[176:179], v[128:131], a[176:179]\n"
    "v_cvt_f32_i32 v232, v232\n"
    "v_cvt_f32_i32 v233, v233\n"
    "v_mfma_f32_16x16x32_bf16 a[176:179], v[180:183], v[144:147], a[176:179]\n"
    "v_cvt_f32_i32 v234, v234\n"
    "v_cvt_f32_i32 v235, v235\n"
    "LM0:\n"
    "v_mfma_f32_16x16x32_bf16 v[64:67], v[160:163], a[192:195], v[96:99]\n"
    "ds_read_b128 v[176:179], v200 offset:4096\n"
    "v_cvt_f32_i32 v236, v236\n"
    "v_mfma_f32_16x16x32_bf16 v[64:67], v[164:167], a[196:199], v[64:67]\n"
    "s_waitcnt lgkmcnt(1)\n"
    "v_cvt_f32_i32 v237, v237\n"
    "v_cvt_f32_i32 v238, v238\n"
    "v_mfma_f32_16x16x32_bf16 v[64:67], v[168:171], a[200:203], v[64:67]\n"
    "v_cvt_f32_i32 v239, v239\n"
    "v_mul_f32_dpp v232, v226, v232 row_newbcast:0 row_mask:0xf bank_mask:0xf\n"
    "v_mfma_f32_16x16x32_bf16 v[68:71], v[168:171], a[224:227], v[100:103]\n"
    "v_mul_f32_dpp v233, v226, v233 row_newbcast:1 row_mask:0xf bank_mask:0xf\n"
    "v_mul_f32_dpp v234, v226, v234 row_newbcast:2 row_mask:0xf bank_mask:0xf\n"
    "v_mfma_f32_16x16x32_bf16 v[68:71], v[164:167], a[220:223], v[68:71]\n"
    "ds_read_b128 v[180:183], v200 offset:5120\n"
    "v_mul_f32_dpp v235, v226, v235 row_newbcast:3 row_mask:0xf bank_mask:0xf\n"
    "v_mfma_f32_16x16x32_bf16 v[68:71], v[160:163], a[216:219], v[68:71]\n"
    "v_mul_f32_dpp v236, v226, v236 row_newbcast:4 row_mask:0xf bank_mask:0xf\n"
    "v_mul_f32_dpp v237, v226, v237 row_newbcast:5 row_mask:0xf bank_mask:0xf\n"
    "v_mfma_f32_16x16x32_bf16 v[72:75], v[160:163], a[240:243], v[104:107]\n"
    "v_mul_f32_dpp v238, v226, v238 row_newbcast:6 row_mask:0xf bank_mask:0xf\n"
    "v_mul_f32_dpp v239, v226, v239 row_newbcast:7 row_mask:0xf bank_mask:0xf\n"
    "v_mfma_f32_16x16x32_bf16 v[72:75], v[164:167], a[244:247], v[72:75]\n"
    "v_add_f32_dpp v232, v227, v232 row_newbcast:0 row_mask:0xf bank_mask:0xf\n"
    "v_add_f32_dpp v233, v227, v233 row_newbcast:1 row_mask:0xf bank_mask:0xf\n"
    "v_mfma_f32_16x16x32_bf16 v[72:75], v[168:171], a[248:251], v[72:75]\n"
    "v_add_f32_dpp v234, v227, v234 row_newbcast:2 row_mask:0xf bank_mask:0xf\n"
    "v_add_f32_dpp v235, v227, v235 row_newbcast:3 row_mask:0xf bank_mask:0xf\n"
    "v_mfma_f32_16x16x32_bf16 v[76:79], v[168:171], v[16:19], v[108:111]\n"
    "v_add_f32_dpp v236, v227, v236 row_newbcast:4 row_mask:0xf bank_mask:0xf\n"
    "v_add_f32_dpp v237, v227, v237 row_newbcast:5 row_mask:0xf bank_mask:0xf\n"
    "v_mfma_f32_16x16x32_bf16 v[76:79], v[164:167], v[12:15], v[76:79]\n"
    "v_add_f32_dpp v238, v227, v238 row_newbcast:6 row_mask:0xf bank_mask:0xf\n"
    "v_add_f32_dpp v239, v227, v239 row_newbcast:7 row_mask:0xf bank_mask:0xf\n"
    "v_mfma_f32_16x16x32_bf16 v[76:79], v[160:163], v[8:11], v[76:79]\n"
    "s_waitcnt lgkmcnt(1)\n"
    "v_cvt_pk_bf16_f32 v232, v232, v233\n"
    "v_cvt_f32_i32 v240, v240\n"
    "v_mfma_f32_16x16x32_bf16 v[76:79], v[172:175], v[20:23], v[76:79]\n"
    "ds_read_b128 v[160:163], v200 offset:9216\n"
    "v_cvt_pk_bf16_f32 v233, v234, v235\n"
    "v_mfma_f32_16x16x32_bf16 v[76:79], v[176:179], v[24:27], v[76:79]\n"
    "s_waitcnt lgkmcnt(1)\n"
    "v_cvt_f32_i32 v241, v241\n"
    "v_cvt_pk_bf16_f32 v234, v236, v237\n"
    "v_mfma_f32_16x16x32_bf16 v[76:79], v[180:183], v[28:31], v[76:79]\n"
    "v_cvt_f32_i32 v242, v242\n"
    "v_cvt_pk_bf16_f32 v235, v238, v239\n"
    "v_mfma_f32_16x16x32_bf16 v[72:75], v[180:183], v[4:7], v[72:75]\n"
    "ds_read_b128 v[164:167], v200 offset:10240\n"
    "v_cvt_f32_i32 v243, v243\n"
    "v_mfma_f32_16x16x32_bf16 v[72:75], v[176:179], v[0:3], v[72:75]\n"
    "v_cvt_f32_i32 v244, v244\n"
    "v_cvt_f32_i32 v245, v245\n"
    "v_mfma_f32_16x16x32_bf16 v[72:75], v[172:175], a[252:255], v[72:75]\n"
    "v_cvt_f32_i32 v246, v246\n"
    "v_cvt_f32_i32 v247, v247\n"
    "v_mfma_f32_16x16x32_bf16 v[68:71], v[172:175], a[228:231], v[68:71]\n"
    "ds_read_b128 v[168:171], v200 offset:11264\n"
    "v_mul_f32_dpp v240, v228, v240 row_newbcast:0 row_mask:0xf bank_mask:0xf\n"
    "v_mfma_f32_16x16x32_bf16 v[68:71], v[176:179], a[232:235], v[68:71]\n"
    "v_mul_f32_dpp v241, v228, v241 row_newbcast:1 row_mask:0xf bank_mask:0xf\n"
    "v_mul_f32_dpp v242, v228, v242 row_newbcast:2 row_mask:0xf bank_mask:0xf\n"
    "v_mfma_f32_16x16x32_bf16 v[68:71], v[180:183], a[236:239], v[68:71]\n"
    "v_mul_f32_dpp v243, v228, v243 row_newbcast:3 row_mask:0xf bank_mask:0xf\n"
    "v_mul_f32_dpp v244, v228, v244 row_newbcast:4 row_mask:0xf bank_mask:0xf\n"
    "v_mfma_f32_16x16x32_bf16 v[64:67], v[180:183], a[212:215], v[64:67]\n"
    "v_mul_f32_dpp v245, v228, v245 row_newbcast:5 row_mask:0xf bank_mask:0xf\n"
    "v_mul_f32_dpp v246, v228, v246 row_newbcast:6 row_mask:0xf bank_mask:0xf\n"
    "v_mfma_f32_16x16x32_bf16 v[64:67], v[176:179], a[208:211], v[64:67]\n"
    "buffer_store_dwordx4 v[232:235], v211, s[36:39], s62 offen sc1\n"
    "v_mul_f32_dpp v247, v228, v247 row_newbcast:7 row_mask:0xf bank_mask:0xf\n"
    "v_add_f32_dpp v240, v229, v240 row_newbcast:0 row_mask:0xf bank_mask:0xf\n"
    "v_add_f32_dpp v241, v229, v241 row_newbcast:1 row_mask:0xf bank_mask:0xf\n"
    "v_mfma_f32_16x16x32_bf16 v[64:67], v[172:175], a[204:207], v[64:67]\n"
    "s_waitcnt lgkmcnt(1)\n"
    "v_add_f32_dpp v242, v229, v242 row_newbcast:2 row_mask:0xf bank_mask:0xf\n"
    "v_add_f32_dpp v243, v229, v243 row_newbcast:3 row_mask:0xf bank_mask:0xf\n"
    "v_mfma_f32_16x16x32_bf16 v[80:83], v[160:163], a[204:207], v[96:99]\n"
    "ds_read_b128 v[172:175], v200 offset:6144\n"
    "v_add_f32_dpp v244, v229, v244 row_newbcast:4 row_mask:0xf bank_mask:0xf\n"
    "v_mfma_f32_16x16x32_bf16 v[80:83], v[164:167], a[208:211], v[80:83]\n"
    "s_waitcnt lgkmcnt(1)\n"
    "v_add_f32_dpp v245, v229, v245 row_newbcast:5 row_mask:0xf bank_mask:0xf\n"
    "v_add_f32_dpp v246, v229, v246 row_newbcast:6 row_mask:0xf bank_mask:0xf\n"
    "v_mfma_f32_16x16x32_bf16 v[80:83], v[168:171], a[212:215], v[80:83]\n"
    "v_add_f32_dpp v247, v229, v247 row_newbcast:7 row_mask:0xf bank_mask:0xf\n"
    "v_cvt_pk_bf16_f32 v240, v240, v241\n"
    "v_mfma_f32_16x16x32_bf16 v[84:87], v[168:171], a[236:239], v[100:103]\n"
    "ds_read_b128 v[176:179], v200 offset:7168\n"
    "v_cvt_pk_bf16_f32 v241, v242, v243\n"
    "v_mfma_f32_16x16x32_bf16 v[84:87], v[164:167], a[232:235], v[84:87]\n"
    "v_cvt_pk_bf16_f32 v242, v244, v245\n"
    "v_cvt_pk_bf16_f32 v243, v246, v247\n"
    "v_mfma_f32_16x16x32_bf16 v[84:87], v[160:163], a[228:231], v[84:87]\n"
    "v_cvt_pk_bf16_f32 v136, v40, v41\n"
    "v_cvt_pk_bf16_f32 v137, v42, v43\n"
    "v_mfma_f32_16x16x32_bf16 v[88:91], v[160:163], a[252:255], v[104:107]\n"
    "ds_read_b128 v[180:183], v200 offset:8192\n"
    "v_cvt_pk_bf16_f32 v132, v36, v37\n"
    "v_mfma_f32_16x16x32_bf16 v[88:91], v[164:167], v[0:3], v[88:91]\n"
    "v_cvt_pk_bf16_f32 v133, v38, v39\n"
    "v_cvt_pk_bf16_f32 v128, v32, v33\n"
    "v_mfma_f32_16x16x32_bf16 v[88:91], v[168:171], v[4:7], v[88:91]\n"
    "v_cvt_pk_bf16_f32 v129, v34, v35\n"
    "v_cvt_pk_bf16_f32 v142, v60, v61\n"
    "v_mfma_f32_16x16x32_bf16 v[92:95], v[168:171], v[28:31], v[108:111]\n"
    "v_exp_f32 v64, v64\n"
    "v_mfma_f32_16x16x32_bf16 v[92:95], v[164:167], v[24:27], v[92:95]\n"
    "v_exp_f32 v65, v65\n"
    "v_mfma_f32_16x16x32_bf16 v[92:95], v[160:163], v[20:23], v[92:95]\n"
    "s_waitcnt lgkmcnt(1)\n"
    "v_exp_f32 v66, v66\n"
    "v_mfma_f32_16x16x32_bf16 v[92:95], v[172:175], v[8:11], v[92:95]\n"
    "ds_read_b128 v[160:163], v200 offset:12288\n"
    "v_cvt_pk_bf16_f32 v143, v62, v63\n"
    "v_mfma_f32_16x16x32_bf16 v[92:95], v[176:179], v[12:15], v[92:95]\n"
    "s_waitcnt lgkmcnt(1)\n"
    "v_exp_f32 v67, v67\n"
    "v_mfma_f32_16x16x32_bf16 v[92:95], v[180:183], v[16:19], v[92:95]\n"
    "buffer_store_dwordx4 v[240:243], v212, s[40:43], s63 offen sc1\n"
    "v_exp_f32 v68, v68\n"
    "v_cvt_pk_bf16_f32 v138, v56, v57\n"
    "v_mfma_f32_16x16x32_bf16 v[88:91], v[180:183], a[248:251], v[88:91]\n"
    "ds_read_b128 v[164:167], v200 offset:13312\n"
    "v_cvt_pk_bf16_f32 v139, v58, v59\n"
    "v_mfma_f32_16x16x32_bf16 v[88:91], v[176:179], a[244:247], v[88:91]\n"
    "v_exp_f32 v69, v69\n"
    "v_mfma_f32_16x16x32_bf16 v[88:91], v[172:175], a[240:243], v[88:91]\n"
    "v_exp_f32 v70, v70\n"
    "v_mfma_f32_16x16x32_bf16 v[84:87], v[172:175], a[216:219], v[84:87]\n"
    "ds_read_b128 v[168:171], v200 offset:14336\n"
    "v_cvt_pk_bf16_f32 v134, v52, v53\n"
    "v_mfma_f32_16x16x32_bf16 v[84:87], v[176:179], a[220:223], v[84:87]\n"
    "v_exp_f32 v71, v71\n"
    "v_mfma_f32_16x16x32_bf16 v[84:87], v[180:183], a[224:227], v[84:87]\n"
    "v_exp_f32 v72, v72\n"
    "v_mfma_f32_16x16x32_bf16 v[80:83], v[180:183], a[200:203], v[80:83]\n"
    "v_cvt_pk_bf16_f32 v135, v54, v55\n"
    "v_cvt_pk_bf16_f32 v130, v48, v49\n"
    "v_mfma_f32_16x16x32_bf16 v[80:83], v[176:179], a[196:199], v[80:83]\n"
    "v_exp_f32 v73, v73\n"
    "v_mfma_f32_16x16x32_bf16 v[80:83], v[172:175], a[192:195], v[80:83]\n"
    "s_waitcnt lgkmcnt(1)\n"
    "v_cvt_pk_bf16_f32 v131, v50, v51\n"
    "v_cvt_pk_bf16_f32 v144, v64, v65\n"
    "v_mfma_f32_16x16x32_bf16 v[32:35], v[160:163], a[192:195], v[96:99]\n"
    "ds_read_b128 v[172:175], v200 offset:15360\n"
    "v_cvt_pk_bf16_f32 v145, v66, v67\n"
    "v_mfma_f32_16x16x32_bf16 v[32:35], v[164:167], a[196:199], v[32:35]\n"
    "s_waitcnt lgkmcnt(1)\n"
    "v_exp_f32 v74, v74\n"
    "v_mfma_f32_16x16x32_bf16 v[32:35], v[168:171], a[200:203], v[32:35]\n"
    "v_exp_f32 v75, v75\n"
    "v_mfma_f32_16x16x32_bf16 v[36:39], v[168:171], a[224:227], v[100:103]\n"
    "ds_read_b128 v[176:179], v200 offset:16384\n"
    "v_cvt_pk_bf16_f32 v148, v68, v69\n"
    "v_mfma_f32_16x16x32_bf16 v[36:39], v[164:167], a[220:223], v[36:39]\n"
    "v_exp_f32 v76, v76\n"
    "v_mfma_f32_16x16x32_bf16 v[36:39], v[160:163], a[216:219], v[36:39]\n"
    "v_exp_f32 v77, v77\n"
    "v_mfma_f32_16x16x32_bf16 v[40:43], v[160:163], a[240:243], v[104:107]\n"
    "ds_read_b128 v[180:183], v200 offset:17408\n"
    "v_cvt_pk_bf16_f32 v149, v70, v71\n"
    "v_mfma_f32_16x16x32_bf16 v[40:43], v[164:167], a[244:247], v[40:43]\n"
    "v_exp_f32 v78, v78\n"
    "v_mfma_f32_16x16x32_bf16 v[40:43], v[168:171], a[248:251], v[40:43]\n"
    "v_exp_f32 v79, v79\n"
    "v_mfma_f32_16x16x32_bf16 v[44:47], v[168:171], v[16:19], v[108:111]\n"
    "v_exp_f32 v80, v80\n"
    "v_mfma_f32_16x16x32_bf16 v[44:47], v[164:167], v[12:15], v[44:47]\n"
    "v_exp_f32 v81, v81\n"
    "v_mfma_f32_16x16x32_bf16 v[44:47], v[160:163], v[8:11], v[44:47]\n"
    "s_waitcnt lgkmcnt(1)\n"
    "v_exp_f32 v82, v82\n"
    "v_mfma_f32_16x16x32_bf16 v[44:47], v[172:175], v[20:23], v[44:47]\n"
    "ds_read_b128 v[160:163], v200 offset:21504\n"
    "v_cvt_pk_bf16_f32 v152, v72, v73\n"
    "v_mfma_f32_16x16x32_bf16 v[44:47], v[176:179], v[24:27], v[44:47]\n"
    "s_waitcnt lgkmcnt(1)\n"
    "v_exp_f32 v83, v83\n"
    "v_mfma_f32_16x16x32_bf16 v[44:47], v[180:183], v[28:31], v[44:47]\n"
    "v_exp_f32 v84, v84\n"
    "v_mfma_f32_16x16x32_bf16 v[40:43], v[180:183], v[4:7], v[40:43]\n"
    "ds_read_b128 v[164:167], v200 offset:22528\n"
    "v_cvt_pk_bf16_f32 v153, v74, v75\n"
    "v_mfma_f32_16x16x32_bf16 v[40:43], v[176:179], v[0:3], v[40:43]\n"
    "v_exp_f32 v85, v85\n"
    "v_mfma_f32_16x16x32_bf16 v[40:43], v[172:175], a[252:255], v[40:43]\n"
    "v_exp_f32 v86, v86\n"
    "v_mfma_f32_16x16x32_bf16 v[36:39], v[172:175], a[228:231], v[36:39]\n"
    "ds_read_b128 v[168:171], v200 offset:23552\n"
    "v_cvt_pk_bf16_f32 v156, v76, v77\n"
    "v_mfma_f32_16x16x32_bf16 v[36:39], v[176:179], a[232:235], v[36:39]\n"
    "v_exp_f32 v87, v87\n"
    "v_mfma_f32_16x16x32_bf16 v[36:39], v[180:183], a[236:239], v[36:39]\n"
    "v_exp_f32 v88, v88\n"
    "v_mfma_f32_16x16x32_bf16 v[32:35], v[180:183], a[212:215], v[32:35]\n"
    "v_exp_f32 v89, v89\n"
    "v_mfma_f32_16x16x32_bf16 v[32:35], v[176:179], a[208:211], v[32:35]\n"
    "v_exp_f32 v90, v90\n"
    "v_mfma_f32_16x16x32_bf16 v[32:35], v[172:175], a[204:207], v[32:35]\n"
    "s_waitcnt lgkmcnt(1)\n"
    "s_mov_b32 m0, 0x6000\n"
    "v_cvt_pk_bf16_f32 v157, v78, v79\n"
    "v_mfma_f32_16x16x32_bf16 v[48:51], v[160:163], a[204:207], v[96:99]\n"
    "ds_read_b128 v[172:175], v200 offset:18432\n"
    "v_cvt_pk_bf16_f32 v146, v80, v81\n"
    "v_mfma_f32_16x16x32_bf16 v[48:51], v[164:167], a[208:211], v[48:51]\n"
    "s_waitcnt lgkmcnt(1)\n"
    "buffer_load_dwordx4 v200, s[36:39], s60 offen sc1 lds\n"
    "v_mfma_f32_16x16x32_bf16 v[48:51], v[168:171], a[212:215], v[48:51]\n"
    "v_exp_f32 v91, v91\n"
    "v_mfma_f32_16x16x32_bf16 v[52:55], v[168:171], a[236:239], v[100:103]\n"
    "ds_read_b128 v[176:179], v200 offset:19456\n"
    "v_cvt_pk_bf16_f32 v147, v82, v83\n"
    "v_mfma_f32_16x16x32_bf16 v[52:55], v[164:167], a[232:235], v[52:55]\n"
    "v_exp_f32 v92, v92\n"
    "v_mfma_f32_16x16x32_bf16 v[52:55], v[160:163], a[228:231], v[52:55]\n"
    "buffer_load_dwordx4 v200, s[36:39], s60 offen offset:1024 sc1 lds\n"
    "v_mfma_f32_16x16x32_bf16 v[56:59], v[160:163], a[252:255], v[104:107]\n"
    "ds_read_b128 v[180:183], v200 offset:20480\n"
    "v_cvt_pk_bf16_f32 v150, v84, v85\n"
    "v_mfma_f32_16x16x32_bf16 v[56:59], v[164:167], v[0:3], v[56:59]\n"
    "v_exp_f32 v93, v93\n"
    "v_mfma_f32_16x16x32_bf16 v[56:59], v[168:171], v[4:7], v[56:59]\n"
    "v_cvt_pk_bf16_f32 v151, v86, v87\n"
    "v_cvt_pk_bf16_f32 v154, v88, v89\n"
    "v_mfma_f32_16x16x32_bf16 v[60:63], v[168:171], v[28:31], v[108:111]\n"
    "buffer_load_dwordx4 v200, s[36:39], s60 offen offset:2048 sc1 lds\n"
    "v_mfma_f32_16x16x32_bf16 v[60:63], v[164:167], v[24:27], v[60:63]\n"
    "v_exp_f32 v94, v94\n"
    "v_mfma_f32_16x16x32_bf16 v[60:63], v[160:163], v[20:23], v[60:63]\n"
    "s_waitcnt lgkmcnt(1)\n"
    "s_add_u32 m0, s71, 0xc000\n"
    "v_cvt_pk_bf16_f32 v155, v90, v91\n"
    "v_mfma_f32_16x16x32_bf16 v[60:63], v[172:175], v[8:11], v[60:63]\n"
    "ds_read_b128 v[160:163], v201 offset:24576\n"
    "v_cvt_pk_bf16_f32 v158, v92, v93\n"
    "v_mfma_f32_16x16x32_bf16 v[60:63], v[176:179], v[12:15], v[60:63]\n"
    "s_waitcnt lgkmcnt(1)\n"
    "buffer_load_dwordx4 v200, s[40:43], s61 offen sc1 lds\n"
    "v_mfma_f32_16x16x32_bf16 v[60:63], v[180:183], v[16:19], v[60:63]\n"
    "v_exp_f32 v95, v95\n"
    "v_mfma_f32_16x16x32_bf16 v[56:59], v[180:183], a[248:251], v[56:59]\n"
    "ds_read_b128 v[164:167], v201 offset:25600\n"
    "v_cvt_pk_bf16_f32 v159, v94, v95\n"
    "v_mfma_f32_16x16x32_bf16 v[56:59], v[176:179], a[244:247], v[56:59]\n"
    "v_exp_f32 v32, v32\n"
    "v_mfma_f32_16x16x32_bf16 v[56:59], v[172:175], a[240:243], v[56:59]\n"
    "buffer_load_dwordx4 v200, s[40:43], s61 offen offset:1024 sc1 lds\n"
    "v_mfma_f32_16x16x32_bf16 v[52:55], v[172:175], a[216:219], v[52:55]\n"
    "ds_read_b128 v[168:171], v201 offset:26624\n"
    "v_add_f32 v113, v113, v64\n"
    "v_mfma_f32_16x16x32_bf16 v[52:55], v[176:179], a[220:223], v[52:55]\n"
    "v_exp_f32 v33, v33\n"
    "v_mfma_f32_16x16x32_bf16 v[52:55], v[180:183], a[224:227], v[52:55]\n"
    "v_exp_f32 v34, v34\n"
    "v_mfma_f32_16x16x32_bf16 v[48:51], v[180:183], a[200:203], v[48:51]\n"
    "buffer_load_dwordx4 v200, s[40:43], s61 offen offset:2048 sc1 lds\n"
    "v_mfma_f32_16x16x32_bf16 v[48:51], v[176:179], a[196:199], v[48:51]\n"
    "v_exp_f32 v35, v35\n"
    "v_mfma_f32_16x16x32_bf16 v[48:51], v[172:175], a[192:195], v[48:51]\n"
    "s_waitcnt lgkmcnt(1)\n"
    "v_exp_f32 v36, v36\n"
    "v_mfma_f32_16x16x32_bf16 a[0:3], v[160:163], v[128:131], a[0:3]\n"
    "ds_read_b128 v[172:175], v201 offset:27648\n"
    "ds_read_b128 v[176:179], v201 offset:28672\n"
    "v_mfma_f32_16x16x32_bf16 a[0:3], v[164:167], v[144:147], a[0:3]\n"
    "buffer_load_dwordx4 v200, s[40:43], s61 offen offset:3072 sc1 lds\n"
    "v_mfma_f32_16x16x32_bf16 a[4:7], v[160:163], v[132:135], a[4:7]\n"
    "s_add_u32 s65, s61, 0x1000\n"
    "v_exp_f32 v37, v37\n"
    "v_mfma_f32_16x16x32_bf16 a[4:7], v[164:167], v[148:151], a[4:7]\n"
    "s_add_u32 m0, s71, 0xd000\n"
    "v_exp_f32 v38, v38\n"
    "v_mfma_f32_16x16x32_bf16 a[8:11], v[160:163], v[136:139], a[8:11]\n"
    "ds_read_b128 v[180:183], v201 offset:29696\n"
    "v_add_f32 v114, v114, v65\n"
    "v_mfma_f32_16x16x32_bf16 a[8:11], v[164:167], v[152:155], a[8:11]\n"
    "buffer_load_dwordx4 v200, s[40:43], s65 offen sc1 lds\n"
    "v_mfma_f32_16x16x32_bf16 a[12:15], v[160:163], v[140:143], a[12:15]\n"
    "v_exp_f32 v39, v39\n"
    "v_mfma_f32_16x16x32_bf16 a[12:15], v[164:167], v[156:159], a[12:15]\n"
    "s_waitcnt lgkmcnt(2)\n"
    "v_exp_f32 v40, v40\n"
    "v_mfma_f32_16x16x32_bf16 a[28:31], v[168:171], v[140:143], a[28:31]\n"
    "ds_read_b128 v[160:163], v201 offset:30720\n"
    "v_add_f32 v113, v113, v66\n"
    "v_mfma_f32_16x16x32_bf16 a[28:31], v[172:175], v[156:159], a[28:31]\n"
    "buffer_load_dwordx4 v200, s[40:43], s65 offen offset:1024 sc1 lds\n"
    "v_mfma_f32_16x16x32_bf16 a[24:27], v[172:175], v[152:155], a[24:27]\n"
    "v_exp_f32 v41, v41\n"
    "v_mfma_f32_16x16x32_bf16 a[24:27], v[168:171], v[136:139], a[24:27]\n"
    "v_exp_f32 v42, v42\n"
    "v_mfma_f32_16x16x32_bf16 a[20:23], v[168:171], v[132:135], a[20:23]\n"
    "ds_read_b128 v[164:167], v201 offset:31744\n"
    "v_add_f32 v114, v114, v67\n"
    "v_mfma_f32_16x16x32_bf16 a[20:23], v[172:175], v[148:151], a[20:23]\n"
    "v_exp_f32 v43, v43\n"
    "v_mfma_f32_16x16x32_bf16 a[16:19], v[172:175], v[144:147], a[16:19]\n"
    "v_exp_f32 v44, v44\n"
    "v_mfma_f32_16x16x32_bf16 a[16:19], v[168:171], v[128:131], a[16:19]\n"
    "s_waitcnt lgkmcnt(2)\n"
    "v_exp_f32 v45, v45\n"
    "v_mfma_f32_16x16x32_bf16 a[32:35], v[176:179], v[128:131], a[32:35]\n"
    "ds_read_b128 v[168:171], v201 offset:32768\n"
    "v_add_f32 v117, v117, v68\n"
    "v_mfma_f32_16x16x32_bf16 a[32:35], v[180:183], v[144:147], a[32:35]\n"
    "buffer_load_dwordx2 v[218:219], v213, s[44:47], 0 offen sc1\n"
    "v_mfma_f32_16x16x32_bf16 a[36:39], v[180:183], v[148:151], a[36:39]\n"
    "v_exp_f32 v46, v46\n"
    "v_mfma_f32_16x16x32_bf16 a[36:39], v[176:179], v[132:135], a[36:39]\n"
    "v_exp_f32 v47, v47\n"
    "v_mfma_f32_16x16x32_bf16 a[40:43], v[176:179], v[136:139], a[40:43]\n"
    "ds_read_b128 v[172:175], v201 offset:33792\n"
    "v_add_f32 v118, v118, v69\n"
    "v_mfma_f32_16x16x32_bf16 a[40:43], v[180:183], v[152:155], a[40:43]\n"
    "v_exp_f32 v48, v48\n"
    "v_mfma_f32_16x16x32_bf16 a[44:47], v[180:183], v[156:159], a[44:47]\n"
    "v_exp_f32 v49, v49\n"
    "v_mfma_f32_16x16x32_bf16 a[44:47], v[176:179], v[140:143], a[44:47]\n"
    "s_waitcnt lgkmcnt(2)\n"
    "v_exp_f32 v50, v50\n"
    "v_mfma_f32_16x16x32_bf16 a[60:63], v[160:163], v[140:143], a[60:63]\n"
    "ds_read_b128 v[176:179], v201 offset:34816\n"
    "v_add_f32 v117, v117, v70\n"
    "v_mfma_f32_16x16x32_bf16 a[60:63], v[164:167], v[156:159], a[60:63]\n"
    "v_exp_f32 v51, v51\n"
    "v_mfma_f32_16x16x32_bf16 a[56:59], v[164:167], v[152:155], a[56:59]\n"
    "v_add_f32 v113, v113, v32\n"
    "v_add_f32 v118, v118, v71\n"
    "v_mfma_f32_16x16x32_bf16 a[56:59], v[160:163], v[136:139], a[56:59]\n"
    "v_exp_f32 v52, v52\n"
    "v_mfma_f32_16x16x32_bf16 a[52:55], v[160:163], v[132:135], a[52:55]\n"
    "ds_read_b128 v[180:183], v201 offset:35840\n"
    "v_add_f32 v114, v114, v33\n"
    "v_mfma_f32_16x16x32_bf16 a[52:55], v[164:167], v[148:151], a[52:55]\n"
    "v_add_f32 v121, v121, v72\n"
    "v_add_f32 v113, v113, v34\n"
    "v_mfma_f32_16x16x32_bf16 a[48:51], v[164:167], v[144:147], a[48:51]\n"
    "v_exp_f32 v53, v53\n"
    "v_mfma_f32_16x16x32_bf16 a[48:51], v[160:163], v[128:131], a[48:51]\n"
    "s_waitcnt lgkmcnt(2)\n"
    "v_add_f32 v122, v122, v73\n"
    "v_add_f32 v114, v114, v35\n"
    "v_mfma_f32_16x16x32_bf16 a[64:67], v[168:171], v[128:131], a[64:67]\n"
    "ds_read_b128 v[160:163], v201 offset:36864\n"
    "v_add_f32 v121, v121, v74\n"
    "v_mfma_f32_16x16x32_bf16 a[64:67], v[172:175], v[144:147], a[64:67]\n"
    "v_add_f32 v117, v117, v36\n"
    "v_add_f32 v122, v122, v75\n"
    "v_mfma_f32_16x16x32_bf16 a[68:71], v[172:175], v[148:151], a[68:71]\n"
    "v_exp_f32 v54, v54\n"
    "v_mfma_f32_16x16x32_bf16 a[68:71], v[168:171], v[132:135], a[68:71]\n"
    "s_add_u32 m0, s51, s95\n"
    "v_add_f32 v118, v118, v37\n"
    "v_add_f32 v125, v125, v76\n"
    "v_mfma_f32_16x16x32_bf16 a[72:75], v[168:171], v[136:139], a[72:75]\n"
    "ds_read_b128 v[164:167], v201 offset:37888\n"
    "v_add_f32 v117, v117, v38\n"
    "v_mfma_f32_16x16x32_bf16 a[72:75], v[172:175], v[152:155], a[72:75]\n"
    "buffer_load_dwordx4 v202, s[32:35], s64 offen lds\n"
    "v_mfma_f32_16x16x32_bf16 a[76:79], v[172:175], v[156:159], a[76:79]\n"
    "v_exp_f32 v55, v55\n"
    "v_mfma_f32_16x16x32_bf16 a[76:79], v[168:171], v[140:143], a[76:79]\n"
    "s_waitcnt lgkmcnt(2)\n"
    "v_add_f32 v126, v126, v77\n"
    "v_add_f32 v118, v118, v39\n"
    "v_mfma_f32_16x16x32_bf16 a[92:95], v[176:179], v[140:143], a[92:95]\n"
    "ds_read_b128 v[168:171], v201 offset:38912\n"
    "v_add_f32 v125, v125, v78\n"
    "v_mfma_f32_16x16x32_bf16 a[92:95], v[180:183], v[156:159], a[92:95]\n"
    "buffer_load_dwordx4 v203, s[32:35], s64 offen offset:1024 lds\n"
    "v_mfma_f32_16x16x32_bf16 a[88:91], v[180:183], v[152:155], a[88:91]\n"
    "v_add_f32 v121, v121, v40\n"
    "v_add_f32 v126, v126, v79\n"
    "v_mfma_f32_16x16x32_bf16 a[88:91], v[176:179], v[136:139], a[88:91]\n"
    "v_exp_f32 v56, v56\n"
    "v_mfma_f32_16x16x32_bf16 a[84:87], v[176:179], v[132:135], a[84:87]\n"
    "ds_read_b128 v[172:175], v201 offset:39936\n"
    "v_add_f32 v122, v122, v41\n"
    "v_mfma_f32_16x16x32_bf16 a[84:87], v[180:183], v[148:151], a[84:87]\n"
    "buffer_load_dwordx4 v204, s[32:35], s64 offen offset:2048 lds\n"
    "v_mfma_f32_16x16x32_bf16 a[80:83], v[180:183], v[144:147], a[80:83]\n"
    "v_add_f32 v113, v113, v80\n"
    "v_add_f32 v121, v121, v42\n"
    "v_mfma_f32_16x16x32_bf16 a[80:83], v[176:179], v[128:131], a[80:83]\n"
    "s_waitcnt lgkmcnt(2)\n"
    "v_exp_f32 v57, v57\n"
    "v_mfma_f32_16x16x32_bf16 a[96:99], v[160:163], v[128:131], a[96:99]\n"
    "s_mov_b32 s95, s94\n"
    "ds_read_b128 v[176:179], v201 offset:40960\n"
    "v_add_f32 v114, v114, v81\n"
    "v_mfma_f32_16x16x32_bf16 a[96:99], v[164:167], v[144:147], a[96:99]\n"
    "s_sub_u32 s94, s94, 0x3000\n"
    "v_add_f32 v122, v122, v43\n"
    "v_add_f32 v113, v113, v82\n"
    "v_mfma_f32_16x16x32_bf16 a[100:103], v[164:167], v[148:151], a[100:103]\n"
    "s_min_u32 s94, s94, 0x6000\n"
    "v_add_f32 v125, v125, v44\n"
    "v_add_f32 v114, v114, v83\n"
    "v_mfma_f32_16x16x32_bf16 a[100:103], v[160:163], v[132:135], a[100:103]\n"
    "s_sub_u32 s93, s94, s95\n"
    "v_exp_f32 v58, v58\n"
    "v_mfma_f32_16x16x32_bf16 a[104:107], v[160:163], v[136:139], a[104:107]\n"
    "ds_read_b128 v[180:183], v201 offset:41984\n"
    "v_add_f32 v126, v126, v45\n"
    "v_mfma_f32_16x16x32_bf16 a[104:107], v[164:167], v[152:155], a[104:107]\n"
    "v_add_f32 v117, v117, v84\n"
    "v_add_f32 v125, v125, v46\n"
    "v_mfma_f32_16x16x32_bf16 a[108:111], v[164:167], v[156:159], a[108:111]\n"
    "v_exp_f32 v59, v59\n"
    "v_mfma_f32_16x16x32_bf16 a[108:111], v[160:163], v[140:143], a[108:111]\n"
    "s_waitcnt lgkmcnt(2)\n"
    "v_add_f32 v118, v118, v85\n"
    "v_add_f32 v126, v126, v47\n"
    "v_mfma_f32_16x16x32_bf16 a[124:127], v[168:171], v[140:143], a[124:127]\n"
    "ds_read_b128 v[160:163], v201 offset:43008\n"
    "v_add_f32 v117, v117, v86\n"
    "v_mfma_f32_16x16x32_bf16 a[124:127], v[172:175], v[156:159], a[124:127]\n"
    "v_add_f32 v113, v113, v48\n"
    "v_add_u32 v205, s93, v205\n"
    "v_mfma_f32_16x16x32_bf16 a[120:123], v[172:175], v[152:155], a[120:123]\n"
    "v_exp_f32 v60, v60\n"
    "v_mfma_f32_16x16x32_bf16 a[120:123], v[168:171], v[136:139], a[120:123]\n"
    "v_add_f32 v118, v118, v87\n"
    "v_add_f32 v114, v114, v49\n"
    "v_mfma_f32_16x16x32_bf16 a[116:119], v[168:171], v[132:135], a[116:119]\n"
    "ds_read_b128 v[164:167], v201 offset:44032\n"
    "v_add_u32 v206, s93, v206\n"
    "v_mfma_f32_16x16x32_bf16 a[116:119], v[172:175], v[148:151], a[116:119]\n"
    "v_add_f32 v121, v121, v88\n"
    "v_add_f32 v113, v113, v50\n"
    "v_mfma_f32_16x16x32_bf16 a[112:115], v[172:175], v[144:147], a[112:115]\n"
    "v_exp_f32 v61, v61\n"
    "v_mfma_f32_16x16x32_bf16 a[112:115], v[168:171], v[128:131], a[112:115]\n"
    "s_waitcnt lgkmcnt(2)\n"
    "v_add_u32 v220, 1, v220\n"
    "v_add_u32 v207, s93, v207\n"
    "v_mfma_f32_16x16x32_bf16 a[128:131], v[176:179], v[128:131], a[128:131]\n"
    "s_add_u32 s64, s64, 0xc000\n"
    "ds_read_b128 v[168:171], v201 offset:45056\n"
    "v_add_f32 v122, v122, v89\n"
    "v_mfma_f32_16x16x32_bf16 a[128:131], v[180:183], v[144:147], a[128:131]\n"
    "s_add_u32 s62, s62, 0x6000\n"
    "v_add_f32 v114, v114, v51\n"
    "v_add_f32 v121, v121, v90\n"
    "v_mfma_f32_16x16x32_bf16 a[132:135], v[180:183], v[148:151], a[132:135]\n"
    "s_add_u32 s63, s63, 0x6000\n"
    "v_add_f32 v117, v117, v52\n"
    "v_add_f32 v122, v122, v91\n"
    "v_mfma_f32_16x16x32_bf16 a[132:135], v[176:179], v[132:135], a[132:135]\n"
    "s_add_u32 s60, s60, 0x6000\n"
    "v_exp_f32 v62, v62\n"
    "v_mfma_f32_16x16x32_bf16 a[136:139], v[176:179], v[136:139], a[136:139]\n"
    "s_add_u32 s61, s61, 0x6000\n"
    "ds_read_b128 v[172:175], v201 offset:46080\n"
    "v_add_f32 v118, v118, v53\n"
    "v_mfma_f32_16x16x32_bf16 a[136:139], v[180:183], v[152:155], a[136:139]\n"
    "s_add_u32 s66, s66, s74\n"
    "v_add_f32 v125, v125, v92\n"
    "v_add_f32 v117, v117, v54\n"
    "v_mfma_f32_16x16x32_bf16 a[140:143], v[180:183], v[156:159], a[140:143]\n"
    "s_min_u32 s66, s66, 33\n"
    "v_exp_f32 v63, v63\n"
    "v_mfma_f32_16x16x32_bf16 a[140:143], v[176:179], v[140:143], a[140:143]\n"
    "s_waitcnt lgkmcnt(2)\n"
    "v_add_f32 v126, v126, v93\n"
    "v_add_f32 v118, v118, v55\n"
    "v_mfma_f32_16x16x32_bf16 a[156:159], v[160:163], v[140:143], a[156:159]\n"
    "ds_read_b128 v[176:179], v201 offset:47104\n"
    "v_add_f32 v125, v125, v94\n"
    "v_mfma_f32_16x16x32_bf16 a[156:159], v[164:167], v[156:159], a[156:159]\n"
    "s_waitcnt vmcnt(3)\n"
    "ds_read_b128 v[180:183], v201 offset:48128\n"
    "v_add_f32 v121, v121, v56\n"
    "v_mfma_f32_16x16x32_bf16 a[152:155], v[164:167], v[152:155], a[152:155]\n"
    "v_cmp_ne_u32 s[80:81], s67, v219\n"
    "v_add_f32 v126, v126, v95\n"
    "v_mfma_f32_16x16x32_bf16 a[152:155], v[160:163], v[136:139], a[152:155]\n"
    "v_cmp_gt_u32 vcc, s66, v218\n"
    "v_add_f32 v122, v122, v57\n"
    "v_mfma_f32_16x16x32_bf16 a[148:151], v[160:163], v[132:135], a[148:151]\n"
    "v_mfma_f32_16x16x32_bf16 a[148:151], v[164:167], v[148:151], a[148:151]\n"
    "s_or_b64 vcc, vcc, s[80:81]\n"
    "s_cbranch_vccnz LSPRE3\n"
    "LSPB3:\n"
    "v_mfma_f32_16x16x32_bf16 a[144:147], v[164:167], v[144:147], a[144:147]\n"
    "v_mfma_f32_16x16x32_bf16 a[144:147], v[160:163], v[128:131], a[144:147]\n"
    "s_waitcnt lgkmcnt(0)\n"
    "v_mfma_f32_16x16x32_bf16 a[160:163], v[168:171], v[128:131], a[160:163]\n"
    "s_barrier\n"
    "LMID:\n"
    "v_mfma_f32_16x16x32_bf16 a[160:163], v[172:175], v[144:147], a[160:163]\n"
    "buffer_store_dwordx2 v[220:221], v214, s[44:47], 0 offen sc1\n"
    "v_mfma_f32_16x16x32_bf16 a[164:167], v[172:175], v[148:151], a[164:167]\n"
    "ds_read_b128 v[160:163], v200 offset:24576\n"
    "ds_read_u16_d16_hi v223, v209 offset:6336\n"
    "v_mfma_f32_16x16x32_bf16 a[164:167], v[168:171], v[132:135], a[164:167]\n"
    "ds_read_u16_d16_hi v222, v209 offset:192\n"
    "v_add_f32 v121, v121, v58\n"
    "v_mfma_f32_16x16x32_bf16 a[168:171], v[168:171], v[136:139], a[168:171]\n"
    "ds_read_b128 v[164:167], v200 offset:25600\n"
    "ds_read_b128 v[232:235], v205 offset:24576\n"
    "v_mfma_f32_16x16x32_bf16 a[168:171], v[172:175], v[152:155], a[168:171]\n"
    "v_add_f32 v122, v122, v59\n"
    "ds_read_b128 v[236:239], v206 offset:24576\n"
    "v_mfma_f32_16x16x32_bf16 a[172:175], v[172:175], v[156:159], a[172:175]\n"
    "v_add_f32 v125, v125, v60\n"
    "v_add_f32 v126, v126, v61\n"
    "v_mfma_f32_16x16x32_bf16 a[172:175], v[168:171], v[140:143], a[172:175]\n"
    "v_add_f32 v125, v125, v62\n"
    "v_add_f32 v126, v126, v63\n"
    "v_mfma_f32_16x16x32_bf16 a[188:191], v[176:179], v[140:143], a[188:191]\n"
    "ds_read_b128 v[168:171], v200 offset:26624\n"
    "ds_read_b64 v[228:229], v210 offset:64\n"
    "v_mfma_f32_16x16x32_bf16 a[188:191], v[180:183], v[156:159], a[188:191]\n"
    "ds_read2st64_b32 v[240:241], v207 offset0:96 offset1:99\n"
    "ds_read2st64_b32 v[242:243], v207 offset0:102 offset1:105\n"
    "v_mfma_f32_16x16x32_bf16 a[184:187], v[176:179], v[136:139], a[184:187]\n"
    "ds_read2st64_b32 v[244:245], v207 offset0:108 offset1:111\n"
    "ds_read2st64_b32 v[246:247], v207 offset0:114 offset1:117\n"
    "v_mfma_f32_16x16x32_bf16 a[184:187], v[180:183], v[152:155], a[184:187]\n"
    "v_cvt_pk_bf16_f32 v140, v44, v45\n"
    "v_cvt_pk_bf16_f32 v141, v46, v47\n"
    "v_mfma_f32_16x16x32_bf16 a[180:183], v[176:179], v[132:135], a[180:183]\n"
    "s_add_u32 s59, s59, 1\n"
    "ds_read_b128 v[172:175], v200 offset:27648\n"
    "s_waitcnt lgkmcnt(7)\n"
    "v_mfma_f32_16x16x32_bf16 a[180:183], v[180:183], v[148:151], a[180:183]\n"
    "v_mul_f32 v226, s75, v223\n"
    "v_fma_f32 v227, -v222, v226, v217\n"
    "v_mfma_f32_16x16x32_bf16 a[176:179], v[176:179], v[128:131], a[176:179]\n"
    "s_cmp_lt_u32 s59, 15\n"
    "v_cvt_f32_i32 v232, v232\n"
    "v_cvt_f32_i32 v233, v233\n"
    "v_mfma_f32_16x16x32_bf16 a[176:179], v[180:183], v[144:147], a[176:179]\n"
    "s_cbranch_scc1 LLOOP\n"
    "LTAIL:\n"
    "s_waitcnt lgkmcnt(0)\n"
    "s_nop 1\n"
    "buffer_load_dword v251, v252, s[52:55], s91 offen\n"
    "v_mfma_f32_16x16x32_bf16 v[64:67], v[160:163], a[192:195], v[96:99]\n"
    "ds_read_b128 v[176:179], v200 offset:28672\n"
    "v_cvt_pk_bf16_f32 v128, v32, v33\n"
    "v_mfma_f32_16x16x32_bf16 v[64:67], v[164:167], a[196:199], v[64:67]\n"
    "s_add_u32 m0, s71, 0\n"
    "v_cvt_pk_bf16_f32 v129, v34, v35\n"
    "v_cvt_pk_bf16_f32 v132, v36, v37\n"
    "v_mfma_f32_16x16x32_bf16 v[68:71], v[164:167], a[220:223], v[100:103]\n"
    "buffer_load_dwordx4 v200, s[36:39], s60 offen sc1 lds\n"
    "v_mfma_f32_16x16x32_bf16 v[68:71], v[160:163], a[216:219], v[68:71]\n"
    "v_cvt_pk_bf16_f32 v133, v38, v39\n"
    "v_cvt_pk_bf16_f32 v136, v40, v41\n"
    "v_mfma_f32_16x16x32_bf16 v[72:75], v[160:163], a[240:243], v[104:107]\n"
    "ds_read_b128 v[180:183], v200 offset:29696\n"
    "v_cvt_pk_bf16_f32 v137, v42, v43\n"
    "v_mfma_f32_16x16x32_bf16 v[72:75], v[164:167], a[244:247], v[72:75]\n"
    "v_cvt_pk_bf16_f32 v130, v48, v49\n"
    "v_cvt_pk_bf16_f32 v131, v50, v51\n"
    "v_mfma_f32_16x16x32_bf16 v[76:79], v[164:167], v[12:15], v[108:111]\n"
    "buffer_load_dwordx4 v200, s[36:39], s60 offen offset:1024 sc1 lds\n"
    "v_mfma_f32_16x16x32_bf16 v[76:79], v[160:163], v[8:11], v[76:79]\n"
    "v_cvt_pk_bf16_f32 v134, v52, v53\n"
    "v_cvt_pk_bf16_f32 v135, v54, v55\n"
    "v_mfma_f32_16x16x32_bf16 v[76:79], v[168:171], v[16:19], v[76:79]\n"
    "ds_read_b128 v[160:163], v200 offset:34816\n"
    "v_cvt_pk_bf16_f32 v138, v56, v57\n"
    "v_mfma_f32_16x16x32_bf16 v[76:79], v[172:175], v[20:23], v[76:79]\n"
    "v_cvt_pk_bf16_f32 v139, v58, v59\n"
    "v_cvt_pk_bf16_f32 v142, v60, v61\n"
    "v_mfma_f32_16x16x32_bf16 v[72:75], v[172:175], a[252:255], v[72:75]\n"
    "buffer_load_dwordx4 v200, s[36:39], s60 offen offset:2048 sc1 lds\n"
    "v_mfma_f32_16x16x32_bf16 v[72:75], v[168:171], a[248:251], v[72:75]\n"
    "v_cvt_pk_bf16_f32 v143, v62, v63\n"
    "v_add_f32 v113, v113, v114\n"
    "v_mfma_f32_16x16x32_bf16 v[68:71], v[168:171], a[224:227], v[68:71]\n"
    "ds_read_b128 v[164:167], v200 offset:35840\n"
    "v_add_f32 v117, v117, v118\n"
    "v_mfma_f32_16x16x32_bf16 v[68:71], v[172:175], a[228:231], v[68:71]\n"
    "v_add_f32 v121, v121, v122\n"
    "v_add_f32 v125, v125, v126\n"
    "v_mfma_f32_16x16x32_bf16 v[64:67], v[172:175], a[204:207], v[64:67]\n"
    "buffer_load_dwordx4 v200, s[36:39], s60 offen offset:3072 sc1 lds\n"
    "v_mfma_f32_16x16x32_bf16 v[64:67], v[168:171], a[200:203], v[64:67]\n"
    "s_waitcnt lgkmcnt(2)\n"
    "v_mov_b32 v115, v113\n"
    "v_mov_b32 v119, v117\n"
    "v_mfma_f32_16x16x32_bf16 v[64:67], v[176:179], a[208:211], v[64:67]\n"
    "s_add_u32 s65, s60, 0x1000\n"
    "ds_read_b128 v[168:171], v200 offset:32768\n"
    "v_mov_b32 v123, v121\n"
    "v_mfma_f32_16x16x32_bf16 v[64:67], v[180:183], a[212:215], v[64:67]\n"
    "s_add_u32 m0, s71, 0x1000\n"
    "v_mov_b32 v127, v125\n"
    "v_permlane32_swap_b32 v113, v115\n"
    "v_mfma_f32_16x16x32_bf16 v[68:71], v[180:183], a[236:239], v[68:71]\n"
    "buffer_load_dwordx4 v200, s[36:39], s65 offen sc1 lds\n"
    "v_mfma_f32_16x16x32_bf16 v[68:71], v[176:179], a[232:235], v[68:71]\n"
    "v_permlane32_swap_b32 v117, v119\n"
    "v_permlane32_swap_b32 v121, v123\n"
    "v_mfma_f32_16x16x32_bf16 v[72:75], v[176:179], v[0:3], v[72:75]\n"
    "ds_read_b128 v[172:175], v200 offset:33792\n"
    "v_permlane32_swap_b32 v125, v127\n"
    "v_mfma_f32_16x16x32_bf16 v[72:75], v[180:183], v[4:7], v[72:75]\n"
    "v_add_f32 v113, v113, v115\n"
    "v_add_f32 v117, v117, v119\n"
    "v_mfma_f32_16x16x32_bf16 v[76:79], v[180:183], v[28:31], v[76:79]\n"
    "buffer_load_dwordx4 v200, s[36:39], s65 offen offset:1024 sc1 lds\n"
    "v_mfma_f32_16x16x32_bf16 v[76:79], v[176:179], v[24:27], v[76:79]\n"
    "s_waitcnt lgkmcnt(2)\n"
    "v_add_f32 v121, v121, v123\n"
    "v_add_f32 v125, v125, v127\n"
    "v_mfma_f32_16x16x32_bf16 v[92:95], v[160:163], v[24:27], v[108:111]\n"
    "ds_read_b128 v[176:179], v200 offset:30720\n"
    "v_mov_b32 v115, v113\n"
    "v_mfma_f32_16x16x32_bf16 v[92:95], v[164:167], v[28:31], v[92:95]\n"
    "s_add_u32 m0, s71, 0x12000\n"
    "v_exp_f32 v64, v64\n"
    "v_mfma_f32_16x16x32_bf16 v[88:91], v[164:167], v[4:7], v[104:107]\n"
    "buffer_load_dwordx4 v200, s[40:43], s61 offen sc1 lds\n"
    "v_mfma_f32_16x16x32_bf16 v[88:91], v[160:163], v[0:3], v[88:91]\n"
    "v_exp_f32 v65, v65\n"
    "v_mfma_f32_16x16x32_bf16 v[84:87], v[160:163], a[232:235], v[100:103]\n"
    "ds_read_b128 v[180:183], v200 offset:31744\n"
    "v_cvt_pk_bf16_f32 v144, v64, v65\n"
    "v_mfma_f32_16x16x32_bf16 v[84:87], v[164:167], a[236:239], v[84:87]\n"
    "v_exp_f32 v66, v66\n"
    "v_mfma_f32_16x16x32_bf16 v[80:83], v[164:167], a[212:215], v[96:99]\n"
    "buffer_load_dwordx4 v200, s[40:43], s61 offen offset:1024 sc1 lds\n"
    "v_mfma_f32_16x16x32_bf16 v[80:83], v[160:163], a[208:211], v[80:83]\n"
    "s_waitcnt lgkmcnt(2)\n"
    "v_exp_f32 v67, v67\n"
    "v_mfma_f32_16x16x32_bf16 v[80:83], v[168:171], a[200:203], v[80:83]\n"
    "ds_read_b128 v[160:163], v200 offset:36864\n"
    "v_cvt_pk_bf16_f32 v145, v66, v67\n"
    "v_mfma_f32_16x16x32_bf16 v[80:83], v[172:175], a[204:207], v[80:83]\n"
    "v_exp_f32 v68, v68\n"
    "v_mfma_f32_16x16x32_bf16 v[84:87], v[172:175], a[228:231], v[84:87]\n"
    "buffer_load_dwordx4 v200, s[40:43], s61 offen offset:2048 sc1 lds\n"
    "v_mfma_f32_16x16x32_bf16 v[84:87], v[168:171], a[224:227], v[84:87]\n"
    "v_exp_f32 v69, v69\n"
    "v_mfma_f32_16x16x32_bf16 v[88:91], v[168:171], a[248:251], v[88:91]\n"
    "ds_read_b128 v[164:167], v200 offset:37888\n"
    "v_cvt_pk_bf16_f32 v148, v68, v69\n"
    "v_mfma_f32_16x16x32_bf16 v[88:91], v[172:175], a[252:255], v[88:91]\n"
    "v_exp_f32 v70, v70\n"
    "v_mfma_f32_16x16x32_bf16 v[92:95], v[172:175], v[20:23], v[92:95]\n"
    "buffer_load_dwordx4 v200, s[40:43], s61 offen offset:3072 sc1 lds\n"
    "v_mfma_f32_16x16x32_bf16 v[92:95], v[168:171], v[16:19], v[92:95]\n"
    "s_waitcnt lgkmcnt(2)\n"
    "v_exp_f32 v71, v71\n"
    "v_mfma_f32_16x16x32_bf16 v[92:95], v[176:179], v[8:11], v[92:95]\n"
    "s_add_u32 s65, s61, 0x1000\n"
    "ds_read_b128 v[168:171], v200 offset:38912\n"
    "v_cvt_pk_bf16_f32 v149, v70, v71\n"
    "v_mfma_f32_16x16x32_bf16 v[92:95], v[180:183], v[12:15], v[92:95]\n"
    "s_add_u32 m0, s71, 0x13000\n"
    "v_exp_f32 v72, v72\n"
    "v_mfma_f32_16x16x32_bf16 v[88:91], v[180:183], a[244:247], v[88:91]\n"
    "buffer_load_dwordx4 v200, s[40:43], s65 offen sc1 lds\n"
    "v_mfma_f32_16x16x32_bf16 v[88:91], v[176:179], a[240:243], v[88:91]\n"
    "v_exp_f32 v73, v73\n"
    "v_mfma_f32_16x16x32_bf16 v[84:87], v[176:179], a[216:219], v[84:87]\n"
    "ds_read_b128 v[172:175], v200 offset:39936\n"
    "v_cvt_pk_bf16_f32 v152, v72, v73\n"
    "v_mfma_f32_16x16x32_bf16 v[84:87], v[180:183], a[220:223], v[84:87]\n"
    "v_exp_f32 v74, v74\n"
    "v_mfma_f32_16x16x32_bf16 v[80:83], v[180:183], a[196:199], v[80:83]\n"
    "buffer_load_dwordx4 v200, s[40:43], s65 offen offset:1024 sc1 lds\n"
    "v_mfma_f32_16x16x32_bf16 v[80:83], v[176:179], a[192:195], v[80:83]\n"
    "s_waitcnt lgkmcnt(2)\n"
    "v_exp_f32 v75, v75\n"
    "v_mfma_f32_16x16x32_bf16 v[32:35], v[160:163], a[192:195], v[96:99]\n"
    "ds_read_b128 v[176:179], v200 offset:40960\n"
    "v_cvt_pk_bf16_f32 v153, v74, v75\n"
    "v_mfma_f32_16x16x32_bf16 v[32:35], v[164:167], a[196:199], v[32:35]\n"
    "v_exp_f32 v76, v76\n"
    "v_mfma_f32_16x16x32_bf16 v[36:39], v[164:167], a[220:223], v[100:103]\n"
    "v_exp_f32 v77, v77\n"
    "v_mfma_f32_16x16x32_bf16 v[36:39], v[160:163], a[216:219], v[36:39]\n"
    "v_exp_f32 v78, v78\n"
    "v_mfma_f32_16x16x32_bf16 v[40:43], v[160:163], a[240:243], v[104:107]\n"
    "ds_read_b128 v[180:183], v200 offset:41984\n"
    "v_cvt_pk_bf16_f32 v156, v76, v77\n"
    "v_mfma_f32_16x16x32_bf16 v[40:43], v[164:167], a[244:247], v[40:43]\n"
    "v_exp_f32 v79, v79\n"
    "v_mfma_f32_16x16x32_bf16 v[44:47], v[164:167], v[12:15], v[108:111]\n"
    "v_exp_f32 v80, v80\n"
    "v_mfma_f32_16x16x32_bf16 v[44:47], v[160:163], v[8:11], v[44:47]\n"
    "s_waitcnt lgkmcnt(2)\n"
    "v_exp_f32 v81, v81\n"
    "v_mfma_f32_16x16x32_bf16 v[44:47], v[168:171], v[16:19], v[44:47]\n"
    "ds_read_b128 v[160:163], v200 offset:47104\n"
    "v_cvt_pk_bf16_f32 v157, v78, v79\n"
    "v_mfma_f32_16x16x32_bf16 v[44:47], v[172:175], v[20:23], v[44:47]\n"
    "v_exp_f32 v82, v82\n"
    "v_mfma_f32_16x16x32_bf16 v[40:43], v[172:175], a[252:255], v[40:43]\n"
    "v_exp_f32 v83, v83\n"
    "v_mfma_f32_16x16x32_bf16 v[40:43], v[168:171], a[248:251], v[40:43]\n"
    "v_exp_f32 v84, v84\n"
    "v_mfma_f32_16x16x32_bf16 v[36:39], v[168:171], a[224:227], v[36:39]\n"
    "ds_read_b128 v[164:167], v200 offset:48128\n"
    "v_cvt_pk_bf16_f32 v146, v80, v81\n"
    "v_mfma_f32_16x16x32_bf16 v[36:39], v[172:175], a[228:231], v[36:39]\n"
    "v_exp_f32 v85, v85\n"
    "v_mfma_f32_16x16x32_bf16 v[32:35], v[172:175], a[204:207], v[32:35]\n"
    "v_exp_f32 v86, v86\n"
    "v_mfma_f32_16x16x32_bf16 v[32:35], v[168:171], a[200:203], v[32:35]\n"
    "s_waitcnt lgkmcnt(2)\n"
    "v_exp_f32 v87, v87\n"
    "v_mfma_f32_16x16x32_bf16 v[32:35], v[176:179], a[208:211], v[32:35]\n"
    "ds_read_b128 v[168:171], v200 offset:45056\n"
    "v_cvt_pk_bf16_f32 v147, v82, v83\n"
    "v_mfma_f32_16x16x32_bf16 v[32:35], v[180:183], a[212:215], v[32:35]\n"
    "v_exp_f32 v88, v88\n"
    "v_mfma_f32_16x16x32_bf16 v[36:39], v[180:183], a[236:239], v[36:39]\n"
    "v_exp_f32 v89, v89\n"
    "v_mfma_f32_16x16x32_bf16 v[36:39], v[176:179], a[232:235], v[36:39]\n"
    "v_exp_f32 v90, v90\n"
    "v_mfma_f32_16x16x32_bf16 v[40:43], v[176:179], v[0:3], v[40:43]\n"
    "ds_read_b128 v[172:175], v200 offset:46080\n"
    "v_cvt_pk_bf16_f32 v150, v84, v85\n"
    "v_mfma_f32_16x16x32_bf16 v[40:43], v[180:183], v[4:7], v[40:43]\n"
    "v_exp_f32 v91, v91\n"
    "v_mfma_f32_16x16x32_bf16 v[44:47], v[180:183], v[28:31], v[44:47]\n"
    "v_exp_f32 v92, v92\n"
    "v_mfma_f32_16x16x32_bf16 v[44:47], v[176:179], v[24:27], v[44:47]\n"
    "s_waitcnt lgkmcnt(2)\n"
    "v_exp_f32 v93, v93\n"
    "v_mfma_f32_16x16x32_bf16 v[60:63], v[160:163], v[24:27], v[108:111]\n"
    "ds_read_b128 v[176:179], v200 offset:43008\n"
    "v_cvt_pk_bf16_f32 v151, v86, v87\n"
    "v_mfma_f32_16x16x32_bf16 v[60:63], v[164:167], v[28:31], v[60:63]\n"
    "v_exp_f32 v94, v94\n"
    "v_mfma_f32_16x16x32_bf16 v[56:59], v[164:167], v[4:7], v[104:107]\n"
    "v_exp_f32 v95, v95\n"
    "v_mfma_f32_16x16x32_bf16 v[56:59], v[160:163], v[0:3], v[56:59]\n"
    "v_exp_f32 v32, v32\n"
    "v_mfma_f32_16x16x32_bf16 v[52:55], v[160:163], a[232:235], v[100:103]\n"
    "ds_read_b128 v[180:183], v200 offset:44032\n"
    "v_cvt_pk_bf16_f32 v154, v88, v89\n"
    "v_mfma_f32_16x16x32_bf16 v[52:55], v[164:167], a[236:239], v[52:55]\n"
    "v_exp_f32 v33, v33\n"
    "v_mfma_f32_16x16x32_bf16 v[48:51], v[164:167], a[212:215], v[96:99]\n"
    "s_waitcnt vmcnt(6)\n"
    "v_exp_f32 v34, v34\n"
    "v_mfma_f32_16x16x32_bf16 v[48:51], v[160:163], a[208:211], v[48:51]\n"
    "s_waitcnt lgkmcnt(2)\n"
    "v_exp_f32 v35, v35\n"
    "v_mfma_f32_16x16x32_bf16 v[48:51], v[168:171], a[200:203], v[48:51]\n"
    "s_barrier\n"
    "v_exp_f32 v36, v36\n"
    "v_mfma_f32_16x16x32_bf16 v[48:51], v[172:175], a[204:207], v[48:51]\n"
    "ds_read_b128 v[160:163], v200\n"
    "v_cvt_pk_bf16_f32 v155, v90, v91\n"
    "v_mfma_f32_16x16x32_bf16 v[52:55], v[172:175], a[228:231], v[52:55]\n"
    "v_exp_f32 v37, v37\n"
    "v_mfma_f32_16x16x32_bf16 v[52:55], v[168:171], a[224:227], v[52:55]\n"
    "v_exp_f32 v38, v38\n"
    "v_mfma_f32_16x16x32_bf16 v[56:59], v[168:171], a[248:251], v[56:59]\n"
    "ds_read_b128 v[164:167], v200 offset:1024\n"
    "v_cvt_pk_bf16_f32 v158, v92, v93\n"
    "v_mfma_f32_16x16x32_bf16 v[56:59], v[172:175], a[252:255], v[56:59]\n"
    "buffer_load_dwordx2 v[218:219], v213, s[44:47], 0 offen sc1\n"
    "v_mfma_f32_16x16x32_bf16 v[60:63], v[172:175], v[20:23], v[60:63]\n"
    "v_exp_f32 v39, v39\n"
    "v_mfma_f32_16x16x32_bf16 v[60:63], v[168:171], v[16:19], v[60:63]\n"
    "s_waitcnt lgkmcnt(2)\n"
    "v_exp_f32 v40, v40\n"
    "v_mfma_f32_16x16x32_bf16 v[60:63], v[176:179], v[8:11], v[60:63]\n"
    "ds_read_b128 v[168:171], v200 offset:2048\n"
    "v_cvt_pk_bf16_f32 v159, v94, v95\n"
    "v_mfma_f32_16x16x32_bf16 v[60:63], v[180:183], v[12:15], v[60:63]\n"
    "v_exp_f32 v41, v41\n"
    "v_mfma_f32_16x16x32_bf16 v[56:59], v[180:183], a[244:247], v[56:59]\n"
    "v_exp_f32 v42, v42\n"
    "v_mfma_f32_16x16x32_bf16 v[56:59], v[176:179], a[240:243], v[56:59]\n"
    "v_exp_f32 v43, v43\n"
    "v_mfma_f32_16x16x32_bf16 v[52:55], v[176:179], a[216:219], v[52:55]\n"
    "ds_read_b128 v[172:175], v200 offset:3072\n"
    "v_cvt_pk_bf16_f32 v32, v32, v33\n"
    "v_mfma_f32_16x16x32_bf16 v[52:55], v[180:183], a[220:223], v[52:55]\n"
    "v_exp_f32 v44, v44\n"
    "v_mfma_f32_16x16x32_bf16 v[48:51], v[180:183], a[196:199], v[48:51]\n"
    "v_exp_f32 v45, v45\n"
    "v_mfma_f32_16x16x32_bf16 v[48:51], v[176:179], a[192:195], v[48:51]\n"
    "s_waitcnt lgkmcnt(2)\n"
    "v_exp_f32 v46, v46\n"
    "v_mfma_f32_16x16x32_bf16 v[64:67], v[160:163], a[192:195], v[96:99]\n"
    "ds_read_b128 v[176:179], v200 offset:4096\n"
    "v_cvt_pk_bf16_f32 v33, v34, v35\n"
    "v_mfma_f32_16x16x32_bf16 v[64:67], v[164:167], a[196:199], v[64:67]\n"
    "v_exp_f32 v47, v47\n"
    "v_mfma_f32_16x16x32_bf16 v[68:71], v[164:167], a[220:223], v[100:103]\n"
    "v_cvt_pk_bf16_f32 v36, v36, v37\n"
    "v_cvt_pk_bf16_f32 v37, v38, v39\n"
    "v_mfma_f32_16x16x32_bf16 v[68:71], v[160:163], a[216:219], v[68:71]\n"
    "v_cvt_pk_bf16_f32 v40, v40, v41\n"
    "v_cvt_pk_bf16_f32 v41, v42, v43\n"
    "v_mfma_f32_16x16x32_bf16 v[72:75], v[160:163], a[240:243], v[104:107]\n"
    "ds_read_b128 v[180:183], v200 offset:5120\n"
    "v_cvt_pk_bf16_f32 v44, v44, v45\n"
    "v_mfma_f32_16x16x32_bf16 v[72:75], v[164:167], a[244:247], v[72:75]\n"
    "v_cvt_pk_bf16_f32 v45, v46, v47\n"
    "v_mov_b32 v119, v117\n"
    "v_mfma_f32_16x16x32_bf16 v[76:79], v[164:167], v[12:15], v[108:111]\n"
    "v_exp_f32 v48, v48\n"
    "v_mfma_f32_16x16x32_bf16 v[76:79], v[160:163], v[8:11], v[76:79]\n"
    "s_waitcnt lgkmcnt(2)\n"
    "v_exp_f32 v49, v49\n"
    "v_mfma_f32_16x16x32_bf16 v[76:79], v[168:171], v[16:19], v[76:79]\n"
    "ds_read_b128 v[160:163], v200 offset:10240\n"
    "v_cvt_pk_bf16_f32 v34, v48, v49\n"
    "v_mfma_f32_16x16x32_bf16 v[76:79], v[172:175], v[20:23], v[76:79]\n"
    "v_exp_f32 v50, v50\n"
    "v_mfma_f32_16x16x32_bf16 v[72:75], v[172:175], a[252:255], v[72:75]\n"
    "v_exp_f32 v51, v51\n"
    "v_mfma_f32_16x16x32_bf16 v[72:75], v[168:171], a[248:251], v[72:75]\n"
    "v_exp_f32 v52, v52\n"
    "v_mfma_f32_16x16x32_bf16 v[68:71], v[168:171], a[224:227], v[68:71]\n"
    "ds_read_b128 v[164:167], v200 offset:11264\n"
    "v_cvt_pk_bf16_f32 v35, v50, v51\n"
    "v_mfma_f32_16x16x32_bf16 v[68:71], v[172:175], a[228:231], v[68:71]\n"
    "v_exp_f32 v53, v53\n"
    "v_mfma_f32_16x16x32_bf16 v[64:67], v[172:175], a[204:207], v[64:67]\n"
    "v_exp_f32 v54, v54\n"
    "v_mfma_f32_16x16x32_bf16 v[64:67], v[168:171], a[200:203], v[64:67]\n"
    "s_waitcnt lgkmcnt(2)\n"
    "v_exp_f32 v55, v55\n"
    "v_mfma_f32_16x16x32_bf16 v[64:67], v[176:179], a[208:211], v[64:67]\n"
    "s_add_u32 s66, s66, s74\n"
    "ds_read_b128 v[168:171], v200 offset:8192\n"
    "v_cvt_pk_bf16_f32 v38, v52, v53\n"
    "v_mfma_f32_16x16x32_bf16 v[64:67], v[180:183], a[212:215], v[64:67]\n"
    "s_add_u32 s60, s72, 0xbd000\n"
    "v_exp_f32 v56, v56\n"
    "v_mfma_f32_16x16x32_bf16 v[68:71], v[180:183], a[236:239], v[68:71]\n"
    "s_add_u32 s61, s71, 0xba000\n"
    "v_exp_f32 v57, v57\n"
    "v_mfma_f32_16x16x32_bf16 v[68:71], v[176:179], a[232:235], v[68:71]\n"
    "v_exp_f32 v58, v58\n"
    "v_mfma_f32_16x16x32_bf16 v[72:75], v[176:179], v[0:3], v[72:75]\n"
    "ds_read_b128 v[172:175], v200 offset:9216\n"
    "v_cvt_pk_bf16_f32 v39, v54, v55\n"
    "v_mfma_f32_16x16x32_bf16 v[72:75], v[180:183], v[4:7], v[72:75]\n"
    "v_exp_f32 v59, v59\n"
    "v_mfma_f32_16x16x32_bf16 v[76:79], v[180:183], v[28:31], v[76:79]\n"
    "s_waitcnt vmcnt(0)\n"
    "v_cmp_ne_u32 s[80:81], s67, v219\n"
    "v_cvt_pk_bf16_f32 v42, v56, v57\n"
    "v_mfma_f32_16x16x32_bf16 v[76:79], v[176:179], v[24:27], v[76:79]\n"
    "s_waitcnt lgkmcnt(2)\n"
    "v_cmp_gt_u32 vcc, s66, v218\n"
    "v_cvt_pk_bf16_f32 v43, v58, v59\n"
    "v_mfma_f32_16x16x32_bf16 v[92:95], v[160:163], v[24:27], v[108:111]\n"
    "ds_read_b128 v[176:179], v200 offset:6144\n"
    "v_mov_b32 v123, v121\n"
    "v_mfma_f32_16x16x32_bf16 v[92:95], v[164:167], v[28:31], v[92:95]\n"
    "s_or_b64 vcc, vcc, s[80:81]\n"
    "v_exp_f32 v64, v64\n"
    "v_mfma_f32_16x16x32_bf16 v[88:91], v[164:167], v[4:7], v[104:107]\n"
    "s_cbranch_vccnz LSPRE5\n"
    "LSPB5:\n"
    "v_exp_f32 v65, v65\n"
    "v_mfma_f32_16x16x32_bf16 v[88:91], v[160:163], v[0:3], v[88:91]\n"
    "v_exp_f32 v66, v66\n"
    "v_mfma_f32_16x16x32_bf16 v[84:87], v[160:163], a[232:235], v[100:103]\n"
    "s_barrier\n"
    "ds_read_b128 v[180:183], v200 offset:7168\n"
    "v_cvt_pk_bf16_f32 v64, v64, v65\n"
    "v_mfma_f32_16x16x32_bf16 v[84:87], v[164:167], a[236:239], v[84:87]\n"
    "s_add_u32 m0, s72, 0x6000\n"
    "v_exp_f32 v67, v67\n"
    "v_mfma_f32_16x16x32_bf16 v[80:83], v[164:167], a[212:215], v[96:99]\n"
    "buffer_load_dwordx4 v200, s[36:39], s60 offen sc1 lds\n"
    "v_mfma_f32_16x16x32_bf16 v[80:83], v[160:163], a[208:211], v[80:83]\n"
    "s_waitcnt lgkmcnt(2)\n"
    "v_exp_f32 v68, v68\n"
    "v_mfma_f32_16x16x32_bf16 v[80:83], v[168:171], a[200:203], v[80:83]\n"
    "ds_read_b128 v[160:163], v200 offset:12288\n"
    "v_cvt_pk_bf16_f32 v65, v66, v67\n"
    "v_mfma_f32_16x16x32_bf16 v[80:83], v[172:175], a[204:207], v[80:83]\n"
    "buffer_load_dwordx4 v200, s[36:39], s60 offen offset:1024 sc1 lds\n"
    "v_mfma_f32_16x16x32_bf16 v[84:87], v[172:175], a[228:231], v[84:87]\n"
    "v_exp_f32 v69, v69\n"
    "v_mfma_f32_16x16x32_bf16 v[84:87], v[168:171], a[224:227], v[84:87]\n"
    "buffer_load_dwordx4 v200, s[36:39], s60 offen offset:2048 sc1 lds\n"
    "v_mfma_f32_16x16x32_bf16 v[88:91], v[168:171], a[248:251], v[88:91]\n"
    "ds_read_b128 v[164:167], v200 offset:13312\n"
    "v_cvt_pk_bf16_f32 v68, v68, v69\n"
    "v_mfma_f32_16x16x32_bf16 v[88:91], v[172:175], a[252:255], v[88:91]\n"
    "v_exp_f32 v60, v60\n"
    "v_mfma_f32_16x16x32_bf16 v[92:95], v[172:175], v[20:23], v[92:95]\n"
    "s_add_u32 m0, s71, 0x18000\n"
    "v_exp_f32 v70, v70\n"
    "v_mfma_f32_16x16x32_bf16 v[92:95], v[168:171], v[16:19], v[92:95]\n"
    "s_waitcnt lgkmcnt(2)\n"
    "buffer_load_dwordx4 v200, s[40:43], s61 offen sc1 lds\n"
    "v_mfma_f32_16x16x32_bf16 v[92:95], v[176:179], v[8:11], v[92:95]\n"
    "ds_read_b128 v[168:171], v200 offset:14336\n"
    "v_mov_b32 v127, v125\n"
    "v_mfma_f32_16x16x32_bf16 v[92:95], v[180:183], v[12:15], v[92:95]\n"
    "v_exp_f32 v61, v61\n"
    "v_mfma_f32_16x16x32_bf16 v[88:91], v[180:183], a[244:247], v[88:91]\n"
    "v_exp_f32 v71, v71\n"
    "v_mfma_f32_16x16x32_bf16 v[88:91], v[176:179], a[240:243], v[88:91]\n"
    "v_exp_f32 v62, v62\n"
    "v_mfma_f32_16x16x32_bf16 v[84:87], v[176:179], a[216:219], v[84:87]\n"
    "ds_read_b128 v[172:175], v200 offset:15360\n"
    "v_cvt_pk_bf16_f32 v46, v60, v61\n"
    "v_mfma_f32_16x16x32_bf16 v[84:87], v[180:183], a[220:223], v[84:87]\n"
    "v_exp_f32 v72, v72\n"
    "v_mfma_f32_16x16x32_bf16 v[80:83], v[180:183], a[196:199], v[80:83]\n"
    "v_exp_f32 v63, v63\n"
    "v_mfma_f32_16x16x32_bf16 v[80:83], v[176:179], a[192:195], v[80:83]\n"
    "s_waitcnt lgkmcnt(2)\n"
    "buffer_load_dwordx4 v200, s[40:43], s61 offen offset:1024 sc1 lds\n"
    "v_mfma_f32_16x16x32_bf16 v[232:235], v[160:163], a[192:195], v[96:99]\n"
    "ds_read_b128 v[176:179], v200 offset:16384\n"
    "v_cvt_pk_bf16_f32 v47, v62, v63\n"
    "v_mfma_f32_16x16x32_bf16 v[232:235], v[164:167], a[196:199], v[232:235]\n"
    "v_exp_f32 v73, v73\n"
    "v_mfma_f32_16x16x32_bf16 v[236:239], v[164:167], a[220:223], v[100:103]\n"
    "v_exp_f32 v74, v74\n"
    "v_mfma_f32_16x16x32_bf16 v[236:239], v[160:163], a[216:219], v[236:239]\n"
    "v_exp_f32 v75, v75\n"
    "v_mfma_f32_16x16x32_bf16 v[240:243], v[160:163], a[240:243], v[104:107]\n"
    "ds_read_b128 v[180:183], v200 offset:17408\n"
    "v_cvt_pk_bf16_f32 v69, v70, v71\n"
    "v_mfma_f32_16x16x32_bf16 v[240:243], v[164:167], a[244:247], v[240:243]\n"
    "v_exp_f32 v76, v76\n"
    "v_mfma_f32_16x16x32_bf16 v[244:247], v[164:167], v[12:15], v[108:111]\n"
    "v_exp_f32 v77, v77\n"
    "v_mfma_f32_16x16x32_bf16 v[244:247], v[160:163], v[8:11], v[244:247]\n"
    "s_waitcnt lgkmcnt(2)\n"
    "buffer_load_dwordx4 v200, s[40:43], s61 offen offset:2048 sc1 lds\n"
    "v_mfma_f32_16x16x32_bf16 v[244:247], v[168:171], v[16:19], v[244:247]\n"
    "ds_read_b128 v[160:163], v200 offset:22528\n"
    "v_cvt_pk_bf16_f32 v72, v72, v73\n"
    "v_mfma_f32_16x16x32_bf16 v[244:247], v[172:175], v[20:23], v[244:247]\n"
    "v_exp_f32 v78, v78\n"
    "v_mfma_f32_16x16x32_bf16 v[240:243], v[172:175], a[252:255], v[240:243]\n"
    "v_exp_f32 v79, v79\n"
    "v_mfma_f32_16x16x32_bf16 v[240:243], v[168:171], a[248:251], v[240:243]\n"
    "v_exp_f32 v80, v80\n"
    "v_mfma_f32_16x16x32_bf16 v[236:239], v[168:171], a[224:227], v[236:239]\n"
    "ds_read_b128 v[164:167], v200 offset:23552\n"
    "v_cvt_pk_bf16_f32 v73, v74, v75\n"
    "v_mfma_f32_16x16x32_bf16 v[236:239], v[172:175], a[228:231], v[236:239]\n"
    "v_exp_f32 v81, v81\n"
    "v_mfma_f32_16x16x32_bf16 v[232:235], v[172:175], a[204:207], v[232:235]\n"
    "v_exp_f32 v82, v82\n"
    "v_mfma_f32_16x16x32_bf16 v[232:235], v[168:171], a[200:203], v[232:235]\n"
    "s_waitcnt lgkmcnt(2)\n"
    "buffer_load_dwordx4 v200, s[40:43], s61 offen offset:3072 sc1 lds\n"
    "v_mfma_f32_16x16x32_bf16 v[232:235], v[176:179], a[208:211], v[232:235]\n"
    "ds_read_b128 v[168:171], v200 offset:20480\n"
    "v_cvt_pk_bf16_f32 v76, v76, v77\n"
    "v_mfma_f32_16x16x32_bf16 v[232:235], v[180:183], a[212:215], v[232:235]\n"
    "v_exp_f32 v83, v83\n"
    "v_mfma_f32_16x16x32_bf16 v[236:239], v[180:183], a[236:239], v[236:239]\n"
    "v_exp_f32 v84, v84\n"
    "v_mfma_f32_16x16x32_bf16 v[236:239], v[176:179], a[232:235], v[236:239]\n"
    "v_exp_f32 v85, v85\n"
    "v_mfma_f32_16x16x32_bf16 v[240:243], v[176:179], v[0:3], v[240:243]\n"
    "ds_read_b128 v[172:175], v200 offset:21504\n"
    "v_cvt_pk_bf16_f32 v77, v78, v79\n"
    "v_mfma_f32_16x16x32_bf16 v[240:243], v[180:183], v[4:7], v[240:243]\n"
    "s_add_u32 s65, s61, 0x1000\n"
    "v_exp_f32 v86, v86\n"
    "v_mfma_f32_16x16x32_bf16 v[244:247], v[180:183], v[28:31], v[244:247]\n"
    "s_add_u32 m0, s71, 0x19000\n"
    "v_exp_f32 v87, v87\n"
    "v_mfma_f32_16x16x32_bf16 v[244:247], v[176:179], v[24:27], v[244:247]\n"
    "s_waitcnt lgkmcnt(2)\n"
    "buffer_load_dwordx4 v200, s[40:43], s65 offen sc1 lds\n"
    "v_mfma_f32_16x16x32_bf16 v[60:63], v[160:163], v[24:27], v[108:111]\n"
    "ds_read_b128 v[176:179], v200 offset:18432\n"
    "v_cvt_pk_bf16_f32 v66, v80, v81\n"
    "v_mfma_f32_16x16x32_bf16 v[60:63], v[164:167], v[28:31], v[60:63]\n"
    "v_exp_f32 v88, v88\n"
    "v_mfma_f32_16x16x32_bf16 v[56:59], v[164:167], v[4:7], v[104:107]\n"
    "v_exp_f32 v89, v89\n"
    "v_mfma_f32_16x16x32_bf16 v[56:59], v[160:163], v[0:3], v[56:59]\n"
    "v_exp_f32 v90, v90\n"
    "v_mfma_f32_16x16x32_bf16 v[52:55], v[160:163], a[232:235], v[100:103]\n"
    "ds_read_b128 v[180:183], v200 offset:19456\n"
    "v_cvt_pk_bf16_f32 v67, v82, v83\n"
    "v_mfma_f32_16x16x32_bf16 v[52:55], v[164:167], a[236:239], v[52:55]\n"
    "v_exp_f32 v91, v91\n"
    "v_mfma_f32_16x16x32_bf16 v[48:51], v[164:167], a[212:215], v[96:99]\n"
    "s_waitcnt vmcnt(5)\n"
    "v_exp_f32 v92, v92\n"
    "v_mfma_f32_16x16x32_bf16 v[48:51], v[160:163], a[208:211], v[48:51]\n"
    "s_waitcnt lgkmcnt(2)\n"
    "buffer_load_dwordx4 v200, s[40:43], s65 offen offset:1024 sc1 lds\n"
    "v_mfma_f32_16x16x32_bf16 v[48:51], v[168:171], a[200:203], v[48:51]\n"
    "s_barrier\n"
    "v_exp_f32 v93, v93\n"
    "v_mfma_f32_16x16x32_bf16 v[48:51], v[172:175], a[204:207], v[48:51]\n"
    "ds_read_b128 v[160:163], v200 offset:24576\n"
    "v_cvt_pk_bf16_f32 v70, v84, v85\n"
    "v_mfma_f32_16x16x32_bf16 v[52:55], v[172:175], a[228:231], v[52:55]\n"
    "v_exp_f32 v94, v94\n"
    "v_mfma_f32_16x16x32_bf16 v[52:55], v[168:171], a[224:227], v[52:55]\n"
    "v_exp_f32 v95, v95\n"
    "v_mfma_f32_16x16x32_bf16 v[56:59], v[168:171], a[248:251], v[56:59]\n"
    "ds_read_b128 v[164:167], v200 offset:25600\n"
    "v_cvt_pk_bf16_f32 v71, v86, v87\n"
    "v_mfma_f32_16x16x32_bf16 v[56:59], v[172:175], a[252:255], v[56:59]\n"
    "v_exp_f32 v232, v232\n"
    "v_mfma_f32_16x16x32_bf16 v[60:63], v[172:175], v[20:23], v[60:63]\n"
    "v_exp_f32 v233, v233\n"
    "v_mfma_f32_16x16x32_bf16 v[60:63], v[168:171], v[16:19], v[60:63]\n"
    "s_waitcnt lgkmcnt(2)\n"
    "v_exp_f32 v234, v234\n"
    "v_mfma_f32_16x16x32_bf16 v[60:63], v[176:179], v[8:11], v[60:63]\n"
    "s_lshl_b32 s76, s56, 3\n"
    "ds_read_b128 v[168:171], v200 offset:26624\n"
    "v_cvt_pk_bf16_f32 v74, v88, v89\n"
    "v_mfma_f32_16x16x32_bf16 v[60:63], v[180:183], v[12:15], v[60:63]\n"
    "s_add_u32 s76, s76, s57\n"
    "v_exp_f32 v235, v235\n"
    "v_mfma_f32_16x16x32_bf16 v[56:59], v[180:183], a[244:247], v[56:59]\n"
    "s_mul_i32 s76, s76, 0x18000\n"
    "v_exp_f32 v236, v236\n"
    "v_mfma_f32_16x16x32_bf16 v[56:59], v[176:179], a[240:243], v[56:59]\n"
    "s_add_u32 s76, s76, s88\n"
    "v_exp_f32 v237, v237\n"
    "v_mfma_f32_16x16x32_bf16 v[52:55], v[176:179], a[216:219], v[52:55]\n"
    "s_add_u32 s28, s24, s76\n"
    "ds_read_b128 v[172:175], v200 offset:27648\n"
    "v_cvt_pk_bf16_f32 v75, v90, v91\n"
    "v_mfma_f32_16x16x32_bf16 v[52:55], v[180:183], a[220:223], v[52:55]\n"
    "s_addc_u32 s29, s25, 0\n"
    "v_exp_f32 v238, v238\n"
    "v_mfma_f32_16x16x32_bf16 v[48:51], v[180:183], a[196:199], v[48:51]\n"
    "s_and_b32 s29, s29, 0xffff\n"
    "v_exp_f32 v239, v239\n"
    "v_mfma_f32_16x16x32_bf16 v[48:51], v[176:179], a[192:195], v[48:51]\n"
    "s_waitcnt lgkmcnt(2)\n"
    "v_exp_f32 v240, v240\n"
    "v_mfma_f32_16x16x32_bf16 v[80:83], v[160:163], a[192:195], v[96:99]\n"
    "s_mov_b32 s30, 0x6000\n"
    "ds_read_b128 v[176:179], v200 offset:28672\n"
    "v_cvt_pk_bf16_f32 v78, v92, v93\n"
    "v_mfma_f32_16x16x32_bf16 v[80:83], v[164:167], a[196:199], v[80:83]\n"
    "s_mov_b32 s31, 0x20000\n"
    "v_cvt_pk_bf16_f32 v79, v94, v95\n"
    "v_permlane16_swap_b32 v113, v115\n"
    "v_mfma_f32_16x16x32_bf16 v[84:87], v[164:167], a[220:223], v[100:103]\n"
    "s_xor_b32 s94, s70, 0x80000000\n"
    "v_exp_f32 v241, v241\n"
    "v_mfma_f32_16x16x32_bf16 v[84:87], v[160:163], a[216:219], v[84:87]\n"
    "s_xor_b32 s95, s70, 0x80000000\n"
    "v_exp_f32 v242, v242\n"
    "v_mfma_f32_16x16x32_bf16 v[88:91], v[160:163], a[240:243], v[104:107]\n"
    "s_mov_b32 s62, 0x1800\n"
    "ds_read_b128 v[180:183], v200 offset:29696\n"
    "v_permlane16_swap_b32 v117, v119\n"
    "v_mfma_f32_16x16x32_bf16 v[88:91], v[164:167], a[244:247], v[88:91]\n"
    "s_mov_b32 s63, 0x3000\n"
    "v_exp_f32 v243, v243\n"
    "v_mfma_f32_16x16x32_bf16 v[92:95], v[164:167], v[12:15], v[108:111]\n"
    "s_mov_b32 s64, 0x4800\n"
    "v_exp_f32 v244, v244\n"
    "v_mfma_f32_16x16x32_bf16 v[92:95], v[160:163], v[8:11], v[92:95]\n"
    "s_waitcnt lgkmcnt(2)\n"
    "v_exp_f32 v245, v245\n"
    "v_mfma_f32_16x16x32_bf16 v[92:95], v[168:171], v[16:19], v[92:95]\n"
    "ds_read_b128 v[160:163], v200 offset:34816\n"
    "v_permlane16_swap_b32 v121, v123\n"
    "v_mfma_f32_16x16x32_bf16 v[92:95], v[172:175], v[20:23], v[92:95]\n"
    "v_exp_f32 v246, v246\n"
    "v_mfma_f32_16x16x32_bf16 v[88:91], v[172:175], a[252:255], v[88:91]\n"
    "v_exp_f32 v247, v247\n"
    "v_mfma_f32_16x16x32_bf16 v[88:91], v[168:171], a[248:251], v[88:91]\n"
    "v_exp_f32 v48, v48\n"
    "v_mfma_f32_16x16x32_bf16 v[84:87], v[168:171], a[224:227], v[84:87]\n"
    "ds_read_b128 v[164:167], v200 offset:35840\n"
    "v_permlane16_swap_b32 v125, v127\n"
    "v_mfma_f32_16x16x32_bf16 v[84:87], v[172:175], a[228:231], v[84:87]\n"
    "v_exp_f32 v49, v49\n"
    "v_mfma_f32_16x16x32_bf16 v[80:83], v[172:175], a[204:207], v[80:83]\n"
    "v_exp_f32 v50, v50\n"
    "v_mfma_f32_16x16x32_bf16 v[80:83], v[168:171], a[200:203], v[80:83]\n"
    "s_waitcnt lgkmcnt(2)\n"
    "v_exp_f32 v51, v51\n"
    "v_mfma_f32_16x16x32_bf16 v[80:83], v[176:179], a[208:211], v[80:83]\n"
    "ds_read_b128 v[168:171], v200 offset:32768\n"
    "v_add_f32 v113, v113, v115\n"
    "v_mfma_f32_16x16x32_bf16 v[80:83], v[180:183], a[212:215], v[80:83]\n"
    "v_exp_f32 v52, v52\n"
    "v_mfma_f32_16x16x32_bf16 v[84:87], v[180:183], a[236:239], v[84:87]\n"
    "v_exp_f32 v53, v53\n"
    "v_mfma_f32_16x16x32_bf16 v[84:87], v[176:179], a[232:235], v[84:87]\n"
    "v_exp_f32 v54, v54\n"
    "v_mfma_f32_16x16x32_bf16 v[88:91], v[176:179], v[0:3], v[88:91]\n"
    "ds_read_b128 v[172:175], v200 offset:33792\n"
    "v_add_f32 v117, v117, v119\n"
    "v_mfma_f32_16x16x32_bf16 v[88:91], v[180:183], v[4:7], v[88:91]\n"
    "v_add_f32 v121, v121, v123\n"
    "v_add_f32 v125, v125, v127\n"
    "v_mfma_f32_16x16x32_bf16 v[92:95], v[180:183], v[28:31], v[92:95]\n"
    "v_exp_f32 v55, v55\n"
    "v_mfma_f32_16x16x32_bf16 v[92:95], v[176:179], v[24:27], v[92:95]\n"
    "s_waitcnt lgkmcnt(2)\n"
    "v_add_f32 v112, v112, v113\n"
    "v_add_f32 v116, v116, v117\n"
    "v_mfma_f32_16x16x32_bf16 v[252:255], v[160:163], v[24:27], v[108:111]\n"
    "ds_read_b128 v[176:179], v200 offset:30720\n"
    "v_add_f32 v120, v120, v121\n"
    "v_mfma_f32_16x16x32_bf16 v[252:255], v[164:167], v[28:31], v[252:255]\n"
    "v_add_f32 v124, v124, v125\n"
    "v_and_b32 v198, 15, v231\n"
    "v_mfma_f32_16x16x32_bf16 v[248:251], v[164:167], v[4:7], v[104:107]\n"
    "v_mul_u32_u24 v205, 0x180, v198\n"
    "v_lshrrev_b32 v199, 4, v231\n"
    "v_mfma_f32_16x16x32_bf16 v[248:251], v[160:163], v[0:3], v[248:251]\n"
    "v_lshl_add_u32 v205, v199, 4, v205\n"
    "v_add_u32 v207, 0x18000, v200\n"
    "v_mfma_f32_16x16x32_bf16 v[188:191], v[160:163], a[232:235], v[100:103]\n"
    "ds_read_b128 v[180:183], v200 offset:31744\n"
    "v_cvt_pk_bf16_f32 v232, v232, v233\n"
    "v_mfma_f32_16x16x32_bf16 v[188:191], v[164:167], a[236:239], v[188:191]\n"
    "v_exp_f32 v56, v56\n"
    "v_mfma_f32_16x16x32_bf16 v[184:187], v[164:167], a[212:215], v[96:99]\n"
    "v_exp_f32 v57, v57\n"
    "v_mfma_f32_16x16x32_bf16 v[184:187], v[160:163], a[208:211], v[184:187]\n"
    "s_waitcnt lgkmcnt(2)\n"
    "v_exp_f32 v58, v58\n"
    "v_mfma_f32_16x16x32_bf16 v[184:187], v[168:171], a[200:203], v[184:187]\n"
    "ds_read_b128 v[160:163], v201\n"
    "v_cvt_pk_bf16_f32 v233, v234, v235\n"
    "v_mfma_f32_16x16x32_bf16 v[184:187], v[172:175], a[204:207], v[184:187]\n"
    "v_exp_f32 v59, v59\n"
    "v_mfma_f32_16x16x32_bf16 v[188:191], v[172:175], a[228:231], v[188:191]\n"
    "v_exp_f32 v60, v60\n"
    "v_mfma_f32_16x16x32_bf16 v[188:191], v[168:171], a[224:227], v[188:191]\n"
    "v_exp_f32 v61, v61\n"
    "v_mfma_f32_16x16x32_bf16 v[248:251], v[168:171], a[248:251], v[248:251]\n"
    "ds_read_b128 v[164:167], v201 offset:1024\n"
    "v_cvt_pk_bf16_f32 v236, v236, v237\n"
    "v_mfma_f32_16x16x32_bf16 v[248:251], v[172:175], a[252:255], v[248:251]\n"
    "v_exp_f32 v62, v62\n"
    "v_mfma_f32_16x16x32_bf16 v[252:255], v[172:175], v[20:23], v[252:255]\n"
    "v_exp_f32 v63, v63\n"
    "v_mfma_f32_16x16x32_bf16 v[252:255], v[168:171], v[16:19], v[252:255]\n"
    "s_waitcnt lgkmcnt(2)\n"
    "v_exp_f32 v80, v80\n"
    "v_mfma_f32_16x16x32_bf16 v[252:255], v[176:179], v[8:11], v[252:255]\n"
    "ds_read_b128 v[168:171], v201 offset:24576\n"
    "v_cvt_pk_bf16_f32 v237, v238, v239\n"
    "v_mfma_f32_16x16x32_bf16 v[252:255], v[180:183], v[12:15], v[252:255]\n"
    "v_exp_f32 v81, v81\n"
    "v_mfma_f32_16x16x32_bf16 v[248:251], v[180:183], a[244:247], v[248:251]\n"
    "v_exp_f32 v82, v82\n"
    "v_mfma_f32_16x16x32_bf16 v[248:251], v[176:179], a[240:243], v[248:251]\n"
    "v_exp_f32 v83, v83\n"
    "v_mfma_f32_16x16x32_bf16 v[188:191], v[176:179], a[216:219], v[188:191]\n"
    "ds_read_b128 v[172:175], v201 offset:25600\n"
    "v_cvt_pk_bf16_f32 v240, v240, v241\n"
    "v_mfma_f32_16x16x32_bf16 v[188:191], v[180:183], a[220:223], v[188:191]\n"
    "v_exp_f32 v84, v84\n"
    "v_mfma_f32_16x16x32_bf16 v[184:187], v[180:183], a[196:199], v[184:187]\n"
    "v_exp_f32 v85, v85\n"
    "v_mfma_f32_16x16x32_bf16 v[184:187], v[176:179], a[192:195], v[184:187]\n"
    "s_waitcnt lgkmcnt(2)\n"
    "v_exp_f32 v86, v86\n"
    "v_mfma_f32_16x16x32_bf16 a[0:3], v[160:163], v[128:131], a[0:3]\n"
    "ds_read_b128 v[176:179], v201 offset:2048\n"
    "v_cvt_pk_bf16_f32 v241, v242, v243\n"
    "v_mfma_f32_16x16x32_bf16 a[0:3], v[164:167], v[144:147], a[0:3]\n"
    "v_exp_f32 v87, v87\n"
    "v_mfma_f32_16x16x32_bf16 a[4:7], v[160:163], v[132:135], a[4:7]\n"
    "v_exp_f32 v88, v88\n"
    "v_mfma_f32_16x16x32_bf16 a[4:7], v[164:167], v[148:151], a[4:7]\n"
    "v_exp_f32 v89, v89\n"
    "v_mfma_f32_16x16x32_bf16 a[8:11], v[160:163], v[136:139], a[8:11]\n"
    "ds_read_b128 v[180:183], v201 offset:3072\n"
    "v_cvt_pk_bf16_f32 v244, v244, v245\n"
    "v_mfma_f32_16x16x32_bf16 a[8:11], v[164:167], v[152:155], a[8:11]\n"
    "v_cvt_pk_bf16_f32 v245, v246, v247\n"
    "v_cvt_pk_bf16_f32 v234, v48, v49\n"
    "v_mfma_f32_16x16x32_bf16 a[12:15], v[160:163], v[140:143], a[12:15]\n"
    "v_cvt_pk_bf16_f32 v235, v50, v51\n"
    "v_cvt_pk_bf16_f32 v238, v52, v53\n"
    "v_mfma_f32_16x16x32_bf16 a[12:15], v[164:167], v[156:159], a[12:15]\n"
    "v_cvt_pk_bf16_f32 v239, v54, v55\n"
    "v_cvt_pk_bf16_f32 v242, v56, v57\n"
    "v_mfma_f32_16x16x32_bf16 v[112:115], v[192:195], v[144:147], v[112:115]\n"
    "v_exp_f32 v90, v90\n"
    "v_mfma_f32_16x16x32_bf16 v[112:115], v[192:195], v[32:35], v[112:115]\n"
    "v_cvt_pk_bf16_f32 v243, v58, v59\n"
    "v_cvt_pk_bf16_f32 v246, v60, v61\n"
    "v_mfma_f32_16x16x32_bf16 v[112:115], v[192:195], v[64:67], v[112:115]\n"
    "v_cvt_pk_bf16_f32 v247, v62, v63\n"
    "v_cvt_pk_bf16_f32 v80, v80, v81\n"
    "v_mfma_f32_16x16x32_bf16 v[116:119], v[192:195], v[148:151], v[116:119]\n"
    "v_exp_f32 v91, v91\n"
    "v_mfma_f32_16x16x32_bf16 v[116:119], v[192:195], v[36:39], v[116:119]\n"
    "v_exp_f32 v92, v92\n"
    "v_mfma_f32_16x16x32_bf16 v[116:119], v[192:195], v[68:71], v[116:119]\n"
    "s_waitcnt lgkmcnt(2)\n"
    "v_exp_f32 v93, v93\n"
    "v_mfma_f32_16x16x32_bf16 a[0:3], v[168:171], v[32:35], a[0:3]\n"
    "ds_read_b128 v[160:163], v201 offset:26624\n"
    "v_cvt_pk_bf16_f32 v81, v82, v83\n"
    "v_mfma_f32_16x16x32_bf16 a[0:3], v[172:175], v[64:67], a[0:3]\n"
    "v_exp_f32 v94, v94\n"
    "v_mfma_f32_16x16x32_bf16 a[4:7], v[168:171], v[36:39], a[4:7]\n"
    "v_exp_f32 v95, v95\n"
    "v_mfma_f32_16x16x32_bf16 a[4:7], v[172:175], v[68:71], a[4:7]\n"
    "v_exp_f32 v184, v184\n"
    "v_mfma_f32_16x16x32_bf16 a[8:11], v[168:171], v[40:43], a[8:11]\n"
    "ds_read_b128 v[164:167], v201 offset:27648\n"
    "v_cvt_pk_bf16_f32 v84, v84, v85\n"
    "v_mfma_f32_16x16x32_bf16 a[8:11], v[172:175], v[72:75], a[8:11]\n"
    "v_exp_f32 v185, v185\n"
    "v_mfma_f32_16x16x32_bf16 a[12:15], v[168:171], v[44:47], a[12:15]\n"
    "v_exp_f32 v186, v186\n"
    "v_mfma_f32_16x16x32_bf16 a[12:15], v[172:175], v[76:79], a[12:15]\n"
    "v_exp_f32 v187, v187\n"
    "v_mfma_f32_16x16x32_bf16 v[120:123], v[192:195], v[152:155], v[120:123]\n"
    "v_exp_f32 v188, v188\n"
    "v_mfma_f32_16x16x32_bf16 v[120:123], v[192:195], v[40:43], v[120:123]\n"
    "v_exp_f32 v189, v189\n"
    "v_mfma_f32_16x16x32_bf16 v[120:123], v[192:195], v[72:75], v[120:123]\n"
    "v_exp_f32 v190, v190\n"
    "v_mfma_f32_16x16x32_bf16 v[124:127], v[192:195], v[156:159], v[124:127]\n"
    "v_exp_f32 v191, v191\n"
    "v_mfma_f32_16x16x32_bf16 v[124:127], v[192:195], v[44:47], v[124:127]\n"
    "s_waitcnt vmcnt(0)\n"
    "v_exp_f32 v248, v248\n"
    "v_mfma_f32_16x16x32_bf16 v[124:127], v[192:195], v[76:79], v[124:127]\n"
    "s_waitcnt lgkmcnt(2)\n"
    "v_exp_f32 v249, v249\n"
    "v_mfma_f32_16x16x32_bf16 a[16:19], v[176:179], v[128:131], a[16:19]\n"
    "s_barrier\n"
    "v_exp_f32 v250, v250\n"
    "v_mfma_f32_16x16x32_bf16 a[16:19], v[180:183], v[144:147], a[16:19]\n"
    "ds_read_b128 v[168:171], v207\n"
    "v_cvt_pk_bf16_f32 v85, v86, v87\n"
    "v_mfma_f32_16x16x32_bf16 a[20:23], v[176:179], v[132:135], a[20:23]\n"
    "v_exp_f32 v251, v251\n"
    "v_mfma_f32_16x16x32_bf16 a[20:23], v[180:183], v[148:151], a[20:23]\n"
    "v_exp_f32 v252, v252\n"
    "v_mfma_f32_16x16x32_bf16 a[24:27], v[176:179], v[136:139], a[24:27]\n"
    "ds_read_b128 v[172:175], v207 offset:1024\n"
    "v_cvt_pk_bf16_f32 v88, v88, v89\n"
    "v_mfma_f32_16x16x32_bf16 a[24:27], v[180:183], v[152:155], a[24:27]\n"
    "v_exp_f32 v253, v253\n"
    "v_mfma_f32_16x16x32_bf16 a[28:31], v[176:179], v[140:143], a[28:31]\n"
    "v_exp_f32 v254, v254\n"
    "v_mfma_f32_16x16x32_bf16 a[28:31], v[180:183], v[156:159], a[28:31]\n"
    "s_waitcnt lgkmcnt(2)\n"
    "v_cvt_pk_bf16_f32 v89, v90, v91\n"
    "v_cvt_pk_bf16_f32 v92, v92, v93\n"
    "v_mfma_f32_16x16x32_bf16 a[16:19], v[160:163], v[32:35], a[16:19]\n"
    "ds_read_b128 v[176:179], v207 offset:2048\n"
    "v_cvt_pk_bf16_f32 v93, v94, v95\n"
    "v_mfma_f32_16x16x32_bf16 a[16:19], v[164:167], v[64:67], a[16:19]\n"
    "ds_read_b128 v[180:183], v207 offset:3072\n"
    "v_cvt_pk_bf16_f32 v82, v184, v185\n"
    "v_mfma_f32_16x16x32_bf16 a[20:23], v[160:163], v[36:39], a[20:23]\n"
    "v_cvt_pk_bf16_f32 v83, v186, v187\n"
    "v_cvt_pk_bf16_f32 v86, v188, v189\n"
    "v_mfma_f32_16x16x32_bf16 a[20:23], v[164:167], v[68:71], a[20:23]\n"
    "v_exp_f32 v255, v255\n"
    "v_mfma_f32_16x16x32_bf16 a[24:27], v[160:163], v[40:43], a[24:27]\n"
    "v_cvt_pk_bf16_f32 v87, v190, v191\n"
    "v_cvt_pk_bf16_f32 v90, v248, v249\n"
    "v_mfma_f32_16x16x32_bf16 a[24:27], v[164:167], v[72:75], a[24:27]\n"
    "v_cvt_pk_bf16_f32 v91, v250, v251\n"
    "v_cvt_pk_bf16_f32 v94, v252, v253\n"
    "v_mfma_f32_16x16x32_bf16 a[28:31], v[160:163], v[44:47], a[28:31]\n"
    "v_cvt_pk_bf16_f32 v95, v254, v255\n"
    "v_mfma_f32_16x16x32_bf16 a[28:31], v[164:167], v[76:79], a[28:31]\n"
    "v_mfma_f32_16x16x32_bf16 v[112:115], v[192:195], v[232:235], v[112:115]\n"
    "ds_read_b128 v[160:163], v201 offset:4096\n"
    "v_mfma_f32_16x16x32_bf16 v[112:115], v[192:195], v[80:83], v[112:115]\n"
    "s_waitcnt lgkmcnt(3)\n"
    "v_mfma_f32_16x16x32_bf16 a[0:3], v[168:171], v[232:235], a[0:3]\n"
    "v_mfma_f32_16x16x32_bf16 a[0:3], v[172:175], v[80:83], a[0:3]\n"
    "s_waitcnt lgkmcnt(1)\n"
    "v_mfma_f32_16x16x32_bf16 a[16:19], v[176:179], v[232:235], a[16:19]\n"
    "ds_read_b128 v[164:167], v201 offset:5120\n"
    "v_mfma_f32_16x16x32_bf16 a[16:19], v[180:183], v[80:83], a[16:19]\n"
    "v_mfma_f32_16x16x32_bf16 v[116:119], v[192:195], v[236:239], v[116:119]\n"
    "v_mul_f32 v222, s68, v112\n"
    "v_rcp_f32 v222, v222\n"
    "v_mfma_f32_16x16x32_bf16 v[116:119], v[192:195], v[84:87], v[116:119]\n"
    "v_accvgpr_read_b32 v96, a0\n"
    "v_accvgpr_read_b32 v97, a1\n"
    "v_accvgpr_read_b32 v98, a2\n"
    "v_mfma_f32_16x16x32_bf16 a[4:7], v[168:171], v[236:239], a[4:7]\n"
    "v_accvgpr_read_b32 v99, a3\n"
    "v_fma_f32 v96, v96, v222, s94\n"
    "v_fma_f32 v97, v97, v222, s94\n"
    "v_mfma_f32_16x16x32_bf16 a[4:7], v[172:175], v[84:87], a[4:7]\n"
    "v_fma_f32 v98, v98, v222, s94\n"
    "v_fma_f32 v99, v99, v222, s94\n"
    "v_cvt_pk_bf16_f32 v48, v96, v97\n"
    "v_mfma_f32_16x16x32_bf16 a[20:23], v[176:179], v[236:239], a[20:23]\n"
    "v_cvt_pk_bf16_f32 v49, v98, v99\n"
    "v_accvgpr_read_b32 v100, a16\n"
    "v_accvgpr_read_b32 v101, a17\n"
    "v_mfma_f32_16x16x32_bf16 a[20:23], v[180:183], v[84:87], a[20:23]\n"
    "v_accvgpr_read_b32 v102, a18\n"
    "v_accvgpr_read_b32 v103, a19\n"
    "v_fma_f32 v100, v100, v222, s94\n"
    "v_mfma_f32_16x16x32_bf16 v[120:123], v[192:195], v[240:243], v[120:123]\n"
    "v_fma_f32 v101, v101, v222, s94\n"
    "v_fma_f32 v102, v102, v222, s94\n"
    "v_fma_f32 v103, v103, v222, s94\n"
    "v_mfma_f32_16x16x32_bf16 v[120:123], v[192:195], v[88:91], v[120:123]\n"
    "v_cvt_pk_bf16_f32 v50, v100, v101\n"
    "v_cvt_pk_bf16_f32 v51, v102, v103\n"
    "v_mfma_f32_16x16x32_bf16 a[8:11], v[168:171], v[240:243], a[8:11]\n"
    "buffer_store_dwordx4 v[48:51], v205, s[28:31], 0 offen sc0 sc1\n"
    "v_mul_f32 v224, s68, v116\n"
    "v_rcp_f32 v224, v224\n"
    "v_accvgpr_read_b32 v104, a4\n"
    "v_mfma_f32_16x16x32_bf16 a[8:11], v[172:175], v[88:91], a[8:11]\n"
    "v_accvgpr_read_b32 v105, a5\n"
    "v_accvgpr_read_b32 v106, a6\n"
    "v_accvgpr_read_b32 v107, a7\n"
    "v_mfma_f32_16x16x32_bf16 a[24:27], v[176:179], v[240:243], a[24:27]\n"
    "v_fma_f32 v104, v104, v224, s94\n"
    "v_fma_f32 v105, v105, v224, s94\n"
    "v_fma_f32 v106, v106, v224, s94\n"
    "v_mfma_f32_16x16x32_bf16 a[24:27], v[180:183], v[88:91], a[24:27]\n"
    "v_fma_f32 v107, v107, v224, s94\n"
    "v_cvt_pk_bf16_f32 v52, v104, v105\n"
    "v_cvt_pk_bf16_f32 v53, v106, v107\n"
    "v_mfma_f32_16x16x32_bf16 v[124:127], v[192:195], v[244:247], v[124:127]\n"
    "v_accvgpr_read_b32 v108, a20\n"
    "v_accvgpr_read_b32 v109, a21\n"
    "v_accvgpr_read_b32 v110, a22\n"
    "v_mfma_f32_16x16x32_bf16 v[124:127], v[192:195], v[92:95], v[124:127]\n"
    "v_accvgpr_read_b32 v111, a23\n"
    "v_fma_f32 v108, v108, v224, s94\n"
    "v_fma_f32 v109, v109, v224, s94\n"
    "v_mfma_f32_16x16x32_bf16 a[12:15], v[168:171], v[244:247], a[12:15]\n"
    "v_fma_f32 v110, v110, v224, s94\n"
    "v_fma_f32 v111, v111, v224, s94\n"
    "v_mfma_f32_16x16x32_bf16 a[12:15], v[172:175], v[92:95], a[12:15]\n"
    "v_cvt_pk_bf16_f32 v54, v108, v109\n"
    "v_cvt_pk_bf16_f32 v55, v110, v111\n"
    "v_mfma_f32_16x16x32_bf16 a[28:31], v[176:179], v[244:247], a[28:31]\n"
    "ds_read_b128 v[168:171], v201 offset:28672\n"
    "v_mfma_f32_16x16x32_bf16 a[28:31], v[180:183], v[92:95], a[28:31]\n"
    "s_waitcnt lgkmcnt(1)\n"
    "ds_read_b128 v[172:175], v201 offset:29696\n"
    "v_mfma_f32_16x16x32_bf16 a[32:35], v[160:163], v[128:131], a[32:35]\n"
    "ds_read_b128 v[176:179], v207 offset:4096\n"
    "v_mfma_f32_16x16x32_bf16 a[32:35], v[164:167], v[144:147], a[32:35]\n"
    "buffer_store_dwordx4 v[52:55], v205, s[28:31], s62 offen sc0 sc1\n"
    "v_mul_f32 v226, s68, v120\n"
    "v_rcp_f32 v226, v226\n"
    "v_mfma_f32_16x16x32_bf16 a[36:39], v[160:163], v[132:135], a[36:39]\n"
    "v_accvgpr_read_b32 v96, a8\n"
    "v_accvgpr_read_b32 v97, a9\n"
    "v_mfma_f32_16x16x32_bf16 a[36:39], v[164:167], v[148:151], a[36:39]\n"
    "v_accvgpr_read_b32 v98, a10\n"
    "v_accvgpr_read_b32 v99, a11\n"
    "v_mfma_f32_16x16x32_bf16 a[40:43], v[160:163], v[136:139], a[40:43]\n"
    "ds_read_b128 v[180:183], v207 offset:5120\n"
    "v_fma_f32 v96, v96, v226, s94\n"
    "v_mfma_f32_16x16x32_bf16 a[40:43], v[164:167], v[152:155], a[40:43]\n"
    "v_fma_f32 v97, v97, v226, s94\n"
    "v_fma_f32 v98, v98, v226, s94\n"
    "v_mfma_f32_16x16x32_bf16 a[44:47], v[160:163], v[140:143], a[44:47]\n"
    "v_fma_f32 v99, v99, v226, s94\n"
    "v_cvt_pk_bf16_f32 v56, v96, v97\n"
    "v_mfma_f32_16x16x32_bf16 a[44:47], v[164:167], v[156:159], a[44:47]\n"
    "s_waitcnt lgkmcnt(2)\n"
    "v_cvt_pk_bf16_f32 v57, v98, v99\n"
    "v_accvgpr_read_b32 v100, a24\n"
    "v_mfma_f32_16x16x32_bf16 a[44:47], v[168:171], v[44:47], a[44:47]\n"
    "ds_read_b128 v[160:163], v201 offset:6144\n"
    "v_accvgpr_read_b32 v101, a25\n"
    "v_mfma_f32_16x16x32_bf16 a[44:47], v[172:175], v[76:79], a[44:47]\n"
    "v_accvgpr_read_b32 v102, a26\n"
    "v_accvgpr_read_b32 v103, a27\n"
    "v_mfma_f32_16x16x32_bf16 a[40:43], v[168:171], v[40:43], a[40:43]\n"
    "v_fma_f32 v100, v100, v226, s94\n"
    "v_fma_f32 v101, v101, v226, s94\n"
    "v_mfma_f32_16x16x32_bf16 a[40:43], v[172:175], v[72:75], a[40:43]\n"
    "v_fma_f32 v102, v102, v226, s94\n"
    "v_fma_f32 v103, v103, v226, s94\n"
    "v_mfma_f32_16x16x32_bf16 a[36:39], v[168:171], v[36:39], a[36:39]\n"
    "ds_read_b128 v[164:167], v201 offset:7168\n"
    "v_cvt_pk_bf16_f32 v58, v100, v101\n"
    "v_mfma_f32_16x16x32_bf16 a[36:39], v[172:175], v[68:71], a[36:39]\n"
    "v_cvt_pk_bf16_f32 v59, v102, v103\n"
    "v_mfma_f32_16x16x32_bf16 a[32:35], v[168:171], v[32:35], a[32:35]\n"
    "buffer_store_dwordx4 v[56:59], v205, s[28:31], s63 offen sc0 sc1\n"
    "v_mul_f32 v228, s68, v124\n"
    "v_rcp_f32 v228, v228\n"
    "v_mfma_f32_16x16x32_bf16 a[32:35], v[172:175], v[64:67], a[32:35]\n"
    "s_waitcnt lgkmcnt(2)\n"
    "v_accvgpr_read_b32 v104, a12\n"
    "v_accvgpr_read_b32 v105, a13\n"
    "v_mfma_f32_16x16x32_bf16 a[32:35], v[176:179], v[232:235], a[32:35]\n"
    "ds_read_b128 v[168:171], v201 offset:30720\n"
    "v_accvgpr_read_b32 v106, a14\n"
    "v_mfma_f32_16x16x32_bf16 a[32:35], v[180:183], v[80:83], a[32:35]\n"
    "v_accvgpr_read_b32 v107, a15\n"
    "v_fma_f32 v104, v104, v228, s94\n"
    "v_mfma_f32_16x16x32_bf16 a[36:39], v[176:179], v[236:239], a[36:39]\n"
    "v_fma_f32 v105, v105, v228, s94\n"
    "v_fma_f32 v106, v106, v228, s94\n"
    "v_mfma_f32_16x16x32_bf16 a[36:39], v[180:183], v[84:87], a[36:39]\n"
    "v_fma_f32 v107, v107, v228, s94\n"
    "v_cvt_pk_bf16_f32 v60, v104, v105\n"
    "v_mfma_f32_16x16x32_bf16 a[40:43], v[176:179], v[240:243], a[40:43]\n"
    "ds_read_b128 v[172:175], v201 offset:31744\n"
    "v_cvt_pk_bf16_f32 v61, v106, v107\n"
    "v_mfma_f32_16x16x32_bf16 a[40:43], v[180:183], v[88:91], a[40:43]\n"
    "v_accvgpr_read_b32 v108, a28\n"
    "v_accvgpr_read_b32 v109, a29\n"
    "v_mfma_f32_16x16x32_bf16 a[44:47], v[176:179], v[244:247], a[44:47]\n"
    "v_accvgpr_read_b32 v110, a30\n"
    "v_accvgpr_read_b32 v111, a31\n"
    "v_mfma_f32_16x16x32_bf16 a[44:47], v[180:183], v[92:95], a[44:47]\n"
    "s_waitcnt lgkmcnt(2)\n"
    "v_fma_f32 v108, v108, v228, s94\n"
    "v_fma_f32 v109, v109, v228, s94\n"
    "v_mfma_f32_16x16x32_bf16 a[48:51], v[160:163], v[128:131], a[48:51]\n"
    "ds_read_b128 v[176:179], v207 offset:6144\n"
    "v_fma_f32 v110, v110, v228, s94\n"
    "v_mfma_f32_16x16x32_bf16 a[48:51], v[164:167], v[144:147], a[48:51]\n"
    "v_fma_f32 v111, v111, v228, s94\n"
    "v_cvt_pk_bf16_f32 v62, v108, v109\n"
    "v_mfma_f32_16x16x32_bf16 a[52:55], v[160:163], v[132:135], a[52:55]\n"
    "v_cvt_pk_bf16_f32 v63, v110, v111\n"
    "v_mfma_f32_16x16x32_bf16 a[52:55], v[164:167], v[148:151], a[52:55]\n"
    "buffer_store_dwordx4 v[60:63], v205, s[28:31], s64 offen sc0 sc1\n"
    "v_accvgpr_read_b32 v96, a32\n"
    "v_accvgpr_read_b32 v97, a33\n"
    "v_accvgpr_read_b32 v98, a34\n"
    "v_mfma_f32_16x16x32_bf16 a[56:59], v[160:163], v[136:139], a[56:59]\n"
    "ds_read_b128 v[180:183], v207 offset:7168\n"
    "v_accvgpr_read_b32 v99, a35\n"
    "v_mfma_f32_16x16x32_bf16 a[56:59], v[164:167], v[152:155], a[56:59]\n"
    "v_fma_f32 v96, v96, v222, s94\n"
    "v_fma_f32 v97, v97, v222, s94\n"
    "v_mfma_f32_16x16x32_bf16 a[60:63], v[160:163], v[140:143], a[60:63]\n"
    "v_fma_f32 v98, v98, v222, s94\n"
    "v_fma_f32 v99, v99, v222, s94\n"
    "v_mfma_f32_16x16x32_bf16 a[60:63], v[164:167], v[156:159], a[60:63]\n"
    "s_waitcnt lgkmcnt(2)\n"
    "v_cvt_pk_bf16_f32 v184, v96, v97\n"
    "v_cvt_pk_bf16_f32 v185, v98, v99\n"
    "v_mfma_f32_16x16x32_bf16 a[60:63], v[168:171], v[44:47], a[60:63]\n"
    "ds_read_b128 v[160:163], v201 offset:8192\n"
    "v_accvgpr_read_b32 v100, a36\n"
    "v_mfma_f32_16x16x32_bf16 a[60:63], v[172:175], v[76:79], a[60:63]\n"
    "v_accvgpr_read_b32 v101, a37\n"
    "v_accvgpr_read_b32 v102, a38\n"
    "v_mfma_f32_16x16x32_bf16 a[56:59], v[168:171], v[40:43], a[56:59]\n"
    "v_accvgpr_read_b32 v103, a39\n"
    "v_fma_f32 v100, v100, v224, s94\n"
    "v_mfma_f32_16x16x32_bf16 a[56:59], v[172:175], v[72:75], a[56:59]\n"
    "v_fma_f32 v101, v101, v224, s94\n"
    "v_fma_f32 v102, v102, v224, s94\n"
    "v_mfma_f32_16x16x32_bf16 a[52:55], v[168:171], v[36:39], a[52:55]\n"
    "ds_read_b128 v[164:167], v201 offset:9216\n"
    "v_fma_f32 v103, v103, v224, s94\n"
    "v_mfma_f32_16x16x32_bf16 a[52:55], v[172:175], v[68:71], a[52:55]\n"
    "v_cvt_pk_bf16_f32 v188, v100, v101\n"
    "v_cvt_pk_bf16_f32 v189, v102, v103\n"
    "v_mfma_f32_16x16x32_bf16 a[48:51], v[168:171], v[32:35], a[48:51]\n"
    "v_accvgpr_read_b32 v104, a40\n"
    "v_accvgpr_read_b32 v105, a41\n"
    "v_mfma_f32_16x16x32_bf16 a[48:51], v[172:175], v[64:67], a[48:51]\n"
    "s_waitcnt lgkmcnt(2)\n"
    "v_accvgpr_read_b32 v106, a42\n"
    "v_accvgpr_read_b32 v107, a43\n"
    "v_mfma_f32_16x16x32_bf16 a[48:51], v[176:179], v[232:235], a[48:51]\n"
    "ds_read_b128 v[168:171], v201 offset:32768\n"
    "v_fma_f32 v104, v104, v226, s94\n"
    "v_mfma_f32_16x16x32_bf16 a[48:51], v[180:183], v[80:83], a[48:51]\n"
    "v_fma_f32 v105, v105, v226, s94\n"
    "v_fma_f32 v106, v106, v226, s94\n"
    "v_mfma_f32_16x16x32_bf16 a[52:55], v[176:179], v[236:239], a[52:55]\n"
    "v_fma_f32 v107, v107, v226, s94\n"
    "v_cvt_pk_bf16_f32 v248, v104, v105\n"
    "v_mfma_f32_16x16x32_bf16 a[52:55], v[180:183], v[84:87], a[52:55]\n"
    "v_cvt_pk_bf16_f32 v249, v106, v107\n"
    "v_accvgpr_read_b32 v108, a44\n"
    "v_mfma_f32_16x16x32_bf16 a[56:59], v[176:179], v[240:243], a[56:59]\n"
    "ds_read_b128 v[172:175], v201 offset:33792\n"
    "v_accvgpr_read_b32 v109, a45\n"
    "v_mfma_f32_16x16x32_bf16 a[56:59], v[180:183], v[88:91], a[56:59]\n"
    "v_accvgpr_read_b32 v110, a46\n"
    "v_accvgpr_read_b32 v111, a47\n"
    "v_mfma_f32_16x16x32_bf16 a[60:63], v[176:179], v[244:247], a[60:63]\n"
    "v_fma_f32 v108, v108, v228, s94\n"
    "v_fma_f32 v109, v109, v228, s94\n"
    "v_mfma_f32_16x16x32_bf16 a[60:63], v[180:183], v[92:95], a[60:63]\n"
    "s_waitcnt lgkmcnt(2)\n"
    "v_fma_f32 v110, v110, v228, s94\n"
    "v_fma_f32 v111, v111, v228, s94\n"
    "v_mfma_f32_16x16x32_bf16 a[64:67], v[160:163], v[128:131], a[64:67]\n"
    "ds_read_b128 v[176:179], v207 offset:8192\n"
    "v_cvt_pk_bf16_f32 v252, v108, v109\n"
    "v_mfma_f32_16x16x32_bf16 a[64:67], v[164:167], v[144:147], a[64:67]\n"
    "v_cvt_pk_bf16_f32 v253, v110, v111\n"
    "v_accvgpr_read_b32 v96, a48\n"
    "v_mfma_f32_16x16x32_bf16 a[68:71], v[160:163], v[132:135], a[68:71]\n"
    "v_accvgpr_read_b32 v97, a49\n"
    "v_accvgpr_read_b32 v98, a50\n"
    "v_mfma_f32_16x16x32_bf16 a[68:71], v[164:167], v[148:151], a[68:71]\n"
    "v_accvgpr_read_b32 v99, a51\n"
    "v_fma_f32 v96, v96, v222, s94\n"
    "v_mfma_f32_16x16x32_bf16 a[72:75], v[160:163], v[136:139], a[72:75]\n"
    "ds_read_b128 v[180:183], v207 offset:9216\n"
    "v_fma_f32 v97, v97, v222, s94\n"
    "v_mfma_f32_16x16x32_bf16 a[72:75], v[164:167], v[152:155], a[72:75]\n"
    "v_fma_f32 v98, v98, v222, s94\n"
    "v_fma_f32 v99, v99, v222, s94\n"
    "v_mfma_f32_16x16x32_bf16 a[76:79], v[160:163], v[140:143], a[76:79]\n"
    "v_cvt_pk_bf16_f32 v186, v96, v97\n"
    "v_cvt_pk_bf16_f32 v187, v98, v99\n"
    "v_mfma_f32_16x16x32_bf16 a[76:79], v[164:167], v[156:159], a[76:79]\n"
    "s_waitcnt lgkmcnt(2)\n"
    "buffer_store_dwordx4 v[184:187], v205, s[28:31], 0 offen offset:64 sc0 sc1\n"
    "v_accvgpr_read_b32 v100, a52\n"
    "v_accvgpr_read_b32 v101, a53\n"
    "v_accvgpr_read_b32 v102, a54\n"
    "v_mfma_f32_16x16x32_bf16 a[76:79], v[168:171], v[44:47], a[76:79]\n"
    "ds_read_b128 v[160:163], v201 offset:10240\n"
    "v_accvgpr_read_b32 v103, a55\n"
    "v_mfma_f32_16x16x32_bf16 a[76:79], v[172:175], v[76:79], a[76:79]\n"
    "v_fma_f32 v100, v100, v224, s94\n"
    "v_fma_f32 v101, v101, v224, s94\n"
    "v_mfma_f32_16x16x32_bf16 a[72:75], v[168:171], v[40:43], a[72:75]\n"
    "v_fma_f32 v102, v102, v224, s94\n"
    "v_fma_f32 v103, v103, v224, s94\n"
    "v_mfma_f32_16x16x32_bf16 a[72:75], v[172:175], v[72:75], a[72:75]\n"
    "v_cvt_pk_bf16_f32 v190, v100, v101\n"
    "v_cvt_pk_bf16_f32 v191, v102, v103\n"
    "v_mfma_f32_16x16x32_bf16 a[68:71], v[168:171], v[36:39], a[68:71]\n"
    "ds_read_b128 v[164:167], v201 offset:11264\n"
    "v_mfma_f32_16x16x32_bf16 a[68:71], v[172:175], v[68:71], a[68:71]\n"
    "buffer_store_dwordx4 v[188:191], v205, s[28:31], s62 offen offset:64 sc0 sc1\n"
    "v_accvgpr_read_b32 v104, a56\n"
    "v_accvgpr_read_b32 v105, a57\n"
    "v_accvgpr_read_b32 v106, a58\n"
    "v_mfma_f32_16x16x32_bf16 a[64:67], v[168:171], v[32:35], a[64:67]\n"
    "v_accvgpr_read_b32 v107, a59\n"
    "v_fma_f32 v104, v104, v226, s94\n"
    "v_mfma_f32_16x16x32_bf16 a[64:67], v[172:175], v[64:67], a[64:67]\n"
    "s_waitcnt lgkmcnt(2)\n"
    "v_fma_f32 v105, v105, v226, s94\n"
    "v_fma_f32 v106, v106, v226, s94\n"
    "v_mfma_f32_16x16x32_bf16 a[64:67], v[176:179], v[232:235], a[64:67]\n"
    "ds_read_b128 v[168:171], v201 offset:34816\n"
    "v_fma_f32 v107, v107, v226, s94\n"
    "v_mfma_f32_16x16x32_bf16 a[64:67], v[180:183], v[80:83], a[64:67]\n"
    "v_cvt_pk_bf16_f32 v250, v104, v105\n"
    "v_cvt_pk_bf16_f32 v251, v106, v107\n"
    "v_mfma_f32_16x16x32_bf16 a[68:71], v[176:179], v[236:239], a[68:71]\n"
    "buffer_store_dwordx4 v[248:251], v205, s[28:31], s63 offen offset:64 sc0 sc1\n"
    "v_accvgpr_read_b32 v108, a60\n"
    "v_accvgpr_read_b32 v109, a61\n"
    "v_accvgpr_read_b32 v110, a62\n"
    "v_mfma_f32_16x16x32_bf16 a[68:71], v[180:183], v[84:87], a[68:71]\n"
    "v_accvgpr_read_b32 v111, a63\n"
    "v_fma_f32 v108, v108, v228, s94\n"
    "v_mfma_f32_16x16x32_bf16 a[72:75], v[176:179], v[240:243], a[72:75]\n"
    "ds_read_b128 v[172:175], v201 offset:35840\n"
    "v_fma_f32 v109, v109, v228, s94\n"
    "v_mfma_f32_16x16x32_bf16 a[72:75], v[180:183], v[88:91], a[72:75]\n"
    "v_fma_f32 v110, v110, v228, s94\n"
    "v_fma_f32 v111, v111, v228, s94\n"
    "v_mfma_f32_16x16x32_bf16 a[76:79], v[176:179], v[244:247], a[76:79]\n"
    "v_cvt_pk_bf16_f32 v254, v108, v109\n"
    "v_cvt_pk_bf16_f32 v255, v110, v111\n"
    "v_mfma_f32_16x16x32_bf16 a[76:79], v[180:183], v[92:95], a[76:79]\n"
    "s_waitcnt lgkmcnt(2)\n"
    "buffer_store_dwordx4 v[252:255], v205, s[28:31], s64 offen offset:64 sc0 sc1\n"
    "v_accvgpr_read_b32 v96, a64\n"
    "v_accvgpr_read_b32 v97, a65\n"
    "v_accvgpr_read_b32 v98, a66\n"
    "v_mfma_f32_16x16x32_bf16 a[80:83], v[160:163], v[128:131], a[80:83]\n"
    "ds_read_b128 v[176:179], v207 offset:10240\n"
    "v_accvgpr_read_b32 v99, a67\n"
    "v_mfma_f32_16x16x32_bf16 a[80:83], v[164:167], v[144:147], a[80:83]\n"
    "v_fma_f32 v96, v96, v222, s94\n"
    "v_fma_f32 v97, v97, v222, s94\n"
    "v_mfma_f32_16x16x32_bf16 a[84:87], v[160:163], v[132:135], a[84:87]\n"
    "v_fma_f32 v98, v98, v222, s94\n"
    "v_fma_f32 v99, v99, v222, s94\n"
    "v_mfma_f32_16x16x32_bf16 a[84:87], v[164:167], v[148:151], a[84:87]\n"
    "v_cvt_pk_bf16_f32 v48, v96, v97\n"
    "v_cvt_pk_bf16_f32 v49, v98, v99\n"
    "v_mfma_f32_16x16x32_bf16 a[88:91], v[160:163], v[136:139], a[88:91]\n"
    "ds_read_b128 v[180:183], v207 offset:11264\n"
    "v_accvgpr_read_b32 v100, a68\n"
    "v_mfma_f32_16x16x32_bf16 a[88:91], v[164:167], v[152:155], a[88:91]\n"
    "v_accvgpr_read_b32 v101, a69\n"
    "v_accvgpr_read_b32 v102, a70\n"
    "v_mfma_f32_16x16x32_bf16 a[92:95], v[160:163], v[140:143], a[92:95]\n"
    "v_accvgpr_read_b32 v103, a71\n"
    "v_fma_f32 v100, v100, v224, s94\n"
    "v_mfma_f32_16x16x32_bf16 a[92:95], v[164:167], v[156:159], a[92:95]\n"
    "s_waitcnt lgkmcnt(2)\n"
    "v_fma_f32 v101, v101, v224, s94\n"
    "v_fma_f32 v102, v102, v224, s94\n"
    "v_mfma_f32_16x16x32_bf16 a[92:95], v[168:171], v[44:47], a[92:95]\n"
    "ds_read_b128 v[160:163], v201 offset:12288\n"
    "v_fma_f32 v103, v103, v224, s94\n"
    "v_mfma_f32_16x16x32_bf16 a[92:95], v[172:175], v[76:79], a[92:95]\n"
    "v_cvt_pk_bf16_f32 v52, v100, v101\n"
    "v_cvt_pk_bf16_f32 v53, v102, v103\n"
    "v_mfma_f32_16x16x32_bf16 a[88:91], v[168:171], v[40:43], a[88:91]\n"
    "v_accvgpr_read_b32 v104, a72\n"
    "v_accvgpr_read_b32 v105, a73\n"
    "v_mfma_f32_16x16x32_bf16 a[88:91], v[172:175], v[72:75], a[88:91]\n"
    "v_accvgpr_read_b32 v106, a74\n"
    "v_accvgpr_read_b32 v107, a75\n"
    "v_mfma_f32_16x16x32_bf16 a[84:87], v[168:171], v[36:39], a[84:87]\n"
    "ds_read_b128 v[164:167], v201 offset:13312\n"
    "v_fma_f32 v104, v104, v226, s94\n"
    "v_mfma_f32_16x16x32_bf16 a[84:87], v[172:175], v[68:71], a[84:87]\n"
    "v_fma_f32 v105, v105, v226, s94\n"
    "v_fma_f32 v106, v106, v226, s94\n"
    "v_mfma_f32_16x16x32_bf16 a[80:83], v[168:171], v[32:35], a[80:83]\n"
    "v_fma_f32 v107, v107, v226, s94\n"
    "v_cvt_pk_bf16_f32 v56, v104, v105\n"
    "v_mfma_f32_16x16x32_bf16 a[80:83], v[172:175], v[64:67], a[80:83]\n"
    "s_waitcnt lgkmcnt(2)\n"
    "v_cvt_pk_bf16_f32 v57, v106, v107\n"
    "v_accvgpr_read_b32 v108, a76\n"
    "v_mfma_f32_16x16x32_bf16 a[80:83], v[176:179], v[232:235], a[80:83]\n"
    "ds_read_b128 v[168:171], v201 offset:36864\n"
    "v_accvgpr_read_b32 v109, a77\n"
    "v_mfma_f32_16x16x32_bf16 a[80:83], v[180:183], v[80:83], a[80:83]\n"
    "v_accvgpr_read_b32 v110, a78\n"
    "v_accvgpr_read_b32 v111, a79\n"
    "v_mfma_f32_16x16x32_bf16 a[84:87], v[176:179], v[236:239], a[84:87]\n"
    "v_fma_f32 v108, v108, v228, s94\n"
    "v_fma_f32 v109, v109, v228, s94\n"
    "v_mfma_f32_16x16x32_bf16 a[84:87], v[180:183], v[84:87], a[84:87]\n"
    "v_fma_f32 v110, v110, v228, s94\n"
    "v_fma_f32 v111, v111, v228, s94\n"
    "v_mfma_f32_16x16x32_bf16 a[88:91], v[176:179], v[240:243], a[88:91]\n"
    "ds_read_b128 v[172:175], v201 offset:37888\n"
    "v_cvt_pk_bf16_f32 v60, v108, v109\n"
    "v_mfma_f32_16x16x32_bf16 a[88:91], v[180:183], v[88:91], a[88:91]\n"
    "v_cvt_pk_bf16_f32 v61, v110, v111\n"
    "v_mfma_f32_16x16x32_bf16 a[92:95], v[176:179], v[244:247], a[92:95]\n"
    "v_mfma_f32_16x16x32_bf16 a[92:95], v[180:183], v[92:95], a[92:95]\n"
    "s_waitcnt lgkmcnt(2)\n"
    "v_accvgpr_read_b32 v96, a80\n"
    "v_accvgpr_read_b32 v97, a81\n"
    "v_mfma_f32_16x16x32_bf16 a[96:99], v[160:163], v[128:131], a[96:99]\n"
    "ds_read_b128 v[176:179], v207 offset:12288\n"
    "v_accvgpr_read_b32 v98, a82\n"
    "v_mfma_f32_16x16x32_bf16 a[96:99], v[164:167], v[144:147], a[96:99]\n"
    "v_accvgpr_read_b32 v99, a83\n"
    "v_fma_f32 v96, v96, v222, s94\n"
    "v_mfma_f32_16x16x32_bf16 a[100:103], v[160:163], v[132:135], a[100:103]\n"
    "v_fma_f32 v97, v97, v222, s94\n"
    "v_fma_f32 v98, v98, v222, s94\n"
    "v_mfma_f32_16x16x32_bf16 a[100:103], v[164:167], v[148:151], a[100:103]\n"
    "v_fma_f32 v99, v99, v222, s94\n"
    "v_cvt_pk_bf16_f32 v50, v96, v97\n"
    "v_mfma_f32_16x16x32_bf16 a[104:107], v[160:163], v[136:139], a[104:107]\n"
    "ds_read_b128 v[180:183], v207 offset:13312\n"
    "v_cvt_pk_bf16_f32 v51, v98, v99\n"
    "v_mfma_f32_16x16x32_bf16 a[104:107], v[164:167], v[152:155], a[104:107]\n"
    "buffer_store_dwordx4 v[48:51], v205, s[28:31], 0 offen offset:128 sc0 sc1\n"
    "v_accvgpr_read_b32 v100, a84\n"
    "v_accvgpr_read_b32 v101, a85\n"
    "v_accvgpr_read_b32 v102, a86\n"
    "v_mfma_f32_16x16x32_bf16 a[108:111], v[160:163], v[140:143], a[108:111]\n"
    "v_accvgpr_read_b32 v103, a87\n"
    "v_fma_f32 v100, v100, v224, s94\n"
    "v_mfma_f32_16x16x32_bf16 a[108:111], v[164:167], v[156:159], a[108:111]\n"
    "s_waitcnt lgkmcnt(2)\n"
    "s_cmp_eq_u32 s57, 0\n"
    "s_cselect_b32 s77, s74, 0\n"
    "v_mov_b32 v196, 0x40000000\n"
    "v_mov_b32 v197, 0x800\n"
    "v_cmp_gt_u32 vcc, s77, v231\n"
    "v_mov_b32 v198, 0\n"
    "v_mov_b32 v198, 0\n"
    "v_cndmask_b32 v197, v196, v197, vcc\n"
    "buffer_store_dword v221, v197, s[44:47], 0 offen sc1\n"
    "v_mfma_f32_16x16x32_bf16 a[108:111], v[168:171], v[44:47], a[108:111]\n"
    "ds_read_b128 v[160:163], v201 offset:14336\n"
    "v_fma_f32 v101, v101, v224, s94\n"
    "v_mfma_f32_16x16x32_bf16 a[108:111], v[172:175], v[76:79], a[108:111]\n"
    "v_fma_f32 v102, v102, v224, s94\n"
    "v_fma_f32 v103, v103, v224, s94\n"
    "v_mfma_f32_16x16x32_bf16 a[104:107], v[168:171], v[40:43], a[104:107]\n"
    "v_cvt_pk_bf16_f32 v54, v100, v101\n"
    "v_cvt_pk_bf16_f32 v55, v102, v103\n"
    "v_mfma_f32_16x16x32_bf16 a[104:107], v[172:175], v[72:75], a[104:107]\n"
    "buffer_store_dwordx4 v[52:55], v205, s[28:31], s62 offen offset:128 sc0 sc1\n"
    "v_accvgpr_read_b32 v104, a88\n"
    "v_accvgpr_read_b32 v105, a89\n"
    "v_accvgpr_read_b32 v106, a90\n"
    "v_mfma_f32_16x16x32_bf16 a[100:103], v[168:171], v[36:39], a[100:103]\n"
    "ds_read_b128 v[164:167], v201 offset:15360\n"
    "v_accvgpr_read_b32 v107, a91\n"
    "v_mfma_f32_16x16x32_bf16 a[100:103], v[172:175], v[68:71], a[100:103]\n"
    "v_fma_f32 v104, v104, v226, s94\n"
    "v_fma_f32 v105, v105, v226, s94\n"
    "v_mfma_f32_16x16x32_bf16 a[96:99], v[168:171], v[32:35], a[96:99]\n"
    "v_fma_f32 v106, v106, v226, s94\n"
    "v_fma_f32 v107, v107, v226, s94\n"
    "v_mfma_f32_16x16x32_bf16 a[96:99], v[172:175], v[64:67], a[96:99]\n"
    "s_waitcnt lgkmcnt(2)\n"
    "v_cvt_pk_bf16_f32 v58, v104, v105\n"
    "v_cvt_pk_bf16_f32 v59, v106, v107\n"
    "v_mfma_f32_16x16x32_bf16 a[96:99], v[176:179], v[232:235], a[96:99]\n"
    "ds_read_b128 v[168:171], v201 offset:38912\n"
    "v_mfma_f32_16x16x32_bf16 a[96:99], v[180:183], v[80:83], a[96:99]\n"
    "buffer_store_dwordx4 v[56:59], v205, s[28:31], s63 offen offset:128 sc0 sc1\n"
    "v_accvgpr_read_b32 v108, a92\n"
    "v_accvgpr_read_b32 v109, a93\n"
    "v_accvgpr_read_b32 v110, a94\n"
    "v_mfma_f32_16x16x32_bf16 a[100:103], v[176:179], v[236:239], a[100:103]\n"
    "v_accvgpr_read_b32 v111, a95\n"
    "v_fma_f32 v108, v108, v228, s94\n"
    "v_mfma_f32_16x16x32_bf16 a[100:103], v[180:183], v[84:87], a[100:103]\n"
    "v_fma_f32 v109, v109, v228, s94\n"
    "v_fma_f32 v110, v110, v228, s94\n"
    "v_mfma_f32_16x16x32_bf16 a[104:107], v[176:179], v[240:243], a[104:107]\n"
    "ds_read_b128 v[172:175], v201 offset:39936\n"
    "v_fma_f32 v111, v111, v228, s94\n"
    "v_mfma_f32_16x16x32_bf16 a[104:107], v[180:183], v[88:91], a[104:107]\n"
    "v_cvt_pk_bf16_f32 v62, v108, v109\n"
    "v_cvt_pk_bf16_f32 v63, v110, v111\n"
    "v_mfma_f32_16x16x32_bf16 a[108:111], v[176:179], v[244:247], a[108:111]\n"
    "buffer_store_dwordx4 v[60:63], v205, s[28:31], s64 offen offset:128 sc0 sc1\n"
    "v_mfma_f32_16x16x32_bf16 a[108:111], v[180:183], v[92:95], a[108:111]\n"
    "s_waitcnt lgkmcnt(2)\n"
    "v_accvgpr_read_b32 v96, a96\n"
    "v_accvgpr_read_b32 v97, a97\n"
    "v_mfma_f32_16x16x32_bf16 a[112:115], v[160:163], v[128:131], a[112:115]\n"
    "ds_read_b128 v[176:179], v207 offset:14336\n"
    "v_accvgpr_read_b32 v98, a98\n"
    "v_mfma_f32_16x16x32_bf16 a[112:115], v[164:167], v[144:147], a[112:115]\n"
    "v_accvgpr_read_b32 v99, a99\n"
    "v_fma_f32 v96, v96, v222, s94\n"
    "v_mfma_f32_16x16x32_bf16 a[116:119], v[160:163], v[132:135], a[116:119]\n"
    "v_fma_f32 v97, v97, v222, s94\n"
    "v_fma_f32 v98, v98, v222, s94\n"
    "v_mfma_f32_16x16x32_bf16 a[116:119], v[164:167], v[148:151], a[116:119]\n"
    "v_fma_f32 v99, v99, v222, s94\n"
    "v_cvt_pk_bf16_f32 v184, v96, v97\n"
    "v_mfma_f32_16x16x32_bf16 a[120:123], v[160:163], v[136:139], a[120:123]\n"
    "ds_read_b128 v[180:183], v207 offset:15360\n"
    "v_cvt_pk_bf16_f32 v185, v98, v99\n"
    "v_mfma_f32_16x16x32_bf16 a[120:123], v[164:167], v[152:155], a[120:123]\n"
    "v_accvgpr_read_b32 v100, a100\n"
    "v_accvgpr_read_b32 v101, a101\n"
    "v_mfma_f32_16x16x32_bf16 a[124:127], v[160:163], v[140:143], a[124:127]\n"
    "v_accvgpr_read_b32 v102, a102\n"
    "v_accvgpr_read_b32 v103, a103\n"
    "v_mfma_f32_16x16x32_bf16 a[124:127], v[164:167], v[156:159], a[124:127]\n"
    "s_waitcnt lgkmcnt(2)\n"
    "v_fma_f32 v100, v100, v224, s94\n"
    "v_fma_f32 v101, v101, v224, s94\n"
    "v_mfma_f32_16x16x32_bf16 a[124:127], v[168:171], v[44:47], a[124:127]\n"
    "ds_read_b128 v[160:163], v201 offset:16384\n"
    "v_fma_f32 v102, v102, v224, s94\n"
    "v_mfma_f32_16x16x32_bf16 a[124:127], v[172:175], v[76:79], a[124:127]\n"
    "v_fma_f32 v103, v103, v224, s94\n"
    "v_cvt_pk_bf16_f32 v188, v100, v101\n"
    "v_mfma_f32_16x16x32_bf16 a[120:123], v[168:171], v[40:43], a[120:123]\n"
    "v_cvt_pk_bf16_f32 v189, v102, v103\n"
    "v_accvgpr_read_b32 v104, a104\n"
    "v_mfma_f32_16x16x32_bf16 a[120:123], v[172:175], v[72:75], a[120:123]\n"
    "v_accvgpr_read_b32 v105, a105\n"
    "v_accvgpr_read_b32 v106, a106\n"
    "v_mfma_f32_16x16x32_bf16 a[116:119], v[168:171], v[36:39], a[116:119]\n"
    "ds_read_b128 v[164:167], v201 offset:17408\n"
    "v_accvgpr_read_b32 v107, a107\n"
    "v_mfma_f32_16x16x32_bf16 a[116:119], v[172:175], v[68:71], a[116:119]\n"
    "v_fma_f32 v104, v104, v226, s94\n"
    "v_fma_f32 v105, v105, v226, s94\n"
    "v_mfma_f32_16x16x32_bf16 a[112:115], v[168:171], v[32:35], a[112:115]\n"
    "v_fma_f32 v106, v106, v226, s94\n"
    "v_fma_f32 v107, v107, v226, s94\n"
    "v_mfma_f32_16x16x32_bf16 a[112:115], v[172:175], v[64:67], a[112:115]\n"
    "s_waitcnt lgkmcnt(2)\n"
    "v_cvt_pk_bf16_f32 v248, v104, v105\n"
    "v_cvt_pk_bf16_f32 v249, v106, v107\n"
    "v_mfma_f32_16x16x32_bf16 a[112:115], v[176:179], v[232:235], a[112:115]\n"
    "ds_read_b128 v[168:171], v201 offset:40960\n"
    "v_accvgpr_read_b32 v108, a108\n"
    "v_mfma_f32_16x16x32_bf16 a[112:115], v[180:183], v[80:83], a[112:115]\n"
    "v_accvgpr_read_b32 v109, a109\n"
    "v_accvgpr_read_b32 v110, a110\n"
    "v_mfma_f32_16x16x32_bf16 a[116:119], v[176:179], v[236:239], a[116:119]\n"
    "v_accvgpr_read_b32 v111, a111\n"
    "v_fma_f32 v108, v108, v228, s94\n"
    "v_mfma_f32_16x16x32_bf16 a[116:119], v[180:183], v[84:87], a[116:119]\n"
    "v_fma_f32 v109, v109, v228, s94\n"
    "v_fma_f32 v110, v110, v228, s94\n"
    "v_mfma_f32_16x16x32_bf16 a[120:123], v[176:179], v[240:243], a[120:123]\n"
    "ds_read_b128 v[172:175], v201 offset:41984\n"
    "v_fma_f32 v111, v111, v228, s94\n"
    "v_mfma_f32_16x16x32_bf16 a[120:123], v[180:183], v[88:91], a[120:123]\n"
    "v_cvt_pk_bf16_f32 v252, v108, v109\n"
    "v_cvt_pk_bf16_f32 v253, v110, v111\n"
    "v_mfma_f32_16x16x32_bf16 a[124:127], v[176:179], v[244:247], a[124:127]\n"
    "v_mfma_f32_16x16x32_bf16 a[124:127], v[180:183], v[92:95], a[124:127]\n"
    "s_waitcnt lgkmcnt(2)\n"
    "v_accvgpr_read_b32 v96, a112\n"
    "v_accvgpr_read_b32 v97, a113\n"
    "v_mfma_f32_16x16x32_bf16 a[128:131], v[160:163], v[128:131], a[128:131]\n"
    "ds_read_b128 v[176:179], v207 offset:16384\n"
    "v_accvgpr_read_b32 v98, a114\n"
    "v_mfma_f32_16x16x32_bf16 a[128:131], v[164:167], v[144:147], a[128:131]\n"
    "v_accvgpr_read_b32 v99, a115\n"
    "v_fma_f32 v96, v96, v222, s94\n"
    "v_mfma_f32_16x16x32_bf16 a[132:135], v[160:163], v[132:135], a[132:135]\n"
    "v_fma_f32 v97, v97, v222, s94\n"
    "v_fma_f32 v98, v98, v222, s94\n"
    "v_mfma_f32_16x16x32_bf16 a[132:135], v[164:167], v[148:151], a[132:135]\n"
    "v_fma_f32 v99, v99, v222, s94\n"
    "v_cvt_pk_bf16_f32 v186, v96, v97\n"
    "v_mfma_f32_16x16x32_bf16 a[136:139], v[160:163], v[136:139], a[136:139]\n"
    "ds_read_b128 v[180:183], v207 offset:17408\n"
    "v_cvt_pk_bf16_f32 v187, v98, v99\n"
    "v_mfma_f32_16x16x32_bf16 a[136:139], v[164:167], v[152:155], a[136:139]\n"
    "buffer_store_dwordx4 v[184:187], v205, s[28:31], 0 offen offset:192 sc0 sc1\n"
    "v_accvgpr_read_b32 v100, a116\n"
    "v_accvgpr_read_b32 v101, a117\n"
    "v_accvgpr_read_b32 v102, a118\n"
    "v_mfma_f32_16x16x32_bf16 a[140:143], v[160:163], v[140:143], a[140:143]\n"
    "v_accvgpr_read_b32 v103, a119\n"
    "v_fma_f32 v100, v100, v224, s94\n"
    "v_mfma_f32_16x16x32_bf16 a[140:143], v[164:167], v[156:159], a[140:143]\n"
    "s_waitcnt lgkmcnt(2)\n"
    "v_fma_f32 v101, v101, v224, s94\n"
    "v_fma_f32 v102, v102, v224, s94\n"
    "v_mfma_f32_16x16x32_bf16 a[140:143], v[168:171], v[44:47], a[140:143]\n"
    "ds_read_b128 v[160:163], v201 offset:18432\n"
    "v_fma_f32 v103, v103, v224, s94\n"
    "v_mfma_f32_16x16x32_bf16 a[140:143], v[172:175], v[76:79], a[140:143]\n"
    "v_cvt_pk_bf16_f32 v190, v100, v101\n"
    "v_cvt_pk_bf16_f32 v191, v102, v103\n"
    "v_mfma_f32_16x16x32_bf16 a[136:139], v[168:171], v[40:43], a[136:139]\n"
    "buffer_store_dwordx4 v[188:191], v205, s[28:31], s62 offen offset:192 sc0 sc1\n"
    "v_accvgpr_read_b32 v104, a120\n"
    "v_accvgpr_read_b32 v105, a121\n"
    "v_accvgpr_read_b32 v106, a122\n"
    "v_mfma_f32_16x16x32_bf16 a[136:139], v[172:175], v[72:75], a[136:139]\n"
    "v_accvgpr_read_b32 v107, a123\n"
    "v_fma_f32 v104, v104, v226, s94\n"
    "v_mfma_f32_16x16x32_bf16 a[132:135], v[168:171], v[36:39], a[132:135]\n"
    "ds_read_b128 v[164:167], v201 offset:19456\n"
    "v_fma_f32 v105, v105, v226, s94\n"
    "v_mfma_f32_16x16x32_bf16 a[132:135], v[172:175], v[68:71], a[132:135]\n"
    "v_fma_f32 v106, v106, v226, s94\n"
    "v_fma_f32 v107, v107, v226, s94\n"
    "v_mfma_f32_16x16x32_bf16 a[128:131], v[168:171], v[32:35], a[128:131]\n"
    "v_cvt_pk_bf16_f32 v250, v104, v105\n"
    "v_cvt_pk_bf16_f32 v251, v106, v107\n"
    "v_mfma_f32_16x16x32_bf16 a[128:131], v[172:175], v[64:67], a[128:131]\n"
    "s_waitcnt lgkmcnt(2)\n"
    "buffer_store_dwordx4 v[248:251], v205, s[28:31], s63 offen offset:192 sc0 sc1\n"
    "v_accvgpr_read_b32 v108, a124\n"
    "v_accvgpr_read_b32 v109, a125\n"
    "v_accvgpr_read_b32 v110, a126\n"
    "v_mfma_f32_16x16x32_bf16 a[128:131], v[176:179], v[232:235], a[128:131]\n"
    "ds_read_b128 v[168:171], v201 offset:43008\n"
    "v_accvgpr_read_b32 v111, a127\n"
    "v_mfma_f32_16x16x32_bf16 a[128:131], v[180:183], v[80:83], a[128:131]\n"
    "v_fma_f32 v108, v108, v228, s94\n"
    "v_fma_f32 v109, v109, v228, s94\n"
    "v_mfma_f32_16x16x32_bf16 a[132:135], v[176:179], v[236:239], a[132:135]\n"
    "v_fma_f32 v110, v110, v228, s94\n"
    "v_fma_f32 v111, v111, v228, s94\n"
    "v_mfma_f32_16x16x32_bf16 a[132:135], v[180:183], v[84:87], a[132:135]\n"
    "v_cvt_pk_bf16_f32 v254, v108, v109\n"
    "v_cvt_pk_bf16_f32 v255, v110, v111\n"
    "v_mfma_f32_16x16x32_bf16 a[136:139], v[176:179], v[240:243], a[136:139]\n"
    "ds_read_b128 v[172:175], v201 offset:44032\n"
    "v_mfma_f32_16x16x32_bf16 a[136:139], v[180:183], v[88:91], a[136:139]\n"
    "buffer_store_dwordx4 v[252:255], v205, s[28:31], s64 offen offset:192 sc0 sc1\n"
    "v_mfma_f32_16x16x32_bf16 a[140:143], v[176:179], v[244:247], a[140:143]\n"
    "v_mfma_f32_16x16x32_bf16 a[140:143], v[180:183], v[92:95], a[140:143]\n"
    "s_waitcnt lgkmcnt(2)\n"
    "v_accvgpr_read_b32 v96, a128\n"
    "v_accvgpr_read_b32 v97, a129\n"
    "v_mfma_f32_16x16x32_bf16 a[144:147], v[160:163], v[128:131], a[144:147]\n"
    "ds_read_b128 v[176:179], v207 offset:18432\n"
    "v_accvgpr_read_b32 v98, a130\n"
    "v_mfma_f32_16x16x32_bf16 a[144:147], v[164:167], v[144:147], a[144:147]\n"
    "v_accvgpr_read_b32 v99, a131\n"
    "v_fma_f32 v96, v96, v222, s94\n"
    "v_mfma_f32_16x16x32_bf16 a[148:151], v[160:163], v[132:135], a[148:151]\n"
    "v_fma_f32 v97, v97, v222, s94\n"
    "v_fma_f32 v98, v98, v222, s94\n"
    "v_mfma_f32_16x16x32_bf16 a[148:151], v[164:167], v[148:151], a[148:151]\n"
    "v_fma_f32 v99, v99, v222, s94\n"
    "v_cvt_pk_bf16_f32 v48, v96, v97\n"
    "v_mfma_f32_16x16x32_bf16 a[152:155], v[160:163], v[136:139], a[152:155]\n"
    "ds_read_b128 v[180:183], v207 offset:19456\n"
    "v_cvt_pk_bf16_f32 v49, v98, v99\n"
    "v_mfma_f32_16x16x32_bf16 a[152:155], v[164:167], v[152:155], a[152:155]\n"
    "v_accvgpr_read_b32 v100, a132\n"
    "v_accvgpr_read_b32 v101, a133\n"
    "v_mfma_f32_16x16x32_bf16 a[156:159], v[160:163], v[140:143], a[156:159]\n"
    "v_accvgpr_read_b32 v102, a134\n"
    "v_accvgpr_read_b32 v103, a135\n"
    "v_mfma_f32_16x16x32_bf16 a[156:159], v[164:167], v[156:159], a[156:159]\n"
    "s_waitcnt lgkmcnt(2)\n"
    "v_fma_f32 v100, v100, v224, s94\n"
    "v_fma_f32 v101, v101, v224, s94\n"
    "v_mfma_f32_16x16x32_bf16 a[156:159], v[168:171], v[44:47], a[156:159]\n"
    "ds_read_b128 v[160:163], v201 offset:20480\n"
    "v_fma_f32 v102, v102, v224, s94\n"
    "v_mfma_f32_16x16x32_bf16 a[156:159], v[172:175], v[76:79], a[156:159]\n"
    "v_fma_f32 v103, v103, v224, s94\n"
    "v_cvt_pk_bf16_f32 v52, v100, v101\n"
    "v_mfma_f32_16x16x32_bf16 a[152:155], v[168:171], v[40:43], a[152:155]\n"
    "v_cvt_pk_bf16_f32 v53, v102, v103\n"
    "v_accvgpr_read_b32 v104, a136\n"
    "v_mfma_f32_16x16x32_bf16 a[152:155], v[172:175], v[72:75], a[152:155]\n"
    "v_accvgpr_read_b32 v105, a137\n"
    "v_accvgpr_read_b32 v106, a138\n"
    "v_mfma_f32_16x16x32_bf16 a[148:151], v[168:171], v[36:39], a[148:151]\n"
    "ds_read_b128 v[164:167], v201 offset:21504\n"
    "v_accvgpr_read_b32 v107, a139\n"
    "v_mfma_f32_16x16x32_bf16 a[148:151], v[172:175], v[68:71], a[148:151]\n"
    "v_fma_f32 v104, v104, v226, s94\n"
    "v_fma_f32 v105, v105, v226, s94\n"
    "v_mfma_f32_16x16x32_bf16 a[144:147], v[168:171], v[32:35], a[144:147]\n"
    "v_fma_f32 v106, v106, v226, s94\n"
    "v_fma_f32 v107, v107, v226, s94\n"
    "v_mfma_f32_16x16x32_bf16 a[144:147], v[172:175], v[64:67], a[144:147]\n"
    "s_waitcnt lgkmcnt(2)\n"
    "v_cvt_pk_bf16_f32 v56, v104, v105\n"
    "v_cvt_pk_bf16_f32 v57, v106, v107\n"
    "v_mfma_f32_16x16x32_bf16 a[144:147], v[176:179], v[232:235], a[144:147]\n"
    "ds_read_b128 v[168:171], v201 offset:45056\n"
    "v_accvgpr_read_b32 v108, a140\n"
    "v_mfma_f32_16x16x32_bf16 a[144:147], v[180:183], v[80:83], a[144:147]\n"
    "v_accvgpr_read_b32 v109, a141\n"
    "v_accvgpr_read_b32 v110, a142\n"
    "v_mfma_f32_16x16x32_bf16 a[148:151], v[176:179], v[236:239], a[148:151]\n"
    "v_accvgpr_read_b32 v111, a143\n"
    "v_fma_f32 v108, v108, v228, s94\n"
    "v_mfma_f32_16x16x32_bf16 a[148:151], v[180:183], v[84:87], a[148:151]\n"
    "v_fma_f32 v109, v109, v228, s94\n"
    "v_fma_f32 v110, v110, v228, s94\n"
    "v_mfma_f32_16x16x32_bf16 a[152:155], v[176:179], v[240:243], a[152:155]\n"
    "ds_read_b128 v[172:175], v201 offset:46080\n"
    "v_fma_f32 v111, v111, v228, s94\n"
    "v_mfma_f32_16x16x32_bf16 a[152:155], v[180:183], v[88:91], a[152:155]\n"
    "v_cvt_pk_bf16_f32 v60, v108, v109\n"
    "v_cvt_pk_bf16_f32 v61, v110, v111\n"
    "v_mfma_f32_16x16x32_bf16 a[156:159], v[176:179], v[244:247], a[156:159]\n"
    "v_mfma_f32_16x16x32_bf16 a[156:159], v[180:183], v[92:95], a[156:159]\n"
    "s_waitcnt lgkmcnt(2)\n"
    "v_accvgpr_read_b32 v96, a144\n"
    "v_accvgpr_read_b32 v97, a145\n"
    "v_mfma_f32_16x16x32_bf16 a[160:163], v[160:163], v[128:131], a[160:163]\n"
    "ds_read_b128 v[176:179], v207 offset:20480\n"
    "v_accvgpr_read_b32 v98, a146\n"
    "v_mfma_f32_16x16x32_bf16 a[160:163], v[164:167], v[144:147], a[160:163]\n"
    "v_accvgpr_read_b32 v99, a147\n"
    "v_fma_f32 v96, v96, v222, s94\n"
    "v_mfma_f32_16x16x32_bf16 a[164:167], v[160:163], v[132:135], a[164:167]\n"
    "v_fma_f32 v97, v97, v222, s94\n"
    "v_fma_f32 v98, v98, v222, s94\n"
    "v_mfma_f32_16x16x32_bf16 a[164:167], v[164:167], v[148:151], a[164:167]\n"
    "v_fma_f32 v99, v99, v222, s94\n"
    "v_cvt_pk_bf16_f32 v50, v96, v97\n"
    "v_mfma_f32_16x16x32_bf16 a[168:171], v[160:163], v[136:139], a[168:171]\n"
    "ds_read_b128 v[180:183], v207 offset:21504\n"
    "v_cvt_pk_bf16_f32 v51, v98, v99\n"
    "v_mfma_f32_16x16x32_bf16 a[168:171], v[164:167], v[152:155], a[168:171]\n"
    "buffer_store_dwordx4 v[48:51], v205, s[28:31], 0 offen offset:256 sc0 sc1\n"
    "v_accvgpr_read_b32 v100, a148\n"
    "v_accvgpr_read_b32 v101, a149\n"
    "v_accvgpr_read_b32 v102, a150\n"
    "v_mfma_f32_16x16x32_bf16 a[172:175], v[160:163], v[140:143], a[172:175]\n"
    "v_accvgpr_read_b32 v103, a151\n"
    "v_fma_f32 v100, v100, v224, s94\n"
    "v_mfma_f32_16x16x32_bf16 a[172:175], v[164:167], v[156:159], a[172:175]\n"
    "s_waitcnt lgkmcnt(2)\n"
    "v_fma_f32 v101, v101, v224, s94\n"
    "v_fma_f32 v102, v102, v224, s94\n"
    "v_mfma_f32_16x16x32_bf16 a[172:175], v[168:171], v[44:47], a[172:175]\n"
    "ds_read_b128 v[160:163], v201 offset:22528\n"
    "v_fma_f32 v103, v103, v224, s94\n"
    "v_mfma_f32_16x16x32_bf16 a[172:175], v[172:175], v[76:79], a[172:175]\n"
    "v_cvt_pk_bf16_f32 v54, v100, v101\n"
    "v_cvt_pk_bf16_f32 v55, v102, v103\n"
    "v_mfma_f32_16x16x32_bf16 a[168:171], v[168:171], v[40:43], a[168:171]\n"
    "buffer_store_dwordx4 v[52:55], v205, s[28:31], s62 offen offset:256 sc0 sc1\n"
    "v_accvgpr_read_b32 v104, a152\n"
    "v_accvgpr_read_b32 v105, a153\n"
    "v_accvgpr_read_b32 v106, a154\n"
    "v_mfma_f32_16x16x32_bf16 a[168:171], v[172:175], v[72:75], a[168:171]\n"
    "v_accvgpr_read_b32 v107, a155\n"
    "v_fma_f32 v104, v104, v226, s94\n"
    "v_mfma_f32_16x16x32_bf16 a[164:167], v[168:171], v[36:39], a[164:167]\n"
    "ds_read_b128 v[164:167], v201 offset:23552\n"
    "v_fma_f32 v105, v105, v226, s94\n"
    "v_mfma_f32_16x16x32_bf16 a[164:167], v[172:175], v[68:71], a[164:167]\n"
    "v_fma_f32 v106, v106, v226, s94\n"
    "v_fma_f32 v107, v107, v226, s94\n"
    "v_mfma_f32_16x16x32_bf16 a[160:163], v[168:171], v[32:35], a[160:163]\n"
    "v_cvt_pk_bf16_f32 v58, v104, v105\n"
    "v_cvt_pk_bf16_f32 v59, v106, v107\n"
    "v_mfma_f32_16x16x32_bf16 a[160:163], v[172:175], v[64:67], a[160:163]\n"
    "s_waitcnt lgkmcnt(2)\n"
    "buffer_store_dwordx4 v[56:59], v205, s[28:31], s63 offen offset:256 sc0 sc1\n"
    "v_accvgpr_read_b32 v108, a156\n"
    "v_accvgpr_read_b32 v109, a157\n"
    "v_accvgpr_read_b32 v110, a158\n"
    "v_mfma_f32_16x16x32_bf16 a[160:163], v[176:179], v[232:235], a[160:163]\n"
    "ds_read_b128 v[168:171], v201 offset:47104\n"
    "v_accvgpr_read_b32 v111, a159\n"
    "v_mfma_f32_16x16x32_bf16 a[160:163], v[180:183], v[80:83], a[160:163]\n"
    "v_fma_f32 v108, v108, v228, s94\n"
    "v_fma_f32 v109, v109, v228, s94\n"
    "v_mfma_f32_16x16x32_bf16 a[164:167], v[176:179], v[236:239], a[164:167]\n"
    "v_fma_f32 v110, v110, v228, s94\n"
    "v_fma_f32 v111, v111, v228, s94\n"
    "v_mfma_f32_16x16x32_bf16 a[164:167], v[180:183], v[84:87], a[164:167]\n"
    "v_cvt_pk_bf16_f32 v62, v108, v109\n"
    "v_cvt_pk_bf16_f32 v63, v110, v111\n"
    "v_mfma_f32_16x16x32_bf16 a[168:171], v[176:179], v[240:243], a[168:171]\n"
    "ds_read_b128 v[172:175], v201 offset:48128\n"
    "v_mfma_f32_16x16x32_bf16 a[168:171], v[180:183], v[88:91], a[168:171]\n"
    "buffer_store_dwordx4 v[60:63], v205, s[28:31], s64 offen offset:256 sc0 sc1\n"
    "v_mfma_f32_16x16x32_bf16 a[172:175], v[176:179], v[244:247], a[172:175]\n"
    "v_mfma_f32_16x16x32_bf16 a[172:175], v[180:183], v[92:95], a[172:175]\n"
    "s_waitcnt lgkmcnt(2)\n"
    "v_accvgpr_read_b32 v96, a160\n"
    "v_accvgpr_read_b32 v97, a161\n"
    "v_mfma_f32_16x16x32_bf16 a[176:179], v[160:163], v[128:131], a[176:179]\n"
    "ds_read_b128 v[176:179], v207 offset:22528\n"
    "v_accvgpr_read_b32 v98, a162\n"
    "v_mfma_f32_16x16x32_bf16 a[176:179], v[164:167], v[144:147], a[176:179]\n"
    "v_accvgpr_read_b32 v99, a163\n"
    "v_fma_f32 v96, v96, v222, s94\n"
    "v_mfma_f32_16x16x32_bf16 a[180:183], v[160:163], v[132:135], a[180:183]\n"
    "v_fma_f32 v97, v97, v222, s94\n"
    "v_fma_f32 v98, v98, v222, s94\n"
    "v_mfma_f32_16x16x32_bf16 a[180:183], v[164:167], v[148:151], a[180:183]\n"
    "v_fma_f32 v99, v99, v222, s94\n"
    "v_cvt_pk_bf16_f32 v184, v96, v97\n"
    "v_mfma_f32_16x16x32_bf16 a[184:187], v[160:163], v[136:139], a[184:187]\n"
    "ds_read_b128 v[180:183], v207 offset:23552\n"
    "v_cvt_pk_bf16_f32 v185, v98, v99\n"
    "v_mfma_f32_16x16x32_bf16 a[184:187], v[164:167], v[152:155], a[184:187]\n"
    "v_accvgpr_read_b32 v100, a164\n"
    "v_accvgpr_read_b32 v101, a165\n"
    "v_mfma_f32_16x16x32_bf16 a[188:191], v[160:163], v[140:143], a[188:191]\n"
    "v_accvgpr_read_b32 v102, a166\n"
    "v_accvgpr_read_b32 v103, a167\n"
    "v_mfma_f32_16x16x32_bf16 a[188:191], v[164:167], v[156:159], a[188:191]\n"
    "s_waitcnt lgkmcnt(2)\n"
    "v_fma_f32 v100, v100, v224, s94\n"
    "v_fma_f32 v101, v101, v224, s94\n"
    "v_mfma_f32_16x16x32_bf16 a[188:191], v[168:171], v[44:47], a[188:191]\n"
    "v_fma_f32 v102, v102, v224, s94\n"
    "v_fma_f32 v103, v103, v224, s94\n"
    "v_mfma_f32_16x16x32_bf16 a[188:191], v[172:175], v[76:79], a[188:191]\n"
    "v_cvt_pk_bf16_f32 v188, v100, v101\n"
    "v_cvt_pk_bf16_f32 v189, v102, v103\n"
    "v_mfma_f32_16x16x32_bf16 a[184:187], v[168:171], v[40:43], a[184:187]\n"
    "v_accvgpr_read_b32 v104, a168\n"
    "v_accvgpr_read_b32 v105, a169\n"
    "v_mfma_f32_16x16x32_bf16 a[184:187], v[172:175], v[72:75], a[184:187]\n"
    "v_accvgpr_read_b32 v106, a170\n"
    "v_accvgpr_read_b32 v107, a171\n"
    "v_mfma_f32_16x16x32_bf16 a[180:183], v[168:171], v[36:39], a[180:183]\n"
    "v_fma_f32 v104, v104, v226, s94\n"
    "v_fma_f32 v105, v105, v226, s94\n"
    "v_mfma_f32_16x16x32_bf16 a[180:183], v[172:175], v[68:71], a[180:183]\n"
    "v_fma_f32 v106, v106, v226, s94\n"
    "v_fma_f32 v107, v107, v226, s94\n"
    "v_mfma_f32_16x16x32_bf16 a[176:179], v[168:171], v[32:35], a[176:179]\n"
    "v_cvt_pk_bf16_f32 v248, v104, v105\n"
    "v_cvt_pk_bf16_f32 v249, v106, v107\n"
    "v_mfma_f32_16x16x32_bf16 a[176:179], v[172:175], v[64:67], a[176:179]\n"
    "s_waitcnt lgkmcnt(0)\n"
    "v_accvgpr_read_b32 v108, a172\n"
    "v_accvgpr_read_b32 v109, a173\n"
    "v_mfma_f32_16x16x32_bf16 a[176:179], v[176:179], v[232:235], a[176:179]\n"
    "v_accvgpr_read_b32 v110, a174\n"
    "v_accvgpr_read_b32 v111, a175\n"
    "v_mfma_f32_16x16x32_bf16 a[176:179], v[180:183], v[80:83], a[176:179]\n"
    "v_fma_f32 v108, v108, v228, s94\n"
    "v_fma_f32 v109, v109, v228, s94\n"
    "v_mfma_f32_16x16x32_bf16 a[180:183], v[176:179], v[236:239], a[180:183]\n"
    "v_fma_f32 v110, v110, v228, s94\n"
    "v_fma_f32 v111, v111, v228, s94\n"
    "v_mfma_f32_16x16x32_bf16 a[180:183], v[180:183], v[84:87], a[180:183]\n"
    "v_cvt_pk_bf16_f32 v252, v108, v109\n"
    "v_cvt_pk_bf16_f32 v253, v110, v111\n"
    "v_mfma_f32_16x16x32_bf16 a[184:187], v[176:179], v[240:243], a[184:187]\n"
    "v_mfma_f32_16x16x32_bf16 a[184:187], v[180:183], v[88:91], a[184:187]\n"
    "v_mfma_f32_16x16x32_bf16 a[188:191], v[176:179], v[244:247], a[188:191]\n"
    "v_mfma_f32_16x16x32_bf16 a[188:191], v[180:183], v[92:95], a[188:191]\n"
    "v_accvgpr_read_b32 v96, a176\n"
    "v_accvgpr_read_b32 v97, a177\n"
    "v_accvgpr_read_b32 v98, a178\n"
    "v_accvgpr_read_b32 v99, a179\n"
    "v_fma_f32 v96, v96, v222, s94\n"
    "v_fma_f32 v97, v97, v222, s94\n"
    "v_fma_f32 v98, v98, v222, s94\n"
    "v_fma_f32 v99, v99, v222, s94\n"
    "v_cvt_pk_bf16_f32 v186, v96, v97\n"
    "v_cvt_pk_bf16_f32 v187, v98, v99\n"
    "buffer_store_dwordx4 v[184:187], v205, s[28:31], 0 offen offset:320 sc0 sc1\n"
    "v_accvgpr_read_b32 v100, a180\n"
    "v_accvgpr_read_b32 v101, a181\n"
    "v_accvgpr_read_b32 v102, a182\n"
    "v_accvgpr_read_b32 v103, a183\n"
    "v_fma_f32 v100, v100, v224, s94\n"
    "v_fma_f32 v101, v101, v224, s94\n"
    "v_fma_f32 v102, v102, v224, s94\n"
    "v_fma_f32 v103, v103, v224, s94\n"
    "v_cvt_pk_bf16_f32 v190, v100, v101\n"
    "v_cvt_pk_bf16_f32 v191, v102, v103\n"
    "buffer_store_dwordx4 v[188:191], v205, s[28:31], s62 offen offset:320 sc0 sc1\n"
    "v_accvgpr_read_b32 v104, a184\n"
    "v_accvgpr_read_b32 v105, a185\n"
    "v_accvgpr_read_b32 v106, a186\n"
    "v_accvgpr_read_b32 v107, a187\n"
    "v_fma_f32 v104, v104, v226, s94\n"
    "v_fma_f32 v105, v105, v226, s94\n"
    "v_fma_f32 v106, v106, v226, s94\n"
    "v_fma_f32 v107, v107, v226, s94\n"
    "v_cvt_pk_bf16_f32 v250, v104, v105\n"
    "v_cvt_pk_bf16_f32 v251, v106, v107\n"
    "buffer_store_dwordx4 v[248:251], v205, s[28:31], s63 offen offset:320 sc0 sc1\n"
    "v_accvgpr_read_b32 v108, a188\n"
    "v_accvgpr_read_b32 v109, a189\n"
    "v_accvgpr_read_b32 v110, a190\n"
    "v_accvgpr_read_b32 v111, a191\n"
    "v_fma_f32 v108, v108, v228, s94\n"
    "v_fma_f32 v109, v109, v228, s94\n"
    "v_fma_f32 v110, v110, v228, s94\n"
    "v_fma_f32 v111, v111, v228, s94\n"
    "v_cvt_pk_bf16_f32 v254, v108, v109\n"
    "v_cvt_pk_bf16_f32 v255, v110, v111\n"
    "buffer_store_dwordx4 v[252:255], v205, s[28:31], s64 offen offset:320 sc0 sc1\n"
    "v_add_f32 v196, v112, v116\n"
    "v_add_f32 v197, v120, v124\n"
    "v_add_f32 v196, v196, v197\n"
    "v_cmp_ngt_f32 vcc, 0x45800000, v196\n"
    "s_barrier\n"
    "s_cbranch_vccnz LSLOW\n"
    "LFIN:\n"
    "s_branch LEND\n"
    "LSPRE0:\n"
    "LSPIN0:\n"
    "s_sleep 1\n"
    "buffer_load_dwordx2 v[218:219], v213, s[44:47], 0 offen sc1\n"
    "s_waitcnt vmcnt(0)\n"
    "s_add_u32 s73, s73, 1\n"
    "s_cmp_gt_u32 s73, 200000\n"
    "s_cbranch_scc1 LSPB0\n"
    "v_cmp_ne_u32 s[80:81], s67, v219\n"
    "v_cmp_gt_u32 vcc, s66, v218\n"
    "s_or_b64 vcc, vcc, s[80:81]\n"
    "s_cbranch_vccnz LSPIN0\n"
    "s_branch LSPB0\n"
    "LSPRE1:\n"
    "LSPIN1:\n"
    "s_sleep 1\n"
    "buffer_load_dwordx2 v[218:219], v213, s[44:47], 0 offen sc1\n"
    "s_waitcnt vmcnt(0)\n"
    "s_add_u32 s73, s73, 1\n"
    "s_cmp_gt_u32 s73, 200000\n"
    "s_cbranch_scc1 LSPB1\n"
    "v_cmp_ne_u32 s[80:81], s67, v219\n"
    "v_cmp_gt_u32 vcc, s66, v218\n"
    "s_or_b64 vcc, vcc, s[80:81]\n"
    "s_cbranch_vccnz LSPIN1\n"
    "s_branch LSPB1\n"
    "LSPRE2:\n"
    "LSPIN2:\n"
    "s_sleep 1\n"
    "buffer_load_dwordx2 v[218:219], v213, s[44:47], 0 offen sc1\n"
    "s_waitcnt vmcnt(0)\n"
    "s_add_u32 s73, s73, 1\n"
    "s_cmp_gt_u32 s73, 200000\n"
    "s_cbranch_scc1 LSPB2\n"
    "v_cmp_ne_u32 s[80:81], s67, v219\n"
    "v_cmp_gt_u32 vcc, s66, v218\n"
    "s_or_b64 vcc, vcc, s[80:81]\n"
    "s_cbranch_vccnz LSPIN2\n"
    "s_branch LSPB2\n"
    "LSPRE3:\n"
    "LSPIN3:\n"
    "s_sleep 1\n"
    "buffer_load_dwordx2 v[218:219], v213, s[44:47], 0 offen sc1\n"
    "s_waitcnt vmcnt(0)\n"
    "s_add_u32 s73, s73, 1\n"
    "s_cmp_gt_u32 s73, 200000\n"
    "s_cbranch_scc1 LSPB3\n"
    "v_cmp_ne_u32 s[80:81], s67, v219\n"
    "v_cmp_gt_u32 vcc, s66, v218\n"
    "s_or_b64 vcc, vcc, s[80:81]\n"
    "s_cbranch_vccnz LSPIN3\n"
    "s_branch LSPB3\n"
    "LSPRE4:\n"
    "LSPIN4:\n"
    "s_sleep 1\n"
    "buffer_load_dwordx2 v[218:219], v213, s[44:47], 0 offen sc1\n"
    "s_waitcnt vmcnt(0)\n"
    "s_add_u32 s73, s73, 1\n"
    "s_cmp_gt_u32 s73, 200000\n"
    "s_cbranch_scc1 LSPB4\n"
    "v_cmp_ne_u32 s[80:81], s67, v219\n"
    "v_cmp_gt_u32 vcc, s66, v218\n"
    "s_or_b64 vcc, vcc, s[80:81]\n"
    "s_cbranch_vccnz LSPIN4\n"
    "s_branch LSPB4\n"
    "LSPRE5:\n"
    "LSPIN5:\n"
    "s_sleep 1\n"
    "buffer_load_dwordx2 v[218:219], v213, s[44:47], 0 offen sc1\n"
    "s_waitcnt vmcnt(0)\n"
    "s_add_u32 s73, s73, 1\n"
    "s_cmp_gt_u32 s73, 200000\n"
    "s_cbranch_scc1 LSPB5\n"
    "v_cmp_ne_u32 s[80:81], s67, v219\n"
    "v_cmp_gt_u32 vcc, s66, v218\n"
    "s_or_b64 vcc, vcc, s[80:81]\n"
    "s_cbranch_vccnz LSPIN5\n"
    "s_branch LSPB5\n"
    "LSLOW:\n"
    "s_waitcnt vmcnt(0)\n"
    "s_nop 15\n"
    "s_mul_i32 s62, s58, 0x9000\n"
    "v_add_u32 v205, s62, v200\n"
    "s_mov_b32 s63, 0\n"
    "v_mov_b32 v128, 0\n"
    "v_mov_b32 v129, 0\n"
    "v_mov_b32 v130, 0\n"
    "v_mov_b32 v131, 0\n"
    "v_mov_b32 v96, 0xff61b1e6\n"
    "v_mov_b32 v100, 0xff61b1e6\n"
    "v_mov_b32 v104, 0xff61b1e6\n"
    "v_mov_b32 v108, 0xff61b1e6\n"
    "v_mfma_f32_16x16x32_bf16 a[0:3], v[128:131], v[128:131], 0\n"
    "v_mfma_f32_16x16x32_bf16 a[4:7], v[128:131], v[128:131], 0\n"
    "v_mfma_f32_16x16x32_bf16 a[8:11], v[128:131], v[128:131], 0\n"
    "v_mfma_f32_16x16x32_bf16 a[12:15], v[128:131], v[128:131], 0\n"
    "v_mfma_f32_16x16x32_bf16 a[16:19], v[128:131], v[128:131], 0\n"
    "v_mfma_f32_16x16x32_bf16 a[20:23], v[128:131], v[128:131], 0\n"
    "v_mfma_f32_16x16x32_bf16 a[24:27], v[128:131], v[128:131], 0\n"
    "v_mfma_f32_16x16x32_bf16 a[28:31], v[128:131], v[128:131], 0\n"
    "v_mfma_f32_16x16x32_bf16 a[32:35], v[128:131], v[128:131], 0\n"
    "v_mfma_f32_16x16x32_bf16 a[36:39], v[128:131], v[128:131], 0\n"
    "v_mfma_f32_16x16x32_bf16 a[40:43], v[128:131], v[128:131], 0\n"
    "v_mfma_f32_16x16x32_bf16 a[44:47], v[128:131], v[128:131], 0\n"
    "v_mfma_f32_16x16x32_bf16 a[48:51], v[128:131], v[128:131], 0\n"
    "v_mfma_f32_16x16x32_bf16 a[52:55], v[128:131], v[128:131], 0\n"
    "v_mfma_f32_16x16x32_bf16 a[56:59], v[128:131], v[128:131], 0\n"
    "v_mfma_f32_16x16x32_bf16 a[60:63], v[128:131], v[128:131], 0\n"
    "v_mfma_f32_16x16x32_bf16 a[64:67], v[128:131], v[128:131], 0\n"
    "v_mfma_f32_16x16x32_bf16 a[68:71], v[128:131], v[128:131], 0\n"
    "v_mfma_f32_16x16x32_bf16 a[72:75], v[128:131], v[128:131], 0\n"
    "v_mfma_f32_16x16x32_bf16 a[76:79], v[128:131], v[128:131], 0\n"
    "v_mfma_f32_16x16x32_bf16 a[80:83], v[128:131], v[128:131], 0\n"
    "v_mfma_f32_16x16x32_bf16 a[84:87], v[128:131], v[128:131], 0\n"
    "v_mfma_f32_16x16x32_bf16 a[88:91], v[128:131], v[128:131], 0\n"
    "v_mfma_f32_16x16x32_bf16 a[92:95], v[128:131], v[128:131], 0\n"
    "v_mfma_f32_16x16x32_bf16 a[96:99], v[128:131], v[128:131], 0\n"
    "v_mfma_f32_16x16x32_bf16 a[100:103], v[128:131], v[128:131], 0\n"
    "v_mfma_f32_16x16x32_bf16 a[104:107], v[128:131], v[128:131], 0\n"
    "v_mfma_f32_16x16x32_bf16 a[108:111], v[128:131], v[128:131], 0\n"
    "v_mfma_f32_16x16x32_bf16 a[112:115], v[128:131], v[128:131], 0\n"
    "v_mfma_f32_16x16x32_bf16 a[116:119], v[128:131], v[128:131], 0\n"
    "v_mfma_f32_16x16x32_bf16 a[120:123], v[128:131], v[128:131], 0\n"
    "v_mfma_f32_16x16x32_bf16 a[124:127], v[128:131], v[128:131], 0\n"
    "v_mfma_f32_16x16x32_bf16 a[128:131], v[128:131], v[128:131], 0\n"
    "v_mfma_f32_16x16x32_bf16 a[132:135], v[128:131], v[128:131], 0\n"
    "v_mfma_f32_16x16x32_bf16 a[136:139], v[128:131], v[128:131], 0\n"
    "v_mfma_f32_16x16x32_bf16 a[140:143], v[128:131], v[128:131], 0\n"
    "v_mfma_f32_16x16x32_bf16 a[144:147], v[128:131], v[128:131], 0\n"
    "v_mfma_f32_16x16x32_bf16 a[148:151], v[128:131], v[128:131], 0\n"
    "v_mfma_f32_16x16x32_bf16 a[152:155], v[128:131], v[128:131], 0\n"
    "v_mfma_f32_16x16x32_bf16 a[156:159], v[128:131], v[128:131], 0\n"
    "v_mfma_f32_16x16x32_bf16 a[160:163], v[128:131], v[128:131], 0\n"
    "v_mfma_f32_16x16x32_bf16 a[164:167], v[128:131], v[128:131], 0\n"
    "v_mfma_f32_16x16x32_bf16 a[168:171], v[128:131], v[128:131], 0\n"
    "v_mfma_f32_16x16x32_bf16 a[172:175], v[128:131], v[128:131], 0\n"
    "v_mfma_f32_16x16x32_bf16 a[176:179], v[128:131], v[128:131], 0\n"
    "v_mfma_f32_16x16x32_bf16 a[180:183], v[128:131], v[128:131], 0\n"
    "v_mfma_f32_16x16x32_bf16 a[184:187], v[128:131], v[128:131], 0\n"
    "v_mfma_f32_16x16x32_bf16 a[188:191], v[128:131], v[128:131], 0\n"
    "v_mfma_f32_16x16x32_bf16 v[112:115], v[128:131], v[128:131], 0\n"
    "v_mfma_f32_16x16x32_bf16 v[116:119], v[128:131], v[128:131], 0\n"
    "v_mfma_f32_16x16x32_bf16 v[120:123], v[128:131], v[128:131], 0\n"
    "v_mfma_f32_16x16x32_bf16 v[124:127], v[128:131], v[128:131], 0\n"
    "s_nop 15\n"
    "LSLOOP:\n"
    "s_add_u32 s78, s63, 0\n"
    "s_add_u32 m0, s62, 0\n"
    "s_nop 0\n"
    "buffer_load_dwordx4 v200, s[36:39], s78 offen sc1 lds\n"
    "buffer_load_dwordx4 v200, s[36:39], s78 offen offset:1024 sc1 lds\n"
    "buffer_load_dwordx4 v200, s[36:39], s78 offen offset:2048 sc1 lds\n"
    "buffer_load_dwordx4 v200, s[36:39], s78 offen offset:3072 sc1 lds\n"
    "s_add_u32 s78, s63, 0x1000\n"
    "s_add_u32 m0, s62, 0x1000\n"
    "s_nop 0\n"
    "buffer_load_dwordx4 v200, s[36:39], s78 offen sc1 lds\n"
    "buffer_load_dwordx4 v200, s[36:39], s78 offen offset:1024 sc1 lds\n"
    "buffer_load_dwordx4 v200, s[36:39], s78 offen offset:2048 sc1 lds\n"
    "buffer_load_dwordx4 v200, s[36:39], s78 offen offset:3072 sc1 lds\n"
    "s_add_u32 s78, s63, 0x2000\n"
    "s_add_u32 m0, s62, 0x2000\n"
    "s_nop 0\n"
    "buffer_load_dwordx4 v200, s[36:39], s78 offen sc1 lds\n"
    "buffer_load_dwordx4 v200, s[36:39], s78 offen offset:1024 sc1 lds\n"
    "buffer_load_dwordx4 v200, s[36:39], s78 offen offset:2048 sc1 lds\n"
    "buffer_load_dwordx4 v200, s[36:39], s78 offen offset:3072 sc1 lds\n"
    "s_add_u32 s78, s63, 0\n"
    "s_add_u32 m0, s62, 0x3000\n"
    "s_nop 0\n"
    "buffer_load_dwordx4 v200, s[40:43], s78 offen sc1 lds\n"
    "buffer_load_dwordx4 v200, s[40:43], s78 offen offset:1024 sc1 lds\n"
    "buffer_load_dwordx4 v200, s[40:43], s78 offen offset:2048 sc1 lds\n"
    "buffer_load_dwordx4 v200, s[40:43], s78 offen offset:3072 sc1 lds\n"
    "s_add_u32 s78, s63, 0x1000\n"
    "s_add_u32 m0, s62, 0x4000\n"
    "s_nop 0\n"
    "buffer_load_dwordx4 v200, s[40:43], s78 offen sc1 lds\n"
    "buffer_load_dwordx4 v200, s[40:43], s78 offen offset:1024 sc1 lds\n"
    "buffer_load_dwordx4 v200, s[40:43], s78 offen offset:2048 sc1 lds\n"
    "buffer_load_dwordx4 v200, s[40:43], s78 offen offset:3072 sc1 lds\n"
    "s_add_u32 s78, s63, 0x2000\n"
    "s_add_u32 m0, s62, 0x5000\n"
    "s_nop 0\n"
    "buffer_load_dwordx4 v200, s[40:43], s78 offen sc1 lds\n"
    "buffer_load_dwordx4 v200, s[40:43], s78 offen offset:1024 sc1 lds\n"
    "buffer_load_dwordx4 v200, s[40:43], s78 offen offset:2048 sc1 lds\n"
    "buffer_load_dwordx4 v200, s[40:43], s78 offen offset:3072 sc1 lds\n"
    "s_add_u32 s78, s63, 0x3000\n"
    "s_add_u32 m0, s62, 0x6000\n"
    "s_nop 0\n"
    "buffer_load_dwordx4 v200, s[40:43], s78 offen sc1 lds\n"
    "buffer_load_dwordx4 v200, s[40:43], s78 offen offset:1024 sc1 lds\n"
    "buffer_load_dwordx4 v200, s[40:43], s78 offen offset:2048 sc1 lds\n"
    "buffer_load_dwordx4 v200, s[40:43], s78 offen offset:3072 sc1 lds\n"
    "s_add_u32 s78, s63, 0x4000\n"
    "s_add_u32 m0, s62, 0x7000\n"
    "s_nop 0\n"
    "buffer_load_dwordx4 v200, s[40:43], s78 offen sc1 lds\n"
    "buffer_load_dwordx4 v200, s[40:43], s78 offen offset:1024 sc1 lds\n"
    "buffer_load_dwordx4 v200, s[40:43], s78 offen offset:2048 sc1 lds\n"
    "buffer_load_dwordx4 v200, s[40:43], s78 offen offset:3072 sc1 lds\n"
    "s_add_u32 s78, s63, 0x5000\n"
    "s_add_u32 m0, s62, 0x8000\n"
    "s_nop 0\n"
    "buffer_load_dwordx4 v200, s[40:43], s78 offen sc1 lds\n"
    "buffer_load_dwordx4 v200, s[40:43], s78 offen offset:1024 sc1 lds\n"
    "buffer_load_dwordx4 v200, s[40:43], s78 offen offset:2048 sc1 lds\n"
    "buffer_load_dwordx4 v200, s[40:43], s78 offen offset:3072 sc1 lds\n"
    "s_waitcnt vmcnt(0)\n"
    "ds_read_b128 v[160:163], v205\n"
    "ds_read_b128 v[164:167], v205 offset:1024\n"
    "s_waitcnt lgkmcnt(1)\n"
    "v_mfma_f32_16x16x32_bf16 v[32:35], v[160:163], a[192:195], 0\n"
    "s_waitcnt lgkmcnt(0)\n"
    "v_mfma_f32_16x16x32_bf16 v[32:35], v[164:167], a[196:199], v[32:35]\n"
    "v_mfma_f32_16x16x32_bf16 v[36:39], v[164:167], a[220:223], 0\n"
    "v_mfma_f32_16x16x32_bf16 v[36:39], v[160:163], a[216:219], v[36:39]\n"
    "v_mfma_f32_16x16x32_bf16 v[40:43], v[160:163], a[240:243], 0\n"
    "v_mfma_f32_16x16x32_bf16 v[40:43], v[164:167], a[244:247], v[40:43]\n"
    "v_mfma_f32_16x16x32_bf16 v[44:47], v[164:167], v[12:15], 0\n"
    "v_mfma_f32_16x16x32_bf16 v[44:47], v[160:163], v[8:11], v[44:47]\n"
    "ds_read_b128 v[168:171], v205 offset:2048\n"
    "ds_read_b128 v[172:175], v205 offset:3072\n"
    "s_waitcnt lgkmcnt(1)\n"
    "v_mfma_f32_16x16x32_bf16 v[44:47], v[168:171], v[16:19], v[44:47]\n"
    "s_waitcnt lgkmcnt(0)\n"
    "v_mfma_f32_16x16x32_bf16 v[44:47], v[172:175], v[20:23], v[44:47]\n"
    "v_mfma_f32_16x16x32_bf16 v[40:43], v[172:175], a[252:255], v[40:43]\n"
    "v_mfma_f32_16x16x32_bf16 v[40:43], v[168:171], a[248:251], v[40:43]\n"
    "v_mfma_f32_16x16x32_bf16 v[36:39], v[168:171], a[224:227], v[36:39]\n"
    "v_mfma_f32_16x16x32_bf16 v[36:39], v[172:175], a[228:231], v[36:39]\n"
    "v_mfma_f32_16x16x32_bf16 v[32:35], v[172:175], a[204:207], v[32:35]\n"
    "v_mfma_f32_16x16x32_bf16 v[32:35], v[168:171], a[200:203], v[32:35]\n"
    "ds_read_b128 v[176:179], v205 offset:4096\n"
    "ds_read_b128 v[180:183], v205 offset:5120\n"
    "s_waitcnt lgkmcnt(1)\n"
    "v_mfma_f32_16x16x32_bf16 v[32:35], v[176:179], a[208:211], v[32:35]\n"
    "s_waitcnt lgkmcnt(0)\n"
    "v_mfma_f32_16x16x32_bf16 v[32:35], v[180:183], a[212:215], v[32:35]\n"
    "v_mfma_f32_16x16x32_bf16 v[36:39], v[180:183], a[236:239], v[36:39]\n"
    "v_mfma_f32_16x16x32_bf16 v[36:39], v[176:179], a[232:235], v[36:39]\n"
    "v_mfma_f32_16x16x32_bf16 v[40:43], v[176:179], v[0:3], v[40:43]\n"
    "v_mfma_f32_16x16x32_bf16 v[40:43], v[180:183], v[4:7], v[40:43]\n"
    "v_mfma_f32_16x16x32_bf16 v[44:47], v[180:183], v[28:31], v[44:47]\n"
    "v_mfma_f32_16x16x32_bf16 v[44:47], v[176:179], v[24:27], v[44:47]\n"
    "ds_read_b128 v[160:163], v205 offset:10240\n"
    "ds_read_b128 v[164:167], v205 offset:11264\n"
    "s_waitcnt lgkmcnt(1)\n"
    "v_mfma_f32_16x16x32_bf16 v[60:63], v[160:163], v[24:27], 0\n"
    "s_waitcnt lgkmcnt(0)\n"
    "v_mfma_f32_16x16x32_bf16 v[60:63], v[164:167], v[28:31], v[60:63]\n"
    "v_mfma_f32_16x16x32_bf16 v[56:59], v[164:167], v[4:7], 0\n"
    "v_mfma_f32_16x16x32_bf16 v[56:59], v[160:163], v[0:3], v[56:59]\n"
    "v_mfma_f32_16x16x32_bf16 v[52:55], v[160:163], a[232:235], 0\n"
    "v_mfma_f32_16x16x32_bf16 v[52:55], v[164:167], a[236:239], v[52:55]\n"
    "v_mfma_f32_16x16x32_bf16 v[48:51], v[164:167], a[212:215], 0\n"
    "v_mfma_f32_16x16x32_bf16 v[48:51], v[160:163], a[208:211], v[48:51]\n"
    "ds_read_b128 v[168:171], v205 offset:8192\n"
    "ds_read_b128 v[172:175], v205 offset:9216\n"
    "s_waitcnt lgkmcnt(1)\n"
    "v_mfma_f32_16x16x32_bf16 v[48:51], v[168:171], a[200:203], v[48:51]\n"
    "s_waitcnt lgkmcnt(0)\n"
    "v_mfma_f32_16x16x32_bf16 v[48:51], v[172:175], a[204:207], v[48:51]\n"
    "v_mfma_f32_16x16x32_bf16 v[52:55], v[172:175], a[228:231], v[52:55]\n"
    "v_mfma_f32_16x16x32_bf16 v[52:55], v[168:171], a[224:227], v[52:55]\n"
    "v_mfma_f32_16x16x32_bf16 v[56:59], v[168:171], a[248:251], v[56:59]\n"
    "v_mfma_f32_16x16x32_bf16 v[56:59], v[172:175], a[252:255], v[56:59]\n"
    "v_mfma_f32_16x16x32_bf16 v[60:63], v[172:175], v[20:23], v[60:63]\n"
    "v_mfma_f32_16x16x32_bf16 v[60:63], v[168:171], v[16:19], v[60:63]\n"
    "ds_read_b128 v[176:179], v205 offset:6144\n"
    "ds_read_b128 v[180:183], v205 offset:7168\n"
    "s_waitcnt lgkmcnt(1)\n"
    "v_mfma_f32_16x16x32_bf16 v[60:63], v[176:179], v[8:11], v[60:63]\n"
    "s_waitcnt lgkmcnt(0)\n"
    "v_mfma_f32_16x16x32_bf16 v[60:63], v[180:183], v[12:15], v[60:63]\n"
    "v_mfma_f32_16x16x32_bf16 v[56:59], v[180:183], a[244:247], v[56:59]\n"
    "v_mfma_f32_16x16x32_bf16 v[56:59], v[176:179], a[240:243], v[56:59]\n"
    "v_mfma_f32_16x16x32_bf16 v[52:55], v[176:179], a[216:219], v[52:55]\n"
    "v_mfma_f32_16x16x32_bf16 v[52:55], v[180:183], a[220:223], v[52:55]\n"
    "v_mfma_f32_16x16x32_bf16 v[48:51], v[180:183], a[196:199], v[48:51]\n"
    "v_mfma_f32_16x16x32_bf16 v[48:51], v[176:179], a[192:195], v[48:51]\n"
    "s_add_u32 s78, s63, 0x3000\n"
    "s_add_u32 m0, s62, 0\n"
    "s_nop 0\n"
    "buffer_load_dwordx4 v200, s[36:39], s78 offen sc1 lds\n"
    "buffer_load_dwordx4 v200, s[36:39], s78 offen offset:1024 sc1 lds\n"
    "buffer_load_dwordx4 v200, s[36:39], s78 offen offset:2048 sc1 lds\n"
    "buffer_load_dwordx4 v200, s[36:39], s78 offen offset:3072 sc1 lds\n"
    "s_add_u32 s78, s63, 0x4000\n"
    "s_add_u32 m0, s62, 0x1000\n"
    "s_nop 0\n"
    "buffer_load_dwordx4 v200, s[36:39], s78 offen sc1 lds\n"
    "buffer_load_dwordx4 v200, s[36:39], s78 offen offset:1024 sc1 lds\n"
    "buffer_load_dwordx4 v200, s[36:39], s78 offen offset:2048 sc1 lds\n"
    "buffer_load_dwordx4 v200, s[36:39], s78 offen offset:3072 sc1 lds\n"
    "s_add_u32 s78, s63, 0x5000\n"
    "s_add_u32 m0, s62, 0x2000\n"
    "s_nop 0\n"
    "buffer_load_dwordx4 v200, s[36:39], s78 offen sc1 lds\n"
    "buffer_load_dwordx4 v200, s[36:39], s78 offen offset:1024 sc1 lds\n"
    "buffer_load_dwordx4 v200, s[36:39], s78 offen offset:2048 sc1 lds\n"
    "buffer_load_dwordx4 v200, s[36:39], s78 offen offset:3072 sc1 lds\n"
    "s_waitcnt vmcnt(0)\n"
    "ds_read_b128 v[160:163], v205\n"
    "ds_read_b128 v[164:167], v205 offset:1024\n"
    "s_waitcnt lgkmcnt(1)\n"
    "v_mfma_f32_16x16x32_bf16 v[64:67], v[160:163], a[192:195], 0\n"
    "s_waitcnt lgkmcnt(0)\n"
    "v_mfma_f32_16x16x32_bf16 v[64:67], v[164:167], a[196:199], v[64:67]\n"
    "v_mfma_f32_16x16x32_bf16 v[68:71], v[164:167], a[220:223], 0\n"
    "v_mfma_f32_16x16x32_bf16 v[68:71], v[160:163], a[216:219], v[68:71]\n"
    "v_mfma_f32_16x16x32_bf16 v[72:75], v[160:163], a[240:243], 0\n"
    "v_mfma_f32_16x16x32_bf16 v[72:75], v[164:167], a[244:247], v[72:75]\n"
    "v_mfma_f32_16x16x32_bf16 v[76:79], v[164:167], v[12:15], 0\n"
    "v_mfma_f32_16x16x32_bf16 v[76:79], v[160:163], v[8:11], v[76:79]\n"
    "ds_read_b128 v[168:171], v205 offset:2048\n"
    "ds_read_b128 v[172:175], v205 offset:3072\n"
    "s_waitcnt lgkmcnt(1)\n"
    "v_mfma_f32_16x16x32_bf16 v[76:79], v[168:171], v[16:19], v[76:79]\n"
    "s_waitcnt lgkmcnt(0)\n"
    "v_mfma_f32_16x16x32_bf16 v[76:79], v[172:175], v[20:23], v[76:79]\n"
    "v_mfma_f32_16x16x32_bf16 v[72:75], v[172:175], a[252:255], v[72:75]\n"
    "v_mfma_f32_16x16x32_bf16 v[72:75], v[168:171], a[248:251], v[72:75]\n"
    "v_mfma_f32_16x16x32_bf16 v[68:71], v[168:171], a[224:227], v[68:71]\n"
    "v_mfma_f32_16x16x32_bf16 v[68:71], v[172:175], a[228:231], v[68:71]\n"
    "v_mfma_f32_16x16x32_bf16 v[64:67], v[172:175], a[204:207], v[64:67]\n"
    "v_mfma_f32_16x16x32_bf16 v[64:67], v[168:171], a[200:203], v[64:67]\n"
    "ds_read_b128 v[176:179], v205 offset:4096\n"
    "ds_read_b128 v[180:183], v205 offset:5120\n"
    "s_waitcnt lgkmcnt(1)\n"
    "v_mfma_f32_16x16x32_bf16 v[64:67], v[176:179], a[208:211], v[64:67]\n"
    "s_waitcnt lgkmcnt(0)\n"
    "v_mfma_f32_16x16x32_bf16 v[64:67], v[180:183], a[212:215], v[64:67]\n"
    "v_mfma_f32_16x16x32_bf16 v[68:71], v[180:183], a[236:239], v[68:71]\n"
    "v_mfma_f32_16x16x32_bf16 v[68:71], v[176:179], a[232:235], v[68:71]\n"
    "v_mfma_f32_16x16x32_bf16 v[72:75], v[176:179], v[0:3], v[72:75]\n"
    "v_mfma_f32_16x16x32_bf16 v[72:75], v[180:183], v[4:7], v[72:75]\n"
    "v_mfma_f32_16x16x32_bf16 v[76:79], v[180:183], v[28:31], v[76:79]\n"
    "v_mfma_f32_16x16x32_bf16 v[76:79], v[176:179], v[24:27], v[76:79]\n"
    "ds_read_b128 v[160:163], v205 offset:10240\n"
    "ds_read_b128 v[164:167], v205 offset:11264\n"
    "s_waitcnt lgkmcnt(1)\n"
    "v_mfma_f32_16x16x32_bf16 v[92:95], v[160:163], v[24:27], 0\n"
    "s_waitcnt lgkmcnt(0)\n"
    "v_mfma_f32_16x16x32_bf16 v[92:95], v[164:167], v[28:31], v[92:95]\n"
    "v_mfma_f32_16x16x32_bf16 v[88:91], v[164:167], v[4:7], 0\n"
    "v_mfma_f32_16x16x32_bf16 v[88:91], v[160:163], v[0:3], v[88:91]\n"
    "v_mfma_f32_16x16x32_bf16 v[84:87], v[160:163], a[232:235], 0\n"
    "v_mfma_f32_16x16x32_bf16 v[84:87], v[164:167], a[236:239], v[84:87]\n"
    "v_mfma_f32_16x16x32_bf16 v[80:83], v[164:167], a[212:215], 0\n"
    "v_mfma_f32_16x16x32_bf16 v[80:83], v[160:163], a[208:211], v[80:83]\n"
    "ds_read_b128 v[168:171], v205 offset:8192\n"
    "ds_read_b128 v[172:175], v205 offset:9216\n"
    "s_waitcnt lgkmcnt(1)\n"
    "v_mfma_f32_16x16x32_bf16 v[80:83], v[168:171], a[200:203], v[80:83]\n"
    "s_waitcnt lgkmcnt(0)\n"
    "v_mfma_f32_16x16x32_bf16 v[80:83], v[172:175], a[204:207], v[80:83]\n"
    "v_mfma_f32_16x16x32_bf16 v[84:87], v[172:175], a[228:231], v[84:87]\n"
    "v_mfma_f32_16x16x32_bf16 v[84:87], v[168:171], a[224:227], v[84:87]\n"
    "v_mfma_f32_16x16x32_bf16 v[88:91], v[168:171], a[248:251], v[88:91]\n"
    "v_mfma_f32_16x16x32_bf16 v[88:91], v[172:175], a[252:255], v[88:91]\n"
    "v_mfma_f32_16x16x32_bf16 v[92:95], v[172:175], v[20:23], v[92:95]\n"
    "v_mfma_f32_16x16x32_bf16 v[92:95], v[168:171], v[16:19], v[92:95]\n"
    "ds_read_b128 v[176:179], v205 offset:6144\n"
    "ds_read_b128 v[180:183], v205 offset:7168\n"
    "s_waitcnt lgkmcnt(1)\n"
    "v_mfma_f32_16x16x32_bf16 v[92:95], v[176:179], v[8:11], v[92:95]\n"
    "s_waitcnt lgkmcnt(0)\n"
    "v_mfma_f32_16x16x32_bf16 v[92:95], v[180:183], v[12:15], v[92:95]\n"
    "v_mfma_f32_16x16x32_bf16 v[88:91], v[180:183], a[244:247], v[88:91]\n"
    "v_mfma_f32_16x16x32_bf16 v[88:91], v[176:179], a[240:243], v[88:91]\n"
    "v_mfma_f32_16x16x32_bf16 v[84:87], v[176:179], a[216:219], v[84:87]\n"
    "v_mfma_f32_16x16x32_bf16 v[84:87], v[180:183], a[220:223], v[84:87]\n"
    "v_mfma_f32_16x16x32_bf16 v[80:83], v[180:183], a[196:199], v[80:83]\n"
    "v_mfma_f32_16x16x32_bf16 v[80:83], v[176:179], a[192:195], v[80:83]\n"
    "v_max3_f32 v196, v32, v33, v34\n"
    "v_max3_f32 v196, v196, v35, v48\n"
    "v_max3_f32 v196, v196, v49, v50\n"
    "v_max3_f32 v196, v196, v51, v64\n"
    "v_max3_f32 v196, v196, v65, v66\n"
    "s_nop 2\n"
    "v_max3_f32 v196, v196, v67, v80\n"
    "v_max3_f32 v196, v196, v81, v82\n"
    "v_max_f32 v196, v196, v83\n"
    "v_mov_b32 v197, v196\n"
    "s_nop 1\n"
    "v_permlane32_swap_b32 v196, v197\n"
    "v_max_f32 v196, v196, v197\n"
    "v_mov_b32 v197, v196\n"
    "s_nop 1\n"
    "v_permlane16_swap_b32 v196, v197\n"
    "v_max_f32 v196, v196, v197\n"
    "v_max_f32 v196, v196, v96\n"
    "v_sub_f32 v197, v96, v196\n"
    "v_mov_b32 v96, v196\n"
    "v_exp_f32 v197, v197\n"
    "v_sub_f32 v32, v32, v196\n"
    "v_sub_f32 v33, v33, v196\n"
    "v_sub_f32 v34, v34, v196\n"
    "v_sub_f32 v35, v35, v196\n"
    "v_sub_f32 v48, v48, v196\n"
    "v_sub_f32 v49, v49, v196\n"
    "v_sub_f32 v50, v50, v196\n"
    "v_sub_f32 v51, v51, v196\n"
    "v_sub_f32 v64, v64, v196\n"
    "v_sub_f32 v65, v65, v196\n"
    "v_sub_f32 v66, v66, v196\n"
    "v_sub_f32 v67, v67, v196\n"
    "v_sub_f32 v80, v80, v196\n"
    "v_sub_f32 v81, v81, v196\n"
    "v_sub_f32 v82, v82, v196\n"
    "v_sub_f32 v83, v83, v196\n"
    "v_mul_f32 v112, v112, v197\n"
    "v_mul_f32 v113, v113, v197\n"
    "v_mul_f32 v114, v114, v197\n"
    "v_mul_f32 v115, v115, v197\n"
    "v_accvgpr_read_b32 v198, a0\n"
    "v_accvgpr_read_b32 v199, a1\n"
    "v_mul_f32 v198, v198, v197\n"
    "v_mul_f32 v199, v199, v197\n"
    "v_accvgpr_write_b32 a0, v198\n"
    "v_accvgpr_write_b32 a1, v199\n"
    "v_accvgpr_read_b32 v198, a2\n"
    "v_accvgpr_read_b32 v199, a3\n"
    "v_mul_f32 v198, v198, v197\n"
    "v_mul_f32 v199, v199, v197\n"
    "v_accvgpr_write_b32 a2, v198\n"
    "v_accvgpr_write_b32 a3, v199\n"
    "v_accvgpr_read_b32 v198, a16\n"
    "v_accvgpr_read_b32 v199, a17\n"
    "v_mul_f32 v198, v198, v197\n"
    "v_mul_f32 v199, v199, v197\n"
    "v_accvgpr_write_b32 a16, v198\n"
    "v_accvgpr_write_b32 a17, v199\n"
    "v_accvgpr_read_b32 v198, a18\n"
    "v_accvgpr_read_b32 v199, a19\n"
    "v_mul_f32 v198, v198, v197\n"
    "v_mul_f32 v199, v199, v197\n"
    "v_accvgpr_write_b32 a18, v198\n"
    "v_accvgpr_write_b32 a19, v199\n"
    "v_accvgpr_read_b32 v198, a32\n"
    "v_accvgpr_read_b32 v199, a33\n"
    "v_mul_f32 v198, v198, v197\n"
    "v_mul_f32 v199, v199, v197\n"
    "v_accvgpr_write_b32 a32, v198\n"
    "v_accvgpr_write_b32 a33, v199\n"
    "v_accvgpr_read_b32 v198, a34\n"
    "v_accvgpr_read_b32 v199, a35\n"
    "v_mul_f32 v198, v198, v197\n"
    "v_mul_f32 v199, v199, v197\n"
    "v_accvgpr_write_b32 a34, v198\n"
    "v_accvgpr_write_b32 a35, v199\n"
    "v_accvgpr_read_b32 v198, a48\n"
    "v_accvgpr_read_b32 v199, a49\n"
    "v_mul_f32 v198, v198, v197\n"
    "v_mul_f32 v199, v199, v197\n"
    "v_accvgpr_write_b32 a48, v198\n"
    "v_accvgpr_write_b32 a49, v199\n"
    "v_accvgpr_read_b32 v198, a50\n"
    "v_accvgpr_read_b32 v199, a51\n"
    "v_mul_f32 v198, v198, v197\n"
    "v_mul_f32 v199, v199, v197\n"
    "v_accvgpr_write_b32 a50, v198\n"
    "v_accvgpr_write_b32 a51, v199\n"
    "v_accvgpr_read_b32 v198, a64\n"
    "v_accvgpr_read_b32 v199, a65\n"
    "v_mul_f32 v198, v198, v197\n"
    "v_mul_f32 v199, v199, v197\n"
    "v_accvgpr_write_b32 a64, v198\n"
    "v_accvgpr_write_b32 a65, v199\n"
    "v_accvgpr_read_b32 v198, a66\n"
    "v_accvgpr_read_b32 v199, a67\n"
    "v_mul_f32 v198, v198, v197\n"
    "v_mul_f32 v199, v199, v197\n"
    "v_accvgpr_write_b32 a66, v198\n"
    "v_accvgpr_write_b32 a67, v199\n"
    "v_accvgpr_read_b32 v198, a80\n"
    "v_accvgpr_read_b32 v199, a81\n"
    "v_mul_f32 v198, v198, v197\n"
    "v_mul_f32 v199, v199, v197\n"
    "v_accvgpr_write_b32 a80, v198\n"
    "v_accvgpr_write_b32 a81, v199\n"
    "v_accvgpr_read_b32 v198, a82\n"
    "v_accvgpr_read_b32 v199, a83\n"
    "v_mul_f32 v198, v198, v197\n"
    "v_mul_f32 v199, v199, v197\n"
    "v_accvgpr_write_b32 a82, v198\n"
    "v_accvgpr_write_b32 a83, v199\n"
    "v_accvgpr_read_b32 v198, a96\n"
    "v_accvgpr_read_b32 v199, a97\n"
    "v_mul_f32 v198, v198, v197\n"
    "v_mul_f32 v199, v199, v197\n"
    "v_accvgpr_write_b32 a96, v198\n"
    "v_accvgpr_write_b32 a97, v199\n"
    "v_accvgpr_read_b32 v198, a98\n"
    "v_accvgpr_read_b32 v199, a99\n"
    "v_mul_f32 v198, v198, v197\n"
    "v_mul_f32 v199, v199, v197\n"
    "v_accvgpr_write_b32 a98, v198\n"
    "v_accvgpr_write_b32 a99, v199\n"
    "v_accvgpr_read_b32 v198, a112\n"
    "v_accvgpr_read_b32 v199, a113\n"
    "v_mul_f32 v198, v198, v197\n"
    "v_mul_f32 v199, v199, v197\n"
    "v_accvgpr_write_b32 a112, v198\n"
    "v_accvgpr_write_b32 a113, v199\n"
    "v_accvgpr_read_b32 v198, a114\n"
    "v_accvgpr_read_b32 v199, a115\n"
    "v_mul_f32 v198, v198, v197\n"
    "v_mul_f32 v199, v199, v197\n"
    "v_accvgpr_write_b32 a114, v198\n"
    "v_accvgpr_write_b32 a115, v199\n"
    "v_accvgpr_read_b32 v198, a128\n"
    "v_accvgpr_read_b32 v199, a129\n"
    "v_mul_f32 v198, v198, v197\n"
    "v_mul_f32 v199, v199, v197\n"
    "v_accvgpr_write_b32 a128, v198\n"
    "v_accvgpr_write_b32 a129, v199\n"
    "v_accvgpr_read_b32 v198, a130\n"
    "v_accvgpr_read_b32 v199, a131\n"
    "v_mul_f32 v198, v198, v197\n"
    "v_mul_f32 v199, v199, v197\n"
    "v_accvgpr_write_b32 a130, v198\n"
    "v_accvgpr_write_b32 a131, v199\n"
    "v_accvgpr_read_b32 v198, a144\n"
    "v_accvgpr_read_b32 v199, a145\n"
    "v_mul_f32 v198, v198, v197\n"
    "v_mul_f32 v199, v199, v197\n"
    "v_accvgpr_write_b32 a144, v198\n"
    "v_accvgpr_write_b32 a145, v199\n"
    "v_accvgpr_read_b32 v198, a146\n"
    "v_accvgpr_read_b32 v199, a147\n"
    "v_mul_f32 v198, v198, v197\n"
    "v_mul_f32 v199, v199, v197\n"
    "v_accvgpr_write_b32 a146, v198\n"
    "v_accvgpr_write_b32 a147, v199\n"
    "v_accvgpr_read_b32 v198, a160\n"
    "v_accvgpr_read_b32 v199, a161\n"
    "v_mul_f32 v198, v198, v197\n"
    "v_mul_f32 v199, v199, v197\n"
    "v_accvgpr_write_b32 a160, v198\n"
    "v_accvgpr_write_b32 a161, v199\n"
    "v_accvgpr_read_b32 v198, a162\n"
    "v_accvgpr_read_b32 v199, a163\n"
    "v_mul_f32 v198, v198, v197\n"
    "v_mul_f32 v199, v199, v197\n"
    "v_accvgpr_write_b32 a162, v198\n"
    "v_accvgpr_write_b32 a163, v199\n"
    "v_accvgpr_read_b32 v198, a176\n"
    "v_accvgpr_read_b32 v199, a177\n"
    "v_mul_f32 v198, v198, v197\n"
    "v_mul_f32 v199, v199, v197\n"
    "v_accvgpr_write_b32 a176, v198\n"
    "v_accvgpr_write_b32 a177, v199\n"
    "v_accvgpr_read_b32 v198, a178\n"
    "v_accvgpr_read_b32 v199, a179\n"
    "v_mul_f32 v198, v198, v197\n"
    "v_mul_f32 v199, v199, v197\n"
    "v_accvgpr_write_b32 a178, v198\n"
    "v_accvgpr_write_b32 a179, v199\n"
    "v_max3_f32 v196, v36, v37, v38\n"
    "v_max3_f32 v196, v196, v39, v52\n"
    "v_max3_f32 v196, v196, v53, v54\n"
    "v_max3_f32 v196, v196, v55, v68\n"
    "v_max3_f32 v196, v196, v69, v70\n"
    "v_max3_f32 v196, v196, v71, v84\n"
    "v_max3_f32 v196, v196, v85, v86\n"
    "v_max_f32 v196, v196, v87\n"
    "v_mov_b32 v197, v196\n"
    "s_nop 1\n"
    "v_permlane32_swap_b32 v196, v197\n"
    "v_max_f32 v196, v196, v197\n"
    "v_mov_b32 v197, v196\n"
    "s_nop 1\n"
    "v_permlane16_swap_b32 v196, v197\n"
    "v_max_f32 v196, v196, v197\n"
    "v_max_f32 v196, v196, v100\n"
    "v_sub_f32 v197, v100, v196\n"
    "v_mov_b32 v100, v196\n"
    "v_exp_f32 v197, v197\n"
    "v_sub_f32 v36, v36, v196\n"
    "v_sub_f32 v37, v37, v196\n"
    "v_sub_f32 v38, v38, v196\n"
    "v_sub_f32 v39, v39, v196\n"
    "v_sub_f32 v52, v52, v196\n"
    "v_sub_f32 v53, v53, v196\n"
    "v_sub_f32 v54, v54, v196\n"
    "v_sub_f32 v55, v55, v196\n"
    "v_sub_f32 v68, v68, v196\n"
    "v_sub_f32 v69, v69, v196\n"
    "v_sub_f32 v70, v70, v196\n"
    "v_sub_f32 v71, v71, v196\n"
    "v_sub_f32 v84, v84, v196\n"
    "v_sub_f32 v85, v85, v196\n"
    "v_sub_f32 v86, v86, v196\n"
    "v_sub_f32 v87, v87, v196\n"
    "v_mul_f32 v116, v116, v197\n"
    "v_mul_f32 v117, v117, v197\n"
    "v_mul_f32 v118, v118, v197\n"
    "v_mul_f32 v119, v119, v197\n"
    "v_accvgpr_read_b32 v198, a4\n"
    "v_accvgpr_read_b32 v199, a5\n"
    "v_mul_f32 v198, v198, v197\n"
    "v_mul_f32 v199, v199, v197\n"
    "v_accvgpr_write_b32 a4, v198\n"
    "v_accvgpr_write_b32 a5, v199\n"
    "v_accvgpr_read_b32 v198, a6\n"
    "v_accvgpr_read_b32 v199, a7\n"
    "v_mul_f32 v198, v198, v197\n"
    "v_mul_f32 v199, v199, v197\n"
    "v_accvgpr_write_b32 a6, v198\n"
    "v_accvgpr_write_b32 a7, v199\n"
    "v_accvgpr_read_b32 v198, a20\n"
    "v_accvgpr_read_b32 v199, a21\n"
    "v_mul_f32 v198, v198, v197\n"
    "v_mul_f32 v199, v199, v197\n"
    "v_accvgpr_write_b32 a20, v198\n"
    "v_accvgpr_write_b32 a21, v199\n"
    "v_accvgpr_read_b32 v198, a22\n"
    "v_accvgpr_read_b32 v199, a23\n"
    "v_mul_f32 v198, v198, v197\n"
    "v_mul_f32 v199, v199, v197\n"
    "v_accvgpr_write_b32 a22, v198\n"
    "v_accvgpr_write_b32 a23, v199\n"
    "v_accvgpr_read_b32 v198, a36\n"
    "v_accvgpr_read_b32 v199, a37\n"
    "v_mul_f32 v198, v198, v197\n"
    "v_mul_f32 v199, v199, v197\n"
    "v_accvgpr_write_b32 a36, v198\n"
    "v_accvgpr_write_b32 a37, v199\n"
    "v_accvgpr_read_b32 v198, a38\n"
    "v_accvgpr_read_b32 v199, a39\n"
    "v_mul_f32 v198, v198, v197\n"
    "v_mul_f32 v199, v199, v197\n"
    "v_accvgpr_write_b32 a38, v198\n"
    "v_accvgpr_write_b32 a39, v199\n"
    "v_accvgpr_read_b32 v198, a52\n"
    "v_accvgpr_read_b32 v199, a53\n"
    "v_mul_f32 v198, v198, v197\n"
    "v_mul_f32 v199, v199, v197\n"
    "v_accvgpr_write_b32 a52, v198\n"
    "v_accvgpr_write_b32 a53, v199\n"
    "v_accvgpr_read_b32 v198, a54\n"
    "v_accvgpr_read_b32 v199, a55\n"
    "v_mul_f32 v198, v198, v197\n"
    "v_mul_f32 v199, v199, v197\n"
    "v_accvgpr_write_b32 a54, v198\n"
    "v_accvgpr_write_b32 a55, v199\n"
    "v_accvgpr_read_b32 v198, a68\n"
    "v_accvgpr_read_b32 v199, a69\n"
    "v_mul_f32 v198, v198, v197\n"
    "v_mul_f32 v199, v199, v197\n"
    "v_accvgpr_write_b32 a68, v198\n"
    "v_accvgpr_write_b32 a69, v199\n"
    "v_accvgpr_read_b32 v198, a70\n"
    "v_accvgpr_read_b32 v199, a71\n"
    "v_mul_f32 v198, v198, v197\n"
    "v_mul_f32 v199, v199, v197\n"
    "v_accvgpr_write_b32 a70, v198\n"
    "v_accvgpr_write_b32 a71, v199\n"
    "v_accvgpr_read_b32 v198, a84\n"
    "v_accvgpr_read_b32 v199, a85\n"
    "v_mul_f32 v198, v198, v197\n"
    "v_mul_f32 v199, v199, v197\n"
    "v_accvgpr_write_b32 a84, v198\n"
    "v_accvgpr_write_b32 a85, v199\n"
    "v_accvgpr_read_b32 v198, a86\n"
    "v_accvgpr_read_b32 v199, a87\n"
    "v_mul_f32 v198, v198, v197\n"
    "v_mul_f32 v199, v199, v197\n"
    "v_accvgpr_write_b32 a86, v198\n"
    "v_accvgpr_write_b32 a87, v199\n"
    "v_accvgpr_read_b32 v198, a100\n"
    "v_accvgpr_read_b32 v199, a101\n"
    "v_mul_f32 v198, v198, v197\n"
    "v_mul_f32 v199, v199, v197\n"
    "v_accvgpr_write_b32 a100, v198\n"
    "v_accvgpr_write_b32 a101, v199\n"
    "v_accvgpr_read_b32 v198, a102\n"
    "v_accvgpr_read_b32 v199, a103\n"
    "v_mul_f32 v198, v198, v197\n"
    "v_mul_f32 v199, v199, v197\n"
    "v_accvgpr_write_b32 a102, v198\n"
    "v_accvgpr_write_b32 a103, v199\n"
    "v_accvgpr_read_b32 v198, a116\n"
    "v_accvgpr_read_b32 v199, a117\n"
    "v_mul_f32 v198, v198, v197\n"
    "v_mul_f32 v199, v199, v197\n"
    "v_accvgpr_write_b32 a116, v198\n"
    "v_accvgpr_write_b32 a117, v199\n"
    "v_accvgpr_read_b32 v198, a118\n"
    "v_accvgpr_read_b32 v199, a119\n"
    "v_mul_f32 v198, v198, v197\n"
    "v_mul_f32 v199, v199, v197\n"
    "v_accvgpr_write_b32 a118, v198\n"
    "v_accvgpr_write_b32 a119, v199\n"
    "v_accvgpr_read_b32 v198, a132\n"
    "v_accvgpr_read_b32 v199, a133\n"
    "v_mul_f32 v198, v198, v197\n"
    "v_mul_f32 v199, v199, v197\n"
    "v_accvgpr_write_b32 a132, v198\n"
    "v_accvgpr_write_b32 a133, v199\n"
    "v_accvgpr_read_b32 v198, a134\n"
    "v_accvgpr_read_b32 v199, a135\n"
    "v_mul_f32 v198, v198, v197\n"
    "v_mul_f32 v199, v199, v197\n"
    "v_accvgpr_write_b32 a134, v198\n"
    "v_accvgpr_write_b32 a135, v199\n"
    "v_accvgpr_read_b32 v198, a148\n"
    "v_accvgpr_read_b32 v199, a149\n"
    "v_mul_f32 v198, v198, v197\n"
    "v_mul_f32 v199, v199, v197\n"
    "v_accvgpr_write_b32 a148, v198\n"
    "v_accvgpr_write_b32 a149, v199\n"
    "v_accvgpr_read_b32 v198, a150\n"
    "v_accvgpr_read_b32 v199, a151\n"
    "v_mul_f32 v198, v198, v197\n"
    "v_mul_f32 v199, v199, v197\n"
    "v_accvgpr_write_b32 a150, v198\n"
    "v_accvgpr_write_b32 a151, v199\n"
    "v_accvgpr_read_b32 v198, a164\n"
    "v_accvgpr_read_b32 v199, a165\n"
    "v_mul_f32 v198, v198, v197\n"
    "v_mul_f32 v199, v199, v197\n"
    "v_accvgpr_write_b32 a164, v198\n"
    "v_accvgpr_write_b32 a165, v199\n"
    "v_accvgpr_read_b32 v198, a166\n"
    "v_accvgpr_read_b32 v199, a167\n"
    "v_mul_f32 v198, v198, v197\n"
    "v_mul_f32 v199, v199, v197\n"
    "v_accvgpr_write_b32 a166, v198\n"
    "v_accvgpr_write_b32 a167, v199\n"
    "v_accvgpr_read_b32 v198, a180\n"
    "v_accvgpr_read_b32 v199, a181\n"
    "v_mul_f32 v198, v198, v197\n"
    "v_mul_f32 v199, v199, v197\n"
    "v_accvgpr_write_b32 a180, v198\n"
    "v_accvgpr_write_b32 a181, v199\n"
    "v_accvgpr_read_b32 v198, a182\n"
    "v_accvgpr_read_b32 v199, a183\n"
    "v_mul_f32 v198, v198, v197\n"
    "v_mul_f32 v199, v199, v197\n"
    "v_accvgpr_write_b32 a182, v198\n"
    "v_accvgpr_write_b32 a183, v199\n"
    "v_max3_f32 v196, v40, v41, v42\n"
    "v_max3_f32 v196, v196, v43, v56\n"
    "v_max3_f32 v196, v196, v57, v58\n"
    "v_max3_f32 v196, v196, v59, v72\n"
    "v_max3_f32 v196, v196, v73, v74\n"
    "v_max3_f32 v196, v196, v75, v88\n"
    "v_max3_f32 v196, v196, v89, v90\n"
    "v_max_f32 v196, v196, v91\n"
    "v_mov_b32 v197, v196\n"
    "s_nop 1\n"
    "v_permlane32_swap_b32 v196, v197\n"
    "v_max_f32 v196, v196, v197\n"
    "v_mov_b32 v197, v196\n"
    "s_nop 1\n"
    "v_permlane16_swap_b32 v196, v197\n"
    "v_max_f32 v196, v196, v197\n"
    "v_max_f32 v196, v196, v104\n"
    "v_sub_f32 v197, v104, v196\n"
    "v_mov_b32 v104, v196\n"
    "v_exp_f32 v197, v197\n"
    "v_sub_f32 v40, v40, v196\n"
    "v_sub_f32 v41, v41, v196\n"
    "v_sub_f32 v42, v42, v196\n"
    "v_sub_f32 v43, v43, v196\n"
    "v_sub_f32 v56, v56, v196\n"
    "v_sub_f32 v57, v57, v196\n"
    "v_sub_f32 v58, v58, v196\n"
    "v_sub_f32 v59, v59, v196\n"
    "v_sub_f32 v72, v72, v196\n"
    "v_sub_f32 v73, v73, v196\n"
    "v_sub_f32 v74, v74, v196\n"
    "v_sub_f32 v75, v75, v196\n"
    "v_sub_f32 v88, v88, v196\n"
    "v_sub_f32 v89, v89, v196\n"
    "v_sub_f32 v90, v90, v196\n"
    "v_sub_f32 v91, v91, v196\n"
    "v_mul_f32 v120, v120, v197\n"
    "v_mul_f32 v121, v121, v197\n"
    "v_mul_f32 v122, v122, v197\n"
    "v_mul_f32 v123, v123, v197\n"
    "v_accvgpr_read_b32 v198, a8\n"
    "v_accvgpr_read_b32 v199, a9\n"
    "v_mul_f32 v198, v198, v197\n"
    "v_mul_f32 v199, v199, v197\n"
    "v_accvgpr_write_b32 a8, v198\n"
    "v_accvgpr_write_b32 a9, v199\n"
    "v_accvgpr_read_b32 v198, a10\n"
    "v_accvgpr_read_b32 v199, a11\n"
    "v_mul_f32 v198, v198, v197\n"
    "v_mul_f32 v199, v199, v197\n"
    "v_accvgpr_write_b32 a10, v198\n"
    "v_accvgpr_write_b32 a11, v199\n"
    "v_accvgpr_read_b32 v198, a24\n"
    "v_accvgpr_read_b32 v199, a25\n"
    "v_mul_f32 v198, v198, v197\n"
    "v_mul_f32 v199, v199, v197\n"
    "v_accvgpr_write_b32 a24, v198\n"
    "v_accvgpr_write_b32 a25, v199\n"
    "v_accvgpr_read_b32 v198, a26\n"
    "v_accvgpr_read_b32 v199, a27\n"
    "v_mul_f32 v198, v198, v197\n"
    "v_mul_f32 v199, v199, v197\n"
    "v_accvgpr_write_b32 a26, v198\n"
    "v_accvgpr_write_b32 a27, v199\n"
    "v_accvgpr_read_b32 v198, a40\n"
    "v_accvgpr_read_b32 v199, a41\n"
    "v_mul_f32 v198, v198, v197\n"
    "v_mul_f32 v199, v199, v197\n"
    "v_accvgpr_write_b32 a40, v198\n"
    "v_accvgpr_write_b32 a41, v199\n"
    "v_accvgpr_read_b32 v198, a42\n"
    "v_accvgpr_read_b32 v199, a43\n"
    "v_mul_f32 v198, v198, v197\n"
    "v_mul_f32 v199, v199, v197\n"
    "v_accvgpr_write_b32 a42, v198\n"
    "v_accvgpr_write_b32 a43, v199\n"
    "v_accvgpr_read_b32 v198, a56\n"
    "v_accvgpr_read_b32 v199, a57\n"
    "v_mul_f32 v198, v198, v197\n"
    "v_mul_f32 v199, v199, v197\n"
    "v_accvgpr_write_b32 a56, v198\n"
    "v_accvgpr_write_b32 a57, v199\n"
    "v_accvgpr_read_b32 v198, a58\n"
    "v_accvgpr_read_b32 v199, a59\n"
    "v_mul_f32 v198, v198, v197\n"
    "v_mul_f32 v199, v199, v197\n"
    "v_accvgpr_write_b32 a58, v198\n"
    "v_accvgpr_write_b32 a59, v199\n"
    "v_accvgpr_read_b32 v198, a72\n"
    "v_accvgpr_read_b32 v199, a73\n"
    "v_mul_f32 v198, v198, v197\n"
    "v_mul_f32 v199, v199, v197\n"
    "v_accvgpr_write_b32 a72, v198\n"
    "v_accvgpr_write_b32 a73, v199\n"
    "v_accvgpr_read_b32 v198, a74\n"
    "v_accvgpr_read_b32 v199, a75\n"
    "v_mul_f32 v198, v198, v197\n"
    "v_mul_f32 v199, v199, v197\n"
    "v_accvgpr_write_b32 a74, v198\n"
    "v_accvgpr_write_b32 a75, v199\n"
    "v_accvgpr_read_b32 v198, a88\n"
    "v_accvgpr_read_b32 v199, a89\n"
    "v_mul_f32 v198, v198, v197\n"
    "v_mul_f32 v199, v199, v197\n"
    "v_accvgpr_write_b32 a88, v198\n"
    "v_accvgpr_write_b32 a89, v199\n"
    "v_accvgpr_read_b32 v198, a90\n"
    "v_accvgpr_read_b32 v199, a91\n"
    "v_mul_f32 v198, v198, v197\n"
    "v_mul_f32 v199, v199, v197\n"
    "v_accvgpr_write_b32 a90, v198\n"
    "v_accvgpr_write_b32 a91, v199\n"
    "v_accvgpr_read_b32 v198, a104\n"
    "v_accvgpr_read_b32 v199, a105\n"
    "v_mul_f32 v198, v198, v197\n"
    "v_mul_f32 v199, v199, v197\n"
    "v_accvgpr_write_b32 a104, v198\n"
    "v_accvgpr_write_b32 a105, v199\n"
    "v_accvgpr_read_b32 v198, a106\n"
    "v_accvgpr_read_b32 v199, a107\n"
    "v_mul_f32 v198, v198, v197\n"
    "v_mul_f32 v199, v199, v197\n"
    "v_accvgpr_write_b32 a106, v198\n"
    "v_accvgpr_write_b32 a107, v199\n"
    "v_accvgpr_read_b32 v198, a120\n"
    "v_accvgpr_read_b32 v199, a121\n"
    "v_mul_f32 v198, v198, v197\n"
    "v_mul_f32 v199, v199, v197\n"
    "v_accvgpr_write_b32 a120, v198\n"
    "v_accvgpr_write_b32 a121, v199\n"
    "v_accvgpr_read_b32 v198, a122\n"
    "v_accvgpr_read_b32 v199, a123\n"
    "v_mul_f32 v198, v198, v197\n"
    "v_mul_f32 v199, v199, v197\n"
    "v_accvgpr_write_b32 a122, v198\n"
    "v_accvgpr_write_b32 a123, v199\n"
    "v_accvgpr_read_b32 v198, a136\n"
    "v_accvgpr_read_b32 v199, a137\n"
    "v_mul_f32 v198, v198, v197\n"
    "v_mul_f32 v199, v199, v197\n"
    "v_accvgpr_write_b32 a136, v198\n"
    "v_accvgpr_write_b32 a137, v199\n"
    "v_accvgpr_read_b32 v198, a138\n"
    "v_accvgpr_read_b32 v199, a139\n"
    "v_mul_f32 v198, v198, v197\n"
    "v_mul_f32 v199, v199, v197\n"
    "v_accvgpr_write_b32 a138, v198\n"
    "v_accvgpr_write_b32 a139, v199\n"
    "v_accvgpr_read_b32 v198, a152\n"
    "v_accvgpr_read_b32 v199, a153\n"
    "v_mul_f32 v198, v198, v197\n"
    "v_mul_f32 v199, v199, v197\n"
    "v_accvgpr_write_b32 a152, v198\n"
    "v_accvgpr_write_b32 a153, v199\n"
    "v_accvgpr_read_b32 v198, a154\n"
    "v_accvgpr_read_b32 v199, a155\n"
    "v_mul_f32 v198, v198, v197\n"
    "v_mul_f32 v199, v199, v197\n"
    "v_accvgpr_write_b32 a154, v198\n"
    "v_accvgpr_write_b32 a155, v199\n"
    "v_accvgpr_read_b32 v198, a168\n"
    "v_accvgpr_read_b32 v199, a169\n"
    "v_mul_f32 v198, v198, v197\n"
    "v_mul_f32 v199, v199, v197\n"
    "v_accvgpr_write_b32 a168, v198\n"
    "v_accvgpr_write_b32 a169, v199\n"
    "v_accvgpr_read_b32 v198, a170\n"
    "v_accvgpr_read_b32 v199, a171\n"
    "v_mul_f32 v198, v198, v197\n"
    "v_mul_f32 v199, v199, v197\n"
    "v_accvgpr_write_b32 a170, v198\n"
    "v_accvgpr_write_b32 a171, v199\n"
    "v_accvgpr_read_b32 v198, a184\n"
    "v_accvgpr_read_b32 v199, a185\n"
    "v_mul_f32 v198, v198, v197\n"
    "v_mul_f32 v199, v199, v197\n"
    "v_accvgpr_write_b32 a184, v198\n"
    "v_accvgpr_write_b32 a185, v199\n"
    "v_accvgpr_read_b32 v198, a186\n"
    "v_accvgpr_read_b32 v199, a187\n"
    "v_mul_f32 v198, v198, v197\n"
    "v_mul_f32 v199, v199, v197\n"
    "v_accvgpr_write_b32 a186, v198\n"
    "v_accvgpr_write_b32 a187, v199\n"
    "v_max3_f32 v196, v44, v45, v46\n"
    "v_max3_f32 v196, v196, v47, v60\n"
    "v_max3_f32 v196, v196, v61, v62\n"
    "v_max3_f32 v196, v196, v63, v76\n"
    "v_max3_f32 v196, v196, v77, v78\n"
    "v_max3_f32 v196, v196, v79, v92\n"
    "v_max3_f32 v196, v196, v93, v94\n"
    "v_max_f32 v196, v196, v95\n"
    "v_mov_b32 v197, v196\n"
    "s_nop 1\n"
    "v_permlane32_swap_b32 v196, v197\n"
    "v_max_f32 v196, v196, v197\n"
    "v_mov_b32 v197, v196\n"
    "s_nop 1\n"
    "v_permlane16_swap_b32 v196, v197\n"
    "v_max_f32 v196, v196, v197\n"
    "v_max_f32 v196, v196, v108\n"
    "v_sub_f32 v197, v108, v196\n"
    "v_mov_b32 v108, v196\n"
    "v_exp_f32 v197, v197\n"
    "v_sub_f32 v44, v44, v196\n"
    "v_sub_f32 v45, v45, v196\n"
    "v_sub_f32 v46, v46, v196\n"
    "v_sub_f32 v47, v47, v196\n"
    "v_sub_f32 v60, v60, v196\n"
    "v_sub_f32 v61, v61, v196\n"
    "v_sub_f32 v62, v62, v196\n"
    "v_sub_f32 v63, v63, v196\n"
    "v_sub_f32 v76, v76, v196\n"
    "v_sub_f32 v77, v77, v196\n"
    "v_sub_f32 v78, v78, v196\n"
    "v_sub_f32 v79, v79, v196\n"
    "v_sub_f32 v92, v92, v196\n"
    "v_sub_f32 v93, v93, v196\n"
    "v_sub_f32 v94, v94, v196\n"
    "v_sub_f32 v95, v95, v196\n"
    "v_mul_f32 v124, v124, v197\n"
    "v_mul_f32 v125, v125, v197\n"
    "v_mul_f32 v126, v126, v197\n"
    "v_mul_f32 v127, v127, v197\n"
    "v_accvgpr_read_b32 v198, a12\n"
    "v_accvgpr_read_b32 v199, a13\n"
    "v_mul_f32 v198, v198, v197\n"
    "v_mul_f32 v199, v199, v197\n"
    "v_accvgpr_write_b32 a12, v198\n"
    "v_accvgpr_write_b32 a13, v199\n"
    "v_accvgpr_read_b32 v198, a14\n"
    "v_accvgpr_read_b32 v199, a15\n"
    "v_mul_f32 v198, v198, v197\n"
    "v_mul_f32 v199, v199, v197\n"
    "v_accvgpr_write_b32 a14, v198\n"
    "v_accvgpr_write_b32 a15, v199\n"
    "v_accvgpr_read_b32 v198, a28\n"
    "v_accvgpr_read_b32 v199, a29\n"
    "v_mul_f32 v198, v198, v197\n"
    "v_mul_f32 v199, v199, v197\n"
    "v_accvgpr_write_b32 a28, v198\n"
    "v_accvgpr_write_b32 a29, v199\n"
    "v_accvgpr_read_b32 v198, a30\n"
    "v_accvgpr_read_b32 v199, a31\n"
    "v_mul_f32 v198, v198, v197\n"
    "v_mul_f32 v199, v199, v197\n"
    "v_accvgpr_write_b32 a30, v198\n"
    "v_accvgpr_write_b32 a31, v199\n"
    "v_accvgpr_read_b32 v198, a44\n"
    "v_accvgpr_read_b32 v199, a45\n"
    "v_mul_f32 v198, v198, v197\n"
    "v_mul_f32 v199, v199, v197\n"
    "v_accvgpr_write_b32 a44, v198\n"
    "v_accvgpr_write_b32 a45, v199\n"
    "v_accvgpr_read_b32 v198, a46\n"
    "v_accvgpr_read_b32 v199, a47\n"
    "v_mul_f32 v198, v198, v197\n"
    "v_mul_f32 v199, v199, v197\n"
    "v_accvgpr_write_b32 a46, v198\n"
    "v_accvgpr_write_b32 a47, v199\n"
    "v_accvgpr_read_b32 v198, a60\n"
    "v_accvgpr_read_b32 v199, a61\n"
    "v_mul_f32 v198, v198, v197\n"
    "v_mul_f32 v199, v199, v197\n"
    "v_accvgpr_write_b32 a60, v198\n"
    "v_accvgpr_write_b32 a61, v199\n"
    "v_accvgpr_read_b32 v198, a62\n"
    "v_accvgpr_read_b32 v199, a63\n"
    "v_mul_f32 v198, v198, v197\n"
    "v_mul_f32 v199, v199, v197\n"
    "v_accvgpr_write_b32 a62, v198\n"
    "v_accvgpr_write_b32 a63, v199\n"
    "v_accvgpr_read_b32 v198, a76\n"
    "v_accvgpr_read_b32 v199, a77\n"
    "v_mul_f32 v198, v198, v197\n"
    "v_mul_f32 v199, v199, v197\n"
    "v_accvgpr_write_b32 a76, v198\n"
    "v_accvgpr_write_b32 a77, v199\n"
    "v_accvgpr_read_b32 v198, a78\n"
    "v_accvgpr_read_b32 v199, a79\n"
    "v_mul_f32 v198, v198, v197\n"
    "v_mul_f32 v199, v199, v197\n"
    "v_accvgpr_write_b32 a78, v198\n"
    "v_accvgpr_write_b32 a79, v199\n"
    "v_accvgpr_read_b32 v198, a92\n"
    "v_accvgpr_read_b32 v199, a93\n"
    "v_mul_f32 v198, v198, v197\n"
    "v_mul_f32 v199, v199, v197\n"
    "v_accvgpr_write_b32 a92, v198\n"
    "v_accvgpr_write_b32 a93, v199\n"
    "v_accvgpr_read_b32 v198, a94\n"
    "v_accvgpr_read_b32 v199, a95\n"
    "v_mul_f32 v198, v198, v197\n"
    "v_mul_f32 v199, v199, v197\n"
    "v_accvgpr_write_b32 a94, v198\n"
    "v_accvgpr_write_b32 a95, v199\n"
    "v_accvgpr_read_b32 v198, a108\n"
    "v_accvgpr_read_b32 v199, a109\n"
    "v_mul_f32 v198, v198, v197\n"
    "v_mul_f32 v199, v199, v197\n"
    "v_accvgpr_write_b32 a108, v198\n"
    "v_accvgpr_write_b32 a109, v199\n"
    "v_accvgpr_read_b32 v198, a110\n"
    "v_accvgpr_read_b32 v199, a111\n"
    "v_mul_f32 v198, v198, v197\n"
    "v_mul_f32 v199, v199, v197\n"
    "v_accvgpr_write_b32 a110, v198\n"
    "v_accvgpr_write_b32 a111, v199\n"
    "v_accvgpr_read_b32 v198, a124\n"
    "v_accvgpr_read_b32 v199, a125\n"
    "v_mul_f32 v198, v198, v197\n"
    "v_mul_f32 v199, v199, v197\n"
    "v_accvgpr_write_b32 a124, v198\n"
    "v_accvgpr_write_b32 a125, v199\n"
    "v_accvgpr_read_b32 v198, a126\n"
    "v_accvgpr_read_b32 v199, a127\n"
    "v_mul_f32 v198, v198, v197\n"
    "v_mul_f32 v199, v199, v197\n"
    "v_accvgpr_write_b32 a126, v198\n"
    "v_accvgpr_write_b32 a127, v199\n"
    "v_accvgpr_read_b32 v198, a140\n"
    "v_accvgpr_read_b32 v199, a141\n"
    "v_mul_f32 v198, v198, v197\n"
    "v_mul_f32 v199, v199, v197\n"
    "v_accvgpr_write_b32 a140, v198\n"
    "v_accvgpr_write_b32 a141, v199\n"
    "v_accvgpr_read_b32 v198, a142\n"
    "v_accvgpr_read_b32 v199, a143\n"
    "v_mul_f32 v198, v198, v197\n"
    "v_mul_f32 v199, v199, v197\n"
    "v_accvgpr_write_b32 a142, v198\n"
    "v_accvgpr_write_b32 a143, v199\n"
    "v_accvgpr_read_b32 v198, a156\n"
    "v_accvgpr_read_b32 v199, a157\n"
    "v_mul_f32 v198, v198, v197\n"
    "v_mul_f32 v199, v199, v197\n"
    "v_accvgpr_write_b32 a156, v198\n"
    "v_accvgpr_write_b32 a157, v199\n"
    "v_accvgpr_read_b32 v198, a158\n"
    "v_accvgpr_read_b32 v199, a159\n"
    "v_mul_f32 v198, v198, v197\n"
    "v_mul_f32 v199, v199, v197\n"
    "v_accvgpr_write_b32 a158, v198\n"
    "v_accvgpr_write_b32 a159, v199\n"
    "v_accvgpr_read_b32 v198, a172\n"
    "v_accvgpr_read_b32 v199, a173\n"
    "v_mul_f32 v198, v198, v197\n"
    "v_mul_f32 v199, v199, v197\n"
    "v_accvgpr_write_b32 a172, v198\n"
    "v_accvgpr_write_b32 a173, v199\n"
    "v_accvgpr_read_b32 v198, a174\n"
    "v_accvgpr_read_b32 v199, a175\n"
    "v_mul_f32 v198, v198, v197\n"
    "v_mul_f32 v199, v199, v197\n"
    "v_accvgpr_write_b32 a174, v198\n"
    "v_accvgpr_write_b32 a175, v199\n"
    "v_accvgpr_read_b32 v198, a188\n"
    "v_accvgpr_read_b32 v199, a189\n"
    "v_mul_f32 v198, v198, v197\n"
    "v_mul_f32 v199, v199, v197\n"
    "v_accvgpr_write_b32 a188, v198\n"
    "v_accvgpr_write_b32 a189, v199\n"
    "v_accvgpr_read_b32 v198, a190\n"
    "v_accvgpr_read_b32 v199, a191\n"
    "v_mul_f32 v198, v198, v197\n"
    "v_mul_f32 v199, v199, v197\n"
    "v_accvgpr_write_b32 a190, v198\n"
    "v_accvgpr_write_b32 a191, v199\n"
    "v_exp_f32 v32, v32\n"
    "v_exp_f32 v33, v33\n"
    "v_exp_f32 v34, v34\n"
    "v_cvt_pk_bf16_f32 v128, v32, v33\n"
    "v_exp_f32 v35, v35\n"
    "v_exp_f32 v36, v36\n"
    "v_cvt_pk_bf16_f32 v129, v34, v35\n"
    "v_exp_f32 v37, v37\n"
    "v_exp_f32 v38, v38\n"
    "v_cvt_pk_bf16_f32 v132, v36, v37\n"
    "v_exp_f32 v39, v39\n"
    "v_exp_f32 v40, v40\n"
    "v_cvt_pk_bf16_f32 v133, v38, v39\n"
    "v_exp_f32 v41, v41\n"
    "v_exp_f32 v42, v42\n"
    "v_cvt_pk_bf16_f32 v136, v40, v41\n"
    "v_exp_f32 v43, v43\n"
    "v_exp_f32 v44, v44\n"
    "v_cvt_pk_bf16_f32 v137, v42, v43\n"
    "v_exp_f32 v45, v45\n"
    "v_exp_f32 v46, v46\n"
    "v_cvt_pk_bf16_f32 v140, v44, v45\n"
    "v_exp_f32 v47, v47\n"
    "v_exp_f32 v48, v48\n"
    "v_cvt_pk_bf16_f32 v141, v46, v47\n"
    "v_exp_f32 v49, v49\n"
    "v_exp_f32 v50, v50\n"
    "v_cvt_pk_bf16_f32 v130, v48, v49\n"
    "v_exp_f32 v51, v51\n"
    "v_exp_f32 v52, v52\n"
    "v_cvt_pk_bf16_f32 v131, v50, v51\n"
    "v_exp_f32 v53, v53\n"
    "v_exp_f32 v54, v54\n"
    "v_cvt_pk_bf16_f32 v134, v52, v53\n"
    "v_exp_f32 v55, v55\n"
    "v_exp_f32 v56, v56\n"
    "v_cvt_pk_bf16_f32 v135, v54, v55\n"
    "v_exp_f32 v57, v57\n"
    "v_exp_f32 v58, v58\n"
    "v_cvt_pk_bf16_f32 v138, v56, v57\n"
    "v_exp_f32 v59, v59\n"
    "v_exp_f32 v60, v60\n"
    "v_cvt_pk_bf16_f32 v139, v58, v59\n"
    "v_exp_f32 v61, v61\n"
    "v_exp_f32 v62, v62\n"
    "v_cvt_pk_bf16_f32 v142, v60, v61\n"
    "v_exp_f32 v63, v63\n"
    "s_nop 0\n"
    "v_cvt_pk_bf16_f32 v143, v62, v63\n"
    "v_exp_f32 v64, v64\n"
    "v_exp_f32 v65, v65\n"
    "v_exp_f32 v66, v66\n"
    "v_cvt_pk_bf16_f32 v144, v64, v65\n"
    "v_exp_f32 v67, v67\n"
    "v_exp_f32 v68, v68\n"
    "v_cvt_pk_bf16_f32 v145, v66, v67\n"
    "v_exp_f32 v69, v69\n"
    "v_exp_f32 v70, v70\n"
    "v_cvt_pk_bf16_f32 v148, v68, v69\n"
    "v_exp_f32 v71, v71\n"
    "v_exp_f32 v72, v72\n"
    "v_cvt_pk_bf16_f32 v149, v70, v71\n"
    "v_exp_f32 v73, v73\n"
    "v_exp_f32 v74, v74\n"
    "v_cvt_pk_bf16_f32 v152, v72, v73\n"
    "v_exp_f32 v75, v75\n"
    "v_exp_f32 v76, v76\n"
    "v_cvt_pk_bf16_f32 v153, v74, v75\n"
    "v_exp_f32 v77, v77\n"
    "v_exp_f32 v78, v78\n"
    "v_cvt_pk_bf16_f32 v156, v76, v77\n"
    "v_exp_f32 v79, v79\n"
    "v_exp_f32 v80, v80\n"
    "v_cvt_pk_bf16_f32 v157, v78, v79\n"
    "v_exp_f32 v81, v81\n"
    "v_exp_f32 v82, v82\n"
    "v_cvt_pk_bf16_f32 v146, v80, v81\n"
    "v_exp_f32 v83, v83\n"
    "v_exp_f32 v84, v84\n"
    "v_cvt_pk_bf16_f32 v147, v82, v83\n"
    "v_exp_f32 v85, v85\n"
    "v_exp_f32 v86, v86\n"
    "v_cvt_pk_bf16_f32 v150, v84, v85\n"
    "v_exp_f32 v87, v87\n"
    "v_exp_f32 v88, v88\n"
    "v_cvt_pk_bf16_f32 v151, v86, v87\n"
    "v_exp_f32 v89, v89\n"
    "v_exp_f32 v90, v90\n"
    "v_cvt_pk_bf16_f32 v154, v88, v89\n"
    "v_exp_f32 v91, v91\n"
    "v_exp_f32 v92, v92\n"
    "v_cvt_pk_bf16_f32 v155, v90, v91\n"
    "v_exp_f32 v93, v93\n"
    "v_exp_f32 v94, v94\n"
    "v_cvt_pk_bf16_f32 v158, v92, v93\n"
    "v_exp_f32 v95, v95\n"
    "s_nop 0\n"
    "v_cvt_pk_bf16_f32 v159, v94, v95\n"
    "ds_read_b128 v[160:163], v205 offset:12288\n"
    "ds_read_b128 v[164:167], v205 offset:13312\n"
    "s_waitcnt lgkmcnt(1)\n"
    "v_mfma_f32_16x16x32_bf16 a[0:3], v[160:163], v[128:131], a[0:3]\n"
    "s_waitcnt lgkmcnt(0)\n"
    "v_mfma_f32_16x16x32_bf16 a[0:3], v[164:167], v[144:147], a[0:3]\n"
    "v_mfma_f32_16x16x32_bf16 a[4:7], v[160:163], v[132:135], a[4:7]\n"
    "v_mfma_f32_16x16x32_bf16 a[4:7], v[164:167], v[148:151], a[4:7]\n"
    "v_mfma_f32_16x16x32_bf16 a[8:11], v[160:163], v[136:139], a[8:11]\n"
    "v_mfma_f32_16x16x32_bf16 a[8:11], v[164:167], v[152:155], a[8:11]\n"
    "v_mfma_f32_16x16x32_bf16 a[12:15], v[160:163], v[140:143], a[12:15]\n"
    "v_mfma_f32_16x16x32_bf16 a[12:15], v[164:167], v[156:159], a[12:15]\n"
    "ds_read_b128 v[168:171], v205 offset:14336\n"
    "ds_read_b128 v[172:175], v205 offset:15360\n"
    "s_waitcnt lgkmcnt(1)\n"
    "v_mfma_f32_16x16x32_bf16 a[28:31], v[168:171], v[140:143], a[28:31]\n"
    "s_waitcnt lgkmcnt(0)\n"
    "v_mfma_f32_16x16x32_bf16 a[28:31], v[172:175], v[156:159], a[28:31]\n"
    "v_mfma_f32_16x16x32_bf16 a[24:27], v[172:175], v[152:155], a[24:27]\n"
    "v_mfma_f32_16x16x32_bf16 a[24:27], v[168:171], v[136:139], a[24:27]\n"
    "v_mfma_f32_16x16x32_bf16 a[20:23], v[168:171], v[132:135], a[20:23]\n"
    "v_mfma_f32_16x16x32_bf16 a[20:23], v[172:175], v[148:151], a[20:23]\n"
    "v_mfma_f32_16x16x32_bf16 a[16:19], v[172:175], v[144:147], a[16:19]\n"
    "v_mfma_f32_16x16x32_bf16 a[16:19], v[168:171], v[128:131], a[16:19]\n"
    "ds_read_b128 v[176:179], v205 offset:16384\n"
    "ds_read_b128 v[180:183], v205 offset:17408\n"
    "s_waitcnt lgkmcnt(1)\n"
    "v_mfma_f32_16x16x32_bf16 a[32:35], v[176:179], v[128:131], a[32:35]\n"
    "s_waitcnt lgkmcnt(0)\n"
    "v_mfma_f32_16x16x32_bf16 a[32:35], v[180:183], v[144:147], a[32:35]\n"
    "v_mfma_f32_16x16x32_bf16 a[36:39], v[180:183], v[148:151], a[36:39]\n"
    "v_mfma_f32_16x16x32_bf16 a[36:39], v[176:179], v[132:135], a[36:39]\n"
    "v_mfma_f32_16x16x32_bf16 a[40:43], v[176:179], v[136:139], a[40:43]\n"
    "v_mfma_f32_16x16x32_bf16 a[40:43], v[180:183], v[152:155], a[40:43]\n"
    "v_mfma_f32_16x16x32_bf16 a[44:47], v[180:183], v[156:159], a[44:47]\n"
    "v_mfma_f32_16x16x32_bf16 a[44:47], v[176:179], v[140:143], a[44:47]\n"
    "ds_read_b128 v[160:163], v205 offset:18432\n"
    "ds_read_b128 v[164:167], v205 offset:19456\n"
    "s_waitcnt lgkmcnt(1)\n"
    "v_mfma_f32_16x16x32_bf16 a[60:63], v[160:163], v[140:143], a[60:63]\n"
    "s_waitcnt lgkmcnt(0)\n"
    "v_mfma_f32_16x16x32_bf16 a[60:63], v[164:167], v[156:159], a[60:63]\n"
    "v_mfma_f32_16x16x32_bf16 a[56:59], v[164:167], v[152:155], a[56:59]\n"
    "v_mfma_f32_16x16x32_bf16 a[56:59], v[160:163], v[136:139], a[56:59]\n"
    "v_mfma_f32_16x16x32_bf16 a[52:55], v[160:163], v[132:135], a[52:55]\n"
    "v_mfma_f32_16x16x32_bf16 a[52:55], v[164:167], v[148:151], a[52:55]\n"
    "v_mfma_f32_16x16x32_bf16 a[48:51], v[164:167], v[144:147], a[48:51]\n"
    "v_mfma_f32_16x16x32_bf16 a[48:51], v[160:163], v[128:131], a[48:51]\n"
    "ds_read_b128 v[168:171], v205 offset:20480\n"
    "ds_read_b128 v[172:175], v205 offset:21504\n"
    "s_waitcnt lgkmcnt(1)\n"
    "v_mfma_f32_16x16x32_bf16 a[64:67], v[168:171], v[128:131], a[64:67]\n"
    "s_waitcnt lgkmcnt(0)\n"
    "v_mfma_f32_16x16x32_bf16 a[64:67], v[172:175], v[144:147], a[64:67]\n"
    "v_mfma_f32_16x16x32_bf16 a[68:71], v[172:175], v[148:151], a[68:71]\n"
    "v_mfma_f32_16x16x32_bf16 a[68:71], v[168:171], v[132:135], a[68:71]\n"
    "v_mfma_f32_16x16x32_bf16 a[72:75], v[168:171], v[136:139], a[72:75]\n"
    "v_mfma_f32_16x16x32_bf16 a[72:75], v[172:175], v[152:155], a[72:75]\n"
    "v_mfma_f32_16x16x32_bf16 a[76:79], v[172:175], v[156:159], a[76:79]\n"
    "v_mfma_f32_16x16x32_bf16 a[76:79], v[168:171], v[140:143], a[76:79]\n"
    "ds_read_b128 v[176:179], v205 offset:22528\n"
    "ds_read_b128 v[180:183], v205 offset:23552\n"
    "s_waitcnt lgkmcnt(1)\n"
    "v_mfma_f32_16x16x32_bf16 a[92:95], v[176:179], v[140:143], a[92:95]\n"
    "s_waitcnt lgkmcnt(0)\n"
    "v_mfma_f32_16x16x32_bf16 a[92:95], v[180:183], v[156:159], a[92:95]\n"
    "v_mfma_f32_16x16x32_bf16 a[88:91], v[180:183], v[152:155], a[88:91]\n"
    "v_mfma_f32_16x16x32_bf16 a[88:91], v[176:179], v[136:139], a[88:91]\n"
    "v_mfma_f32_16x16x32_bf16 a[84:87], v[176:179], v[132:135], a[84:87]\n"
    "v_mfma_f32_16x16x32_bf16 a[84:87], v[180:183], v[148:151], a[84:87]\n"
    "v_mfma_f32_16x16x32_bf16 a[80:83], v[180:183], v[144:147], a[80:83]\n"
    "v_mfma_f32_16x16x32_bf16 a[80:83], v[176:179], v[128:131], a[80:83]\n"
    "ds_read_b128 v[160:163], v205 offset:24576\n"
    "ds_read_b128 v[164:167], v205 offset:25600\n"
    "s_waitcnt lgkmcnt(1)\n"
    "v_mfma_f32_16x16x32_bf16 a[96:99], v[160:163], v[128:131], a[96:99]\n"
    "s_waitcnt lgkmcnt(0)\n"
    "v_mfma_f32_16x16x32_bf16 a[96:99], v[164:167], v[144:147], a[96:99]\n"
    "v_mfma_f32_16x16x32_bf16 a[100:103], v[164:167], v[148:151], a[100:103]\n"
    "v_mfma_f32_16x16x32_bf16 a[100:103], v[160:163], v[132:135], a[100:103]\n"
    "v_mfma_f32_16x16x32_bf16 a[104:107], v[160:163], v[136:139], a[104:107]\n"
    "v_mfma_f32_16x16x32_bf16 a[104:107], v[164:167], v[152:155], a[104:107]\n"
    "v_mfma_f32_16x16x32_bf16 a[108:111], v[164:167], v[156:159], a[108:111]\n"
    "v_mfma_f32_16x16x32_bf16 a[108:111], v[160:163], v[140:143], a[108:111]\n"
    "ds_read_b128 v[168:171], v205 offset:26624\n"
    "ds_read_b128 v[172:175], v205 offset:27648\n"
    "s_waitcnt lgkmcnt(1)\n"
    "v_mfma_f32_16x16x32_bf16 a[124:127], v[168:171], v[140:143], a[124:127]\n"
    "s_waitcnt lgkmcnt(0)\n"
    "v_mfma_f32_16x16x32_bf16 a[124:127], v[172:175], v[156:159], a[124:127]\n"
    "v_mfma_f32_16x16x32_bf16 a[120:123], v[172:175], v[152:155], a[120:123]\n"
    "v_mfma_f32_16x16x32_bf16 a[120:123], v[168:171], v[136:139], a[120:123]\n"
    "v_mfma_f32_16x16x32_bf16 a[116:119], v[168:171], v[132:135], a[116:119]\n"
    "v_mfma_f32_16x16x32_bf16 a[116:119], v[172:175], v[148:151], a[116:119]\n"
    "v_mfma_f32_16x16x32_bf16 a[112:115], v[172:175], v[144:147], a[112:115]\n"
    "v_mfma_f32_16x16x32_bf16 a[112:115], v[168:171], v[128:131], a[112:115]\n"
    "ds_read_b128 v[176:179], v205 offset:28672\n"
    "ds_read_b128 v[180:183], v205 offset:29696\n"
    "s_waitcnt lgkmcnt(1)\n"
    "v_mfma_f32_16x16x32_bf16 a[128:131], v[176:179], v[128:131], a[128:131]\n"
    "s_waitcnt lgkmcnt(0)\n"
    "v_mfma_f32_16x16x32_bf16 a[128:131], v[180:183], v[144:147], a[128:131]\n"
    "v_mfma_f32_16x16x32_bf16 a[132:135], v[180:183], v[148:151], a[132:135]\n"
    "v_mfma_f32_16x16x32_bf16 a[132:135], v[176:179], v[132:135], a[132:135]\n"
    "v_mfma_f32_16x16x32_bf16 a[136:139], v[176:179], v[136:139], a[136:139]\n"
    "v_mfma_f32_16x16x32_bf16 a[136:139], v[180:183], v[152:155], a[136:139]\n"
    "v_mfma_f32_16x16x32_bf16 a[140:143], v[180:183], v[156:159], a[140:143]\n"
    "v_mfma_f32_16x16x32_bf16 a[140:143], v[176:179], v[140:143], a[140:143]\n"
    "ds_read_b128 v[160:163], v205 offset:30720\n"
    "ds_read_b128 v[164:167], v205 offset:31744\n"
    "s_waitcnt lgkmcnt(1)\n"
    "v_mfma_f32_16x16x32_bf16 a[156:159], v[160:163], v[140:143], a[156:159]\n"
    "s_waitcnt lgkmcnt(0)\n"
    "v_mfma_f32_16x16x32_bf16 a[156:159], v[164:167], v[156:159], a[156:159]\n"
    "v_mfma_f32_16x16x32_bf16 a[152:155], v[164:167], v[152:155], a[152:155]\n"
    "v_mfma_f32_16x16x32_bf16 a[152:155], v[160:163], v[136:139], a[152:155]\n"
    "v_mfma_f32_16x16x32_bf16 a[148:151], v[160:163], v[132:135], a[148:151]\n"
    "v_mfma_f32_16x16x32_bf16 a[148:151], v[164:167], v[148:151], a[148:151]\n"
    "v_mfma_f32_16x16x32_bf16 a[144:147], v[164:167], v[144:147], a[144:147]\n"
    "v_mfma_f32_16x16x32_bf16 a[144:147], v[160:163], v[128:131], a[144:147]\n"
    "ds_read_b128 v[168:171], v205 offset:32768\n"
    "ds_read_b128 v[172:175], v205 offset:33792\n"
    "s_waitcnt lgkmcnt(1)\n"
    "v_mfma_f32_16x16x32_bf16 a[160:163], v[168:171], v[128:131], a[160:163]\n"
    "s_waitcnt lgkmcnt(0)\n"
    "v_mfma_f32_16x16x32_bf16 a[160:163], v[172:175], v[144:147], a[160:163]\n"
    "v_mfma_f32_16x16x32_bf16 a[164:167], v[172:175], v[148:151], a[164:167]\n"
    "v_mfma_f32_16x16x32_bf16 a[164:167], v[168:171], v[132:135], a[164:167]\n"
    "v_mfma_f32_16x16x32_bf16 a[168:171], v[168:171], v[136:139], a[168:171]\n"
    "v_mfma_f32_16x16x32_bf16 a[168:171], v[172:175], v[152:155], a[168:171]\n"
    "v_mfma_f32_16x16x32_bf16 a[172:175], v[172:175], v[156:159], a[172:175]\n"
    "v_mfma_f32_16x16x32_bf16 a[172:175], v[168:171], v[140:143], a[172:175]\n"
    "ds_read_b128 v[176:179], v205 offset:34816\n"
    "ds_read_b128 v[180:183], v205 offset:35840\n"
    "s_waitcnt lgkmcnt(1)\n"
    "v_mfma_f32_16x16x32_bf16 a[188:191], v[176:179], v[140:143], a[188:191]\n"
    "s_waitcnt lgkmcnt(0)\n"
    "v_mfma_f32_16x16x32_bf16 a[188:191], v[180:183], v[156:159], a[188:191]\n"
    "v_mfma_f32_16x16x32_bf16 a[184:187], v[176:179], v[136:139], a[184:187]\n"
    "v_mfma_f32_16x16x32_bf16 a[184:187], v[180:183], v[152:155], a[184:187]\n"
    "v_mfma_f32_16x16x32_bf16 a[180:183], v[176:179], v[132:135], a[180:183]\n"
    "v_mfma_f32_16x16x32_bf16 a[180:183], v[180:183], v[148:151], a[180:183]\n"
    "v_mfma_f32_16x16x32_bf16 a[176:179], v[176:179], v[128:131], a[176:179]\n"
    "v_mfma_f32_16x16x32_bf16 a[176:179], v[180:183], v[144:147], a[176:179]\n"
    "v_mfma_f32_16x16x32_bf16 v[112:115], v[192:195], v[128:131], v[112:115]\n"
    "v_mfma_f32_16x16x32_bf16 v[112:115], v[192:195], v[144:147], v[112:115]\n"
    "v_mfma_f32_16x16x32_bf16 v[116:119], v[192:195], v[132:135], v[116:119]\n"
    "v_mfma_f32_16x16x32_bf16 v[116:119], v[192:195], v[148:151], v[116:119]\n"
    "v_mfma_f32_16x16x32_bf16 v[120:123], v[192:195], v[136:139], v[120:123]\n"
    "v_mfma_f32_16x16x32_bf16 v[120:123], v[192:195], v[152:155], v[120:123]\n"
    "v_mfma_f32_16x16x32_bf16 v[124:127], v[192:195], v[140:143], v[124:127]\n"
    "v_mfma_f32_16x16x32_bf16 v[124:127], v[192:195], v[156:159], v[124:127]\n"
    "s_nop 15\n"
    "s_add_u32 s63, s63, 0x6000\n"
    "s_cmp_lt_u32 s63, 0xc0000\n"
    "s_cbranch_scc1 LSLOOP\n"
    "s_branch LEPI\n"
    "LEPI:\n"
    "s_lshl_b32 s76, s56, 3\n"
    "s_add_u32 s76, s76, s57\n"
    "s_mul_i32 s76, s76, 0x18000\n"
    "s_add_u32 s76, s76, s88\n"
    "s_add_u32 s28, s24, s76\n"
    "s_addc_u32 s29, s25, 0\n"
    "s_and_b32 s29, s29, 0xffff\n"
    "s_mov_b32 s30, 0x6000\n"
    "s_mov_b32 s31, 0x20000\n"
    "s_xor_b32 s94, s70, 0x80000000\n"
    "s_xor_b32 s95, s70, 0x80000000\n"
    "s_mov_b32 s62, 0x1800\n"
    "s_mov_b32 s63, 0x3000\n"
    "s_mov_b32 s64, 0x4800\n"
    "v_and_b32 v198, 15, v231\n"
    "v_mul_u32_u24 v205, 0x180, v198\n"
    "v_lshrrev_b32 v199, 4, v231\n"
    "v_lshl_add_u32 v205, v199, 4, v205\n"
    "v_mul_f32 v222, s68, v112\n"
    "v_rcp_f32 v222, v222\n"
    "v_mul_f32 v224, s68, v116\n"
    "v_rcp_f32 v224, v224\n"
    "v_mul_f32 v226, s68, v120\n"
    "v_rcp_f32 v226, v226\n"
    "v_mul_f32 v228, s68, v124\n"
    "v_rcp_f32 v228, v228\n"
    "v_mov_b32 v223, v222\n"
    "v_mov_b32 v225, v224\n"
    "v_mov_b32 v227, v226\n"
    "v_mov_b32 v229, v228\n"
    "v_accvgpr_read_b32 v96, a0\n"
    "v_accvgpr_read_b32 v97, a1\n"
    "v_accvgpr_read_b32 v98, a2\n"
    "v_accvgpr_read_b32 v99, a3\n"
    "v_pk_fma_f32 v[96:97], v[96:97], v[222:223], s[94:95]\n"
    "v_pk_fma_f32 v[98:99], v[98:99], v[222:223], s[94:95]\n"
    "v_cvt_pk_bf16_f32 v32, v96, v97\n"
    "v_cvt_pk_bf16_f32 v33, v98, v99\n"
    "v_accvgpr_read_b32 v100, a16\n"
    "v_accvgpr_read_b32 v101, a17\n"
    "v_accvgpr_read_b32 v102, a18\n"
    "v_accvgpr_read_b32 v103, a19\n"
    "v_pk_fma_f32 v[100:101], v[100:101], v[222:223], s[94:95]\n"
    "v_pk_fma_f32 v[102:103], v[102:103], v[222:223], s[94:95]\n"
    "v_cvt_pk_bf16_f32 v34, v100, v101\n"
    "v_cvt_pk_bf16_f32 v35, v102, v103\n"
    "buffer_store_dwordx4 v[32:35], v205, s[28:31], 0 offen sc0 sc1\n"
    "v_accvgpr_read_b32 v104, a32\n"
    "v_accvgpr_read_b32 v105, a33\n"
    "v_accvgpr_read_b32 v106, a34\n"
    "v_accvgpr_read_b32 v107, a35\n"
    "v_pk_fma_f32 v[104:105], v[104:105], v[222:223], s[94:95]\n"
    "v_pk_fma_f32 v[106:107], v[106:107], v[222:223], s[94:95]\n"
    "v_cvt_pk_bf16_f32 v36, v104, v105\n"
    "v_cvt_pk_bf16_f32 v37, v106, v107\n"
    "v_accvgpr_read_b32 v108, a48\n"
    "v_accvgpr_read_b32 v109, a49\n"
    "v_accvgpr_read_b32 v110, a50\n"
    "v_accvgpr_read_b32 v111, a51\n"
    "v_pk_fma_f32 v[108:109], v[108:109], v[222:223], s[94:95]\n"
    "v_pk_fma_f32 v[110:111], v[110:111], v[222:223], s[94:95]\n"
    "v_cvt_pk_bf16_f32 v38, v108, v109\n"
    "v_cvt_pk_bf16_f32 v39, v110, v111\n"
    "buffer_store_dwordx4 v[36:39], v205, s[28:31], 0 offen offset:64 sc0 sc1\n"
    "v_accvgpr_read_b32 v96, a64\n"
    "v_accvgpr_read_b32 v97, a65\n"
    "v_accvgpr_read_b32 v98, a66\n"
    "v_accvgpr_read_b32 v99, a67\n"
    "v_pk_fma_f32 v[96:97], v[96:97], v[222:223], s[94:95]\n"
    "v_pk_fma_f32 v[98:99], v[98:99], v[222:223], s[94:95]\n"
    "v_cvt_pk_bf16_f32 v40, v96, v97\n"
    "v_cvt_pk_bf16_f32 v41, v98, v99\n"
    "v_accvgpr_read_b32 v100, a80\n"
    "v_accvgpr_read_b32 v101, a81\n"
    "v_accvgpr_read_b32 v102, a82\n"
    "v_accvgpr_read_b32 v103, a83\n"
    "v_pk_fma_f32 v[100:101], v[100:101], v[222:223], s[94:95]\n"
    "v_pk_fma_f32 v[102:103], v[102:103], v[222:223], s[94:95]\n"
    "v_cvt_pk_bf16_f32 v42, v100, v101\n"
    "v_cvt_pk_bf16_f32 v43, v102, v103\n"
    "buffer_store_dwordx4 v[40:43], v205, s[28:31], 0 offen offset:128 sc0 sc1\n"
    "v_accvgpr_read_b32 v104, a96\n"
    "v_accvgpr_read_b32 v105, a97\n"
    "v_accvgpr_read_b32 v106, a98\n"
    "v_accvgpr_read_b32 v107, a99\n"
    "v_pk_fma_f32 v[104:105], v[104:105], v[222:223], s[94:95]\n"
    "v_pk_fma_f32 v[106:107], v[106:107], v[222:223], s[94:95]\n"
    "v_cvt_pk_bf16_f32 v44, v104, v105\n"
    "v_cvt_pk_bf16_f32 v45, v106, v107\n"
    "v_accvgpr_read_b32 v108, a112\n"
    "v_accvgpr_read_b32 v109, a113\n"
    "v_accvgpr_read_b32 v110, a114\n"
    "v_accvgpr_read_b32 v111, a115\n"
    "v_pk_fma_f32 v[108:109], v[108:109], v[222:223], s[94:95]\n"
    "v_pk_fma_f32 v[110:111], v[110:111], v[222:223], s[94:95]\n"
    "v_cvt_pk_bf16_f32 v46, v108, v109\n"
    "v_cvt_pk_bf16_f32 v47, v110, v111\n"
    "buffer_store_dwordx4 v[44:47], v205, s[28:31], 0 offen offset:192 sc0 sc1\n"
    "v_accvgpr_read_b32 v96, a128\n"
    "v_accvgpr_read_b32 v97, a129\n"
    "v_accvgpr_read_b32 v98, a130\n"
    "v_accvgpr_read_b32 v99, a131\n"
    "v_pk_fma_f32 v[96:97], v[96:97], v[222:223], s[94:95]\n"
    "v_pk_fma_f32 v[98:99], v[98:99], v[222:223], s[94:95]\n"
    "v_cvt_pk_bf16_f32 v48, v96, v97\n"
    "v_cvt_pk_bf16_f32 v49, v98, v99\n"
    "v_accvgpr_read_b32 v100, a144\n"
    "v_accvgpr_read_b32 v101, a145\n"
    "v_accvgpr_read_b32 v102, a146\n"
    "v_accvgpr_read_b32 v103, a147\n"
    "v_pk_fma_f32 v[100:101], v[100:101], v[222:223], s[94:95]\n"
    "v_pk_fma_f32 v[102:103], v[102:103], v[222:223], s[94:95]\n"
    "v_cvt_pk_bf16_f32 v50, v100, v101\n"
    "v_cvt_pk_bf16_f32 v51, v102, v103\n"
    "buffer_store_dwordx4 v[48:51], v205, s[28:31], 0 offen offset:256 sc0 sc1\n"
    "v_accvgpr_read_b32 v104, a160\n"
    "v_accvgpr_read_b32 v105, a161\n"
    "v_accvgpr_read_b32 v106, a162\n"
    "v_accvgpr_read_b32 v107, a163\n"
    "v_pk_fma_f32 v[104:105], v[104:105], v[222:223], s[94:95]\n"
    "v_pk_fma_f32 v[106:107], v[106:107], v[222:223], s[94:95]\n"
    "v_cvt_pk_bf16_f32 v52, v104, v105\n"
    "v_cvt_pk_bf16_f32 v53, v106, v107\n"
    "v_accvgpr_read_b32 v108, a176\n"
    "v_accvgpr_read_b32 v109, a177\n"
    "v_accvgpr_read_b32 v110, a178\n"
    "v_accvgpr_read_b32 v111, a179\n"
    "v_pk_fma_f32 v[108:109], v[108:109], v[222:223], s[94:95]\n"
    "v_pk_fma_f32 v[110:111], v[110:111], v[222:223], s[94:95]\n"
    "v_cvt_pk_bf16_f32 v54, v108, v109\n"
    "v_cvt_pk_bf16_f32 v55, v110, v111\n"
    "buffer_store_dwordx4 v[52:55], v205, s[28:31], 0 offen offset:320 sc0 sc1\n"
    "v_accvgpr_read_b32 v96, a4\n"
    "v_accvgpr_read_b32 v97, a5\n"
    "v_accvgpr_read_b32 v98, a6\n"
    "v_accvgpr_read_b32 v99, a7\n"
    "v_pk_fma_f32 v[96:97], v[96:97], v[224:225], s[94:95]\n"
    "v_pk_fma_f32 v[98:99], v[98:99], v[224:225], s[94:95]\n"
    "v_cvt_pk_bf16_f32 v56, v96, v97\n"
    "v_cvt_pk_bf16_f32 v57, v98, v99\n"
    "v_accvgpr_read_b32 v100, a20\n"
    "v_accvgpr_read_b32 v101, a21\n"
    "v_accvgpr_read_b32 v102, a22\n"
    "v_accvgpr_read_b32 v103, a23\n"
    "v_pk_fma_f32 v[100:101], v[100:101], v[224:225], s[94:95]\n"
    "v_pk_fma_f32 v[102:103], v[102:103], v[224:225], s[94:95]\n"
    "v_cvt_pk_bf16_f32 v58, v100, v101\n"
    "v_cvt_pk_bf16_f32 v59, v102, v103\n"
    "buffer_store_dwordx4 v[56:59], v205, s[28:31], s62 offen sc0 sc1\n"
    "v_accvgpr_read_b32 v104, a36\n"
    "v_accvgpr_read_b32 v105, a37\n"
    "v_accvgpr_read_b32 v106, a38\n"
    "v_accvgpr_read_b32 v107, a39\n"
    "v_pk_fma_f32 v[104:105], v[104:105], v[224:225], s[94:95]\n"
    "v_pk_fma_f32 v[106:107], v[106:107], v[224:225], s[94:95]\n"
    "v_cvt_pk_bf16_f32 v60, v104, v105\n"
    "v_cvt_pk_bf16_f32 v61, v106, v107\n"
    "v_accvgpr_read_b32 v108, a52\n"
    "v_accvgpr_read_b32 v109, a53\n"
    "v_accvgpr_read_b32 v110, a54\n"
    "v_accvgpr_read_b32 v111, a55\n"
    "v_pk_fma_f32 v[108:109], v[108:109], v[224:225], s[94:95]\n"
    "v_pk_fma_f32 v[110:111], v[110:111], v[224:225], s[94:95]\n"
    "v_cvt_pk_bf16_f32 v62, v108, v109\n"
    "v_cvt_pk_bf16_f32 v63, v110, v111\n"
    "buffer_store_dwordx4 v[60:63], v205, s[28:31], s62 offen offset:64 sc0 sc1\n"
    "v_accvgpr_read_b32 v96, a68\n"
    "v_accvgpr_read_b32 v97, a69\n"
    "v_accvgpr_read_b32 v98, a70\n"
    "v_accvgpr_read_b32 v99, a71\n"
    "v_pk_fma_f32 v[96:97], v[96:97], v[224:225], s[94:95]\n"
    "v_pk_fma_f32 v[98:99], v[98:99], v[224:225], s[94:95]\n"
    "v_cvt_pk_bf16_f32 v32, v96, v97\n"
    "v_cvt_pk_bf16_f32 v33, v98, v99\n"
    "v_accvgpr_read_b32 v100, a84\n"
    "v_accvgpr_read_b32 v101, a85\n"
    "v_accvgpr_read_b32 v102, a86\n"
    "v_accvgpr_read_b32 v103, a87\n"
    "v_pk_fma_f32 v[100:101], v[100:101], v[224:225], s[94:95]\n"
    "v_pk_fma_f32 v[102:103], v[102:103], v[224:225], s[94:95]\n"
    "v_cvt_pk_bf16_f32 v34, v100, v101\n"
    "v_cvt_pk_bf16_f32 v35, v102, v103\n"
    "buffer_store_dwordx4 v[32:35], v205, s[28:31], s62 offen offset:128 sc0 sc1\n"
    "v_accvgpr_read_b32 v104, a100\n"
    "v_accvgpr_read_b32 v105, a101\n"
    "v_accvgpr_read_b32 v106, a102\n"
    "v_accvgpr_read_b32 v107, a103\n"
    "v_pk_fma_f32 v[104:105], v[104:105], v[224:225], s[94:95]\n"
    "v_pk_fma_f32 v[106:107], v[106:107], v[224:225], s[94:95]\n"
    "v_cvt_pk_bf16_f32 v36, v104, v105\n"
    "v_cvt_pk_bf16_f32 v37, v106, v107\n"
    "v_accvgpr_read_b32 v108, a116\n"
    "v_accvgpr_read_b32 v109, a117\n"
    "v_accvgpr_read_b32 v110, a118\n"
    "v_accvgpr_read_b32 v111, a119\n"
    "v_pk_fma_f32 v[108:109], v[108:109], v[224:225], s[94:95]\n"
    "v_pk_fma_f32 v[110:111], v[110:111], v[224:225], s[94:95]\n"
    "v_cvt_pk_bf16_f32 v38, v108, v109\n"
    "v_cvt_pk_bf16_f32 v39, v110, v111\n"
    "buffer_store_dwordx4 v[36:39], v205, s[28:31], s62 offen offset:192 sc0 sc1\n"
    "v_accvgpr_read_b32 v96, a132\n"
    "v_accvgpr_read_b32 v97, a133\n"
    "v_accvgpr_read_b32 v98, a134\n"
    "v_accvgpr_read_b32 v99, a135\n"
    "v_pk_fma_f32 v[96:97], v[96:97], v[224:225], s[94:95]\n"
    "v_pk_fma_f32 v[98:99], v[98:99], v[224:225], s[94:95]\n"
    "v_cvt_pk_bf16_f32 v40, v96, v97\n"
    "v_cvt_pk_bf16_f32 v41, v98, v99\n"
    "v_accvgpr_read_b32 v100, a148\n"
    "v_accvgpr_read_b32 v101, a149\n"
    "v_accvgpr_read_b32 v102, a150\n"
    "v_accvgpr_read_b32 v103, a151\n"
    "v_pk_fma_f32 v[100:101], v[100:101], v[224:225], s[94:95]\n"
    "v_pk_fma_f32 v[102:103], v[102:103], v[224:225], s[94:95]\n"
    "v_cvt_pk_bf16_f32 v42, v100, v101\n"
    "v_cvt_pk_bf16_f32 v43, v102, v103\n"
    "buffer_store_dwordx4 v[40:43], v205, s[28:31], s62 offen offset:256 sc0 sc1\n"
    "v_accvgpr_read_b32 v104, a164\n"
    "v_accvgpr_read_b32 v105, a165\n"
    "v_accvgpr_read_b32 v106, a166\n"
    "v_accvgpr_read_b32 v107, a167\n"
    "v_pk_fma_f32 v[104:105], v[104:105], v[224:225], s[94:95]\n"
    "v_pk_fma_f32 v[106:107], v[106:107], v[224:225], s[94:95]\n"
    "v_cvt_pk_bf16_f32 v44, v104, v105\n"
    "v_cvt_pk_bf16_f32 v45, v106, v107\n"
    "v_accvgpr_read_b32 v108, a180\n"
    "v_accvgpr_read_b32 v109, a181\n"
    "v_accvgpr_read_b32 v110, a182\n"
    "v_accvgpr_read_b32 v111, a183\n"
    "v_pk_fma_f32 v[108:109], v[108:109], v[224:225], s[94:95]\n"
    "v_pk_fma_f32 v[110:111], v[110:111], v[224:225], s[94:95]\n"
    "v_cvt_pk_bf16_f32 v46, v108, v109\n"
    "v_cvt_pk_bf16_f32 v47, v110, v111\n"
    "buffer_store_dwordx4 v[44:47], v205, s[28:31], s62 offen offset:320 sc0 sc1\n"
    "v_accvgpr_read_b32 v96, a8\n"
    "v_accvgpr_read_b32 v97, a9\n"
    "v_accvgpr_read_b32 v98, a10\n"
    "v_accvgpr_read_b32 v99, a11\n"
    "v_pk_fma_f32 v[96:97], v[96:97], v[226:227], s[94:95]\n"
    "v_pk_fma_f32 v[98:99], v[98:99], v[226:227], s[94:95]\n"
    "v_cvt_pk_bf16_f32 v48, v96, v97\n"
    "v_cvt_pk_bf16_f32 v49, v98, v99\n"
    "v_accvgpr_read_b32 v100, a24\n"
    "v_accvgpr_read_b32 v101, a25\n"
    "v_accvgpr_read_b32 v102, a26\n"
    "v_accvgpr_read_b32 v103, a27\n"
    "v_pk_fma_f32 v[100:101], v[100:101], v[226:227], s[94:95]\n"
    "v_pk_fma_f32 v[102:103], v[102:103], v[226:227], s[94:95]\n"
    "v_cvt_pk_bf16_f32 v50, v100, v101\n"
    "v_cvt_pk_bf16_f32 v51, v102, v103\n"
    "buffer_store_dwordx4 v[48:51], v205, s[28:31], s63 offen sc0 sc1\n"
    "v_accvgpr_read_b32 v104, a40\n"
    "v_accvgpr_read_b32 v105, a41\n"
    "v_accvgpr_read_b32 v106, a42\n"
    "v_accvgpr_read_b32 v107, a43\n"
    "v_pk_fma_f32 v[104:105], v[104:105], v[226:227], s[94:95]\n"
    "v_pk_fma_f32 v[106:107], v[106:107], v[226:227], s[94:95]\n"
    "v_cvt_pk_bf16_f32 v52, v104, v105\n"
    "v_cvt_pk_bf16_f32 v53, v106, v107\n"
    "v_accvgpr_read_b32 v108, a56\n"
    "v_accvgpr_read_b32 v109, a57\n"
    "v_accvgpr_read_b32 v110, a58\n"
    "v_accvgpr_read_b32 v111, a59\n"
    "v_pk_fma_f32 v[108:109], v[108:109], v[226:227], s[94:95]\n"
    "v_pk_fma_f32 v[110:111], v[110:111], v[226:227], s[94:95]\n"
    "v_cvt_pk_bf16_f32 v54, v108, v109\n"
    "v_cvt_pk_bf16_f32 v55, v110, v111\n"
    "buffer_store_dwordx4 v[52:55], v205, s[28:31], s63 offen offset:64 sc0 sc1\n"
    "v_accvgpr_read_b32 v96, a72\n"
    "v_accvgpr_read_b32 v97, a73\n"
    "v_accvgpr_read_b32 v98, a74\n"
    "v_accvgpr_read_b32 v99, a75\n"
    "v_pk_fma_f32 v[96:97], v[96:97], v[226:227], s[94:95]\n"
    "v_pk_fma_f32 v[98:99], v[98:99], v[226:227], s[94:95]\n"
    "v_cvt_pk_bf16_f32 v56, v96, v97\n"
    "v_cvt_pk_bf16_f32 v57, v98, v99\n"
    "v_accvgpr_read_b32 v100, a88\n"
    "v_accvgpr_read_b32 v101, a89\n"
    "v_accvgpr_read_b32 v102, a90\n"
    "v_accvgpr_read_b32 v103, a91\n"
    "v_pk_fma_f32 v[100:101], v[100:101], v[226:227], s[94:95]\n"
    "v_pk_fma_f32 v[102:103], v[102:103], v[226:227], s[94:95]\n"
    "v_cvt_pk_bf16_f32 v58, v100, v101\n"
    "v_cvt_pk_bf16_f32 v59, v102, v103\n"
    "buffer_store_dwordx4 v[56:59], v205, s[28:31], s63 offen offset:128 sc0 sc1\n"
    "v_accvgpr_read_b32 v104, a104\n"
    "v_accvgpr_read_b32 v105, a105\n"
    "v_accvgpr_read_b32 v106, a106\n"
    "v_accvgpr_read_b32 v107, a107\n"
    "v_pk_fma_f32 v[104:105], v[104:105], v[226:227], s[94:95]\n"
    "v_pk_fma_f32 v[106:107], v[106:107], v[226:227], s[94:95]\n"
    "v_cvt_pk_bf16_f32 v60, v104, v105\n"
    "v_cvt_pk_bf16_f32 v61, v106, v107\n"
    "v_accvgpr_read_b32 v108, a120\n"
    "v_accvgpr_read_b32 v109, a121\n"
    "v_accvgpr_read_b32 v110, a122\n"
    "v_accvgpr_read_b32 v111, a123\n"
    "v_pk_fma_f32 v[108:109], v[108:109], v[226:227], s[94:95]\n"
    "v_pk_fma_f32 v[110:111], v[110:111], v[226:227], s[94:95]\n"
    "v_cvt_pk_bf16_f32 v62, v108, v109\n"
    "v_cvt_pk_bf16_f32 v63, v110, v111\n"
    "buffer_store_dwordx4 v[60:63], v205, s[28:31], s63 offen offset:192 sc0 sc1\n"
    "v_accvgpr_read_b32 v96, a136\n"
    "v_accvgpr_read_b32 v97, a137\n"
    "v_accvgpr_read_b32 v98, a138\n"
    "v_accvgpr_read_b32 v99, a139\n"
    "v_pk_fma_f32 v[96:97], v[96:97], v[226:227], s[94:95]\n"
    "v_pk_fma_f32 v[98:99], v[98:99], v[226:227], s[94:95]\n"
    "v_cvt_pk_bf16_f32 v32, v96, v97\n"
    "v_cvt_pk_bf16_f32 v33, v98, v99\n"
    "v_accvgpr_read_b32 v100, a152\n"
    "v_accvgpr_read_b32 v101, a153\n"
    "v_accvgpr_read_b32 v102, a154\n"
    "v_accvgpr_read_b32 v103, a155\n"
    "v_pk_fma_f32 v[100:101], v[100:101], v[226:227], s[94:95]\n"
    "v_pk_fma_f32 v[102:103], v[102:103], v[226:227], s[94:95]\n"
    "v_cvt_pk_bf16_f32 v34, v100, v101\n"
    "v_cvt_pk_bf16_f32 v35, v102, v103\n"
    "buffer_store_dwordx4 v[32:35], v205, s[28:31], s63 offen offset:256 sc0 sc1\n"
    "v_accvgpr_read_b32 v104, a168\n"
    "v_accvgpr_read_b32 v105, a169\n"
    "v_accvgpr_read_b32 v106, a170\n"
    "v_accvgpr_read_b32 v107, a171\n"
    "v_pk_fma_f32 v[104:105], v[104:105], v[226:227], s[94:95]\n"
    "v_pk_fma_f32 v[106:107], v[106:107], v[226:227], s[94:95]\n"
    "v_cvt_pk_bf16_f32 v36, v104, v105\n"
    "v_cvt_pk_bf16_f32 v37, v106, v107\n"
    "v_accvgpr_read_b32 v108, a184\n"
    "v_accvgpr_read_b32 v109, a185\n"
    "v_accvgpr_read_b32 v110, a186\n"
    "v_accvgpr_read_b32 v111, a187\n"
    "v_pk_fma_f32 v[108:109], v[108:109], v[226:227], s[94:95]\n"
    "v_pk_fma_f32 v[110:111], v[110:111], v[226:227], s[94:95]\n"
    "v_cvt_pk_bf16_f32 v38, v108, v109\n"
    "v_cvt_pk_bf16_f32 v39, v110, v111\n"
    "buffer_store_dwordx4 v[36:39], v205, s[28:31], s63 offen offset:320 sc0 sc1\n"
    "v_accvgpr_read_b32 v96, a12\n"
    "v_accvgpr_read_b32 v97, a13\n"
    "v_accvgpr_read_b32 v98, a14\n"
    "v_accvgpr_read_b32 v99, a15\n"
    "v_pk_fma_f32 v[96:97], v[96:97], v[228:229], s[94:95]\n"
    "v_pk_fma_f32 v[98:99], v[98:99], v[228:229], s[94:95]\n"
    "v_cvt_pk_bf16_f32 v40, v96, v97\n"
    "v_cvt_pk_bf16_f32 v41, v98, v99\n"
    "v_accvgpr_read_b32 v100, a28\n"
    "v_accvgpr_read_b32 v101, a29\n"
    "v_accvgpr_read_b32 v102, a30\n"
    "v_accvgpr_read_b32 v103, a31\n"
    "v_pk_fma_f32 v[100:101], v[100:101], v[228:229], s[94:95]\n"
    "v_pk_fma_f32 v[102:103], v[102:103], v[228:229], s[94:95]\n"
    "v_cvt_pk_bf16_f32 v42, v100, v101\n"
    "v_cvt_pk_bf16_f32 v43, v102, v103\n"
    "buffer_store_dwordx4 v[40:43], v205, s[28:31], s64 offen sc0 sc1\n"
    "v_accvgpr_read_b32 v104, a44\n"
    "v_accvgpr_read_b32 v105, a45\n"
    "v_accvgpr_read_b32 v106, a46\n"
    "v_accvgpr_read_b32 v107, a47\n"
    "v_pk_fma_f32 v[104:105], v[104:105], v[228:229], s[94:95]\n"
    "v_pk_fma_f32 v[106:107], v[106:107], v[228:229], s[94:95]\n"
    "v_cvt_pk_bf16_f32 v44, v104, v105\n"
    "v_cvt_pk_bf16_f32 v45, v106, v107\n"
    "v_accvgpr_read_b32 v108, a60\n"
    "v_accvgpr_read_b32 v109, a61\n"
    "v_accvgpr_read_b32 v110, a62\n"
    "v_accvgpr_read_b32 v111, a63\n"
    "v_pk_fma_f32 v[108:109], v[108:109], v[228:229], s[94:95]\n"
    "v_pk_fma_f32 v[110:111], v[110:111], v[228:229], s[94:95]\n"
    "v_cvt_pk_bf16_f32 v46, v108, v109\n"
    "v_cvt_pk_bf16_f32 v47, v110, v111\n"
    "buffer_store_dwordx4 v[44:47], v205, s[28:31], s64 offen offset:64 sc0 sc1\n"
    "v_accvgpr_read_b32 v96, a76\n"
    "v_accvgpr_read_b32 v97, a77\n"
    "v_accvgpr_read_b32 v98, a78\n"
    "v_accvgpr_read_b32 v99, a79\n"
    "v_pk_fma_f32 v[96:97], v[96:97], v[228:229], s[94:95]\n"
    "v_pk_fma_f32 v[98:99], v[98:99], v[228:229], s[94:95]\n"
    "v_cvt_pk_bf16_f32 v48, v96, v97\n"
    "v_cvt_pk_bf16_f32 v49, v98, v99\n"
    "v_accvgpr_read_b32 v100, a92\n"
    "v_accvgpr_read_b32 v101, a93\n"
    "v_accvgpr_read_b32 v102, a94\n"
    "v_accvgpr_read_b32 v103, a95\n"
    "v_pk_fma_f32 v[100:101], v[100:101], v[228:229], s[94:95]\n"
    "v_pk_fma_f32 v[102:103], v[102:103], v[228:229], s[94:95]\n"
    "v_cvt_pk_bf16_f32 v50, v100, v101\n"
    "v_cvt_pk_bf16_f32 v51, v102, v103\n"
    "buffer_store_dwordx4 v[48:51], v205, s[28:31], s64 offen offset:128 sc0 sc1\n"
    "v_accvgpr_read_b32 v104, a108\n"
    "v_accvgpr_read_b32 v105, a109\n"
    "v_accvgpr_read_b32 v106, a110\n"
    "v_accvgpr_read_b32 v107, a111\n"
    "v_pk_fma_f32 v[104:105], v[104:105], v[228:229], s[94:95]\n"
    "v_pk_fma_f32 v[106:107], v[106:107], v[228:229], s[94:95]\n"
    "v_cvt_pk_bf16_f32 v52, v104, v105\n"
    "v_cvt_pk_bf16_f32 v53, v106, v107\n"
    "v_accvgpr_read_b32 v108, a124\n"
    "v_accvgpr_read_b32 v109, a125\n"
    "v_accvgpr_read_b32 v110, a126\n"
    "v_accvgpr_read_b32 v111, a127\n"
    "v_pk_fma_f32 v[108:109], v[108:109], v[228:229], s[94:95]\n"
    "v_pk_fma_f32 v[110:111], v[110:111], v[228:229], s[94:95]\n"
    "v_cvt_pk_bf16_f32 v54, v108, v109\n"
    "v_cvt_pk_bf16_f32 v55, v110, v111\n"
    "buffer_store_dwordx4 v[52:55], v205, s[28:31], s64 offen offset:192 sc0 sc1\n"
    "v_accvgpr_read_b32 v96, a140\n"
    "v_accvgpr_read_b32 v97, a141\n"
    "v_accvgpr_read_b32 v98, a142\n"
    "v_accvgpr_read_b32 v99, a143\n"
    "v_pk_fma_f32 v[96:97], v[96:97], v[228:229], s[94:95]\n"
    "v_pk_fma_f32 v[98:99], v[98:99], v[228:229], s[94:95]\n"
    "v_cvt_pk_bf16_f32 v56, v96, v97\n"
    "v_cvt_pk_bf16_f32 v57, v98, v99\n"
    "v_accvgpr_read_b32 v100, a156\n"
    "v_accvgpr_read_b32 v101, a157\n"
    "v_accvgpr_read_b32 v102, a158\n"
    "v_accvgpr_read_b32 v103, a159\n"
    "v_pk_fma_f32 v[100:101], v[100:101], v[228:229], s[94:95]\n"
    "v_pk_fma_f32 v[102:103], v[102:103], v[228:229], s[94:95]\n"
    "v_cvt_pk_bf16_f32 v58, v100, v101\n"
    "v_cvt_pk_bf16_f32 v59, v102, v103\n"
    "buffer_store_dwordx4 v[56:59], v205, s[28:31], s64 offen offset:256 sc0 sc1\n"
    "v_accvgpr_read_b32 v104, a172\n"
    "v_accvgpr_read_b32 v105, a173\n"
    "v_accvgpr_read_b32 v106, a174\n"
    "v_accvgpr_read_b32 v107, a175\n"
    "v_pk_fma_f32 v[104:105], v[104:105], v[228:229], s[94:95]\n"
    "v_pk_fma_f32 v[106:107], v[106:107], v[228:229], s[94:95]\n"
    "v_cvt_pk_bf16_f32 v60, v104, v105\n"
    "v_cvt_pk_bf16_f32 v61, v106, v107\n"
    "v_accvgpr_read_b32 v108, a188\n"
    "v_accvgpr_read_b32 v109, a189\n"
    "v_accvgpr_read_b32 v110, a190\n"
    "v_accvgpr_read_b32 v111, a191\n"
    "v_pk_fma_f32 v[108:109], v[108:109], v[228:229], s[94:95]\n"
    "v_pk_fma_f32 v[110:111], v[110:111], v[228:229], s[94:95]\n"
    "v_cvt_pk_bf16_f32 v62, v108, v109\n"
    "v_cvt_pk_bf16_f32 v63, v110, v111\n"
    "buffer_store_dwordx4 v[60:63], v205, s[28:31], s64 offen offset:320 sc0 sc1\n"
    "s_branch LFIN\n"
    "LEND:\n"
    :
    : "s"(__builtin_amdgcn_kernarg_segment_ptr()), "s"(blockIdx.x), "s"(wid)
    : "v0", "v1", "v2", "v3", "v4", "v5", "v6", "v7", "v8", "v9", "v10", "v11", "v12", "v13", "v14", "v15", "v16", "v17", "v18", "v19", "v20", "v21", "v22", "v23", "v24", "v25", "v26", "v27", "v28", "v29", "v30", "v31", "v32", "v33", "v34", "v35", "v36", "v37", "v38", "v39", "v40", "v41", "v42", "v43", "v44", "v45", "v46", "v47", "v48", "v49", "v50", "v51", "v52", "v53", "v54", "v55", "v56", "v57", "v58", "v59", "v60", "v61", "v62", "v63", "v64", "v65", "v66", "v67", "v68", "v69", "v70", "v71", "v72", "v73", "v74", "v75", "v76", "v77", "v78", "v79", "v80", "v81", "v82", "v83", "v84", "v85", "v86", "v87", "v88", "v89", "v90", "v91", "v92", "v93", "v94", "v95", "v96", "v97", "v98", "v99", "v100", "v101", "v102", "v103", "v104", "v105", "v106", "v107", "v108", "v109", "v110", "v111", "v112", "v113", "v114", "v115", "v116", "v117", "v118", "v119", "v120", "v121", "v122", "v123", "v124", "v125", "v126", "v127", "v128", "v129", "v130", "v131", "v132", "v133", "v134", "v135", "v136", "v137", "v138", "v139", "v140", "v141", "v142", "v143", "v144", "v145", "v146", "v147", "v148", "v149", "v150", "v151", "v152", "v153", "v154", "v155", "v156", "v157", "v158", "v159", "v160", "v161", "v162", "v163", "v164", "v165", "v166", "v167", "v168", "v169", "v170", "v171", "v172", "v173", "v174", "v175", "v176", "v177", "v178", "v179", "v180", "v181", "v182", "v183", "v184", "v185", "v186", "v187", "v188", "v189", "v190", "v191", "v192", "v193", "v194", "v195", "v196", "v197", "v198", "v199", "v200", "v201", "v202", "v203", "v204", "v205", "v206", "v207", "v208", "v209", "v210", "v211", "v212", "v213", "v214", "v215", "v216", "v217", "v218", "v219", "v220", "v221", "v222", "v223", "v224", "v225", "v226", "v227", "v228", "v229", "v230", "v231", "v232", "v233", "v234", "v235", "v236", "v237", "v238", "v239", "v240", "v241", "v242", "v243", "v244", "v245", "v246", "v247", "v248", "v249", "v250", "v251", "v252", "v253", "v254", "v255", "a0", "a1", "a2", "a3", "a4", "a5", "a6", "a7", "a8", "a9", "a10", "a11", "a12", "a13", "a14", "a15", "a16", "a17", "a18", "a19", "a20", "a21", "a22", "a23", "a24", "a25", "a26", "a27", "a28", "a29", "a30", "a31", "a32", "a33", "a34", "a35", "a36", "a37", "a38", "a39", "a40", "a41", "a42", "a43", "a44", "a45", "a46", "a47", "a48", "a49", "a50", "a51", "a52", "a53", "a54", "a55", "a56", "a57", "a58", "a59", "a60", "a61", "a62", "a63", "a64", "a65", "a66", "a67", "a68", "a69", "a70", "a71", "a72", "a73", "a74", "a75", "a76", "a77", "a78", "a79", "a80", "a81", "a82", "a83", "a84", "a85", "a86", "a87", "a88", "a89", "a90", "a91", "a92", "a93", "a94", "a95", "a96", "a97", "a98", "a99", "a100", "a101", "a102", "a103", "a104", "a105", "a106", "a107", "a108", "a109", "a110", "a111", "a112", "a113", "a114", "a115", "a116", "a117", "a118", "a119", "a120", "a121", "a122", "a123", "a124", "a125", "a126", "a127", "a128", "a129", "a130", "a131", "a132", "a133", "a134", "a135", "a136", "a137", "a138", "a139", "a140", "a141", "a142", "a143", "a144", "a145", "a146", "a147", "a148", "a149", "a150", "a151", "a152", "a153", "a154", "a155", "a156", "a157", "a158", "a159", "a160", "a161", "a162", "a163", "a164", "a165", "a166", "a167", "a168", "a169", "a170", "a171", "a172", "a173", "a174", "a175", "a176", "a177", "a178", "a179", "a180", "a181", "a182", "a183", "a184", "a185", "a186", "a187", "a188", "a189", "a190", "a191", "a192", "a193", "a194", "a195", "a196", "a197", "a198", "a199", "a200", "a201", "a202", "a203", "a204", "a205", "a206", "a207", "a208", "a209", "a210", "a211", "a212", "a213", "a214", "a215", "a216", "a217", "a218", "a219", "a220", "a221", "a222", "a223", "a224", "a225", "a226", "a227", "a228", "a229", "a230", "a231", "a232", "a233", "a234", "a235", "a236", "a237", "a238", "a239", "a240", "a241", "a242", "a243", "a244", "a245", "a246", "a247", "a248", "a249", "a250", "a251", "a252", "a253", "a254", "a255", "s8", "s9", "s10", "s11", "s12", "s13", "s14", "s15", "s16", "s17", "s18", "s19", "s20", "s21", "s22", "s23", "s24", "s25", "s26", "s27", "s28", "s29", "s30", "s31", "s32", "s33", "s34", "s35", "s36", "s37", "s38", "s39", "s40", "s41", "s42", "s43", "s44", "s45", "s46", "s47", "s48", "s49", "s50", "s51", "s52", "s53", "s54", "s55", "s56", "s57", "s58", "s59", "s60", "s61", "s62", "s63", "s64", "s65", "s66", "s67", "s68", "s69", "s70", "s71", "s72", "s73", "s74", "s75", "s76", "s77", "s78", "s79", "s80", "s81", "s82", "s83", "s84", "s85", "s86", "s87", "s88", "s89", "s90", "s91", "s92", "s93", "s94", "s95", "s96", "s97", "s98", "s99", "s100", "s101", "vcc", "scc", "memory");
}

extern "C" void kernel_launch(void* const* d_in, const int* in_sizes, int n_in, void* d_out, int out_size, void* d_ws, size_t ws_size,
                              hipStream_t stream) {
  (void)hipFuncSetAttribute((const void*)attn_fused, hipFuncAttributeMaxDynamicSharedMemorySize, 153088);
  attn_fused<<<256, 256, 153088, stream>>>(d_in[0], d_in[1], d_in[2], d_in[3], d_in[4], d_in[5], d_in[6], d_ws, d_out);
}
```

```cpp
#include <hip/hip_runtime.h>

__global__ __launch_bounds__(256, 1) void attn_fused(const void* q, const void* qk, const void* qv, const void* ks, const void* kz,
                                                     const void* vs, const void* vz, void* ws, void* out) {
  const int wid = __builtin_amdgcn_readfirstlane(threadIdx.x >> 6);
  asm volatile(
    "LS0:\n"
    "s_load_dwordx16 s[8:23], %0, 0x0\n"
    "s_mov_b32 s58, %2\n"
    "s_and_b32 s76, %1, 7\n"
    "s_lshr_b32 s77, %1, 3\n"
    "s_and_b32 s78, s77, 3\n"
    "s_lshl_b32 s56, s76, 2\n"
    "s_add_u32 s56, s56, s78\n"
    "s_lshr_b32 s57, s77, 2\n"
    "v_mbcnt_lo_u32_b32 v231, -1, 0\n"
    "v_mbcnt_hi_u32_b32 v231, -1, v231\n"
    "s_getpc_b64 s[44:45]\n"
    "LPC:\n"
    "s_mov_b32 s46, LSLOW-LPC\n"
    "s_mov_b32 s47, 0x20000\n"
    "s_lshr_b32 s76, %1, 3\n"
    "s_lshl_b32 s76, s76, 2\n"
    "s_add_u32 s76, s76, s58\n"
    "s_lshl_b32 s76, s76, 9\n"
    "v_lshlrev_b32 v196, 3, v231\n"
    "buffer_load_dword v251, v196, s[44:47], s76 offen\n"
    "s_mul_i32 s72, s58, 0xc00\n"
    "s_lshr_b32 s86, s57, 2\n"
    "s_mul_i32 s64, s86, 0xc000\n"
    "s_lshl_b32 s86, s86, 3\n"
    "s_and_b32 s79, s57, 3\n"
    "s_add_u32 s86, s86, s79\n"
    "s_lshl_b32 s86, s86, 2\n"
    "s_xor_b32 s76, s57, 4\n"
    "s_and_b32 s77, s76, 6\n"
    "s_mul_i32 s77, s77, 15\n"
    "s_add_u32 s76, s76, s77\n"
    "s_and_b32 s77, s58, 1\n"
    "s_mul_i32 s78, s77, 12\n"
    "s_add_u32 s78, s78, s86\n"
    "s_lshl_b32 s78, s78, 1\n"
    "s_mul_i32 s77, s77, 0xc0\n"
    "s_movk_i32 s90, 0x556\n"
    "s_lshl_b32 s60, s56, 12\n"
    "s_cmp_lt_u32 s58, 2\n"
    "s_cselect_b32 s76, s76, s78\n"
    "s_cmovk_i32 s90, 0xaab\n"
    "s_cselect_b32 s91, 24, 0\n"
    "s_cselect_b32 s92, 1, 10\n"
    "s_cselect_b32 s89, 7, 0\n"
    "s_cselect_b32 s64, s64, 0\n"
    "s_add_u32 s60, s60, s76\n"
    "s_mul_i32 s60, s60, 0x180\n"
    "s_mul_i32 s76, s76, 0x180\n"
    "s_sub_u32 s34, 0x180000, s76\n"
    "s_mov_b32 s35, 0x20000\n"
    "v_add_u32 v144, s77, v231\n"
    "v_add_u32 v145, 64, v144\n"
    "v_add_u32 v146, 64, v145\n"
    "v_mul_u32_u24 v197, s90, v144\n"
    "v_lshrrev_b32 v197, 16, v197\n"
    "v_mul_u32_u24 v198, s92, v197\n"
    "v_lshrrev_b32 v198, 1, v198\n"
    "v_and_b32 v198, s89, v198\n"
    "v_xor_b32 v198, v198, v144\n"
    "v_mul_u32_u24 v197, s91, v197\n"
    "v_add_lshl_u32 v202, v197, v198, 4\n"
    "v_mul_u32_u24 v197, s90, v145\n"
    "v_lshrrev_b32 v197, 16, v197\n"
    "v_mul_u32_u24 v198, s92, v197\n"
    "v_lshrrev_b32 v198, 1, v198\n"
    "v_and_b32 v198, s89, v198\n"
    "v_xor_b32 v198, v198, v144\n"
    "v_mul_u32_u24 v197, s91, v197\n"
    "v_add_lshl_u32 v203, v197, v198, 4\n"
    "v_mul_u32_u24 v197, s90, v146\n"
    "v_lshrrev_b32 v197, 16, v197\n"
    "v_mul_u32_u24 v198, s92, v197\n"
    "v_lshrrev_b32 v198, 1, v198\n"
    "v_and_b32 v198, s89, v198\n"
    "v_xor_b32 v198, v198, v144\n"
    "v_mul_u32_u24 v197, s91, v197\n"
    "v_add_lshl_u32 v204, v197, v198, 4\n"
    "s_waitcnt lgkmcnt(0)\n"
    "s_cmp_lt_u32 s58, 2\n"
    "s_cselect_b64 s[80:81], s[10:11], s[12:13]\n"
    "s_cselect_b64 s[82:83], s[16:17], s[14:15]\n"
    "s_add_u32 s32, s80, s60\n"
    "s_addc_u32 s33, s81, 0\n"
    "s_cmp_eq_u32 s64, 0\n"
    "s_cbranch_scc1 LNOR\n"
    "s_add_u32 m0, s72, 0x12000\n"
    "s_nop 0\n"
    "buffer_load_dwordx4 v202, s[32:35], 0 offen lds\n"
    "buffer_load_dwordx4 v203, s[32:35], 0 offen offset:1024 lds\n"
    "buffer_load_dwordx4 v204, s[32:35], 0 offen offset:2048 lds\n"
    "LNOR:\n"
    "s_add_u32 m0, s72, 0x15000\n"
    "s_nop 0\n"
    "buffer_load_dwordx4 v202, s[32:35], s64 offen lds\n"
    "buffer_load_dwordx4 v203, s[32:35], s64 offen offset:1024 lds\n"
    "buffer_load_dwordx4 v204, s[32:35], s64 offen offset:2048 lds\n"
    "LR0:\n"
    "s_load_dwordx2 s[24:25], %0, 0x40\n"
    "s_mul_i32 s71, s58, 0x1800\n"
    "s_lshr_b32 s87, s57, 2\n"
    "s_mul_i32 s87, s87, 0x6000\n"
    "s_mul_i32 s61, s56, 0x3000\n"
    "s_lshl_b32 s62, s56, 12\n"
    "s_lshl_b32 s59, s56, 12\n"
    "s_mov_b32 s50, 0x3000\n"
    "s_mov_b32 s51, 0x20000\n"
    "s_and_b32 s77, s57, 1\n"
    "s_mul_i32 s77, s77, 0xc0\n"
    "v_mul_u32_u24 v197, 0x1556, v144\n"
    "v_lshrrev_b32 v197, 16, v197\n"
    "v_mul_u32_u24 v196, 12, v197\n"
    "v_sub_u32 v196, v144, v196\n"
    "v_mul_u32_u24 v248, 0x180, v197\n"
    "v_lshl_add_u32 v248, v196, 4, v248\n"
    "v_add_u32 v248, s77, v248\n"
    "s_add_u32 s48, s82, s61\n"
    "s_addc_u32 s49, s83, 0\n"
    "s_add_u32 m0, s72, 0x21000\n"
    "s_nop 0\n"
    "buffer_load_dwordx4 v248, s[48:51], 0 offen lds\n"
    "s_add_u32 s40, s20, s62\n"
    "s_addc_u32 s41, s21, 0\n"
    "s_add_u32 s52, s18, s62\n"
    "s_addc_u32 s53, s19, 0\n"
    "s_mov_b32 s42, 0x1000\n"
    "s_mov_b32 s43, 0x20000\n"
    "s_mov_b32 s54, 0x1000\n"
    "s_mov_b32 s55, 0x20000\n"
    "v_lshlrev_b32 v196, 1, v231\n"
    "buffer_load_ushort v44, v196, s[40:43], 0 offen\n"
    "buffer_load_ushort v45, v196, s[52:55], 0 offen\n"
    "s_lshl_b32 s77, s58, 6\n"
    "v_add_u32 v41, s77, v231\n"
    "v_bfe_u32 v197, v231, 2, 1\n"
    "v_mul_u32_u24 v197, 12, v197\n"
    "v_and_b32 v198, 7, v231\n"
    "v_add3_u32 v197, v197, v198, s86\n"
    "v_lshrrev_b32 v40, 3, v41\n"
    "v_lshlrev_b32 v40, 7, v40\n"
    "v_lshl_add_u32 v40, v197, 1, v40\n"
    "buffer_load_ushort v42, v40, s[40:43], 0 offen\n"
    "buffer_load_ushort v43, v40, s[52:55], 0 offen\n"
    "s_add_u32 s44, s22, s59\n"
    "s_addc_u32 s45, s23, 0\n"
    "s_mov_b32 s46, 0x900\n"
    "s_mov_b32 s47, 0x20000\n"
    "v_mov_b32 v196, 0x40000000\n"
    "s_cmp_eq_u32 s58, 0\n"
    "s_cselect_b32 s77, 64, 0\n"
    "s_cselect_b32 s74, 1, 0\n"
    "v_and_b32 v213, 7, v231\n"
    "v_mul_u32_u24 v213, 0x100, v213\n"
    "v_cmp_gt_u32 vcc, s77, v231\n"
    "v_mov_b32 v197, 0x800\n"
    "s_mul_i32 s77, s57, 0x100\n"
    "v_cndmask_b32 v213, v196, v213, vcc\n"
    "v_cndmask_b32 v197, v196, v197, vcc\n"
    "buffer_load_dword v221, v197, s[44:47], 0 offen sc1\n"
    "v_cmp_gt_u32 vcc, s74, v231\n"
    "v_mov_b32 v214, s77\n"
    "s_mul_i32 s66, s74, 3\n"
    "v_cndmask_b32 v214, v196, v214, vcc\n"
    "s_lshl_b32 s76, s56, 3\n"
    "s_add_u32 s76, s76, s57\n"
    "s_mul_i32 s76, s76, 0x18000\n"
    "s_add_u32 s28, s8, s76\n"
    "s_addc_u32 s29, s9, 0\n"
    "s_mov_b32 s30, 0x18000\n"
    "s_mov_b32 s31, 0x20000\n"
    "s_mul_i32 s88, s58, 0x6000\n"
    "v_mul_u32_u24 v197, 0xaab, v231\n"
    "v_lshrrev_b32 v197, 16, v197\n"
    "v_mul_u32_u24 v196, 24, v197\n"
    "v_sub_u32 v196, v231, v196\n"
    "v_mul_u32_u24 v152, 0x180, v197\n"
    "v_lshrrev_b32 v197, 1, v197\n"
    "v_xor_b32 v196, v196, v197\n"
    "v_lshl_add_u32 v152, v196, 4, v152\n"
    "v_add_u32 v198, 64, v231\n"
    "v_mul_u32_u24 v197, 0xaab, v198\n"
    "v_lshrrev_b32 v197, 16, v197\n"
    "v_mul_u32_u24 v196, 24, v197\n"
    "v_sub_u32 v196, v198, v196\n"
    "v_mul_u32_u24 v153, 0x180, v197\n"
    "v_lshrrev_b32 v197, 1, v197\n"
    "v_xor_b32 v196, v196, v197\n"
    "v_lshl_add_u32 v153, v196, 4, v153\n"
    "v_add_u32 v198, 0x80, v231\n"
    "v_mul_u32_u24 v197, 0xaab, v198\n"
    "v_lshrrev_b32 v197, 16, v197\n"
    "v_mul_u32_u24 v196, 24, v197\n"
    "v_sub_u32 v196, v198, v196\n"
    "v_mul_u32_u24 v154, 0x180, v197\n"
    "v_lshrrev_b32 v197, 1, v197\n"
    "v_xor_b32 v196, v196, v197\n"
    "v_lshl_add_u32 v154, v196, 4, v154\n"
    "s_mov_b32 s76, 0xc00\n"
    "v_xad_u32 v155, v152, 64, s76\n"
    "v_xad_u32 v156, v153, 64, s76\n"
    "v_xad_u32 v157, v154, 64, s76\n"
    "s_add_u32 s78, s88, 0\n"
    "s_add_u32 m0, s71, 0\n"
    "s_nop 0\n"
    "buffer_load_dwordx4 v152, s[28:31], s78 offen nt lds\n"
    "s_add_u32 s78, s88, 0\n"
    "s_add_u32 m0, s71, 0x400\n"
    "s_nop 0\n"
    "buffer_load_dwordx4 v153, s[28:31], s78 offen nt lds\n"
    "s_add_u32 s78, s64, 0xc000\n"
    "s_add_u32 m0, s72, 0x1e000\n"
    "s_nop 0\n"
    "buffer_load_dwordx4 v202, s[32:35], s78 offen lds\n"
    "buffer_load_dwordx4 v203, s[32:35], s78 offen offset:1024 lds\n"
    "buffer_load_dwordx4 v204, s[32:35], s78 offen offset:2048 lds\n"
    "v_mov_b32 v196, 0x40000000\n"
    "v_and_b32 v46, 15, v231\n"
    "v_lshrrev_b32 v47, 4, v231\n"
    "s_mov_b32 s75, 0x3dd53b95\n"
    "v_min_u32 v199, 2, v47\n"
    "v_lshlrev_b32 v197, 3, v199\n"
    "s_lshl_b32 s76, s58, 1\n"
    "v_add_u32 v197, s76, v197\n"
    "v_lshrrev_b32 v198, 1, v46\n"
    "v_xor_b32 v197, v197, v198\n"
    "v_mul_u32_u24 v205, 0x180, v46\n"
    "v_lshl_add_u32 v205, v197, 4, v205\n"
    "v_add_u32 v205, 0x12000, v205\n"
    "v_xor_b32 v206, 16, v205\n"
    "v_and_b32 v197, 7, v231\n"
    "v_lshl_add_u32 v209, v199, 5, v197\n"
    "s_lshl_b32 s76, s58, 3\n"
    "v_add_lshl_u32 v209, v209, s76, 1\n"
    "v_add_u32 v217, 0x21000, v209\n"
    "s_lshr_b32 s76, s57, 2\n"
    "s_mul_i32 s76, s76, 0xc0\n"
    "s_add_u32 s76, s76, 0x20f40\n"
    "v_add_u32 v209, s76, v209\n"
    "v_lshrrev_b32 v198, 3, v231\n"
    "s_mul_i32 s76, s58, 12\n"
    "v_lshl_add_u32 v199, v198, 1, s76\n"
    "v_mul_u32_u24 v207, 5, v197\n"
    "v_and_b32 v207, 9, v207\n"
    "v_xor_b32 v199, v199, v207\n"
    "v_mul_u32_u24 v207, 0x300, v197\n"
    "v_lshl_add_u32 v207, v199, 4, v207\n"
    "v_add_u32 v207, 0x13800, v207\n"
    "v_xor_b32 v208, 16, v207\n"
    "s_mul_i32 s76, s58, 0xc0\n"
    "s_add_u32 s76, s76, 0x13800\n"
    "v_lshl_add_u32 v207, v231, 2, s76\n"
    "v_lshlrev_b32 v210, 3, v197\n"
    "v_add_u32 v210, 0x24400, v210\n"
    "s_mul_i32 s76, s58, 0x480\n"
    "s_add_u32 s76, s76, 0x24400\n"
    "v_mul_u32_u24 v215, 0x90, v197\n"
    "v_lshl_add_u32 v215, v198, 4, v215\n"
    "v_add_u32 v215, s76, v215\n"
    "v_and_b32 v197, 1, v197\n"
    "v_lshlrev_b32 v197, 3, v197\n"
    "v_sub_u32 v215, v215, v197\n"
    "v_bfe_u32 v197, v231, 2, 2\n"
    "v_mul_u32_u24 v216, 0x90, v197\n"
    "v_lshl_add_u32 v216, v47, 5, v216\n"
    "v_and_b32 v197, 1, v197\n"
    "v_lshlrev_b32 v197, 3, v197\n"
    "v_sub_u32 v216, v216, v197\n"
    "v_and_b32 v197, 3, v231\n"
    "v_lshl_add_u32 v216, v197, 3, v216\n"
    "v_add_u32 v216, s76, v216\n"
    "s_lshr_b32 s76, s57, 1\n"
    "s_xor_b32 s76, s76, 2\n"
    "s_mul_i32 s76, s76, 0x1800\n"
    "s_and_b32 s77, s57, 1\n"
    "s_mul_i32 s77, s77, 0xc00\n"
    "s_add_u32 s76, s76, s77\n"
    "s_lshl_b32 s77, s58, 8\n"
    "s_add_u32 s76, s76, s77\n"
    "v_lshlrev_b32 v197, 4, v46\n"
    "v_lshl_add_u32 v211, v47, 10, v197\n"
    "v_add_u32 v211, s76, v211\n"
    "s_lshr_b32 s76, s57, 2\n"
    "s_lshl_b32 s76, s76, 10\n"
    "s_lshl_b32 s77, s79, 8\n"
    "s_add_u32 s76, s76, s77\n"
    "s_mul_i32 s77, s58, 48\n"
    "v_add_u32 v198, s77, v231\n"
    "v_lshrrev_b32 v197, 5, v198\n"
    "v_lshl_add_u32 v212, v197, 12, s76\n"
    "v_bfe_u32 v197, v198, 2, 1\n"
    "v_lshl_add_u32 v212, v197, 11, v212\n"
    "v_bfe_u32 v197, v198, 3, 2\n"
    "v_lshl_add_u32 v212, v197, 6, v212\n"
    "v_cmp_gt_u32 vcc, 48, v231\n"
    "v_and_b32 v197, 3, v198\n"
    "v_lshl_add_u32 v212, v197, 4, v212\n"
    "v_cndmask_b32 v211, v196, v211, vcc\n"
    "v_cndmask_b32 v212, v196, v212, vcc\n"
    "v_mov_b32 v222, 0\n"
    "v_mov_b32 v223, 0\n"
    "v_mov_b32 v224, 0\n"
    "v_mov_b32 v225, 0\n"
    "v_mov_b32 v160, 0\n"
    "v_mov_b32 v161, 0\n"
    "v_mov_b32 v162, 0\n"
    "v_mov_b32 v163, 0\n"
    "v_mov_b32 v164, 0\n"
    "v_mov_b32 v165, 0\n"
    "v_mov_b32 v172, 0\n"
    "v_mov_b32 v173, 0\n"
    "v_mov_b32 v174, 0\n"
    "v_mov_b32 v175, 0\n"
    "v_lshlrev_b32 v41, 3, v41\n"
    "v_add_u32 v41, 0x24400, v41\n"
    "s_mul_i32 s76, s56, 0xc0000\n"
    "s_add_u32 s77, s76, 0x40000\n"
    "s_add_u32 s36, s22, s77\n"
    "s_addc_u32 s37, s23, 0\n"
    "s_mov_b32 s38, 0xc0000\n"
    "s_mov_b32 s39, 0x20000\n"
    "s_cmp_eq_u32 s87, 0\n"
    "s_cselect_b32 s62, 0x40000000, 0\n"
    "v_lshlrev_b32 v200, 4, v231\n"
    "v_add_u32 v201, 0xc000, v200\n"
    "v_mul_u32_u24 v198, 0x180, v46\n"
    "v_add_u32 v198, s71, v198\n"
    "v_lshrrev_b32 v197, 1, v46\n"
    "v_xor_b32 v197, v197, v47\n"
    "v_lshl_add_u32 v158, v197, 4, v198\n"
    "v_xor_b32 v159, 64, v158\n"
    "v_and_b32 v197, 3, v231\n"
    "v_cmp_eq_u32 vcc, 0, v197\n"
    "v_mov_b32 v198, 0x3f803f80\n"
    "s_nop 0\n"
    "v_cndmask_b32 v192, 0, v198, vcc\n"
    "v_cndmask_b32 v193, 0, v198, vcc\n"
    "v_cndmask_b32 v194, 0, v198, vcc\n"
    "v_cndmask_b32 v195, 0, v198, vcc\n"
    "s_and_b32 s77, s57, 1\n"
    "s_mul_i32 s77, s77, 0xc0\n"
    "s_sub_u32 s77, s77, 0x400\n"
    "v_mul_u32_u24 v197, 0x1556, v145\n"
    "v_lshrrev_b32 v197, 16, v197\n"
    "v_mul_u32_u24 v196, 12, v197\n"
    "v_sub_u32 v196, v145, v196\n"
    "v_mul_u32_u24 v249, 0x180, v197\n"
    "v_lshl_add_u32 v249, v196, 4, v249\n"
    "v_add_u32 v249, s77, v249\n"
    "s_and_b32 s77, s57, 1\n"
    "s_mul_i32 s77, s77, 0xc0\n"
    "s_sub_u32 s77, s77, 0x800\n"
    "v_mul_u32_u24 v197, 0x1556, v146\n"
    "v_lshrrev_b32 v197, 16, v197\n"
    "v_mul_u32_u24 v196, 12, v197\n"
    "v_sub_u32 v196, v146, v196\n"
    "v_mul_u32_u24 v250, 0x180, v197\n"
    "v_lshl_add_u32 v250, v196, 4, v250\n"
    "v_add_u32 v250, s77, v250\n"
    "s_waitcnt vmcnt(9)\n"
    "v_lshlrev_b32 v44, 16, v44\n"
    "s_waitcnt vmcnt(8)\n"
    "v_lshlrev_b32 v45, 16, v45\n"
    "v_mul_f32 v197, v44, v45\n"
    "s_nop 1\n"
    "v_min_f32_dpp v197, v197, v197 row_ror:8 row_mask:0xf bank_mask:0xf\n"
    "s_mul_i32 s76, s56, 0xc0000\n"
    "s_add_u32 s77, s76, 0x1840000\n"
    "v_min_f32_dpp v197, v197, v197 row_ror:4 row_mask:0xf bank_mask:0xf\n"
    "s_add_u32 s40, s22, s77\n"
    "s_addc_u32 s41, s23, 0\n"
    "v_min_f32_dpp v197, v197, v197 row_ror:2 row_mask:0xf bank_mask:0xf\n"
    "s_mov_b32 s42, 0xc0000\n"
    "s_mov_b32 s43, 0x20000\n"
    "v_min_f32_dpp v197, v197, v197 row_ror:1 row_mask:0xf bank_mask:0xf\n"
    "v_mov_b32 v198, v197\n"
    "s_nop 1\n"
    "v_permlane32_swap_b32 v197, v198\n"
    "v_min_f32 v197, v197, v198\n"
    "v_mov_b32 v198, v197\n"
    "s_nop 1\n"
    "v_permlane16_swap_b32 v197, v198\n"
    "v_min_f32 v197, v197, v198\n"
    "v_rcp_f32 v198, v197\n"
    "v_subrev_u32 v199, 0x37800000, v197\n"
    "v_mul_f32 v197, 0x3fcccccd, v197\n"
    "v_cmp_gt_u32 vcc, 0x10000000, v199\n"
    "v_mul_f32 v198, 0x3f3425ed, v198\n"
    "v_mov_b32 v199, 0x3f800000\n"
    "v_cndmask_b32 v197, 0, v197, vcc\n"
    "v_cndmask_b32 v198, v199, v198, vcc\n"
    "v_readfirstlane_b32 s70, v197\n"
    "v_mul_f32 v197, v197, v198\n"
    "v_readfirstlane_b32 s68, v198\n"
    "v_readfirstlane_b32 s69, v197\n"
    "s_waitcnt vmcnt(7)\n"
    "v_lshlrev_b32 v42, 16, v42\n"
    "s_waitcnt vmcnt(6)\n"
    "v_lshlrev_b32 v43, 16, v43\n"
    "v_mul_f32 v44, s68, v43\n"
    "v_fma_f32 v45, -v42, v44, s69\n"
    "ds_write_b64 v41, v[44:45]\n"
    "s_add_u32 s78, s88, 0\n"
    "s_add_u32 m0, s71, 0x800\n"
    "s_nop 0\n"
    "buffer_load_dwordx4 v154, s[28:31], s78 offen nt lds\n"
    "s_add_u32 s78, s88, 0\n"
    "s_add_u32 m0, s71, 0xc00\n"
    "s_nop 0\n"
    "buffer_load_dwordx4 v155, s[28:31], s78 offen nt lds\n"
    "s_waitcnt lgkmcnt(0)\n"
    "s_barrier\n"
    "ds_read_u16_d16_hi v172, v217 offset:6144\n"
    "ds_read_u16_d16_hi v173, v217\n"
    "ds_read_u16_d16_hi v174, v217 offset:6336\n"
    "ds_read_u16_d16_hi v175, v217 offset:192\n"
    "ds_read_b64 v[228:229], v210\n"
    "ds_read2st64_b32 v[240:241], v207 offset0:48 offset1:51\n"
    "ds_read2st64_b32 v[242:243], v207 offset0:54 offset1:57\n"
    "ds_read2st64_b32 v[244:245], v207 offset0:60 offset1:63\n"
    "ds_read2st64_b32 v[246:247], v207 offset0:66 offset1:69\n"
    "ds_read_u16_d16_hi v223, v209 offset:6144\n"
    "ds_read_u16_d16_hi v222, v209\n"
    "ds_read_b128 v[232:235], v205\n"
    "ds_read_b128 v[236:239], v206\n"
    "ds_read_u16_d16_hi v161, v209 offset:6336\n"
    "ds_read_u16_d16_hi v160, v209 offset:192\n"
    "s_waitcnt lgkmcnt(11)\n"
    "ds_read_b128 v[64:67], v205 offset:12288\n"
    "ds_read_b128 v[68:71], v206 offset:12288\n"
    "v_mul_f32 v197, v172, v173\n"
    "v_mul_f32 v198, v174, v175\n"
    "v_min_f32 v197, v197, v198\n"
    "v_max_f32 v197, 0, v197\n"
    "v_mul_f32 v217, 0x3e3fe8d2, v197\n"
    "s_waitcnt lgkmcnt(7)\n"
    "v_mul_f32 v226, s75, v223\n"
    "s_waitcnt lgkmcnt(6)\n"
    "v_fma_f32 v227, -v222, v226, v217\n"
    "s_waitcnt lgkmcnt(5)\n"
    "v_cvt_f32_i32 v232, v232\n"
    "v_cvt_f32_i32 v233, v233\n"
    "v_cvt_f32_i32 v234, v234\n"
    "v_cvt_f32_i32 v235, v235\n"
    "s_waitcnt lgkmcnt(4)\n"
    "v_cvt_f32_i32 v236, v236\n"
    "v_cvt_f32_i32 v237, v237\n"
    "v_cvt_f32_i32 v238, v238\n"
    "v_cvt_f32_i32 v239, v239\n"
    "v_mul_f32_dpp v232, v226, v232 row_newbcast:0 row_mask:0xf bank_mask:0xf\n"
    "v_mul_f32_dpp v233, v226, v233 row_newbcast:1 row_mask:0xf bank_mask:0xf\n"
    "v_mul_f32_dpp v234, v226, v234 row_newbcast:2 row_mask:0xf bank_mask:0xf\n"
    "v_mul_f32_dpp v235, v226, v235 row_newbcast:3 row_mask:0xf bank_mask:0xf\n"
    "v_mul_f32_dpp v236, v226, v236 row_newbcast:4 row_mask:0xf bank_mask:0xf\n"
    "v_mul_f32_dpp v237, v226, v237 row_newbcast:5 row_mask:0xf bank_mask:0xf\n"
    "v_mul_f32_dpp v238, v226, v238 row_newbcast:6 row_mask:0xf bank_mask:0xf\n"
    "v_mul_f32_dpp v239, v226, v239 row_newbcast:7 row_mask:0xf bank_mask:0xf\n"
    "v_add_f32_dpp v232, v227, v232 row_newbcast:0 row_mask:0xf bank_mask:0xf\n"
    "v_add_f32_dpp v233, v227, v233 row_newbcast:1 row_mask:0xf bank_mask:0xf\n"
    "v_add_f32_dpp v234, v227, v234 row_newbcast:2 row_mask:0xf bank_mask:0xf\n"
    "v_add_f32_dpp v235, v227, v235 row_newbcast:3 row_mask:0xf bank_mask:0xf\n"
    "v_add_f32_dpp v236, v227, v236 row_newbcast:4 row_mask:0xf bank_mask:0xf\n"
    "v_add_f32_dpp v237, v227, v237 row_newbcast:5 row_mask:0xf bank_mask:0xf\n"
    "v_add_f32_dpp v238, v227, v238 row_newbcast:6 row_mask:0xf bank_mask:0xf\n"
    "v_add_f32_dpp v239, v227, v239 row_newbcast:7 row_mask:0xf bank_mask:0xf\n"
    "v_cvt_pk_bf16_f32 v232, v232, v233\n"
    "v_cvt_pk_bf16_f32 v233, v234, v235\n"
    "v_cvt_pk_bf16_f32 v234, v236, v237\n"
    "v_cvt_pk_bf16_f32 v235, v238, v239\n"
    "buffer_store_dwordx4 v[232:235], v211, s[36:39], s62 offen sc1\n"
    "v_cvt_f32_i32 v240, v240\n"
    "v_cvt_f32_i32 v241, v241\n"
    "v_cvt_f32_i32 v242, v242\n"
    "v_cvt_f32_i32 v243, v243\n"
    "v_cvt_f32_i32 v244, v244\n"
    "v_cvt_f32_i32 v245, v245\n"
    "v_cvt_f32_i32 v246, v246\n"
    "v_cvt_f32_i32 v247, v247\n"
    "v_mul_f32_dpp v240, v228, v240 row_newbcast:0 row_mask:0xf bank_mask:0xf\n"
    "v_mul_f32_dpp v241, v228, v241 row_newbcast:1 row_mask:0xf bank_mask:0xf\n"
    "v_mul_f32_dpp v242, v228, v242 row_newbcast:2 row_mask:0xf bank_mask:0xf\n"
    "v_mul_f32_dpp v243, v228, v243 row_newbcast:3 row_mask:0xf bank_mask:0xf\n"
    "v_mul_f32_dpp v244, v228, v244 row_newbcast:4 row_mask:0xf bank_mask:0xf\n"
    "v_mul_f32_dpp v245, v228, v245 row_newbcast:5 row_mask:0xf bank_mask:0xf\n"
    "v_mul_f32_dpp v246, v228, v246 row_newbcast:6 row_mask:0xf bank_mask:0xf\n"
    "v_mul_f32_dpp v247, v228, v247 row_newbcast:7 row_mask:0xf bank_mask:0xf\n"
    "v_add_f32_dpp v240, v229, v240 row_newbcast:0 row_mask:0xf bank_mask:0xf\n"
    "v_add_f32_dpp v241, v229, v241 row_newbcast:1 row_mask:0xf bank_mask:0xf\n"
    "v_add_f32_dpp v242, v229, v242 row_newbcast:2 row_mask:0xf bank_mask:0xf\n"
    "v_add_f32_dpp v243, v229, v243 row_newbcast:3 row_mask:0xf bank_mask:0xf\n"
    "v_add_f32_dpp v244, v229, v244 row_newbcast:4 row_mask:0xf bank_mask:0xf\n"
    "v_add_f32_dpp v245, v229, v245 row_newbcast:5 row_mask:0xf bank_mask:0xf\n"
    "v_add_f32_dpp v246, v229, v246 row_newbcast:6 row_mask:0xf bank_mask:0xf\n"
    "v_add_f32_dpp v247, v229, v247 row_newbcast:7 row_mask:0xf bank_mask:0xf\n"
    "v_cvt_pk_bf16_f32 v240, v240, v241\n"
    "v_cvt_pk_bf16_f32 v241, v242, v243\n"
    "v_cvt_pk_bf16_f32 v242, v244, v245\n"
    "v_cvt_pk_bf16_f32 v243, v246, v247\n"
    "s_mov_b32 s78, 0\n"
    "buffer_store_dwordx4 v[240:243], v212, s[40:43], s78 offen sc1\n"
    "s_waitcnt lgkmcnt(3)\n"
    "v_mul_f32 v166, s75, v161\n"
    "s_waitcnt lgkmcnt(2)\n"
    "v_fma_f32 v167, -v160, v166, v217\n"
    "s_waitcnt lgkmcnt(1)\n"
    "v_cvt_f32_i32 v64, v64\n"
    "v_cvt_f32_i32 v65, v65\n"
    "v_cvt_f32_i32 v66, v66\n"
    "v_cvt_f32_i32 v67, v67\n"
    "s_waitcnt lgkmcnt(0)\n"
    "v_cvt_f32_i32 v68, v68\n"
    "v_cvt_f32_i32 v69, v69\n"
    "v_cvt_f32_i32 v70, v70\n"
    "v_cvt_f32_i32 v71, v71\n"
    "v_mul_f32_dpp v64, v166, v64 row_newbcast:0 row_mask:0xf bank_mask:0xf\n"
    "v_mul_f32_dpp v65, v166, v65 row_newbcast:1 row_mask:0xf bank_mask:0xf\n"
    "v_mul_f32_dpp v66, v166, v66 row_newbcast:2 row_mask:0xf bank_mask:0xf\n"
    "v_mul_f32_dpp v67, v166, v67 row_newbcast:3 row_mask:0xf bank_mask:0xf\n"
    "v_mul_f32_dpp v68, v166, v68 row_newbcast:4 row_mask:0xf bank_mask:0xf\n"
    "v_mul_f32_dpp v69, v166, v69 row_newbcast:5 row_mask:0xf bank_mask:0xf\n"
    "v_mul_f32_dpp v70, v166, v70 row_newbcast:6 row_mask:0xf bank_mask:0xf\n"
    "v_mul_f32_dpp v71, v166, v71 row_newbcast:7 row_mask:0xf bank_mask:0xf\n"
    "v_add_f32_dpp v64, v167, v64 row_newbcast:0 row_mask:0xf bank_mask:0xf\n"
    "v_add_f32_dpp v65, v167, v65 row_newbcast:1 row_mask:0xf bank_mask:0xf\n"
    "v_add_f32_dpp v66, v167, v66 row_newbcast:2 row_mask:0xf bank_mask:0xf\n"
    "v_add_f32_dpp v67, v167, v67 row_newbcast:3 row_mask:0xf bank_mask:0xf\n"
    "v_add_f32_dpp v68, v167, v68 row_newbcast:4 row_mask:0xf bank_mask:0xf\n"
    "v_add_f32_dpp v69, v167, v69 row_newbcast:5 row_mask:0xf bank_mask:0xf\n"
    "v_add_f32_dpp v70, v167, v70 row_newbcast:6 row_mask:0xf bank_mask:0xf\n"
    "v_add_f32_dpp v71, v167, v71 row_newbcast:7 row_mask:0xf bank_mask:0xf\n"
    "v_cvt_pk_bf16_f32 v64, v64, v65\n"
    "v_cvt_pk_bf16_f32 v65, v66, v67\n"
    "v_cvt_pk_bf16_f32 v66, v68, v69\n"
    "v_cvt_pk_bf16_f32 v67, v70, v71\n"
    "s_add_u32 s78, s87, 0\n"
    "buffer_store_dwordx4 v[64:67], v211, s[36:39], s78 offen sc1\n"
    "s_waitcnt vmcnt(10)\n"
    "v_add_u32 v221, s74, v221\n"
    "s_mov_b32 s73, 0\n"
    "v_readfirstlane_b32 s67, v221\n"
    "s_mul_i32 s66, s74, 2\n"
    "s_waitcnt vmcnt(0)\n"
    "s_barrier\n"
    "v_mov_b32 v220, 2\n"
    "buffer_store_dwordx2 v[220:221], v214, s[44:47], 0 offen sc1\n"
    "s_add_u32 s78, s64, 0x18000\n"
    "s_add_u32 m0, s72, 0x1b000\n"
    "s_nop 0\n"
    "buffer_load_dwordx4 v202, s[32:35], s78 offen lds\n"
    "buffer_load_dwordx4 v203, s[32:35], s78 offen offset:1024 lds\n"
    "buffer_load_dwordx4 v204, s[32:35], s78 offen offset:2048 lds\n"
    "s_add_u32 s78, s88, 0\n"
    "s_add_u32 m0, s71, 0x1000\n"
    "s_nop 0\n"
    "buffer_load_dwordx4 v156, s[28:31], s78 offen nt lds\n"
    "s_add_u32 s78, s88, 0\n"
    "s_add_u32 m0, s71, 0x1400\n"
    "s_nop 0\n"
    "buffer_load_dwordx4 v157, s[28:31], s78 offen nt lds\n"
    "s_add_u32 s78, s88, 0x1800\n"
    "s_add_u32 m0, s71, 0x6000\n"
    "s_nop 0\n"
    "buffer_load_dwordx4 v152, s[28:31], s78 offen nt lds\n"
    "s_add_u32 s78, s88, 0x1800\n"
    "s_add_u32 m0, s71, 0x6400\n"
    "s_nop 0\n"
    "buffer_load_dwordx4 v153, s[28:31], s78 offen nt lds\n"
    "s_barrier\n"
    "ds_read_u16_d16_hi v163, v209 offset:6528\n"
    "ds_read_u16_d16_hi v162, v209 offset:384\n"
    "ds_read_b128 v[72:75], v205 offset:49152\n"
    "ds_read_b128 v[76:79], v206 offset:49152\n"
    "ds_read_b64 v[170:171], v210 offset:64\n"
    "ds_read2st64_b32 v[80:81], v207 offset0:192 offset1:195\n"
    "ds_read2st64_b32 v[82:83], v207 offset0:198 offset1:201\n"
    "ds_read2st64_b32 v[84:85], v207 offset0:204 offset1:207\n"
    "ds_read2st64_b32 v[86:87], v207 offset0:210 offset1:213\n"
    "s_waitcnt lgkmcnt(8)\n"
    "v_mul_f32 v168, s75, v163\n"
    "s_waitcnt lgkmcnt(7)\n"
    "v_fma_f32 v169, -v162, v168, v217\n"
    "s_waitcnt lgkmcnt(6)\n"
    "v_cvt_f32_i32 v72, v72\n"
    "v_cvt_f32_i32 v73, v73\n"
    "v_cvt_f32_i32 v74, v74\n"
    "v_cvt_f32_i32 v75, v75\n"
    "s_waitcnt lgkmcnt(5)\n"
    "v_cvt_f32_i32 v76, v76\n"
    "v_cvt_f32_i32 v77, v77\n"
    "v_cvt_f32_i32 v78, v78\n"
    "v_cvt_f32_i32 v79, v79\n"
    "v_mul_f32_dpp v72, v168, v72 row_newbcast:0 row_mask:0xf bank_mask:0xf\n"
    "v_mul_f32_dpp v73, v168, v73 row_newbcast:1 row_mask:0xf bank_mask:0xf\n"
    "v_mul_f32_dpp v74, v168, v74 row_newbcast:2 row_mask:0xf bank_mask:0xf\n"
    "v_mul_f32_dpp v75, v168, v75 row_newbcast:3 row_mask:0xf bank_mask:0xf\n"
    "v_mul_f32_dpp v76, v168, v76 row_newbcast:4 row_mask:0xf bank_mask:0xf\n"
    "v_mul_f32_dpp v77, v168, v77 row_newbcast:5 row_mask:0xf bank_mask:0xf\n"
    "v_mul_f32_dpp v78, v168, v78 row_newbcast:6 row_mask:0xf bank_mask:0xf\n"
    "v_mul_f32_dpp v79, v168, v79 row_newbcast:7 row_mask:0xf bank_mask:0xf\n"
    "v_add_f32_dpp v72, v169, v72 row_newbcast:0 row_mask:0xf bank_mask:0xf\n"
    "v_add_f32_dpp v73, v169, v73 row_newbcast:1 row_mask:0xf bank_mask:0xf\n"
    "v_add_f32_dpp v74, v169, v74 row_newbcast:2 row_mask:0xf bank_mask:0xf\n"
    "v_add_f32_dpp v75, v169, v75 row_newbcast:3 row_mask:0xf bank_mask:0xf\n"
    "v_add_f32_dpp v76, v169, v76 row_newbcast:4 row_mask:0xf bank_mask:0xf\n"
    "v_add_f32_dpp v77, v169, v77 row_newbcast:5 row_mask:0xf bank_mask:0xf\n"
    "v_add_f32_dpp v78, v169, v78 row_newbcast:6 row_mask:0xf bank_mask:0xf\n"
    "v_add_f32_dpp v79, v169, v79 row_newbcast:7 row_mask:0xf bank_mask:0xf\n"
    "v_cvt_pk_bf16_f32 v72, v72, v73\n"
    "v_cvt_pk_bf16_f32 v73, v74, v75\n"
    "v_cvt_pk_bf16_f32 v74, v76, v77\n"
    "v_cvt_pk_bf16_f32 v75, v78, v79\n"
    "s_add_u32 s78, s87, 0x6000\n"
    "buffer_store_dwordx4 v[72:75], v211, s[36:39], s78 offen sc1\n"
    "s_waitcnt lgkmcnt(3)\n"
    "v_cvt_f32_i32 v80, v80\n"
    "v_cvt_f32_i32 v81, v81\n"
    "s_waitcnt lgkmcnt(2)\n"
    "v_cvt_f32_i32 v82, v82\n"
    "v_cvt_f32_i32 v83, v83\n"
    "s_waitcnt lgkmcnt(1)\n"
    "v_cvt_f32_i32 v84, v84\n"
    "v_cvt_f32_i32 v85, v85\n"
    "s_waitcnt lgkmcnt(0)\n"
    "v_cvt_f32_i32 v86, v86\n"
    "v_cvt_f32_i32 v87, v87\n"
    "v_mul_f32_dpp v80, v170, v80 row_newbcast:0 row_mask:0xf bank_mask:0xf\n"
    "v_mul_f32_dpp v81, v170, v81 row_newbcast:1 row_mask:0xf bank_mask:0xf\n"
    "v_mul_f32_dpp v82, v170, v82 row_newbcast:2 row_mask:0xf bank_mask:0xf\n"
    "v_mul_f32_dpp v83, v170, v83 row_newbcast:3 row_mask:0xf bank_mask:0xf\n"
    "v_mul_f32_dpp v84, v170, v84 row_newbcast:4 row_mask:0xf bank_mask:0xf\n"
    "v_mul_f32_dpp v85, v170, v85 row_newbcast:5 row_mask:0xf bank_mask:0xf\n"
    "v_mul_f32_dpp v86, v170, v86 row_newbcast:6 row_mask:0xf bank_mask:0xf\n"
    "v_mul_f32_dpp v87, v170, v87 row_newbcast:7 row_mask:0xf bank_mask:0xf\n"
    "v_add_f32_dpp v80, v171, v80 row_newbcast:0 row_mask:0xf bank_mask:0xf\n"
    "v_add_f32_dpp v81, v171, v81 row_newbcast:1 row_mask:0xf bank_mask:0xf\n"
    "v_add_f32_dpp v82, v171, v82 row_newbcast:2 row_mask:0xf bank_mask:0xf\n"
    "v_add_f32_dpp v83, v171, v83 row_newbcast:3 row_mask:0xf bank_mask:0xf\n"
    "v_add_f32_dpp v84, v171, v84 row_newbcast:4 row_mask:0xf bank_mask:0xf\n"
    "v_add_f32_dpp v85, v171, v85 row_newbcast:5 row_mask:0xf bank_mask:0xf\n"
    "v_add_f32_dpp v86, v171, v86 row_newbcast:6 row_mask:0xf bank_mask:0xf\n"
    "v_add_f32_dpp v87, v171, v87 row_newbcast:7 row_mask:0xf bank_mask:0xf\n"
    "v_cvt_pk_bf16_f32 v80, v80, v81\n"
    "v_cvt_pk_bf16_f32 v81, v82, v83\n"
    "v_cvt_pk_bf16_f32 v82, v84, v85\n"
    "v_cvt_pk_bf16_f32 v83, v86, v87\n"
    "s_mov_b32 s78, 0x6000\n"
    "buffer_store_dwordx4 v[80:83], v212, s[40:43], s78 offen sc1\n"
    "buffer_load_dwordx2 v[218:219], v213, s[44:47], 0 offen sc1\n"
    "s_add_u32 s78, s88, 0x1800\n"
    "s_add_u32 m0, s71, 0x6800\n"
    "s_nop 0\n"
    "buffer_load_dwordx4 v154, s[28:31], s78 offen nt lds\n"
    "s_add_u32 s78, s88, 0x1800\n"
    "s_add_u32 m0, s71, 0x6c00\n"
    "s_nop 0\n"
    "buffer_load_dwordx4 v155, s[28:31], s78 offen nt lds\n"
    "s_add_u32 s78, s88, 0x1800\n"
    "s_add_u32 m0, s71, 0x7000\n"
    "s_nop 0\n"
    "buffer_load_dwordx4 v156, s[28:31], s78 offen nt lds\n"
    "s_add_u32 s78, s88, 0x1800\n"
    "s_add_u32 m0, s71, 0x7400\n"
    "s_nop 0\n"
    "buffer_load_dwordx4 v157, s[28:31], s78 offen nt lds\n"
    "s_waitcnt vmcnt(9)\n"
    "ds_read_b128 a[192:195], v158\n"
    "ds_read_b128 a[196:199], v159\n"
    "ds_read_b128 a[200:203], v158 offset:128\n"
    "ds_read_b128 a[204:207], v159 offset:128\n"
    "ds_read_b128 a[208:211], v158 offset:256\n"
    "ds_read_b128 a[212:215], v159 offset:256\n"
    "s_waitcnt vmcnt(4)\n"
    "v_cmp_ne_u32 s[80:81], s67, v219\n"
    "v_cmp_gt_u32 vcc, s66, v218\n"
    "s_or_b64 vcc, vcc, s[80:81]\n"
    "s_cbranch_vccnz LSPRE0\n"
    "LSPB0:\n"
    "s_waitcnt lgkmcnt(0)\n"
    "s_barrier\n"
    "v_mov_b32 v220, 3\n"
    "buffer_store_dwordx2 v[220:221], v214, s[44:47], 0 offen sc1\n"
    "s_add_u32 m0, s72, 0x1e000\n"
    "s_nop 0\n"
    "buffer_load_dwordx4 v200, s[36:39], s72 offen sc1 lds\n"
    "s_add_u32 m0, s72, 0x1e000\n"
    "s_nop 0\n"
    "buffer_load_dwordx4 v200, s[36:39], s72 offen offset:1024 sc1 lds\n"
    "s_add_u32 m0, s72, 0x1e000\n"
    "s_nop 0\n"
    "buffer_load_dwordx4 v200, s[36:39], s72 offen offset:2048 sc1 lds\n"
    "s_add_u32 s65, s71, 0x3000\n"
    "s_add_u32 m0, s71, 0\n"
    "s_nop 0\n"
    "buffer_load_dwordx4 v200, s[36:39], s65 offen sc1 lds\n"
    "s_add_u32 s65, s71, 0x3400\n"
    "s_add_u32 m0, s71, 0x400\n"
    "s_nop 0\n"
    "buffer_load_dwordx4 v200, s[36:39], s65 offen sc1 lds\n"
    "s_add_u32 s65, s71, 0x3800\n"
    "s_add_u32 m0, s71, 0x800\n"
    "s_nop 0\n"
    "buffer_load_dwordx4 v200, s[36:39], s65 offen sc1 lds\n"
    "s_add_u32 s65, s71, 0x3c00\n"
    "s_add_u32 m0, s71, 0xc00\n"
    "s_nop 0\n"
    "buffer_load_dwordx4 v200, s[36:39], s65 offen sc1 lds\n"
    "s_add_u32 s65, s71, 0x4000\n"
    "s_add_u32 m0, s71, 0x1000\n"
    "s_nop 0\n"
    "buffer_load_dwordx4 v200, s[36:39], s65 offen sc1 lds\n"
    "s_add_u32 s65, s71, 0x4400\n"
    "s_add_u32 m0, s71, 0x1400\n"
    "s_nop 0\n"
    "buffer_load_dwordx4 v200, s[36:39], s65 offen sc1 lds\n"
    "s_add_u32 s65, s71, 0\n"
    "s_add_u32 m0, s71, 0x12000\n"
    "s_nop 0\n"
    "buffer_load_dwordx4 v200, s[40:43], s65 offen sc1 lds\n"
    "s_add_u32 s65, s71, 0x400\n"
    "s_add_u32 m0, s71, 0x12400\n"
    "s_nop 0\n"
    "buffer_load_dwordx4 v200, s[40:43], s65 offen sc1 lds\n"
    "s_add_u32 s65, s71, 0x800\n"
    "s_add_u32 m0, s71, 0x12800\n"
    "s_nop 0\n"
    "buffer_load_dwordx4 v200, s[40:43], s65 offen sc1 lds\n"
    "s_add_u32 s65, s71, 0xc00\n"
    "s_add_u32 m0, s71, 0x12c00\n"
    "s_nop 0\n"
    "buffer_load_dwordx4 v200, s[40:43], s65 offen sc1 lds\n"
    "s_add_u32 s65, s71, 0x1000\n"
    "s_add_u32 m0, s71, 0x13000\n"
    "s_nop 0\n"
    "buffer_load_dwordx4 v200, s[40:43], s65 offen sc1 lds\n"
    "s_add_u32 s65, s71, 0x1400\n"
    "s_add_u32 m0, s71, 0x13400\n"
    "s_nop 0\n"
    "buffer_load_dwordx4 v200, s[40:43], s65 offen sc1 lds\n"
    "s_add_u32 s78, s64, 0x24000\n"
    "s_add_u32 m0, s72, 0x18000\n"
    "s_nop 0\n"
    "buffer_load_dwordx4 v202, s[32:35], s78 offen lds\n"
    "buffer_load_dwordx4 v203, s[32:35], s78 offen offset:1024 lds\n"
    "buffer_load_dwordx4 v204, s[32:35], s78 offen offset:2048 lds\n"
    "s_add_u32 s78, s88, 0x3000\n"
    "s_add_u32 m0, s71, 0xc000\n"
    "s_nop 0\n"
    "buffer_load_dwordx4 v152, s[28:31], s78 offen nt lds\n"
    "s_mul_i32 s66, s74, 3\n"
    "s_waitcnt vmcnt(20)\n"
    "ds_read_b128 a[216:219], v158 offset:24576\n"
    "ds_read_b128 a[220:223], v159 offset:24576\n"
    "ds_read_b128 a[224:227], v158 offset:24704\n"
    "ds_read_b128 a[228:231], v159 offset:24704\n"
    "ds_read_b128 a[232:235], v158 offset:24832\n"
    "ds_read_b128 a[236:239], v159 offset:24832\n"
    "s_add_u32 m0, s72, 0x21000\n"
    "s_nop 0\n"
    "buffer_load_dwordx4 v249, s[48:51], 0 offen offset:1024 lds\n"
    "buffer_load_dwordx2 v[218:219], v213, s[44:47], 0 offen sc1\n"
    "v_add_u32 v255, 0x1e000, v200\n"
    "s_add_u32 s60, s71, 0x9000\n"
    "s_add_u32 s61, s71, 0x6000\n"
    "s_add_u32 s62, s87, 0x12000\n"
    "s_mov_b32 s63, 0x12000\n"
    "s_mov_b32 s59, 0\n"
    "s_mul_i32 s66, s74, 3\n"
    "s_getpc_b64 s[52:53]\n"
    "LPC2:\n"
    "s_add_u32 s52, s52, LTAIL-LPC2\n"
    "s_addc_u32 s53, s53, 0\n"
    "s_mov_b32 s54, LSLOW-LTAIL\n"
    "s_lshr_b32 s91, %1, 3\n"
    "s_mul_i32 s91, s91, 4\n"
    "s_add_u32 s91, s91, s58\n"
    "s_sub_u32 s91, s91, 0\n"
    "s_lshl_b32 s91, s91, 9\n"
    "v_lshlrev_b32 v252, 3, v231\n"
    "s_mul_i32 s90, s58, 0x1c00\n"
    "s_sub_u32 s90, s90, 0x1000\n"
    "s_sub_u32 s92, s90, s71\n"
    "s_waitcnt vmcnt(12) lgkmcnt(0)\n"
    "s_barrier\n"
    "ds_read_b128 v[160:163], v255\n"
    "ds_read_b128 v[164:167], v255 offset:1024\n"
    "ds_read_b128 v[168:171], v255 offset:2048\n"
    "ds_read_b128 v[172:175], v255 offset:3072\n"
    "ds_read_b128 v[176:179], v255 offset:4096\n"
    "ds_read_b128 v[180:183], v255 offset:5120\n"
    "s_add_u32 s76, s87, 0xc000\n"
    "s_mov_b32 s77, 0xc000\n"
    "s_waitcnt lgkmcnt(4)\n"
    "v_mfma_f32_16x16x32_bf16 v[40:43], v[160:163], a[192:195], 0\n"
    "ds_read_b128 v[184:187], v255 offset:6144\n"
    "ds_read_b64 v[228:229], v210 offset:128\n"
    "v_mfma_f32_16x16x32_bf16 v[40:43], v[164:167], a[196:199], v[40:43]\n"
    "s_add_u32 s78, s88, 0x3000\n"
    "ds_read2st64_b32 v[240:241], v207 offset0:144 offset1:147\n"
    "ds_read2st64_b32 v[242:243], v207 offset0:150 offset1:153\n"
    "v_mfma_f32_16x16x32_bf16 v[44:47], v[160:163], a[216:219], 0\n"
    "s_add_u32 m0, s71, 0xc400\n"
    "ds_read_b128 v[188:191], v255 offset:7168\n"
    "ds_read2st64_b32 v[244:245], v207 offset0:156 offset1:159\n"
    "v_mfma_f32_16x16x32_bf16 v[44:47], v[164:167], a[220:223], v[44:47]\n"
    "s_waitcnt lgkmcnt(8)\n"
    "buffer_load_dwordx4 v153, s[28:31], s78 offen nt lds\n"
    "v_mfma_f32_16x16x32_bf16 v[40:43], v[168:171], a[200:203], v[40:43]\n"
    "ds_read_b128 v[160:163], v255 offset:8192\n"
    "ds_read2st64_b32 v[246:247], v207 offset0:162 offset1:165\n"
    "v_mfma_f32_16x16x32_bf16 v[40:43], v[172:175], a[204:207], v[40:43]\n"
    "ds_read_u16_d16_hi v223, v209 offset:6720\n"
    "ds_read_u16_d16_hi v222, v209 offset:576\n"
    "v_mfma_f32_16x16x32_bf16 v[44:47], v[168:171], a[224:227], v[44:47]\n"
    "ds_read_b128 v[164:167], v255 offset:9216\n"
    "ds_read_b128 v[232:235], v205 offset:36864\n"
    "v_mfma_f32_16x16x32_bf16 v[44:47], v[172:175], a[228:231], v[44:47]\n"
    "s_waitcnt lgkmcnt(12)\n"
    "ds_read_b128 v[236:239], v206 offset:36864\n"
    "v_mfma_f32_16x16x32_bf16 v[40:43], v[176:179], a[208:211], v[40:43]\n"
    "ds_read_b128 v[168:171], v255 offset:10240\n"
    "v_mfma_f32_16x16x32_bf16 v[40:43], v[180:183], a[212:215], v[40:43]\n"
    "v_mfma_f32_16x16x32_bf16 v[44:47], v[176:179], a[232:235], v[44:47]\n"
    "ds_read_b128 v[172:175], v255 offset:11264\n"
    "v_mfma_f32_16x16x32_bf16 v[44:47], v[180:183], a[236:239], v[44:47]\n"
    "s_waitcnt lgkmcnt(10)\n"
    "s_add_u32 s78, s88, 0x3000\n"
    "v_cvt_f32_i32 v240, v240\n"
    "v_mfma_f32_16x16x32_bf16 v[56:59], v[184:187], a[192:195], 0\n"
    "s_add_u32 m0, s71, 0xc800\n"
    "ds_read_b128 v[176:179], v200\n"
    "v_cvt_f32_i32 v241, v241\n"
    "v_mfma_f32_16x16x32_bf16 v[56:59], v[188:191], a[196:199], v[56:59]\n"
    "buffer_load_dwordx4 v154, s[28:31], s78 offen nt lds\n"
    "v_mfma_f32_16x16x32_bf16 v[60:63], v[184:187], a[216:219], 0\n"
    "ds_read_b128 v[180:183], v200 offset:1024\n"
    "v_cvt_f32_i32 v242, v242\n"
    "v_mfma_f32_16x16x32_bf16 v[60:63], v[188:191], a[220:223], v[60:63]\n"
    "s_waitcnt lgkmcnt(6)\n"
    "v_cvt_f32_i32 v243, v243\n"
    "v_cvt_f32_i32 v244, v244\n"
    "v_mfma_f32_16x16x32_bf16 v[56:59], v[160:163], a[200:203], v[56:59]\n"
    "ds_read_b128 v[184:187], v200 offset:2048\n"
    "v_cvt_f32_i32 v245, v245\n"
    "v_mfma_f32_16x16x32_bf16 v[56:59], v[164:167], a[204:207], v[56:59]\n"
    "v_cvt_f32_i32 v246, v246\n"
    "v_mul_f32 v226, s75, v223\n"
    "v_mfma_f32_16x16x32_bf16 v[60:63], v[160:163], a[224:227], v[60:63]\n"
    "ds_read_b128 v[188:191], v200 offset:3072\n"
    "v_cvt_f32_i32 v247, v247\n"
    "v_mfma_f32_16x16x32_bf16 v[60:63], v[164:167], a[228:231], v[60:63]\n"
    "s_waitcnt lgkmcnt(4)\n"
    "v_fma_f32 v227, -v222, v226, v217\n"
    "v_mul_f32_dpp v240, v228, v240 row_newbcast:0 row_mask:0xf bank_mask:0xf\n"
    "v_mfma_f32_16x16x32_bf16 v[56:59], v[168:171], a[208:211], v[56:59]\n"
    "ds_read_b128 v[160:163], v200 offset:4096\n"
    "v_cvt_f32_i32 v232, v232\n"
    "v_mfma_f32_16x16x32_bf16 v[56:59], v[172:175], a[212:215], v[56:59]\n"
    "s_add_u32 s78, s88, 0x3000\n"
    "v_mul_f32_dpp v241, v228, v241 row_newbcast:1 row_mask:0xf bank_mask:0xf\n"
    "v_cvt_f32_i32 v233, v233\n"
    "v_mfma_f32_16x16x32_bf16 v[60:63], v[168:171], a[232:235], v[60:63]\n"
    "s_add_u32 m0, s71, 0xcc00\n"
    "ds_read_b128 v[164:167], v200 offset:5120\n"
    "v_mul_f32_dpp v242, v228, v242 row_newbcast:2 row_mask:0xf bank_mask:0xf\n"
    "v_mfma_f32_16x16x32_bf16 v[60:63], v[172:175], a[236:239], v[60:63]\n"
    "s_waitcnt lgkmcnt(4)\n"
    "buffer_load_dwordx4 v155, s[28:31], s78 offen nt lds\n"
    "v_mfma_f32_16x16x32_bf16 v[64:67], v[176:179], a[192:195], 0\n"
    "ds_read_b128 v[168:171], v200 offset:6144\n"
    "v_cvt_f32_i32 v234, v234\n"
    "v_mul_f32_dpp v243, v228, v243 row_newbcast:3 row_mask:0xf bank_mask:0xf\n"
    "v_cvt_f32_i32 v235, v235\n"
    "v_mfma_f32_16x16x32_bf16 v[64:67], v[180:183], a[196:199], v[64:67]\n"
    "v_mul_f32_dpp v244, v228, v244 row_newbcast:4 row_mask:0xf bank_mask:0xf\n"
    "v_cvt_f32_i32 v236, v236\n"
    "v_mul_f32_dpp v245, v228, v245 row_newbcast:5 row_mask:0xf bank_mask:0xf\n"
    "v_cvt_f32_i32 v237, v237\n"
    "v_mfma_f32_16x16x32_bf16 v[68:71], v[176:179], a[216:219], 0\n"
    "ds_read_b128 v[172:175], v200 offset:7168\n"
    "v_mul_f32_dpp v246, v228, v246 row_newbcast:6 row_mask:0xf bank_mask:0xf\n"
    "v_cvt_f32_i32 v238, v238\n"
    "v_mul_f32_dpp v247, v228, v247 row_newbcast:7 row_mask:0xf bank_mask:0xf\n"
    "v_mfma_f32_16x16x32_bf16 v[68:71], v[180:183], a[220:223], v[68:71]\n"
    "s_waitcnt lgkmcnt(4)\n"
    "v_cvt_f32_i32 v239, v239\n"
    "v_add_f32_dpp v240, v229, v240 row_newbcast:0 row_mask:0xf bank_mask:0xf\n"
    "v_mul_f32_dpp v232, v226, v232 row_newbcast:0 row_mask:0xf bank_mask:0xf\n"
    "v_add_f32_dpp v241, v229, v241 row_newbcast:1 row_mask:0xf bank_mask:0xf\n"
    "v_mfma_f32_16x16x32_bf16 v[64:67], v[184:187], a[200:203], v[64:67]\n"
    "ds_read_b128 v[176:179], v200 offset:8192\n"
    "v_mul_f32_dpp v233, v226, v233 row_newbcast:1 row_mask:0xf bank_mask:0xf\n"
    "v_add_f32_dpp v242, v229, v242 row_newbcast:2 row_mask:0xf bank_mask:0xf\n"
    "v_mul_f32_dpp v234, v226, v234 row_newbcast:2 row_mask:0xf bank_mask:0xf\n"
    "v_mfma_f32_16x16x32_bf16 v[64:67], v[188:191], a[204:207], v[64:67]\n"
    "v_add_f32_dpp v243, v229, v243 row_newbcast:3 row_mask:0xf bank_mask:0xf\n"
    "v_mul_f32_dpp v235, v226, v235 row_newbcast:3 row_mask:0xf bank_mask:0xf\n"
    "v_add_f32_dpp v244, v229, v244 row_newbcast:4 row_mask:0xf bank_mask:0xf\n"
    "v_mul_f32_dpp v236, v226, v236 row_newbcast:4 row_mask:0xf bank_mask:0xf\n"
    "v_mfma_f32_16x16x32_bf16 v[68:71], v[184:187], a[224:227], v[68:71]\n"
    "ds_read_b128 v[180:183], v200 offset:9216\n"
    "v_add_f32_dpp v245, v229, v245 row_newbcast:5 row_mask:0xf bank_mask:0xf\n"
    "v_mul_f32_dpp v237, v226, v237 row_newbcast:5 row_mask:0xf bank_mask:0xf\n"
    "v_add_f32_dpp v246, v229, v246 row_newbcast:6 row_mask:0xf bank_mask:0xf\n"
    "v_mul_f32_dpp v238, v226, v238 row_newbcast:6 row_mask:0xf bank_mask:0xf\n"
    "v_add_f32_dpp v247, v229, v247 row_newbcast:7 row_mask:0xf bank_mask:0xf\n"
    "v_mul_f32_dpp v239, v226, v239 row_newbcast:7 row_mask:0xf bank_mask:0xf\n"
    "v_cvt_pk_bf16_f32 v240, v240, v241\n"
    "v_add_f32_dpp v232, v227, v232 row_newbcast:0 row_mask:0xf bank_mask:0xf\n"
    "v_cvt_pk_bf16_f32 v241, v242, v243\n"
    "v_add_f32_dpp v233, v227, v233 row_newbcast:1 row_mask:0xf bank_mask:0xf\n"
    "v_cvt_pk_bf16_f32 v242, v244, v245\n"
    "v_add_f32_dpp v234, v227, v234 row_newbcast:2 row_mask:0xf bank_mask:0xf\n"
    "v_cvt_pk_bf16_f32 v243, v246, v247\n"
    "v_add_f32_dpp v235, v227, v235 row_newbcast:3 row_mask:0xf bank_mask:0xf\n"
    "v_add_f32_dpp v236, v227, v236 row_newbcast:4 row_mask:0xf bank_mask:0xf\n"
    "v_add_f32_dpp v237, v227, v237 row_newbcast:5 row_mask:0xf bank_mask:0xf\n"
    "v_add_f32_dpp v238, v227, v238 row_newbcast:6 row_mask:0xf bank_mask:0xf\n"
    "v_add_f32_dpp v239, v227, v239 row_newbcast:7 row_mask:0xf bank_mask:0xf\n"
    "v_cvt_pk_bf16_f32 v232, v232, v233\n"
    "v_cvt_pk_bf16_f32 v233, v234, v235\n"
    "v_cvt_pk_bf16_f32 v234, v236, v237\n"
    "v_cvt_pk_bf16_f32 v235, v238, v239\n"
    "v_mfma_f32_16x16x32_bf16 v[68:71], v[188:191], a[228:231], v[68:71]\n"
    "s_waitcnt lgkmcnt(4)\n"
    "s_add_u32 s78, s88, 0x3000\n"
    "v_max3_f32 v196, v40, v41, v42\n"
    "v_max3_f32 v196, v196, v43, v56\n"
    "v_max3_f32 v196, v196, v57, v58\n"
    "buffer_store_dwordx4 v[232:235], v211, s[36:39], s76 offen sc1\n"
    "v_mfma_f32_16x16x32_bf16 v[64:67], v[160:163], a[208:211], v[64:67]\n"
    "s_add_u32 m0, s71, 0xd000\n"
    "ds_read_b128 v[184:187], v200 offset:10240\n"
    "v_max_f32 v196, v196, v59\n"
    "v_max3_f32 v198, v44, v45, v46\n"
    "v_mov_b32 v197, v196\n"
    "v_mfma_f32_16x16x32_bf16 v[64:67], v[164:167], a[212:215], v[64:67]\n"
    "buffer_load_dwordx4 v156, s[28:31], s78 offen nt lds\n"
    "v_max3_f32 v198, v198, v47, v60\n"
    "v_permlane32_swap_b32 v196, v197\n"
    "v_mfma_f32_16x16x32_bf16 v[68:71], v[160:163], a[232:235], v[68:71]\n"
    "ds_read_b128 v[188:191], v200 offset:11264\n"
    "v_max3_f32 v198, v198, v61, v62\n"
    "v_max_f32 v196, v196, v197\n"
    "v_max_f32 v198, v198, v63\n"
    "v_mfma_f32_16x16x32_bf16 v[68:71], v[164:167], a[236:239], v[68:71]\n"
    "s_waitcnt lgkmcnt(4)\n"
    "v_mov_b32 v197, v196\n"
    "v_mov_b32 v199, v198\n"
    "s_nop 0\n"
    "v_permlane16_swap_b32 v196, v197\n"
    "v_permlane32_swap_b32 v198, v199\n"
    "v_mfma_f32_16x16x32_bf16 v[80:83], v[168:171], a[192:195], 0\n"
    "ds_read_b128 v[160:163], v200 offset:12288\n"
    "v_max_f32 v196, v196, v197\n"
    "v_max_f32 v198, v198, v199\n"
    "v_add_f32 v196, 0x42640000, v196\n"
    "v_mfma_f32_16x16x32_bf16 v[80:83], v[172:175], a[196:199], v[80:83]\n"
    "buffer_store_dwordx4 v[240:243], v212, s[40:43], s77 offen sc1\n"
    "v_mov_b32 v199, v198\n"
    "v_sub_f32 v96, 0, v196\n"
    "s_nop 0\n"
    "v_permlane16_swap_b32 v198, v199\n"
    "v_sub_f32 v97, 0, v196\n"
    "v_max_f32 v198, v198, v199\n"
    "v_mfma_f32_16x16x32_bf16 v[84:87], v[168:171], a[216:219], 0\n"
    "ds_read_b128 v[164:167], v200 offset:13312\n"
    "v_sub_f32 v98, 0, v196\n"
    "v_add_f32 v198, 0x42640000, v198\n"
    "v_sub_f32 v99, 0, v196\n"
    "v_mfma_f32_16x16x32_bf16 v[84:87], v[172:175], a[220:223], v[84:87]\n"
    "s_waitcnt lgkmcnt(4)\n"
    "v_sub_f32 v100, 0, v198\n"
    "v_add_f32 v40, v40, v96\n"
    "v_sub_f32 v101, 0, v198\n"
    "v_add_f32 v41, v41, v96\n"
    "v_mfma_f32_16x16x32_bf16 v[80:83], v[176:179], a[200:203], v[80:83]\n"
    "ds_read_b128 v[168:171], v200 offset:14336\n"
    "v_sub_f32 v102, 0, v198\n"
    "v_add_f32 v42, v42, v96\n"
    "v_sub_f32 v103, 0, v198\n"
    "v_mfma_f32_16x16x32_bf16 v[80:83], v[180:183], a[204:207], v[80:83]\n"
    "s_add_u32 s78, s88, 0x3000\n"
    "v_add_f32 v43, v43, v96\n"
    "v_add_f32 v44, v44, v100\n"
    "v_add_f32 v56, v56, v96\n"
    "v_add_f32 v45, v45, v100\n"
    "v_mfma_f32_16x16x32_bf16 v[84:87], v[176:179], a[224:227], v[84:87]\n"
    "s_add_u32 m0, s71, 0xd400\n"
    "ds_read_b128 v[172:175], v200 offset:15360\n"
    "v_add_f32 v57, v57, v96\n"
    "v_add_f32 v46, v46, v100\n"
    "v_add_f32 v58, v58, v96\n"
    "v_mfma_f32_16x16x32_bf16 v[84:87], v[180:183], a[228:231], v[84:87]\n"
    "s_waitcnt lgkmcnt(4)\n"
    "buffer_load_dwordx4 v157, s[28:31], s78 offen nt lds\n"
    "v_add_f32 v47, v47, v100\n"
    "v_add_f32 v59, v59, v96\n"
    "v_mfma_f32_16x16x32_bf16 v[80:83], v[184:187], a[208:211], v[80:83]\n"
    "ds_read_b128 v[176:179], v200 offset:16384\n"
    "v_add_f32 v60, v60, v100\n"
    "v_exp_f32 v40, v40\n"
    "v_mfma_f32_16x16x32_bf16 v[80:83], v[188:191], a[212:215], v[80:83]\n"
    "v_add_f32 v61, v61, v100\n"
    "v_add_f32 v62, v62, v100\n"
    "v_exp_f32 v41, v41\n"
    "v_mfma_f32_16x16x32_bf16 v[84:87], v[184:187], a[232:235], v[84:87]\n"
    "ds_read_b128 v[180:183], v200 offset:17408\n"
    "v_add_f32 v63, v63, v100\n"
    "v_exp_f32 v44, v44\n"
    "v_mfma_f32_16x16x32_bf16 v[84:87], v[188:191], a[236:239], v[84:87]\n"
    "s_waitcnt lgkmcnt(4)\n"
    "v_exp_f32 v42, v42\n"
    "v_exp_f32 v45, v45\n"
    "v_mfma_f32_16x16x32_bf16 v[32:35], v[160:163], a[192:195], v[96:99]\n"
    "ds_read_b128 v[184:187], v200 offset:18432\n"
    "v_exp_f32 v43, v43\n"
    "v_mfma_f32_16x16x32_bf16 v[32:35], v[164:167], a[196:199], v[32:35]\n"
    "v_exp_f32 v46, v46\n"
    "v_exp_f32 v56, v56\n"
    "v_mfma_f32_16x16x32_bf16 v[36:39], v[160:163], a[216:219], v[100:103]\n"
    "ds_read_b128 v[188:191], v200 offset:19456\n"
    "v_exp_f32 v47, v47\n"
    "v_mfma_f32_16x16x32_bf16 v[36:39], v[164:167], a[220:223], v[36:39]\n"
    "s_waitcnt lgkmcnt(4)\n"
    "s_add_u32 s78, s88, 0x4800\n"
    "v_exp_f32 v57, v57\n"
    "v_mfma_f32_16x16x32_bf16 v[32:35], v[168:171], a[200:203], v[32:35]\n"
    "s_add_u32 m0, s71, 0x6000\n"
    "ds_read_b128 v[160:163], v200 offset:20480\n"
    "v_exp_f32 v60, v60\n"
    "v_mfma_f32_16x16x32_bf16 v[32:35], v[172:175], a[204:207], v[32:35]\n"
    "buffer_load_dwordx4 v152, s[28:31], s78 offen nt lds\n"
    "v_exp_f32 v58, v58\n"
    "v_mfma_f32_16x16x32_bf16 v[36:39], v[168:171], a[224:227], v[36:39]\n"
    "ds_read_b128 v[164:167], v200 offset:21504\n"
    "v_exp_f32 v61, v61\n"
    "v_mfma_f32_16x16x32_bf16 v[36:39], v[172:175], a[228:231], v[36:39]\n"
    "s_waitcnt lgkmcnt(4)\n"
    "v_exp_f32 v59, v59\n"
    "v_exp_f32 v62, v62\n"
    "v_mfma_f32_16x16x32_bf16 v[32:35], v[176:179], a[208:211], v[32:35]\n"
    "ds_read_b128 v[168:171], v200 offset:22528\n"
    "v_cvt_pk_bf16_f32 v128, v40, v41\n"
    "v_cvt_pk_bf16_f32 v129, v42, v43\n"
    "v_cvt_pk_bf16_f32 v130, v56, v57\n"
    "v_mfma_f32_16x16x32_bf16 v[32:35], v[180:183], a[212:215], v[32:35]\n"
    "v_exp_f32 v63, v63\n"
    "v_cvt_pk_bf16_f32 v131, v58, v59\n"
    "v_cvt_pk_bf16_f32 v132, v44, v45\n"
    "v_mfma_f32_16x16x32_bf16 v[36:39], v[176:179], a[232:235], v[36:39]\n"
    "ds_read_b128 v[172:175], v200 offset:23552\n"
    "v_add_f32 v64, v64, v96\n"
    "v_cvt_pk_bf16_f32 v133, v46, v47\n"
    "v_add_f32 v65, v65, v96\n"
    "v_mfma_f32_16x16x32_bf16 v[36:39], v[180:183], a[236:239], v[36:39]\n"
    "s_waitcnt lgkmcnt(4)\n"
    "v_cvt_pk_bf16_f32 v134, v60, v61\n"
    "v_add_f32 v66, v66, v96\n"
    "s_waitcnt vmcnt(11)\n"
    "s_barrier\n"
    "v_mfma_f32_16x16x32_bf16 v[48:51], v[184:187], a[192:195], v[96:99]\n"
    "ds_read_b128 v[176:179], v201 offset:24576\n"
    "v_cvt_pk_bf16_f32 v135, v62, v63\n"
    "v_add_f32 v67, v67, v96\n"
    "v_add_f32 v68, v68, v100\n"
    "v_mfma_f32_16x16x32_bf16 v[48:51], v[188:191], a[196:199], v[48:51]\n"
    "s_add_u32 s78, s88, 0x4800\n"
    "v_add_f32 v80, v80, v96\n"
    "v_add_f32 v69, v69, v100\n"
    "v_add_f32 v81, v81, v96\n"
    "v_add_f32 v70, v70, v100\n"
    "v_mfma_f32_16x16x32_bf16 v[52:55], v[184:187], a[216:219], v[100:103]\n"
    "s_add_u32 m0, s71, 0x6400\n"
    "ds_read_b128 v[180:183], v201 offset:25600\n"
    "v_add_f32 v82, v82, v96\n"
    "v_add_f32 v71, v71, v100\n"
    "v_add_f32 v83, v83, v96\n"
    "v_mfma_f32_16x16x32_bf16 v[52:55], v[188:191], a[220:223], v[52:55]\n"
    "s_waitcnt lgkmcnt(4)\n"
    "buffer_load_dwordx4 v153, s[28:31], s78 offen nt lds\n"
    "v_add_f32 v84, v84, v100\n"
    "v_add_f32 v85, v85, v100\n"
    "v_mfma_f32_16x16x32_bf16 v[48:51], v[160:163], a[200:203], v[48:51]\n"
    "ds_read_b128 v[184:187], v201 offset:26624\n"
    "v_exp_f32 v64, v64\n"
    "v_add_f32 v86, v86, v100\n"
    "v_mfma_f32_16x16x32_bf16 v[48:51], v[164:167], a[204:207], v[48:51]\n"
    "v_exp_f32 v65, v65\n"
    "v_add_f32 v87, v87, v100\n"
    "ds_read_b64 v[228:229], v210 offset:192\n"
    "v_mfma_f32_16x16x32_bf16 v[52:55], v[160:163], a[224:227], v[52:55]\n"
    "ds_read_b128 v[188:191], v201 offset:27648\n"
    "v_exp_f32 v68, v68\n"
    "ds_read2st64_b32 v[240:241], v207 offset0:96 offset1:99\n"
    "v_mfma_f32_16x16x32_bf16 v[52:55], v[164:167], a[228:231], v[52:55]\n"
    "s_waitcnt lgkmcnt(6)\n"
    "v_exp_f32 v66, v66\n"
    "v_exp_f32 v69, v69\n"
    "v_mfma_f32_16x16x32_bf16 v[48:51], v[168:171], a[208:211], v[48:51]\n"
    "ds_read_b128 v[160:163], v201 offset:28672\n"
    "v_exp_f32 v67, v67\n"
    "ds_read2st64_b32 v[242:243], v207 offset0:102 offset1:105\n"
    "v_mfma_f32_16x16x32_bf16 v[48:51], v[172:175], a[212:215], v[48:51]\n"
    "v_exp_f32 v70, v70\n"
    "v_exp_f32 v80, v80\n"
    "v_mfma_f32_16x16x32_bf16 v[52:55], v[168:171], a[232:235], v[52:55]\n"
    "ds_read_b128 v[164:167], v201 offset:29696\n"
    "v_exp_f32 v71, v71\n"
    "ds_read2st64_b32 v[244:245], v207 offset0:108 offset1:111\n"
    "v_exp_f32 v81, v81\n"
    "v_exp_f32 v82, v82\n"
    "v_exp_f32 v83, v83\n"
    "v_cvt_pk_bf16_f32 v144, v64, v65\n"
    "v_cvt_pk_bf16_f32 v145, v66, v67\n"
    "v_cvt_pk_bf16_f32 v146, v80, v81\n"
    "v_cvt_pk_bf16_f32 v147, v82, v83\n"
    "v_mfma_f32_16x16x32_bf16 v[52:55], v[172:175], a[236:239], v[52:55]\n"
    "s_waitcnt lgkmcnt(8)\n"
    "s_add_u32 s78, s88, 0x4800\n"
    "v_exp_f32 v84, v84\n"
    "ds_read2st64_b32 v[246:247], v207 offset0:114 offset1:117\n"
    "v_mfma_f32_16x16x32_bf16 a[0:3], v[176:179], v[128:131], 0\n"
    "s_add_u32 m0, s71, 0x6800\n"
    "ds_read_b128 v[168:171], v201 offset:30720\n"
    "ds_read_u16_d16_hi v223, v209 offset:6912\n"
    "v_exp_f32 v85, v85\n"
    "v_exp_f32 v86, v86\n"
    "v_exp_f32 v87, v87\n"
    "v_cvt_pk_bf16_f32 v148, v68, v69\n"
    "v_cvt_pk_bf16_f32 v149, v70, v71\n"
    "v_cvt_pk_bf16_f32 v150, v84, v85\n"
    "v_cvt_pk_bf16_f32 v151, v86, v87\n"
    "v_mfma_f32_16x16x32_bf16 a[0:3], v[180:183], v[144:147], a[0:3]\n"
    "buffer_load_dwordx4 v154, s[28:31], s78 offen nt lds\n"
    "v_mfma_f32_16x16x32_bf16 a[4:7], v[176:179], v[132:135], 0\n"
    "ds_read_b128 v[172:175], v201 offset:31744\n"
    "ds_read_u16_d16_hi v222, v209 offset:768\n"
    "v_mfma_f32_16x16x32_bf16 a[4:7], v[180:183], v[148:151], a[4:7]\n"
    "s_waitcnt lgkmcnt(10)\n"
    "v_exp_f32 v32, v32\n"
    "v_mfma_f32_16x16x32_bf16 a[16:19], v[184:187], v[128:131], 0\n"
    "ds_read_b128 v[176:179], v201 offset:32768\n"
    "ds_read_b128 v[232:235], v205 offset:24576\n"
    "v_mfma_f32_16x16x32_bf16 a[16:19], v[188:191], v[144:147], a[16:19]\n"
    "ds_read_b128 v[236:239], v206 offset:24576\n"
    "v_mfma_f32_16x16x32_bf16 a[20:23], v[184:187], v[132:135], 0\n"
    "ds_read_b128 v[180:183], v201 offset:33792\n"
    "s_waitcnt lgkmcnt(13)\n"
    "v_cvt_f32_i32 v240, v240\n"
    "v_mfma_f32_16x16x32_bf16 a[20:23], v[188:191], v[148:151], a[20:23]\n"
    "s_waitcnt lgkmcnt(10)\n"
    "v_exp_f32 v33, v33\n"
    "v_mfma_f32_16x16x32_bf16 a[32:35], v[160:163], v[128:131], 0\n"
    "ds_read_b128 v[184:187], v201 offset:34816\n"
    "v_cvt_f32_i32 v241, v241\n"
    "v_mfma_f32_16x16x32_bf16 a[32:35], v[164:167], v[144:147], a[32:35]\n"
    "s_add_u32 s78, s88, 0x4800\n"
    "v_exp_f32 v34, v34\n"
    "v_mfma_f32_16x16x32_bf16 a[36:39], v[160:163], v[132:135], 0\n"
    "s_add_u32 m0, s71, 0x6c00\n"
    "ds_read_b128 v[188:191], v201 offset:35840\n"
    "v_cvt_f32_i32 v242, v242\n"
    "v_mfma_f32_16x16x32_bf16 a[36:39], v[164:167], v[148:151], a[36:39]\n"
    "buffer_load_dwordx4 v155, s[28:31], s78 offen nt lds\n"
    "v_mfma_f32_16x16x32_bf16 v[112:115], v[192:195], v[128:131], 0\n"
    "v_exp_f32 v35, v35\n"
    "v_mfma_f32_16x16x32_bf16 v[112:115], v[192:195], v[144:147], v[112:115]\n"
    "s_waitcnt lgkmcnt(7)\n"
    "v_exp_f32 v36, v36\n"
    "v_mfma_f32_16x16x32_bf16 a[48:51], v[168:171], v[128:131], 0\n"
    "ds_read_b128 v[160:163], v201 offset:36864\n"
    "v_cvt_f32_i32 v243, v243\n"
    "v_mfma_f32_16x16x32_bf16 a[48:51], v[172:175], v[144:147], a[48:51]\n"
    "v_cvt_f32_i32 v244, v244\n"
    "v_cvt_f32_i32 v245, v245\n"
    "v_mfma_f32_16x16x32_bf16 a[52:55], v[168:171], v[132:135], 0\n"
    "ds_read_b128 v[164:167], v201 offset:37888\n"
    "v_cvt_f32_i32 v246, v246\n"
    "v_mfma_f32_16x16x32_bf16 a[52:55], v[172:175], v[148:151], a[52:55]\n"
    "v_exp_f32 v37, v37\n"
    "v_mfma_f32_16x16x32_bf16 v[116:119], v[192:195], v[132:135], 0\n"
    "v_exp_f32 v38, v38\n"
    "v_mfma_f32_16x16x32_bf16 v[116:119], v[192:195], v[148:151], v[116:119]\n"
    "s_waitcnt lgkmcnt(4)\n"
    "s_add_u32 s78, s88, 0x4800\n"
    "v_mul_f32 v226, s75, v223\n"
    "v_mfma_f32_16x16x32_bf16 a[64:67], v[176:179], v[128:131], 0\n"
    "s_add_u32 m0, s71, 0x7000\n"
    "ds_read_b128 v[168:171], v201 offset:38912\n"
    "v_cvt_f32_i32 v247, v247\n"
    "v_mfma_f32_16x16x32_bf16 a[64:67], v[180:183], v[144:147], a[64:67]\n"
    "buffer_load_dwordx4 v156, s[28:31], s78 offen nt lds\n"
    "v_mfma_f32_16x16x32_bf16 a[68:71], v[176:179], v[132:135], 0\n"
    "ds_read_b128 v[172:175], v201 offset:39936\n"
    "v_fma_f32 v227, -v222, v226, v217\n"
    "v_mfma_f32_16x16x32_bf16 a[68:71], v[180:183], v[148:151], a[68:71]\n"
    "s_waitcnt lgkmcnt(4)\n"
    "v_mul_f32_dpp v240, v228, v240 row_newbcast:0 row_mask:0xf bank_mask:0xf\n"
    "v_cvt_f32_i32 v232, v232\n"
    "v_mfma_f32_16x16x32_bf16 a[80:83], v[184:187], v[128:131], 0\n"
    "ds_read_b128 v[176:179], v201 offset:40960\n"
    "v_mul_f32_dpp v241, v228, v241 row_newbcast:1 row_mask:0xf bank_mask:0xf\n"
    "v_mfma_f32_16x16x32_bf16 a[80:83], v[188:191], v[144:147], a[80:83]\n"
    "v_exp_f32 v39, v39\n"
    "v_mfma_f32_16x16x32_bf16 a[84:87], v[184:187], v[132:135], 0\n"
    "ds_read_b128 v[180:183], v201 offset:41984\n"
    "v_cvt_f32_i32 v233, v233\n"
    "v_mfma_f32_16x16x32_bf16 a[84:87], v[188:191], v[148:151], a[84:87]\n"
    "s_waitcnt lgkmcnt(4)\n"
    "v_mul_f32_dpp v242, v228, v242 row_newbcast:2 row_mask:0xf bank_mask:0xf\n"
    "v_cvt_f32_i32 v234, v234\n"
    "v_mfma_f32_16x16x32_bf16 a[96:99], v[160:163], v[128:131], 0\n"
    "ds_read_b128 v[184:187], v201 offset:43008\n"
    "v_mul_f32_dpp v243, v228, v243 row_newbcast:3 row_mask:0xf bank_mask:0xf\n"
    "v_mfma_f32_16x16x32_bf16 a[96:99], v[164:167], v[144:147], a[96:99]\n"
    "s_add_u32 s78, s88, 0x4800\n"
    "v_exp_f32 v48, v48\n"
    "v_mfma_f32_16x16x32_bf16 a[100:103], v[160:163], v[132:135], 0\n"
    "s_add_u32 m0, s71, 0x7400\n"
    "ds_read_b128 v[188:191], v201 offset:44032\n"
    "v_cvt_f32_i32 v235, v235\n"
    "v_mfma_f32_16x16x32_bf16 a[100:103], v[164:167], v[148:151], a[100:103]\n"
    "s_waitcnt lgkmcnt(4)\n"
    "buffer_load_dwordx4 v157, s[28:31], s78 offen nt lds\n"
    "v_mfma_f32_16x16x32_bf16 a[112:115], v[168:171], v[128:131], 0\n"
    "ds_read_b128 v[160:163], v201 offset:45056\n"
    "v_mul_f32_dpp v244, v228, v244 row_newbcast:4 row_mask:0xf bank_mask:0xf\n"
    "v_mfma_f32_16x16x32_bf16 a[112:115], v[172:175], v[144:147], a[112:115]\n"
    "v_exp_f32 v49, v49\n"
    "v_mfma_f32_16x16x32_bf16 a[116:119], v[168:171], v[132:135], 0\n"
    "ds_read_b128 v[164:167], v201 offset:46080\n"
    "v_cvt_f32_i32 v236, v236\n"
    "v_mfma_f32_16x16x32_bf16 a[116:119], v[172:175], v[148:151], a[116:119]\n"
    "s_waitcnt lgkmcnt(4)\n"
    "v_mul_f32_dpp v245, v228, v245 row_newbcast:5 row_mask:0xf bank_mask:0xf\n"
    "v_cvt_f32_i32 v237, v237\n"
    "v_mfma_f32_16x16x32_bf16 a[128:131], v[176:179], v[128:131], 0\n"
    "ds_read_b128 v[168:171], v201 offset:47104\n"
    "v_mul_f32_dpp v246, v228, v246 row_newbcast:6 row_mask:0xf bank_mask:0xf\n"
    "v_mfma_f32_16x16x32_bf16 a[128:131], v[180:183], v[144:147], a[128:131]\n"
    "v_exp_f32 v50, v50\n"
    "v_mfma_f32_16x16x32_bf16 a[132:135], v[176:179], v[132:135], 0\n"
    "ds_read_b128 v[172:175], v201 offset:48128\n"
    "v_cvt_f32_i32 v238, v238\n"
    "v_mfma_f32_16x16x32_bf16 a[132:135], v[180:183], v[148:151], a[132:135]\n"
    "s_waitcnt lgkmcnt(4)\n"
    "v_mul_f32_dpp v247, v228, v247 row_newbcast:7 row_mask:0xf bank_mask:0xf\n"
    "v_cvt_f32_i32 v239, v239\n"
    "v_mfma_f32_16x16x32_bf16 a[144:147], v[184:187], v[128:131], 0\n"
    "v_exp_f32 v51, v51\n"
    "v_mfma_f32_16x16x32_bf16 a[144:147], v[188:191], v[144:147], a[144:147]\n"
    "v_add_f32_dpp v240, v229, v240 row_newbcast:0 row_mask:0xf bank_mask:0xf\n"
    "v_mul_f32_dpp v232, v226, v232 row_newbcast:0 row_mask:0xf bank_mask:0xf\n"
    "v_mfma_f32_16x16x32_bf16 a[148:151], v[184:187], v[132:135], 0\n"
    "v_add_f32_dpp v241, v229, v241 row_newbcast:1 row_mask:0xf bank_mask:0xf\n"
    "v_mul_f32_dpp v233, v226, v233 row_newbcast:1 row_mask:0xf bank_mask:0xf\n"
    "v_mfma_f32_16x16x32_bf16 a[148:151], v[188:191], v[148:151], a[148:151]\n"
    "s_waitcnt lgkmcnt(2)\n"
    "v_exp_f32 v52, v52\n"
    "v_mfma_f32_16x16x32_bf16 a[160:163], v[160:163], v[128:131], 0\n"
    "v_add_f32_dpp v242, v229, v242 row_newbcast:2 row_mask:0xf bank_mask:0xf\n"
    "v_mul_f32_dpp v234, v226, v234 row_newbcast:2 row_mask:0xf bank_mask:0xf\n"
    "v_mfma_f32_16x16x32_bf16 a[160:163], v[164:167], v[144:147], a[160:163]\n"
    "v_add_f32_dpp v243, v229, v243 row_newbcast:3 row_mask:0xf bank_mask:0xf\n"
    "v_mul_f32_dpp v235, v226, v235 row_newbcast:3 row_mask:0xf bank_mask:0xf\n"
    "v_mfma_f32_16x16x32_bf16 a[164:167], v[160:163], v[132:135], 0\n"
    "v_exp_f32 v53, v53\n"
    "v_mfma_f32_16x16x32_bf16 a[164:167], v[164:167], v[148:151], a[164:167]\n"
    "s_waitcnt lgkmcnt(0)\n"
    "v_add_f32_dpp v244, v229, v244 row_newbcast:4 row_mask:0xf bank_mask:0xf\n"
    "v_mul_f32_dpp v236, v226, v236 row_newbcast:4 row_mask:0xf bank_mask:0xf\n"
    "v_mfma_f32_16x16x32_bf16 a[176:179], v[168:171], v[128:131], 0\n"
    "v_add_f32_dpp v245, v229, v245 row_newbcast:5 row_mask:0xf bank_mask:0xf\n"
    "v_mul_f32_dpp v237, v226, v237 row_newbcast:5 row_mask:0xf bank_mask:0xf\n"
    "v_mfma_f32_16x16x32_bf16 a[176:179], v[172:175], v[144:147], a[176:179]\n"
    "v_exp_f32 v54, v54\n"
    "v_mfma_f32_16x16x32_bf16 a[180:183], v[168:171], v[132:135], 0\n"
    "v_add_f32_dpp v246, v229, v246 row_newbcast:6 row_mask:0xf bank_mask:0xf\n"
    "v_mul_f32_dpp v238, v226, v238 row_newbcast:6 row_mask:0xf bank_mask:0xf\n"
    "v_add_f32_dpp v247, v229, v247 row_newbcast:7 row_mask:0xf bank_mask:0xf\n"
    "v_exp_f32 v55, v55\n"
    "v_mul_f32_dpp v239, v226, v239 row_newbcast:7 row_mask:0xf bank_mask:0xf\n"
    "v_cvt_pk_bf16_f32 v240, v240, v241\n"
    "v_add_f32_dpp v232, v227, v232 row_newbcast:0 row_mask:0xf bank_mask:0xf\n"
    "v_cvt_pk_bf16_f32 v241, v242, v243\n"
    "v_add_f32_dpp v233, v227, v233 row_newbcast:1 row_mask:0xf bank_mask:0xf\n"
    "v_cvt_pk_bf16_f32 v242, v244, v245\n"
    "v_add_f32_dpp v234, v227, v234 row_newbcast:2 row_mask:0xf bank_mask:0xf\n"
    "v_cvt_pk_bf16_f32 v243, v246, v247\n"
    "v_add_f32_dpp v235, v227, v235 row_newbcast:3 row_mask:0xf bank_mask:0xf\n"
    "v_add_f32_dpp v236, v227, v236 row_newbcast:4 row_mask:0xf bank_mask:0xf\n"
    "v_add_f32_dpp v237, v227, v237 row_newbcast:5 row_mask:0xf bank_mask:0xf\n"
    "v_add_f32_dpp v238, v227, v238 row_newbcast:6 row_mask:0xf bank_mask:0xf\n"
    "v_add_f32_dpp v239, v227, v239 row_newbcast:7 row_mask:0xf bank_mask:0xf\n"
    "v_cvt_pk_bf16_f32 v232, v232, v233\n"
    "v_cvt_pk_bf16_f32 v233, v234, v235\n"
    "v_cvt_pk_bf16_f32 v234, v236, v237\n"
    "v_cvt_pk_bf16_f32 v235, v238, v239\n"
    "v_mfma_f32_16x16x32_bf16 a[180:183], v[172:175], v[148:151], a[180:183]\n"
    "v_add_f32 v113, v113, v32\n"
    "v_add_f32 v114, v114, v33\n"
    "v_add_f32 v113, v113, v34\n"
    "v_add_f32 v114, v114, v35\n"
    "v_add_f32 v117, v117, v36\n"
    "v_add_f32 v118, v118, v37\n"
    "v_add_f32 v117, v117, v38\n"
    "v_add_f32 v118, v118, v39\n"
    "v_add_f32 v113, v113, v48\n"
    "v_add_f32 v114, v114, v49\n"
    "v_add_f32 v113, v113, v50\n"
    "v_add_f32 v114, v114, v51\n"
    "v_add_f32 v117, v117, v52\n"
    "buffer_store_dwordx4 v[232:235], v211, s[36:39], s62 offen sc1\n"
    "buffer_store_dwordx4 v[240:243], v212, s[40:43], s63 offen sc1\n"
    "v_add_f32 v118, v118, v53\n"
    "v_add_f32 v117, v117, v54\n"
    "v_add_f32 v118, v118, v55\n"
    "s_waitcnt vmcnt(8)\n"
    "ds_read_b128 a[240:243], v158 offset:49152\n"
    "ds_read_b128 a[244:247], v159 offset:49152\n"
    "ds_read_b128 a[248:251], v158 offset:49280\n"
    "ds_read_b128 a[252:255], v159 offset:49280\n"
    "ds_read_b128 v[0:3], v158 offset:49408\n"
    "ds_read_b128 v[4:7], v159 offset:49408\n"
    "v_cmp_ne_u32 s[80:81], s67, v219\n"
    "v_cmp_gt_u32 vcc, s66, v218\n"
    "s_or_b64 vcc, vcc, s[80:81]\n"
    "s_cbranch_vccnz LSPRE1\n"
    "LSPB1:\n"
    "v_mov_b32 v220, 4\n"
    "s_add_u32 s66, s66, s74\n"
    "s_waitcnt lgkmcnt(0)\n"
    "s_barrier\n"
    "buffer_store_dwordx2 v[220:221], v214, s[44:47], 0 offen sc1\n"
    "ds_read_b128 v[160:163], v255\n"
    "ds_read_b128 v[164:167], v255 offset:1024\n"
    "ds_read_b128 v[168:171], v255 offset:2048\n"
    "ds_read_b128 v[172:175], v255 offset:3072\n"
    "ds_read_b128 v[176:179], v255 offset:4096\n"
    "ds_read_b128 v[180:183], v255 offset:5120\n"
    "s_waitcnt lgkmcnt(4)\n"
    "v_mfma_f32_16x16x32_bf16 v[44:47], v[160:163], a[240:243], 0\n"
    "ds_read_b128 v[184:187], v255 offset:6144\n"
    "v_mfma_f32_16x16x32_bf16 v[44:47], v[164:167], a[244:247], v[44:47]\n"
    "s_waitcnt lgkmcnt(3)\n"
    "ds_read_b128 v[188:191], v255 offset:7168\n"
    "v_mfma_f32_16x16x32_bf16 v[44:47], v[168:171], a[248:251], v[44:47]\n"
    "s_add_u32 m0, s71, 0xc000\n"
    "ds_read_b128 v[160:163], v255 offset:8192\n"
    "v_mfma_f32_16x16x32_bf16 v[44:47], v[172:175], a[252:255], v[44:47]\n"
    "s_waitcnt lgkmcnt(3)\n"
    "ds_read_b128 v[164:167], v255 offset:9216\n"
    "buffer_load_dwordx4 v200, s[40:43], s61 offen sc1 lds\n"
    "v_mfma_f32_16x16x32_bf16 v[44:47], v[176:179], v[0:3], v[44:47]\n"
    "ds_read_b128 v[168:171], v255 offset:10240\n"
    "v_mfma_f32_16x16x32_bf16 v[44:47], v[180:183], v[4:7], v[44:47]\n"
    "s_waitcnt lgkmcnt(3)\n"
    "ds_read_b128 v[172:175], v255 offset:11264\n"
    "v_mfma_f32_16x16x32_bf16 v[60:63], v[184:187], a[240:243], 0\n"
    "ds_read_b128 v[176:179], v200\n"
    "v_mfma_f32_16x16x32_bf16 v[60:63], v[188:191], a[244:247], v[60:63]\n"
    "s_waitcnt lgkmcnt(3)\n"
    "ds_read_b128 v[180:183], v200 offset:1024\n"
    "v_mfma_f32_16x16x32_bf16 v[60:63], v[160:163], a[248:251], v[60:63]\n"
    "ds_read_b128 v[184:187], v200 offset:2048\n"
    "buffer_load_dwordx4 v200, s[40:43], s61 offen offset:1024 sc1 lds\n"
    "v_mfma_f32_16x16x32_bf16 v[60:63], v[164:167], a[252:255], v[60:63]\n"
    "s_waitcnt lgkmcnt(3)\n"
    "ds_read_b128 v[188:191], v200 offset:3072\n"
    "v_mfma_f32_16x16x32_bf16 v[60:63], v[168:171], v[0:3], v[60:63]\n"
    "ds_read_b128 v[160:163], v200 offset:4096\n"
    "v_mfma_f32_16x16x32_bf16 v[60:63], v[172:175], v[4:7], v[60:63]\n"
    "s_waitcnt lgkmcnt(3)\n"
    "ds_read_b128 v[164:167], v200 offset:5120\n"
    "v_mfma_f32_16x16x32_bf16 v[72:75], v[176:179], a[240:243], 0\n"
    "ds_read_b128 v[168:171], v200 offset:6144\n"
    "v_mfma_f32_16x16x32_bf16 v[72:75], v[180:183], a[244:247], v[72:75]\n"
    "s_waitcnt lgkmcnt(3)\n"
    "ds_read_b128 v[172:175], v200 offset:7168\n"
    "buffer_load_dwordx4 v200, s[40:43], s61 offen offset:2048 sc1 lds\n"
    "v_mfma_f32_16x16x32_bf16 v[72:75], v[184:187], a[248:251], v[72:75]\n"
    "ds_read_b128 v[176:179], v200 offset:8192\n"
    "v_mfma_f32_16x16x32_bf16 v[72:75], v[188:191], a[252:255], v[72:75]\n"
    "s_waitcnt lgkmcnt(3)\n"
    "ds_read_b128 v[180:183], v200 offset:9216\n"
    "v_mfma_f32_16x16x32_bf16 v[72:75], v[160:163], v[0:3], v[72:75]\n"
    "ds_read_b128 v[184:187], v200 offset:10240\n"
    "v_mfma_f32_16x16x32_bf16 v[72:75], v[164:167], v[4:7], v[72:75]\n"
    "s_waitcnt lgkmcnt(3)\n"
    "ds_read_b128 v[188:191], v200 offset:11264\n"
    "v_max3_f32 v196, v44, v45, v46\n"
    "v_max3_f32 v196, v196, v47, v60\n"
    "v_max3_f32 v196, v196, v61, v62\n"
    "v_mfma_f32_16x16x32_bf16 v[88:91], v[168:171], a[240:243], 0\n"
    "ds_read_b128 v[160:163], v200 offset:12288\n"
    "buffer_load_dwordx4 v200, s[40:43], s61 offen offset:3072 sc1 lds\n"
    "v_max_f32 v196, v196, v63\n"
    "v_mfma_f32_16x16x32_bf16 v[88:91], v[172:175], a[244:247], v[88:91]\n"
    "s_waitcnt lgkmcnt(3)\n"
    "ds_read_b128 v[164:167], v200 offset:13312\n"
    "v_mov_b32 v197, v196\n"
    "s_nop 1\n"
    "v_permlane32_swap_b32 v196, v197\n"
    "v_max_f32 v196, v196, v197\n"
    "v_mfma_f32_16x16x32_bf16 v[88:91], v[176:179], a[248:251], v[88:91]\n"
    "ds_read_b128 v[168:171], v200 offset:14336\n"
    "v_mov_b32 v197, v196\n"
    "s_nop 1\n"
    "v_permlane16_swap_b32 v196, v197\n"
    "v_max_f32 v196, v196, v197\n"
    "v_mfma_f32_16x16x32_bf16 v[88:91], v[180:183], a[252:255], v[88:91]\n"
    "s_waitcnt lgkmcnt(3)\n"
    "s_add_u32 s65, s61, 0x1000\n"
    "ds_read_b128 v[172:175], v200 offset:15360\n"
    "v_add_f32 v196, 0x42640000, v196\n"
    "v_sub_f32 v104, 0, v196\n"
    "v_mfma_f32_16x16x32_bf16 v[88:91], v[184:187], v[0:3], v[88:91]\n"
    "s_add_u32 m0, s71, 0xd000\n"
    "ds_read_b128 v[176:179], v200 offset:16384\n"
    "v_sub_f32 v105, 0, v196\n"
    "v_sub_f32 v106, 0, v196\n"
    "v_sub_f32 v107, 0, v196\n"
    "v_mfma_f32_16x16x32_bf16 v[88:91], v[188:191], v[4:7], v[88:91]\n"
    "s_waitcnt lgkmcnt(3)\n"
    "ds_read_b128 v[180:183], v200 offset:17408\n"
    "buffer_load_dwordx4 v200, s[40:43], s65 offen sc1 lds\n"
    "v_add_f32 v44, v44, v104\n"
    "v_mfma_f32_16x16x32_bf16 v[40:43], v[160:163], a[240:243], v[104:107]\n"
    "ds_read_b128 v[184:187], v200 offset:18432\n"
    "v_add_f32 v45, v45, v104\n"
    "v_add_f32 v46, v46, v104\n"
    "v_add_f32 v47, v47, v104\n"
    "v_mfma_f32_16x16x32_bf16 v[40:43], v[164:167], a[244:247], v[40:43]\n"
    "s_waitcnt lgkmcnt(3)\n"
    "ds_read_b128 v[188:191], v200 offset:19456\n"
    "v_add_f32 v60, v60, v104\n"
    "v_add_f32 v61, v61, v104\n"
    "v_add_f32 v62, v62, v104\n"
    "v_mfma_f32_16x16x32_bf16 v[40:43], v[168:171], a[248:251], v[40:43]\n"
    "ds_read_b128 v[160:163], v200 offset:20480\n"
    "v_add_f32 v63, v63, v104\n"
    "v_exp_f32 v44, v44\n"
    "v_mfma_f32_16x16x32_bf16 v[40:43], v[172:175], a[252:255], v[40:43]\n"
    "s_waitcnt lgkmcnt(3)\n"
    "ds_read_b128 v[164:167], v200 offset:21504\n"
    "v_exp_f32 v45, v45\n"
    "v_mfma_f32_16x16x32_bf16 v[40:43], v[176:179], v[0:3], v[40:43]\n"
    "ds_read_b128 v[168:171], v200 offset:22528\n"
    "buffer_load_dwordx4 v200, s[40:43], s65 offen offset:1024 sc1 lds\n"
    "v_mfma_f32_16x16x32_bf16 v[40:43], v[180:183], v[4:7], v[40:43]\n"
    "s_waitcnt lgkmcnt(3)\n"
    "ds_read_b128 v[172:175], v200 offset:23552\n"
    "v_exp_f32 v46, v46\n"
    "v_mfma_f32_16x16x32_bf16 v[56:59], v[184:187], a[240:243], v[104:107]\n"
    "ds_read_b128 v[176:179], v201 offset:24576\n"
    "v_exp_f32 v47, v47\n"
    "v_mfma_f32_16x16x32_bf16 v[56:59], v[188:191], a[244:247], v[56:59]\n"
    "s_waitcnt lgkmcnt(3)\n"
    "s_add_u32 s78, s64, 0x30000\n"
    "ds_read_b128 v[180:183], v201 offset:25600\n"
    "v_exp_f32 v60, v60\n"
    "v_mfma_f32_16x16x32_bf16 v[56:59], v[160:163], a[248:251], v[56:59]\n"
    "s_add_u32 m0, s72, 0x1b000\n"
    "ds_read_b128 v[184:187], v201 offset:26624\n"
    "v_exp_f32 v61, v61\n"
    "v_mfma_f32_16x16x32_bf16 v[56:59], v[164:167], a[252:255], v[56:59]\n"
    "s_waitcnt lgkmcnt(3)\n"
    "ds_read_b128 v[188:191], v201 offset:27648\n"
    "buffer_load_dwordx4 v202, s[32:35], s78 offen lds\n"
    "v_mfma_f32_16x16x32_bf16 v[56:59], v[168:171], v[0:3], v[56:59]\n"
    "ds_read_b128 v[160:163], v201 offset:28672\n"
    "v_exp_f32 v62, v62\n"
    "v_exp_f32 v63, v63\n"
    "v_cvt_pk_bf16_f32 v136, v44, v45\n"
    "v_cvt_pk_bf16_f32 v137, v46, v47\n"
    "v_cvt_pk_bf16_f32 v138, v60, v61\n"
    "v_cvt_pk_bf16_f32 v139, v62, v63\n"
    "v_add_f32 v72, v72, v104\n"
    "v_add_f32 v73, v73, v104\n"
    "v_add_f32 v74, v74, v104\n"
    "v_add_f32 v75, v75, v104\n"
    "v_add_f32 v88, v88, v104\n"
    "v_add_f32 v89, v89, v104\n"
    "v_add_f32 v90, v90, v104\n"
    "v_add_f32 v91, v91, v104\n"
    "v_exp_f32 v72, v72\n"
    "v_exp_f32 v73, v73\n"
    "v_exp_f32 v74, v74\n"
    "v_exp_f32 v75, v75\n"
    "v_exp_f32 v88, v88\n"
    "v_exp_f32 v89, v89\n"
    "v_exp_f32 v90, v90\n"
    "v_exp_f32 v91, v91\n"
    "v_cvt_pk_bf16_f32 v152, v72, v73\n"
    "v_cvt_pk_bf16_f32 v153, v74, v75\n"
    "v_cvt_pk_bf16_f32 v154, v88, v89\n"
    "v_cvt_pk_bf16_f32 v155, v90, v91\n"
    "v_mfma_f32_16x16x32_bf16 v[56:59], v[172:175], v[4:7], v[56:59]\n"
    "s_waitcnt lgkmcnt(3)\n"
    "ds_read_b128 v[164:167], v201 offset:29696\n"
    "v_exp_f32 v40, v40\n"
    "v_mfma_f32_16x16x32_bf16 a[8:11], v[176:179], v[136:139], 0\n"
    "ds_read_b128 v[168:171], v201 offset:30720\n"
    "v_mfma_f32_16x16x32_bf16 a[8:11], v[180:183], v[152:155], a[8:11]\n"
    "s_waitcnt lgkmcnt(3)\n"
    "ds_read_b128 v[172:175], v201 offset:31744\n"
    "v_mfma_f32_16x16x32_bf16 a[24:27], v[184:187], v[136:139], 0\n"
    "ds_read_b128 v[176:179], v201 offset:32768\n"
    "buffer_load_dwordx4 v203, s[32:35], s78 offen offset:1024 lds\n"
    "v_mfma_f32_16x16x32_bf16 a[24:27], v[188:191], v[152:155], a[24:27]\n"
    "s_waitcnt lgkmcnt(3)\n"
    "ds_read_b128 v[180:183], v201 offset:33792\n"
    "v_mfma_f32_16x16x32_bf16 a[40:43], v[160:163], v[136:139], 0\n"
    "ds_read_b128 v[184:187], v201 offset:34816\n"
    "v_mfma_f32_16x16x32_bf16 a[40:43], v[164:167], v[152:155], a[40:43]\n"
    "ds_read_b128 v[188:191], v201 offset:35840\n"
    "v_mfma_f32_16x16x32_bf16 v[120:123], v[192:195], v[136:139], 0\n"
    "v_exp_f32 v41, v41\n"
    "v_mfma_f32_16x16x32_bf16 v[120:123], v[192:195], v[152:155], v[120:123]\n"
    "s_waitcnt lgkmcnt(4)\n"
    "buffer_load_dwordx4 v204, s[32:35], s78 offen offset:2048 lds\n"
    "v_mfma_f32_16x16x32_bf16 a[56:59], v[168:171], v[136:139], 0\n"
    "ds_read_b128 v[160:163], v201 offset:36864\n"
    "v_mfma_f32_16x16x32_bf16 a[56:59], v[172:175], v[152:155], a[56:59]\n"
    "s_waitcnt lgkmcnt(3)\n"
    "ds_read_b128 v[164:167], v201 offset:37888\n"
    "v_mfma_f32_16x16x32_bf16 a[72:75], v[176:179], v[136:139], 0\n"
    "ds_read_b128 v[168:171], v201 offset:38912\n"
    "v_mfma_f32_16x16x32_bf16 a[72:75], v[180:183], v[152:155], a[72:75]\n"
    "s_waitcnt lgkmcnt(3)\n"
    "ds_read_b128 v[172:175], v201 offset:39936\n"
    "v_mfma_f32_16x16x32_bf16 a[88:91], v[184:187], v[136:139], 0\n"
    "ds_read_b128 v[176:179], v201 offset:40960\n"
    "v_mfma_f32_16x16x32_bf16 a[88:91], v[188:191], v[152:155], a[88:91]\n"
    "s_waitcnt lgkmcnt(3)\n"
    "ds_read_b128 v[180:183], v201 offset:41984\n"
    "v_mfma_f32_16x16x32_bf16 a[104:107], v[160:163], v[136:139], 0\n"
    "ds_read_b128 v[184:187], v201 offset:43008\n"
    "v_mfma_f32_16x16x32_bf16 a[104:107], v[164:167], v[152:155], a[104:107]\n"
    "s_waitcnt lgkmcnt(3)\n"
    "ds_read_b128 v[188:191], v201 offset:44032\n"
    "v_mfma_f32_16x16x32_bf16 a[120:123], v[168:171], v[136:139], 0\n"
    "ds_read_b128 v[160:163], v201 offset:45056\n"
    "v_mfma_f32_16x16x32_bf16 a[120:123], v[172:175], v[152:155], a[120:123]\n"
    "s_waitcnt lgkmcnt(3)\n"
    "ds_read_b128 v[164:167], v201 offset:46080\n"
    "v_mfma_f32_16x16x32_bf16 a[136:139], v[176:179], v[136:139], 0\n"
    "ds_read_b128 v[168:171], v201 offset:47104\n"
    "v_mfma_f32_16x16x32_bf16 a[136:139], v[180:183], v[152:155], a[136:139]\n"
    "s_waitcnt lgkmcnt(3)\n"
    "ds_read_b128 v[172:175], v201 offset:48128\n"
    "v_mfma_f32_16x16x32_bf16 a[152:155], v[184:187], v[136:139], 0\n"
    "v_exp_f32 v42, v42\n"
    "v_mfma_f32_16x16x32_bf16 a[152:155], v[188:191], v[152:155], a[152:155]\n"
    "s_waitcnt lgkmcnt(2)\n"
    "v_exp_f32 v43, v43\n"
    "v_mfma_f32_16x16x32_bf16 a[168:171], v[160:163], v[136:139], 0\n"
    "v_exp_f32 v56, v56\n"
    "v_mfma_f32_16x16x32_bf16 a[168:171], v[164:167], v[152:155], a[168:171]\n"
    "s_waitcnt lgkmcnt(0)\n"
    "v_exp_f32 v57, v57\n"
    "v_mfma_f32_16x16x32_bf16 a[184:187], v[168:171], v[136:139], 0\n"
    "v_exp_f32 v58, v58\n"
    "v_exp_f32 v59, v59\n"
    "v_mfma_f32_16x16x32_bf16 a[184:187], v[172:175], v[152:155], a[184:187]\n"
    "v_add_f32 v121, v121, v40\n"
    "v_add_f32 v122, v122, v41\n"
    "v_add_f32 v121, v121, v42\n"
    "v_add_f32 v122, v122, v43\n"
    "v_add_f32 v121, v121, v56\n"
    "v_add_f32 v122, v122, v57\n"
    "s_waitcnt vmcnt(12)\n"
    "ds_read_b128 v[8:11], v158 offset:24576\n"
    "ds_read_b128 v[12:15], v159 offset:24576\n"
    "ds_read_b128 v[16:19], v158 offset:24704\n"
    "ds_read_b128 v[20:23], v159 offset:24704\n"
    "ds_read_b128 v[24:27], v158 offset:24832\n"
    "ds_read_b128 v[28:31], v159 offset:24832\n"
    "s_waitcnt lgkmcnt(0)\n"
    "ds_read_b128 v[160:163], v255\n"
    "ds_read_b128 v[164:167], v255 offset:1024\n"
    "ds_read_b128 v[168:171], v255 offset:2048\n"
    "ds_read_b128 v[172:175], v255 offset:3072\n"
    "ds_read_b128 v[176:179], v255 offset:4096\n"
    "ds_read_b128 v[180:183], v255 offset:5120\n"
    "s_waitcnt lgkmcnt(4)\n"
    "v_mfma_f32_16x16x32_bf16 v[64:67], v[160:163], v[8:11], 0\n"
    "ds_read_b128 v[184:187], v255 offset:6144\n"
    "v_mfma_f32_16x16x32_bf16 v[64:67], v[164:167], v[12:15], v[64:67]\n"
    "s_waitcnt lgkmcnt(3)\n"
    "s_add_u32 m0, s71, 0x6000\n"
    "ds_read_b128 v[188:191], v255 offset:7168\n"
    "v_mfma_f32_16x16x32_bf16 v[64:67], v[168:171], v[16:19], v[64:67]\n"
    "ds_read_b128 v[160:163], v255 offset:8192\n"
    "buffer_load_dwordx4 v200, s[36:39], s60 offen sc1 lds\n"
    "v_mfma_f32_16x16x32_bf16 v[64:67], v[172:175], v[20:23], v[64:67]\n"
    "s_waitcnt lgkmcnt(3)\n"
    "ds_read_b128 v[164:167], v255 offset:9216\n"
    "v_mfma_f32_16x16x32_bf16 v[64:67], v[176:179], v[24:27], v[64:67]\n"
    "ds_read_b128 v[168:171], v255 offset:10240\n"
    "v_mfma_f32_16x16x32_bf16 v[64:67], v[180:183], v[28:31], v[64:67]\n"
    "s_waitcnt lgkmcnt(3)\n"
    "ds_read_b128 v[172:175], v255 offset:11264\n"
    "buffer_load_dwordx4 v200, s[36:39], s60 offen offset:1024 sc1 lds\n"
    "v_mfma_f32_16x16x32_bf16 v[80:83], v[184:187], v[8:11], 0\n"
    "ds_read_b128 v[176:179], v200\n"
    "v_mfma_f32_16x16x32_bf16 v[80:83], v[188:191], v[12:15], v[80:83]\n"
    "s_waitcnt lgkmcnt(3)\n"
    "ds_read_b128 v[180:183], v200 offset:1024\n"
    "v_mfma_f32_16x16x32_bf16 v[80:83], v[160:163], v[16:19], v[80:83]\n"
    "ds_read_b128 v[184:187], v200 offset:2048\n"
    "buffer_load_dwordx4 v200, s[36:39], s60 offen offset:2048 sc1 lds\n"
    "v_mfma_f32_16x16x32_bf16 v[80:83], v[164:167], v[20:23], v[80:83]\n"
    "s_waitcnt lgkmcnt(3)\n"
    "ds_read_b128 v[188:191], v200 offset:3072\n"
    "v_mfma_f32_16x16x32_bf16 v[80:83], v[168:171], v[24:27], v[80:83]\n"
    "ds_read_b128 v[160:163], v200 offset:4096\n"
    "v_mfma_f32_16x16x32_bf16 v[80:83], v[172:175], v[28:31], v[80:83]\n"
    "s_waitcnt lgkmcnt(3)\n"
    "ds_read_b128 v[164:167], v200 offset:5120\n"
    "buffer_load_dwordx4 v200, s[36:39], s60 offen offset:3072 sc1 lds\n"
    "v_mfma_f32_16x16x32_bf16 v[76:79], v[176:179], v[8:11], 0\n"
    "s_add_u32 s65, s60, 0x1000\n"
    "ds_read_b128 v[168:171], v200 offset:6144\n"
    "v_mfma_f32_16x16x32_bf16 v[76:79], v[180:183], v[12:15], v[76:79]\n"
    "s_waitcnt lgkmcnt(3)\n"
    "s_add_u32 m0, s71, 0x7000\n"
    "ds_read_b128 v[172:175], v200 offset:7168\n"
    "v_mfma_f32_16x16x32_bf16 v[76:79], v[184:187], v[16:19], v[76:79]\n"
    "ds_read_b128 v[176:179], v200 offset:8192\n"
    "buffer_load_dwordx4 v200, s[36:39], s65 offen sc1 lds\n"
    "v_mfma_f32_16x16x32_bf16 v[76:79], v[188:191], v[20:23], v[76:79]\n"
    "s_waitcnt lgkmcnt(3)\n"
    "ds_read_b128 v[180:183], v200 offset:9216\n"
    "v_mfma_f32_16x16x32_bf16 v[76:79], v[160:163], v[24:27], v[76:79]\n"
    "ds_read_b128 v[184:187], v200 offset:10240\n"
    "v_mfma_f32_16x16x32_bf16 v[76:79], v[164:167], v[28:31], v[76:79]\n"
    "s_waitcnt lgkmcnt(3)\n"
    "ds_read_b128 v[188:191], v200 offset:11264\n"
    "buffer_load_dwordx4 v200, s[36:39], s65 offen offset:1024 sc1 lds\n"
    "v_max3_f32 v196, v64, v65, v66\n"
    "v_mfma_f32_16x16x32_bf16 v[92:95], v[168:171], v[8:11], 0\n"
    "ds_read_b128 v[160:163], v200 offset:12288\n"
    "v_max3_f32 v196, v196, v67, v80\n"
    "v_max3_f32 v196, v196, v81, v82\n"
    "v_max_f32 v196, v196, v83\n"
    "v_mfma_f32_16x16x32_bf16 v[92:95], v[172:175], v[12:15], v[92:95]\n"
    "s_waitcnt lgkmcnt(3)\n"
    "ds_read_b128 v[164:167], v200 offset:13312\n"
    "v_mov_b32 v197, v196\n"
    "s_nop 1\n"
    "v_permlane32_swap_b32 v196, v197\n"
    "v_max_f32 v196, v196, v197\n"
    "v_mfma_f32_16x16x32_bf16 v[92:95], v[176:179], v[16:19], v[92:95]\n"
    "ds_read_b128 v[168:171], v200 offset:14336\n"
    "v_mov_b32 v197, v196\n"
    "s_nop 1\n"
    "v_permlane16_swap_b32 v196, v197\n"
    "v_max_f32 v196, v196, v197\n"
    "v_mfma_f32_16x16x32_bf16 v[92:95], v[180:183], v[20:23], v[92:95]\n"
    "s_waitcnt lgkmcnt(3)\n"
    "ds_read_b128 v[172:175], v200 offset:15360\n"
    "v_add_f32 v196, 0x42640000, v196\n"
    "v_sub_f32 v108, 0, v196\n"
    "v_sub_f32 v109, 0, v196\n"
    "v_mfma_f32_16x16x32_bf16 v[92:95], v[184:187], v[24:27], v[92:95]\n"
    "ds_read_b128 v[176:179], v200 offset:16384\n"
    "v_sub_f32 v110, 0, v196\n"
    "v_sub_f32 v111, 0, v196\n"
    "v_add_f32 v64, v64, v108\n"
    "v_mfma_f32_16x16x32_bf16 v[92:95], v[188:191], v[28:31], v[92:95]\n"
    "s_waitcnt lgkmcnt(3)\n"
    "ds_read_b128 v[180:183], v200 offset:17408\n"
    "v_add_f32 v65, v65, v108\n"
    "v_add_f32 v66, v66, v108\n"
    "v_add_f32 v67, v67, v108\n"
    "v_mfma_f32_16x16x32_bf16 v[44:47], v[160:163], v[8:11], v[108:111]\n"
    "ds_read_b128 v[184:187], v200 offset:18432\n"
    "v_add_f32 v80, v80, v108\n"
    "v_add_f32 v81, v81, v108\n"
    "v_add_f32 v82, v82, v108\n"
    "v_mfma_f32_16x16x32_bf16 v[44:47], v[164:167], v[12:15], v[44:47]\n"
    "s_waitcnt lgkmcnt(3)\n"
    "ds_read_b128 v[188:191], v200 offset:19456\n"
    "v_add_f32 v83, v83, v108\n"
    "v_exp_f32 v64, v64\n"
    "v_mfma_f32_16x16x32_bf16 v[44:47], v[168:171], v[16:19], v[44:47]\n"
    "ds_read_b128 v[160:163], v200 offset:20480\n"
    "buffer_load_dwordx2 v[218:219], v213, s[44:47], 0 offen sc1\n"
    "v_mfma_f32_16x16x32_bf16 v[44:47], v[172:175], v[20:23], v[44:47]\n"
    "s_waitcnt lgkmcnt(3)\n"
    "ds_read_b128 v[164:167], v200 offset:21504\n"
    "v_exp_f32 v65, v65\n"
    "v_mfma_f32_16x16x32_bf16 v[44:47], v[176:179], v[24:27], v[44:47]\n"
    "ds_read_b128 v[168:171], v200 offset:22528\n"
    "v_exp_f32 v66, v66\n"
    "v_mfma_f32_16x16x32_bf16 v[44:47], v[180:183], v[28:31], v[44:47]\n"
    "s_waitcnt lgkmcnt(3)\n"
    "ds_read_b128 v[172:175], v200 offset:23552\n"
    "v_exp_f32 v67, v67\n"
    "v_mfma_f32_16x16x32_bf16 v[60:63], v[184:187], v[8:11], v[108:111]\n"
    "ds_read_b128 v[176:179], v201 offset:24576\n"
    "v_exp_f32 v80, v80\n"
    "v_mfma_f32_16x16x32_bf16 v[60:63], v[188:191], v[12:15], v[60:63]\n"
    "s_waitcnt lgkmcnt(3)\n"
    "ds_read_b128 v[180:183], v201 offset:25600\n"
    "v_exp_f32 v81, v81\n"
    "v_mfma_f32_16x16x32_bf16 v[60:63], v[160:163], v[16:19], v[60:63]\n"
    "ds_read_b128 v[184:187], v201 offset:26624\n"
    "v_exp_f32 v82, v82\n"
    "v_mfma_f32_16x16x32_bf16 v[60:63], v[164:167], v[20:23], v[60:63]\n"
    "s_waitcnt lgkmcnt(3)\n"
    "ds_read_b128 v[188:191], v201 offset:27648\n"
    "v_exp_f32 v83, v83\n"
    "v_cvt_pk_bf16_f32 v140, v64, v65\n"
    "v_mfma_f32_16x16x32_bf16 v[60:63], v[168:171], v[24:27], v[60:63]\n"
    "ds_read_b128 v[160:163], v201 offset:28672\n"
    "v_cvt_pk_bf16_f32 v141, v66, v67\n"
    "v_cvt_pk_bf16_f32 v142, v80, v81\n"
    "v_cvt_pk_bf16_f32 v143, v82, v83\n"
    "v_add_f32 v76, v76, v108\n"
    "v_add_f32 v77, v77, v108\n"
    "v_add_f32 v78, v78, v108\n"
    "v_add_f32 v79, v79, v108\n"
    "v_add_f32 v92, v92, v108\n"
    "v_add_f32 v93, v93, v108\n"
    "v_add_f32 v94, v94, v108\n"
    "v_add_f32 v95, v95, v108\n"
    "v_exp_f32 v76, v76\n"
    "v_exp_f32 v77, v77\n"
    "v_exp_f32 v78, v78\n"
    "v_exp_f32 v79, v79\n"
    "v_exp_f32 v92, v92\n"
    "v_exp_f32 v93, v93\n"
    "v_exp_f32 v94, v94\n"
    "v_exp_f32 v95, v95\n"
    "v_cvt_pk_bf16_f32 v156, v76, v77\n"
    "v_cvt_pk_bf16_f32 v157, v78, v79\n"
    "v_cvt_pk_bf16_f32 v158, v92, v93\n"
    "v_cvt_pk_bf16_f32 v159, v94, v95\n"
    "v_mfma_f32_16x16x32_bf16 v[60:63], v[172:175], v[28:31], v[60:63]\n"
    "s_waitcnt lgkmcnt(3)\n"
    "ds_read_b128 v[164:167], v201 offset:29696\n"
    "v_exp_f32 v44, v44\n"
    "v_mfma_f32_16x16x32_bf16 a[12:15], v[176:179], v[140:143], 0\n"
    "ds_read_b128 v[168:171], v201 offset:30720\n"
    "v_mfma_f32_16x16x32_bf16 a[12:15], v[180:183], v[156:159], a[12:15]\n"
    "s_waitcnt lgkmcnt(3)\n"
    "ds_read_b128 v[172:175], v201 offset:31744\n"
    "v_mfma_f32_16x16x32_bf16 a[28:31], v[184:187], v[140:143], 0\n"
    "ds_read_b128 v[176:179], v201 offset:32768\n"
    "v_mfma_f32_16x16x32_bf16 a[28:31], v[188:191], v[156:159], a[28:31]\n"
    "s_waitcnt lgkmcnt(3)\n"
    "ds_read_b128 v[180:183], v201 offset:33792\n"
    "v_mfma_f32_16x16x32_bf16 a[44:47], v[160:163], v[140:143], 0\n"
    "ds_read_b128 v[184:187], v201 offset:34816\n"
    "v_mfma_f32_16x16x32_bf16 a[44:47], v[164:167], v[156:159], a[44:47]\n"
    "ds_read_b128 v[188:191], v201 offset:35840\n"
    "v_mfma_f32_16x16x32_bf16 v[124:127], v[192:195], v[140:143], 0\n"
    "v_exp_f32 v45, v45\n"
    "v_mfma_f32_16x16x32_bf16 v[124:127], v[192:195], v[156:159], v[124:127]\n"
    "s_waitcnt lgkmcnt(4)\n"
    "v_exp_f32 v46, v46\n"
    "v_mfma_f32_16x16x32_bf16 a[60:63], v[168:171], v[140:143], 0\n"
    "ds_read_b128 v[160:163], v201 offset:36864\n"
    "v_mfma_f32_16x16x32_bf16 a[60:63], v[172:175], v[156:159], a[60:63]\n"
    "s_waitcnt lgkmcnt(3)\n"
    "ds_read_b128 v[164:167], v201 offset:37888\n"
    "v_mfma_f32_16x16x32_bf16 a[76:79], v[176:179], v[140:143], 0\n"
    "ds_read_b128 v[168:171], v201 offset:38912\n"
    "v_mfma_f32_16x16x32_bf16 a[76:79], v[180:183], v[156:159], a[76:79]\n"
    "s_waitcnt lgkmcnt(3)\n"
    "ds_read_b128 v[172:175], v201 offset:39936\n"
    "v_mfma_f32_16x16x32_bf16 a[92:95], v[184:187], v[140:143], 0\n"
    "ds_read_b128 v[176:179], v201 offset:40960\n"
    "v_mfma_f32_16x16x32_bf16 a[92:95], v[188:191], v[156:159], a[92:95]\n"
    "s_waitcnt lgkmcnt(3)\n"
    "ds_read_b128 v[180:183], v201 offset:41984\n"
    "v_mfma_f32_16x16x32_bf16 a[108:111], v[160:163], v[140:143], 0\n"
    "ds_read_b128 v[184:187], v201 offset:43008\n"
    "v_mfma_f32_16x16x32_bf16 a[108:111], v[164:167], v[156:159], a[108:111]\n"
    "s_waitcnt lgkmcnt(3)\n"
    "ds_read_b128 v[188:191], v201 offset:44032\n"
    "v_mfma_f32_16x16x32_bf16 a[124:127], v[168:171], v[140:143], 0\n"
    "ds_read_b128 v[160:163], v201 offset:45056\n"
    "v_mfma_f32_16x16x32_bf16 a[124:127], v[172:175], v[156:159], a[124:127]\n"
    "s_waitcnt lgkmcnt(3)\n"
    "ds_read_b128 v[164:167], v201 offset:46080\n"
    "v_mfma_f32_16x16x32_bf16 a[140:143], v[176:179], v[140:143], 0\n"
    "ds_read_b128 v[168:171], v201 offset:47104\n"
    "v_mfma_f32_16x16x32_bf16 a[140:143], v[180:183], v[156:159], a[140:143]\n"
    "s_waitcnt lgkmcnt(3)\n"
    "ds_read_b128 v[172:175], v201 offset:48128\n"
    "v_mfma_f32_16x16x32_bf16 a[156:159], v[184:187], v[140:143], 0\n"
    "v_exp_f32 v47, v47\n"
    "v_mfma_f32_16x16x32_bf16 a[156:159], v[188:191], v[156:159], a[156:159]\n"
    "s_waitcnt lgkmcnt(2)\n"
    "v_exp_f32 v60, v60\n"
    "v_mfma_f32_16x16x32_bf16 a[172:175], v[160:163], v[140:143], 0\n"
    "v_exp_f32 v61, v61\n"
    "v_mfma_f32_16x16x32_bf16 a[172:175], v[164:167], v[156:159], a[172:175]\n"
    "s_waitcnt lgkmcnt(0)\n"
    "v_exp_f32 v62, v62\n"
    "v_mfma_f32_16x16x32_bf16 a[188:191], v[168:171], v[140:143], 0\n"
    "v_exp_f32 v63, v63\n"
    "v_mfma_f32_16x16x32_bf16 a[188:191], v[172:175], v[156:159], a[188:191]\n"
    "v_add_f32 v125, v125, v44\n"
    "v_add_f32 v126, v126, v45\n"
    "v_add_f32 v125, v125, v46\n"
    "v_add_f32 v126, v126, v47\n"
    "s_add_u32 s64, s64, 0x3c000\n"
    "s_add_u32 s62, s62, 0x6000\n"
    "s_add_u32 s63, s63, 0x6000\n"
    "s_add_u32 s60, s60, 0x6000\n"
    "s_add_u32 s61, s61, 0x6000\n"
    "v_add_u32 v209, 0x300, v209\n"
    "v_add_u32 v210, 0xc0, v210\n"
    "v_mov_b32 v220, 5\n"
    "s_waitcnt vmcnt(0)\n"
    "v_cmp_ne_u32 s[80:81], s67, v219\n"
    "v_cmp_gt_u32 vcc, s66, v218\n"
    "s_or_b64 vcc, vcc, s[80:81]\n"
    "s_cbranch_vccnz LSPRE2\n"
    "LSPB2:\n"
    "s_barrier\n"
    "s_cmp_eq_u32 s58, 0\n"
    "s_cbranch_scc1 LENT0\n"
    "LENT1:\n"
    "s_mov_b32 s59, 1\n"
    "s_add_u32 m0, s72, 0x21000\n"
    "s_nop 0\n"
    "buffer_load_dwordx4 v250, s[48:51], 0 offen offset:2048 lds\n"
    "s_add_u32 m0, s72, 0x18000\n"
    "s_nop 0\n"
    "buffer_load_dwordx4 v202, s[32:35], s64 offen lds\n"
    "buffer_load_dwordx4 v203, s[32:35], s64 offen offset:1024 lds\n"
    "buffer_load_dwordx4 v204, s[32:35], s64 offen offset:2048 lds\n"
    "s_add_u32 s64, s64, 0xc000\n"
    "s_mov_b32 s94, 0x3000\n"
    "s_mov_b32 s95, 0x6000\n"
    "s_add_u32 s51, s72, 0x18000\n"
    "v_add_u32 v205, 0x3000, v205\n"
    "v_add_u32 v206, 0x3000, v206\n"
    "v_add_u32 v207, 0x3000, v207\n"
    "ds_read_u16_d16_hi v223, v209 offset:6336\n"
    "ds_read_u16_d16_hi v222, v209 offset:192\n"
    "ds_read_b128 v[160:163], v200 offset:24576\n"
    "v_add_f32 v121, v121, v58\n"
    "ds_read_b128 v[232:235], v205 offset:24576\n"
    "v_add_f32 v122, v122, v59\n"
    "ds_read_b128 v[164:167], v200 offset:25600\n"
    "ds_read_b128 v[236:239], v206 offset:24576\n"
    "v_add_f32 v125, v125, v60\n"
    "v_add_f32 v126, v126, v61\n"
    "v_add_f32 v125, v125, v62\n"
    "v_add_f32 v126, v126, v63\n"
    "ds_read_b64 v[228:229], v210 offset:64\n"
    "ds_read2st64_b32 v[240:241], v207 offset0:96 offset1:99\n"
    "ds_read_b128 v[168:171], v200 offset:26624\n"
    "ds_read2st64_b32 v[242:243], v207 offset0:102 offset1:105\n"
    "ds_read2st64_b32 v[244:245], v207 offset0:108 offset1:111\n"
    "ds_read2st64_b32 v[246:247], v207 offset0:114 offset1:117\n"
    "v_cvt_pk_bf16_f32 v140, v44, v45\n"
    "v_cvt_pk_bf16_f32 v141, v46, v47\n"
    "s_waitcnt lgkmcnt(6)\n"
    "v_mul_f32 v226, s75, v223\n"
    "ds_read_b128 v[172:175], v200 offset:27648\n"
    "v_fma_f32 v227, -v222, v226, v217\n"
    "v_cvt_f32_i32 v232, v232\n"
    "v_cvt_f32_i32 v233, v233\n"
    "v_cvt_f32_i32 v234, v234\n"
    "v_cvt_f32_i32 v235, v235\n"
    "s_nop 7\n"
    ".p2align 6\n"
    "LLOOP1:\n"
    "v_mfma_f32_16x16x32_bf16 v[64:67], v[160:163], a[192:195], v[96:99]\n"
    "ds_read_b128 v[176:179], v200 offset:28672\n"
    "v_cvt_f32_i32 v236, v236\n"
    "v_mfma_f32_16x16x32_bf16 v[64:67], v[164:167], a[196:199], v[64:67]\n"
    "s_waitcnt lgkmcnt(1)\n"
    "v_cvt_f32_i32 v237, v237\n"
    "v_cvt_f32_i32 v238, v238\n"
    "v_mfma_f32_16x16x32_bf16 v[64:67], v[168:171], a[200:203], v[64:67]\n"
    "v_cvt_f32_i32 v239, v239\n"
    "v_mul_f32_dpp v232, v226, v232 row_newbcast:0 row_mask:0xf bank_mask:0xf\n"
    "v_mfma_f32_16x16x32_bf16 v[68:71], v[168:171], a[224:227], v[100:103]\n"
    "v_mul_f32_dpp v233, v226, v233 row_newbcast:1 row_mask:0xf bank_mask:0xf\n"
    "v_mul_f32_dpp v234, v226, v234 row_newbcast:2 row_mask:0xf bank_mask:0xf\n"
    "v_mfma_f32_16x16x32_bf16 v[68:71], v[164:167], a[220:223], v[68:71]\n"
    "ds_read_b128 v[180:183], v200 offset:29696\n"
    "v_mul_f32_dpp v235, v226, v235 row_newbcast:3 row_mask:0xf bank_mask:0xf\n"
    "v_mfma_f32_16x16x32_bf16 v[68:71], v[160:163], a[216:219], v[68:71]\n"
    "v_mul_f32_dpp v236, v226, v236 row_newbcast:4 row_mask:0xf bank_mask:0xf\n"
    "v_mul_f32_dpp v237, v226, v237 row_newbcast:5 row_mask:0xf bank_mask:0xf\n"
    "v_mfma_f32_16x16x32_bf16 v[72:75], v[160:163], a[240:243], v[104:107]\n"
    "v_mul_f32_dpp v238, v226, v238 row_newbcast:6 row_mask:0xf bank_mask:0xf\n"
    "v_mul_f32_dpp v239, v226, v239 row_newbcast:7 row_mask:0xf bank_mask:0xf\n"
    "v_mfma_f32_16x16x32_bf16 v[72:75], v[164:167], a[244:247], v[72:75]\n"
    "v_add_f32_dpp v232, v227, v232 row_newbcast:0 row_mask:0xf bank_mask:0xf\n"
    "v_add_f32_dpp v233, v227, v233 row_newbcast:1 row_mask:0xf bank_mask:0xf\n"
    "v_mfma_f32_16x16x32_bf16 v[72:75], v[168:171], a[248:251], v[72:75]\n"
    "v_add_f32_dpp v234, v227, v234 row_newbcast:2 row_mask:0xf bank_mask:0xf\n"
    "v_add_f32_dpp v235, v227, v235 row_newbcast:3 row_mask:0xf bank_mask:0xf\n"
    "v_mfma_f32_16x16x32_bf16 v[76:79], v[168:171], v[16:19], v[108:111]\n"
    "v_add_f32_dpp v236, v227, v236 row_newbcast:4 row_mask:0xf bank_mask:0xf\n"
    "v_add_f32_dpp v237, v227, v237 row_newbcast:5 row_mask:0xf bank_mask:0xf\n"
    "v_mfma_f32_16x16x32_bf16 v[76:79], v[164:167], v[12:15], v[76:79]\n"
    "v_add_f32_dpp v238, v227, v238 row_newbcast:6 row_mask:0xf bank_mask:0xf\n"
    "v_add_f32_dpp v239, v227, v239 row_newbcast:7 row_mask:0xf bank_mask:0xf\n"
    "v_mfma_f32_16x16x32_bf16 v[76:79], v[160:163], v[8:11], v[76:79]\n"
    "s_waitcnt lgkmcnt(1)\n"
    "v_cvt_pk_bf16_f32 v232, v232, v233\n"
    "v_cvt_f32_i32 v240, v240\n"
    "v_mfma_f32_16x16x32_bf16 v[76:79], v[172:175], v[20:23], v[76:79]\n"
    "ds_read_b128 v[160:163], v200 offset:33792\n"
    "v_cvt_pk_bf16_f32 v233, v234, v235\n"
    "v_mfma_f32_16x16x32_bf16 v[76:79], v[176:179], v[24:27], v[76:79]\n"
    "s_waitcnt lgkmcnt(1)\n"
    "v_cvt_f32_i32 v241, v241\n"
    "v_cvt_pk_bf16_f32 v234, v236, v237\n"
    "v_mfma_f32_16x16x32_bf16 v[76:79], v[180:183], v[28:31], v[76:79]\n"
    "v_cvt_f32_i32 v242, v242\n"
    "v_cvt_pk_bf16_f32 v235, v238, v239\n"
    "v_mfma_f32_16x16x32_bf16 v[72:75], v[180:183], v[4:7], v[72:75]\n"
    "ds_read_b128 v[164:167], v200 offset:34816\n"
    "v_cvt_f32_i32 v243, v243\n"
    "v_mfma_f32_16x16x32_bf16 v[72:75], v[176:179], v[0:3], v[72:75]\n"
    "v_cvt_f32_i32 v244, v244\n"
    "v_cvt_f32_i32 v245, v245\n"
    "v_mfma_f32_16x16x32_bf16 v[72:75], v[172:175], a[252:255], v[72:75]\n"
    "buffer_store_dwordx4 v[232:235], v211, s[36:39], s62 offen sc1\n"
    "v_cvt_f32_i32 v246, v246\n"
    "v_cvt_f32_i32 v247, v247\n"
    "v_mul_f32_dpp v240, v228, v240 row_newbcast:0 row_mask:0xf bank_mask:0xf\n"
    "v_mfma_f32_16x16x32_bf16 v[68:71], v[172:175], a[228:231], v[68:71]\n"
    "ds_read_b128 v[168:171], v200 offset:35840\n"
    "v_mul_f32_dpp v241, v228, v241 row_newbcast:1 row_mask:0xf bank_mask:0xf\n"
    "v_mfma_f32_16x16x32_bf16 v[68:71], v[176:179], a[232:235], v[68:71]\n"
    "v_mul_f32_dpp v242, v228, v242 row_newbcast:2 row_mask:0xf bank_mask:0xf\n"
    "v_mul_f32_dpp v243, v228, v243 row_newbcast:3 row_mask:0xf bank_mask:0xf\n"
    "v_mfma_f32_16x16x32_bf16 v[68:71], v[180:183], a[236:239], v[68:71]\n"
    "v_mul_f32_dpp v244, v228, v244 row_newbcast:4 row_mask:0xf bank_mask:0xf\n"
    "v_mul_f32_dpp v245, v228, v245 row_newbcast:5 row_mask:0xf bank_mask:0xf\n"
    "v_mfma_f32_16x16x32_bf16 v[64:67], v[180:183], a[212:215], v[64:67]\n"
    "v_mul_f32_dpp v246, v228, v246 row_newbcast:6 row_mask:0xf bank_mask:0xf\n"
    "v_mul_f32_dpp v247, v228, v247 row_newbcast:7 row_mask:0xf bank_mask:0xf\n"
    "v_mfma_f32_16x16x32_bf16 v[64:67], v[176:179], a[208:211], v[64:67]\n"
    "v_add_f32_dpp v240, v229, v240 row_newbcast:0 row_mask:0xf bank_mask:0xf\n"
    "v_add_f32_dpp v241, v229, v241 row_newbcast:1 row_mask:0xf bank_mask:0xf\n"
    "v_mfma_f32_16x16x32_bf16 v[64:67], v[172:175], a[204:207], v[64:67]\n"
    "s_waitcnt lgkmcnt(1)\n"
    "v_add_f32_dpp v242, v229, v242 row_newbcast:2 row_mask:0xf bank_mask:0xf\n"
    "v_add_f32_dpp v243, v229, v243 row_newbcast:3 row_mask:0xf bank_mask:0xf\n"
    "v_mfma_f32_16x16x32_bf16 v[80:83], v[160:163], a[204:207], v[96:99]\n"
    "ds_read_b128 v[172:175], v200 offset:30720\n"
    "v_add_f32_dpp v244, v229, v244 row_newbcast:4 row_mask:0xf bank_mask:0xf\n"
    "v_mfma_f32_16x16x32_bf16 v[80:83], v[164:167], a[208:211], v[80:83]\n"
    "s_waitcnt lgkmcnt(1)\n"
    "v_add_f32_dpp v245, v229, v245 row_newbcast:5 row_mask:0xf bank_mask:0xf\n"
    "v_add_f32_dpp v246, v229, v246 row_newbcast:6 row_mask:0xf bank_mask:0xf\n"
    "v_mfma_f32_16x16x32_bf16 v[80:83], v[168:171], a[212:215], v[80:83]\n"
    "v_add_f32_dpp v247, v229, v247 row_newbcast:7 row_mask:0xf bank_mask:0xf\n"
    "v_cvt_pk_bf16_f32 v240, v240, v241\n"
    "v_mfma_f32_16x16x32_bf16 v[84:87], v[168:171], a[236:239], v[100:103]\n"
    "ds_read_b128 v[176:179], v200 offset:31744\n"
    "v_cvt_pk_bf16_f32 v241, v242, v243\n"
    "v_mfma_f32_16x16x32_bf16 v[84:87], v[164:167], a[232:235], v[84:87]\n"
    "v_cvt_pk_bf16_f32 v242, v244, v245\n"
    "v_cvt_pk_bf16_f32 v243, v246, v247\n"
    "v_mfma_f32_16x16x32_bf16 v[84:87], v[160:163], a[228:231], v[84:87]\n"
    "buffer_store_dwordx4 v[240:243], v212, s[40:43], s63 offen sc1\n"
    "v_cvt_pk_bf16_f32 v136, v40, v41\n"
    "v_cvt_pk_bf16_f32 v137, v42, v43\n"
    "v_cvt_pk_bf16_f32 v132, v36, v37\n"
    "v_mfma_f32_16x16x32_bf16 v[88:91], v[160:163], a[252:255], v[104:107]\n"
    "ds_read_b128 v[180:183], v200 offset:32768\n"
    "v_cvt_pk_bf16_f32 v133, v38, v39\n"
    "v_mfma_f32_16x16x32_bf16 v[88:91], v[164:167], v[0:3], v[88:91]\n"
    "v_cvt_pk_bf16_f32 v128, v32, v33\n"
    "v_cvt_pk_bf16_f32 v129, v34, v35\n"
    "v_mfma_f32_16x16x32_bf16 v[88:91], v[168:171], v[4:7], v[88:91]\n"
    "v_exp_f32 v64, v64\n"
    "v_mfma_f32_16x16x32_bf16 v[92:95], v[168:171], v[28:31], v[108:111]\n"
    "v_exp_f32 v65, v65\n"
    "v_mfma_f32_16x16x32_bf16 v[92:95], v[164:167], v[24:27], v[92:95]\n"
    "v_exp_f32 v66, v66\n"
    "v_mfma_f32_16x16x32_bf16 v[92:95], v[160:163], v[20:23], v[92:95]\n"
    "s_waitcnt lgkmcnt(1)\n"
    "v_exp_f32 v67, v67\n"
    "v_mfma_f32_16x16x32_bf16 v[92:95], v[172:175], v[8:11], v[92:95]\n"
    "ds_read_b128 v[160:163], v200 offset:36864\n"
    "v_cvt_pk_bf16_f32 v142, v60, v61\n"
    "v_mfma_f32_16x16x32_bf16 v[92:95], v[176:179], v[12:15], v[92:95]\n"
    "s_waitcnt lgkmcnt(1)\n"
    "s_add_u32 s65, s60, s92\n"
    "v_cvt_pk_bf16_f32 v143, v62, v63\n"
    "v_mfma_f32_16x16x32_bf16 v[92:95], v[180:183], v[16:19], v[92:95]\n"
    "s_add_u32 m0, s90, 0\n"
    "v_exp_f32 v68, v68\n"
    "v_mfma_f32_16x16x32_bf16 v[88:91], v[180:183], a[248:251], v[88:91]\n"
    "ds_read_b128 v[164:167], v200 offset:37888\n"
    "v_cvt_pk_bf16_f32 v138, v56, v57\n"
    "v_mfma_f32_16x16x32_bf16 v[88:91], v[176:179], a[244:247], v[88:91]\n"
    "buffer_load_dwordx4 v200, s[36:39], s65 offen sc1 lds\n"
    "v_mfma_f32_16x16x32_bf16 v[88:91], v[172:175], a[240:243], v[88:91]\n"
    "v_exp_f32 v69, v69\n"
    "v_mfma_f32_16x16x32_bf16 v[84:87], v[172:175], a[216:219], v[84:87]\n"
    "ds_read_b128 v[168:171], v200 offset:38912\n"
    "v_cvt_pk_bf16_f32 v139, v58, v59\n"
    "v_mfma_f32_16x16x32_bf16 v[84:87], v[176:179], a[220:223], v[84:87]\n"
    "v_exp_f32 v70, v70\n"
    "v_mfma_f32_16x16x32_bf16 v[84:87], v[180:183], a[224:227], v[84:87]\n"
    "buffer_load_dwordx4 v200, s[36:39], s65 offen offset:1024 sc1 lds\n"
    "v_mfma_f32_16x16x32_bf16 v[80:83], v[180:183], a[200:203], v[80:83]\n"
    "v_exp_f32 v71, v71\n"
    "v_mfma_f32_16x16x32_bf16 v[80:83], v[176:179], a[196:199], v[80:83]\n"
    "v_exp_f32 v72, v72\n"
    "v_mfma_f32_16x16x32_bf16 v[80:83], v[172:175], a[192:195], v[80:83]\n"
    "s_waitcnt lgkmcnt(1)\n"
    "v_cvt_pk_bf16_f32 v134, v52, v53\n"
    "v_cvt_pk_bf16_f32 v135, v54, v55\n"
    "v_mfma_f32_16x16x32_bf16 v[32:35], v[160:163], a[192:195], v[96:99]\n"
    "ds_read_b128 v[172:175], v200 offset:39936\n"
    "v_cvt_pk_bf16_f32 v130, v48, v49\n"
    "v_mfma_f32_16x16x32_bf16 v[32:35], v[164:167], a[196:199], v[32:35]\n"
    "s_waitcnt lgkmcnt(1)\n"
    "buffer_load_dwordx4 v200, s[36:39], s65 offen offset:2048 sc1 lds\n"
    "v_mfma_f32_16x16x32_bf16 v[32:35], v[168:171], a[200:203], v[32:35]\n"
    "v_exp_f32 v73, v73\n"
    "v_mfma_f32_16x16x32_bf16 v[36:39], v[168:171], a[224:227], v[100:103]\n"
    "ds_read_b128 v[176:179], v200 offset:40960\n"
    "v_cvt_pk_bf16_f32 v131, v50, v51\n"
    "v_mfma_f32_16x16x32_bf16 v[36:39], v[164:167], a[220:223], v[36:39]\n"
    "v_exp_f32 v74, v74\n"
    "v_mfma_f32_16x16x32_bf16 v[36:39], v[160:163], a[216:219], v[36:39]\n"
    "buffer_load_dwordx4 v200, s[36:39], s65 offen offset:3072 sc1 lds\n"
    "v_mfma_f32_16x16x32_bf16 v[40:43], v[160:163], a[240:243], v[104:107]\n"
    "s_add_u32 s65, s65, 0x1000\n"
    "ds_read_b128 v[180:183], v200 offset:41984\n"
    "v_cvt_pk_bf16_f32 v144, v64, v65\n"
    "v_mfma_f32_16x16x32_bf16 v[40:43], v[164:167], a[244:247], v[40:43]\n"
    "s_add_u32 m0, s90, 0x1000\n"
    "v_exp_f32 v75, v75\n"
    "v_mfma_f32_16x16x32_bf16 v[40:43], v[168:171], a[248:251], v[40:43]\n"
    "v_exp_f32 v76, v76\n"
    "v_mfma_f32_16x16x32_bf16 v[44:47], v[168:171], v[16:19], v[108:111]\n"
    "buffer_load_dwordx4 v200, s[36:39], s65 offen sc1 lds\n"
    "v_mfma_f32_16x16x32_bf16 v[44:47], v[164:167], v[12:15], v[44:47]\n"
    "v_exp_f32 v77, v77\n"
    "v_mfma_f32_16x16x32_bf16 v[44:47], v[160:163], v[8:11], v[44:47]\n"
    "s_waitcnt lgkmcnt(1)\n"
    "v_exp_f32 v78, v78\n"
    "v_mfma_f32_16x16x32_bf16 v[44:47], v[172:175], v[20:23], v[44:47]\n"
    "ds_read_b128 v[160:163], v200 offset:46080\n"
    "v_cvt_pk_bf16_f32 v145, v66, v67\n"
    "v_mfma_f32_16x16x32_bf16 v[44:47], v[176:179], v[24:27], v[44:47]\n"
    "s_waitcnt lgkmcnt(1)\n"
    "buffer_load_dwordx4 v200, s[36:39], s65 offen offset:1024 sc1 lds\n"
    "v_mfma_f32_16x16x32_bf16 v[44:47], v[180:183], v[28:31], v[44:47]\n"
    "v_exp_f32 v79, v79\n"
    "v_mfma_f32_16x16x32_bf16 v[40:43], v[180:183], v[4:7], v[40:43]\n"
    "ds_read_b128 v[164:167], v200 offset:47104\n"
    "v_cvt_pk_bf16_f32 v148, v68, v69\n"
    "v_mfma_f32_16x16x32_bf16 v[40:43], v[176:179], v[0:3], v[40:43]\n"
    "v_exp_f32 v80, v80\n"
    "v_mfma_f32_16x16x32_bf16 v[40:43], v[172:175], a[252:255], v[40:43]\n"
    "buffer_load_dwordx4 v200, s[36:39], s65 offen offset:2048 sc1 lds\n"
    "v_mfma_f32_16x16x32_bf16 v[36:39], v[172:175], a[228:231], v[36:39]\n"
    "ds_read_b128 v[168:171], v200 offset:48128\n"
    "v_cvt_pk_bf16_f32 v149, v70, v71\n"
    "v_mfma_f32_16x16x32_bf16 v[36:39], v[176:179], a[232:235], v[36:39]\n"
    "s_add_u32 m0, s71, 0x12000\n"
    "v_exp_f32 v81, v81\n"
    "v_mfma_f32_16x16x32_bf16 v[36:39], v[180:183], a[236:239], v[36:39]\n"
    "v_exp_f32 v82, v82\n"
    "v_mfma_f32_16x16x32_bf16 v[32:35], v[180:183], a[212:215], v[32:35]\n"
    "buffer_load_dwordx4 v200, s[40:43], s61 offen sc1 lds\n"
    "v_mfma_f32_16x16x32_bf16 v[32:35], v[176:179], a[208:211], v[32:35]\n"
    "v_exp_f32 v83, v83\n"
    "v_mfma_f32_16x16x32_bf16 v[32:35], v[172:175], a[204:207], v[32:35]\n"
    "s_waitcnt lgkmcnt(1)\n"
    "v_exp_f32 v84, v84\n"
    "v_mfma_f32_16x16x32_bf16 v[48:51], v[160:163], a[204:207], v[96:99]\n"
    "ds_read_b128 v[172:175], v200 offset:43008\n"
    "v_cvt_pk_bf16_f32 v152, v72, v73\n"
    "v_mfma_f32_16x16x32_bf16 v[48:51], v[164:167], a[208:211], v[48:51]\n"
    "s_waitcnt lgkmcnt(1)\n"
    "buffer_load_dwordx4 v200, s[40:43], s61 offen offset:1024 sc1 lds\n"
    "v_mfma_f32_16x16x32_bf16 v[48:51], v[168:171], a[212:215], v[48:51]\n"
    "v_exp_f32 v85, v85\n"
    "v_mfma_f32_16x16x32_bf16 v[52:55], v[168:171], a[236:239], v[100:103]\n"
    "ds_read_b128 v[176:179], v200 offset:44032\n"
    "v_cvt_pk_bf16_f32 v153, v74, v75\n"
    "v_mfma_f32_16x16x32_bf16 v[52:55], v[164:167], a[232:235], v[52:55]\n"
    "v_exp_f32 v86, v86\n"
    "v_mfma_f32_16x16x32_bf16 v[52:55], v[160:163], a[228:231], v[52:55]\n"
    "buffer_load_dwordx4 v200, s[40:43], s61 offen offset:2048 sc1 lds\n"
    "v_mfma_f32_16x16x32_bf16 v[56:59], v[160:163], a[252:255], v[104:107]\n"
    "ds_read_b128 v[180:183], v200 offset:45056\n"
    "v_cvt_pk_bf16_f32 v156, v76, v77\n"
    "v_mfma_f32_16x16x32_bf16 v[56:59], v[164:167], v[0:3], v[56:59]\n"
    "v_exp_f32 v87, v87\n"
    "v_mfma_f32_16x16x32_bf16 v[56:59], v[168:171], v[4:7], v[56:59]\n"
    "v_exp_f32 v88, v88\n"
    "v_mfma_f32_16x16x32_bf16 v[60:63], v[168:171], v[28:31], v[108:111]\n"
    "buffer_load_dwordx4 v200, s[40:43], s61 offen offset:3072 sc1 lds\n"
    "v_mfma_f32_16x16x32_bf16 v[60:63], v[164:167], v[24:27], v[60:63]\n"
    "s_add_u32 s65, s61, 0x1000\n"
    "v_exp_f32 v89, v89\n"
    "v_mfma_f32_16x16x32_bf16 v[60:63], v[160:163], v[20:23], v[60:63]\n"
    "s_waitcnt lgkmcnt(1)\n"
    "s_add_u32 m0, s71, 0x13000\n"
    "v_cvt_pk_bf16_f32 v157, v78, v79\n"
    "v_mfma_f32_16x16x32_bf16 v[60:63], v[172:175], v[8:11], v[60:63]\n"
    "ds_read_b128 v[160:163], v201\n"
    "v_cvt_pk_bf16_f32 v146, v80, v81\n"
    "v_mfma_f32_16x16x32_bf16 v[60:63], v[176:179], v[12:15], v[60:63]\n"
    "s_waitcnt lgkmcnt(1)\n"
    "buffer_load_dwordx4 v200, s[40:43], s65 offen sc1 lds\n"
    "v_mfma_f32_16x16x32_bf16 v[60:63], v[180:183], v[16:19], v[60:63]\n"
    "v_exp_f32 v90, v90\n"
    "v_mfma_f32_16x16x32_bf16 v[56:59], v[180:183], a[248:251], v[56:59]\n"
    "ds_read_b128 v[164:167], v201 offset:1024\n"
    "v_cvt_pk_bf16_f32 v147, v82, v83\n"
    "v_mfma_f32_16x16x32_bf16 v[56:59], v[176:179], a[244:247], v[56:59]\n"
    "v_exp_f32 v91, v91\n"
    "v_mfma_f32_16x16x32_bf16 v[56:59], v[172:175], a[240:243], v[56:59]\n"
    "buffer_load_dwordx4 v200, s[40:43], s65 offen offset:1024 sc1 lds\n"
    "v_mfma_f32_16x16x32_bf16 v[52:55], v[172:175], a[216:219], v[52:55]\n"
    "ds_read_b128 v[168:171], v201 offset:2048\n"
    "v_cvt_pk_bf16_f32 v150, v84, v85\n"
    "v_mfma_f32_16x16x32_bf16 v[52:55], v[176:179], a[220:223], v[52:55]\n"
    "v_exp_f32 v92, v92\n"
    "v_mfma_f32_16x16x32_bf16 v[52:55], v[180:183], a[224:227], v[52:55]\n"
    "v_exp_f32 v93, v93\n"
    "v_mfma_f32_16x16x32_bf16 v[48:51], v[180:183], a[200:203], v[48:51]\n"
    "v_cvt_pk_bf16_f32 v151, v86, v87\n"
    "v_cvt_pk_bf16_f32 v154, v88, v89\n"
    "v_mfma_f32_16x16x32_bf16 v[48:51], v[176:179], a[196:199], v[48:51]\n"
    "v_exp_f32 v94, v94\n"
    "v_mfma_f32_16x16x32_bf16 v[48:51], v[172:175], a[192:195], v[48:51]\n"
    "s_waitcnt lgkmcnt(1)\n"
    "v_cvt_pk_bf16_f32 v155, v90, v91\n"
    "v_cvt_pk_bf16_f32 v158, v92, v93\n"
    "v_mfma_f32_16x16x32_bf16 a[0:3], v[160:163], v[128:131], a[0:3]\n"
    "ds_read_b128 v[172:175], v201 offset:3072\n"
    "ds_read_b128 v[176:179], v201 offset:4096\n"
    "v_mfma_f32_16x16x32_bf16 a[0:3], v[164:167], v[144:147], a[0:3]\n"
    "v_exp_f32 v95, v95\n"
    "v_mfma_f32_16x16x32_bf16 a[4:7], v[160:163], v[132:135], a[4:7]\n"
    "v_cvt_pk_bf16_f32 v159, v94, v95\n"
    "v_add_f32 v113, v113, v64\n"
    "v_mfma_f32_16x16x32_bf16 a[4:7], v[164:167], v[148:151], a[4:7]\n"
    "v_exp_f32 v32, v32\n"
    "v_mfma_f32_16x16x32_bf16 a[8:11], v[160:163], v[136:139], a[8:11]\n"
    "ds_read_b128 v[180:183], v201 offset:5120\n"
    "v_add_f32 v114, v114, v65\n"
    "v_mfma_f32_16x16x32_bf16 a[8:11], v[164:167], v[152:155], a[8:11]\n"
    "v_exp_f32 v33, v33\n"
    "v_mfma_f32_16x16x32_bf16 a[12:15], v[160:163], v[140:143], a[12:15]\n"
    "v_exp_f32 v34, v34\n"
    "v_mfma_f32_16x16x32_bf16 a[12:15], v[164:167], v[156:159], a[12:15]\n"
    "s_waitcnt lgkmcnt(2)\n"
    "v_exp_f32 v35, v35\n"
    "v_mfma_f32_16x16x32_bf16 a[28:31], v[168:171], v[140:143], a[28:31]\n"
    "ds_read_b128 v[160:163], v201 offset:6144\n"
    "v_add_f32 v113, v113, v66\n"
    "v_mfma_f32_16x16x32_bf16 a[28:31], v[172:175], v[156:159], a[28:31]\n"
    "v_exp_f32 v36, v36\n"
    "v_mfma_f32_16x16x32_bf16 a[24:27], v[172:175], v[152:155], a[24:27]\n"
    "v_exp_f32 v37, v37\n"
    "v_mfma_f32_16x16x32_bf16 a[24:27], v[168:171], v[136:139], a[24:27]\n"
    "v_exp_f32 v38, v38\n"
    "v_mfma_f32_16x16x32_bf16 a[20:23], v[168:171], v[132:135], a[20:23]\n"
    "ds_read_b128 v[164:167], v201 offset:7168\n"
    "v_add_f32 v114, v114, v67\n"
    "v_mfma_f32_16x16x32_bf16 a[20:23], v[172:175], v[148:151], a[20:23]\n"
    "v_exp_f32 v39, v39\n"
    "v_mfma_f32_16x16x32_bf16 a[16:19], v[172:175], v[144:147], a[16:19]\n"
    "v_exp_f32 v40, v40\n"
    "v_mfma_f32_16x16x32_bf16 a[16:19], v[168:171], v[128:131], a[16:19]\n"
    "s_waitcnt lgkmcnt(2)\n"
    "v_exp_f32 v41, v41\n"
    "v_mfma_f32_16x16x32_bf16 a[32:35], v[176:179], v[128:131], a[32:35]\n"
    "ds_read_b128 v[168:171], v201 offset:8192\n"
    "v_add_f32 v117, v117, v68\n"
    "v_mfma_f32_16x16x32_bf16 a[32:35], v[180:183], v[144:147], a[32:35]\n"
    "v_exp_f32 v42, v42\n"
    "v_mfma_f32_16x16x32_bf16 a[36:39], v[180:183], v[148:151], a[36:39]\n"
    "v_exp_f32 v43, v43\n"
    "v_mfma_f32_16x16x32_bf16 a[36:39], v[176:179], v[132:135], a[36:39]\n"
    "v_exp_f32 v44, v44\n"
    "v_mfma_f32_16x16x32_bf16 a[40:43], v[176:179], v[136:139], a[40:43]\n"
    "ds_read_b128 v[172:175], v201 offset:9216\n"
    "v_add_f32 v118, v118, v69\n"
    "v_mfma_f32_16x16x32_bf16 a[40:43], v[180:183], v[152:155], a[40:43]\n"
    "v_exp_f32 v45, v45\n"
    "v_mfma_f32_16x16x32_bf16 a[44:47], v[180:183], v[156:159], a[44:47]\n"
    "v_exp_f32 v46, v46\n"
    "v_mfma_f32_16x16x32_bf16 a[44:47], v[176:179], v[140:143], a[44:47]\n"
    "s_waitcnt lgkmcnt(2)\n"
    "v_exp_f32 v47, v47\n"
    "v_mfma_f32_16x16x32_bf16 a[60:63], v[160:163], v[140:143], a[60:63]\n"
    "ds_read_b128 v[176:179], v201 offset:10240\n"
    "v_add_f32 v117, v117, v70\n"
    "v_mfma_f32_16x16x32_bf16 a[60:63], v[164:167], v[156:159], a[60:63]\n"
    "v_exp_f32 v48, v48\n"
    "v_mfma_f32_16x16x32_bf16 a[56:59], v[164:167], v[152:155], a[56:59]\n"
    "v_exp_f32 v49, v49\n"
    "v_mfma_f32_16x16x32_bf16 a[56:59], v[160:163], v[136:139], a[56:59]\n"
    "v_exp_f32 v50, v50\n"
    "v_mfma_f32_16x16x32_bf16 a[52:55], v[160:163], v[132:135], a[52:55]\n"
    "ds_read_b128 v[180:183], v201 offset:11264\n"
    "v_add_f32 v113, v113, v32\n"
    "v_mfma_f32_16x16x32_bf16 a[52:55], v[164:167], v[148:151], a[52:55]\n"
    "v_exp_f32 v51, v51\n"
    "v_mfma_f32_16x16x32_bf16 a[48:51], v[164:167], v[144:147], a[48:51]\n"
    "v_exp_f32 v52, v52\n"
    "v_mfma_f32_16x16x32_bf16 a[48:51], v[160:163], v[128:131], a[48:51]\n"
    "s_waitcnt lgkmcnt(2)\n"
    "v_add_f32 v118, v118, v71\n"
    "v_add_f32 v114, v114, v33\n"
    "v_mfma_f32_16x16x32_bf16 a[64:67], v[168:171], v[128:131], a[64:67]\n"
    "ds_read_b128 v[160:163], v201 offset:12288\n"
    "v_add_f32 v121, v121, v72\n"
    "v_mfma_f32_16x16x32_bf16 a[64:67], v[172:175], v[144:147], a[64:67]\n"
    "v_add_f32 v113, v113, v34\n"
    "v_add_f32 v122, v122, v73\n"
    "v_mfma_f32_16x16x32_bf16 a[68:71], v[172:175], v[148:151], a[68:71]\n"
    "v_exp_f32 v53, v53\n"
    "v_mfma_f32_16x16x32_bf16 a[68:71], v[168:171], v[132:135], a[68:71]\n"
    "s_add_u32 m0, s51, s95\n"
    "v_add_f32 v114, v114, v35\n"
    "v_add_f32 v121, v121, v74\n"
    "v_mfma_f32_16x16x32_bf16 a[72:75], v[168:171], v[136:139], a[72:75]\n"
    "ds_read_b128 v[164:167], v201 offset:13312\n"
    "v_add_f32 v117, v117, v36\n"
    "v_mfma_f32_16x16x32_bf16 a[72:75], v[172:175], v[152:155], a[72:75]\n"
    "buffer_load_dwordx4 v202, s[32:35], s64 offen lds\n"
    "v_mfma_f32_16x16x32_bf16 a[76:79], v[172:175], v[156:159], a[76:79]\n"
    "v_exp_f32 v54, v54\n"
    "v_mfma_f32_16x16x32_bf16 a[76:79], v[168:171], v[140:143], a[76:79]\n"
    "s_waitcnt lgkmcnt(2)\n"
    "v_add_f32 v122, v122, v75\n"
    "v_add_f32 v118, v118, v37\n"
    "v_mfma_f32_16x16x32_bf16 a[92:95], v[176:179], v[140:143], a[92:95]\n"
    "ds_read_b128 v[168:171], v201 offset:14336\n"
    "v_add_f32 v125, v125, v76\n"
    "v_mfma_f32_16x16x32_bf16 a[92:95], v[180:183], v[156:159], a[92:95]\n"
    "buffer_load_dwordx4 v203, s[32:35], s64 offen offset:1024 lds\n"
    "v_mfma_f32_16x16x32_bf16 a[88:91], v[180:183], v[152:155], a[88:91]\n"
    "v_add_f32 v117, v117, v38\n"
    "v_add_f32 v126, v126, v77\n"
    "v_mfma_f32_16x16x32_bf16 a[88:91], v[176:179], v[136:139], a[88:91]\n"
    "v_exp_f32 v55, v55\n"
    "v_mfma_f32_16x16x32_bf16 a[84:87], v[176:179], v[132:135], a[84:87]\n"
    "ds_read_b128 v[172:175], v201 offset:15360\n"
    "v_add_f32 v118, v118, v39\n"
    "v_mfma_f32_16x16x32_bf16 a[84:87], v[180:183], v[148:151], a[84:87]\n"
    "buffer_load_dwordx4 v204, s[32:35], s64 offen offset:2048 lds\n"
    "v_mfma_f32_16x16x32_bf16 a[80:83], v[180:183], v[144:147], a[80:83]\n"
    "v_add_f32 v125, v125, v78\n"
    "v_add_f32 v121, v121, v40\n"
    "v_mfma_f32_16x16x32_bf16 a[80:83], v[176:179], v[128:131], a[80:83]\n"
    "s_waitcnt lgkmcnt(2)\n"
    "v_exp_f32 v56, v56\n"
    "v_mfma_f32_16x16x32_bf16 a[96:99], v[160:163], v[128:131], a[96:99]\n"
    "s_mov_b32 s95, s94\n"
    "ds_read_b128 v[176:179], v201 offset:16384\n"
    "v_add_f32 v126, v126, v79\n"
    "v_mfma_f32_16x16x32_bf16 a[96:99], v[164:167], v[144:147], a[96:99]\n"
    "s_sub_u32 s94, s94, 0x3000\n"
    "v_add_f32 v122, v122, v41\n"
    "v_add_f32 v113, v113, v80\n"
    "v_mfma_f32_16x16x32_bf16 a[100:103], v[164:167], v[148:151], a[100:103]\n"
    "s_min_u32 s94, s94, 0x6000\n"
    "v_add_f32 v121, v121, v42\n"
    "v_add_f32 v114, v114, v81\n"
    "v_mfma_f32_16x16x32_bf16 a[100:103], v[160:163], v[132:135], a[100:103]\n"
    "s_sub_u32 s93, s94, s95\n"
    "v_exp_f32 v57, v57\n"
    "v_mfma_f32_16x16x32_bf16 a[104:107], v[160:163], v[136:139], a[104:107]\n"
    "ds_read_b128 v[180:183], v201 offset:17408\n"
    "v_add_f32 v122, v122, v43\n"
    "v_mfma_f32_16x16x32_bf16 a[104:107], v[164:167], v[152:155], a[104:107]\n"
    "v_add_f32 v113, v113, v82\n"
    "v_add_f32 v125, v125, v44\n"
    "v_mfma_f32_16x16x32_bf16 a[108:111], v[164:167], v[156:159], a[108:111]\n"
    "v_exp_f32 v58, v58\n"
    "v_mfma_f32_16x16x32_bf16 a[108:111], v[160:163], v[140:143], a[108:111]\n"
    "s_waitcnt lgkmcnt(2)\n"
    "v_add_f32 v114, v114, v83\n"
    "v_add_f32 v126, v126, v45\n"
    "v_mfma_f32_16x16x32_bf16 a[124:127], v[168:171], v[140:143], a[124:127]\n"
    "ds_read_b128 v[160:163], v201 offset:18432\n"
    "v_add_f32 v117, v117, v84\n"
    "v_mfma_f32_16x16x32_bf16 a[124:127], v[172:175], v[156:159], a[124:127]\n"
    "v_add_f32 v125, v125, v46\n"
    "v_add_f32 v118, v118, v85\n"
    "v_mfma_f32_16x16x32_bf16 a[120:123], v[172:175], v[152:155], a[120:123]\n"
    "v_exp_f32 v59, v59\n"
    "v_mfma_f32_16x16x32_bf16 a[120:123], v[168:171], v[136:139], a[120:123]\n"
    "v_add_f32 v126, v126, v47\n"
    "v_add_f32 v117, v117, v86\n"
    "v_mfma_f32_16x16x32_bf16 a[116:119], v[168:171], v[132:135], a[116:119]\n"
    "ds_read_b128 v[164:167], v201 offset:19456\n"
    "v_add_f32 v113, v113, v48\n"
    "v_mfma_f32_16x16x32_bf16 a[116:119], v[172:175], v[148:151], a[116:119]\n"
    "v_exp_f32 v60, v60\n"
    "v_mfma_f32_16x16x32_bf16 a[112:115], v[172:175], v[144:147], a[112:115]\n"
    "v_add_u32 v205, s93, v205\n"
    "v_add_f32 v118, v118, v87\n"
    "v_mfma_f32_16x16x32_bf16 a[112:115], v[168:171], v[128:131], a[112:115]\n"
    "s_waitcnt lgkmcnt(2)\n"
    "v_add_f32 v114, v114, v49\n"
    "v_add_u32 v209, 0x180, v209\n"
    "v_mfma_f32_16x16x32_bf16 a[128:131], v[176:179], v[128:131], a[128:131]\n"
    "ds_read_b128 v[168:171], v201 offset:20480\n"
    "v_add_u32 v206, s93, v206\n"
    "v_mfma_f32_16x16x32_bf16 a[128:131], v[180:183], v[144:147], a[128:131]\n"
    "v_add_f32 v121, v121, v88\n"
    "v_add_f32 v113, v113, v50\n"
    "v_mfma_f32_16x16x32_bf16 a[132:135], v[180:183], v[148:151], a[132:135]\n"
    "s_add_u32 s64, s64, 0xc000\n"
    "v_exp_f32 v61, v61\n"
    "v_mfma_f32_16x16x32_bf16 a[132:135], v[176:179], v[132:135], a[132:135]\n"
    "s_add_u32 s62, s62, 0x6000\n"
    "v_add_u32 v210, 0x80, v210\n"
    "v_add_u32 v207, s93, v207\n"
    "v_mfma_f32_16x16x32_bf16 a[136:139], v[176:179], v[136:139], a[136:139]\n"
    "s_add_u32 s63, s63, 0x6000\n"
    "ds_read_b128 v[172:175], v201 offset:21504\n"
    "v_add_f32 v122, v122, v89\n"
    "v_mfma_f32_16x16x32_bf16 a[136:139], v[180:183], v[152:155], a[136:139]\n"
    "s_add_u32 s60, s60, 0x6000\n"
    "v_add_f32 v114, v114, v51\n"
    "v_add_f32 v121, v121, v90\n"
    "v_mfma_f32_16x16x32_bf16 a[140:143], v[180:183], v[156:159], a[140:143]\n"
    "s_add_u32 s61, s61, 0x6000\n"
    "v_add_f32 v117, v117, v52\n"
    "v_add_f32 v122, v122, v91\n"
    "v_mfma_f32_16x16x32_bf16 a[140:143], v[176:179], v[140:143], a[140:143]\n"
    "s_waitcnt lgkmcnt(2)\n"
    "v_exp_f32 v62, v62\n"
    "v_mfma_f32_16x16x32_bf16 a[156:159], v[160:163], v[140:143], a[156:159]\n"
    "ds_read_b128 v[176:179], v201 offset:22528\n"
    "v_add_f32 v118, v118, v53\n"
    "v_mfma_f32_16x16x32_bf16 a[156:159], v[164:167], v[156:159], a[156:159]\n"
    "s_waitcnt vmcnt(3)\n"
    "ds_read_b128 v[180:183], v201 offset:23552\n"
    "v_add_f32 v125, v125, v92\n"
    "v_mfma_f32_16x16x32_bf16 a[152:155], v[164:167], v[152:155], a[152:155]\n"
    "v_add_f32 v117, v117, v54\n"
    "v_add_f32 v126, v126, v93\n"
    "v_mfma_f32_16x16x32_bf16 a[152:155], v[160:163], v[136:139], a[152:155]\n"
    "v_exp_f32 v63, v63\n"
    "v_mfma_f32_16x16x32_bf16 a[148:151], v[160:163], v[132:135], a[148:151]\n"
    "v_add_f32 v118, v118, v55\n"
    "v_add_f32 v125, v125, v94\n"
    "v_mfma_f32_16x16x32_bf16 a[148:151], v[164:167], v[148:151], a[148:151]\n"
    "v_add_f32 v121, v121, v56\n"
    "v_add_f32 v126, v126, v95\n"
    "v_mfma_f32_16x16x32_bf16 a[144:147], v[164:167], v[144:147], a[144:147]\n"
    "v_add_f32 v122, v122, v57\n"
    "v_mfma_f32_16x16x32_bf16 a[144:147], v[160:163], v[128:131], a[144:147]\n"
    "s_waitcnt lgkmcnt(0)\n"
    "v_mfma_f32_16x16x32_bf16 a[160:163], v[168:171], v[128:131], a[160:163]\n"
    "s_barrier\n"
    "v_mfma_f32_16x16x32_bf16 a[160:163], v[172:175], v[144:147], a[160:163]\n"
    "ds_read_u16_d16_hi v223, v209 offset:6144\n"
    "ds_read_u16_d16_hi v222, v209\n"
    "v_mfma_f32_16x16x32_bf16 a[164:167], v[172:175], v[148:151], a[164:167]\n"
    "ds_read_b128 v[160:163], v200\n"
    "v_add_f32 v121, v121, v58\n"
    "v_mfma_f32_16x16x32_bf16 a[164:167], v[168:171], v[132:135], a[164:167]\n"
    "ds_read_b128 v[232:235], v205 offset:24576\n"
    "v_add_f32 v122, v122, v59\n"
    "v_mfma_f32_16x16x32_bf16 a[168:171], v[168:171], v[136:139], a[168:171]\n"
    "ds_read_b128 v[164:167], v200 offset:1024\n"
    "ds_read_b128 v[236:239], v206 offset:24576\n"
    "v_mfma_f32_16x16x32_bf16 a[168:171], v[172:175], v[152:155], a[168:171]\n"
    "v_add_f32 v125, v125, v60\n"
    "v_add_f32 v126, v126, v61\n"
    "v_mfma_f32_16x16x32_bf16 a[172:175], v[172:175], v[156:159], a[172:175]\n"
    "v_add_f32 v125, v125, v62\n"
    "v_add_f32 v126, v126, v63\n"
    "v_mfma_f32_16x16x32_bf16 a[172:175], v[168:171], v[140:143], a[172:175]\n"
    "ds_read_b64 v[228:229], v210\n"
    "ds_read2st64_b32 v[240:241], v207 offset0:96 offset1:99\n"
    "v_mfma_f32_16x16x32_bf16 a[188:191], v[176:179], v[140:143], a[188:191]\n"
    "ds_read_b128 v[168:171], v200 offset:2048\n"
    "ds_read2st64_b32 v[242:243], v207 offset0:102 offset1:105\n"
    "v_mfma_f32_16x16x32_bf16 a[188:191], v[180:183], v[156:159], a[188:191]\n"
    "ds_read2st64_b32 v[244:245], v207 offset0:108 offset1:111\n"
    "ds_read2st64_b32 v[246:247], v207 offset0:114 offset1:117\n"
    "v_mfma_f32_16x16x32_bf16 a[184:187], v[176:179], v[136:139], a[184:187]\n"
    "v_cvt_pk_bf16_f32 v140, v44, v45\n"
    "v_cvt_pk_bf16_f32 v141, v46, v47\n"
    "v_mfma_f32_16x16x32_bf16 a[184:187], v[180:183], v[152:155], a[184:187]\n"
    "s_waitcnt lgkmcnt(6)\n"
    "v_mul_f32 v226, s75, v223\n"
    "v_mfma_f32_16x16x32_bf16 a[180:183], v[176:179], v[132:135], a[180:183]\n"
    "ds_read_b128 v[172:175], v200 offset:3072\n"
    "v_fma_f32 v227, -v222, v226, v217\n"
    "v_mfma_f32_16x16x32_bf16 a[180:183], v[180:183], v[148:151], a[180:183]\n"
    "v_cvt_f32_i32 v232, v232\n"
    "v_cvt_f32_i32 v233, v233\n"
    "v_mfma_f32_16x16x32_bf16 a[176:179], v[176:179], v[128:131], a[176:179]\n"
    "v_cvt_f32_i32 v234, v234\n"
    "v_cvt_f32_i32 v235, v235\n"
    "v_mfma_f32_16x16x32_bf16 a[176:179], v[180:183], v[144:147], a[176:179]\n"
    "v_cvt_f32_i32 v236, v236\n"
    "v_cvt_f32_i32 v237, v237\n"
    "LM01:\n"
    "v_mfma_f32_16x16x32_bf16 v[64:67], v[160:163], a[192:195], v[96:99]\n"
    "ds_read_b128 v[176:179], v200 offset:4096\n"
    "v_cvt_f32_i32 v238, v238\n"
    "v_mfma_f32_16x16x32_bf16 v[64:67], v[164:167], a[196:199], v[64:67]\n"
    "s_waitcnt lgkmcnt(1)\n"
    "v_cvt_f32_i32 v239, v239\n"
    "v_mul_f32_dpp v232, v226, v232 row_newbcast:0 row_mask:0xf bank_mask:0xf\n"
    "v_mfma_f32_16x16x32_bf16 v[64:67], v[168:171], a[200:203], v[64:67]\n"
    "v_mul_f32_dpp v233, v226, v233 row_newbcast:1 row_mask:0xf bank_mask:0xf\n"
    "v_mul_f32_dpp v234, v226, v234 row_newbcast:2 row_mask:0xf bank_mask:0xf\n"
    "v_mfma_f32_16x16x32_bf16 v[68:71], v[168:171], a[224:227], v[100:103]\n"
    "v_mul_f32_dpp v235, v226, v235 row_newbcast:3 row_mask:0xf bank_mask:0xf\n"
    "v_mul_f32_dpp v236, v226, v236 row_newbcast:4 row_mask:0xf bank_mask:0xf\n"
    "v_mfma_f32_16x16x32_bf16 v[68:71], v[164:167], a[220:223], v[68:71]\n"
    "ds_read_b128 v[180:183], v200 offset:5120\n"
    "v_mul_f32_dpp v237, v226, v237 row_newbcast:5 row_mask:0xf bank_mask:0xf\n"
    "v_mfma_f32_16x16x32_bf16 v[68:71], v[160:163], a[216:219], v[68:71]\n"
    "v_mul_f32_dpp v238, v226, v238 row_newbcast:6 row_mask:0xf bank_mask:0xf\n"
    "v_mul_f32_dpp v239, v226, v239 row_newbcast:7 row_mask:0xf bank_mask:0xf\n"
    "v_mfma_f32_16x16x32_bf16 v[72:75], v[160:163], a[240:243], v[104:107]\n"
    "v_add_f32_dpp v232, v227, v232 row_newbcast:0 row_mask:0xf bank_mask:0xf\n"
    "v_add_f32_dpp v233, v227, v233 row_newbcast:1 row_mask:0xf bank_mask:0xf\n"
    "v_mfma_f32_16x16x32_bf16 v[72:75], v[164:167], a[244:247], v[72:75]\n"
    "v_add_f32_dpp v234, v227, v234 row_newbcast:2 row_mask:0xf bank_mask:0xf\n"
    "v_add_f32_dpp v235, v227, v235 row_newbcast:3 row_mask:0xf bank_mask:0xf\n"
    "v_mfma_f32_16x16x32_bf16 v[72:75], v[168:171], a[248:251], v[72:75]\n"
    "v_add_f32_dpp v236, v227, v236 row_newbcast:4 row_mask:0xf bank_mask:0xf\n"
    "v_add_f32_dpp v237, v227, v237 row_newbcast:5 row_mask:0xf bank_mask:0xf\n"
    "v_mfma_f32_16x16x32_bf16 v[76:79], v[168:171], v[16:19], v[108:111]\n"
    "v_add_f32_dpp v238, v227, v238 row_newbcast:6 row_mask:0xf bank_mask:0xf\n"
    "v_add_f32_dpp v239, v227, v239 row_newbcast:7 row_mask:0xf bank_mask:0xf\n"
    "v_mfma_f32_16x16x32_bf16 v[76:79], v[164:167], v[12:15], v[76:79]\n"
    "v_cvt_pk_bf16_f32 v232, v232, v233\n"
    "v_cvt_f32_i32 v240, v240\n"
    "v_mfma_f32_16x16x32_bf16 v[76:79], v[160:163], v[8:11], v[76:79]\n"
    "s_waitcnt lgkmcnt(1)\n"
    "v_cvt_pk_bf16_f32 v233, v234, v235\n"
    "v_cvt_f32_i32 v241, v241\n"
    "v_mfma_f32_16x16x32_bf16 v[76:79], v[172:175], v[20:23], v[76:79]\n"
    "ds_read_b128 v[160:163], v200 offset:9216\n"
    "v_cvt_pk_bf16_f32 v234, v236, v237\n"
    "v_mfma_f32_16x16x32_bf16 v[76:79], v[176:179], v[24:27], v[76:79]\n"
    "s_waitcnt lgkmcnt(1)\n"
    "v_cvt_f32_i32 v242, v242\n"
    "v_cvt_pk_bf16_f32 v235, v238, v239\n"
    "v_mfma_f32_16x16x32_bf16 v[76:79], v[180:183], v[28:31], v[76:79]\n"
    "v_cvt_f32_i32 v243, v243\n"
    "v_cvt_f32_i32 v244, v244\n"
    "v_mfma_f32_16x16x32_bf16 v[72:75], v[180:183], v[4:7], v[72:75]\n"
    "ds_read_b128 v[164:167], v200 offset:10240\n"
    "v_cvt_f32_i32 v245, v245\n"
    "v_mfma_f32_16x16x32_bf16 v[72:75], v[176:179], v[0:3], v[72:75]\n"
    "buffer_store_dwordx4 v[232:235], v211, s[36:39], s62 offen sc1\n"
    "v_cvt_f32_i32 v246, v246\n"
    "v_cvt_f32_i32 v247, v247\n"
    "v_mul_f32_dpp v240, v228, v240 row_newbcast:0 row_mask:0xf bank_mask:0xf\n"
    "v_mfma_f32_16x16x32_bf16 v[72:75], v[172:175], a[252:255], v[72:75]\n"
    "v_mul_f32_dpp v241, v228, v241 row_newbcast:1 row_mask:0xf bank_mask:0xf\n"
    "v_mul_f32_dpp v242, v228, v242 row_newbcast:2 row_mask:0xf bank_mask:0xf\n"
    "v_mfma_f32_16x16x32_bf16 v[68:71], v[172:175], a[228:231], v[68:71]\n"
    "ds_read_b128 v[168:171], v200 offset:11264\n"
    "v_mul_f32_dpp v243, v228, v243 row_newbcast:3 row_mask:0xf bank_mask:0xf\n"
    "v_mfma_f32_16x16x32_bf16 v[68:71], v[176:179], a[232:235], v[68:71]\n"
    "v_mul_f32_dpp v244, v228, v244 row_newbcast:4 row_mask:0xf bank_mask:0xf\n"
    "v_mul_f32_dpp v245, v228, v245 row_newbcast:5 row_mask:0xf bank_mask:0xf\n"
    "v_mfma_f32_16x16x32_bf16 v[68:71], v[180:183], a[236:239], v[68:71]\n"
    "v_mul_f32_dpp v246, v228, v246 row_newbcast:6 row_mask:0xf bank_mask:0xf\n"
    "v_mul_f32_dpp v247, v228, v247 row_newbcast:7 row_mask:0xf bank_mask:0xf\n"
    "v_mfma_f32_16x16x32_bf16 v[64:67], v[180:183], a[212:215], v[64:67]\n"
    "v_add_f32_dpp v240, v229, v240 row_newbcast:0 row_mask:0xf bank_mask:0xf\n"
    "v_add_f32_dpp v241, v229, v241 row_newbcast:1 row_mask:0xf bank_mask:0xf\n"
    "v_mfma_f32_16x16x32_bf16 v[64:67], v[176:179], a[208:211], v[64:67]\n"
    "v_add_f32_dpp v242, v229, v242 row_newbcast:2 row_mask:0xf bank_mask:0xf\n"
    "v_add_f32_dpp v243, v229, v243 row_newbcast:3 row_mask:0xf bank_mask:0xf\n"
    "v_mfma_f32_16x16x32_bf16 v[64:67], v[172:175], a[204:207], v[64:67]\n"
    "s_waitcnt lgkmcnt(1)\n"
    "v_add_f32_dpp v244, v229, v244 row_newbcast:4 row_mask:0xf bank_mask:0xf\n"
    "v_add_f32_dpp v245, v229, v245 row_newbcast:5 row_mask:0xf bank_mask:0xf\n"
    "v_mfma_f32_16x16x32_bf16 v[80:83], v[160:163], a[204:207], v[96:99]\n"
    "ds_read_b128 v[172:175], v200 offset:6144\n"
    "v_add_f32_dpp v246, v229, v246 row_newbcast:6 row_mask:0xf bank_mask:0xf\n"
    "v_mfma_f32_16x16x32_bf16 v[80:83], v[164:167], a[208:211], v[80:83]\n"
    "s_waitcnt lgkmcnt(1)\n"
    "v_add_f32_dpp v247, v229, v247 row_newbcast:7 row_mask:0xf bank_mask:0xf\n"
    "v_cvt_pk_bf16_f32 v240, v240, v241\n"
    "v_mfma_f32_16x16x32_bf16 v[80:83], v[168:171], a[212:215], v[80:83]\n"
    "v_cvt_pk_bf16_f32 v241, v242, v243\n"
    "v_cvt_pk_bf16_f32 v242, v244, v245\n"
    "v_mfma_f32_16x16x32_bf16 v[84:87], v[168:171], a[236:239], v[100:103]\n"
    "ds_read_b128 v[176:179], v200 offset:7168\n"
    "v_cvt_pk_bf16_f32 v243, v246, v247\n"
    "v_mfma_f32_16x16x32_bf16 v[84:87], v[164:167], a[232:235], v[84:87]\n"
    "buffer_store_dwordx4 v[240:243], v212, s[40:43], s63 offen sc1\n"
    "v_cvt_pk_bf16_f32 v136, v40, v41\n"
    "v_cvt_pk_bf16_f32 v137, v42, v43\n"
    "v_cvt_pk_bf16_f32 v132, v36, v37\n"
    "v_mfma_f32_16x16x32_bf16 v[84:87], v[160:163], a[228:231], v[84:87]\n"
    "v_cvt_pk_bf16_f32 v133, v38, v39\n"
    "v_cvt_pk_bf16_f32 v128, v32, v33\n"
    "v_mfma_f32_16x16x32_bf16 v[88:91], v[160:163], a[252:255], v[104:107]\n"
    "ds_read_b128 v[180:183], v200 offset:8192\n"
    "v_cvt_pk_bf16_f32 v129, v34, v35\n"
    "v_mfma_f32_16x16x32_bf16 v[88:91], v[164:167], v[0:3], v[88:91]\n"
    "v_exp_f32 v64, v64\n"
    "v_mfma_f32_16x16x32_bf16 v[88:91], v[168:171], v[4:7], v[88:91]\n"
    "v_exp_f32 v65, v65\n"
    "v_mfma_f32_16x16x32_bf16 v[92:95], v[168:171], v[28:31], v[108:111]\n"
    "v_exp_f32 v66, v66\n"
    "v_mfma_f32_16x16x32_bf16 v[92:95], v[164:167], v[24:27], v[92:95]\n"
    "v_exp_f32 v67, v67\n"
    "v_mfma_f32_16x16x32_bf16 v[92:95], v[160:163], v[20:23], v[92:95]\n"
    "s_waitcnt lgkmcnt(1)\n"
    "v_exp_f32 v68, v68\n"
    "v_mfma_f32_16x16x32_bf16 v[92:95], v[172:175], v[8:11], v[92:95]\n"
    "ds_read_b128 v[160:163], v200 offset:12288\n"
    "v_cvt_pk_bf16_f32 v142, v60, v61\n"
    "v_mfma_f32_16x16x32_bf16 v[92:95], v[176:179], v[12:15], v[92:95]\n"
    "s_waitcnt lgkmcnt(1)\n"
    "s_add_u32 s65, s60, s92\n"
    "v_cvt_pk_bf16_f32 v143, v62, v63\n"
    "v_mfma_f32_16x16x32_bf16 v[92:95], v[180:183], v[16:19], v[92:95]\n"
    "s_add_u32 m0, s90, 0x6000\n"
    "v_exp_f32 v69, v69\n"
    "v_mfma_f32_16x16x32_bf16 v[88:91], v[180:183], a[248:251], v[88:91]\n"
    "ds_read_b128 v[164:167], v200 offset:13312\n"
    "v_cvt_pk_bf16_f32 v138, v56, v57\n"
    "v_mfma_f32_16x16x32_bf16 v[88:91], v[176:179], a[244:247], v[88:91]\n"
    "buffer_load_dwordx4 v200, s[36:39], s65 offen sc1 lds\n"
    "v_mfma_f32_16x16x32_bf16 v[88:91], v[172:175], a[240:243], v[88:91]\n"
    "v_exp_f32 v70, v70\n"
    "v_mfma_f32_16x16x32_bf16 v[84:87], v[172:175], a[216:219], v[84:87]\n"
    "ds_read_b128 v[168:171], v200 offset:14336\n"
    "v_cvt_pk_bf16_f32 v139, v58, v59\n"
    "v_mfma_f32_16x16x32_bf16 v[84:87], v[176:179], a[220:223], v[84:87]\n"
    "v_exp_f32 v71, v71\n"
    "v_mfma_f32_16x16x32_bf16 v[84:87], v[180:183], a[224:227], v[84:87]\n"
    "buffer_load_dwordx4 v200, s[36:39], s65 offen offset:1024 sc1 lds\n"
    "v_mfma_f32_16x16x32_bf16 v[80:83], v[180:183], a[200:203], v[80:83]\n"
    "v_exp_f32 v72, v72\n"
    "v_mfma_f32_16x16x32_bf16 v[80:83], v[176:179], a[196:199], v[80:83]\n"
    "v_cvt_pk_bf16_f32 v134, v52, v53\n"
    "v_cvt_pk_bf16_f32 v135, v54, v55\n"
    "v_mfma_f32_16x16x32_bf16 v[80:83], v[172:175], a[192:195], v[80:83]\n"
    "s_waitcnt lgkmcnt(1)\n"
    "v_exp_f32 v73, v73\n"
    "v_mfma_f32_16x16x32_bf16 v[32:35], v[160:163], a[192:195], v[96:99]\n"
    "ds_read_b128 v[172:175], v200 offset:15360\n"
    "v_cvt_pk_bf16_f32 v130, v48, v49\n"
    "v_mfma_f32_16x16x32_bf16 v[32:35], v[164:167], a[196:199], v[32:35]\n"
    "s_waitcnt lgkmcnt(1)\n"
    "buffer_load_dwordx4 v200, s[36:39], s65 offen offset:2048 sc1 lds\n"
    "v_mfma_f32_16x16x32_bf16 v[32:35], v[168:171], a[200:203], v[32:35]\n"
    "v_cvt_pk_bf16_f32 v131, v50, v51\n"
    "v_cvt_pk_bf16_f32 v144, v64, v65\n"
    "v_mfma_f32_16x16x32_bf16 v[36:39], v[168:171], a[224:227], v[100:103]\n"
    "ds_read_b128 v[176:179], v200 offset:16384\n"
    "v_cvt_pk_bf16_f32 v145, v66, v67\n"
    "v_mfma_f32_16x16x32_bf16 v[36:39], v[164:167], a[220:223], v[36:39]\n"
    "v_exp_f32 v74, v74\n"
    "v_mfma_f32_16x16x32_bf16 v[36:39], v[160:163], a[216:219], v[36:39]\n"
    "buffer_load_dwordx4 v200, s[36:39], s65 offen offset:3072 sc1 lds\n"
    "v_mfma_f32_16x16x32_bf16 v[40:43], v[160:163], a[240:243], v[104:107]\n"
    "s_add_u32 s65, s65, 0x1000\n"
    "ds_read_b128 v[180:183], v200 offset:17408\n"
    "v_cvt_pk_bf16_f32 v148, v68, v69\n"
    "v_mfma_f32_16x16x32_bf16 v[40:43], v[164:167], a[244:247], v[40:43]\n"
    "s_add_u32 m0, s90, 0x7000\n"
    "v_exp_f32 v75, v75\n"
    "v_mfma_f32_16x16x32_bf16 v[40:43], v[168:171], a[248:251], v[40:43]\n"
    "v_exp_f32 v76, v76\n"
    "v_mfma_f32_16x16x32_bf16 v[44:47], v[168:171], v[16:19], v[108:111]\n"
    "buffer_load_dwordx4 v200, s[36:39], s65 offen sc1 lds\n"
    "v_mfma_f32_16x16x32_bf16 v[44:47], v[164:167], v[12:15], v[44:47]\n"
    "v_exp_f32 v77, v77\n"
    "v_mfma_f32_16x16x32_bf16 v[44:47], v[160:163], v[8:11], v[44:47]\n"
    "s_waitcnt lgkmcnt(1)\n"
    "v_exp_f32 v78, v78\n"
    "v_mfma_f32_16x16x32_bf16 v[44:47], v[172:175], v[20:23], v[44:47]\n"
    "ds_read_b128 v[160:163], v200 offset:21504\n"
    "v_cvt_pk_bf16_f32 v149, v70, v71\n"
    "v_mfma_f32_16x16x32_bf16 v[44:47], v[176:179], v[24:27], v[44:47]\n"
    "s_waitcnt lgkmcnt(1)\n"
    "buffer_load_dwordx4 v200, s[36:39], s65 offen offset:1024 sc1 lds\n"
    "v_mfma_f32_16x16x32_bf16 v[44:47], v[180:183], v[28:31], v[44:47]\n"
    "v_exp_f32 v79, v79\n"
    "v_mfma_f32_16x16x32_bf16 v[40:43], v[180:183], v[4:7], v[40:43]\n"
    "ds_read_b128 v[164:167], v200 offset:22528\n"
    "v_cvt_pk_bf16_f32 v152, v72, v73\n"
    "v_mfma_f32_16x16x32_bf16 v[40:43], v[176:179], v[0:3], v[40:43]\n"
    "v_exp_f32 v80, v80\n"
    "v_mfma_f32_16x16x32_bf16 v[40:43], v[172:175], a[252:255], v[40:43]\n"
    "buffer_load_dwordx4 v200, s[36:39], s65 offen offset:2048 sc1 lds\n"
    "v_mfma_f32_16x16x32_bf16 v[36:39], v[172:175], a[228:231], v[36:39]\n"
    "ds_read_b128 v[168:171], v200 offset:23552\n"
    "v_cvt_pk_bf16_f32 v153, v74, v75\n"
    "v_mfma_f32_16x16x32_bf16 v[36:39], v[176:179], a[232:235], v[36:39]\n"
    "s_add_u32 m0, s71, 0xc000\n"
    "v_exp_f32 v81, v81\n"
    "v_mfma_f32_16x16x32_bf16 v[36:39], v[180:183], a[236:239], v[36:39]\n"
    "v_exp_f32 v82, v82\n"
    "v_mfma_f32_16x16x32_bf16 v[32:35], v[180:183], a[212:215], v[32:35]\n"
    "buffer_load_dwordx4 v200, s[40:43], s61 offen sc1 lds\n"
    "v_mfma_f32_16x16x32_bf16 v[32:35], v[176:179], a[208:211], v[32:35]\n"
    "v_exp_f32 v83, v83\n"
    "v_mfma_f32_16x16x32_bf16 v[32:35], v[172:175], a[204:207], v[32:35]\n"
    "s_waitcnt lgkmcnt(1)\n"
    "v_exp_f32 v84, v84\n"
    "v_mfma_f32_16x16x32_bf16 v[48:51], v[160:163], a[204:207], v[96:99]\n"
    "ds_read_b128 v[172:175], v200 offset:18432\n"
    "v_cvt_pk_bf16_f32 v156, v76, v77\n"
    "v_mfma_f32_16x16x32_bf16 v[48:51], v[164:167], a[208:211], v[48:51]\n"
    "s_waitcnt lgkmcnt(1)\n"
    "buffer_load_dwordx4 v200, s[40:43], s61 offen offset:1024 sc1 lds\n"
    "v_mfma_f32_16x16x32_bf16 v[48:51], v[168:171], a[212:215], v[48:51]\n"
    "v_exp_f32 v85, v85\n"
    "v_mfma_f32_16x16x32_bf16 v[52:55], v[168:171], a[236:239], v[100:103]\n"
    "ds_read_b128 v[176:179], v200 offset:19456\n"
    "v_cvt_pk_bf16_f32 v157, v78, v79\n"
    "v_mfma_f32_16x16x32_bf16 v[52:55], v[164:167], a[232:235], v[52:55]\n"
    "v_exp_f32 v86, v86\n"
    "v_mfma_f32_16x16x32_bf16 v[52:55], v[160:163], a[228:231], v[52:55]\n"
    "buffer_load_dwordx4 v200, s[40:43], s61 offen offset:2048 sc1 lds\n"
    "v_mfma_f32_16x16x32_bf16 v[56:59], v[160:163], a[252:255], v[104:107]\n"
    "ds_read_b128 v[180:183], v200 offset:20480\n"
    "v_cvt_pk_bf16_f32 v146, v80, v81\n"
    "v_mfma_f32_16x16x32_bf16 v[56:59], v[164:167], v[0:3], v[56:59]\n"
    "v_exp_f32 v87, v87\n"
    "v_mfma_f32_16x16x32_bf16 v[56:59], v[168:171], v[4:7], v[56:59]\n"
    "v_exp_f32 v88, v88\n"
    "v_mfma_f32_16x16x32_bf16 v[60:63], v[168:171], v[28:31], v[108:111]\n"
    "buffer_load_dwordx4 v200, s[40:43], s61 offen offset:3072 sc1 lds\n"
    "v_mfma_f32_16x16x32_bf16 v[60:63], v[164:167], v[24:27], v[60:63]\n"
    "s_add_u32 s65, s61, 0x1000\n"
    "v_exp_f32 v89, v89\n"
    "v_mfma_f32_16x16x32_bf16 v[60:63], v[160:163], v[20:23], v[60:63]\n"
    "s_waitcnt lgkmcnt(1)\n"
    "s_add_u32 m0, s71, 0xd000\n"
    "v_cvt_pk_bf16_f32 v147, v82, v83\n"
    "v_mfma_f32_16x16x32_bf16 v[60:63], v[172:175], v[8:11], v[60:63]\n"
    "ds_read_b128 v[160:163], v201 offset:24576\n"
    "v_cvt_pk_bf16_f32 v150, v84, v85\n"
    "v_mfma_f32_16x16x32_bf16 v[60:63], v[176:179], v[12:15], v[60:63]\n"
    "s_waitcnt lgkmcnt(1)\n"
    "buffer_load_dwordx4 v200, s[40:43], s65 offen sc1 lds\n"
    "v_mfma_f32_16x16x32_bf16 v[60:63], v[180:183], v[16:19], v[60:63]\n"
    "v_exp_f32 v90, v90\n"
    "v_mfma_f32_16x16x32_bf16 v[56:59], v[180:183], a[248:251], v[56:59]\n"
    "ds_read_b128 v[164:167], v201 offset:25600\n"
    "v_cvt_pk_bf16_f32 v151, v86, v87\n"
    "v_mfma_f32_16x16x32_bf16 v[56:59], v[176:179], a[244:247], v[56:59]\n"
    "v_exp_f32 v91, v91\n"
    "v_mfma_f32_16x16x32_bf16 v[56:59], v[172:175], a[240:243], v[56:59]\n"
    "buffer_load_dwordx4 v200, s[40:43], s65 offen offset:1024 sc1 lds\n"
    "v_mfma_f32_16x16x32_bf16 v[52:55], v[172:175], a[216:219], v[52:55]\n"
    "ds_read_b128 v[168:171], v201 offset:26624\n"
    "v_cvt_pk_bf16_f32 v154, v88, v89\n"
    "v_mfma_f32_16x16x32_bf16 v[52:55], v[176:179], a[220:223], v[52:55]\n"
    "v_exp_f32 v92, v92\n"
    "v_mfma_f32_16x16x32_bf16 v[52:55], v[180:183], a[224:227], v[52:55]\n"
    "v_exp_f32 v93, v93\n"
    "v_mfma_f32_16x16x32_bf16 v[48:51], v[180:183], a[200:203], v[48:51]\n"
    "v_exp_f32 v94, v94\n"
    "v_mfma_f32_16x16x32_bf16 v[48:51], v[176:179], a[196:199], v[48:51]\n"
    "v_cvt_pk_bf16_f32 v155, v90, v91\n"
    "v_cvt_pk_bf16_f32 v158, v92, v93\n"
    "v_mfma_f32_16x16x32_bf16 v[48:51], v[172:175], a[192:195], v[48:51]\n"
    "s_waitcnt lgkmcnt(1)\n"
    "v_exp_f32 v95, v95\n"
    "v_mfma_f32_16x16x32_bf16 a[0:3], v[160:163], v[128:131], a[0:3]\n"
    "ds_read_b128 v[172:175], v201 offset:27648\n"
    "ds_read_b128 v[176:179], v201 offset:28672\n"
    "v_mfma_f32_16x16x32_bf16 a[0:3], v[164:167], v[144:147], a[0:3]\n"
    "v_cvt_pk_bf16_f32 v159, v94, v95\n"
    "v_add_f32 v113, v113, v64\n"
    "v_mfma_f32_16x16x32_bf16 a[4:7], v[160:163], v[132:135], a[4:7]\n"
    "v_exp_f32 v32, v32\n"
    "v_mfma_f32_16x16x32_bf16 a[4:7], v[164:167], v[148:151], a[4:7]\n"
    "v_exp_f32 v33, v33\n"
    "v_mfma_f32_16x16x32_bf16 a[8:11], v[160:163], v[136:139], a[8:11]\n"
    "ds_read_b128 v[180:183], v201 offset:29696\n"
    "v_add_f32 v114, v114, v65\n"
    "v_mfma_f32_16x16x32_bf16 a[8:11], v[164:167], v[152:155], a[8:11]\n"
    "v_exp_f32 v34, v34\n"
    "v_mfma_f32_16x16x32_bf16 a[12:15], v[160:163], v[140:143], a[12:15]\n"
    "v_exp_f32 v35, v35\n"
    "v_mfma_f32_16x16x32_bf16 a[12:15], v[164:167], v[156:159], a[12:15]\n"
    "s_waitcnt lgkmcnt(2)\n"
    "v_exp_f32 v36, v36\n"
    "v_mfma_f32_16x16x32_bf16 a[28:31], v[168:171], v[140:143], a[28:31]\n"
    "ds_read_b128 v[160:163], v201 offset:30720\n"
    "v_add_f32 v113, v113, v66\n"
    "v_mfma_f32_16x16x32_bf16 a[28:31], v[172:175], v[156:159], a[28:31]\n"
    "v_exp_f32 v37, v37\n"
    "v_mfma_f32_16x16x32_bf16 a[24:27], v[172:175], v[152:155], a[24:27]\n"
    "v_exp_f32 v38, v38\n"
    "v_mfma_f32_16x16x32_bf16 a[24:27], v[168:171], v[136:139], a[24:27]\n"
    "v_exp_f32 v39, v39\n"
    "v_mfma_f32_16x16x32_bf16 a[20:23], v[168:171], v[132:135], a[20:23]\n"
    "ds_read_b128 v[164:167], v201 offset:31744\n"
    "v_add_f32 v114, v114, v67\n"
    "v_mfma_f32_16x16x32_bf16 a[20:23], v[172:175], v[148:151], a[20:23]\n"
    "v_exp_f32 v40, v40\n"
    "v_mfma_f32_16x16x32_bf16 a[16:19], v[172:175], v[144:147], a[16:19]\n"
    "v_exp_f32 v41, v41\n"
    "v_mfma_f32_16x16x32_bf16 a[16:19], v[168:171], v[128:131], a[16:19]\n"
    "s_waitcnt lgkmcnt(2)\n"
    "v_exp_f32 v42, v42\n"
    "v_mfma_f32_16x16x32_bf16 a[32:35], v[176:179], v[128:131], a[32:35]\n"
    "ds_read_b128 v[168:171], v201 offset:32768\n"
    "v_add_f32 v117, v117, v68\n"
    "v_mfma_f32_16x16x32_bf16 a[32:35], v[180:183], v[144:147], a[32:35]\n"
    "v_exp_f32 v43, v43\n"
    "v_mfma_f32_16x16x32_bf16 a[36:39], v[180:183], v[148:151], a[36:39]\n"
    "v_exp_f32 v44, v44\n"
    "v_mfma_f32_16x16x32_bf16 a[36:39], v[176:179], v[132:135], a[36:39]\n"
    "v_exp_f32 v45, v45\n"
    "v_mfma_f32_16x16x32_bf16 a[40:43], v[176:179], v[136:139], a[40:43]\n"
    "ds_read_b128 v[172:175], v201 offset:33792\n"
    "v_add_f32 v118, v118, v69\n"
    "v_mfma_f32_16x16x32_bf16 a[40:43], v[180:183], v[152:155], a[40:43]\n"
    "v_exp_f32 v46, v46\n"
    "v_mfma_f32_16x16x32_bf16 a[44:47], v[180:183], v[156:159], a[44:47]\n"
    "v_exp_f32 v47, v47\n"
    "v_mfma_f32_16x16x32_bf16 a[44:47], v[176:179], v[140:143], a[44:47]\n"
    "s_waitcnt lgkmcnt(2)\n"
    "v_exp_f32 v48, v48\n"
    "v_mfma_f32_16x16x32_bf16 a[60:63], v[160:163], v[140:143], a[60:63]\n"
    "ds_read_b128 v[176:179], v201 offset:34816\n"
    "v_add_f32 v117, v117, v70\n"
    "v_mfma_f32_16x16x32_bf16 a[60:63], v[164:167], v[156:159], a[60:63]\n"
    "v_exp_f32 v49, v49\n"
    "v_mfma_f32_16x16x32_bf16 a[56:59], v[164:167], v[152:155], a[56:59]\n"
    "v_exp_f32 v50, v50\n"
    "v_mfma_f32_16x16x32_bf16 a[56:59], v[160:163], v[136:139], a[56:59]\n"
    "v_exp_f32 v51, v51\n"
    "v_mfma_f32_16x16x32_bf16 a[52:55], v[160:163], v[132:135], a[52:55]\n"
    "ds_read_b128 v[180:183], v201 offset:35840\n"
    "v_add_f32 v113, v113, v32\n"
    "v_mfma_f32_16x16x32_bf16 a[52:55], v[164:167], v[148:151], a[52:55]\n"
    "v_exp_f32 v52, v52\n"
    "v_mfma_f32_16x16x32_bf16 a[48:51], v[164:167], v[144:147], a[48:51]\n"
    "v_add_f32 v118, v118, v71\n"
    "v_add_f32 v114, v114, v33\n"
    "v_mfma_f32_16x16x32_bf16 a[48:51], v[160:163], v[128:131], a[48:51]\n"
    "s_waitcnt lgkmcnt(2)\n"
    "v_add_f32 v121, v121, v72\n"
    "v_add_f32 v113, v113, v34\n"
    "v_mfma_f32_16x16x32_bf16 a[64:67], v[168:171], v[128:131], a[64:67]\n"
    "ds_read_b128 v[160:163], v201 offset:36864\n"
    "v_add_f32 v122, v122, v73\n"
    "v_mfma_f32_16x16x32_bf16 a[64:67], v[172:175], v[144:147], a[64:67]\n"
    "v_exp_f32 v53, v53\n"
    "v_mfma_f32_16x16x32_bf16 a[68:71], v[172:175], v[148:151], a[68:71]\n"
    "v_add_f32 v114, v114, v35\n"
    "v_add_f32 v121, v121, v74\n"
    "v_mfma_f32_16x16x32_bf16 a[68:71], v[168:171], v[132:135], a[68:71]\n"
    "s_add_u32 m0, s51, s95\n"
    "v_add_f32 v117, v117, v36\n"
    "v_add_f32 v122, v122, v75\n"
    "v_mfma_f32_16x16x32_bf16 a[72:75], v[168:171], v[136:139], a[72:75]\n"
    "ds_read_b128 v[164:167], v201 offset:37888\n"
    "v_add_f32 v118, v118, v37\n"
    "v_mfma_f32_16x16x32_bf16 a[72:75], v[172:175], v[152:155], a[72:75]\n"
    "buffer_load_dwordx4 v202, s[32:35], s64 offen lds\n"
    "v_mfma_f32_16x16x32_bf16 a[76:79], v[172:175], v[156:159], a[76:79]\n"
    "v_exp_f32 v54, v54\n"
    "v_mfma_f32_16x16x32_bf16 a[76:79], v[168:171], v[140:143], a[76:79]\n"
    "s_waitcnt lgkmcnt(2)\n"
    "v_add_f32 v125, v125, v76\n"
    "v_add_f32 v117, v117, v38\n"
    "v_mfma_f32_16x16x32_bf16 a[92:95], v[176:179], v[140:143], a[92:95]\n"
    "ds_read_b128 v[168:171], v201 offset:38912\n"
    "v_add_f32 v126, v126, v77\n"
    "v_mfma_f32_16x16x32_bf16 a[92:95], v[180:183], v[156:159], a[92:95]\n"
    "buffer_load_dwordx4 v203, s[32:35], s64 offen offset:1024 lds\n"
    "v_mfma_f32_16x16x32_bf16 a[88:91], v[180:183], v[152:155], a[88:91]\n"
    "v_exp_f32 v55, v55\n"
    "v_mfma_f32_16x16x32_bf16 a[88:91], v[176:179], v[136:139], a[88:91]\n"
    "v_add_f32 v118, v118, v39\n"
    "v_add_f32 v125, v125, v78\n"
    "v_mfma_f32_16x16x32_bf16 a[84:87], v[176:179], v[132:135], a[84:87]\n"
    "ds_read_b128 v[172:175], v201 offset:39936\n"
    "v_add_f32 v121, v121, v40\n"
    "v_mfma_f32_16x16x32_bf16 a[84:87], v[180:183], v[148:151], a[84:87]\n"
    "buffer_load_dwordx4 v204, s[32:35], s64 offen offset:2048 lds\n"
    "v_mfma_f32_16x16x32_bf16 a[80:83], v[180:183], v[144:147], a[80:83]\n"
    "v_exp_f32 v56, v56\n"
    "v_mfma_f32_16x16x32_bf16 a[80:83], v[176:179], v[128:131], a[80:83]\n"
    "s_waitcnt lgkmcnt(2)\n"
    "v_add_f32 v126, v126, v79\n"
    "v_add_f32 v122, v122, v41\n"
    "v_mfma_f32_16x16x32_bf16 a[96:99], v[160:163], v[128:131], a[96:99]\n"
    "s_mov_b32 s95, s94\n"
    "ds_read_b128 v[176:179], v201 offset:40960\n"
    "v_add_f32 v113, v113, v80\n"
    "v_mfma_f32_16x16x32_bf16 a[96:99], v[164:167], v[144:147], a[96:99]\n"
    "s_sub_u32 s94, s94, 0x3000\n"
    "v_add_f32 v121, v121, v42\n"
    "v_add_f32 v114, v114, v81\n"
    "v_mfma_f32_16x16x32_bf16 a[100:103], v[164:167], v[148:151], a[100:103]\n"
    "s_min_u32 s94, s94, 0x6000\n"
    "v_exp_f32 v57, v57\n"
    "v_mfma_f32_16x16x32_bf16 a[100:103], v[160:163], v[132:135], a[100:103]\n"
    "s_sub_u32 s93, s94, s95\n"
    "v_add_f32 v122, v122, v43\n"
    "v_add_f32 v113, v113, v82\n"
    "v_mfma_f32_16x16x32_bf16 a[104:107], v[160:163], v[136:139], a[104:107]\n"
    "ds_read_b128 v[180:183], v201 offset:41984\n"
    "v_add_f32 v125, v125, v44\n"
    "v_mfma_f32_16x16x32_bf16 a[104:107], v[164:167], v[152:155], a[104:107]\n"
    "v_exp_f32 v58, v58\n"
    "v_mfma_f32_16x16x32_bf16 a[108:111], v[164:167], v[156:159], a[108:111]\n"
    "v_add_f32 v114, v114, v83\n"
    "v_add_f32 v126, v126, v45\n"
    "v_mfma_f32_16x16x32_bf16 a[108:111], v[160:163], v[140:143], a[108:111]\n"
    "s_waitcnt lgkmcnt(2)\n"
    "v_add_f32 v117, v117, v84\n"
    "v_add_f32 v125, v125, v46\n"
    "v_mfma_f32_16x16x32_bf16 a[124:127], v[168:171], v[140:143], a[124:127]\n"
    "ds_read_b128 v[160:163], v201 offset:43008\n"
    "v_add_f32 v118, v118, v85\n"
    "v_mfma_f32_16x16x32_bf16 a[124:127], v[172:175], v[156:159], a[124:127]\n"
    "v_exp_f32 v59, v59\n"
    "v_mfma_f32_16x16x32_bf16 a[120:123], v[172:175], v[152:155], a[120:123]\n"
    "v_add_f32 v126, v126, v47\n"
    "v_add_f32 v117, v117, v86\n"
    "v_mfma_f32_16x16x32_bf16 a[120:123], v[168:171], v[136:139], a[120:123]\n"
    "v_add_f32 v113, v113, v48\n"
    "v_add_u32 v205, s93, v205\n"
    "v_mfma_f32_16x16x32_bf16 a[116:119], v[168:171], v[132:135], a[116:119]\n"
    "ds_read_b128 v[164:167], v201 offset:44032\n"
    "v_add_f32 v118, v118, v87\n"
    "v_mfma_f32_16x16x32_bf16 a[116:119], v[172:175], v[148:151], a[116:119]\n"
    "v_exp_f32 v60, v60\n"
    "v_mfma_f32_16x16x32_bf16 a[112:115], v[172:175], v[144:147], a[112:115]\n"
    "v_add_f32 v114, v114, v49\n"
    "v_add_u32 v206, s93, v206\n"
    "v_mfma_f32_16x16x32_bf16 a[112:115], v[168:171], v[128:131], a[112:115]\n"
    "s_waitcnt lgkmcnt(2)\n"
    "v_add_f32 v121, v121, v88\n"
    "v_add_f32 v113, v113, v50\n"
    "v_mfma_f32_16x16x32_bf16 a[128:131], v[176:179], v[128:131], a[128:131]\n"
    "ds_read_b128 v[168:171], v201 offset:45056\n"
    "v_add_u32 v207, s93, v207\n"
    "v_mfma_f32_16x16x32_bf16 a[128:131], v[180:183], v[144:147], a[128:131]\n"
    "v_exp_f32 v61, v61\n"
    "v_mfma_f32_16x16x32_bf16 a[132:135], v[180:183], v[148:151], a[132:135]\n"
    "s_add_u32 s64, s64, 0xc000\n"
    "v_add_f32 v122, v122, v89\n"
    "v_add_f32 v114, v114, v51\n"
    "v_mfma_f32_16x16x32_bf16 a[132:135], v[176:179], v[132:135], a[132:135]\n"
    "s_add_u32 s62, s62, 0x6000\n"
    "v_add_f32 v121, v121, v90\n"
    "v_add_f32 v117, v117, v52\n"
    "v_mfma_f32_16x16x32_bf16 a[136:139], v[176:179], v[136:139], a[136:139]\n"
    "s_add_u32 s63, s63, 0x6000\n"
    "ds_read_b128 v[172:175], v201 offset:46080\n"
    "v_add_f32 v122, v122, v91\n"
    "v_mfma_f32_16x16x32_bf16 a[136:139], v[180:183], v[152:155], a[136:139]\n"
    "s_add_u32 s60, s60, 0x6000\n"
    "v_exp_f32 v62, v62\n"
    "v_mfma_f32_16x16x32_bf16 a[140:143], v[180:183], v[156:159], a[140:143]\n"
    "s_add_u32 s61, s61, 0x6000\n"
    "v_add_f32 v118, v118, v53\n"
    "v_add_f32 v125, v125, v92\n"
    "v_mfma_f32_16x16x32_bf16 a[140:143], v[176:179], v[140:143], a[140:143]\n"
    "s_waitcnt lgkmcnt(2)\n"
    "v_add_f32 v117, v117, v54\n"
    "v_add_f32 v126, v126, v93\n"
    "v_mfma_f32_16x16x32_bf16 a[156:159], v[160:163], v[140:143], a[156:159]\n"
    "ds_read_b128 v[176:179], v201 offset:47104\n"
    "v_add_f32 v118, v118, v55\n"
    "v_mfma_f32_16x16x32_bf16 a[156:159], v[164:167], v[156:159], a[156:159]\n"
    "s_waitcnt vmcnt(3)\n"
    "ds_read_b128 v[180:183], v201 offset:48128\n"
    "v_add_f32 v125, v125, v94\n"
    "v_mfma_f32_16x16x32_bf16 a[152:155], v[164:167], v[152:155], a[152:155]\n"
    "v_exp_f32 v63, v63\n"
    "v_mfma_f32_16x16x32_bf16 a[152:155], v[160:163], v[136:139], a[152:155]\n"
    "v_add_f32 v121, v121, v56\n"
    "v_add_f32 v126, v126, v95\n"
    "v_mfma_f32_16x16x32_bf16 a[148:151], v[160:163], v[132:135], a[148:151]\n"
    "v_add_f32 v122, v122, v57\n"
    "v_mfma_f32_16x16x32_bf16 a[148:151], v[164:167], v[148:151], a[148:151]\n"
    "v_mfma_f32_16x16x32_bf16 a[144:147], v[164:167], v[144:147], a[144:147]\n"
    "v_mfma_f32_16x16x32_bf16 a[144:147], v[160:163], v[128:131], a[144:147]\n"
    "s_waitcnt lgkmcnt(0)\n"
    "v_mfma_f32_16x16x32_bf16 a[160:163], v[168:171], v[128:131], a[160:163]\n"
    "s_barrier\n"
    "LMID1:\n"
    "v_mfma_f32_16x16x32_bf16 a[160:163], v[172:175], v[144:147], a[160:163]\n"
    "ds_read_u16_d16_hi v223, v209 offset:6336\n"
    "ds_read_u16_d16_hi v222, v209 offset:192\n"
    "v_mfma_f32_16x16x32_bf16 a[164:167], v[172:175], v[148:151], a[164:167]\n"
    "ds_read_b128 v[160:163], v200 offset:24576\n"
    "v_add_f32 v121, v121, v58\n"
    "v_mfma_f32_16x16x32_bf16 a[164:167], v[168:171], v[132:135], a[164:167]\n"
    "ds_read_b128 v[232:235], v205 offset:24576\n"
    "v_add_f32 v122, v122, v59\n"
    "v_mfma_f32_16x16x32_bf16 a[168:171], v[168:171], v[136:139], a[168:171]\n"
    "ds_read_b128 v[164:167], v200 offset:25600\n"
    "ds_read_b128 v[236:239], v206 offset:24576\n"
    "v_mfma_f32_16x16x32_bf16 a[168:171], v[172:175], v[152:155], a[168:171]\n"
    "v_add_f32 v125, v125, v60\n"
    "v_add_f32 v126, v126, v61\n"
    "v_mfma_f32_16x16x32_bf16 a[172:175], v[172:175], v[156:159], a[172:175]\n"
    "v_add_f32 v125, v125, v62\n"
    "v_add_f32 v126, v126, v63\n"
    "v_mfma_f32_16x16x32_bf16 a[172:175], v[168:171], v[140:143], a[172:175]\n"
    "ds_read_b64 v[228:229], v210 offset:64\n"
    "ds_read2st64_b32 v[240:241], v207 offset0:96 offset1:99\n"
    "v_mfma_f32_16x16x32_bf16 a[188:191], v[176:179], v[140:143], a[188:191]\n"
    "ds_read_b128 v[168:171], v200 offset:26624\n"
    "ds_read2st64_b32 v[242:243], v207 offset0:102 offset1:105\n"
    "v_mfma_f32_16x16x32_bf16 a[188:191], v[180:183], v[156:159], a[188:191]\n"
    "ds_read2st64_b32 v[244:245], v207 offset0:108 offset1:111\n"
    "ds_read2st64_b32 v[246:247], v207 offset0:114 offset1:117\n"
    "v_mfma_f32_16x16x32_bf16 a[184:187], v[176:179], v[136:139], a[184:187]\n"
    "v_cvt_pk_bf16_f32 v140, v44, v45\n"
    "v_cvt_pk_bf16_f32 v141, v46, v47\n"
    "v_mfma_f32_16x16x32_bf16 a[184:187], v[180:183], v[152:155], a[184:187]\n"
    "s_waitcnt lgkmcnt(6)\n"
    "v_mul_f32 v226, s75, v223\n"
    "v_mfma_f32_16x16x32_bf16 a[180:183], v[176:179], v[132:135], a[180:183]\n"
    "s_add_u32 s59, s59, 1\n"
    "ds_read_b128 v[172:175], v200 offset:27648\n"
    "v_fma_f32 v227, -v222, v226, v217\n"
    "v_mfma_f32_16x16x32_bf16 a[180:183], v[180:183], v[148:151], a[180:183]\n"
    "v_cvt_f32_i32 v232, v232\n"
    "v_cvt_f32_i32 v233, v233\n"
    "v_mfma_f32_16x16x32_bf16 a[176:179], v[176:179], v[128:131], a[176:179]\n"
    "s_cmp_lt_u32 s59, 15\n"
    "v_cvt_f32_i32 v234, v234\n"
    "v_cvt_f32_i32 v235, v235\n"
    "v_mfma_f32_16x16x32_bf16 a[176:179], v[180:183], v[144:147], a[176:179]\n"
    "s_cbranch_scc1 LLOOP1\n"
    "s_branch LTAIL\n"
    "LENT0:\n"
    "s_mov_b32 s59, 1\n"
    "s_add_u32 m0, s72, 0x21000\n"
    "s_nop 0\n"
    "buffer_load_dwordx4 v250, s[48:51], 0 offen offset:2048 lds\n"
    "s_add_u32 m0, s72, 0x18000\n"
    "s_nop 0\n"
    "buffer_load_dwordx4 v202, s[32:35], s64 offen lds\n"
    "buffer_load_dwordx4 v203, s[32:35], s64 offen offset:1024 lds\n"
    "buffer_load_dwordx4 v204, s[32:35], s64 offen offset:2048 lds\n"
    "s_add_u32 s64, s64, 0xc000\n"
    "s_mov_b32 s94, 0x3000\n"
    "s_mov_b32 s95, 0x6000\n"
    "s_add_u32 s51, s72, 0x18000\n"
    "v_add_u32 v205, 0x3000, v205\n"
    "v_add_u32 v206, 0x3000, v206\n"
    "v_add_u32 v207, 0x3000, v207\n"
    "buffer_store_dwordx2 v[220:221], v214, s[44:47], 0 offen sc1\n"
    "ds_read_b128 v[160:163], v200 offset:24576\n"
    "ds_read_u16_d16_hi v223, v209 offset:6336\n"
    "ds_read_u16_d16_hi v222, v209 offset:192\n"
    "v_add_f32 v121, v121, v58\n"
    "ds_read_b128 v[164:167], v200 offset:25600\n"
    "ds_read_b128 v[232:235], v205 offset:24576\n"
    "v_add_f32 v122, v122, v59\n"
    "ds_read_b128 v[236:239], v206 offset:24576\n"
    "v_add_f32 v125, v125, v60\n"
    "v_add_f32 v126, v126, v61\n"
    "v_add_f32 v125, v125, v62\n"
    "v_add_f32 v126, v126, v63\n"
    "ds_read_b128 v[168:171], v200 offset:26624\n"
    "ds_read_b64 v[228:229], v210 offset:64\n"
    "ds_read2st64_b32 v[240:241], v207 offset0:96 offset1:99\n"
    "ds_read2st64_b32 v[242:243], v207 offset0:102 offset1:105\n"
    "ds_read2st64_b32 v[244:245], v207 offset0:108 offset1:111\n"
    "ds_read2st64_b32 v[246:247], v207 offset0:114 offset1:117\n"
    "v_cvt_pk_bf16_f32 v140, v44, v45\n"
    "v_cvt_pk_bf16_f32 v141, v46, v47\n"
    "ds_read_b128 v[172:175], v200 offset:27648\n"
    "s_waitcnt lgkmcnt(7)\n"
    "v_mul_f32 v226, s75, v223\n"
    "v_fma_f32 v227, -v222, v226, v217\n"
    "v_cvt_f32_i32 v232, v232\n"
    "v_cvt_f32_i32 v233, v233\n"
    "s_nop 7\n"
    ".p2align 6\n"
    "LLOOP:\n"
    "v_mfma_f32_16x16x32_bf16 v[64:67], v[160:163], a[192:195], v[96:99]\n"
    "ds_read_b128 v[176:179], v200 offset:28672\n"
    "v_cvt_f32_i32 v234, v234\n"
    "v_mfma_f32_16x16x32_bf16 v[64:67], v[164:167], a[196:199], v[64:67]\n"
    "s_waitcnt lgkmcnt(1)\n"
    "v_cvt_f32_i32 v235, v235\n"
    "v_cvt_f32_i32 v236, v236\n"
    "v_mfma_f32_16x16x32_bf16 v[64:67], v[168:171], a[200:203], v[64:67]\n"
    "v_cvt_f32_i32 v237, v237\n"
    "v_cvt_f32_i32 v238, v238\n"
    "v_mfma_f32_16x16x32_bf16 v[68:71], v[168:171], a[224:227], v[100:103]\n"
    "v_cvt_f32_i32 v239, v239\n"
    "v_mul_f32_dpp v232, v226, v232 row_newbcast:0 row_mask:0xf bank_mask:0xf\n"
    "v_mfma_f32_16x16x32_bf16 v[68:71], v[164:167], a[220:223], v[68:71]\n"
    "ds_read_b128 v[180:183], v200 offset:29696\n"
    "v_mul_f32_dpp v233, v226, v233 row_newbcast:1 row_mask:0xf bank_mask:0xf\n"
    "v_mfma_f32_16x16x32_bf16 v[68:71], v[160:163], a[216:219], v[68:71]\n"
    "v_mul_f32_dpp v234, v226, v234 row_newbcast:2 row_mask:0xf bank_mask:0xf\n"
    "v_mul_f32_dpp v235, v226, v235 row_newbcast:3 row_mask:0xf bank_mask:0xf\n"
    "v_mfma_f32_16x16x32_bf16 v[72:75], v[160:163], a[240:243], v[104:107]\n"
    "v_mul_f32_dpp v236, v226, v236 row_newbcast:4 row_mask:0xf bank_mask:0xf\n"
    "v_mul_f32_dpp v237, v226, v237 row_newbcast:5 row_mask:0xf bank_mask:0xf\n"
    "v_mfma_f32_16x16x32_bf16 v[72:75], v[164:167], a[244:247], v[72:75]\n"
    "v_mul_f32_dpp v238, v226, v238 row_newbcast:6 row_mask:0xf bank_mask:0xf\n"
    "v_mul_f32_dpp v239, v226, v239 row_newbcast:7 row_mask:0xf bank_mask:0xf\n"
    "v_mfma_f32_16x16x32_bf16 v[72:75], v[168:171], a[248:251], v[72:75]\n"
    "v_add_f32_dpp v232, v227, v232 row_newbcast:0 row_mask:0xf bank_mask:0xf\n"
    "v_add_f32_dpp v233, v227, v233 row_newbcast:1 row_mask:0xf bank_mask:0xf\n"
    "v_mfma_f32_16x16x32_bf16 v[76:79], v[168:171], v[16:19], v[108:111]\n"
    "v_add_f32_dpp v234, v227, v234 row_newbcast:2 row_mask:0xf bank_mask:0xf\n"
    "v_add_f32_dpp v235, v227, v235 row_newbcast:3 row_mask:0xf bank_mask:0xf\n"
    "v_mfma_f32_16x16x32_bf16 v[76:79], v[164:167], v[12:15], v[76:79]\n"
    "v_add_f32_dpp v236, v227, v236 row_newbcast:4 row_mask:0xf bank_mask:0xf\n"
    "v_add_f32_dpp v237, v227, v237 row_newbcast:5 row_mask:0xf bank_mask:0xf\n"
    "v_mfma_f32_16x16x32_bf16 v[76:79], v[160:163], v[8:11], v[76:79]\n"
    "s_waitcnt lgkmcnt(1)\n"
    "v_add_f32_dpp v238, v227, v238 row_newbcast:6 row_mask:0xf bank_mask:0xf\n"
    "v_add_f32_dpp v239, v227, v239 row_newbcast:7 row_mask:0xf bank_mask:0xf\n"
    "v_mfma_f32_16x16x32_bf16 v[76:79], v[172:175], v[20:23], v[76:79]\n"
    "ds_read_b128 v[160:163], v200 offset:33792\n"
    "v_cvt_pk_bf16_f32 v232, v232, v233\n"
    "v_mfma_f32_16x16x32_bf16 v[76:79], v[176:179], v[24:27], v[76:79]\n"
    "s_waitcnt lgkmcnt(1)\n"
    "v_cvt_f32_i32 v240, v240\n"
    "v_cvt_pk_bf16_f32 v233, v234, v235\n"
    "v_mfma_f32_16x16x32_bf16 v[76:79], v[180:183], v[28:31], v[76:79]\n"
    "v_cvt_f32_i32 v241, v241\n"
    "v_cvt_pk_bf16_f32 v234, v236, v237\n"
    "v_mfma_f32_16x16x32_bf16 v[72:75], v[180:183], v[4:7], v[72:75]\n"
    "ds_read_b128 v[164:167], v200 offset:34816\n"
    "v_cvt_f32_i32 v242, v242\n"
    "v_mfma_f32_16x16x32_bf16 v[72:75], v[176:179], v[0:3], v[72:75]\n"
    "v_cvt_pk_bf16_f32 v235, v238, v239\n"
    "v_cvt_f32_i32 v243, v243\n"
    "v_mfma_f32_16x16x32_bf16 v[72:75], v[172:175], a[252:255], v[72:75]\n"
    "v_cvt_f32_i32 v244, v244\n"
    "v_cvt_f32_i32 v245, v245\n"
    "v_mfma_f32_16x16x32_bf16 v[68:71], v[172:175], a[228:231], v[68:71]\n"
    "ds_read_b128 v[168:171], v200 offset:35840\n"
    "v_cvt_f32_i32 v246, v246\n"
    "v_mfma_f32_16x16x32_bf16 v[68:71], v[176:179], a[232:235], v[68:71]\n"
    "v_cvt_f32_i32 v247, v247\n"
    "v_mul_f32_dpp v240, v228, v240 row_newbcast:0 row_mask:0xf bank_mask:0xf\n"
    "v_mfma_f32_16x16x32_bf16 v[68:71], v[180:183], a[236:239], v[68:71]\n"
    "v_mul_f32_dpp v241, v228, v241 row_newbcast:1 row_mask:0xf bank_mask:0xf\n"
    "v_mul_f32_dpp v242, v228, v242 row_newbcast:2 row_mask:0xf bank_mask:0xf\n"
    "v_mfma_f32_16x16x32_bf16 v[64:67], v[180:183], a[212:215], v[64:67]\n"
    "v_mul_f32_dpp v243, v228, v243 row_newbcast:3 row_mask:0xf bank_mask:0xf\n"
    "v_mul_f32_dpp v244, v228, v244 row_newbcast:4 row_mask:0xf bank_mask:0xf\n"
    "v_mfma_f32_16x16x32_bf16 v[64:67], v[176:179], a[208:211], v[64:67]\n"
    "buffer_store_dwordx4 v[232:235], v211, s[36:39], s62 offen sc1\n"
    "v_mul_f32_dpp v245, v228, v245 row_newbcast:5 row_mask:0xf bank_mask:0xf\n"
    "v_mul_f32_dpp v246, v228, v246 row_newbcast:6 row_mask:0xf bank_mask:0xf\n"
    "v_mul_f32_dpp v247, v228, v247 row_newbcast:7 row_mask:0xf bank_mask:0xf\n"
    "v_mfma_f32_16x16x32_bf16 v[64:67], v[172:175], a[204:207], v[64:67]\n"
    "s_waitcnt lgkmcnt(1)\n"
    "v_add_f32_dpp v240, v229, v240 row_newbcast:0 row_mask:0xf bank_mask:0xf\n"
    "v_add_f32_dpp v241, v229, v241 row_newbcast:1 row_mask:0xf bank_mask:0xf\n"
    "v_mfma_f32_16x16x32_bf16 v[80:83], v[160:163], a[204:207], v[96:99]\n"
    "ds_read_b128 v[172:175], v200 offset:30720\n"
    "v_add_f32_dpp v242, v229, v242 row_newbcast:2 row_mask:0xf bank_mask:0xf\n"
    "v_mfma_f32_16x16x32_bf16 v[80:83], v[164:167], a[208:211], v[80:83]\n"
    "s_waitcnt lgkmcnt(1)\n"
    "v_add_f32_dpp v243, v229, v243 row_newbcast:3 row_mask:0xf bank_mask:0xf\n"
    "v_add_f32_dpp v244, v229, v244 row_newbcast:4 row_mask:0xf bank_mask:0xf\n"
    "v_mfma_f32_16x16x32_bf16 v[80:83], v[168:171], a[212:215], v[80:83]\n"
    "v_add_f32_dpp v245, v229, v245 row_newbcast:5 row_mask:0xf bank_mask:0xf\n"
    "v_add_f32_dpp v246, v229, v246 row_newbcast:6 row_mask:0xf bank_mask:0xf\n"
    "v_mfma_f32_16x16x32_bf16 v[84:87], v[168:171], a[236:239], v[100:103]\n"
    "ds_read_b128 v[176:179], v200 offset:31744\n"
    "v_add_f32_dpp v247, v229, v247 row_newbcast:7 row_mask:0xf bank_mask:0xf\n"
    "v_mfma_f32_16x16x32_bf16 v[84:87], v[164:167], a[232:235], v[84:87]\n"
    "v_cvt_pk_bf16_f32 v240, v240, v241\n"
    "v_cvt_pk_bf16_f32 v241, v242, v243\n"
    "v_mfma_f32_16x16x32_bf16 v[84:87], v[160:163], a[228:231], v[84:87]\n"
    "v_cvt_pk_bf16_f32 v242, v244, v245\n"
    "v_cvt_pk_bf16_f32 v243, v246, v247\n"
    "v_mfma_f32_16x16x32_bf16 v[88:91], v[160:163], a[252:255], v[104:107]\n"
    "ds_read_b128 v[180:183], v200 offset:32768\n"
    "v_cvt_pk_bf16_f32 v136, v40, v41\n"
    "v_mfma_f32_16x16x32_bf16 v[88:91], v[164:167], v[0:3], v[88:91]\n"
    "v_cvt_pk_bf16_f32 v137, v42, v43\n"
    "v_cvt_pk_bf16_f32 v132, v36, v37\n"
    "v_mfma_f32_16x16x32_bf16 v[88:91], v[168:171], v[4:7], v[88:91]\n"
    "v_cvt_pk_bf16_f32 v133, v38, v39\n"
    "v_cvt_pk_bf16_f32 v128, v32, v33\n"
    "v_mfma_f32_16x16x32_bf16 v[92:95], v[168:171], v[28:31], v[108:111]\n"
    "v_cvt_pk_bf16_f32 v129, v34, v35\n"
    "v_cvt_pk_bf16_f32 v142, v60, v61\n"
    "v_mfma_f32_16x16x32_bf16 v[92:95], v[164:167], v[24:27], v[92:95]\n"
    "v_exp_f32 v64, v64\n"
    "v_mfma_f32_16x16x32_bf16 v[92:95], v[160:163], v[20:23], v[92:95]\n"
    "s_waitcnt lgkmcnt(1)\n"
    "v_exp_f32 v65, v65\n"
    "v_mfma_f32_16x16x32_bf16 v[92:95], v[172:175], v[8:11], v[92:95]\n"
    "ds_read_b128 v[160:163], v200 offset:36864\n"
    "v_cvt_pk_bf16_f32 v143, v62, v63\n"
    "v_mfma_f32_16x16x32_bf16 v[92:95], v[176:179], v[12:15], v[92:95]\n"
    "s_waitcnt lgkmcnt(1)\n"
    "v_exp_f32 v66, v66\n"
    "v_mfma_f32_16x16x32_bf16 v[92:95], v[180:183], v[16:19], v[92:95]\n"
    "buffer_store_dwordx4 v[240:243], v212, s[40:43], s63 offen sc1\n"
    "v_exp_f32 v67, v67\n"
    "v_cvt_pk_bf16_f32 v138, v56, v57\n"
    "v_mfma_f32_16x16x32_bf16 v[88:91], v[180:183], a[248:251], v[88:91]\n"
    "ds_read_b128 v[164:167], v200 offset:37888\n"
    "v_cvt_pk_bf16_f32 v139, v58, v59\n"
    "v_mfma_f32_16x16x32_bf16 v[88:91], v[176:179], a[244:247], v[88:91]\n"
    "v_exp_f32 v68, v68\n"
    "v_mfma_f32_16x16x32_bf16 v[88:91], v[172:175], a[240:243], v[88:91]\n"
    "v_exp_f32 v69, v69\n"
    "v_mfma_f32_16x16x32_bf16 v[84:87], v[172:175], a[216:219], v[84:87]\n"
    "ds_read_b128 v[168:171], v200 offset:38912\n"
    "v_cvt_pk_bf16_f32 v134, v52, v53\n"
    "v_mfma_f32_16x16x32_bf16 v[84:87], v[176:179], a[220:223], v[84:87]\n"
    "v_exp_f32 v70, v70\n"
    "v_mfma_f32_16x16x32_bf16 v[84:87], v[180:183], a[224:227], v[84:87]\n"
    "v_exp_f32 v71, v71\n"
    "v_mfma_f32_16x16x32_bf16 v[80:83], v[180:183], a[200:203], v[80:83]\n"
    "v_exp_f32 v72, v72\n"
    "v_mfma_f32_16x16x32_bf16 v[80:83], v[176:179], a[196:199], v[80:83]\n"
    "v_cvt_pk_bf16_f32 v135, v54, v55\n"
    "v_cvt_pk_bf16_f32 v130, v48, v49\n"
    "v_mfma_f32_16x16x32_bf16 v[80:83], v[172:175], a[192:195], v[80:83]\n"
    "s_waitcnt lgkmcnt(1)\n"
    "v_exp_f32 v73, v73\n"
    "v_mfma_f32_16x16x32_bf16 v[32:35], v[160:163], a[192:195], v[96:99]\n"
    "ds_read_b128 v[172:175], v200 offset:39936\n"
    "v_cvt_pk_bf16_f32 v131, v50, v51\n"
    "v_mfma_f32_16x16x32_bf16 v[32:35], v[164:167], a[196:199], v[32:35]\n"
    "s_waitcnt lgkmcnt(1)\n"
    "v_exp_f32 v74, v74\n"
    "v_mfma_f32_16x16x32_bf16 v[32:35], v[168:171], a[200:203], v[32:35]\n"
    "v_exp_f32 v75, v75\n"
    "v_mfma_f32_16x16x32_bf16 v[36:39], v[168:171], a[224:227], v[100:103]\n"
    "ds_read_b128 v[176:179], v200 offset:40960\n"
    "v_cvt_pk_bf16_f32 v144, v64, v65\n"
    "v_mfma_f32_16x16x32_bf16 v[36:39], v[164:167], a[220:223], v[36:39]\n"
    "v_exp_f32 v76, v76\n"
    "v_mfma_f32_16x16x32_bf16 v[36:39], v[160:163], a[216:219], v[36:39]\n"
    "v_exp_f32 v77, v77\n"
    "v_mfma_f32_16x16x32_bf16 v[40:43], v[160:163], a[240:243], v[104:107]\n"
    "ds_read_b128 v[180:183], v200 offset:41984\n"
    "v_cvt_pk_bf16_f32 v145, v66, v67\n"
    "v_mfma_f32_16x16x32_bf16 v[40:43], v[164:167], a[244:247], v[40:43]\n"
    "v_exp_f32 v78, v78\n"
    "v_mfma_f32_16x16x32_bf16 v[40:43], v[168:171], a[248:251], v[40:43]\n"
    "v_exp_f32 v79, v79\n"
    "v_mfma_f32_16x16x32_bf16 v[44:47], v[168:171], v[16:19], v[108:111]\n"
    "v_exp_f32 v80, v80\n"
    "v_mfma_f32_16x16x32_bf16 v[44:47], v[164:167], v[12:15], v[44:47]\n"
    "v_exp_f32 v81, v81\n"
    "v_mfma_f32_16x16x32_bf16 v[44:47], v[160:163], v[8:11], v[44:47]\n"
    "s_waitcnt lgkmcnt(1)\n"
    "v_exp_f32 v82, v82\n"
    "v_mfma_f32_16x16x32_bf16 v[44:47], v[172:175], v[20:23], v[44:47]\n"
    "ds_read_b128 v[160:163], v200 offset:46080\n"
    "v_cvt_pk_bf16_f32 v148, v68, v69\n"
    "v_mfma_f32_16x16x32_bf16 v[44:47], v[176:179], v[24:27], v[44:47]\n"
    "s_waitcnt lgkmcnt(1)\n"
    "v_exp_f32 v83, v83\n"
    "v_mfma_f32_16x16x32_bf16 v[44:47], v[180:183], v[28:31], v[44:47]\n"
    "v_exp_f32 v84, v84\n"
    "v_mfma_f32_16x16x32_bf16 v[40:43], v[180:183], v[4:7], v[40:43]\n"
    "ds_read_b128 v[164:167], v200 offset:47104\n"
    "v_cvt_pk_bf16_f32 v149, v70, v71\n"
    "v_mfma_f32_16x16x32_bf16 v[40:43], v[176:179], v[0:3], v[40:43]\n"
    "v_exp_f32 v85, v85\n"
    "v_mfma_f32_16x16x32_bf16 v[40:43], v[172:175], a[252:255], v[40:43]\n"
    "v_exp_f32 v86, v86\n"
    "v_mfma_f32_16x16x32_bf16 v[36:39], v[172:175], a[228:231], v[36:39]\n"
    "ds_read_b128 v[168:171], v200 offset:48128\n"
    "v_cvt_pk_bf16_f32 v152, v72, v73\n"
    "v_mfma_f32_16x16x32_bf16 v[36:39], v[176:179], a[232:235], v[36:39]\n"
    "v_exp_f32 v87, v87\n"
    "v_mfma_f32_16x16x32_bf16 v[36:39], v[180:183], a[236:239], v[36:39]\n"
    "v_exp_f32 v88, v88\n"
    "v_mfma_f32_16x16x32_bf16 v[32:35], v[180:183], a[212:215], v[32:35]\n"
    "v_exp_f32 v89, v89\n"
    "v_mfma_f32_16x16x32_bf16 v[32:35], v[176:179], a[208:211], v[32:35]\n"
    "v_cvt_pk_bf16_f32 v153, v74, v75\n"
    "v_cvt_pk_bf16_f32 v156, v76, v77\n"
    "v_mfma_f32_16x16x32_bf16 v[32:35], v[172:175], a[204:207], v[32:35]\n"
    "s_waitcnt lgkmcnt(1)\n"
    "s_mov_b32 m0, 0\n"
    "v_cvt_pk_bf16_f32 v157, v78, v79\n"
    "v_mfma_f32_16x16x32_bf16 v[48:51], v[160:163], a[204:207], v[96:99]\n"
    "ds_read_b128 v[172:175], v200 offset:43008\n"
    "v_cvt_pk_bf16_f32 v146, v80, v81\n"
    "v_mfma_f32_16x16x32_bf16 v[48:51], v[164:167], a[208:211], v[48:51]\n"
    "s_waitcnt lgkmcnt(1)\n"
    "buffer_load_dwordx4 v200, s[36:39], s60 offen sc1 lds\n"
    "v_mfma_f32_16x16x32_bf16 v[48:51], v[168:171], a[212:215], v[48:51]\n"
    "v_exp_f32 v90, v90\n"
    "v_mfma_f32_16x16x32_bf16 v[52:55], v[168:171], a[236:239], v[100:103]\n"
    "ds_read_b128 v[176:179], v200 offset:44032\n"
    "v_cvt_pk_bf16_f32 v147, v82, v83\n"
    "v_mfma_f32_16x16x32_bf16 v[52:55], v[164:167], a[232:235], v[52:55]\n"
    "v_exp_f32 v91, v91\n"
    "v_mfma_f32_16x16x32_bf16 v[52:55], v[160:163], a[228:231], v[52:55]\n"
    "buffer_load_dwordx4 v200, s[36:39], s60 offen offset:1024 sc1 lds\n"
    "v_mfma_f32_16x16x32_bf16 v[56:59], v[160:163], a[252:255], v[104:107]\n"
    "ds_read_b128 v[180:183], v200 offset:45056\n"
    "v_cvt_pk_bf16_f32 v150, v84, v85\n"
    "v_mfma_f32_16x16x32_bf16 v[56:59], v[164:167], v[0:3], v[56:59]\n"
    "v_exp_f32 v92, v92\n"
    "v_mfma_f32_16x16x32_bf16 v[56:59], v[168:171], v[4:7], v[56:59]\n"
    "v_exp_f32 v93, v93\n"
    "v_mfma_f32_16x16x32_bf16 v[60:63], v[168:171], v[28:31], v[108:111]\n"
    "buffer_load_dwordx4 v200, s[36:39], s60 offen offset:2048 sc1 lds\n"
    "v_mfma_f32_16x16x32_bf16 v[60:63], v[164:167], v[24:27], v[60:63]\n"
    "v_cvt_pk_bf16_f32 v151, v86, v87\n"
    "v_cvt_pk_bf16_f32 v154, v88, v89\n"
    "v_mfma_f32_16x16x32_bf16 v[60:63], v[160:163], v[20:23], v[60:63]\n"
    "s_waitcnt lgkmcnt(1)\n"
    "s_add_u32 m0, s71, 0x12000\n"
    "v_cvt_pk_bf16_f32 v155, v90, v91\n"
    "v_mfma_f32_16x16x32_bf16 v[60:63], v[172:175], v[8:11], v[60:63]\n"
    "ds_read_b128 v[160:163], v201\n"
    "v_cvt_pk_bf16_f32 v158, v92, v93\n"
    "v_mfma_f32_16x16x32_bf16 v[60:63], v[176:179], v[12:15], v[60:63]\n"
    "s_waitcnt lgkmcnt(1)\n"
    "buffer_load_dwordx4 v200, s[40:43], s61 offen sc1 lds\n"
    "v_mfma_f32_16x16x32_bf16 v[60:63], v[180:183], v[16:19], v[60:63]\n"
    "v_exp_f32 v94, v94\n"
    "v_mfma_f32_16x16x32_bf16 v[56:59], v[180:183], a[248:251], v[56:59]\n"
    "ds_read_b128 v[164:167], v201 offset:1024\n"
    "v_add_f32 v113, v113, v64\n"
    "v_mfma_f32_16x16x32_bf16 v[56:59], v[176:179], a[244:247], v[56:59]\n"
    "v_exp_f32 v95, v95\n"
    "v_mfma_f32_16x16x32_bf16 v[56:59], v[172:175], a[240:243], v[56:59]\n"
    "buffer_load_dwordx4 v200, s[40:43], s61 offen offset:1024 sc1 lds\n"
    "v_mfma_f32_16x16x32_bf16 v[52:55], v[172:175], a[216:219], v[52:55]\n"
    "ds_read_b128 v[168:171], v201 offset:2048\n"
    "v_cvt_pk_bf16_f32 v159, v94, v95\n"
    "v_mfma_f32_16x16x32_bf16 v[52:55], v[176:179], a[220:223], v[52:55]\n"
    "v_exp_f32 v32, v32\n"
    "v_mfma_f32_16x16x32_bf16 v[52:55], v[180:183], a[224:227], v[52:55]\n"
    "v_exp_f32 v33, v33\n"
    "v_mfma_f32_16x16x32_bf16 v[48:51], v[180:183], a[200:203], v[48:51]\n"
    "buffer_load_dwordx4 v200, s[40:43], s61 offen offset:2048 sc1 lds\n"
    "v_mfma_f32_16x16x32_bf16 v[48:51], v[176:179], a[196:199], v[48:51]\n"
    "v_exp_f32 v34, v34\n"
    "v_mfma_f32_16x16x32_bf16 v[48:51], v[172:175], a[192:195], v[48:51]\n"
    "s_waitcnt lgkmcnt(1)\n"
    "v_exp_f32 v35, v35\n"
    "v_mfma_f32_16x16x32_bf16 a[0:3], v[160:163], v[128:131], a[0:3]\n"
    "ds_read_b128 v[172:175], v201 offset:3072\n"
    "ds_read_b128 v[176:179], v201 offset:4096\n"
    "v_mfma_f32_16x16x32_bf16 a[0:3], v[164:167], v[144:147], a[0:3]\n"
    "buffer_load_dwordx4 v200, s[40:43], s61 offen offset:3072 sc1 lds\n"
    "v_mfma_f32_16x16x32_bf16 a[4:7], v[160:163], v[132:135], a[4:7]\n"
    "s_add_u32 s65, s61, 0x1000\n"
    "v_exp_f32 v36, v36\n"
    "v_mfma_f32_16x16x32_bf16 a[4:7], v[164:167], v[148:151], a[4:7]\n"
    "s_add_u32 m0, s71, 0x13000\n"
    "v_exp_f32 v37, v37\n"
    "v_mfma_f32_16x16x32_bf16 a[8:11], v[160:163], v[136:139], a[8:11]\n"
    "ds_read_b128 v[180:183], v201 offset:5120\n"
    "v_add_f32 v114, v114, v65\n"
    "v_mfma_f32_16x16x32_bf16 a[8:11], v[164:167], v[152:155], a[8:11]\n"
    "buffer_load_dwordx4 v200, s[40:43], s65 offen sc1 lds\n"
    "v_mfma_f32_16x16x32_bf16 a[12:15], v[160:163], v[140:143], a[12:15]\n"
    "v_exp_f32 v38, v38\n"
    "v_mfma_f32_16x16x32_bf16 a[12:15], v[164:167], v[156:159], a[12:15]\n"
    "s_waitcnt lgkmcnt(2)\n"
    "v_exp_f32 v39, v39\n"
    "v_mfma_f32_16x16x32_bf16 a[28:31], v[168:171], v[140:143], a[28:31]\n"
    "ds_read_b128 v[160:163], v201 offset:6144\n"
    "v_add_f32 v113, v113, v66\n"
    "v_mfma_f32_16x16x32_bf16 a[28:31], v[172:175], v[156:159], a[28:31]\n"
    "buffer_load_dwordx4 v200, s[40:43], s65 offen offset:1024 sc1 lds\n"
    "v_mfma_f32_16x16x32_bf16 a[24:27], v[172:175], v[152:155], a[24:27]\n"
    "v_exp_f32 v40, v40\n"
    "v_mfma_f32_16x16x32_bf16 a[24:27], v[168:171], v[136:139], a[24:27]\n"
    "v_exp_f32 v41, v41\n"
    "v_mfma_f32_16x16x32_bf16 a[20:23], v[168:171], v[132:135], a[20:23]\n"
    "ds_read_b128 v[164:167], v201 offset:7168\n"
    "v_add_f32 v114, v114, v67\n"
    "v_mfma_f32_16x16x32_bf16 a[20:23], v[172:175], v[148:151], a[20:23]\n"
    "v_exp_f32 v42, v42\n"
    "v_mfma_f32_16x16x32_bf16 a[16:19], v[172:175], v[144:147], a[16:19]\n"
    "v_exp_f32 v43, v43\n"
    "v_mfma_f32_16x16x32_bf16 a[16:19], v[168:171], v[128:131], a[16:19]\n"
    "s_waitcnt lgkmcnt(2)\n"
    "v_exp_f32 v44, v44\n"
    "v_mfma_f32_16x16x32_bf16 a[32:35], v[176:179], v[128:131], a[32:35]\n"
    "ds_read_b128 v[168:171], v201 offset:8192\n"
    "v_add_f32 v117, v117, v68\n"
    "v_mfma_f32_16x16x32_bf16 a[32:35], v[180:183], v[144:147], a[32:35]\n"
    "buffer_load_dwordx2 v[218:219], v213, s[44:47], 0 offen sc1\n"
    "v_mfma_f32_16x16x32_bf16 a[36:39], v[180:183], v[148:151], a[36:39]\n"
    "v_exp_f32 v45, v45\n"
    "v_mfma_f32_16x16x32_bf16 a[36:39], v[176:179], v[132:135], a[36:39]\n"
    "v_exp_f32 v46, v46\n"
    "v_mfma_f32_16x16x32_bf16 a[40:43], v[176:179], v[136:139], a[40:43]\n"
    "ds_read_b128 v[172:175], v201 offset:9216\n"
    "v_add_f32 v118, v118, v69\n"
    "v_mfma_f32_16x16x32_bf16 a[40:43], v[180:183], v[152:155], a[40:43]\n"
    "v_exp_f32 v47, v47\n"
    "v_mfma_f32_16x16x32_bf16 a[44:47], v[180:183], v[156:159], a[44:47]\n"
    "v_exp_f32 v48, v48\n"
    "v_mfma_f32_16x16x32_bf16 a[44:47], v[176:179], v[140:143], a[44:47]\n"
    "s_waitcnt lgkmcnt(2)\n"
    "v_exp_f32 v49, v49\n"
    "v_mfma_f32_16x16x32_bf16 a[60:63], v[160:163], v[140:143], a[60:63]\n"
    "ds_read_b128 v[176:179], v201 offset:10240\n"
    "v_add_f32 v117, v117, v70\n"
    "v_mfma_f32_16x16x32_bf16 a[60:63], v[164:167], v[156:159], a[60:63]\n"
    "v_exp_f32 v50, v50\n"
    "v_mfma_f32_16x16x32_bf16 a[56:59], v[164:167], v[152:155], a[56:59]\n"
    "v_exp_f32 v51, v51\n"
    "v_mfma_f32_16x16x32_bf16 a[56:59], v[160:163], v[136:139], a[56:59]\n"
    "v_add_f32 v113, v113, v32\n"
    "v_add_f32 v118, v118, v71\n"
    "v_mfma_f32_16x16x32_bf16 a[52:55], v[160:163], v[132:135], a[52:55]\n"
    "ds_read_b128 v[180:183], v201 offset:11264\n"
    "v_add_f32 v114, v114, v33\n"
    "v_mfma_f32_16x16x32_bf16 a[52:55], v[164:167], v[148:151], a[52:55]\n"
    "v_exp_f32 v52, v52\n"
    "v_mfma_f32_16x16x32_bf16 a[48:51], v[164:167], v[144:147], a[48:51]\n"
    "v_add_f32 v121, v121, v72\n"
    "v_add_f32 v113, v113, v34\n"
    "v_mfma_f32_16x16x32_bf16 a[48:51], v[160:163], v[128:131], a[48:51]\n"
    "s_waitcnt lgkmcnt(2)\n"
    "v_exp_f32 v53, v53\n"
    "v_mfma_f32_16x16x32_bf16 a[64:67], v[168:171], v[128:131], a[64:67]\n"
    "ds_read_b128 v[160:163], v201 offset:12288\n"
    "v_add_f32 v122, v122, v73\n"
    "v_mfma_f32_16x16x32_bf16 a[64:67], v[172:175], v[144:147], a[64:67]\n"
    "v_add_f32 v114, v114, v35\n"
    "v_add_f32 v121, v121, v74\n"
    "v_mfma_f32_16x16x32_bf16 a[68:71], v[172:175], v[148:151], a[68:71]\n"
    "v_add_f32 v117, v117, v36\n"
    "v_add_f32 v122, v122, v75\n"
    "v_mfma_f32_16x16x32_bf16 a[68:71], v[168:171], v[132:135], a[68:71]\n"
    "s_add_u32 m0, s51, s95\n"
    "v_exp_f32 v54, v54\n"
    "v_mfma_f32_16x16x32_bf16 a[72:75], v[168:171], v[136:139], a[72:75]\n"
    "ds_read_b128 v[164:167], v201 offset:13312\n"
    "v_add_f32 v118, v118, v37\n"
    "v_mfma_f32_16x16x32_bf16 a[72:75], v[172:175], v[152:155], a[72:75]\n"
    "buffer_load_dwordx4 v202, s[32:35], s64 offen lds\n"
    "v_mfma_f32_16x16x32_bf16 a[76:79], v[172:175], v[156:159], a[76:79]\n"
    "v_add_f32 v125, v125, v76\n"
    "v_add_f32 v117, v117, v38\n"
    "v_mfma_f32_16x16x32_bf16 a[76:79], v[168:171], v[140:143], a[76:79]\n"
    "s_waitcnt lgkmcnt(2)\n"
    "v_exp_f32 v55, v55\n"
    "v_mfma_f32_16x16x32_bf16 a[92:95], v[176:179], v[140:143], a[92:95]\n"
    "ds_read_b128 v[168:171], v201 offset:14336\n"
    "v_add_f32 v126, v126, v77\n"
    "v_mfma_f32_16x16x32_bf16 a[92:95], v[180:183], v[156:159], a[92:95]\n"
    "buffer_load_dwordx4 v203, s[32:35], s64 offen offset:1024 lds\n"
    "v_mfma_f32_16x16x32_bf16 a[88:91], v[180:183], v[152:155], a[88:91]\n"
    "v_add_f32 v118, v118, v39\n"
    "v_add_f32 v125, v125, v78\n"
    "v_mfma_f32_16x16x32_bf16 a[88:91], v[176:179], v[136:139], a[88:91]\n"
    "v_add_f32 v121, v121, v40\n"
    "v_add_f32 v126, v126, v79\n"
    "v_mfma_f32_16x16x32_bf16 a[84:87], v[176:179], v[132:135], a[84:87]\n"
    "ds_read_b128 v[172:175], v201 offset:15360\n"
    "v_add_f32 v122, v122, v41\n"
    "v_mfma_f32_16x16x32_bf16 a[84:87], v[180:183], v[148:151], a[84:87]\n"
    "buffer_load_dwordx4 v204, s[32:35], s64 offen offset:2048 lds\n"
    "v_mfma_f32_16x16x32_bf16 a[80:83], v[180:183], v[144:147], a[80:83]\n"
    "v_exp_f32 v56, v56\n"
    "v_mfma_f32_16x16x32_bf16 a[80:83], v[176:179], v[128:131], a[80:83]\n"
    "s_waitcnt lgkmcnt(2)\n"
    "v_add_f32 v113, v113, v80\n"
    "v_add_f32 v121, v121, v42\n"
    "v_mfma_f32_16x16x32_bf16 a[96:99], v[160:163], v[128:131], a[96:99]\n"
    "s_mov_b32 s95, s94\n"
    "ds_read_b128 v[176:179], v201 offset:16384\n"
    "v_add_f32 v114, v114, v81\n"
    "v_mfma_f32_16x16x32_bf16 a[96:99], v[164:167], v[144:147], a[96:99]\n"
    "s_sub_u32 s94, s94, 0x3000\n"
    "v_exp_f32 v57, v57\n"
    "v_mfma_f32_16x16x32_bf16 a[100:103], v[164:167], v[148:151], a[100:103]\n"
    "s_min_u32 s94, s94, 0x6000\n"
    "v_add_f32 v122, v122, v43\n"
    "v_add_f32 v113, v113, v82\n"
    "v_mfma_f32_16x16x32_bf16 a[100:103], v[160:163], v[132:135], a[100:103]\n"
    "s_sub_u32 s93, s94, s95\n"
    "v_add_f32 v125, v125, v44\n"
    "v_add_f32 v114, v114, v83\n"
    "v_mfma_f32_16x16x32_bf16 a[104:107], v[160:163], v[136:139], a[104:107]\n"
    "ds_read_b128 v[180:183], v201 offset:17408\n"
    "v_add_f32 v126, v126, v45\n"
    "v_mfma_f32_16x16x32_bf16 a[104:107], v[164:167], v[152:155], a[104:107]\n"
    "v_exp_f32 v58, v58\n"
    "v_mfma_f32_16x16x32_bf16 a[108:111], v[164:167], v[156:159], a[108:111]\n"
    "v_add_f32 v117, v117, v84\n"
    "v_add_f32 v125, v125, v46\n"
    "v_mfma_f32_16x16x32_bf16 a[108:111], v[160:163], v[140:143], a[108:111]\n"
    "s_waitcnt lgkmcnt(2)\n"
    "v_exp_f32 v59, v59\n"
    "v_mfma_f32_16x16x32_bf16 a[124:127], v[168:171], v[140:143], a[124:127]\n"
    "ds_read_b128 v[160:163], v201 offset:18432\n"
    "v_add_f32 v118, v118, v85\n"
    "v_mfma_f32_16x16x32_bf16 a[124:127], v[172:175], v[156:159], a[124:127]\n"
    "v_add_f32 v126, v126, v47\n"
    "v_add_f32 v117, v117, v86\n"
    "v_mfma_f32_16x16x32_bf16 a[120:123], v[172:175], v[152:155], a[120:123]\n"
    "v_add_f32 v113, v113, v48\n"
    "v_add_u32 v220, 1, v220\n"
    "v_mfma_f32_16x16x32_bf16 a[120:123], v[168:171], v[136:139], a[120:123]\n"
    "v_exp_f32 v60, v60\n"
    "v_mfma_f32_16x16x32_bf16 a[116:119], v[168:171], v[132:135], a[116:119]\n"
    "ds_read_b128 v[164:167], v201 offset:19456\n"
    "v_add_u32 v205, s93, v205\n"
    "v_mfma_f32_16x16x32_bf16 a[116:119], v[172:175], v[148:151], a[116:119]\n"
    "v_add_f32 v118, v118, v87\n"
    "v_add_f32 v114, v114, v49\n"
    "v_mfma_f32_16x16x32_bf16 a[112:115], v[172:175], v[144:147], a[112:115]\n"
    "v_add_u32 v209, 0x180, v209\n"
    "v_add_u32 v206, s93, v206\n"
    "v_mfma_f32_16x16x32_bf16 a[112:115], v[168:171], v[128:131], a[112:115]\n"
    "s_waitcnt lgkmcnt(2)\n"
    "v_add_f32 v121, v121, v88\n"
    "v_add_f32 v113, v113, v50\n"
    "v_mfma_f32_16x16x32_bf16 a[128:131], v[176:179], v[128:131], a[128:131]\n"
    "s_add_u32 s64, s64, 0xc000\n"
    "ds_read_b128 v[168:171], v201 offset:20480\n"
    "v_add_u32 v210, 0x80, v210\n"
    "v_mfma_f32_16x16x32_bf16 a[128:131], v[180:183], v[144:147], a[128:131]\n"
    "s_add_u32 s62, s62, 0x6000\n"
    "v_exp_f32 v61, v61\n"
    "v_mfma_f32_16x16x32_bf16 a[132:135], v[180:183], v[148:151], a[132:135]\n"
    "s_add_u32 s63, s63, 0x6000\n"
    "v_add_u32 v207, s93, v207\n"
    "v_add_f32 v122, v122, v89\n"
    "v_mfma_f32_16x16x32_bf16 a[132:135], v[176:179], v[132:135], a[132:135]\n"
    "s_add_u32 s60, s60, 0x6000\n"
    "v_add_f32 v114, v114, v51\n"
    "v_add_f32 v121, v121, v90\n"
    "v_mfma_f32_16x16x32_bf16 a[136:139], v[176:179], v[136:139], a[136:139]\n"
    "s_add_u32 s61, s61, 0x6000\n"
    "ds_read_b128 v[172:175], v201 offset:21504\n"
    "v_add_f32 v117, v117, v52\n"
    "v_mfma_f32_16x16x32_bf16 a[136:139], v[180:183], v[152:155], a[136:139]\n"
    "s_add_u32 s66, s66, s74\n"
    "v_exp_f32 v62, v62\n"
    "v_mfma_f32_16x16x32_bf16 a[140:143], v[180:183], v[156:159], a[140:143]\n"
    "s_min_u32 s66, s66, 33\n"
    "v_add_f32 v122, v122, v91\n"
    "v_add_f32 v118, v118, v53\n"
    "v_mfma_f32_16x16x32_bf16 a[140:143], v[176:179], v[140:143], a[140:143]\n"
    "s_waitcnt lgkmcnt(2)\n"
    "v_add_f32 v125, v125, v92\n"
    "v_add_f32 v117, v117, v54\n"
    "v_mfma_f32_16x16x32_bf16 a[156:159], v[160:163], v[140:143], a[156:159]\n"
    "ds_read_b128 v[176:179], v201 offset:22528\n"
    "v_add_f32 v126, v126, v93\n"
    "v_mfma_f32_16x16x32_bf16 a[156:159], v[164:167], v[156:159], a[156:159]\n"
    "s_waitcnt vmcnt(3)\n"
    "ds_read_b128 v[180:183], v201 offset:23552\n"
    "v_add_f32 v118, v118, v55\n"
    "v_mfma_f32_16x16x32_bf16 a[152:155], v[164:167], v[152:155], a[152:155]\n"
    "v_cmp_ne_u32 s[80:81], s67, v219\n"
    "v_add_f32 v125, v125, v94\n"
    "v_mfma_f32_16x16x32_bf16 a[152:155], v[160:163], v[136:139], a[152:155]\n"
    "v_cmp_gt_u32 vcc, s66, v218\n"
    "v_add_f32 v121, v121, v56\n"
    "v_mfma_f32_16x16x32_bf16 a[148:151], v[160:163], v[132:135], a[148:151]\n"
    "v_exp_f32 v63, v63\n"
    "v_mfma_f32_16x16x32_bf16 a[148:151], v[164:167], v[148:151], a[148:151]\n"
    "s_or_b64 vcc, vcc, s[80:81]\n"
    "s_cbranch_vccnz LSPRE4\n"
    "LSPB4:\n"
    "v_add_f32 v126, v126, v95\n"
    "v_mfma_f32_16x16x32_bf16 a[144:147], v[164:167], v[144:147], a[144:147]\n"
    "v_add_f32 v122, v122, v57\n"
    "v_mfma_f32_16x16x32_bf16 a[144:147], v[160:163], v[128:131], a[144:147]\n"
    "s_waitcnt lgkmcnt(0)\n"
    "v_mfma_f32_16x16x32_bf16 a[160:163], v[168:171], v[128:131], a[160:163]\n"
    "s_barrier\n"
    "v_mfma_f32_16x16x32_bf16 a[160:163], v[172:175], v[144:147], a[160:163]\n"
    "buffer_store_dwordx2 v[220:221], v214, s[44:47], 0 offen sc1\n"
    "v_mfma_f32_16x16x32_bf16 a[164:167], v[172:175], v[148:151], a[164:167]\n"
    "ds_read_b128 v[160:163], v200\n"
    "ds_read_u16_d16_hi v223, v209 offset:6144\n"
    "v_mfma_f32_16x16x32_bf16 a[164:167], v[168:171], v[132:135], a[164:167]\n"
    "ds_read_u16_d16_hi v222, v209\n"
    "v_add_f32 v121, v121, v58\n"
    "v_mfma_f32_16x16x32_bf16 a[168:171], v[168:171], v[136:139], a[168:171]\n"
    "ds_read_b128 v[164:167], v200 offset:1024\n"
    "ds_read_b128 v[232:235], v205 offset:24576\n"
    "v_mfma_f32_16x16x32_bf16 a[168:171], v[172:175], v[152:155], a[168:171]\n"
    "v_add_f32 v122, v122, v59\n"
    "ds_read_b128 v[236:239], v206 offset:24576\n"
    "v_mfma_f32_16x16x32_bf16 a[172:175], v[172:175], v[156:159], a[172:175]\n"
    "v_add_f32 v125, v125, v60\n"
    "v_add_f32 v126, v126, v61\n"
    "v_mfma_f32_16x16x32_bf16 a[172:175], v[168:171], v[140:143], a[172:175]\n"
    "v_add_f32 v125, v125, v62\n"
    "v_add_f32 v126, v126, v63\n"
    "v_mfma_f32_16x16x32_bf16 a[188:191], v[176:179], v[140:143], a[188:191]\n"
    "ds_read_b128 v[168:171], v200 offset:2048\n"
    "ds_read_b64 v[228:229], v210\n"
    "v_mfma_f32_16x16x32_bf16 a[188:191], v[180:183], v[156:159], a[188:191]\n"
    "ds_read2st64_b32 v[240:241], v207 offset0:96 offset1:99\n"
    "ds_read2st64_b32 v[242:243], v207 offset0:102 offset1:105\n"
    "v_mfma_f32_16x16x32_bf16 a[184:187], v[176:179], v[136:139], a[184:187]\n"
    "ds_read2st64_b32 v[244:245], v207 offset0:108 offset1:111\n"
    "ds_read2st64_b32 v[246:247], v207 offset0:114 offset1:117\n"
    "v_mfma_f32_16x16x32_bf16 a[184:187], v[180:183], v[152:155], a[184:187]\n"
    "v_cvt_pk_bf16_f32 v140, v44, v45\n"
    "v_cvt_pk_bf16_f32 v141, v46, v47\n"
    "v_mfma_f32_16x16x32_bf16 a[180:183], v[176:179], v[132:135], a[180:183]\n"
    "ds_read_b128 v[172:175], v200 offset:3072\n"
    "s_waitcnt lgkmcnt(7)\n"
    "v_mfma_f32_16x16x32_bf16 a[180:183], v[180:183], v[148:151], a[180:183]\n"
    "v_mul_f32 v226, s75, v223\n"
    "v_fma_f32 v227, -v222, v226, v217\n"
    "v_mfma_f32_16x16x32_bf16 a[176:179], v[176:179], v[128:131], a[176:179]\n"
    "v_cvt_f32_i32 v232, v232\n"
    "v_cvt_f32_i32 v233, v233\n"
    "v_mfma_f32_16x16x32_bf16 a[176:179], v[180:183], v[144:147], a[176:179]\n"
    "v_cvt_f32_i32 v234, v234\n"
    "v_cvt_f32_i32 v235, v235\n"
    "LM0:\n"
    "v_mfma_f32_16x16x32_bf16 v[64:67], v[160:163], a[192:195], v[96:99]\n"
    "ds_read_b128 v[176:179], v200 offset:4096\n"
    "v_cvt_f32_i32 v236, v236\n"
    "v_mfma_f32_16x16x32_bf16 v[64:67], v[164:167], a[196:199], v[64:67]\n"
    "s_waitcnt lgkmcnt(1)\n"
    "v_cvt_f32_i32 v237, v237\n"
    "v_cvt_f32_i32 v238, v238\n"
    "v_mfma_f32_16x16x32_bf16 v[64:67], v[168:171], a[200:203], v[64:67]\n"
    "v_cvt_f32_i32 v239, v239\n"
    "v_mul_f32_dpp v232, v226, v232 row_newbcast:0 row_mask:0xf bank_mask:0xf\n"
    "v_mfma_f32_16x16x32_bf16 v[68:71], v[168:171], a[224:227], v[100:103]\n"
    "v_mul_f32_dpp v233, v226, v233 row_newbcast:1 row_mask:0xf bank_mask:0xf\n"
    "v_mul_f32_dpp v234, v226, v234 row_newbcast:2 row_mask:0xf bank_mask:0xf\n"
    "v_mfma_f32_16x16x32_bf16 v[68:71], v[164:167], a[220:223], v[68:71]\n"
    "ds_read_b128 v[180:183], v200 offset:5120\n"
    "v_mul_f32_dpp v235, v226, v235 row_newbcast:3 row_mask:0xf bank_mask:0xf\n"
    "v_mfma_f32_16x16x32_bf16 v[68:71], v[160:163], a[216:219], v[68:71]\n"
    "v_mul_f32_dpp v236, v226, v236 row_newbcast:4 row_mask:0xf bank_mask:0xf\n"
    "v_mul_f32_dpp v237, v226, v237 row_newbcast:5 row_mask:0xf bank_mask:0xf\n"
    "v_mfma_f32_16x16x32_bf16 v[72:75], v[160:163], a[240:243], v[104:107]\n"
    "v_mul_f32_dpp v238, v226, v238 row_newbcast:6 row_mask:0xf bank_mask:0xf\n"
    "v_mul_f32_dpp v239, v226, v239 row_newbcast:7 row_mask:0xf bank_mask:0xf\n"
    "v_mfma_f32_16x16x32_bf16 v[72:75], v[164:167], a[244:247], v[72:75]\n"
    "v_add_f32_dpp v232, v227, v232 row_newbcast:0 row_mask:0xf bank_mask:0xf\n"
    "v_add_f32_dpp v233, v227, v233 row_newbcast:1 row_mask:0xf bank_mask:0xf\n"
    "v_mfma_f32_16x16x32_bf16 v[72:75], v[168:171], a[248:251], v[72:75]\n"
    "v_add_f32_dpp v234, v227, v234 row_newbcast:2 row_mask:0xf bank_mask:0xf\n"
    "v_add_f32_dpp v235, v227, v235 row_newbcast:3 row_mask:0xf bank_mask:0xf\n"
    "v_mfma_f32_16x16x32_bf16 v[76:79], v[168:171], v[16:19], v[108:111]\n"
    "v_add_f32_dpp v236, v227, v236 row_newbcast:4 row_mask:0xf bank_mask:0xf\n"
    "v_add_f32_dpp v237, v227, v237 row_newbcast:5 row_mask:0xf bank_mask:0xf\n"
    "v_mfma_f32_16x16x32_bf16 v[76:79], v[164:167], v[12:15], v[76:79]\n"
    "v_add_f32_dpp v238, v227, v238 row_newbcast:6 row_mask:0xf bank_mask:0xf\n"
    "v_add_f32_dpp v239, v227, v239 row_newbcast:7 row_mask:0xf bank_mask:0xf\n"
    "v_mfma_f32_16x16x32_bf16 v[76:79], v[160:163], v[8:11], v[76:79]\n"
    "s_waitcnt lgkmcnt(1)\n"
    "v_cvt_pk_bf16_f32 v232, v232, v233\n"
    "v_cvt_f32_i32 v240, v240\n"
    "v_mfma_f32_16x16x32_bf16 v[76:79], v[172:175], v[20:23], v[76:79]\n"
    "ds_read_b128 v[160:163], v200 offset:9216\n"
    "v_cvt_pk_bf16_f32 v233, v234, v235\n"
    "v_mfma_f32_16x16x32_bf16 v[76:79], v[176:179], v[24:27], v[76:79]\n"
    "s_waitcnt lgkmcnt(1)\n"
    "v_cvt_f32_i32 v241, v241\n"
    "v_cvt_pk_bf16_f32 v234, v236, v237\n"
    "v_mfma_f32_16x16x32_bf16 v[76:79], v[180:183], v[28:31], v[76:79]\n"
    "v_cvt_f32_i32 v242, v242\n"
    "v_cvt_pk_bf16_f32 v235, v238, v239\n"
    "v_mfma_f32_16x16x32_bf16 v[72:75], v[180:183], v[4:7], v[72:75]\n"
    "ds_read_b128 v[164:167], v200 offset:10240\n"
    "v_cvt_f32_i32 v243, v243\n"
    "v_mfma_f32_16x16x32_bf16 v[72:75], v[176:179], v[0:3], v[72:75]\n"
    "v_cvt_f32_i32 v244, v244\n"
    "v_cvt_f32_i32 v245, v245\n"
    "v_mfma_f32_16x16x32_bf16 v[72:75], v[172:175], a[252:255], v[72:75]\n"
    "v_cvt_f32_i32 v246, v246\n"
    "v_cvt_f32_i32 v247, v247\n"
    "v_mfma_f32_16x16x32_bf16 v[68:71], v[172:175], a[228:231], v[68:71]\n"
    "ds_read_b128 v[168:171], v200 offset:11264\n"
    "v_mul_f32_dpp v240, v228, v240 row_newbcast:0 row_mask:0xf bank_mask:0xf\n"
    "v_mfma_f32_16x16x32_bf16 v[68:71], v[176:179], a[232:235], v[68:71]\n"
    "v_mul_f32_dpp v241, v228, v241 row_newbcast:1 row_mask:0xf bank_mask:0xf\n"
    "v_mul_f32_dpp v242, v228, v242 row_newbcast:2 row_mask:0xf bank_mask:0xf\n"
    "v_mfma_f32_16x16x32_bf16 v[68:71], v[180:183], a[236:239], v[68:71]\n"
    "v_mul_f32_dpp v243, v228, v243 row_newbcast:3 row_mask:0xf bank_mask:0xf\n"
    "v_mul_f32_dpp v244, v228, v244 row_newbcast:4 row_mask:0xf bank_mask:0xf\n"
    "v_mfma_f32_16x16x32_bf16 v[64:67], v[180:183], a[212:215], v[64:67]\n"
    "v_mul_f32_dpp v245, v228, v245 row_newbcast:5 row_mask:0xf bank_mask:0xf\n"
    "v_mul_f32_dpp v246, v228, v246 row_newbcast:6 row_mask:0xf bank_mask:0xf\n"
    "v_mfma_f32_16x16x32_bf16 v[64:67], v[176:179], a[208:211], v[64:67]\n"
    "buffer_store_dwordx4 v[232:235], v211, s[36:39], s62 offen sc1\n"
    "v_mul_f32_dpp v247, v228, v247 row_newbcast:7 row_mask:0xf bank_mask:0xf\n"
    "v_add_f32_dpp v240, v229, v240 row_newbcast:0 row_mask:0xf bank_mask:0xf\n"
    "v_add_f32_dpp v241, v229, v241 row_newbcast:1 row_mask:0xf bank_mask:0xf\n"
    "v_mfma_f32_16x16x32_bf16 v[64:67], v[172:175], a[204:207], v[64:67]\n"
    "s_waitcnt lgkmcnt(1)\n"
    "v_add_f32_dpp v242, v229, v242 row_newbcast:2 row_mask:0xf bank_mask:0xf\n"
    "v_add_f32_dpp v243, v229, v243 row_newbcast:3 row_mask:0xf bank_mask:0xf\n"
    "v_mfma_f32_16x16x32_bf16 v[80:83], v[160:163], a[204:207], v[96:99]\n"
    "ds_read_b128 v[172:175], v200 offset:6144\n"
    "v_add_f32_dpp v244, v229, v244 row_newbcast:4 row_mask:0xf bank_mask:0xf\n"
    "v_mfma_f32_16x16x32_bf16 v[80:83], v[164:167], a[208:211], v[80:83]\n"
    "s_waitcnt lgkmcnt(1)\n"
    "v_add_f32_dpp v245, v229, v245 row_newbcast:5 row_mask:0xf bank_mask:0xf\n"
    "v_add_f32_dpp v246, v229, v246 row_newbcast:6 row_mask:0xf bank_mask:0xf\n"
    "v_mfma_f32_16x16x32_bf16 v[80:83], v[168:171], a[212:215], v[80:83]\n"
    "v_add_f32_dpp v247, v229, v247 row_newbcast:7 row_mask:0xf bank_mask:0xf\n"
    "v_cvt_pk_bf16_f32 v240, v240, v241\n"
    "v_mfma_f32_16x16x32_bf16 v[84:87], v[168:171], a[236:239], v[100:103]\n"
    "ds_read_b128 v[176:179], v200 offset:7168\n"
    "v_cvt_pk_bf16_f32 v241, v242, v243\n"
    "v_mfma_f32_16x16x32_bf16 v[84:87], v[164:167], a[232:235], v[84:87]\n"
    "v_cvt_pk_bf16_f32 v242, v244, v245\n"
    "v_cvt_pk_bf16_f32 v243, v246, v247\n"
    "v_mfma_f32_16x16x32_bf16 v[84:87], v[160:163], a[228:231], v[84:87]\n"
    "v_cvt_pk_bf16_f32 v136, v40, v41\n"
    "v_cvt_pk_bf16_f32 v137, v42, v43\n"
    "v_mfma_f32_16x16x32_bf16 v[88:91], v[160:163], a[252:255], v[104:107]\n"
    "ds_read_b128 v[180:183], v200 offset:8192\n"
    "v_cvt_pk_bf16_f32 v132, v36, v37\n"
    "v_mfma_f32_16x16x32_bf16 v[88:91], v[164:167], v[0:3], v[88:91]\n"
    "v_cvt_pk_bf16_f32 v133, v38, v39\n"
    "v_cvt_pk_bf16_f32 v128, v32, v33\n"
    "v_mfma_f32_16x16x32_bf16 v[88:91], v[168:171], v[4:7], v[88:91]\n"
    "v_cvt_pk_bf16_f32 v129, v34, v35\n"
    "v_cvt_pk_bf16_f32 v142, v60, v61\n"
    "v_mfma_f32_16x16x32_bf16 v[92:95], v[168:171], v[28:31], v[108:111]\n"
    "v_exp_f32 v64, v64\n"
    "v_mfma_f32_16x16x32_bf16 v[92:95], v[164:167], v[24:27], v[92:95]\n"
    "v_exp_f32 v65, v65\n"
    "v_mfma_f32_16x16x32_bf16 v[92:95], v[160:163], v[20:23], v[92:95]\n"
    "s_waitcnt lgkmcnt(1)\n"
    "v_exp_f32 v66, v66\n"
    "v_mfma_f32_16x16x32_bf16 v[92:95], v[172:175], v[8:11], v[92:95]\n"
    "ds_read_b128 v[160:163], v200 offset:12288\n"
    "v_cvt_pk_bf16_f32 v143, v62, v63\n"
    "v_mfma_f32_16x16x32_bf16 v[92:95], v[176:179], v[12:15], v[92:95]\n"
    "s_waitcnt lgkmcnt(1)\n"
    "v_exp_f32 v67, v67\n"
    "v_mfma_f32_16x16x32_bf16 v[92:95], v[180:183], v[16:19], v[92:95]\n"
    "buffer_store_dwordx4 v[240:243], v212, s[40:43], s63 offen sc1\n"
    "v_exp_f32 v68, v68\n"
    "v_cvt_pk_bf16_f32 v138, v56, v57\n"
    "v_mfma_f32_16x16x32_bf16 v[88:91], v[180:183], a[248:251], v[88:91]\n"
    "ds_read_b128 v[164:167], v200 offset:13312\n"
    "v_cvt_pk_bf16_f32 v139, v58, v59\n"
    "v_mfma_f32_16x16x32_bf16 v[88:91], v[176:179], a[244:247], v[88:91]\n"
    "v_exp_f32 v69, v69\n"
    "v_mfma_f32_16x16x32_bf16 v[88:91], v[172:175], a[240:243], v[88:91]\n"
    "v_exp_f32 v70, v70\n"
    "v_mfma_f32_16x16x32_bf16 v[84:87], v[172:175], a[216:219], v[84:87]\n"
    "ds_read_b128 v[168:171], v200 offset:14336\n"
    "v_cvt_pk_bf16_f32 v134, v52, v53\n"
    "v_mfma_f32_16x16x32_bf16 v[84:87], v[176:179], a[220:223], v[84:87]\n"
    "v_exp_f32 v71, v71\n"
    "v_mfma_f32_16x16x32_bf16 v[84:87], v[180:183], a[224:227], v[84:87]\n"
    "v_exp_f32 v72, v72\n"
    "v_mfma_f32_16x16x32_bf16 v[80:83], v[180:183], a[200:203], v[80:83]\n"
    "v_cvt_pk_bf16_f32 v135, v54, v55\n"
    "v_cvt_pk_bf16_f32 v130, v48, v49\n"
    "v_mfma_f32_16x16x32_bf16 v[80:83], v[176:179], a[196:199], v[80:83]\n"
    "v_exp_f32 v73, v73\n"
    "v_mfma_f32_16x16x32_bf16 v[80:83], v[172:175], a[192:195], v[80:83]\n"
    "s_waitcnt lgkmcnt(1)\n"
    "v_cvt_pk_bf16_f32 v131, v50, v51\n"
    "v_cvt_pk_bf16_f32 v144, v64, v65\n"
    "v_mfma_f32_16x16x32_bf16 v[32:35], v[160:163], a[192:195], v[96:99]\n"
    "ds_read_b128 v[172:175], v200 offset:15360\n"
    "v_cvt_pk_bf16_f32 v145, v66, v67\n"
    "v_mfma_f32_16x16x32_bf16 v[32:35], v[164:167], a[196:199], v[32:35]\n"
    "s_waitcnt lgkmcnt(1)\n"
    "v_exp_f32 v74, v74\n"
    "v_mfma_f32_16x16x32_bf16 v[32:35], v[168:171], a[200:203], v[32:35]\n"
    "v_exp_f32 v75, v75\n"
    "v_mfma_f32_16x16x32_bf16 v[36:39], v[168:171], a[224:227], v[100:103]\n"
    "ds_read_b128 v[176:179], v200 offset:16384\n"
    "v_cvt_pk_bf16_f32 v148, v68, v69\n"
    "v_mfma_f32_16x16x32_bf16 v[36:39], v[164:167], a[220:223], v[36:39]\n"
    "v_exp_f32 v76, v76\n"
    "v_mfma_f32_16x16x32_bf16 v[36:39], v[160:163], a[216:219], v[36:39]\n"
    "v_exp_f32 v77, v77\n"
    "v_mfma_f32_16x16x32_bf16 v[40:43], v[160:163], a[240:243], v[104:107]\n"
    "ds_read_b128 v[180:183], v200 offset:17408\n"
    "v_cvt_pk_bf16_f32 v149, v70, v71\n"
    "v_mfma_f32_16x16x32_bf16 v[40:43], v[164:167], a[244:247], v[40:43]\n"
    "v_exp_f32 v78, v78\n"
    "v_mfma_f32_16x16x32_bf16 v[40:43], v[168:171], a[248:251], v[40:43]\n"
    "v_exp_f32 v79, v79\n"
    "v_mfma_f32_16x16x32_bf16 v[44:47], v[168:171], v[16:19], v[108:111]\n"
    "v_exp_f32 v80, v80\n"
    "v_mfma_f32_16x16x32_bf16 v[44:47], v[164:167], v[12:15], v[44:47]\n"
    "v_exp_f32 v81, v81\n"
    "v_mfma_f32_16x16x32_bf16 v[44:47], v[160:163], v[8:11], v[44:47]\n"
    "s_waitcnt lgkmcnt(1)\n"
    "v_exp_f32 v82, v82\n"
    "v_mfma_f32_16x16x32_bf16 v[44:47], v[172:175], v[20:23], v[44:47]\n"
    "ds_read_b128 v[160:163], v200 offset:21504\n"
    "v_cvt_pk_bf16_f32 v152, v72, v73\n"
    "v_mfma_f32_16x16x32_bf16 v[44:47], v[176:179], v[24:27], v[44:47]\n"
    "s_waitcnt lgkmcnt(1)\n"
    "v_exp_f32 v83, v83\n"
    "v_mfma_f32_16x16x32_bf16 v[44:47], v[180:183], v[28:31], v[44:47]\n"
    "v_exp_f32 v84, v84\n"
    "v_mfma_f32_16x16x32_bf16 v[40:43], v[180:183], v[4:7], v[40:43]\n"
    "ds_read_b128 v[164:167], v200 offset:22528\n"
    "v_cvt_pk_bf16_f32 v153, v74, v75\n"
    "v_mfma_f32_16x16x32_bf16 v[40:43], v[176:179], v[0:3], v[40:43]\n"
    "v_exp_f32 v85, v85\n"
    "v_mfma_f32_16x16x32_bf16 v[40:43], v[172:175], a[252:255], v[40:43]\n"
    "v_exp_f32 v86, v86\n"
    "v_mfma_f32_16x16x32_bf16 v[36:39], v[172:175], a[228:231], v[36:39]\n"
    "ds_read_b128 v[168:171], v200 offset:23552\n"
    "v_cvt_pk_bf16_f32 v156, v76, v77\n"
    "v_mfma_f32_16x16x32_bf16 v[36:39], v[176:179], a[232:235], v[36:39]\n"
    "v_exp_f32 v87, v87\n"
    "v_mfma_f32_16x16x32_bf16 v[36:39], v[180:183], a[236:239], v[36:39]\n"
    "v_exp_f32 v88, v88\n"
    "v_mfma_f32_16x16x32_bf16 v[32:35], v[180:183], a[212:215], v[32:35]\n"
    "v_exp_f32 v89, v89\n"
    "v_mfma_f32_16x16x32_bf16 v[32:35], v[176:179], a[208:211], v[32:35]\n"
    "v_exp_f32 v90, v90\n"
    "v_mfma_f32_16x16x32_bf16 v[32:35], v[172:175], a[204:207], v[32:35]\n"
    "s_waitcnt lgkmcnt(1)\n"
    "s_mov_b32 m0, 0x6000\n"
    "v_cvt_pk_bf16_f32 v157, v78, v79\n"
    "v_mfma_f32_16x16x32_bf16 v[48:51], v[160:163], a[204:207], v[96:99]\n"
    "ds_read_b128 v[172:175], v200 offset:18432\n"
    "v_cvt_pk_bf16_f32 v146, v80, v81\n"
    "v_mfma_f32_16x16x32_bf16 v[48:51], v[164:167], a[208:211], v[48:51]\n"
    "s_waitcnt lgkmcnt(1)\n"
    "buffer_load_dwordx4 v200, s[36:39], s60 offen sc1 lds\n"
    "v_mfma_f32_16x16x32_bf16 v[48:51], v[168:171], a[212:215], v[48:51]\n"
    "v_exp_f32 v91, v91\n"
    "v_mfma_f32_16x16x32_bf16 v[52:55], v[168:171], a[236:239], v[100:103]\n"
    "ds_read_b128 v[176:179], v200 offset:19456\n"
    "v_cvt_pk_bf16_f32 v147, v82, v83\n"
    "v_mfma_f32_16x16x32_bf16 v[52:55], v[164:167], a[232:235], v[52:55]\n"
    "v_exp_f32 v92, v92\n"
    "v_mfma_f32_16x16x32_bf16 v[52:55], v[160:163], a[228:231], v[52:55]\n"
    "buffer_load_dwordx4 v200, s[36:39], s60 offen offset:1024 sc1 lds\n"
    "v_mfma_f32_16x16x32_bf16 v[56:59], v[160:163], a[252:255], v[104:107]\n"
    "ds_read_b128 v[180:183], v200 offset:20480\n"
    "v_cvt_pk_bf16_f32 v150, v84, v85\n"
    "v_mfma_f32_16x16x32_bf16 v[56:59], v[164:167], v[0:3], v[56:59]\n"
    "v_exp_f32 v93, v93\n"
    "v_mfma_f32_16x16x32_bf16 v[56:59], v[168:171], v[4:7], v[56:59]\n"
    "v_cvt_pk_bf16_f32 v151, v86, v87\n"
    "v_cvt_pk_bf16_f32 v154, v88, v89\n"
    "v_mfma_f32_16x16x32_bf16 v[60:63], v[168:171], v[28:31], v[108:111]\n"
    "buffer_load_dwordx4 v200, s[36:39], s60 offen offset:2048 sc1 lds\n"
    "v_mfma_f32_16x16x32_bf16 v[60:63], v[164:167], v[24:27], v[60:63]\n"
    "v_exp_f32 v94, v94\n"
    "v_mfma_f32_16x16x32_bf16 v[60:63], v[160:163], v[20:23], v[60:63]\n"
    "s_waitcnt lgkmcnt(1)\n"
    "s_add_u32 m0, s71, 0xc000\n"
    "v_cvt_pk_bf16_f32 v155, v90, v91\n"
    "v_mfma_f32_16x16x32_bf16 v[60:63], v[172:175], v[8:11], v[60:63]\n"
    "ds_read_b128 v[160:163], v201 offset:24576\n"
    "v_cvt_pk_bf16_f32 v158, v92, v93\n"
    "v_mfma_f32_16x16x32_bf16 v[60:63], v[176:179], v[12:15], v[60:63]\n"
    "s_waitcnt lgkmcnt(1)\n"
    "buffer_load_dwordx4 v200, s[40:43], s61 offen sc1 lds\n"
    "v_mfma_f32_16x16x32_bf16 v[60:63], v[180:183], v[16:19], v[60:63]\n"
    "v_exp_f32 v95, v95\n"
    "v_mfma_f32_16x16x32_bf16 v[56:59], v[180:183], a[248:251], v[56:59]\n"
    "ds_read_b128 v[164:167], v201 offset:25600\n"
    "v_cvt_pk_bf16_f32 v159, v94, v95\n"
    "v_mfma_f32_16x16x32_bf16 v[56:59], v[176:179], a[244:247], v[56:59]\n"
    "v_exp_f32 v32, v32\n"
    "v_mfma_f32_16x16x32_bf16 v[56:59], v[172:175], a[240:243], v[56:59]\n"
    "buffer_load_dwordx4 v200, s[40:43], s61 offen offset:1024 sc1 lds\n"
    "v_mfma_f32_16x16x32_bf16 v[52:55], v[172:175], a[216:219], v[52:55]\n"
    "ds_read_b128 v[168:171], v201 offset:26624\n"
    "v_add_f32 v113, v113, v64\n"
    "v_mfma_f32_16x16x32_bf16 v[52:55], v[176:179], a[220:223], v[52:55]\n"
    "v_exp_f32 v33, v33\n"
    "v_mfma_f32_16x16x32_bf16 v[52:55], v[180:183], a[224:227], v[52:55]\n"
    "v_exp_f32 v34, v34\n"
    "v_mfma_f32_16x16x32_bf16 v[48:51], v[180:183], a[200:203], v[48:51]\n"
    "buffer_load_dwordx4 v200, s[40:43], s61 offen offset:2048 sc1 lds\n"
    "v_mfma_f32_16x16x32_bf16 v[48:51], v[176:179], a[196:199], v[48:51]\n"
    "v_exp_f32 v35, v35\n"
    "v_mfma_f32_16x16x32_bf16 v[48:51], v[172:175], a[192:195], v[48:51]\n"
    "s_waitcnt lgkmcnt(1)\n"
    "v_exp_f32 v36, v36\n"
    "v_mfma_f32_16x16x32_bf16 a[0:3], v[160:163], v[128:131], a[0:3]\n"
    "ds_read_b128 v[172:175], v201 offset:27648\n"
    "ds_read_b128 v[176:179], v201 offset:28672\n"
    "v_mfma_f32_16x16x32_bf16 a[0:3], v[164:167], v[144:147], a[0:3]\n"
    "buffer_load_dwordx4 v200, s[40:43], s61 offen offset:3072 sc1 lds\n"
    "v_mfma_f32_16x16x32_bf16 a[4:7], v[160:163], v[132:135], a[4:7]\n"
    "s_add_u32 s65, s61, 0x1000\n"
    "v_exp_f32 v37, v37\n"
    "v_mfma_f32_16x16x32_bf16 a[4:7], v[164:167], v[148:151], a[4:7]\n"
    "s_add_u32 m0, s71, 0xd000\n"
    "v_exp_f32 v38, v38\n"
    "v_mfma_f32_16x16x32_bf16 a[8:11], v[160:163], v[136:139], a[8:11]\n"
    "ds_read_b128 v[180:183], v201 offset:29696\n"
    "v_add_f32 v114, v114, v65\n"
    "v_mfma_f32_16x16x32_bf16 a[8:11], v[164:167], v[152:155], a[8:11]\n"
    "buffer_load_dwordx4 v200, s[40:43], s65 offen sc1 lds\n"
    "v_mfma_f32_16x16x32_bf16 a[12:15], v[160:163], v[140:143], a[12:15]\n"
    "v_exp_f32 v39, v39\n"
    "v_mfma_f32_16x16x32_bf16 a[12:15], v[164:167], v[156:159], a[12:15]\n"
    "s_waitcnt lgkmcnt(2)\n"
    "v_exp_f32 v40, v40\n"
    "v_mfma_f32_16x16x32_bf16 a[28:31], v[168:171], v[140:143], a[28:31]\n"
    "ds_read_b128 v[160:163], v201 offset:30720\n"
    "v_add_f32 v113, v113, v66\n"
    "v_mfma_f32_16x16x32_bf16 a[28:31], v[172:175], v[156:159], a[28:31]\n"
    "buffer_load_dwordx4 v200, s[40:43], s65 offen offset:1024 sc1 lds\n"
    "v_mfma_f32_16x16x32_bf16 a[24:27], v[172:175], v[152:155], a[24:27]\n"
    "v_exp_f32 v41, v41\n"
    "v_mfma_f32_16x16x32_bf16 a[24:27], v[168:171], v[136:139], a[24:27]\n"
    "v_exp_f32 v42, v42\n"
    "v_mfma_f32_16x16x32_bf16 a[20:23], v[168:171], v[132:135], a[20:23]\n"
    "ds_read_b128 v[164:167], v201 offset:31744\n"
    "v_add_f32 v114, v114, v67\n"
    "v_mfma_f32_16x16x32_bf16 a[20:23], v[172:175], v[148:151], a[20:23]\n"
    "v_exp_f32 v43, v43\n"
    "v_mfma_f32_16x16x32_bf16 a[16:19], v[172:175], v[144:147], a[16:19]\n"
    "v_exp_f32 v44, v44\n"
    "v_mfma_f32_16x16x32_bf16 a[16:19], v[168:171], v[128:131], a[16:19]\n"
    "s_waitcnt lgkmcnt(2)\n"
    "v_exp_f32 v45, v45\n"
    "v_mfma_f32_16x16x32_bf16 a[32:35], v[176:179], v[128:131], a[32:35]\n"
    "ds_read_b128 v[168:171], v201 offset:32768\n"
    "v_add_f32 v117, v117, v68\n"
    "v_mfma_f32_16x16x32_bf16 a[32:35], v[180:183], v[144:147], a[32:35]\n"
    "buffer_load_dwordx2 v[218:219], v213, s[44:47], 0 offen sc1\n"
    "v_mfma_f32_16x16x32_bf16 a[36:39], v[180:183], v[148:151], a[36:39]\n"
    "v_exp_f32 v46, v46\n"
    "v_mfma_f32_16x16x32_bf16 a[36:39], v[176:179], v[132:135], a[36:39]\n"
    "v_exp_f32 v47, v47\n"
    "v_mfma_f32_16x16x32_bf16 a[40:43], v[176:179], v[136:139], a[40:43]\n"
    "ds_read_b128 v[172:175], v201 offset:33792\n"
    "v_add_f32 v118, v118, v69\n"
    "v_mfma_f32_16x16x32_bf16 a[40:43], v[180:183], v[152:155], a[40:43]\n"
    "v_exp_f32 v48, v48\n"
    "v_mfma_f32_16x16x32_bf16 a[44:47], v[180:183], v[156:159], a[44:47]\n"
    "v_exp_f32 v49, v49\n"
    "v_mfma_f32_16x16x32_bf16 a[44:47], v[176:179], v[140:143], a[44:47]\n"
    "s_waitcnt lgkmcnt(2)\n"
    "v_exp_f32 v50, v50\n"
    "v_mfma_f32_16x16x32_bf16 a[60:63], v[160:163], v[140:143], a[60:63]\n"
    "ds_read_b128 v[176:179], v201 offset:34816\n"
    "v_add_f32 v117, v117, v70\n"
    "v_mfma_f32_16x16x32_bf16 a[60:63], v[164:167], v[156:159], a[60:63]\n"
    "v_exp_f32 v51, v51\n"
    "v_mfma_f32_16x16x32_bf16 a[56:59], v[164:167], v[152:155], a[56:59]\n"
    "v_add_f32 v113, v113, v32\n"
    "v_add_f32 v118, v118, v71\n"
    "v_mfma_f32_16x16x32_bf16 a[56:59], v[160:163], v[136:139], a[56:59]\n"
    "v_exp_f32 v52, v52\n"
    "v_mfma_f32_16x16x32_bf16 a[52:55], v[160:163], v[132:135], a[52:55]\n"
    "ds_read_b128 v[180:183], v201 offset:35840\n"
    "v_add_f32 v114, v114, v33\n"
    "v_mfma_f32_16x16x32_bf16 a[52:55], v[164:167], v[148:151], a[52:55]\n"
    "v_add_f32 v121, v121, v72\n"
    "v_add_f32 v113, v113, v34\n"
    "v_mfma_f32_16x16x32_bf16 a[48:51], v[164:167], v[144:147], a[48:51]\n"
    "v_exp_f32 v53, v53\n"
    "v_mfma_f32_16x16x32_bf16 a[48:51], v[160:163], v[128:131], a[48:51]\n"
    "s_waitcnt lgkmcnt(2)\n"
    "v_add_f32 v122, v122, v73\n"
    "v_add_f32 v114, v114, v35\n"
    "v_mfma_f32_16x16x32_bf16 a[64:67], v[168:171], v[128:131], a[64:67]\n"
    "ds_read_b128 v[160:163], v201 offset:36864\n"
    "v_add_f32 v121, v121, v74\n"
    "v_mfma_f32_16x16x32_bf16 a[64:67], v[172:175], v[144:147], a[64:67]\n"
    "v_add_f32 v117, v117, v36\n"
    "v_add_f32 v122, v122, v75\n"
    "v_mfma_f32_16x16x32_bf16 a[68:71], v[172:175], v[148:151], a[68:71]\n"
    "v_exp_f32 v54, v54\n"
    "v_mfma_f32_16x16x32_bf16 a[68:71], v[168:171], v[132:135], a[68:71]\n"
    "s_add_u32 m0, s51, s95\n"
    "v_add_f32 v118, v118, v37\n"
    "v_add_f32 v125, v125, v76\n"
    "v_mfma_f32_16x16x32_bf16 a[72:75], v[168:171], v[136:139], a[72:75]\n"
    "ds_read_b128 v[164:167], v201 offset:37888\n"
    "v_add_f32 v117, v117, v38\n"
    "v_mfma_f32_16x16x32_bf16 a[72:75], v[172:175], v[152:155], a[72:75]\n"
    "buffer_load_dwordx4 v202, s[32:35], s64 offen lds\n"
    "v_mfma_f32_16x16x32_bf16 a[76:79], v[172:175], v[156:159], a[76:79]\n"
    "v_exp_f32 v55, v55\n"
    "v_mfma_f32_16x16x32_bf16 a[76:79], v[168:171], v[140:143], a[76:79]\n"
    "s_waitcnt lgkmcnt(2)\n"
    "v_add_f32 v126, v126, v77\n"
    "v_add_f32 v118, v118, v39\n"
    "v_mfma_f32_16x16x32_bf16 a[92:95], v[176:179], v[140:143], a[92:95]\n"
    "ds_read_b128 v[168:171], v201 offset:38912\n"
    "v_add_f32 v125, v125, v78\n"
    "v_mfma_f32_16x16x32_bf16 a[92:95], v[180:183], v[156:159], a[92:95]\n"
    "buffer_load_dwordx4 v203, s[32:35], s64 offen offset:1024 lds\n"
    "v_mfma_f32_16x16x32_bf16 a[88:91], v[180:183], v[152:155], a[88:91]\n"
    "v_add_f32 v121, v121, v40\n"
    "v_add_f32 v126, v126, v79\n"
    "v_mfma_f32_16x16x32_bf16 a[88:91], v[176:179], v[136:139], a[88:91]\n"
    "v_exp_f32 v56, v56\n"
    "v_mfma_f32_16x16x32_bf16 a[84:87], v[176:179], v[132:135], a[84:87]\n"
    "ds_read_b128 v[172:175], v201 offset:39936\n"
    "v_add_f32 v122, v122, v41\n"
    "v_mfma_f32_16x16x32_bf16 a[84:87], v[180:183], v[148:151], a[84:87]\n"
    "buffer_load_dwordx4 v204, s[32:35], s64 offen offset:2048 lds\n"
    "v_mfma_f32_16x16x32_bf16 a[80:83], v[180:183], v[144:147], a[80:83]\n"
    "v_add_f32 v113, v113, v80\n"
    "v_add_f32 v121, v121, v42\n"
    "v_mfma_f32_16x16x32_bf16 a[80:83], v[176:179], v[128:131], a[80:83]\n"
    "s_waitcnt lgkmcnt(2)\n"
    "v_exp_f32 v57, v57\n"
    "v_mfma_f32_16x16x32_bf16 a[96:99], v[160:163], v[128:131], a[96:99]\n"
    "s_mov_b32 s95, s94\n"
    "ds_read_b128 v[176:179], v201 offset:40960\n"
    "v_add_f32 v114, v114, v81\n"
    "v_mfma_f32_16x16x32_bf16 a[96:99], v[164:167], v[144:147], a[96:99]\n"
    "s_sub_u32 s94, s94, 0x3000\n"
    "v_add_f32 v122, v122, v43\n"
    "v_add_f32 v113, v113, v82\n"
    "v_mfma_f32_16x16x32_bf16 a[100:103], v[164:167], v[148:151], a[100:103]\n"
    "s_min_u32 s94, s94, 0x6000\n"
    "v_add_f32 v125, v125, v44\n"
    "v_add_f32 v114, v114, v83\n"
    "v_mfma_f32_16x16x32_bf16 a[100:103], v[160:163], v[132:135], a[100:103]\n"
    "s_sub_u32 s93, s94, s95\n"
    "v_exp_f32 v58, v58\n"
    "v_mfma_f32_16x16x32_bf16 a[104:107], v[160:163], v[136:139], a[104:107]\n"
    "ds_read_b128 v[180:183], v201 offset:41984\n"
    "v_add_f32 v126, v126, v45\n"
    "v_mfma_f32_16x16x32_bf16 a[104:107], v[164:167], v[152:155], a[104:107]\n"
    "v_add_f32 v117, v117, v84\n"
    "v_add_f32 v125, v125, v46\n"
    "v_mfma_f32_16x16x32_bf16 a[108:111], v[164:167], v[156:159], a[108:111]\n"
    "v_exp_f32 v59, v59\n"
    "v_mfma_f32_16x16x32_bf16 a[108:111], v[160:163], v[140:143], a[108:111]\n"
    "s_waitcnt lgkmcnt(2)\n"
    "v_add_f32 v118, v118, v85\n"
    "v_add_f32 v126, v126, v47\n"
    "v_mfma_f32_16x16x32_bf16 a[124:127], v[168:171], v[140:143], a[124:127]\n"
    "ds_read_b128 v[160:163], v201 offset:43008\n"
    "v_add_f32 v117, v117, v86\n"
    "v_mfma_f32_16x16x32_bf16 a[124:127], v[172:175], v[156:159], a[124:127]\n"
    "v_add_f32 v113, v113, v48\n"
    "v_add_u32 v205, s93, v205\n"
    "v_mfma_f32_16x16x32_bf16 a[120:123], v[172:175], v[152:155], a[120:123]\n"
    "v_exp_f32 v60, v60\n"
    "v_mfma_f32_16x16x32_bf16 a[120:123], v[168:171], v[136:139], a[120:123]\n"
    "v_add_f32 v118, v118, v87\n"
    "v_add_f32 v114, v114, v49\n"
    "v_mfma_f32_16x16x32_bf16 a[116:119], v[168:171], v[132:135], a[116:119]\n"
    "ds_read_b128 v[164:167], v201 offset:44032\n"
    "v_add_u32 v206, s93, v206\n"
    "v_mfma_f32_16x16x32_bf16 a[116:119], v[172:175], v[148:151], a[116:119]\n"
    "v_add_f32 v121, v121, v88\n"
    "v_add_f32 v113, v113, v50\n"
    "v_mfma_f32_16x16x32_bf16 a[112:115], v[172:175], v[144:147], a[112:115]\n"
    "v_exp_f32 v61, v61\n"
    "v_mfma_f32_16x16x32_bf16 a[112:115], v[168:171], v[128:131], a[112:115]\n"
    "s_waitcnt lgkmcnt(2)\n"
    "v_add_u32 v220, 1, v220\n"
    "v_add_u32 v207, s93, v207\n"
    "v_mfma_f32_16x16x32_bf16 a[128:131], v[176:179], v[128:131], a[128:131]\n"
    "s_add_u32 s64, s64, 0xc000\n"
    "ds_read_b128 v[168:171], v201 offset:45056\n"
    "v_add_f32 v122, v122, v89\n"
    "v_mfma_f32_16x16x32_bf16 a[128:131], v[180:183], v[144:147], a[128:131]\n"
    "s_add_u32 s62, s62, 0x6000\n"
    "v_add_f32 v114, v114, v51\n"
    "v_add_f32 v121, v121, v90\n"
    "v_mfma_f32_16x16x32_bf16 a[132:135], v[180:183], v[148:151], a[132:135]\n"
    "s_add_u32 s63, s63, 0x6000\n"
    "v_add_f32 v117, v117, v52\n"
    "v_add_f32 v122, v122, v91\n"
    "v_mfma_f32_16x16x32_bf16 a[132:135], v[176:179], v[132:135], a[132:135]\n"
    "s_add_u32 s60, s60, 0x6000\n"
    "v_exp_f32 v62, v62\n"
    "v_mfma_f32_16x16x32_bf16 a[136:139], v[176:179], v[136:139], a[136:139]\n"
    "s_add_u32 s61, s61, 0x6000\n"
    "ds_read_b128 v[172:175], v201 offset:46080\n"
    "v_add_f32 v118, v118, v53\n"
    "v_mfma_f32_16x16x32_bf16 a[136:139], v[180:183], v[152:155], a[136:139]\n"
    "s_add_u32 s66, s66, s74\n"
    "v_add_f32 v125, v125, v92\n"
    "v_add_f32 v117, v117, v54\n"
    "v_mfma_f32_16x16x32_bf16 a[140:143], v[180:183], v[156:159], a[140:143]\n"
    "s_min_u32 s66, s66, 33\n"
    "v_exp_f32 v63, v63\n"
    "v_mfma_f32_16x16x32_bf16 a[140:143], v[176:179], v[140:143], a[140:143]\n"
    "s_waitcnt lgkmcnt(2)\n"
    "v_add_f32 v126, v126, v93\n"
    "v_add_f32 v118, v118, v55\n"
    "v_mfma_f32_16x16x32_bf16 a[156:159], v[160:163], v[140:143], a[156:159]\n"
    "ds_read_b128 v[176:179], v201 offset:47104\n"
    "v_add_f32 v125, v125, v94\n"
    "v_mfma_f32_16x16x32_bf16 a[156:159], v[164:167], v[156:159], a[156:159]\n"
    "s_waitcnt vmcnt(3)\n"
    "ds_read_b128 v[180:183], v201 offset:48128\n"
    "v_add_f32 v121, v121, v56\n"
    "v_mfma_f32_16x16x32_bf16 a[152:155], v[164:167], v[152:155], a[152:155]\n"
    "v_cmp_ne_u32 s[80:81], s67, v219\n"
    "v_add_f32 v126, v126, v95\n"
    "v_mfma_f32_16x16x32_bf16 a[152:155], v[160:163], v[136:139], a[152:155]\n"
    "v_cmp_gt_u32 vcc, s66, v218\n"
    "v_add_f32 v122, v122, v57\n"
    "v_mfma_f32_16x16x32_bf16 a[148:151], v[160:163], v[132:135], a[148:151]\n"
    "v_mfma_f32_16x16x32_bf16 a[148:151], v[164:167], v[148:151], a[148:151]\n"
    "s_or_b64 vcc, vcc, s[80:81]\n"
    "s_cbranch_vccnz LSPRE3\n"
    "LSPB3:\n"
    "v_mfma_f32_16x16x32_bf16 a[144:147], v[164:167], v[144:147], a[144:147]\n"
    "v_mfma_f32_16x16x32_bf16 a[144:147], v[160:163], v[128:131], a[144:147]\n"
    "s_waitcnt lgkmcnt(0)\n"
    "v_mfma_f32_16x16x32_bf16 a[160:163], v[168:171], v[128:131], a[160:163]\n"
    "s_barrier\n"
    "LMID:\n"
    "v_mfma_f32_16x16x32_bf16 a[160:163], v[172:175], v[144:147], a[160:163]\n"
    "buffer_store_dwordx2 v[220:221], v214, s[44:47], 0 offen sc1\n"
    "v_mfma_f32_16x16x32_bf16 a[164:167], v[172:175], v[148:151], a[164:167]\n"
    "ds_read_b128 v[160:163], v200 offset:24576\n"
    "ds_read_u16_d16_hi v223, v209 offset:6336\n"
    "v_mfma_f32_16x16x32_bf16 a[164:167], v[168:171], v[132:135], a[164:167]\n"
    "ds_read_u16_d16_hi v222, v209 offset:192\n"
    "v_add_f32 v121, v121, v58\n"
    "v_mfma_f32_16x16x32_bf16 a[168:171], v[168:171], v[136:139], a[168:171]\n"
    "ds_read_b128 v[164:167], v200 offset:25600\n"
    "ds_read_b128 v[232:235], v205 offset:24576\n"
    "v_mfma_f32_16x16x32_bf16 a[168:171], v[172:175], v[152:155], a[168:171]\n"
    "v_add_f32 v122, v122, v59\n"
    "ds_read_b128 v[236:239], v206 offset:24576\n"
    "v_mfma_f32_16x16x32_bf16 a[172:175], v[172:175], v[156:159], a[172:175]\n"
    "v_add_f32 v125, v125, v60\n"
    "v_add_f32 v126, v126, v61\n"
    "v_mfma_f32_16x16x32_bf16 a[172:175], v[168:171], v[140:143], a[172:175]\n"
    "v_add_f32 v125, v125, v62\n"
    "v_add_f32 v126, v126, v63\n"
    "v_mfma_f32_16x16x32_bf16 a[188:191], v[176:179], v[140:143], a[188:191]\n"
    "ds_read_b128 v[168:171], v200 offset:26624\n"
    "ds_read_b64 v[228:229], v210 offset:64\n"
    "v_mfma_f32_16x16x32_bf16 a[188:191], v[180:183], v[156:159], a[188:191]\n"
    "ds_read2st64_b32 v[240:241], v207 offset0:96 offset1:99\n"
    "ds_read2st64_b32 v[242:243], v207 offset0:102 offset1:105\n"
    "v_mfma_f32_16x16x32_bf16 a[184:187], v[176:179], v[136:139], a[184:187]\n"
    "ds_read2st64_b32 v[244:245], v207 offset0:108 offset1:111\n"
    "ds_read2st64_b32 v[246:247], v207 offset0:114 offset1:117\n"
    "v_mfma_f32_16x16x32_bf16 a[184:187], v[180:183], v[152:155], a[184:187]\n"
    "v_cvt_pk_bf16_f32 v140, v44, v45\n"
    "v_cvt_pk_bf16_f32 v141, v46, v47\n"
    "v_mfma_f32_16x16x32_bf16 a[180:183], v[176:179], v[132:135], a[180:183]\n"
    "s_add_u32 s59, s59, 1\n"
    "ds_read_b128 v[172:175], v200 offset:27648\n"
    "s_waitcnt lgkmcnt(7)\n"
    "v_mfma_f32_16x16x32_bf16 a[180:183], v[180:183], v[148:151], a[180:183]\n"
    "v_mul_f32 v226, s75, v223\n"
    "v_fma_f32 v227, -v222, v226, v217\n"
    "v_mfma_f32_16x16x32_bf16 a[176:179], v[176:179], v[128:131], a[176:179]\n"
    "s_cmp_lt_u32 s59, 15\n"
    "v_cvt_f32_i32 v232, v232\n"
    "v_cvt_f32_i32 v233, v233\n"
    "v_mfma_f32_16x16x32_bf16 a[176:179], v[180:183], v[144:147], a[176:179]\n"
    "s_cbranch_scc1 LLOOP\n"
    "LTAIL:\n"
    "s_waitcnt lgkmcnt(0)\n"
    "s_nop 1\n"
    "buffer_load_dword v251, v252, s[52:55], s91 offen\n"
    "v_mfma_f32_16x16x32_bf16 v[64:67], v[160:163], a[192:195], v[96:99]\n"
    "ds_read_b128 v[176:179], v200 offset:28672\n"
    "v_cvt_pk_bf16_f32 v128, v32, v33\n"
    "v_mfma_f32_16x16x32_bf16 v[64:67], v[164:167], a[196:199], v[64:67]\n"
    "s_add_u32 m0, s71, 0\n"
    "v_cvt_pk_bf16_f32 v129, v34, v35\n"
    "v_cvt_pk_bf16_f32 v132, v36, v37\n"
    "v_mfma_f32_16x16x32_bf16 v[68:71], v[164:167], a[220:223], v[100:103]\n"
    "buffer_load_dwordx4 v200, s[36:39], s60 offen sc1 lds\n"
    "v_mfma_f32_16x16x32_bf16 v[68:71], v[160:163], a[216:219], v[68:71]\n"
    "v_cvt_pk_bf16_f32 v133, v38, v39\n"
    "v_cvt_pk_bf16_f32 v136, v40, v41\n"
    "v_mfma_f32_16x16x32_bf16 v[72:75], v[160:163], a[240:243], v[104:107]\n"
    "ds_read_b128 v[180:183], v200 offset:29696\n"
    "v_cvt_pk_bf16_f32 v137, v42, v43\n"
    "v_mfma_f32_16x16x32_bf16 v[72:75], v[164:167], a[244:247], v[72:75]\n"
    "v_cvt_pk_bf16_f32 v130, v48, v49\n"
    "v_cvt_pk_bf16_f32 v131, v50, v51\n"
    "v_mfma_f32_16x16x32_bf16 v[76:79], v[164:167], v[12:15], v[108:111]\n"
    "buffer_load_dwordx4 v200, s[36:39], s60 offen offset:1024 sc1 lds\n"
    "v_mfma_f32_16x16x32_bf16 v[76:79], v[160:163], v[8:11], v[76:79]\n"
    "v_cvt_pk_bf16_f32 v134, v52, v53\n"
    "v_cvt_pk_bf16_f32 v135, v54, v55\n"
    "v_mfma_f32_16x16x32_bf16 v[76:79], v[168:171], v[16:19], v[76:79]\n"
    "ds_read_b128 v[160:163], v200 offset:34816\n"
    "v_cvt_pk_bf16_f32 v138, v56, v57\n"
    "v_mfma_f32_16x16x32_bf16 v[76:79], v[172:175], v[20:23], v[76:79]\n"
    "v_cvt_pk_bf16_f32 v139, v58, v59\n"
    "v_cvt_pk_bf16_f32 v142, v60, v61\n"
    "v_mfma_f32_16x16x32_bf16 v[72:75], v[172:175], a[252:255], v[72:75]\n"
    "buffer_load_dwordx4 v200, s[36:39], s60 offen offset:2048 sc1 lds\n"
    "v_mfma_f32_16x16x32_bf16 v[72:75], v[168:171], a[248:251], v[72:75]\n"
    "v_cvt_pk_bf16_f32 v143, v62, v63\n"
    "v_add_f32 v113, v113, v114\n"
    "v_mfma_f32_16x16x32_bf16 v[68:71], v[168:171], a[224:227], v[68:71]\n"
    "ds_read_b128 v[164:167], v200 offset:35840\n"
    "v_add_f32 v117, v117, v118\n"
    "v_mfma_f32_16x16x32_bf16 v[68:71], v[172:175], a[228:231], v[68:71]\n"
    "v_add_f32 v121, v121, v122\n"
    "v_add_f32 v125, v125, v126\n"
    "v_mfma_f32_16x16x32_bf16 v[64:67], v[172:175], a[204:207], v[64:67]\n"
    "buffer_load_dwordx4 v200, s[36:39], s60 offen offset:3072 sc1 lds\n"
    "v_mfma_f32_16x16x32_bf16 v[64:67], v[168:171], a[200:203], v[64:67]\n"
    "s_waitcnt lgkmcnt(2)\n"
    "v_mov_b32 v115, v113\n"
    "v_mov_b32 v119, v117\n"
    "v_mfma_f32_16x16x32_bf16 v[64:67], v[176:179], a[208:211], v[64:67]\n"
    "s_add_u32 s65, s60, 0x1000\n"
    "ds_read_b128 v[168:171], v200 offset:32768\n"
    "v_mov_b32 v123, v121\n"
    "v_mfma_f32_16x16x32_bf16 v[64:67], v[180:183], a[212:215], v[64:67]\n"
    "s_add_u32 m0, s71, 0x1000\n"
    "v_mov_b32 v127, v125\n"
    "v_permlane32_swap_b32 v113, v115\n"
    "v_mfma_f32_16x16x32_bf16 v[68:71], v[180:183], a[236:239], v[68:71]\n"
    "buffer_load_dwordx4 v200, s[36:39], s65 offen sc1 lds\n"
    "v_mfma_f32_16x16x32_bf16 v[68:71], v[176:179], a[232:235], v[68:71]\n"
    "v_permlane32_swap_b32 v117, v119\n"
    "v_permlane32_swap_b32 v121, v123\n"
    "v_mfma_f32_16x16x32_bf16 v[72:75], v[176:179], v[0:3], v[72:75]\n"
    "ds_read_b128 v[172:175], v200 offset:33792\n"
    "v_permlane32_swap_b32 v125, v127\n"
    "v_mfma_f32_16x16x32_bf16 v[72:75], v[180:183], v[4:7], v[72:75]\n"
    "v_add_f32 v113, v113, v115\n"
    "v_add_f32 v117, v117, v119\n"
    "v_mfma_f32_16x16x32_bf16 v[76:79], v[180:183], v[28:31], v[76:79]\n"
    "buffer_load_dwordx4 v200, s[36:39], s65 offen offset:1024 sc1 lds\n"
    "v_mfma_f32_16x16x32_bf16 v[76:79], v[176:179], v[24:27], v[76:79]\n"
    "s_waitcnt lgkmcnt(2)\n"
    "v_add_f32 v121, v121, v123\n"
    "v_add_f32 v125, v125, v127\n"
    "v_mfma_f32_16x16x32_bf16 v[92:95], v[160:163], v[24:27], v[108:111]\n"
    "ds_read_b128 v[176:179], v200 offset:30720\n"
    "v_mov_b32 v115, v113\n"
    "v_mfma_f32_16x16x32_bf16 v[92:95], v[164:167], v[28:31], v[92:95]\n"
    "s_add_u32 m0, s71, 0x12000\n"
    "v_exp_f32 v64, v64\n"
    "v_mfma_f32_16x16x32_bf16 v[88:91], v[164:167], v[4:7], v[104:107]\n"
    "buffer_load_dwordx4 v200, s[40:43], s61 offen sc1 lds\n"
    "v_mfma_f32_16x16x32_bf16 v[88:91], v[160:163], v[0:3], v[88:91]\n"
    "v_exp_f32 v65, v65\n"
    "v_mfma_f32_16x16x32_bf16 v[84:87], v[160:163], a[232:235], v[100:103]\n"
    "ds_read_b128 v[180:183], v200 offset:31744\n"
    "v_cvt_pk_bf16_f32 v144, v64, v65\n"
    "v_mfma_f32_16x16x32_bf16 v[84:87], v[164:167], a[236:239], v[84:87]\n"
    "v_exp_f32 v66, v66\n"
    "v_mfma_f32_16x16x32_bf16 v[80:83], v[164:167], a[212:215], v[96:99]\n"
    "buffer_load_dwordx4 v200, s[40:43], s61 offen offset:1024 sc1 lds\n"
    "v_mfma_f32_16x16x32_bf16 v[80:83], v[160:163], a[208:211], v[80:83]\n"
    "s_waitcnt lgkmcnt(2)\n"
    "v_exp_f32 v67, v67\n"
    "v_mfma_f32_16x16x32_bf16 v[80:83], v[168:171], a[200:203], v[80:83]\n"
    "ds_read_b128 v[160:163], v200 offset:36864\n"
    "v_cvt_pk_bf16_f32 v145, v66, v67\n"
    "v_mfma_f32_16x16x32_bf16 v[80:83], v[172:175], a[204:207], v[80:83]\n"
    "v_exp_f32 v68, v68\n"
    "v_mfma_f32_16x16x32_bf16 v[84:87], v[172:175], a[228:231], v[84:87]\n"
    "buffer_load_dwordx4 v200, s[40:43], s61 offen offset:2048 sc1 lds\n"
    "v_mfma_f32_16x16x32_bf16 v[84:87], v[168:171], a[224:227], v[84:87]\n"
    "v_exp_f32 v69, v69\n"
    "v_mfma_f32_16x16x32_bf16 v[88:91], v[168:171], a[248:251], v[88:91]\n"
    "ds_read_b128 v[164:167], v200 offset:37888\n"
    "v_cvt_pk_bf16_f32 v148, v68, v69\n"
    "v_mfma_f32_16x16x32_bf16 v[88:91], v[172:175], a[252:255], v[88:91]\n"
    "v_exp_f32 v70, v70\n"
    "v_mfma_f32_16x16x32_bf16 v[92:95], v[172:175], v[20:23], v[92:95]\n"
    "buffer_load_dwordx4 v200, s[40:43], s61 offen offset:3072 sc1 lds\n"
    "v_mfma_f32_16x16x32_bf16 v[92:95], v[168:171], v[16:19], v[92:95]\n"
    "s_waitcnt lgkmcnt(2)\n"
    "v_exp_f32 v71, v71\n"
    "v_mfma_f32_16x16x32_bf16 v[92:95], v[176:179], v[8:11], v[92:95]\n"
    "s_add_u32 s65, s61, 0x1000\n"
    "ds_read_b128 v[168:171], v200 offset:38912\n"
    "v_cvt_pk_bf16_f32 v149, v70, v71\n"
    "v_mfma_f32_16x16x32_bf16 v[92:95], v[180:183], v[12:15], v[92:95]\n"
    "s_add_u32 m0, s71, 0x13000\n"
    "v_exp_f32 v72, v72\n"
    "v_mfma_f32_16x16x32_bf16 v[88:91], v[180:183], a[244:247], v[88:91]\n"
    "buffer_load_dwordx4 v200, s[40:43], s65 offen sc1 lds\n"
    "v_mfma_f32_16x16x32_bf16 v[88:91], v[176:179], a[240:243], v[88:91]\n"
    "v_exp_f32 v73, v73\n"
    "v_mfma_f32_16x16x32_bf16 v[84:87], v[176:179], a[216:219], v[84:87]\n"
    "ds_read_b128 v[172:175], v200 offset:39936\n"
    "v_cvt_pk_bf16_f32 v152, v72, v73\n"
    "v_mfma_f32_16x16x32_bf16 v[84:87], v[180:183], a[220:223], v[84:87]\n"
    "v_exp_f32 v74, v74\n"
    "v_mfma_f32_16x16x32_bf16 v[80:83], v[180:183], a[196:199], v[80:83]\n"
    "buffer_load_dwordx4 v200, s[40:43], s65 offen offset:1024 sc1 lds\n"
    "v_mfma_f32_16x16x32_bf16 v[80:83], v[176:179], a[192:195], v[80:83]\n"
    "s_waitcnt lgkmcnt(2)\n"
    "v_exp_f32 v75, v75\n"
    "v_mfma_f32_16x16x32_bf16 v[32:35], v[160:163], a[192:195], v[96:99]\n"
    "ds_read_b128 v[176:179], v200 offset:40960\n"
    "v_cvt_pk_bf16_f32 v153, v74, v75\n"
    "v_mfma_f32_16x16x32_bf16 v[32:35], v[164:167], a[196:199], v[32:35]\n"
    "v_exp_f32 v76, v76\n"
    "v_mfma_f32_16x16x32_bf16 v[36:39], v[164:167], a[220:223], v[100:103]\n"
    "v_exp_f32 v77, v77\n"
    "v_mfma_f32_16x16x32_bf16 v[36:39], v[160:163], a[216:219], v[36:39]\n"
    "v_exp_f32 v78, v78\n"
    "v_mfma_f32_16x16x32_bf16 v[40:43], v[160:163], a[240:243], v[104:107]\n"
    "ds_read_b128 v[180:183], v200 offset:41984\n"
    "v_cvt_pk_bf16_f32 v156, v76, v77\n"
    "v_mfma_f32_16x16x32_bf16 v[40:43], v[164:167], a[244:247], v[40:43]\n"
    "v_exp_f32 v79, v79\n"
    "v_mfma_f32_16x16x32_bf16 v[44:47], v[164:167], v[12:15], v[108:111]\n"
    "v_exp_f32 v80, v80\n"
    "v_mfma_f32_16x16x32_bf16 v[44:47], v[160:163], v[8:11], v[44:47]\n"
    "s_waitcnt lgkmcnt(2)\n"
    "v_exp_f32 v81, v81\n"
    "v_mfma_f32_16x16x32_bf16 v[44:47], v[168:171], v[16:19], v[44:47]\n"
    "ds_read_b128 v[160:163], v200 offset:47104\n"
    "v_cvt_pk_bf16_f32 v157, v78, v79\n"
    "v_mfma_f32_16x16x32_bf16 v[44:47], v[172:175], v[20:23], v[44:47]\n"
    "v_exp_f32 v82, v82\n"
    "v_mfma_f32_16x16x32_bf16 v[40:43], v[172:175], a[252:255], v[40:43]\n"
    "v_exp_f32 v83, v83\n"
    "v_mfma_f32_16x16x32_bf16 v[40:43], v[168:171], a[248:251], v[40:43]\n"
    "v_exp_f32 v84, v84\n"
    "v_mfma_f32_16x16x32_bf16 v[36:39], v[168:171], a[224:227], v[36:39]\n"
    "ds_read_b128 v[164:167], v200 offset:48128\n"
    "v_cvt_pk_bf16_f32 v146, v80, v81\n"
    "v_mfma_f32_16x16x32_bf16 v[36:39], v[172:175], a[228:231], v[36:39]\n"
    "v_exp_f32 v85, v85\n"
    "v_mfma_f32_16x16x32_bf16 v[32:35], v[172:175], a[204:207], v[32:35]\n"
    "v_exp_f32 v86, v86\n"
    "v_mfma_f32_16x16x32_bf16 v[32:35], v[168:171], a[200:203], v[32:35]\n"
    "s_waitcnt lgkmcnt(2)\n"
    "v_exp_f32 v87, v87\n"
    "v_mfma_f32_16x16x32_bf16 v[32:35], v[176:179], a[208:211], v[32:35]\n"
    "ds_read_b128 v[168:171], v200 offset:45056\n"
    "v_cvt_pk_bf16_f32 v147, v82, v83\n"
    "v_mfma_f32_16x16x32_bf16 v[32:35], v[180:183], a[212:215], v[32:35]\n"
    "v_exp_f32 v88, v88\n"
    "v_mfma_f32_16x16x32_bf16 v[36:39], v[180:183], a[236:239], v[36:39]\n"
    "v_exp_f32 v89, v89\n"
    "v_mfma_f32_16x16x32_bf16 v[36:39], v[176:179], a[232:235], v[36:39]\n"
    "v_exp_f32 v90, v90\n"
    "v_mfma_f32_16x16x32_bf16 v[40:43], v[176:179], v[0:3], v[40:43]\n"
    "ds_read_b128 v[172:175], v200 offset:46080\n"
    "v_cvt_pk_bf16_f32 v150, v84, v85\n"
    "v_mfma_f32_16x16x32_bf16 v[40:43], v[180:183], v[4:7], v[40:43]\n"
    "v_exp_f32 v91, v91\n"
    "v_mfma_f32_16x16x32_bf16 v[44:47], v[180:183], v[28:31], v[44:47]\n"
    "v_exp_f32 v92, v92\n"
    "v_mfma_f32_16x16x32_bf16 v[44:47], v[176:179], v[24:27], v[44:47]\n"
    "s_waitcnt lgkmcnt(2)\n"
    "v_exp_f32 v93, v93\n"
    "v_mfma_f32_16x16x32_bf16 v[60:63], v[160:163], v[24:27], v[108:111]\n"
    "ds_read_b128 v[176:179], v200 offset:43008\n"
    "v_cvt_pk_bf16_f32 v151, v86, v87\n"
    "v_mfma_f32_16x16x32_bf16 v[60:63], v[164:167], v[28:31], v[60:63]\n"
    "v_exp_f32 v94, v94\n"
    "v_mfma_f32_16x16x32_bf16 v[56:59], v[164:167], v[4:7], v[104:107]\n"
    "v_exp_f32 v95, v95\n"
    "v_mfma_f32_16x16x32_bf16 v[56:59], v[160:163], v[0:3], v[56:59]\n"
    "v_exp_f32 v32, v32\n"
    "v_mfma_f32_16x16x32_bf16 v[52:55], v[160:163], a[232:235], v[100:103]\n"
    "ds_read_b128 v[180:183], v200 offset:44032\n"
    "v_cvt_pk_bf16_f32 v154, v88, v89\n"
    "v_mfma_f32_16x16x32_bf16 v[52:55], v[164:167], a[236:239], v[52:55]\n"
    "v_exp_f32 v33, v33\n"
    "v_mfma_f32_16x16x32_bf16 v[48:51], v[164:167], a[212:215], v[96:99]\n"
    "s_waitcnt vmcnt(6)\n"
    "v_exp_f32 v34, v34\n"
    "v_mfma_f32_16x16x32_bf16 v[48:51], v[160:163], a[208:211], v[48:51]\n"
    "s_waitcnt lgkmcnt(2)\n"
    "v_exp_f32 v35, v35\n"
    "v_mfma_f32_16x16x32_bf16 v[48:51], v[168:171], a[200:203], v[48:51]\n"
    "s_barrier\n"
    "v_exp_f32 v36, v36\n"
    "v_mfma_f32_16x16x32_bf16 v[48:51], v[172:175], a[204:207], v[48:51]\n"
    "ds_read_b128 v[160:163], v200\n"
    "v_cvt_pk_bf16_f32 v155, v90, v91\n"
    "v_mfma_f32_16x16x32_bf16 v[52:55], v[172:175], a[228:231], v[52:55]\n"
    "v_exp_f32 v37, v37\n"
    "v_mfma_f32_16x16x32_bf16 v[52:55], v[168:171], a[224:227], v[52:55]\n"
    "v_exp_f32 v38, v38\n"
    "v_mfma_f32_16x16x32_bf16 v[56:59], v[168:171], a[248:251], v[56:59]\n"
    "ds_read_b128 v[164:167], v200 offset:1024\n"
    "v_cvt_pk_bf16_f32 v158, v92, v93\n"
    "v_mfma_f32_16x16x32_bf16 v[56:59], v[172:175], a[252:255], v[56:59]\n"
    "buffer_load_dwordx2 v[218:219], v213, s[44:47], 0 offen sc1\n"
    "v_mfma_f32_16x16x32_bf16 v[60:63], v[172:175], v[20:23], v[60:63]\n"
    "v_exp_f32 v39, v39\n"
    "v_mfma_f32_16x16x32_bf16 v[60:63], v[168:171], v[16:19], v[60:63]\n"
    "s_waitcnt lgkmcnt(2)\n"
    "v_exp_f32 v40, v40\n"
    "v_mfma_f32_16x16x32_bf16 v[60:63], v[176:179], v[8:11], v[60:63]\n"
    "ds_read_b128 v[168:171], v200 offset:2048\n"
    "v_cvt_pk_bf16_f32 v159, v94, v95\n"
    "v_mfma_f32_16x16x32_bf16 v[60:63], v[180:183], v[12:15], v[60:63]\n"
    "v_exp_f32 v41, v41\n"
    "v_mfma_f32_16x16x32_bf16 v[56:59], v[180:183], a[244:247], v[56:59]\n"
    "v_exp_f32 v42, v42\n"
    "v_mfma_f32_16x16x32_bf16 v[56:59], v[176:179], a[240:243], v[56:59]\n"
    "v_exp_f32 v43, v43\n"
    "v_mfma_f32_16x16x32_bf16 v[52:55], v[176:179], a[216:219], v[52:55]\n"
    "ds_read_b128 v[172:175], v200 offset:3072\n"
    "v_cvt_pk_bf16_f32 v32, v32, v33\n"
    "v_mfma_f32_16x16x32_bf16 v[52:55], v[180:183], a[220:223], v[52:55]\n"
    "v_exp_f32 v44, v44\n"
    "v_mfma_f32_16x16x32_bf16 v[48:51], v[180:183], a[196:199], v[48:51]\n"
    "v_exp_f32 v45, v45\n"
    "v_mfma_f32_16x16x32_bf16 v[48:51], v[176:179], a[192:195], v[48:51]\n"
    "s_waitcnt lgkmcnt(2)\n"
    "v_exp_f32 v46, v46\n"
    "v_mfma_f32_16x16x32_bf16 v[64:67], v[160:163], a[192:195], v[96:99]\n"
    "ds_read_b128 v[176:179], v200 offset:4096\n"
    "v_cvt_pk_bf16_f32 v33, v34, v35\n"
    "v_mfma_f32_16x16x32_bf16 v[64:67], v[164:167], a[196:199], v[64:67]\n"
    "v_exp_f32 v47, v47\n"
    "v_mfma_f32_16x16x32_bf16 v[68:71], v[164:167], a[220:223], v[100:103]\n"
    "v_cvt_pk_bf16_f32 v36, v36, v37\n"
    "v_cvt_pk_bf16_f32 v37, v38, v39\n"
    "v_mfma_f32_16x16x32_bf16 v[68:71], v[160:163], a[216:219], v[68:71]\n"
    "v_cvt_pk_bf16_f32 v40, v40, v41\n"
    "v_cvt_pk_bf16_f32 v41, v42, v43\n"
    "v_mfma_f32_16x16x32_bf16 v[72:75], v[160:163], a[240:243], v[104:107]\n"
    "ds_read_b128 v[180:183], v200 offset:5120\n"
    "v_cvt_pk_bf16_f32 v44, v44, v45\n"
    "v_mfma_f32_16x16x32_bf16 v[72:75], v[164:167], a[244:247], v[72:75]\n"
    "v_cvt_pk_bf16_f32 v45, v46, v47\n"
    "v_mov_b32 v119, v117\n"
    "v_mfma_f32_16x16x32_bf16 v[76:79], v[164:167], v[12:15], v[108:111]\n"
    "v_exp_f32 v48, v48\n"
    "v_mfma_f32_16x16x32_bf16 v[76:79], v[160:163], v[8:11], v[76:79]\n"
    "s_waitcnt lgkmcnt(2)\n"
    "v_exp_f32 v49, v49\n"
    "v_mfma_f32_16x16x32_bf16 v[76:79], v[168:171], v[16:19], v[76:79]\n"
    "ds_read_b128 v[160:163], v200 offset:10240\n"
    "v_cvt_pk_bf16_f32 v34, v48, v49\n"
    "v_mfma_f32_16x16x32_bf16 v[76:79], v[172:175], v[20:23], v[76:79]\n"
    "v_exp_f32 v50, v50\n"
    "v_mfma_f32_16x16x32_bf16 v[72:75], v[172:175], a[252:255], v[72:75]\n"
    "v_exp_f32 v51, v51\n"
    "v_mfma_f32_16x16x32_bf16 v[72:75], v[168:171], a[248:251], v[72:75]\n"
    "v_exp_f32 v52, v52\n"
    "v_mfma_f32_16x16x32_bf16 v[68:71], v[168:171], a[224:227], v[68:71]\n"
    "ds_read_b128 v[164:167], v200 offset:11264\n"
    "v_cvt_pk_bf16_f32 v35, v50, v51\n"
    "v_mfma_f32_16x16x32_bf16 v[68:71], v[172:175], a[228:231], v[68:71]\n"
    "v_exp_f32 v53, v53\n"
    "v_mfma_f32_16x16x32_bf16 v[64:67], v[172:175], a[204:207], v[64:67]\n"
    "v_exp_f32 v54, v54\n"
    "v_mfma_f32_16x16x32_bf16 v[64:67], v[168:171], a[200:203], v[64:67]\n"
    "s_waitcnt lgkmcnt(2)\n"
    "v_exp_f32 v55, v55\n"
    "v_mfma_f32_16x16x32_bf16 v[64:67], v[176:179], a[208:211], v[64:67]\n"
    "s_add_u32 s66, s66, s74\n"
    "ds_read_b128 v[168:171], v200 offset:8192\n"
    "v_cvt_pk_bf16_f32 v38, v52, v53\n"
    "v_mfma_f32_16x16x32_bf16 v[64:67], v[180:183], a[212:215], v[64:67]\n"
    "s_add_u32 s60, s72, 0xbd000\n"
    "v_exp_f32 v56, v56\n"
    "v_mfma_f32_16x16x32_bf16 v[68:71], v[180:183], a[236:239], v[68:71]\n"
    "s_add_u32 s61, s71, 0xba000\n"
    "v_exp_f32 v57, v57\n"
    "v_mfma_f32_16x16x32_bf16 v[68:71], v[176:179], a[232:235], v[68:71]\n"
    "v_exp_f32 v58, v58\n"
    "v_mfma_f32_16x16x32_bf16 v[72:75], v[176:179], v[0:3], v[72:75]\n"
    "ds_read_b128 v[172:175], v200 offset:9216\n"
    "v_cvt_pk_bf16_f32 v39, v54, v55\n"
    "v_mfma_f32_16x16x32_bf16 v[72:75], v[180:183], v[4:7], v[72:75]\n"
    "v_exp_f32 v59, v59\n"
    "v_mfma_f32_16x16x32_bf16 v[76:79], v[180:183], v[28:31], v[76:79]\n"
    "s_waitcnt vmcnt(0)\n"
    "v_cmp_ne_u32 s[80:81], s67, v219\n"
    "v_cvt_pk_bf16_f32 v42, v56, v57\n"
    "v_mfma_f32_16x16x32_bf16 v[76:79], v[176:179], v[24:27], v[76:79]\n"
    "s_waitcnt lgkmcnt(2)\n"
    "v_cmp_gt_u32 vcc, s66, v218\n"
    "v_cvt_pk_bf16_f32 v43, v58, v59\n"
    "v_mfma_f32_16x16x32_bf16 v[92:95], v[160:163], v[24:27], v[108:111]\n"
    "ds_read_b128 v[176:179], v200 offset:6144\n"
    "v_mov_b32 v123, v121\n"
    "v_mfma_f32_16x16x32_bf16 v[92:95], v[164:167], v[28:31], v[92:95]\n"
    "s_or_b64 vcc, vcc, s[80:81]\n"
    "v_exp_f32 v64, v64\n"
    "v_mfma_f32_16x16x32_bf16 v[88:91], v[164:167], v[4:7], v[104:107]\n"
    "s_cbranch_vccnz LSPRE5\n"
    "LSPB5:\n"
    "v_exp_f32 v65, v65\n"
    "v_mfma_f32_16x16x32_bf16 v[88:91], v[160:163], v[0:3], v[88:91]\n"
    "v_exp_f32 v66, v66\n"
    "v_mfma_f32_16x16x32_bf16 v[84:87], v[160:163], a[232:235], v[100:103]\n"
    "s_barrier\n"
    "ds_read_b128 v[180:183], v200 offset:7168\n"
    "v_cvt_pk_bf16_f32 v64, v64, v65\n"
    "v_mfma_f32_16x16x32_bf16 v[84:87], v[164:167], a[236:239], v[84:87]\n"
    "s_add_u32 m0, s72, 0x6000\n"
    "v_exp_f32 v67, v67\n"
    "v_mfma_f32_16x16x32_bf16 v[80:83], v[164:167], a[212:215], v[96:99]\n"
    "buffer_load_dwordx4 v200, s[36:39], s60 offen sc1 lds\n"
    "v_mfma_f32_16x16x32_bf16 v[80:83], v[160:163], a[208:211], v[80:83]\n"
    "s_waitcnt lgkmcnt(2)\n"
    "v_exp_f32 v68, v68\n"
    "v_mfma_f32_16x16x32_bf16 v[80:83], v[168:171], a[200:203], v[80:83]\n"
    "ds_read_b128 v[160:163], v200 offset:12288\n"
    "v_cvt_pk_bf16_f32 v65, v66, v67\n"
    "v_mfma_f32_16x16x32_bf16 v[80:83], v[172:175], a[204:207], v[80:83]\n"
    "buffer_load_dwordx4 v200, s[36:39], s60 offen offset:1024 sc1 lds\n"
    "v_mfma_f32_16x16x32_bf16 v[84:87], v[172:175], a[228:231], v[84:87]\n"
    "v_exp_f32 v69, v69\n"
    "v_mfma_f32_16x16x32_bf16 v[84:87], v[168:171], a[224:227], v[84:87]\n"
    "buffer_load_dwordx4 v200, s[36:39], s60 offen offset:2048 sc1 lds\n"
    "v_mfma_f32_16x16x32_bf16 v[88:91], v[168:171], a[248:251], v[88:91]\n"
    "ds_read_b128 v[164:167], v200 offset:13312\n"
    "v_cvt_pk_bf16_f32 v68, v68, v69\n"
    "v_mfma_f32_16x16x32_bf16 v[88:91], v[172:175], a[252:255], v[88:91]\n"
    "v_exp_f32 v60, v60\n"
    "v_mfma_f32_16x16x32_bf16 v[92:95], v[172:175], v[20:23], v[92:95]\n"
    "s_add_u32 m0, s71, 0x18000\n"
    "v_exp_f32 v70, v70\n"
    "v_mfma_f32_16x16x32_bf16 v[92:95], v[168:171], v[16:19], v[92:95]\n"
    "s_waitcnt lgkmcnt(2)\n"
    "buffer_load_dwordx4 v200, s[40:43], s61 offen sc1 lds\n"
    "v_mfma_f32_16x16x32_bf16 v[92:95], v[176:179], v[8:11], v[92:95]\n"
    "ds_read_b128 v[168:171], v200 offset:14336\n"
    "v_mov_b32 v127, v125\n"
    "v_mfma_f32_16x16x32_bf16 v[92:95], v[180:183], v[12:15], v[92:95]\n"
    "v_exp_f32 v61, v61\n"
    "v_mfma_f32_16x16x32_bf16 v[88:91], v[180:183], a[244:247], v[88:91]\n"
    "v_exp_f32 v71, v71\n"
    "v_mfma_f32_16x16x32_bf16 v[88:91], v[176:179], a[240:243], v[88:91]\n"
    "v_exp_f32 v62, v62\n"
    "v_mfma_f32_16x16x32_bf16 v[84:87], v[176:179], a[216:219], v[84:87]\n"
    "ds_read_b128 v[172:175], v200 offset:15360\n"
    "v_cvt_pk_bf16_f32 v46, v60, v61\n"
    "v_mfma_f32_16x16x32_bf16 v[84:87], v[180:183], a[220:223], v[84:87]\n"
    "v_exp_f32 v72, v72\n"
    "v_mfma_f32_16x16x32_bf16 v[80:83], v[180:183], a[196:199], v[80:83]\n"
    "v_exp_f32 v63, v63\n"
    "v_mfma_f32_16x16x32_bf16 v[80:83], v[176:179], a[192:195], v[80:83]\n"
    "s_waitcnt lgkmcnt(2)\n"
    "buffer_load_dwordx4 v200, s[40:43], s61 offen offset:1024 sc1 lds\n"
    "v_mfma_f32_16x16x32_bf16 v[232:235], v[160:163], a[192:195], v[96:99]\n"
    "ds_read_b128 v[176:179], v200 offset:16384\n"
    "v_cvt_pk_bf16_f32 v47, v62, v63\n"
    "v_mfma_f32_16x16x32_bf16 v[232:235], v[164:167], a[196:199], v[232:235]\n"
    "v_exp_f32 v73, v73\n"
    "v_mfma_f32_16x16x32_bf16 v[236:239], v[164:167], a[220:223], v[100:103]\n"
    "v_exp_f32 v74, v74\n"
    "v_mfma_f32_16x16x32_bf16 v[236:239], v[160:163], a[216:219], v[236:239]\n"
    "v_exp_f32 v75, v75\n"
    "v_mfma_f32_16x16x32_bf16 v[240:243], v[160:163], a[240:243], v[104:107]\n"
    "ds_read_b128 v[180:183], v200 offset:17408\n"
    "v_cvt_pk_bf16_f32 v69, v70, v71\n"
    "v_mfma_f32_16x16x32_bf16 v[240:243], v[164:167], a[244:247], v[240:243]\n"
    "v_exp_f32 v76, v76\n"
    "v_mfma_f32_16x16x32_bf16 v[244:247], v[164:167], v[12:15], v[108:111]\n"
    "v_exp_f32 v77, v77\n"
    "v_mfma_f32_16x16x32_bf16 v[244:247], v[160:163], v[8:11], v[244:247]\n"
    "s_waitcnt lgkmcnt(2)\n"
    "buffer_load_dwordx4 v200, s[40:43], s61 offen offset:2048 sc1 lds\n"
    "v_mfma_f32_16x16x32_bf16 v[244:247], v[168:171], v[16:19], v[244:247]\n"
    "ds_read_b128 v[160:163], v200 offset:22528\n"
    "v_cvt_pk_bf16_f32 v72, v72, v73\n"
    "v_mfma_f32_16x16x32_bf16 v[244:247], v[172:175], v[20:23], v[244:247]\n"
    "v_exp_f32 v78, v78\n"
    "v_mfma_f32_16x16x32_bf16 v[240:243], v[172:175], a[252:255], v[240:243]\n"
    "v_exp_f32 v79, v79\n"
    "v_mfma_f32_16x16x32_bf16 v[240:243], v[168:171], a[248:251], v[240:243]\n"
    "v_exp_f32 v80, v80\n"
    "v_mfma_f32_16x16x32_bf16 v[236:239], v[168:171], a[224:227], v[236:239]\n"
    "ds_read_b128 v[164:167], v200 offset:23552\n"
    "v_cvt_pk_bf16_f32 v73, v74, v75\n"
    "v_mfma_f32_16x16x32_bf16 v[236:239], v[172:175], a[228:231], v[236:239]\n"
    "v_exp_f32 v81, v81\n"
    "v_mfma_f32_16x16x32_bf16 v[232:235], v[172:175], a[204:207], v[232:235]\n"
    "v_exp_f32 v82, v82\n"
    "v_mfma_f32_16x16x32_bf16 v[232:235], v[168:171], a[200:203], v[232:235]\n"
    "s_waitcnt lgkmcnt(2)\n"
    "buffer_load_dwordx4 v200, s[40:43], s61 offen offset:3072 sc1 lds\n"
    "v_mfma_f32_16x16x32_bf16 v[232:235], v[176:179], a[208:211], v[232:235]\n"
    "ds_read_b128 v[168:171], v200 offset:20480\n"
    "v_cvt_pk_bf16_f32 v76, v76, v77\n"
    "v_mfma_f32_16x16x32_bf16 v[232:235], v[180:183], a[212:215], v[232:235]\n"
    "v_exp_f32 v83, v83\n"
    "v_mfma_f32_16x16x32_bf16 v[236:239], v[180:183], a[236:239], v[236:239]\n"
    "v_exp_f32 v84, v84\n"
    "v_mfma_f32_16x16x32_bf16 v[236:239], v[176:179], a[232:235], v[236:239]\n"
    "v_exp_f32 v85, v85\n"
    "v_mfma_f32_16x16x32_bf16 v[240:243], v[176:179], v[0:3], v[240:243]\n"
    "ds_read_b128 v[172:175], v200 offset:21504\n"
    "v_cvt_pk_bf16_f32 v77, v78, v79\n"
    "v_mfma_f32_16x16x32_bf16 v[240:243], v[180:183], v[4:7], v[240:243]\n"
    "s_add_u32 s65, s61, 0x1000\n"
    "v_exp_f32 v86, v86\n"
    "v_mfma_f32_16x16x32_bf16 v[244:247], v[180:183], v[28:31], v[244:247]\n"
    "s_add_u32 m0, s71, 0x19000\n"
    "v_exp_f32 v87, v87\n"
    "v_mfma_f32_16x16x32_bf16 v[244:247], v[176:179], v[24:27], v[244:247]\n"
    "s_waitcnt lgkmcnt(2)\n"
    "buffer_load_dwordx4 v200, s[40:43], s65 offen sc1 lds\n"
    "v_mfma_f32_16x16x32_bf16 v[60:63], v[160:163], v[24:27], v[108:111]\n"
    "ds_read_b128 v[176:179], v200 offset:18432\n"
    "v_cvt_pk_bf16_f32 v66, v80, v81\n"
    "v_mfma_f32_16x16x32_bf16 v[60:63], v[164:167], v[28:31], v[60:63]\n"
    "v_exp_f32 v88, v88\n"
    "v_mfma_f32_16x16x32_bf16 v[56:59], v[164:167], v[4:7], v[104:107]\n"
    "v_exp_f32 v89, v89\n"
    "v_mfma_f32_16x16x32_bf16 v[56:59], v[160:163], v[0:3], v[56:59]\n"
    "v_exp_f32 v90, v90\n"
    "v_mfma_f32_16x16x32_bf16 v[52:55], v[160:163], a[232:235], v[100:103]\n"
    "ds_read_b128 v[180:183], v200 offset:19456\n"
    "v_cvt_pk_bf16_f32 v67, v82, v83\n"
    "v_mfma_f32_16x16x32_bf16 v[52:55], v[164:167], a[236:239], v[52:55]\n"
    "v_exp_f32 v91, v91\n"
    "v_mfma_f32_16x16x32_bf16 v[48:51], v[164:167], a[212:215], v[96:99]\n"
    "s_waitcnt vmcnt(5)\n"
    "v_exp_f32 v92, v92\n"
    "v_mfma_f32_16x16x32_bf16 v[48:51], v[160:163], a[208:211], v[48:51]\n"
    "s_waitcnt lgkmcnt(2)\n"
    "buffer_load_dwordx4 v200, s[40:43], s65 offen offset:1024 sc1 lds\n"
    "v_mfma_f32_16x16x32_bf16 v[48:51], v[168:171], a[200:203], v[48:51]\n"
    "s_barrier\n"
    "v_exp_f32 v93, v93\n"
    "v_mfma_f32_16x16x32_bf16 v[48:51], v[172:175], a[204:207], v[48:51]\n"
    "ds_read_b128 v[160:163], v200 offset:24576\n"
    "v_cvt_pk_bf16_f32 v70, v84, v85\n"
    "v_mfma_f32_16x16x32_bf16 v[52:55], v[172:175], a[228:231], v[52:55]\n"
    "v_exp_f32 v94, v94\n"
    "v_mfma_f32_16x16x32_bf16 v[52:55], v[168:171], a[224:227], v[52:55]\n"
    "v_exp_f32 v95, v95\n"
    "v_mfma_f32_16x16x32_bf16 v[56:59], v[168:171], a[248:251], v[56:59]\n"
    "ds_read_b128 v[164:167], v200 offset:25600\n"
    "v_cvt_pk_bf16_f32 v71, v86, v87\n"
    "v_mfma_f32_16x16x32_bf16 v[56:59], v[172:175], a[252:255], v[56:59]\n"
    "v_exp_f32 v232, v232\n"
    "v_mfma_f32_16x16x32_bf16 v[60:63], v[172:175], v[20:23], v[60:63]\n"
    "v_exp_f32 v233, v233\n"
    "v_mfma_f32_16x16x32_bf16 v[60:63], v[168:171], v[16:19], v[60:63]\n"
    "s_waitcnt lgkmcnt(2)\n"
    "v_exp_f32 v234, v234\n"
    "v_mfma_f32_16x16x32_bf16 v[60:63], v[176:179], v[8:11], v[60:63]\n"
    "s_lshl_b32 s76, s56, 3\n"
    "ds_read_b128 v[168:171], v200 offset:26624\n"
    "v_cvt_pk_bf16_f32 v74, v88, v89\n"
    "v_mfma_f32_16x16x32_bf16 v[60:63], v[180:183], v[12:15], v[60:63]\n"
    "s_add_u32 s76, s76, s57\n"
    "v_exp_f32 v235, v235\n"
    "v_mfma_f32_16x16x32_bf16 v[56:59], v[180:183], a[244:247], v[56:59]\n"
    "s_mul_i32 s76, s76, 0x18000\n"
    "v_exp_f32 v236, v236\n"
    "v_mfma_f32_16x16x32_bf16 v[56:59], v[176:179], a[240:243], v[56:59]\n"
    "s_add_u32 s76, s76, s88\n"
    "v_exp_f32 v237, v237\n"
    "v_mfma_f32_16x16x32_bf16 v[52:55], v[176:179], a[216:219], v[52:55]\n"
    "s_add_u32 s28, s24, s76\n"
    "ds_read_b128 v[172:175], v200 offset:27648\n"
    "v_cvt_pk_bf16_f32 v75, v90, v91\n"
    "v_mfma_f32_16x16x32_bf16 v[52:55], v[180:183], a[220:223], v[52:55]\n"
    "s_addc_u32 s29, s25, 0\n"
    "v_exp_f32 v238, v238\n"
    "v_mfma_f32_16x16x32_bf16 v[48:51], v[180:183], a[196:199], v[48:51]\n"
    "s_and_b32 s29, s29, 0xffff\n"
    "v_exp_f32 v239, v239\n"
    "v_mfma_f32_16x16x32_bf16 v[48:51], v[176:179], a[192:195], v[48:51]\n"
    "s_waitcnt lgkmcnt(2)\n"
    "v_exp_f32 v240, v240\n"
    "v_mfma_f32_16x16x32_bf16 v[80:83], v[160:163], a[192:195], v[96:99]\n"
    "s_mov_b32 s30, 0x6000\n"
    "ds_read_b128 v[176:179], v200 offset:28672\n"
    "v_cvt_pk_bf16_f32 v78, v92, v93\n"
    "v_mfma_f32_16x16x32_bf16 v[80:83], v[164:167], a[196:199], v[80:83]\n"
    "s_mov_b32 s31, 0x20000\n"
    "v_cvt_pk_bf16_f32 v79, v94, v95\n"
    "v_permlane16_swap_b32 v113, v115\n"
    "v_mfma_f32_16x16x32_bf16 v[84:87], v[164:167], a[220:223], v[100:103]\n"
    "s_xor_b32 s94, s70, 0x80000000\n"
    "v_exp_f32 v241, v241\n"
    "v_mfma_f32_16x16x32_bf16 v[84:87], v[160:163], a[216:219], v[84:87]\n"
    "s_xor_b32 s95, s70, 0x80000000\n"
    "v_exp_f32 v242, v242\n"
    "v_mfma_f32_16x16x32_bf16 v[88:91], v[160:163], a[240:243], v[104:107]\n"
    "s_mov_b32 s62, 0x1800\n"
    "ds_read_b128 v[180:183], v200 offset:29696\n"
    "v_permlane16_swap_b32 v117, v119\n"
    "v_mfma_f32_16x16x32_bf16 v[88:91], v[164:167], a[244:247], v[88:91]\n"
    "s_mov_b32 s63, 0x3000\n"
    "v_exp_f32 v243, v243\n"
    "v_mfma_f32_16x16x32_bf16 v[92:95], v[164:167], v[12:15], v[108:111]\n"
    "s_mov_b32 s64, 0x4800\n"
    "v_exp_f32 v244, v244\n"
    "v_mfma_f32_16x16x32_bf16 v[92:95], v[160:163], v[8:11], v[92:95]\n"
    "s_waitcnt lgkmcnt(2)\n"
    "v_exp_f32 v245, v245\n"
    "v_mfma_f32_16x16x32_bf16 v[92:95], v[168:171], v[16:19], v[92:95]\n"
    "ds_read_b128 v[160:163], v200 offset:34816\n"
    "v_permlane16_swap_b32 v121, v123\n"
    "v_mfma_f32_16x16x32_bf16 v[92:95], v[172:175], v[20:23], v[92:95]\n"
    "v_exp_f32 v246, v246\n"
    "v_mfma_f32_16x16x32_bf16 v[88:91], v[172:175], a[252:255], v[88:91]\n"
    "v_exp_f32 v247, v247\n"
    "v_mfma_f32_16x16x32_bf16 v[88:91], v[168:171], a[248:251], v[88:91]\n"
    "v_exp_f32 v48, v48\n"
    "v_mfma_f32_16x16x32_bf16 v[84:87], v[168:171], a[224:227], v[84:87]\n"
    "ds_read_b128 v[164:167], v200 offset:35840\n"
    "v_permlane16_swap_b32 v125, v127\n"
    "v_mfma_f32_16x16x32_bf16 v[84:87], v[172:175], a[228:231], v[84:87]\n"
    "v_exp_f32 v49, v49\n"
    "v_mfma_f32_16x16x32_bf16 v[80:83], v[172:175], a[204:207], v[80:83]\n"
    "v_exp_f32 v50, v50\n"
    "v_mfma_f32_16x16x32_bf16 v[80:83], v[168:171], a[200:203], v[80:83]\n"
    "s_waitcnt lgkmcnt(2)\n"
    "v_exp_f32 v51, v51\n"
    "v_mfma_f32_16x16x32_bf16 v[80:83], v[176:179], a[208:211], v[80:83]\n"
    "ds_read_b128 v[168:171], v200 offset:32768\n"
    "v_add_f32 v113, v113, v115\n"
    "v_mfma_f32_16x16x32_bf16 v[80:83], v[180:183], a[212:215], v[80:83]\n"
    "v_exp_f32 v52, v52\n"
    "v_mfma_f32_16x16x32_bf16 v[84:87], v[180:183], a[236:239], v[84:87]\n"
    "v_exp_f32 v53, v53\n"
    "v_mfma_f32_16x16x32_bf16 v[84:87], v[176:179], a[232:235], v[84:87]\n"
    "v_exp_f32 v54, v54\n"
    "v_mfma_f32_16x16x32_bf16 v[88:91], v[176:179], v[0:3], v[88:91]\n"
    "ds_read_b128 v[172:175], v200 offset:33792\n"
    "v_add_f32 v117, v117, v119\n"
    "v_mfma_f32_16x16x32_bf16 v[88:91], v[180:183], v[4:7], v[88:91]\n"
    "v_add_f32 v121, v121, v123\n"
    "v_add_f32 v125, v125, v127\n"
    "v_mfma_f32_16x16x32_bf16 v[92:95], v[180:183], v[28:31], v[92:95]\n"
    "v_exp_f32 v55, v55\n"
    "v_mfma_f32_16x16x32_bf16 v[92:95], v[176:179], v[24:27], v[92:95]\n"
    "s_waitcnt lgkmcnt(2)\n"
    "v_add_f32 v112, v112, v113\n"
    "v_add_f32 v116, v116, v117\n"
    "v_mfma_f32_16x16x32_bf16 v[252:255], v[160:163], v[24:27], v[108:111]\n"
    "ds_read_b128 v[176:179], v200 offset:30720\n"
    "v_add_f32 v120, v120, v121\n"
    "v_mfma_f32_16x16x32_bf16 v[252:255], v[164:167], v[28:31], v[252:255]\n"
    "v_add_f32 v124, v124, v125\n"
    "v_and_b32 v198, 15, v231\n"
    "v_mfma_f32_16x16x32_bf16 v[248:251], v[164:167], v[4:7], v[104:107]\n"
    "v_mul_u32_u24 v205, 0x180, v198\n"
    "v_lshrrev_b32 v199, 4, v231\n"
    "v_mfma_f32_16x16x32_bf16 v[248:251], v[160:163], v[0:3], v[248:251]\n"
    "v_lshl_add_u32 v205, v199, 4, v205\n"
    "v_add_u32 v207, 0x18000, v200\n"
    "v_mfma_f32_16x16x32_bf16 v[188:191], v[160:163], a[232:235], v[100:103]\n"
    "ds_read_b128 v[180:183], v200 offset:31744\n"
    "v_cvt_pk_bf16_f32 v232, v232, v233\n"
    "v_mfma_f32_16x16x32_bf16 v[188:191], v[164:167], a[236:239], v[188:191]\n"
    "v_exp_f32 v56, v56\n"
    "v_mfma_f32_16x16x32_bf16 v[184:187], v[164:167], a[212:215], v[96:99]\n"
    "v_exp_f32 v57, v57\n"
    "v_mfma_f32_16x16x32_bf16 v[184:187], v[160:163], a[208:211], v[184:187]\n"
    "s_waitcnt lgkmcnt(2)\n"
    "v_exp_f32 v58, v58\n"
    "v_mfma_f32_16x16x32_bf16 v[184:187], v[168:171], a[200:203], v[184:187]\n"
    "ds_read_b128 v[160:163], v201\n"
    "v_cvt_pk_bf16_f32 v233, v234, v235\n"
    "v_mfma_f32_16x16x32_bf16 v[184:187], v[172:175], a[204:207], v[184:187]\n"
    "v_exp_f32 v59, v59\n"
    "v_mfma_f32_16x16x32_bf16 v[188:191], v[172:175], a[228:231], v[188:191]\n"
    "v_exp_f32 v60, v60\n"
    "v_mfma_f32_16x16x32_bf16 v[188:191], v[168:171], a[224:227], v[188:191]\n"
    "v_exp_f32 v61, v61\n"
    "v_mfma_f32_16x16x32_bf16 v[248:251], v[168:171], a[248:251], v[248:251]\n"
    "ds_read_b128 v[164:167], v201 offset:1024\n"
    "v_cvt_pk_bf16_f32 v236, v236, v237\n"
    "v_mfma_f32_16x16x32_bf16 v[248:251], v[172:175], a[252:255], v[248:251]\n"
    "v_exp_f32 v62, v62\n"
    "v_mfma_f32_16x16x32_bf16 v[252:255], v[172:175], v[20:23], v[252:255]\n"
    "v_exp_f32 v63, v63\n"
    "v_mfma_f32_16x16x32_bf16 v[252:255], v[168:171], v[16:19], v[252:255]\n"
    "s_waitcnt lgkmcnt(2)\n"
    "v_exp_f32 v80, v80\n"
    "v_mfma_f32_16x16x32_bf16 v[252:255], v[176:179], v[8:11], v[252:255]\n"
    "ds_read_b128 v[168:171], v201 offset:24576\n"
    "v_cvt_pk_bf16_f32 v237, v238, v239\n"
    "v_mfma_f32_16x16x32_bf16 v[252:255], v[180:183], v[12:15], v[252:255]\n"
    "v_exp_f32 v81, v81\n"
    "v_mfma_f32_16x16x32_bf16 v[248:251], v[180:183], a[244:247], v[248:251]\n"
    "v_exp_f32 v82, v82\n"
    "v_mfma_f32_16x16x32_bf16 v[248:251], v[176:179], a[240:243], v[248:251]\n"
    "v_exp_f32 v83, v83\n"
    "v_mfma_f32_16x16x32_bf16 v[188:191], v[176:179], a[216:219], v[188:191]\n"
    "ds_read_b128 v[172:175], v201 offset:25600\n"
    "v_cvt_pk_bf16_f32 v240, v240, v241\n"
    "v_mfma_f32_16x16x32_bf16 v[188:191], v[180:183], a[220:223], v[188:191]\n"
    "v_exp_f32 v84, v84\n"
    "v_mfma_f32_16x16x32_bf16 v[184:187], v[180:183], a[196:199], v[184:187]\n"
    "v_exp_f32 v85, v85\n"
    "v_mfma_f32_16x16x32_bf16 v[184:187], v[176:179], a[192:195], v[184:187]\n"
    "s_waitcnt lgkmcnt(2)\n"
    "v_exp_f32 v86, v86\n"
    "v_mfma_f32_16x16x32_bf16 a[0:3], v[160:163], v[128:131], a[0:3]\n"
    "ds_read_b128 v[176:179], v201 offset:2048\n"
    "v_cvt_pk_bf16_f32 v241, v242, v243\n"
    "v_mfma_f32_16x16x32_bf16 a[0:3], v[164:167], v[144:147], a[0:3]\n"
    "v_exp_f32 v87, v87\n"
    "v_mfma_f32_16x16x32_bf16 a[4:7], v[160:163], v[132:135], a[4:7]\n"
    "v_exp_f32 v88, v88\n"
    "v_mfma_f32_16x16x32_bf16 a[4:7], v[164:167], v[148:151], a[4:7]\n"
    "v_exp_f32 v89, v89\n"
    "v_mfma_f32_16x16x32_bf16 a[8:11], v[160:163], v[136:139], a[8:11]\n"
    "ds_read_b128 v[180:183], v201 offset:3072\n"
    "v_cvt_pk_bf16_f32 v244, v244, v245\n"
    "v_mfma_f32_16x16x32_bf16 a[8:11], v[164:167], v[152:155], a[8:11]\n"
    "v_cvt_pk_bf16_f32 v245, v246, v247\n"
    "v_cvt_pk_bf16_f32 v234, v48, v49\n"
    "v_mfma_f32_16x16x32_bf16 a[12:15], v[160:163], v[140:143], a[12:15]\n"
    "v_cvt_pk_bf16_f32 v235, v50, v51\n"
    "v_cvt_pk_bf16_f32 v238, v52, v53\n"
    "v_mfma_f32_16x16x32_bf16 a[12:15], v[164:167], v[156:159], a[12:15]\n"
    "v_cvt_pk_bf16_f32 v239, v54, v55\n"
    "v_cvt_pk_bf16_f32 v242, v56, v57\n"
    "v_mfma_f32_16x16x32_bf16 v[112:115], v[192:195], v[144:147], v[112:115]\n"
    "v_exp_f32 v90, v90\n"
    "v_mfma_f32_16x16x32_bf16 v[112:115], v[192:195], v[32:35], v[112:115]\n"
    "v_cvt_pk_bf16_f32 v243, v58, v59\n"
    "v_cvt_pk_bf16_f32 v246, v60, v61\n"
    "v_mfma_f32_16x16x32_bf16 v[112:115], v[192:195], v[64:67], v[112:115]\n"
    "v_cvt_pk_bf16_f32 v247, v62, v63\n"
    "v_cvt_pk_bf16_f32 v80, v80, v81\n"
    "v_mfma_f32_16x16x32_bf16 v[116:119], v[192:195], v[148:151], v[116:119]\n"
    "v_exp_f32 v91, v91\n"
    "v_mfma_f32_16x16x32_bf16 v[116:119], v[192:195], v[36:39], v[116:119]\n"
    "v_exp_f32 v92, v92\n"
    "v_mfma_f32_16x16x32_bf16 v[116:119], v[192:195], v[68:71], v[116:119]\n"
    "s_waitcnt lgkmcnt(2)\n"
    "v_exp_f32 v93, v93\n"
    "v_mfma_f32_16x16x32_bf16 a[0:3], v[168:171], v[32:35], a[0:3]\n"
    "ds_read_b128 v[160:163], v201 offset:26624\n"
    "v_cvt_pk_bf16_f32 v81, v82, v83\n"
    "v_mfma_f32_16x16x32_bf16 a[0:3], v[172:175], v[64:67], a[0:3]\n"
    "v_exp_f32 v94, v94\n"
    "v_mfma_f32_16x16x32_bf16 a[4:7], v[168:171], v[36:39], a[4:7]\n"
    "v_exp_f32 v95, v95\n"
    "v_mfma_f32_16x16x32_bf16 a[4:7], v[172:175], v[68:71], a[4:7]\n"
    "v_exp_f32 v184, v184\n"
    "v_mfma_f32_16x16x32_bf16 a[8:11], v[168:171], v[40:43], a[8:11]\n"
    "ds_read_b128 v[164:167], v201 offset:27648\n"
    "v_cvt_pk_bf16_f32 v84, v84, v85\n"
    "v_mfma_f32_16x16x32_bf16 a[8:11], v[172:175], v[72:75], a[8:11]\n"
    "v_exp_f32 v185, v185\n"
    "v_mfma_f32_16x16x32_bf16 a[12:15], v[168:171], v[44:47], a[12:15]\n"
    "v_exp_f32 v186, v186\n"
    "v_mfma_f32_16x16x32_bf16 a[12:15], v[172:175], v[76:79], a[12:15]\n"
    "v_exp_f32 v187, v187\n"
    "v_mfma_f32_16x16x32_bf16 v[120:123], v[192:195], v[152:155], v[120:123]\n"
    "v_exp_f32 v188, v188\n"
    "v_mfma_f32_16x16x32_bf16 v[120:123], v[192:195], v[40:43], v[120:123]\n"
    "v_exp_f32 v189, v189\n"
    "v_mfma_f32_16x16x32_bf16 v[120:123], v[192:195], v[72:75], v[120:123]\n"
    "v_exp_f32 v190, v190\n"
    "v_mfma_f32_16x16x32_bf16 v[124:127], v[192:195], v[156:159], v[124:127]\n"
    "v_exp_f32 v191, v191\n"
    "v_mfma_f32_16x16x32_bf16 v[124:127], v[192:195], v[44:47], v[124:127]\n"
    "s_waitcnt vmcnt(0)\n"
    "v_exp_f32 v248, v248\n"
    "v_mfma_f32_16x16x32_bf16 v[124:127], v[192:195], v[76:79], v[124:127]\n"
    "s_waitcnt lgkmcnt(2)\n"
    "v_exp_f32 v249, v249\n"
    "v_mfma_f32_16x16x32_bf16 a[16:19], v[176:179], v[128:131], a[16:19]\n"
    "s_barrier\n"
    "v_exp_f32 v250, v250\n"
    "v_mfma_f32_16x16x32_bf16 a[16:19], v[180:183], v[144:147], a[16:19]\n"
    "ds_read_b128 v[168:171], v207\n"
    "v_cvt_pk_bf16_f32 v85, v86, v87\n"
    "v_mfma_f32_16x16x32_bf16 a[20:23], v[176:179], v[132:135], a[20:23]\n"
    "v_exp_f32 v251, v251\n"
    "v_mfma_f32_16x16x32_bf16 a[20:23], v[180:183], v[148:151], a[20:23]\n"
    "v_exp_f32 v252, v252\n"
    "v_mfma_f32_16x16x32_bf16 a[24:27], v[176:179], v[136:139], a[24:27]\n"
    "ds_read_b128 v[172:175], v207 offset:1024\n"
    "v_cvt_pk_bf16_f32 v88, v88, v89\n"
    "v_mfma_f32_16x16x32_bf16 a[24:27], v[180:183], v[152:155], a[24:27]\n"
    "v_exp_f32 v253, v253\n"
    "v_mfma_f32_16x16x32_bf16 a[28:31], v[176:179], v[140:143], a[28:31]\n"
    "v_exp_f32 v254, v254\n"
    "v_mfma_f32_16x16x32_bf16 a[28:31], v[180:183], v[156:159], a[28:31]\n"
    "s_waitcnt lgkmcnt(2)\n"
    "v_cvt_pk_bf16_f32 v89, v90, v91\n"
    "v_cvt_pk_bf16_f32 v92, v92, v93\n"
    "v_mfma_f32_16x16x32_bf16 a[16:19], v[160:163], v[32:35], a[16:19]\n"
    "ds_read_b128 v[176:179], v207 offset:2048\n"
    "v_cvt_pk_bf16_f32 v93, v94, v95\n"
    "v_mfma_f32_16x16x32_bf16 a[16:19], v[164:167], v[64:67], a[16:19]\n"
    "ds_read_b128 v[180:183], v207 offset:3072\n"
    "v_cvt_pk_bf16_f32 v82, v184, v185\n"
    "v_mfma_f32_16x16x32_bf16 a[20:23], v[160:163], v[36:39], a[20:23]\n"
    "v_cvt_pk_bf16_f32 v83, v186, v187\n"
    "v_cvt_pk_bf16_f32 v86, v188, v189\n"
    "v_mfma_f32_16x16x32_bf16 a[20:23], v[164:167], v[68:71], a[20:23]\n"
    "v_exp_f32 v255, v255\n"
    "v_mfma_f32_16x16x32_bf16 a[24:27], v[160:163], v[40:43], a[24:27]\n"
    "v_cvt_pk_bf16_f32 v87, v190, v191\n"
    "v_cvt_pk_bf16_f32 v90, v248, v249\n"
    "v_mfma_f32_16x16x32_bf16 a[24:27], v[164:167], v[72:75], a[24:27]\n"
    "v_cvt_pk_bf16_f32 v91, v250, v251\n"
    "v_cvt_pk_bf16_f32 v94, v252, v253\n"
    "v_mfma_f32_16x16x32_bf16 a[28:31], v[160:163], v[44:47], a[28:31]\n"
    "v_cvt_pk_bf16_f32 v95, v254, v255\n"
    "v_mfma_f32_16x16x32_bf16 a[28:31], v[164:167], v[76:79], a[28:31]\n"
    "v_mfma_f32_16x16x32_bf16 v[112:115], v[192:195], v[232:235], v[112:115]\n"
    "ds_read_b128 v[160:163], v201 offset:4096\n"
    "v_mfma_f32_16x16x32_bf16 v[112:115], v[192:195], v[80:83], v[112:115]\n"
    "s_waitcnt lgkmcnt(3)\n"
    "v_mfma_f32_16x16x32_bf16 a[0:3], v[168:171], v[232:235], a[0:3]\n"
    "v_mfma_f32_16x16x32_bf16 a[0:3], v[172:175], v[80:83], a[0:3]\n"
    "s_waitcnt lgkmcnt(1)\n"
    "v_mfma_f32_16x16x32_bf16 a[16:19], v[176:179], v[232:235], a[16:19]\n"
    "ds_read_b128 v[164:167], v201 offset:5120\n"
    "v_mfma_f32_16x16x32_bf16 a[16:19], v[180:183], v[80:83], a[16:19]\n"
    "v_mfma_f32_16x16x32_bf16 v[116:119], v[192:195], v[236:239], v[116:119]\n"
    "v_mul_f32 v222, s68, v112\n"
    "v_rcp_f32 v222, v222\n"
    "v_mfma_f32_16x16x32_bf16 v[116:119], v[192:195], v[84:87], v[116:119]\n"
    "v_accvgpr_read_b32 v96, a0\n"
    "v_accvgpr_read_b32 v97, a1\n"
    "v_accvgpr_read_b32 v98, a2\n"
    "v_mfma_f32_16x16x32_bf16 a[4:7], v[168:171], v[236:239], a[4:7]\n"
    "v_accvgpr_read_b32 v99, a3\n"
    "v_fma_f32 v96, v96, v222, s94\n"
    "v_fma_f32 v97, v97, v222, s94\n"
    "v_mfma_f32_16x16x32_bf16 a[4:7], v[172:175], v[84:87], a[4:7]\n"
    "v_fma_f32 v98, v98, v222, s94\n"
    "v_fma_f32 v99, v99, v222, s94\n"
    "v_cvt_pk_bf16_f32 v48, v96, v97\n"
    "v_mfma_f32_16x16x32_bf16 a[20:23], v[176:179], v[236:239], a[20:23]\n"
    "v_cvt_pk_bf16_f32 v49, v98, v99\n"
    "v_accvgpr_read_b32 v100, a16\n"
    "v_accvgpr_read_b32 v101, a17\n"
    "v_mfma_f32_16x16x32_bf16 a[20:23], v[180:183], v[84:87], a[20:23]\n"
    "v_accvgpr_read_b32 v102, a18\n"
    "v_accvgpr_read_b32 v103, a19\n"
    "v_fma_f32 v100, v100, v222, s94\n"
    "v_mfma_f32_16x16x32_bf16 v[120:123], v[192:195], v[240:243], v[120:123]\n"
    "v_fma_f32 v101, v101, v222, s94\n"
    "v_fma_f32 v102, v102, v222, s94\n"
    "v_fma_f32 v103, v103, v222, s94\n"
    "v_mfma_f32_16x16x32_bf16 v[120:123], v[192:195], v[88:91], v[120:123]\n"
    "v_cvt_pk_bf16_f32 v50, v100, v101\n"
    "v_cvt_pk_bf16_f32 v51, v102, v103\n"
    "v_mfma_f32_16x16x32_bf16 a[8:11], v[168:171], v[240:243], a[8:11]\n"
    "buffer_store_dwordx4 v[48:51], v205, s[28:31], 0 offen sc0 sc1\n"
    "v_mul_f32 v224, s68, v116\n"
    "v_rcp_f32 v224, v224\n"
    "v_accvgpr_read_b32 v104, a4\n"
    "v_mfma_f32_16x16x32_bf16 a[8:11], v[172:175], v[88:91], a[8:11]\n"
    "v_accvgpr_read_b32 v105, a5\n"
    "v_accvgpr_read_b32 v106, a6\n"
    "v_accvgpr_read_b32 v107, a7\n"
    "v_mfma_f32_16x16x32_bf16 a[24:27], v[176:179], v[240:243], a[24:27]\n"
    "v_fma_f32 v104, v104, v224, s94\n"
    "v_fma_f32 v105, v105, v224, s94\n"
    "v_fma_f32 v106, v106, v224, s94\n"
    "v_mfma_f32_16x16x32_bf16 a[24:27], v[180:183], v[88:91], a[24:27]\n"
    "v_fma_f32 v107, v107, v224, s94\n"
    "v_cvt_pk_bf16_f32 v52, v104, v105\n"
    "v_cvt_pk_bf16_f32 v53, v106, v107\n"
    "v_mfma_f32_16x16x32_bf16 v[124:127], v[192:195], v[244:247], v[124:127]\n"
    "v_accvgpr_read_b32 v108, a20\n"
    "v_accvgpr_read_b32 v109, a21\n"
    "v_accvgpr_read_b32 v110, a22\n"
    "v_mfma_f32_16x16x32_bf16 v[124:127], v[192:195], v[92:95], v[124:127]\n"
    "v_accvgpr_read_b32 v111, a23\n"
    "v_fma_f32 v108, v108, v224, s94\n"
    "v_fma_f32 v109, v109, v224, s94\n"
    "v_mfma_f32_16x16x32_bf16 a[12:15], v[168:171], v[244:247], a[12:15]\n"
    "v_fma_f32 v110, v110, v224, s94\n"
    "v_fma_f32 v111, v111, v224, s94\n"
    "v_mfma_f32_16x16x32_bf16 a[12:15], v[172:175], v[92:95], a[12:15]\n"
    "v_cvt_pk_bf16_f32 v54, v108, v109\n"
    "v_cvt_pk_bf16_f32 v55, v110, v111\n"
    "v_mfma_f32_16x16x32_bf16 a[28:31], v[176:179], v[244:247], a[28:31]\n"
    "ds_read_b128 v[168:171], v201 offset:28672\n"
    "v_mfma_f32_16x16x32_bf16 a[28:31], v[180:183], v[92:95], a[28:31]\n"
    "s_waitcnt lgkmcnt(1)\n"
    "ds_read_b128 v[172:175], v201 offset:29696\n"
    "v_mfma_f32_16x16x32_bf16 a[32:35], v[160:163], v[128:131], a[32:35]\n"
    "ds_read_b128 v[176:179], v207 offset:4096\n"
    "v_mfma_f32_16x16x32_bf16 a[32:35], v[164:167], v[144:147], a[32:35]\n"
    "buffer_store_dwordx4 v[52:55], v205, s[28:31], s62 offen sc0 sc1\n"
    "v_mul_f32 v226, s68, v120\n"
    "v_rcp_f32 v226, v226\n"
    "v_mfma_f32_16x16x32_bf16 a[36:39], v[160:163], v[132:135], a[36:39]\n"
    "v_accvgpr_read_b32 v96, a8\n"
    "v_accvgpr_read_b32 v97, a9\n"
    "v_mfma_f32_16x16x32_bf16 a[36:39], v[164:167], v[148:151], a[36:39]\n"
    "v_accvgpr_read_b32 v98, a10\n"
    "v_accvgpr_read_b32 v99, a11\n"
    "v_mfma_f32_16x16x32_bf16 a[40:43], v[160:163], v[136:139], a[40:43]\n"
    "ds_read_b128 v[180:183], v207 offset:5120\n"
    "v_fma_f32 v96, v96, v226, s94\n"
    "v_mfma_f32_16x16x32_bf16 a[40:43], v[164:167], v[152:155], a[40:43]\n"
    "v_fma_f32 v97, v97, v226, s94\n"
    "v_fma_f32 v98, v98, v226, s94\n"
    "v_mfma_f32_16x16x32_bf16 a[44:47], v[160:163], v[140:143], a[44:47]\n"
    "v_fma_f32 v99, v99, v226, s94\n"
    "v_cvt_pk_bf16_f32 v56, v96, v97\n"
    "v_mfma_f32_16x16x32_bf16 a[44:47], v[164:167], v[156:159], a[44:47]\n"
    "s_waitcnt lgkmcnt(2)\n"
    "v_cvt_pk_bf16_f32 v57, v98, v99\n"
    "v_accvgpr_read_b32 v100, a24\n"
    "v_mfma_f32_16x16x32_bf16 a[44:47], v[168:171], v[44:47], a[44:47]\n"
    "ds_read_b128 v[160:163], v201 offset:6144\n"
    "v_accvgpr_read_b32 v101, a25\n"
    "v_mfma_f32_16x16x32_bf16 a[44:47], v[172:175], v[76:79], a[44:47]\n"
    "v_accvgpr_read_b32 v102, a26\n"
    "v_accvgpr_read_b32 v103, a27\n"
    "v_mfma_f32_16x16x32_bf16 a[40:43], v[168:171], v[40:43], a[40:43]\n"
    "v_fma_f32 v100, v100, v226, s94\n"
    "v_fma_f32 v101, v101, v226, s94\n"
    "v_mfma_f32_16x16x32_bf16 a[40:43], v[172:175], v[72:75], a[40:43]\n"
    "v_fma_f32 v102, v102, v226, s94\n"
    "v_fma_f32 v103, v103, v226, s94\n"
    "v_mfma_f32_16x16x32_bf16 a[36:39], v[168:171], v[36:39], a[36:39]\n"
    "ds_read_b128 v[164:167], v201 offset:7168\n"
    "v_cvt_pk_bf16_f32 v58, v100, v101\n"
    "v_mfma_f32_16x16x32_bf16 a[36:39], v[172:175], v[68:71], a[36:39]\n"
    "v_cvt_pk_bf16_f32 v59, v102, v103\n"
    "v_mfma_f32_16x16x32_bf16 a[32:35], v[168:171], v[32:35], a[32:35]\n"
    "buffer_store_dwordx4 v[56:59], v205, s[28:31], s63 offen sc0 sc1\n"
    "v_mul_f32 v228, s68, v124\n"
    "v_rcp_f32 v228, v228\n"
    "v_mfma_f32_16x16x32_bf16 a[32:35], v[172:175], v[64:67], a[32:35]\n"
    "s_waitcnt lgkmcnt(2)\n"
    "v_accvgpr_read_b32 v104, a12\n"
    "v_accvgpr_read_b32 v105, a13\n"
    "v_mfma_f32_16x16x32_bf16 a[32:35], v[176:179], v[232:235], a[32:35]\n"
    "ds_read_b128 v[168:171], v201 offset:30720\n"
    "v_accvgpr_read_b32 v106, a14\n"
    "v_mfma_f32_16x16x32_bf16 a[32:35], v[180:183], v[80:83], a[32:35]\n"
    "v_accvgpr_read_b32 v107, a15\n"
    "v_fma_f32 v104, v104, v228, s94\n"
    "v_mfma_f32_16x16x32_bf16 a[36:39], v[176:179], v[236:239], a[36:39]\n"
    "v_fma_f32 v105, v105, v228, s94\n"
    "v_fma_f32 v106, v106, v228, s94\n"
    "v_mfma_f32_16x16x32_bf16 a[36:39], v[180:183], v[84:87], a[36:39]\n"
    "v_fma_f32 v107, v107, v228, s94\n"
    "v_cvt_pk_bf16_f32 v60, v104, v105\n"
    "v_mfma_f32_16x16x32_bf16 a[40:43], v[176:179], v[240:243], a[40:43]\n"
    "ds_read_b128 v[172:175], v201 offset:31744\n"
    "v_cvt_pk_bf16_f32 v61, v106, v107\n"
    "v_mfma_f32_16x16x32_bf16 a[40:43], v[180:183], v[88:91], a[40:43]\n"
    "v_accvgpr_read_b32 v108, a28\n"
    "v_accvgpr_read_b32 v109, a29\n"
    "v_mfma_f32_16x16x32_bf16 a[44:47], v[176:179], v[244:247], a[44:47]\n"
    "v_accvgpr_read_b32 v110, a30\n"
    "v_accvgpr_read_b32 v111, a31\n"
    "v_mfma_f32_16x16x32_bf16 a[44:47], v[180:183], v[92:95], a[44:47]\n"
    "s_waitcnt lgkmcnt(2)\n"
    "v_fma_f32 v108, v108, v228, s94\n"
    "v_fma_f32 v109, v109, v228, s94\n"
    "v_mfma_f32_16x16x32_bf16 a[48:51], v[160:163], v[128:131], a[48:51]\n"
    "ds_read_b128 v[176:179], v207 offset:6144\n"
    "v_fma_f32 v110, v110, v228, s94\n"
    "v_mfma_f32_16x16x32_bf16 a[48:51], v[164:167], v[144:147], a[48:51]\n"
    "v_fma_f32 v111, v111, v228, s94\n"
    "v_cvt_pk_bf16_f32 v62, v108, v109\n"
    "v_mfma_f32_16x16x32_bf16 a[52:55], v[160:163], v[132:135], a[52:55]\n"
    "v_cvt_pk_bf16_f32 v63, v110, v111\n"
    "v_mfma_f32_16x16x32_bf16 a[52:55], v[164:167], v[148:151], a[52:55]\n"
    "buffer_store_dwordx4 v[60:63], v205, s[28:31], s64 offen sc0 sc1\n"
    "v_accvgpr_read_b32 v96, a32\n"
    "v_accvgpr_read_b32 v97, a33\n"
    "v_accvgpr_read_b32 v98, a34\n"
    "v_mfma_f32_16x16x32_bf16 a[56:59], v[160:163], v[136:139], a[56:59]\n"
    "ds_read_b128 v[180:183], v207 offset:7168\n"
    "v_accvgpr_read_b32 v99, a35\n"
    "v_mfma_f32_16x16x32_bf16 a[56:59], v[164:167], v[152:155], a[56:59]\n"
    "v_fma_f32 v96, v96, v222, s94\n"
    "v_fma_f32 v97, v97, v222, s94\n"
    "v_mfma_f32_16x16x32_bf16 a[60:63], v[160:163], v[140:143], a[60:63]\n"
    "v_fma_f32 v98, v98, v222, s94\n"
    "v_fma_f32 v99, v99, v222, s94\n"
    "v_mfma_f32_16x16x32_bf16 a[60:63], v[164:167], v[156:159], a[60:63]\n"
    "s_waitcnt lgkmcnt(2)\n"
    "v_cvt_pk_bf16_f32 v184, v96, v97\n"
    "v_cvt_pk_bf16_f32 v185, v98, v99\n"
    "v_mfma_f32_16x16x32_bf16 a[60:63], v[168:171], v[44:47], a[60:63]\n"
    "ds_read_b128 v[160:163], v201 offset:8192\n"
    "v_accvgpr_read_b32 v100, a36\n"
    "v_mfma_f32_16x16x32_bf16 a[60:63], v[172:175], v[76:79], a[60:63]\n"
    "v_accvgpr_read_b32 v101, a37\n"
    "v_accvgpr_read_b32 v102, a38\n"
    "v_mfma_f32_16x16x32_bf16 a[56:59], v[168:171], v[40:43], a[56:59]\n"
    "v_accvgpr_read_b32 v103, a39\n"
    "v_fma_f32 v100, v100, v224, s94\n"
    "v_mfma_f32_16x16x32_bf16 a[56:59], v[172:175], v[72:75], a[56:59]\n"
    "v_fma_f32 v101, v101, v224, s94\n"
    "v_fma_f32 v102, v102, v224, s94\n"
    "v_mfma_f32_16x16x32_bf16 a[52:55], v[168:171], v[36:39], a[52:55]\n"
    "ds_read_b128 v[164:167], v201 offset:9216\n"
    "v_fma_f32 v103, v103, v224, s94\n"
    "v_mfma_f32_16x16x32_bf16 a[52:55], v[172:175], v[68:71], a[52:55]\n"
    "v_cvt_pk_bf16_f32 v188, v100, v101\n"
    "v_cvt_pk_bf16_f32 v189, v102, v103\n"
    "v_mfma_f32_16x16x32_bf16 a[48:51], v[168:171], v[32:35], a[48:51]\n"
    "v_accvgpr_read_b32 v104, a40\n"
    "v_accvgpr_read_b32 v105, a41\n"
    "v_mfma_f32_16x16x32_bf16 a[48:51], v[172:175], v[64:67], a[48:51]\n"
    "s_waitcnt lgkmcnt(2)\n"
    "v_accvgpr_read_b32 v106, a42\n"
    "v_accvgpr_read_b32 v107, a43\n"
    "v_mfma_f32_16x16x32_bf16 a[48:51], v[176:179], v[232:235], a[48:51]\n"
    "ds_read_b128 v[168:171], v201 offset:32768\n"
    "v_fma_f32 v104, v104, v226, s94\n"
    "v_mfma_f32_16x16x32_bf16 a[48:51], v[180:183], v[80:83], a[48:51]\n"
    "v_fma_f32 v105, v105, v226, s94\n"
    "v_fma_f32 v106, v106, v226, s94\n"
    "v_mfma_f32_16x16x32_bf16 a[52:55], v[176:179], v[236:239], a[52:55]\n"
    "v_fma_f32 v107, v107, v226, s94\n"
    "v_cvt_pk_bf16_f32 v248, v104, v105\n"
    "v_mfma_f32_16x16x32_bf16 a[52:55], v[180:183], v[84:87], a[52:55]\n"
    "v_cvt_pk_bf16_f32 v249, v106, v107\n"
    "v_accvgpr_read_b32 v108, a44\n"
    "v_mfma_f32_16x16x32_bf16 a[56:59], v[176:179], v[240:243], a[56:59]\n"
    "ds_read_b128 v[172:175], v201 offset:33792\n"
    "v_accvgpr_read_b32 v109, a45\n"
    "v_mfma_f32_16x16x32_bf16 a[56:59], v[180:183], v[88:91], a[56:59]\n"
    "v_accvgpr_read_b32 v110, a46\n"
    "v_accvgpr_read_b32 v111, a47\n"
    "v_mfma_f32_16x16x32_bf16 a[60:63], v[176:179], v[244:247], a[60:63]\n"
    "v_fma_f32 v108, v108, v228, s94\n"
    "v_fma_f32 v109, v109, v228, s94\n"
    "v_mfma_f32_16x16x32_bf16 a[60:63], v[180:183], v[92:95], a[60:63]\n"
    "s_waitcnt lgkmcnt(2)\n"
    "v_fma_f32 v110, v110, v228, s94\n"
    "v_fma_f32 v111, v111, v228, s94\n"
    "v_mfma_f32_16x16x32_bf16 a[64:67], v[160:163], v[128:131], a[64:67]\n"
    "ds_read_b128 v[176:179], v207 offset:8192\n"
    "v_cvt_pk_bf16_f32 v252, v108, v109\n"
    "v_mfma_f32_16x16x32_bf16 a[64:67], v[164:167], v[144:147], a[64:67]\n"
    "v_cvt_pk_bf16_f32 v253, v110, v111\n"
    "v_accvgpr_read_b32 v96, a48\n"
    "v_mfma_f32_16x16x32_bf16 a[68:71], v[160:163], v[132:135], a[68:71]\n"
    "v_accvgpr_read_b32 v97, a49\n"
    "v_accvgpr_read_b32 v98, a50\n"
    "v_mfma_f32_16x16x32_bf16 a[68:71], v[164:167], v[148:151], a[68:71]\n"
    "v_accvgpr_read_b32 v99, a51\n"
    "v_fma_f32 v96, v96, v222, s94\n"
    "v_mfma_f32_16x16x32_bf16 a[72:75], v[160:163], v[136:139], a[72:75]\n"
    "ds_read_b128 v[180:183], v207 offset:9216\n"
    "v_fma_f32 v97, v97, v222, s94\n"
    "v_mfma_f32_16x16x32_bf16 a[72:75], v[164:167], v[152:155], a[72:75]\n"
    "v_fma_f32 v98, v98, v222, s94\n"
    "v_fma_f32 v99, v99, v222, s94\n"
    "v_mfma_f32_16x16x32_bf16 a[76:79], v[160:163], v[140:143], a[76:79]\n"
    "v_cvt_pk_bf16_f32 v186, v96, v97\n"
    "v_cvt_pk_bf16_f32 v187, v98, v99\n"
    "v_mfma_f32_16x16x32_bf16 a[76:79], v[164:167], v[156:159], a[76:79]\n"
    "s_waitcnt lgkmcnt(2)\n"
    "buffer_store_dwordx4 v[184:187], v205, s[28:31], 0 offen offset:64 sc0 sc1\n"
    "v_accvgpr_read_b32 v100, a52\n"
    "v_accvgpr_read_b32 v101, a53\n"
    "v_accvgpr_read_b32 v102, a54\n"
    "v_mfma_f32_16x16x32_bf16 a[76:79], v[168:171], v[44:47], a[76:79]\n"
    "ds_read_b128 v[160:163], v201 offset:10240\n"
    "v_accvgpr_read_b32 v103, a55\n"
    "v_mfma_f32_16x16x32_bf16 a[76:79], v[172:175], v[76:79], a[76:79]\n"
    "v_fma_f32 v100, v100, v224, s94\n"
    "v_fma_f32 v101, v101, v224, s94\n"
    "v_mfma_f32_16x16x32_bf16 a[72:75], v[168:171], v[40:43], a[72:75]\n"
    "v_fma_f32 v102, v102, v224, s94\n"
    "v_fma_f32 v103, v103, v224, s94\n"
    "v_mfma_f32_16x16x32_bf16 a[72:75], v[172:175], v[72:75], a[72:75]\n"
    "v_cvt_pk_bf16_f32 v190, v100, v101\n"
    "v_cvt_pk_bf16_f32 v191, v102, v103\n"
    "v_mfma_f32_16x16x32_bf16 a[68:71], v[168:171], v[36:39], a[68:71]\n"
    "ds_read_b128 v[164:167], v201 offset:11264\n"
    "v_mfma_f32_16x16x32_bf16 a[68:71], v[172:175], v[68:71], a[68:71]\n"
    "buffer_store_dwordx4 v[188:191], v205, s[28:31], s62 offen offset:64 sc0 sc1\n"
    "v_accvgpr_read_b32 v104, a56\n"
    "v_accvgpr_read_b32 v105, a57\n"
    "v_accvgpr_read_b32 v106, a58\n"
    "v_mfma_f32_16x16x32_bf16 a[64:67], v[168:171], v[32:35], a[64:67]\n"
    "v_accvgpr_read_b32 v107, a59\n"
    "v_fma_f32 v104, v104, v226, s94\n"
    "v_mfma_f32_16x16x32_bf16 a[64:67], v[172:175], v[64:67], a[64:67]\n"
    "s_waitcnt lgkmcnt(2)\n"
    "v_fma_f32 v105, v105, v226, s94\n"
    "v_fma_f32 v106, v106, v226, s94\n"
    "v_mfma_f32_16x16x32_bf16 a[64:67], v[176:179], v[232:235], a[64:67]\n"
    "ds_read_b128 v[168:171], v201 offset:34816\n"
    "v_fma_f32 v107, v107, v226, s94\n"
    "v_mfma_f32_16x16x32_bf16 a[64:67], v[180:183], v[80:83], a[64:67]\n"
    "v_cvt_pk_bf16_f32 v250, v104, v105\n"
    "v_cvt_pk_bf16_f32 v251, v106, v107\n"
    "v_mfma_f32_16x16x32_bf16 a[68:71], v[176:179], v[236:239], a[68:71]\n"
    "buffer_store_dwordx4 v[248:251], v205, s[28:31], s63 offen offset:64 sc0 sc1\n"
    "v_accvgpr_read_b32 v108, a60\n"
    "v_accvgpr_read_b32 v109, a61\n"
    "v_accvgpr_read_b32 v110, a62\n"
    "v_mfma_f32_16x16x32_bf16 a[68:71], v[180:183], v[84:87], a[68:71]\n"
    "v_accvgpr_read_b32 v111, a63\n"
    "v_fma_f32 v108, v108, v228, s94\n"
    "v_mfma_f32_16x16x32_bf16 a[72:75], v[176:179], v[240:243], a[72:75]\n"
    "ds_read_b128 v[172:175], v201 offset:35840\n"
    "v_fma_f32 v109, v109, v228, s94\n"
    "v_mfma_f32_16x16x32_bf16 a[72:75], v[180:183], v[88:91], a[72:75]\n"
    "v_fma_f32 v110, v110, v228, s94\n"
    "v_fma_f32 v111, v111, v228, s94\n"
    "v_mfma_f32_16x16x32_bf16 a[76:79], v[176:179], v[244:247], a[76:79]\n"
    "v_cvt_pk_bf16_f32 v254, v108, v109\n"
    "v_cvt_pk_bf16_f32 v255, v110, v111\n"
    "v_mfma_f32_16x16x32_bf16 a[76:79], v[180:183], v[92:95], a[76:79]\n"
    "s_waitcnt lgkmcnt(2)\n"
    "buffer_store_dwordx4 v[252:255], v205, s[28:31], s64 offen offset:64 sc0 sc1\n"
    "v_accvgpr_read_b32 v96, a64\n"
    "v_accvgpr_read_b32 v97, a65\n"
    "v_accvgpr_read_b32 v98, a66\n"
    "v_mfma_f32_16x16x32_bf16 a[80:83], v[160:163], v[128:131], a[80:83]\n"
    "ds_read_b128 v[176:179], v207 offset:10240\n"
    "v_accvgpr_read_b32 v99, a67\n"
    "v_mfma_f32_16x16x32_bf16 a[80:83], v[164:167], v[144:147], a[80:83]\n"
    "v_fma_f32 v96, v96, v222, s94\n"
    "v_fma_f32 v97, v97, v222, s94\n"
    "v_mfma_f32_16x16x32_bf16 a[84:87], v[160:163], v[132:135], a[84:87]\n"
    "v_fma_f32 v98, v98, v222, s94\n"
    "v_fma_f32 v99, v99, v222, s94\n"
    "v_mfma_f32_16x16x32_bf16 a[84:87], v[164:167], v[148:151], a[84:87]\n"
    "v_cvt_pk_bf16_f32 v48, v96, v97\n"
    "v_cvt_pk_bf16_f32 v49, v98, v99\n"
    "v_mfma_f32_16x16x32_bf16 a[88:91], v[160:163], v[136:139], a[88:91]\n"
    "ds_read_b128 v[180:183], v207 offset:11264\n"
    "v_accvgpr_read_b32 v100, a68\n"
    "v_mfma_f32_16x16x32_bf16 a[88:91], v[164:167], v[152:155], a[88:91]\n"
    "v_accvgpr_read_b32 v101, a69\n"
    "v_accvgpr_read_b32 v102, a70\n"
    "v_mfma_f32_16x16x32_bf16 a[92:95], v[160:163], v[140:143], a[92:95]\n"
    "v_accvgpr_read_b32 v103, a71\n"
    "v_fma_f32 v100, v100, v224, s94\n"
    "v_mfma_f32_16x16x32_bf16 a[92:95], v[164:167], v[156:159], a[92:95]\n"
    "s_waitcnt lgkmcnt(2)\n"
    "v_fma_f32 v101, v101, v224, s94\n"
    "v_fma_f32 v102, v102, v224, s94\n"
    "v_mfma_f32_16x16x32_bf16 a[92:95], v[168:171], v[44:47], a[92:95]\n"
    "ds_read_b128 v[160:163], v201 offset:12288\n"
    "v_fma_f32 v103, v103, v224, s94\n"
    "v_mfma_f32_16x16x32_bf16 a[92:95], v[172:175], v[76:79], a[92:95]\n"
    "v_cvt_pk_bf16_f32 v52, v100, v101\n"
    "v_cvt_pk_bf16_f32 v53, v102, v103\n"
    "v_mfma_f32_16x16x32_bf16 a[88:91], v[168:171], v[40:43], a[88:91]\n"
    "v_accvgpr_read_b32 v104, a72\n"
    "v_accvgpr_read_b32 v105, a73\n"
    "v_mfma_f32_16x16x32_bf16 a[88:91], v[172:175], v[72:75], a[88:91]\n"
    "v_accvgpr_read_b32 v106, a74\n"
    "v_accvgpr_read_b32 v107, a75\n"
    "v_mfma_f32_16x16x32_bf16 a[84:87], v[168:171], v[36:39], a[84:87]\n"
    "ds_read_b128 v[164:167], v201 offset:13312\n"
    "v_fma_f32 v104, v104, v226, s94\n"
    "v_mfma_f32_16x16x32_bf16 a[84:87], v[172:175], v[68:71], a[84:87]\n"
    "v_fma_f32 v105, v105, v226, s94\n"
    "v_fma_f32 v106, v106, v226, s94\n"
    "v_mfma_f32_16x16x32_bf16 a[80:83], v[168:171], v[32:35], a[80:83]\n"
    "v_fma_f32 v107, v107, v226, s94\n"
    "v_cvt_pk_bf16_f32 v56, v104, v105\n"
    "v_mfma_f32_16x16x32_bf16 a[80:83], v[172:175], v[64:67], a[80:83]\n"
    "s_waitcnt lgkmcnt(2)\n"
    "v_cvt_pk_bf16_f32 v57, v106, v107\n"
    "v_accvgpr_read_b32 v108, a76\n"
    "v_mfma_f32_16x16x32_bf16 a[80:83], v[176:179], v[232:235], a[80:83]\n"
    "ds_read_b128 v[168:171], v201 offset:36864\n"
    "v_accvgpr_read_b32 v109, a77\n"
    "v_mfma_f32_16x16x32_bf16 a[80:83], v[180:183], v[80:83], a[80:83]\n"
    "v_accvgpr_read_b32 v110, a78\n"
    "v_accvgpr_read_b32 v111, a79\n"
    "v_mfma_f32_16x16x32_bf16 a[84:87], v[176:179], v[236:239], a[84:87]\n"
    "v_fma_f32 v108, v108, v228, s94\n"
    "v_fma_f32 v109, v109, v228, s94\n"
    "v_mfma_f32_16x16x32_bf16 a[84:87], v[180:183], v[84:87], a[84:87]\n"
    "v_fma_f32 v110, v110, v228, s94\n"
    "v_fma_f32 v111, v111, v228, s94\n"
    "v_mfma_f32_16x16x32_bf16 a[88:91], v[176:179], v[240:243], a[88:91]\n"
    "ds_read_b128 v[172:175], v201 offset:37888\n"
    "v_cvt_pk_bf16_f32 v60, v108, v109\n"
    "v_mfma_f32_16x16x32_bf16 a[88:91], v[180:183], v[88:91], a[88:91]\n"
    "v_cvt_pk_bf16_f32 v61, v110, v111\n"
    "v_mfma_f32_16x16x32_bf16 a[92:95], v[176:179], v[244:247], a[92:95]\n"
    "v_mfma_f32_16x16x32_bf16 a[92:95], v[180:183], v[92:95], a[92:95]\n"
    "s_waitcnt lgkmcnt(2)\n"
    "v_accvgpr_read_b32 v96, a80\n"
    "v_accvgpr_read_b32 v97, a81\n"
    "v_mfma_f32_16x16x32_bf16 a[96:99], v[160:163], v[128:131], a[96:99]\n"
    "ds_read_b128 v[176:179], v207 offset:12288\n"
    "v_accvgpr_read_b32 v98, a82\n"
    "v_mfma_f32_16x16x32_bf16 a[96:99], v[164:167], v[144:147], a[96:99]\n"
    "v_accvgpr_read_b32 v99, a83\n"
    "v_fma_f32 v96, v96, v222, s94\n"
    "v_mfma_f32_16x16x32_bf16 a[100:103], v[160:163], v[132:135], a[100:103]\n"
    "v_fma_f32 v97, v97, v222, s94\n"
    "v_fma_f32 v98, v98, v222, s94\n"
    "v_mfma_f32_16x16x32_bf16 a[100:103], v[164:167], v[148:151], a[100:103]\n"
    "v_fma_f32 v99, v99, v222, s94\n"
    "v_cvt_pk_bf16_f32 v50, v96, v97\n"
    "v_mfma_f32_16x16x32_bf16 a[104:107], v[160:163], v[136:139], a[104:107]\n"
    "ds_read_b128 v[180:183], v207 offset:13312\n"
    "v_cvt_pk_bf16_f32 v51, v98, v99\n"
    "v_mfma_f32_16x16x32_bf16 a[104:107], v[164:167], v[152:155], a[104:107]\n"
    "buffer_store_dwordx4 v[48:51], v205, s[28:31], 0 offen offset:128 sc0 sc1\n"
    "v_accvgpr_read_b32 v100, a84\n"
    "v_accvgpr_read_b32 v101, a85\n"
    "v_accvgpr_read_b32 v102, a86\n"
    "v_mfma_f32_16x16x32_bf16 a[108:111], v[160:163], v[140:143], a[108:111]\n"
    "v_accvgpr_read_b32 v103, a87\n"
    "v_fma_f32 v100, v100, v224, s94\n"
    "v_mfma_f32_16x16x32_bf16 a[108:111], v[164:167], v[156:159], a[108:111]\n"
    "s_waitcnt lgkmcnt(2)\n"
    "s_cmp_eq_u32 s57, 0\n"
    "s_cselect_b32 s77, s74, 0\n"
    "v_mov_b32 v196, 0x40000000\n"
    "v_mov_b32 v197, 0x800\n"
    "v_cmp_gt_u32 vcc, s77, v231\n"
    "v_mov_b32 v198, 0\n"
    "v_mov_b32 v198, 0\n"
    "v_cndmask_b32 v197, v196, v197, vcc\n"
    "buffer_store_dword v221, v197, s[44:47], 0 offen sc1\n"
    "v_mfma_f32_16x16x32_bf16 a[108:111], v[168:171], v[44:47], a[108:111]\n"
    "ds_read_b128 v[160:163], v201 offset:14336\n"
    "v_fma_f32 v101, v101, v224, s94\n"
    "v_mfma_f32_16x16x32_bf16 a[108:111], v[172:175], v[76:79], a[108:111]\n"
    "v_fma_f32 v102, v102, v224, s94\n"
    "v_fma_f32 v103, v103, v224, s94\n"
    "v_mfma_f32_16x16x32_bf16 a[104:107], v[168:171], v[40:43], a[104:107]\n"
    "v_cvt_pk_bf16_f32 v54, v100, v101\n"
    "v_cvt_pk_bf16_f32 v55, v102, v103\n"
    "v_mfma_f32_16x16x32_bf16 a[104:107], v[172:175], v[72:75], a[104:107]\n"
    "buffer_store_dwordx4 v[52:55], v205, s[28:31], s62 offen offset:128 sc0 sc1\n"
    "v_accvgpr_read_b32 v104, a88\n"
    "v_accvgpr_read_b32 v105, a89\n"
    "v_accvgpr_read_b32 v106, a90\n"
    "v_mfma_f32_16x16x32_bf16 a[100:103], v[168:171], v[36:39], a[100:103]\n"
    "ds_read_b128 v[164:167], v201 offset:15360\n"
    "v_accvgpr_read_b32 v107, a91\n"
    "v_mfma_f32_16x16x32_bf16 a[100:103], v[172:175], v[68:71], a[100:103]\n"
    "v_fma_f32 v104, v104, v226, s94\n"
    "v_fma_f32 v105, v105, v226, s94\n"
    "v_mfma_f32_16x16x32_bf16 a[96:99], v[168:171], v[32:35], a[96:99]\n"
    "v_fma_f32 v106, v106, v226, s94\n"
    "v_fma_f32 v107, v107, v226, s94\n"
    "v_mfma_f32_16x16x32_bf16 a[96:99], v[172:175], v[64:67], a[96:99]\n"
    "s_waitcnt lgkmcnt(2)\n"
    "v_cvt_pk_bf16_f32 v58, v104, v105\n"
    "v_cvt_pk_bf16_f32 v59, v106, v107\n"
    "v_mfma_f32_16x16x32_bf16 a[96:99], v[176:179], v[232:235], a[96:99]\n"
    "ds_read_b128 v[168:171], v201 offset:38912\n"
    "v_mfma_f32_16x16x32_bf16 a[96:99], v[180:183], v[80:83], a[96:99]\n"
    "buffer_store_dwordx4 v[56:59], v205, s[28:31], s63 offen offset:128 sc0 sc1\n"
    "v_accvgpr_read_b32 v108, a92\n"
    "v_accvgpr_read_b32 v109, a93\n"
    "v_accvgpr_read_b32 v110, a94\n"
    "v_mfma_f32_16x16x32_bf16 a[100:103], v[176:179], v[236:239], a[100:103]\n"
    "v_accvgpr_read_b32 v111, a95\n"
    "v_fma_f32 v108, v108, v228, s94\n"
    "v_mfma_f32_16x16x32_bf16 a[100:103], v[180:183], v[84:87], a[100:103]\n"
    "v_fma_f32 v109, v109, v228, s94\n"
    "v_fma_f32 v110, v110, v228, s94\n"
    "v_mfma_f32_16x16x32_bf16 a[104:107], v[176:179], v[240:243], a[104:107]\n"
    "ds_read_b128 v[172:175], v201 offset:39936\n"
    "v_fma_f32 v111, v111, v228, s94\n"
    "v_mfma_f32_16x16x32_bf16 a[104:107], v[180:183], v[88:91], a[104:107]\n"
    "v_cvt_pk_bf16_f32 v62, v108, v109\n"
    "v_cvt_pk_bf16_f32 v63, v110, v111\n"
    "v_mfma_f32_16x16x32_bf16 a[108:111], v[176:179], v[244:247], a[108:111]\n"
    "buffer_store_dwordx4 v[60:63], v205, s[28:31], s64 offen offset:128 sc0 sc1\n"
    "v_mfma_f32_16x16x32_bf16 a[108:111], v[180:183], v[92:95], a[108:111]\n"
    "s_waitcnt lgkmcnt(2)\n"
    "v_accvgpr_read_b32 v96, a96\n"
    "v_accvgpr_read_b32 v97, a97\n"
    "v_mfma_f32_16x16x32_bf16 a[112:115], v[160:163], v[128:131], a[112:115]\n"
    "ds_read_b128 v[176:179], v207 offset:14336\n"
    "v_accvgpr_read_b32 v98, a98\n"
    "v_mfma_f32_16x16x32_bf16 a[112:115], v[164:167], v[144:147], a[112:115]\n"
    "v_accvgpr_read_b32 v99, a99\n"
    "v_fma_f32 v96, v96, v222, s94\n"
    "v_mfma_f32_16x16x32_bf16 a[116:119], v[160:163], v[132:135], a[116:119]\n"
    "v_fma_f32 v97, v97, v222, s94\n"
    "v_fma_f32 v98, v98, v222, s94\n"
    "v_mfma_f32_16x16x32_bf16 a[116:119], v[164:167], v[148:151], a[116:119]\n"
    "v_fma_f32 v99, v99, v222, s94\n"
    "v_cvt_pk_bf16_f32 v184, v96, v97\n"
    "v_mfma_f32_16x16x32_bf16 a[120:123], v[160:163], v[136:139], a[120:123]\n"
    "ds_read_b128 v[180:183], v207 offset:15360\n"
    "v_cvt_pk_bf16_f32 v185, v98, v99\n"
    "v_mfma_f32_16x16x32_bf16 a[120:123], v[164:167], v[152:155], a[120:123]\n"
    "v_accvgpr_read_b32 v100, a100\n"
    "v_accvgpr_read_b32 v101, a101\n"
    "v_mfma_f32_16x16x32_bf16 a[124:127], v[160:163], v[140:143], a[124:127]\n"
    "v_accvgpr_read_b32 v102, a102\n"
    "v_accvgpr_read_b32 v103, a103\n"
    "v_mfma_f32_16x16x32_bf16 a[124:127], v[164:167], v[156:159], a[124:127]\n"
    "s_waitcnt lgkmcnt(2)\n"
    "v_fma_f32 v100, v100, v224, s94\n"
    "v_fma_f32 v101, v101, v224, s94\n"
    "v_mfma_f32_16x16x32_bf16 a[124:127], v[168:171], v[44:47], a[124:127]\n"
    "ds_read_b128 v[160:163], v201 offset:16384\n"
    "v_fma_f32 v102, v102, v224, s94\n"
    "v_mfma_f32_16x16x32_bf16 a[124:127], v[172:175], v[76:79], a[124:127]\n"
    "v_fma_f32 v103, v103, v224, s94\n"
    "v_cvt_pk_bf16_f32 v188, v100, v101\n"
    "v_mfma_f32_16x16x32_bf16 a[120:123], v[168:171], v[40:43], a[120:123]\n"
    "v_cvt_pk_bf16_f32 v189, v102, v103\n"
    "v_accvgpr_read_b32 v104, a104\n"
    "v_mfma_f32_16x16x32_bf16 a[120:123], v[172:175], v[72:75], a[120:123]\n"
    "v_accvgpr_read_b32 v105, a105\n"
    "v_accvgpr_read_b32 v106, a106\n"
    "v_mfma_f32_16x16x32_bf16 a[116:119], v[168:171], v[36:39], a[116:119]\n"
    "ds_read_b128 v[164:167], v201 offset:17408\n"
    "v_accvgpr_read_b32 v107, a107\n"
    "v_mfma_f32_16x16x32_bf16 a[116:119], v[172:175], v[68:71], a[116:119]\n"
    "v_fma_f32 v104, v104, v226, s94\n"
    "v_fma_f32 v105, v105, v226, s94\n"
    "v_mfma_f32_16x16x32_bf16 a[112:115], v[168:171], v[32:35], a[112:115]\n"
    "v_fma_f32 v106, v106, v226, s94\n"
    "v_fma_f32 v107, v107, v226, s94\n"
    "v_mfma_f32_16x16x32_bf16 a[112:115], v[172:175], v[64:67], a[112:115]\n"
    "s_waitcnt lgkmcnt(2)\n"
    "v_cvt_pk_bf16_f32 v248, v104, v105\n"
    "v_cvt_pk_bf16_f32 v249, v106, v107\n"
    "v_mfma_f32_16x16x32_bf16 a[112:115], v[176:179], v[232:235], a[112:115]\n"
    "ds_read_b128 v[168:171], v201 offset:40960\n"
    "v_accvgpr_read_b32 v108, a108\n"
    "v_mfma_f32_16x16x32_bf16 a[112:115], v[180:183], v[80:83], a[112:115]\n"
    "v_accvgpr_read_b32 v109, a109\n"
    "v_accvgpr_read_b32 v110, a110\n"
    "v_mfma_f32_16x16x32_bf16 a[116:119], v[176:179], v[236:239], a[116:119]\n"
    "v_accvgpr_read_b32 v111, a111\n"
    "v_fma_f32 v108, v108, v228, s94\n"
    "v_mfma_f32_16x16x32_bf16 a[116:119], v[180:183], v[84:87], a[116:119]\n"
    "v_fma_f32 v109, v109, v228, s94\n"
    "v_fma_f32 v110, v110, v228, s94\n"
    "v_mfma_f32_16x16x32_bf16 a[120:123], v[176:179], v[240:243], a[120:123]\n"
    "ds_read_b128 v[172:175], v201 offset:41984\n"
    "v_fma_f32 v111, v111, v228, s94\n"
    "v_mfma_f32_16x16x32_bf16 a[120:123], v[180:183], v[88:91], a[120:123]\n"
    "v_cvt_pk_bf16_f32 v252, v108, v109\n"
    "v_cvt_pk_bf16_f32 v253, v110, v111\n"
    "v_mfma_f32_16x16x32_bf16 a[124:127], v[176:179], v[244:247], a[124:127]\n"
    "v_mfma_f32_16x16x32_bf16 a[124:127], v[180:183], v[92:95], a[124:127]\n"
    "s_waitcnt lgkmcnt(2)\n"
    "v_accvgpr_read_b32 v96, a112\n"
    "v_accvgpr_read_b32 v97, a113\n"
    "v_mfma_f32_16x16x32_bf16 a[128:131], v[160:163], v[128:131], a[128:131]\n"
    "ds_read_b128 v[176:179], v207 offset:16384\n"
    "v_accvgpr_read_b32 v98, a114\n"
    "v_mfma_f32_16x16x32_bf16 a[128:131], v[164:167], v[144:147], a[128:131]\n"
    "v_accvgpr_read_b32 v99, a115\n"
    "v_fma_f32 v96, v96, v222, s94\n"
    "v_mfma_f32_16x16x32_bf16 a[132:135], v[160:163], v[132:135], a[132:135]\n"
    "v_fma_f32 v97, v97, v222, s94\n"
    "v_fma_f32 v98, v98, v222, s94\n"
    "v_mfma_f32_16x16x32_bf16 a[132:135], v[164:167], v[148:151], a[132:135]\n"
    "v_fma_f32 v99, v99, v222, s94\n"
    "v_cvt_pk_bf16_f32 v186, v96, v97\n"
    "v_mfma_f32_16x16x32_bf16 a[136:139], v[160:163], v[136:139], a[136:139]\n"
    "ds_read_b128 v[180:183], v207 offset:17408\n"
    "v_cvt_pk_bf16_f32 v187, v98, v99\n"
    "v_mfma_f32_16x16x32_bf16 a[136:139], v[164:167], v[152:155], a[136:139]\n"
    "buffer_store_dwordx4 v[184:187], v205, s[28:31], 0 offen offset:192 sc0 sc1\n"
    "v_accvgpr_read_b32 v100, a116\n"
    "v_accvgpr_read_b32 v101, a117\n"
    "v_accvgpr_read_b32 v102, a118\n"
    "v_mfma_f32_16x16x32_bf16 a[140:143], v[160:163], v[140:143], a[140:143]\n"
    "v_accvgpr_read_b32 v103, a119\n"
    "v_fma_f32 v100, v100, v224, s94\n"
    "v_mfma_f32_16x16x32_bf16 a[140:143], v[164:167], v[156:159], a[140:143]\n"
    "s_waitcnt lgkmcnt(2)\n"
    "v_fma_f32 v101, v101, v224, s94\n"
    "v_fma_f32 v102, v102, v224, s94\n"
    "v_mfma_f32_16x16x32_bf16 a[140:143], v[168:171], v[44:47], a[140:143]\n"
    "ds_read_b128 v[160:163], v201 offset:18432\n"
    "v_fma_f32 v103, v103, v224, s94\n"
    "v_mfma_f32_16x16x32_bf16 a[140:143], v[172:175], v[76:79], a[140:143]\n"
    "v_cvt_pk_bf16_f32 v190, v100, v101\n"
    "v_cvt_pk_bf16_f32 v191, v102, v103\n"
    "v_mfma_f32_16x16x32_bf16 a[136:139], v[168:171], v[40:43], a[136:139]\n"
    "buffer_store_dwordx4 v[188:191], v205, s[28:31], s62 offen offset:192 sc0 sc1\n"
    "v_accvgpr_read_b32 v104, a120\n"
    "v_accvgpr_read_b32 v105, a121\n"
    "v_accvgpr_read_b32 v106, a122\n"
    "v_mfma_f32_16x16x32_bf16 a[136:139], v[172:175], v[72:75], a[136:139]\n"
    "v_accvgpr_read_b32 v107, a123\n"
    "v_fma_f32 v104, v104, v226, s94\n"
    "v_mfma_f32_16x16x32_bf16 a[132:135], v[168:171], v[36:39], a[132:135]\n"
    "ds_read_b128 v[164:167], v201 offset:19456\n"
    "v_fma_f32 v105, v105, v226, s94\n"
    "v_mfma_f32_16x16x32_bf16 a[132:135], v[172:175], v[68:71], a[132:135]\n"
    "v_fma_f32 v106, v106, v226, s94\n"
    "v_fma_f32 v107, v107, v226, s94\n"
    "v_mfma_f32_16x16x32_bf16 a[128:131], v[168:171], v[32:35], a[128:131]\n"
    "v_cvt_pk_bf16_f32 v250, v104, v105\n"
    "v_cvt_pk_bf16_f32 v251, v106, v107\n"
    "v_mfma_f32_16x16x32_bf16 a[128:131], v[172:175], v[64:67], a[128:131]\n"
    "s_waitcnt lgkmcnt(2)\n"
    "buffer_store_dwordx4 v[248:251], v205, s[28:31], s63 offen offset:192 sc0 sc1\n"
    "v_accvgpr_read_b32 v108, a124\n"
    "v_accvgpr_read_b32 v109, a125\n"
    "v_accvgpr_read_b32 v110, a126\n"
    "v_mfma_f32_16x16x32_bf16 a[128:131], v[176:179], v[232:235], a[128:131]\n"
    "ds_read_b128 v[168:171], v201 offset:43008\n"
    "v_accvgpr_read_b32 v111, a127\n"
    "v_mfma_f32_16x16x32_bf16 a[128:131], v[180:183], v[80:83], a[128:131]\n"
    "v_fma_f32 v108, v108, v228, s94\n"
    "v_fma_f32 v109, v109, v228, s94\n"
    "v_mfma_f32_16x16x32_bf16 a[132:135], v[176:179], v[236:239], a[132:135]\n"
    "v_fma_f32 v110, v110, v228, s94\n"
    "v_fma_f32 v111, v111, v228, s94\n"
    "v_mfma_f32_16x16x32_bf16 a[132:135], v[180:183], v[84:87], a[132:135]\n"
    "v_cvt_pk_bf16_f32 v254, v108, v109\n"
    "v_cvt_pk_bf16_f32 v255, v110, v111\n"
    "v_mfma_f32_16x16x32_bf16 a[136:139], v[176:179], v[240:243], a[136:139]\n"
    "ds_read_b128 v[172:175], v201 offset:44032\n"
    "v_mfma_f32_16x16x32_bf16 a[136:139], v[180:183], v[88:91], a[136:139]\n"
    "buffer_store_dwordx4 v[252:255], v205, s[28:31], s64 offen offset:192 sc0 sc1\n"
    "v_mfma_f32_16x16x32_bf16 a[140:143], v[176:179], v[244:247], a[140:143]\n"
    "v_mfma_f32_16x16x32_bf16 a[140:143], v[180:183], v[92:95], a[140:143]\n"
    "s_waitcnt lgkmcnt(2)\n"
    "v_accvgpr_read_b32 v96, a128\n"
    "v_accvgpr_read_b32 v97, a129\n"
    "v_mfma_f32_16x16x32_bf16 a[144:147], v[160:163], v[128:131], a[144:147]\n"
    "ds_read_b128 v[176:179], v207 offset:18432\n"
    "v_accvgpr_read_b32 v98, a130\n"
    "v_mfma_f32_16x16x32_bf16 a[144:147], v[164:167], v[144:147], a[144:147]\n"
    "v_accvgpr_read_b32 v99, a131\n"
    "v_fma_f32 v96, v96, v222, s94\n"
    "v_mfma_f32_16x16x32_bf16 a[148:151], v[160:163], v[132:135], a[148:151]\n"
    "v_fma_f32 v97, v97, v222, s94\n"
    "v_fma_f32 v98, v98, v222, s94\n"
    "v_mfma_f32_16x16x32_bf16 a[148:151], v[164:167], v[148:151], a[148:151]\n"
    "v_fma_f32 v99, v99, v222, s94\n"
    "v_cvt_pk_bf16_f32 v48, v96, v97\n"
    "v_mfma_f32_16x16x32_bf16 a[152:155], v[160:163], v[136:139], a[152:155]\n"
    "ds_read_b128 v[180:183], v207 offset:19456\n"
    "v_cvt_pk_bf16_f32 v49, v98, v99\n"
    "v_mfma_f32_16x16x32_bf16 a[152:155], v[164:167], v[152:155], a[152:155]\n"
    "v_accvgpr_read_b32 v100, a132\n"
    "v_accvgpr_read_b32 v101, a133\n"
    "v_mfma_f32_16x16x32_bf16 a[156:159], v[160:163], v[140:143], a[156:159]\n"
    "v_accvgpr_read_b32 v102, a134\n"
    "v_accvgpr_read_b32 v103, a135\n"
    "v_mfma_f32_16x16x32_bf16 a[156:159], v[164:167], v[156:159], a[156:159]\n"
    "s_waitcnt lgkmcnt(2)\n"
    "v_fma_f32 v100, v100, v224, s94\n"
    "v_fma_f32 v101, v101, v224, s94\n"
    "v_mfma_f32_16x16x32_bf16 a[156:159], v[168:171], v[44:47], a[156:159]\n"
    "ds_read_b128 v[160:163], v201 offset:20480\n"
    "v_fma_f32 v102, v102, v224, s94\n"
    "v_mfma_f32_16x16x32_bf16 a[156:159], v[172:175], v[76:79], a[156:159]\n"
    "v_fma_f32 v103, v103, v224, s94\n"
    "v_cvt_pk_bf16_f32 v52, v100, v101\n"
    "v_mfma_f32_16x16x32_bf16 a[152:155], v[168:171], v[40:43], a[152:155]\n"
    "v_cvt_pk_bf16_f32 v53, v102, v103\n"
    "v_accvgpr_read_b32 v104, a136\n"
    "v_mfma_f32_16x16x32_bf16 a[152:155], v[172:175], v[72:75], a[152:155]\n"
    "v_accvgpr_read_b32 v105, a137\n"
    "v_accvgpr_read_b32 v106, a138\n"
    "v_mfma_f32_16x16x32_bf16 a[148:151], v[168:171], v[36:39], a[148:151]\n"
    "ds_read_b128 v[164:167], v201 offset:21504\n"
    "v_accvgpr_read_b32 v107, a139\n"
    "v_mfma_f32_16x16x32_bf16 a[148:151], v[172:175], v[68:71], a[148:151]\n"
    "v_fma_f32 v104, v104, v226, s94\n"
    "v_fma_f32 v105, v105, v226, s94\n"
    "v_mfma_f32_16x16x32_bf16 a[144:147], v[168:171], v[32:35], a[144:147]\n"
    "v_fma_f32 v106, v106, v226, s94\n"
    "v_fma_f32 v107, v107, v226, s94\n"
    "v_mfma_f32_16x16x32_bf16 a[144:147], v[172:175], v[64:67], a[144:147]\n"
    "s_waitcnt lgkmcnt(2)\n"
    "v_cvt_pk_bf16_f32 v56, v104, v105\n"
    "v_cvt_pk_bf16_f32 v57, v106, v107\n"
    "v_mfma_f32_16x16x32_bf16 a[144:147], v[176:179], v[232:235], a[144:147]\n"
    "ds_read_b128 v[168:171], v201 offset:45056\n"
    "v_accvgpr_read_b32 v108, a140\n"
    "v_mfma_f32_16x16x32_bf16 a[144:147], v[180:183], v[80:83], a[144:147]\n"
    "v_accvgpr_read_b32 v109, a141\n"
    "v_accvgpr_read_b32 v110, a142\n"
    "v_mfma_f32_16x16x32_bf16 a[148:151], v[176:179], v[236:239], a[148:151]\n"
    "v_accvgpr_read_b32 v111, a143\n"
    "v_fma_f32 v108, v108, v228, s94\n"
    "v_mfma_f32_16x16x32_bf16 a[148:151], v[180:183], v[84:87], a[148:151]\n"
    "v_fma_f32 v109, v109, v228, s94\n"
    "v_fma_f32 v110, v110, v228, s94\n"
    "v_mfma_f32_16x16x32_bf16 a[152:155], v[176:179], v[240:243], a[152:155]\n"
    "ds_read_b128 v[172:175], v201 offset:46080\n"
    "v_fma_f32 v111, v111, v228, s94\n"
    "v_mfma_f32_16x16x32_bf16 a[152:155], v[180:183], v[88:91], a[152:155]\n"
    "v_cvt_pk_bf16_f32 v60, v108, v109\n"
    "v_cvt_pk_bf16_f32 v61, v110, v111\n"
    "v_mfma_f32_16x16x32_bf16 a[156:159], v[176:179], v[244:247], a[156:159]\n"
    "v_mfma_f32_16x16x32_bf16 a[156:159], v[180:183], v[92:95], a[156:159]\n"
    "s_waitcnt lgkmcnt(2)\n"
    "v_accvgpr_read_b32 v96, a144\n"
    "v_accvgpr_read_b32 v97, a145\n"
    "v_mfma_f32_16x16x32_bf16 a[160:163], v[160:163], v[128:131], a[160:163]\n"
    "ds_read_b128 v[176:179], v207 offset:20480\n"
    "v_accvgpr_read_b32 v98, a146\n"
    "v_mfma_f32_16x16x32_bf16 a[160:163], v[164:167], v[144:147], a[160:163]\n"
    "v_accvgpr_read_b32 v99, a147\n"
    "v_fma_f32 v96, v96, v222, s94\n"
    "v_mfma_f32_16x16x32_bf16 a[164:167], v[160:163], v[132:135], a[164:167]\n"
    "v_fma_f32 v97, v97, v222, s94\n"
    "v_fma_f32 v98, v98, v222, s94\n"
    "v_mfma_f32_16x16x32_bf16 a[164:167], v[164:167], v[148:151], a[164:167]\n"
    "v_fma_f32 v99, v99, v222, s94\n"
    "v_cvt_pk_bf16_f32 v50, v96, v97\n"
    "v_mfma_f32_16x16x32_bf16 a[168:171], v[160:163], v[136:139], a[168:171]\n"
    "ds_read_b128 v[180:183], v207 offset:21504\n"
    "v_cvt_pk_bf16_f32 v51, v98, v99\n"
    "v_mfma_f32_16x16x32_bf16 a[168:171], v[164:167], v[152:155], a[168:171]\n"
    "buffer_store_dwordx4 v[48:51], v205, s[28:31], 0 offen offset:256 sc0 sc1\n"
    "v_accvgpr_read_b32 v100, a148\n"
    "v_accvgpr_read_b32 v101, a149\n"
    "v_accvgpr_read_b32 v102, a150\n"
    "v_mfma_f32_16x16x32_bf16 a[172:175], v[160:163], v[140:143], a[172:175]\n"
    "v_accvgpr_read_b32 v103, a151\n"
    "v_fma_f32 v100, v100, v224, s94\n"
    "v_mfma_f32_16x16x32_bf16 a[172:175], v[164:167], v[156:159], a[172:175]\n"
    "s_waitcnt lgkmcnt(2)\n"
    "v_fma_f32 v101, v101, v224, s94\n"
    "v_fma_f32 v102, v102, v224, s94\n"
    "v_mfma_f32_16x16x32_bf16 a[172:175], v[168:171], v[44:47], a[172:175]\n"
    "ds_read_b128 v[160:163], v201 offset:22528\n"
    "v_fma_f32 v103, v103, v224, s94\n"
    "v_mfma_f32_16x16x32_bf16 a[172:175], v[172:175], v[76:79], a[172:175]\n"
    "v_cvt_pk_bf16_f32 v54, v100, v101\n"
    "v_cvt_pk_bf16_f32 v55, v102, v103\n"
    "v_mfma_f32_16x16x32_bf16 a[168:171], v[168:171], v[40:43], a[168:171]\n"
    "buffer_store_dwordx4 v[52:55], v205, s[28:31], s62 offen offset:256 sc0 sc1\n"
    "v_accvgpr_read_b32 v104, a152\n"
    "v_accvgpr_read_b32 v105, a153\n"
    "v_accvgpr_read_b32 v106, a154\n"
    "v_mfma_f32_16x16x32_bf16 a[168:171], v[172:175], v[72:75], a[168:171]\n"
    "v_accvgpr_read_b32 v107, a155\n"
    "v_fma_f32 v104, v104, v226, s94\n"
    "v_mfma_f32_16x16x32_bf16 a[164:167], v[168:171], v[36:39], a[164:167]\n"
    "ds_read_b128 v[164:167], v201 offset:23552\n"
    "v_fma_f32 v105, v105, v226, s94\n"
    "v_mfma_f32_16x16x32_bf16 a[164:167], v[172:175], v[68:71], a[164:167]\n"
    "v_fma_f32 v106, v106, v226, s94\n"
    "v_fma_f32 v107, v107, v226, s94\n"
    "v_mfma_f32_16x16x32_bf16 a[160:163], v[168:171], v[32:35], a[160:163]\n"
    "v_cvt_pk_bf16_f32 v58, v104, v105\n"
    "v_cvt_pk_bf16_f32 v59, v106, v107\n"
    "v_mfma_f32_16x16x32_bf16 a[160:163], v[172:175], v[64:67], a[160:163]\n"
    "s_waitcnt lgkmcnt(2)\n"
    "buffer_store_dwordx4 v[56:59], v205, s[28:31], s63 offen offset:256 sc0 sc1\n"
    "v_accvgpr_read_b32 v108, a156\n"
    "v_accvgpr_read_b32 v109, a157\n"
    "v_accvgpr_read_b32 v110, a158\n"
    "v_mfma_f32_16x16x32_bf16 a[160:163], v[176:179], v[232:235], a[160:163]\n"
    "ds_read_b128 v[168:171], v201 offset:47104\n"
    "v_accvgpr_read_b32 v111, a159\n"
    "v_mfma_f32_16x16x32_bf16 a[160:163], v[180:183], v[80:83], a[160:163]\n"
    "v_fma_f32 v108, v108, v228, s94\n"
    "v_fma_f32 v109, v109, v228, s94\n"
    "v_mfma_f32_16x16x32_bf16 a[164:167], v[176:179], v[236:239], a[164:167]\n"
    "v_fma_f32 v110, v110, v228, s94\n"
    "v_fma_f32 v111, v111, v228, s94\n"
    "v_mfma_f32_16x16x32_bf16 a[164:167], v[180:183], v[84:87], a[164:167]\n"
    "v_cvt_pk_bf16_f32 v62, v108, v109\n"
    "v_cvt_pk_bf16_f32 v63, v110, v111\n"
    "v_mfma_f32_16x16x32_bf16 a[168:171], v[176:179], v[240:243], a[168:171]\n"
    "ds_read_b128 v[172:175], v201 offset:48128\n"
    "v_mfma_f32_16x16x32_bf16 a[168:171], v[180:183], v[88:91], a[168:171]\n"
    "buffer_store_dwordx4 v[60:63], v205, s[28:31], s64 offen offset:256 sc0 sc1\n"
    "v_mfma_f32_16x16x32_bf16 a[172:175], v[176:179], v[244:247], a[172:175]\n"
    "v_mfma_f32_16x16x32_bf16 a[172:175], v[180:183], v[92:95], a[172:175]\n"
    "s_waitcnt lgkmcnt(2)\n"
    "v_accvgpr_read_b32 v96, a160\n"
    "v_accvgpr_read_b32 v97, a161\n"
    "v_mfma_f32_16x16x32_bf16 a[176:179], v[160:163], v[128:131], a[176:179]\n"
    "ds_read_b128 v[176:179], v207 offset:22528\n"
    "v_accvgpr_read_b32 v98, a162\n"
    "v_mfma_f32_16x16x32_bf16 a[176:179], v[164:167], v[144:147], a[176:179]\n"
    "v_accvgpr_read_b32 v99, a163\n"
    "v_fma_f32 v96, v96, v222, s94\n"
    "v_mfma_f32_16x16x32_bf16 a[180:183], v[160:163], v[132:135], a[180:183]\n"
    "v_fma_f32 v97, v97, v222, s94\n"
    "v_fma_f32 v98, v98, v222, s94\n"
    "v_mfma_f32_16x16x32_bf16 a[180:183], v[164:167], v[148:151], a[180:183]\n"
    "v_fma_f32 v99, v99, v222, s94\n"
    "v_cvt_pk_bf16_f32 v184, v96, v97\n"
    "v_mfma_f32_16x16x32_bf16 a[184:187], v[160:163], v[136:139], a[184:187]\n"
    "ds_read_b128 v[180:183], v207 offset:23552\n"
    "v_cvt_pk_bf16_f32 v185, v98, v99\n"
    "v_mfma_f32_16x16x32_bf16 a[184:187], v[164:167], v[152:155], a[184:187]\n"
    "v_accvgpr_read_b32 v100, a164\n"
    "v_accvgpr_read_b32 v101, a165\n"
    "v_mfma_f32_16x16x32_bf16 a[188:191], v[160:163], v[140:143], a[188:191]\n"
    "v_accvgpr_read_b32 v102, a166\n"
    "v_accvgpr_read_b32 v103, a167\n"
    "v_mfma_f32_16x16x32_bf16 a[188:191], v[164:167], v[156:159], a[188:191]\n"
    "s_waitcnt lgkmcnt(2)\n"
    "v_fma_f32 v100, v100, v224, s94\n"
    "v_fma_f32 v101, v101, v224, s94\n"
    "v_mfma_f32_16x16x32_bf16 a[188:191], v[168:171], v[44:47], a[188:191]\n"
    "v_fma_f32 v102, v102, v224, s94\n"
    "v_fma_f32 v103, v103, v224, s94\n"
    "v_mfma_f32_16x16x32_bf16 a[188:191], v[172:175], v[76:79], a[188:191]\n"
    "v_cvt_pk_bf16_f32 v188, v100, v101\n"
    "v_cvt_pk_bf16_f32 v189, v102, v103\n"
    "v_mfma_f32_16x16x32_bf16 a[184:187], v[168:171], v[40:43], a[184:187]\n"
    "v_accvgpr_read_b32 v104, a168\n"
    "v_accvgpr_read_b32 v105, a169\n"
    "v_mfma_f32_16x16x32_bf16 a[184:187], v[172:175], v[72:75], a[184:187]\n"
    "v_accvgpr_read_b32 v106, a170\n"
    "v_accvgpr_read_b32 v107, a171\n"
    "v_mfma_f32_16x16x32_bf16 a[180:183], v[168:171], v[36:39], a[180:183]\n"
    "v_fma_f32 v104, v104, v226, s94\n"
    "v_fma_f32 v105, v105, v226, s94\n"
    "v_mfma_f32_16x16x32_bf16 a[180:183], v[172:175], v[68:71], a[180:183]\n"
    "v_fma_f32 v106, v106, v226, s94\n"
    "v_fma_f32 v107, v107, v226, s94\n"
    "v_mfma_f32_16x16x32_bf16 a[176:179], v[168:171], v[32:35], a[176:179]\n"
    "v_cvt_pk_bf16_f32 v248, v104, v105\n"
    "v_cvt_pk_bf16_f32 v249, v106, v107\n"
    "v_mfma_f32_16x16x32_bf16 a[176:179], v[172:175], v[64:67], a[176:179]\n"
    "s_waitcnt lgkmcnt(0)\n"
    "v_accvgpr_read_b32 v108, a172\n"
    "v_accvgpr_read_b32 v109, a173\n"
    "v_mfma_f32_16x16x32_bf16 a[176:179], v[176:179], v[232:235], a[176:179]\n"
    "v_accvgpr_read_b32 v110, a174\n"
    "v_accvgpr_read_b32 v111, a175\n"
    "v_mfma_f32_16x16x32_bf16 a[176:179], v[180:183], v[80:83], a[176:179]\n"
    "v_fma_f32 v108, v108, v228, s94\n"
    "v_fma_f32 v109, v109, v228, s94\n"
    "v_mfma_f32_16x16x32_bf16 a[180:183], v[176:179], v[236:239], a[180:183]\n"
    "v_fma_f32 v110, v110, v228, s94\n"
    "v_fma_f32 v111, v111, v228, s94\n"
    "v_mfma_f32_16x16x32_bf16 a[180:183], v[180:183], v[84:87], a[180:183]\n"
    "v_cvt_pk_bf16_f32 v252, v108, v109\n"
    "v_cvt_pk_bf16_f32 v253, v110, v111\n"
    "v_mfma_f32_16x16x32_bf16 a[184:187], v[176:179], v[240:243], a[184:187]\n"
    "v_mfma_f32_16x16x32_bf16 a[184:187], v[180:183], v[88:91], a[184:187]\n"
    "v_mfma_f32_16x16x32_bf16 a[188:191], v[176:179], v[244:247], a[188:191]\n"
    "v_mfma_f32_16x16x32_bf16 a[188:191], v[180:183], v[92:95], a[188:191]\n"
    "v_accvgpr_read_b32 v96, a176\n"
    "v_accvgpr_read_b32 v97, a177\n"
    "v_accvgpr_read_b32 v98, a178\n"
    "v_accvgpr_read_b32 v99, a179\n"
    "v_fma_f32 v96, v96, v222, s94\n"
    "v_fma_f32 v97, v97, v222, s94\n"
    "v_fma_f32 v98, v98, v222, s94\n"
    "v_fma_f32 v99, v99, v222, s94\n"
    "v_cvt_pk_bf16_f32 v186, v96, v97\n"
    "v_cvt_pk_bf16_f32 v187, v98, v99\n"
    "buffer_store_dwordx4 v[184:187], v205, s[28:31], 0 offen offset:320 sc0 sc1\n"
    "v_accvgpr_read_b32 v100, a180\n"
    "v_accvgpr_read_b32 v101, a181\n"
    "v_accvgpr_read_b32 v102, a182\n"
    "v_accvgpr_read_b32 v103, a183\n"
    "v_fma_f32 v100, v100, v224, s94\n"
    "v_fma_f32 v101, v101, v224, s94\n"
    "v_fma_f32 v102, v102, v224, s94\n"
    "v_fma_f32 v103, v103, v224, s94\n"
    "v_cvt_pk_bf16_f32 v190, v100, v101\n"
    "v_cvt_pk_bf16_f32 v191, v102, v103\n"
    "buffer_store_dwordx4 v[188:191], v205, s[28:31], s62 offen offset:320 sc0 sc1\n"
    "v_accvgpr_read_b32 v104, a184\n"
    "v_accvgpr_read_b32 v105, a185\n"
    "v_accvgpr_read_b32 v106, a186\n"
    "v_accvgpr_read_b32 v107, a187\n"
    "v_fma_f32 v104, v104, v226, s94\n"
    "v_fma_f32 v105, v105, v226, s94\n"
    "v_fma_f32 v106, v106, v226, s94\n"
    "v_fma_f32 v107, v107, v226, s94\n"
    "v_cvt_pk_bf16_f32 v250, v104, v105\n"
    "v_cvt_pk_bf16_f32 v251, v106, v107\n"
    "buffer_store_dwordx4 v[248:251], v205, s[28:31], s63 offen offset:320 sc0 sc1\n"
    "v_accvgpr_read_b32 v108, a188\n"
    "v_accvgpr_read_b32 v109, a189\n"
    "v_accvgpr_read_b32 v110, a190\n"
    "v_accvgpr_read_b32 v111, a191\n"
    "v_fma_f32 v108, v108, v228, s94\n"
    "v_fma_f32 v109, v109, v228, s94\n"
    "v_fma_f32 v110, v110, v228, s94\n"
    "v_fma_f32 v111, v111, v228, s94\n"
    "v_cvt_pk_bf16_f32 v254, v108, v109\n"
    "v_cvt_pk_bf16_f32 v255, v110, v111\n"
    "buffer_store_dwordx4 v[252:255], v205, s[28:31], s64 offen offset:320 sc0 sc1\n"
    "v_add_f32 v196, v112, v116\n"
    "v_add_f32 v197, v120, v124\n"
    "v_add_f32 v196, v196, v197\n"
    "v_cmp_ngt_f32 vcc, 0x45800000, v196\n"
    "s_barrier\n"
    "s_cbranch_vccnz LSLOW\n"
    "LFIN:\n"
    "s_branch LEND\n"
    "LSPRE0:\n"
    "LSPIN0:\n"
    "s_sleep 1\n"
    "buffer_load_dwordx2 v[218:219], v213, s[44:47], 0 offen sc1\n"
    "s_waitcnt vmcnt(0)\n"
    "s_add_u32 s73, s73, 1\n"
    "s_cmp_gt_u32 s73, 200000\n"
    "s_cbranch_scc1 LSPB0\n"
    "v_cmp_ne_u32 s[80:81], s67, v219\n"
    "v_cmp_gt_u32 vcc, s66, v218\n"
    "s_or_b64 vcc, vcc, s[80:81]\n"
    "s_cbranch_vccnz LSPIN0\n"
    "s_branch LSPB0\n"
    "LSPRE1:\n"
    "LSPIN1:\n"
    "s_sleep 1\n"
    "buffer_load_dwordx2 v[218:219], v213, s[44:47], 0 offen sc1\n"
    "s_waitcnt vmcnt(0)\n"
    "s_add_u32 s73, s73, 1\n"
    "s_cmp_gt_u32 s73, 200000\n"
    "s_cbranch_scc1 LSPB1\n"
    "v_cmp_ne_u32 s[80:81], s67, v219\n"
    "v_cmp_gt_u32 vcc, s66, v218\n"
    "s_or_b64 vcc, vcc, s[80:81]\n"
    "s_cbranch_vccnz LSPIN1\n"
    "s_branch LSPB1\n"
    "LSPRE2:\n"
    "LSPIN2:\n"
    "s_sleep 1\n"
    "buffer_load_dwordx2 v[218:219], v213, s[44:47], 0 offen sc1\n"
    "s_waitcnt vmcnt(0)\n"
    "s_add_u32 s73, s73, 1\n"
    "s_cmp_gt_u32 s73, 200000\n"
    "s_cbranch_scc1 LSPB2\n"
    "v_cmp_ne_u32 s[80:81], s67, v219\n"
    "v_cmp_gt_u32 vcc, s66, v218\n"
    "s_or_b64 vcc, vcc, s[80:81]\n"
    "s_cbranch_vccnz LSPIN2\n"
    "s_branch LSPB2\n"
    "LSPRE3:\n"
    "LSPIN3:\n"
    "s_sleep 1\n"
    "buffer_load_dwordx2 v[218:219], v213, s[44:47], 0 offen sc1\n"
    "s_waitcnt vmcnt(0)\n"
    "s_add_u32 s73, s73, 1\n"
    "s_cmp_gt_u32 s73, 200000\n"
    "s_cbranch_scc1 LSPB3\n"
    "v_cmp_ne_u32 s[80:81], s67, v219\n"
    "v_cmp_gt_u32 vcc, s66, v218\n"
    "s_or_b64 vcc, vcc, s[80:81]\n"
    "s_cbranch_vccnz LSPIN3\n"
    "s_branch LSPB3\n"
    "LSPRE4:\n"
    "LSPIN4:\n"
    "s_sleep 1\n"
    "buffer_load_dwordx2 v[218:219], v213, s[44:47], 0 offen sc1\n"
    "s_waitcnt vmcnt(0)\n"
    "s_add_u32 s73, s73, 1\n"
    "s_cmp_gt_u32 s73, 200000\n"
    "s_cbranch_scc1 LSPB4\n"
    "v_cmp_ne_u32 s[80:81], s67, v219\n"
    "v_cmp_gt_u32 vcc, s66, v218\n"
    "s_or_b64 vcc, vcc, s[80:81]\n"
    "s_cbranch_vccnz LSPIN4\n"
    "s_branch LSPB4\n"
    "LSPRE5:\n"
    "LSPIN5:\n"
    "s_sleep 1\n"
    "buffer_load_dwordx2 v[218:219], v213, s[44:47], 0 offen sc1\n"
    "s_waitcnt vmcnt(0)\n"
    "s_add_u32 s73, s73, 1\n"
    "s_cmp_gt_u32 s73, 200000\n"
    "s_cbranch_scc1 LSPB5\n"
    "v_cmp_ne_u32 s[80:81], s67, v219\n"
    "v_cmp_gt_u32 vcc, s66, v218\n"
    "s_or_b64 vcc, vcc, s[80:81]\n"
    "s_cbranch_vccnz LSPIN5\n"
    "s_branch LSPB5\n"
    "LSLOW:\n"
    "s_waitcnt vmcnt(0)\n"
    "s_nop 15\n"
    "s_mul_i32 s62, s58, 0x9000\n"
    "v_add_u32 v205, s62, v200\n"
    "s_mov_b32 s63, 0\n"
    "v_mov_b32 v128, 0\n"
    "v_mov_b32 v129, 0\n"
    "v_mov_b32 v130, 0\n"
    "v_mov_b32 v131, 0\n"
    "v_mov_b32 v96, 0xff61b1e6\n"
    "v_mov_b32 v100, 0xff61b1e6\n"
    "v_mov_b32 v104, 0xff61b1e6\n"
    "v_mov_b32 v108, 0xff61b1e6\n"
    "v_mfma_f32_16x16x32_bf16 a[0:3], v[128:131], v[128:131], 0\n"
    "v_mfma_f32_16x16x32_bf16 a[4:7], v[128:131], v[128:131], 0\n"
    "v_mfma_f32_16x16x32_bf16 a[8:11], v[128:131], v[128:131], 0\n"
    "v_mfma_f32_16x16x32_bf16 a[12:15], v[128:131], v[128:131], 0\n"
    "v_mfma_f32_16x16x32_bf16 a[16:19], v[128:131], v[128:131], 0\n"
    "v_mfma_f32_16x16x32_bf16 a[20:23], v[128:131], v[128:131], 0\n"
    "v_mfma_f32_16x16x32_bf16 a[24:27], v[128:131], v[128:131], 0\n"
    "v_mfma_f32_16x16x32_bf16 a[28:31], v[128:131], v[128:131], 0\n"
    "v_mfma_f32_16x16x32_bf16 a[32:35], v[128:131], v[128:131], 0\n"
    "v_mfma_f32_16x16x32_bf16 a[36:39], v[128:131], v[128:131], 0\n"
    "v_mfma_f32_16x16x32_bf16 a[40:43], v[128:131], v[128:131], 0\n"
    "v_mfma_f32_16x16x32_bf16 a[44:47], v[128:131], v[128:131], 0\n"
    "v_mfma_f32_16x16x32_bf16 a[48:51], v[128:131], v[128:131], 0\n"
    "v_mfma_f32_16x16x32_bf16 a[52:55], v[128:131], v[128:131], 0\n"
    "v_mfma_f32_16x16x32_bf16 a[56:59], v[128:131], v[128:131], 0\n"
    "v_mfma_f32_16x16x32_bf16 a[60:63], v[128:131], v[128:131], 0\n"
    "v_mfma_f32_16x16x32_bf16 a[64:67], v[128:131], v[128:131], 0\n"
    "v_mfma_f32_16x16x32_bf16 a[68:71], v[128:131], v[128:131], 0\n"
    "v_mfma_f32_16x16x32_bf16 a[72:75], v[128:131], v[128:131], 0\n"
    "v_mfma_f32_16x16x32_bf16 a[76:79], v[128:131], v[128:131], 0\n"
    "v_mfma_f32_16x16x32_bf16 a[80:83], v[128:131], v[128:131], 0\n"
    "v_mfma_f32_16x16x32_bf16 a[84:87], v[128:131], v[128:131], 0\n"
    "v_mfma_f32_16x16x32_bf16 a[88:91], v[128:131], v[128:131], 0\n"
    "v_mfma_f32_16x16x32_bf16 a[92:95], v[128:131], v[128:131], 0\n"
    "v_mfma_f32_16x16x32_bf16 a[96:99], v[128:131], v[128:131], 0\n"
    "v_mfma_f32_16x16x32_bf16 a[100:103], v[128:131], v[128:131], 0\n"
    "v_mfma_f32_16x16x32_bf16 a[104:107], v[128:131], v[128:131], 0\n"
    "v_mfma_f32_16x16x32_bf16 a[108:111], v[128:131], v[128:131], 0\n"
    "v_mfma_f32_16x16x32_bf16 a[112:115], v[128:131], v[128:131], 0\n"
    "v_mfma_f32_16x16x32_bf16 a[116:119], v[128:131], v[128:131], 0\n"
    "v_mfma_f32_16x16x32_bf16 a[120:123], v[128:131], v[128:131], 0\n"
    "v_mfma_f32_16x16x32_bf16 a[124:127], v[128:131], v[128:131], 0\n"
    "v_mfma_f32_16x16x32_bf16 a[128:131], v[128:131], v[128:131], 0\n"
    "v_mfma_f32_16x16x32_bf16 a[132:135], v[128:131], v[128:131], 0\n"
    "v_mfma_f32_16x16x32_bf16 a[136:139], v[128:131], v[128:131], 0\n"
    "v_mfma_f32_16x16x32_bf16 a[140:143], v[128:131], v[128:131], 0\n"
    "v_mfma_f32_16x16x32_bf16 a[144:147], v[128:131], v[128:131], 0\n"
    "v_mfma_f32_16x16x32_bf16 a[148:151], v[128:131], v[128:131], 0\n"
    "v_mfma_f32_16x16x32_bf16 a[152:155], v[128:131], v[128:131], 0\n"
    "v_mfma_f32_16x16x32_bf16 a[156:159], v[128:131], v[128:131], 0\n"
    "v_mfma_f32_16x16x32_bf16 a[160:163], v[128:131], v[128:131], 0\n"
    "v_mfma_f32_16x16x32_bf16 a[164:167], v[128:131], v[128:131], 0\n"
    "v_mfma_f32_16x16x32_bf16 a[168:171], v[128:131], v[128:131], 0\n"
    "v_mfma_f32_16x16x32_bf16 a[172:175], v[128:131], v[128:131], 0\n"
    "v_mfma_f32_16x16x32_bf16 a[176:179], v[128:131], v[128:131], 0\n"
    "v_mfma_f32_16x16x32_bf16 a[180:183], v[128:131], v[128:131], 0\n"
    "v_mfma_f32_16x16x32_bf16 a[184:187], v[128:131], v[128:131], 0\n"
    "v_mfma_f32_16x16x32_bf16 a[188:191], v[128:131], v[128:131], 0\n"
    "v_mfma_f32_16x16x32_bf16 v[112:115], v[128:131], v[128:131], 0\n"
    "v_mfma_f32_16x16x32_bf16 v[116:119], v[128:131], v[128:131], 0\n"
    "v_mfma_f32_16x16x32_bf16 v[120:123], v[128:131], v[128:131], 0\n"
    "v_mfma_f32_16x16x32_bf16 v[124:127], v[128:131], v[128:131], 0\n"
    "s_nop 15\n"
    "LSLOOP:\n"
    "s_add_u32 s78, s63, 0\n"
    "s_add_u32 m0, s62, 0\n"
    "s_nop 0\n"
    "buffer_load_dwordx4 v200, s[36:39], s78 offen sc1 lds\n"
    "buffer_load_dwordx4 v200, s[36:39], s78 offen offset:1024 sc1 lds\n"
    "buffer_load_dwordx4 v200, s[36:39], s78 offen offset:2048 sc1 lds\n"
    "buffer_load_dwordx4 v200, s[36:39], s78 offen offset:3072 sc1 lds\n"
    "s_add_u32 s78, s63, 0x1000\n"
    "s_add_u32 m0, s62, 0x1000\n"
    "s_nop 0\n"
    "buffer_load_dwordx4 v200, s[36:39], s78 offen sc1 lds\n"
    "buffer_load_dwordx4 v200, s[36:39], s78 offen offset:1024 sc1 lds\n"
    "buffer_load_dwordx4 v200, s[36:39], s78 offen offset:2048 sc1 lds\n"
    "buffer_load_dwordx4 v200, s[36:39], s78 offen offset:3072 sc1 lds\n"
    "s_add_u32 s78, s63, 0x2000\n"
    "s_add_u32 m0, s62, 0x2000\n"
    "s_nop 0\n"
    "buffer_load_dwordx4 v200, s[36:39], s78 offen sc1 lds\n"
    "buffer_load_dwordx4 v200, s[36:39], s78 offen offset:1024 sc1 lds\n"
    "buffer_load_dwordx4 v200, s[36:39], s78 offen offset:2048 sc1 lds\n"
    "buffer_load_dwordx4 v200, s[36:39], s78 offen offset:3072 sc1 lds\n"
    "s_add_u32 s78, s63, 0\n"
    "s_add_u32 m0, s62, 0x3000\n"
    "s_nop 0\n"
    "buffer_load_dwordx4 v200, s[40:43], s78 offen sc1 lds\n"
    "buffer_load_dwordx4 v200, s[40:43], s78 offen offset:1024 sc1 lds\n"
    "buffer_load_dwordx4 v200, s[40:43], s78 offen offset:2048 sc1 lds\n"
    "buffer_load_dwordx4 v200, s[40:43], s78 offen offset:3072 sc1 lds\n"
    "s_add_u32 s78, s63, 0x1000\n"
    "s_add_u32 m0, s62, 0x4000\n"
    "s_nop 0\n"
    "buffer_load_dwordx4 v200, s[40:43], s78 offen sc1 lds\n"
    "buffer_load_dwordx4 v200, s[40:43], s78 offen offset:1024 sc1 lds\n"
    "buffer_load_dwordx4 v200, s[40:43], s78 offen offset:2048 sc1 lds\n"
    "buffer_load_dwordx4 v200, s[40:43], s78 offen offset:3072 sc1 lds\n"
    "s_add_u32 s78, s63, 0x2000\n"
    "s_add_u32 m0, s62, 0x5000\n"
    "s_nop 0\n"
    "buffer_load_dwordx4 v200, s[40:43], s78 offen sc1 lds\n"
    "buffer_load_dwordx4 v200, s[40:43], s78 offen offset:1024 sc1 lds\n"
    "buffer_load_dwordx4 v200, s[40:43], s78 offen offset:2048 sc1 lds\n"
    "buffer_load_dwordx4 v200, s[40:43], s78 offen offset:3072 sc1 lds\n"
    "s_add_u32 s78, s63, 0x3000\n"
    "s_add_u32 m0, s62, 0x6000\n"
    "s_nop 0\n"
    "buffer_load_dwordx4 v200, s[40:43], s78 offen sc1 lds\n"
    "buffer_load_dwordx4 v200, s[40:43], s78 offen offset:1024 sc1 lds\n"
    "buffer_load_dwordx4 v200, s[40:43], s78 offen offset:2048 sc1 lds\n"
    "buffer_load_dwordx4 v200, s[40:43], s78 offen offset:3072 sc1 lds\n"
    "s_add_u32 s78, s63, 0x4000\n"
    "s_add_u32 m0, s62, 0x7000\n"
    "s_nop 0\n"
    "buffer_load_dwordx4 v200, s[40:43], s78 offen sc1 lds\n"
    "buffer_load_dwordx4 v200, s[40:43], s78 offen offset:1024 sc1 lds\n"
    "buffer_load_dwordx4 v200, s[40:43], s78 offen offset:2048 sc1 lds\n"
    "buffer_load_dwordx4 v200, s[40:43], s78 offen offset:3072 sc1 lds\n"
    "s_add_u32 s78, s63, 0x5000\n"
    "s_add_u32 m0, s62, 0x8000\n"
    "s_nop 0\n"
    "buffer_load_dwordx4 v200, s[40:43], s78 offen sc1 lds\n"
    "buffer_load_dwordx4 v200, s[40:43], s78 offen offset:1024 sc1 lds\n"
    "buffer_load_dwordx4 v200, s[40:43], s78 offen offset:2048 sc1 lds\n"
    "buffer_load_dwordx4 v200, s[40:43], s78 offen offset:3072 sc1 lds\n"
    "s_waitcnt vmcnt(0)\n"
    "ds_read_b128 v[160:163], v205\n"
    "ds_read_b128 v[164:167], v205 offset:1024\n"
    "s_waitcnt lgkmcnt(1)\n"
    "v_mfma_f32_16x16x32_bf16 v[32:35], v[160:163], a[192:195], 0\n"
    "s_waitcnt lgkmcnt(0)\n"
    "v_mfma_f32_16x16x32_bf16 v[32:35], v[164:167], a[196:199], v[32:35]\n"
    "v_mfma_f32_16x16x32_bf16 v[36:39], v[164:167], a[220:223], 0\n"
    "v_mfma_f32_16x16x32_bf16 v[36:39], v[160:163], a[216:219], v[36:39]\n"
    "v_mfma_f32_16x16x32_bf16 v[40:43], v[160:163], a[240:243], 0\n"
    "v_mfma_f32_16x16x32_bf16 v[40:43], v[164:167], a[244:247], v[40:43]\n"
    "v_mfma_f32_16x16x32_bf16 v[44:47], v[164:167], v[12:15], 0\n"
    "v_mfma_f32_16x16x32_bf16 v[44:47], v[160:163], v[8:11], v[44:47]\n"
    "ds_read_b128 v[168:171], v205 offset:2048\n"
    "ds_read_b128 v[172:175], v205 offset:3072\n"
    "s_waitcnt lgkmcnt(1)\n"
    "v_mfma_f32_16x16x32_bf16 v[44:47], v[168:171], v[16:19], v[44:47]\n"
    "s_waitcnt lgkmcnt(0)\n"
    "v_mfma_f32_16x16x32_bf16 v[44:47], v[172:175], v[20:23], v[44:47]\n"
    "v_mfma_f32_16x16x32_bf16 v[40:43], v[172:175], a[252:255], v[40:43]\n"
    "v_mfma_f32_16x16x32_bf16 v[40:43], v[168:171], a[248:251], v[40:43]\n"
    "v_mfma_f32_16x16x32_bf16 v[36:39], v[168:171], a[224:227], v[36:39]\n"
    "v_mfma_f32_16x16x32_bf16 v[36:39], v[172:175], a[228:231], v[36:39]\n"
    "v_mfma_f32_16x16x32_bf16 v[32:35], v[172:175], a[204:207], v[32:35]\n"
    "v_mfma_f32_16x16x32_bf16 v[32:35], v[168:171], a[200:203], v[32:35]\n"
    "ds_read_b128 v[176:179], v205 offset:4096\n"
    "ds_read_b128 v[180:183], v205 offset:5120\n"
    "s_waitcnt lgkmcnt(1)\n"
    "v_mfma_f32_16x16x32_bf16 v[32:35], v[176:179], a[208:211], v[32:35]\n"
    "s_waitcnt lgkmcnt(0)\n"
    "v_mfma_f32_16x16x32_bf16 v[32:35], v[180:183], a[212:215], v[32:35]\n"
    "v_mfma_f32_16x16x32_bf16 v[36:39], v[180:183], a[236:239], v[36:39]\n"
    "v_mfma_f32_16x16x32_bf16 v[36:39], v[176:179], a[232:235], v[36:39]\n"
    "v_mfma_f32_16x16x32_bf16 v[40:43], v[176:179], v[0:3], v[40:43]\n"
    "v_mfma_f32_16x16x32_bf16 v[40:43], v[180:183], v[4:7], v[40:43]\n"
    "v_mfma_f32_16x16x32_bf16 v[44:47], v[180:183], v[28:31], v[44:47]\n"
    "v_mfma_f32_16x16x32_bf16 v[44:47], v[176:179], v[24:27], v[44:47]\n"
    "ds_read_b128 v[160:163], v205 offset:10240\n"
    "ds_read_b128 v[164:167], v205 offset:11264\n"
    "s_waitcnt lgkmcnt(1)\n"
    "v_mfma_f32_16x16x32_bf16 v[60:63], v[160:163], v[24:27], 0\n"
    "s_waitcnt lgkmcnt(0)\n"
    "v_mfma_f32_16x16x32_bf16 v[60:63], v[164:167], v[28:31], v[60:63]\n"
    "v_mfma_f32_16x16x32_bf16 v[56:59], v[164:167], v[4:7], 0\n"
    "v_mfma_f32_16x16x32_bf16 v[56:59], v[160:163], v[0:3], v[56:59]\n"
    "v_mfma_f32_16x16x32_bf16 v[52:55], v[160:163], a[232:235], 0\n"
    "v_mfma_f32_16x16x32_bf16 v[52:55], v[164:167], a[236:239], v[52:55]\n"
    "v_mfma_f32_16x16x32_bf16 v[48:51], v[164:167], a[212:215], 0\n"
    "v_mfma_f32_16x16x32_bf16 v[48:51], v[160:163], a[208:211], v[48:51]\n"
    "ds_read_b128 v[168:171], v205 offset:8192\n"
    "ds_read_b128 v[172:175], v205 offset:9216\n"
    "s_waitcnt lgkmcnt(1)\n"
    "v_mfma_f32_16x16x32_bf16 v[48:51], v[168:171], a[200:203], v[48:51]\n"
    "s_waitcnt lgkmcnt(0)\n"
    "v_mfma_f32_16x16x32_bf16 v[48:51], v[172:175], a[204:207], v[48:51]\n"
    "v_mfma_f32_16x16x32_bf16 v[52:55], v[172:175], a[228:231], v[52:55]\n"
    "v_mfma_f32_16x16x32_bf16 v[52:55], v[168:171], a[224:227], v[52:55]\n"
    "v_mfma_f32_16x16x32_bf16 v[56:59], v[168:171], a[248:251], v[56:59]\n"
    "v_mfma_f32_16x16x32_bf16 v[56:59], v[172:175], a[252:255], v[56:59]\n"
    "v_mfma_f32_16x16x32_bf16 v[60:63], v[172:175], v[20:23], v[60:63]\n"
    "v_mfma_f32_16x16x32_bf16 v[60:63], v[168:171], v[16:19], v[60:63]\n"
    "ds_read_b128 v[176:179], v205 offset:6144\n"
    "ds_read_b128 v[180:183], v205 offset:7168\n"
    "s_waitcnt lgkmcnt(1)\n"
    "v_mfma_f32_16x16x32_bf16 v[60:63], v[176:179], v[8:11], v[60:63]\n"
    "s_waitcnt lgkmcnt(0)\n"
    "v_mfma_f32_16x16x32_bf16 v[60:63], v[180:183], v[12:15], v[60:63]\n"
    "v_mfma_f32_16x16x32_bf16 v[56:59], v[180:183], a[244:247], v[56:59]\n"
    "v_mfma_f32_16x16x32_bf16 v[56:59], v[176:179], a[240:243], v[56:59]\n"
    "v_mfma_f32_16x16x32_bf16 v[52:55], v[176:179], a[216:219], v[52:55]\n"
    "v_mfma_f32_16x16x32_bf16 v[52:55], v[180:183], a[220:223], v[52:55]\n"
    "v_mfma_f32_16x16x32_bf16 v[48:51], v[180:183], a[196:199], v[48:51]\n"
    "v_mfma_f32_16x16x32_bf16 v[48:51], v[176:179], a[192:195], v[48:51]\n"
    "s_add_u32 s78, s63, 0x3000\n"
    "s_add_u32 m0, s62, 0\n"
    "s_nop 0\n"
    "buffer_load_dwordx4 v200, s[36:39], s78 offen sc1 lds\n"
    "buffer_load_dwordx4 v200, s[36:39], s78 offen offset:1024 sc1 lds\n"
    "buffer_load_dwordx4 v200, s[36:39], s78 offen offset:2048 sc1 lds\n"
    "buffer_load_dwordx4 v200, s[36:39], s78 offen offset:3072 sc1 lds\n"
    "s_add_u32 s78, s63, 0x4000\n"
    "s_add_u32 m0, s62, 0x1000\n"
    "s_nop 0\n"
    "buffer_load_dwordx4 v200, s[36:39], s78 offen sc1 lds\n"
    "buffer_load_dwordx4 v200, s[36:39], s78 offen offset:1024 sc1 lds\n"
    "buffer_load_dwordx4 v200, s[36:39], s78 offen offset:2048 sc1 lds\n"
    "buffer_load_dwordx4 v200, s[36:39], s78 offen offset:3072 sc1 lds\n"
    "s_add_u32 s78, s63, 0x5000\n"
    "s_add_u32 m0, s62, 0x2000\n"
    "s_nop 0\n"
    "buffer_load_dwordx4 v200, s[36:39], s78 offen sc1 lds\n"
    "buffer_load_dwordx4 v200, s[36:39], s78 offen offset:1024 sc1 lds\n"
    "buffer_load_dwordx4 v200, s[36:39], s78 offen offset:2048 sc1 lds\n"
    "buffer_load_dwordx4 v200, s[36:39], s78 offen offset:3072 sc1 lds\n"
    "s_waitcnt vmcnt(0)\n"
    "ds_read_b128 v[160:163], v205\n"
    "ds_read_b128 v[164:167], v205 offset:1024\n"
    "s_waitcnt lgkmcnt(1)\n"
    "v_mfma_f32_16x16x32_bf16 v[64:67], v[160:163], a[192:195], 0\n"
    "s_waitcnt lgkmcnt(0)\n"
    "v_mfma_f32_16x16x32_bf16 v[64:67], v[164:167], a[196:199], v[64:67]\n"
    "v_mfma_f32_16x16x32_bf16 v[68:71], v[164:167], a[220:223], 0\n"
    "v_mfma_f32_16x16x32_bf16 v[68:71], v[160:163], a[216:219], v[68:71]\n"
    "v_mfma_f32_16x16x32_bf16 v[72:75], v[160:163], a[240:243], 0\n"
    "v_mfma_f32_16x16x32_bf16 v[72:75], v[164:167], a[244:247], v[72:75]\n"
    "v_mfma_f32_16x16x32_bf16 v[76:79], v[164:167], v[12:15], 0\n"
    "v_mfma_f32_16x16x32_bf16 v[76:79], v[160:163], v[8:11], v[76:79]\n"
    "ds_read_b128 v[168:171], v205 offset:2048\n"
    "ds_read_b128 v[172:175], v205 offset:3072\n"
    "s_waitcnt lgkmcnt(1)\n"
    "v_mfma_f32_16x16x32_bf16 v[76:79], v[168:171], v[16:19], v[76:79]\n"
    "s_waitcnt lgkmcnt(0)\n"
    "v_mfma_f32_16x16x32_bf16 v[76:79], v[172:175], v[20:23], v[76:79]\n"
    "v_mfma_f32_16x16x32_bf16 v[72:75], v[172:175], a[252:255], v[72:75]\n"
    "v_mfma_f32_16x16x32_bf16 v[72:75], v[168:171], a[248:251], v[72:75]\n"
    "v_mfma_f32_16x16x32_bf16 v[68:71], v[168:171], a[224:227], v[68:71]\n"
    "v_mfma_f32_16x16x32_bf16 v[68:71], v[172:175], a[228:231], v[68:71]\n"
    "v_mfma_f32_16x16x32_bf16 v[64:67], v[172:175], a[204:207], v[64:67]\n"
    "v_mfma_f32_16x16x32_bf16 v[64:67], v[168:171], a[200:203], v[64:67]\n"
    "ds_read_b128 v[176:179], v205 offset:4096\n"
    "ds_read_b128 v[180:183], v205 offset:5120\n"
    "s_waitcnt lgkmcnt(1)\n"
    "v_mfma_f32_16x16x32_bf16 v[64:67], v[176:179], a[208:211], v[64:67]\n"
    "s_waitcnt lgkmcnt(0)\n"
    "v_mfma_f32_16x16x32_bf16 v[64:67], v[180:183], a[212:215], v[64:67]\n"
    "v_mfma_f32_16x16x32_bf16 v[68:71], v[180:183], a[236:239], v[68:71]\n"
    "v_mfma_f32_16x16x32_bf16 v[68:71], v[176:179], a[232:235], v[68:71]\n"
    "v_mfma_f32_16x16x32_bf16 v[72:75], v[176:179], v[0:3], v[72:75]\n"
    "v_mfma_f32_16x16x32_bf16 v[72:75], v[180:183], v[4:7], v[72:75]\n"
    "v_mfma_f32_16x16x32_bf16 v[76:79], v[180:183], v[28:31], v[76:79]\n"
    "v_mfma_f32_16x16x32_bf16 v[76:79], v[176:179], v[24:27], v[76:79]\n"
    "ds_read_b128 v[160:163], v205 offset:10240\n"
    "ds_read_b128 v[164:167], v205 offset:11264\n"
    "s_waitcnt lgkmcnt(1)\n"
    "v_mfma_f32_16x16x32_bf16 v[92:95], v[160:163], v[24:27], 0\n"
    "s_waitcnt lgkmcnt(0)\n"
    "v_mfma_f32_16x16x32_bf16 v[92:95], v[164:167], v[28:31], v[92:95]\n"
    "v_mfma_f32_16x16x32_bf16 v[88:91], v[164:167], v[4:7], 0\n"
    "v_mfma_f32_16x16x32_bf16 v[88:91], v[160:163], v[0:3], v[88:91]\n"
    "v_mfma_f32_16x16x32_bf16 v[84:87], v[160:163], a[232:235], 0\n"
    "v_mfma_f32_16x16x32_bf16 v[84:87], v[164:167], a[236:239], v[84:87]\n"
    "v_mfma_f32_16x16x32_bf16 v[80:83], v[164:167], a[212:215], 0\n"
    "v_mfma_f32_16x16x32_bf16 v[80:83], v[160:163], a[208:211], v[80:83]\n"
    "ds_read_b128 v[168:171], v205 offset:8192\n"
    "ds_read_b128 v[172:175], v205 offset:9216\n"
    "s_waitcnt lgkmcnt(1)\n"
    "v_mfma_f32_16x16x32_bf16 v[80:83], v[168:171], a[200:203], v[80:83]\n"
    "s_waitcnt lgkmcnt(0)\n"
    "v_mfma_f32_16x16x32_bf16 v[80:83], v[172:175], a[204:207], v[80:83]\n"
    "v_mfma_f32_16x16x32_bf16 v[84:87], v[172:175], a[228:231], v[84:87]\n"
    "v_mfma_f32_16x16x32_bf16 v[84:87], v[168:171], a[224:227], v[84:87]\n"
    "v_mfma_f32_16x16x32_bf16 v[88:91], v[168:171], a[248:251], v[88:91]\n"
    "v_mfma_f32_16x16x32_bf16 v[88:91], v[172:175], a[252:255], v[88:91]\n"
    "v_mfma_f32_16x16x32_bf16 v[92:95], v[172:175], v[20:23], v[92:95]\n"
    "v_mfma_f32_16x16x32_bf16 v[92:95], v[168:171], v[16:19], v[92:95]\n"
    "ds_read_b128 v[176:179], v205 offset:6144\n"
    "ds_read_b128 v[180:183], v205 offset:7168\n"
    "s_waitcnt lgkmcnt(1)\n"
    "v_mfma_f32_16x16x32_bf16 v[92:95], v[176:179], v[8:11], v[92:95]\n"
    "s_waitcnt lgkmcnt(0)\n"
    "v_mfma_f32_16x16x32_bf16 v[92:95], v[180:183], v[12:15], v[92:95]\n"
    "v_mfma_f32_16x16x32_bf16 v[88:91], v[180:183], a[244:247], v[88:91]\n"
    "v_mfma_f32_16x16x32_bf16 v[88:91], v[176:179], a[240:243], v[88:91]\n"
    "v_mfma_f32_16x16x32_bf16 v[84:87], v[176:179], a[216:219], v[84:87]\n"
    "v_mfma_f32_16x16x32_bf16 v[84:87], v[180:183], a[220:223], v[84:87]\n"
    "v_mfma_f32_16x16x32_bf16 v[80:83], v[180:183], a[196:199], v[80:83]\n"
    "v_mfma_f32_16x16x32_bf16 v[80:83], v[176:179], a[192:195], v[80:83]\n"
    "v_max3_f32 v196, v32, v33, v34\n"
    "v_max3_f32 v196, v196, v35, v48\n"
    "v_max3_f32 v196, v196, v49, v50\n"
    "v_max3_f32 v196, v196, v51, v64\n"
    "v_max3_f32 v196, v196, v65, v66\n"
    "s_nop 2\n"
    "v_max3_f32 v196, v196, v67, v80\n"
    "v_max3_f32 v196, v196, v81, v82\n"
    "v_max_f32 v196, v196, v83\n"
    "v_mov_b32 v197, v196\n"
    "s_nop 1\n"
    "v_permlane32_swap_b32 v196, v197\n"
    "v_max_f32 v196, v196, v197\n"
    "v_mov_b32 v197, v196\n"
    "s_nop 1\n"
    "v_permlane16_swap_b32 v196, v197\n"
    "v_max_f32 v196, v196, v197\n"
    "v_max_f32 v196, v196, v96\n"
    "v_sub_f32 v197, v96, v196\n"
    "v_mov_b32 v96, v196\n"
    "v_exp_f32 v197, v197\n"
    "v_sub_f32 v32, v32, v196\n"
    "v_sub_f32 v33, v33, v196\n"
    "v_sub_f32 v34, v34, v196\n"
    "v_sub_f32 v35, v35, v196\n"
    "v_sub_f32 v48, v48, v196\n"
    "v_sub_f32 v49, v49, v196\n"
    "v_sub_f32 v50, v50, v196\n"
    "v_sub_f32 v51, v51, v196\n"
    "v_sub_f32 v64, v64, v196\n"
    "v_sub_f32 v65, v65, v196\n"
    "v_sub_f32 v66, v66, v196\n"
    "v_sub_f32 v67, v67, v196\n"
    "v_sub_f32 v80, v80, v196\n"
    "v_sub_f32 v81, v81, v196\n"
    "v_sub_f32 v82, v82, v196\n"
    "v_sub_f32 v83, v83, v196\n"
    "v_mul_f32 v112, v112, v197\n"
    "v_mul_f32 v113, v113, v197\n"
    "v_mul_f32 v114, v114, v197\n"
    "v_mul_f32 v115, v115, v197\n"
    "v_accvgpr_read_b32 v198, a0\n"
    "v_accvgpr_read_b32 v199, a1\n"
    "v_mul_f32 v198, v198, v197\n"
    "v_mul_f32 v199, v199, v197\n"
    "v_accvgpr_write_b32 a0, v198\n"
    "v_accvgpr_write_b32 a1, v199\n"
    "v_accvgpr_read_b32 v198, a2\n"
    "v_accvgpr_read_b32 v199, a3\n"
    "v_mul_f32 v198, v198, v197\n"
    "v_mul_f32 v199, v199, v197\n"
    "v_accvgpr_write_b32 a2, v198\n"
    "v_accvgpr_write_b32 a3, v199\n"
    "v_accvgpr_read_b32 v198, a16\n"
    "v_accvgpr_read_b32 v199, a17\n"
    "v_mul_f32 v198, v198, v197\n"
    "v_mul_f32 v199, v199, v197\n"
    "v_accvgpr_write_b32 a16, v198\n"
    "v_accvgpr_write_b32 a17, v199\n"
    "v_accvgpr_read_b32 v198, a18\n"
    "v_accvgpr_read_b32 v199, a19\n"
    "v_mul_f32 v198, v198, v197\n"
    "v_mul_f32 v199, v199, v197\n"
    "v_accvgpr_write_b32 a18, v198\n"
    "v_accvgpr_write_b32 a19, v199\n"
    "v_accvgpr_read_b32 v198, a32\n"
    "v_accvgpr_read_b32 v199, a33\n"
    "v_mul_f32 v198, v198, v197\n"
    "v_mul_f32 v199, v199, v197\n"
    "v_accvgpr_write_b32 a32, v198\n"
    "v_accvgpr_write_b32 a33, v199\n"
    "v_accvgpr_read_b32 v198, a34\n"
    "v_accvgpr_read_b32 v199, a35\n"
    "v_mul_f32 v198, v198, v197\n"
    "v_mul_f32 v199, v199, v197\n"
    "v_accvgpr_write_b32 a34, v198\n"
    "v_accvgpr_write_b32 a35, v199\n"
    "v_accvgpr_read_b32 v198, a48\n"
    "v_accvgpr_read_b32 v199, a49\n"
    "v_mul_f32 v198, v198, v197\n"
    "v_mul_f32 v199, v199, v197\n"
    "v_accvgpr_write_b32 a48, v198\n"
    "v_accvgpr_write_b32 a49, v199\n"
    "v_accvgpr_read_b32 v198, a50\n"
    "v_accvgpr_read_b32 v199, a51\n"
    "v_mul_f32 v198, v198, v197\n"
    "v_mul_f32 v199, v199, v197\n"
    "v_accvgpr_write_b32 a50, v198\n"
    "v_accvgpr_write_b32 a51, v199\n"
    "v_accvgpr_read_b32 v198, a64\n"
    "v_accvgpr_read_b32 v199, a65\n"
    "v_mul_f32 v198, v198, v197\n"
    "v_mul_f32 v199, v199, v197\n"
    "v_accvgpr_write_b32 a64, v198\n"
    "v_accvgpr_write_b32 a65, v199\n"
    "v_accvgpr_read_b32 v198, a66\n"
    "v_accvgpr_read_b32 v199, a67\n"
    "v_mul_f32 v198, v198, v197\n"
    "v_mul_f32 v199, v199, v197\n"
    "v_accvgpr_write_b32 a66, v198\n"
    "v_accvgpr_write_b32 a67, v199\n"
    "v_accvgpr_read_b32 v198, a80\n"
    "v_accvgpr_read_b32 v199, a81\n"
    "v_mul_f32 v198, v198, v197\n"
    "v_mul_f32 v199, v199, v197\n"
    "v_accvgpr_write_b32 a80, v198\n"
    "v_accvgpr_write_b32 a81, v199\n"
    "v_accvgpr_read_b32 v198, a82\n"
    "v_accvgpr_read_b32 v199, a83\n"
    "v_mul_f32 v198, v198, v197\n"
    "v_mul_f32 v199, v199, v197\n"
    "v_accvgpr_write_b32 a82, v198\n"
    "v_accvgpr_write_b32 a83, v199\n"
    "v_accvgpr_read_b32 v198, a96\n"
    "v_accvgpr_read_b32 v199, a97\n"
    "v_mul_f32 v198, v198, v197\n"
    "v_mul_f32 v199, v199, v197\n"
    "v_accvgpr_write_b32 a96, v198\n"
    "v_accvgpr_write_b32 a97, v199\n"
    "v_accvgpr_read_b32 v198, a98\n"
    "v_accvgpr_read_b32 v199, a99\n"
    "v_mul_f32 v198, v198, v197\n"
    "v_mul_f32 v199, v199, v197\n"
    "v_accvgpr_write_b32 a98, v198\n"
    "v_accvgpr_write_b32 a99, v199\n"
    "v_accvgpr_read_b32 v198, a112\n"
    "v_accvgpr_read_b32 v199, a113\n"
    "v_mul_f32 v198, v198, v197\n"
    "v_mul_f32 v199, v199, v197\n"
    "v_accvgpr_write_b32 a112, v198\n"
    "v_accvgpr_write_b32 a113, v199\n"
    "v_accvgpr_read_b32 v198, a114\n"
    "v_accvgpr_read_b32 v199, a115\n"
    "v_mul_f32 v198, v198, v197\n"
    "v_mul_f32 v199, v199, v197\n"
    "v_accvgpr_write_b32 a114, v198\n"
    "v_accvgpr_write_b32 a115, v199\n"
    "v_accvgpr_read_b32 v198, a128\n"
    "v_accvgpr_read_b32 v199, a129\n"
    "v_mul_f32 v198, v198, v197\n"
    "v_mul_f32 v199, v199, v197\n"
    "v_accvgpr_write_b32 a128, v198\n"
    "v_accvgpr_write_b32 a129, v199\n"
    "v_accvgpr_read_b32 v198, a130\n"
    "v_accvgpr_read_b32 v199, a131\n"
    "v_mul_f32 v198, v198, v197\n"
    "v_mul_f32 v199, v199, v197\n"
    "v_accvgpr_write_b32 a130, v198\n"
    "v_accvgpr_write_b32 a131, v199\n"
    "v_accvgpr_read_b32 v198, a144\n"
    "v_accvgpr_read_b32 v199, a145\n"
    "v_mul_f32 v198, v198, v197\n"
    "v_mul_f32 v199, v199, v197\n"
    "v_accvgpr_write_b32 a144, v198\n"
    "v_accvgpr_write_b32 a145, v199\n"
    "v_accvgpr_read_b32 v198, a146\n"
    "v_accvgpr_read_b32 v199, a147\n"
    "v_mul_f32 v198, v198, v197\n"
    "v_mul_f32 v199, v199, v197\n"
    "v_accvgpr_write_b32 a146, v198\n"
    "v_accvgpr_write_b32 a147, v199\n"
    "v_accvgpr_read_b32 v198, a160\n"
    "v_accvgpr_read_b32 v199, a161\n"
    "v_mul_f32 v198, v198, v197\n"
    "v_mul_f32 v199, v199, v197\n"
    "v_accvgpr_write_b32 a160, v198\n"
    "v_accvgpr_write_b32 a161, v199\n"
    "v_accvgpr_read_b32 v198, a162\n"
    "v_accvgpr_read_b32 v199, a163\n"
    "v_mul_f32 v198, v198, v197\n"
    "v_mul_f32 v199, v199, v197\n"
    "v_accvgpr_write_b32 a162, v198\n"
    "v_accvgpr_write_b32 a163, v199\n"
    "v_accvgpr_read_b32 v198, a176\n"
    "v_accvgpr_read_b32 v199, a177\n"
    "v_mul_f32 v198, v198, v197\n"
    "v_mul_f32 v199, v199, v197\n"
    "v_accvgpr_write_b32 a176, v198\n"
    "v_accvgpr_write_b32 a177, v199\n"
    "v_accvgpr_read_b32 v198, a178\n"
    "v_accvgpr_read_b32 v199, a179\n"
    "v_mul_f32 v198, v198, v197\n"
    "v_mul_f32 v199, v199, v197\n"
    "v_accvgpr_write_b32 a178, v198\n"
    "v_accvgpr_write_b32 a179, v199\n"
    "v_max3_f32 v196, v36, v37, v38\n"
    "v_max3_f32 v196, v196, v39, v52\n"
    "v_max3_f32 v196, v196, v53, v54\n"
    "v_max3_f32 v196, v196, v55, v68\n"
    "v_max3_f32 v196, v196, v69, v70\n"
    "v_max3_f32 v196, v196, v71, v84\n"
    "v_max3_f32 v196, v196, v85, v86\n"
    "v_max_f32 v196, v196, v87\n"
    "v_mov_b32 v197, v196\n"
    "s_nop 1\n"
    "v_permlane32_swap_b32 v196, v197\n"
    "v_max_f32 v196, v196, v197\n"
    "v_mov_b32 v197, v196\n"
    "s_nop 1\n"
    "v_permlane16_swap_b32 v196, v197\n"
    "v_max_f32 v196, v196, v197\n"
    "v_max_f32 v196, v196, v100\n"
    "v_sub_f32 v197, v100, v196\n"
    "v_mov_b32 v100, v196\n"
    "v_exp_f32 v197, v197\n"
    "v_sub_f32 v36, v36, v196\n"
    "v_sub_f32 v37, v37, v196\n"
    "v_sub_f32 v38, v38, v196\n"
    "v_sub_f32 v39, v39, v196\n"
    "v_sub_f32 v52, v52, v196\n"
    "v_sub_f32 v53, v53, v196\n"
    "v_sub_f32 v54, v54, v196\n"
    "v_sub_f32 v55, v55, v196\n"
    "v_sub_f32 v68, v68, v196\n"
    "v_sub_f32 v69, v69, v196\n"
    "v_sub_f32 v70, v70, v196\n"
    "v_sub_f32 v71, v71, v196\n"
    "v_sub_f32 v84, v84, v196\n"
    "v_sub_f32 v85, v85, v196\n"
    "v_sub_f32 v86, v86, v196\n"
    "v_sub_f32 v87, v87, v196\n"
    "v_mul_f32 v116, v116, v197\n"
    "v_mul_f32 v117, v117, v197\n"
    "v_mul_f32 v118, v118, v197\n"
    "v_mul_f32 v119, v119, v197\n"
    "v_accvgpr_read_b32 v198, a4\n"
    "v_accvgpr_read_b32 v199, a5\n"
    "v_mul_f32 v198, v198, v197\n"
    "v_mul_f32 v199, v199, v197\n"
    "v_accvgpr_write_b32 a4, v198\n"
    "v_accvgpr_write_b32 a5, v199\n"
    "v_accvgpr_read_b32 v198, a6\n"
    "v_accvgpr_read_b32 v199, a7\n"
    "v_mul_f32 v198, v198, v197\n"
    "v_mul_f32 v199, v199, v197\n"
    "v_accvgpr_write_b32 a6, v198\n"
    "v_accvgpr_write_b32 a7, v199\n"
    "v_accvgpr_read_b32 v198, a20\n"
    "v_accvgpr_read_b32 v199, a21\n"
    "v_mul_f32 v198, v198, v197\n"
    "v_mul_f32 v199, v199, v197\n"
    "v_accvgpr_write_b32 a20, v198\n"
    "v_accvgpr_write_b32 a21, v199\n"
    "v_accvgpr_read_b32 v198, a22\n"
    "v_accvgpr_read_b32 v199, a23\n"
    "v_mul_f32 v198, v198, v197\n"
    "v_mul_f32 v199, v199, v197\n"
    "v_accvgpr_write_b32 a22, v198\n"
    "v_accvgpr_write_b32 a23, v199\n"
    "v_accvgpr_read_b32 v198, a36\n"
    "v_accvgpr_read_b32 v199, a37\n"
    "v_mul_f32 v198, v198, v197\n"
    "v_mul_f32 v199, v199, v197\n"
    "v_accvgpr_write_b32 a36, v198\n"
    "v_accvgpr_write_b32 a37, v199\n"
    "v_accvgpr_read_b32 v198, a38\n"
    "v_accvgpr_read_b32 v199, a39\n"
    "v_mul_f32 v198, v198, v197\n"
    "v_mul_f32 v199, v199, v197\n"
    "v_accvgpr_write_b32 a38, v198\n"
    "v_accvgpr_write_b32 a39, v199\n"
    "v_accvgpr_read_b32 v198, a52\n"
    "v_accvgpr_read_b32 v199, a53\n"
    "v_mul_f32 v198, v198, v197\n"
    "v_mul_f32 v199, v199, v197\n"
    "v_accvgpr_write_b32 a52, v198\n"
    "v_accvgpr_write_b32 a53, v199\n"
    "v_accvgpr_read_b32 v198, a54\n"
    "v_accvgpr_read_b32 v199, a55\n"
    "v_mul_f32 v198, v198, v197\n"
    "v_mul_f32 v199, v199, v197\n"
    "v_accvgpr_write_b32 a54, v198\n"
    "v_accvgpr_write_b32 a55, v199\n"
    "v_accvgpr_read_b32 v198, a68\n"
    "v_accvgpr_read_b32 v199, a69\n"
    "v_mul_f32 v198, v198, v197\n"
    "v_mul_f32 v199, v199, v197\n"
    "v_accvgpr_write_b32 a68, v198\n"
    "v_accvgpr_write_b32 a69, v199\n"
    "v_accvgpr_read_b32 v198, a70\n"
    "v_accvgpr_read_b32 v199, a71\n"
    "v_mul_f32 v198, v198, v197\n"
    "v_mul_f32 v199, v199, v197\n"
    "v_accvgpr_write_b32 a70, v198\n"
    "v_accvgpr_write_b32 a71, v199\n"
    "v_accvgpr_read_b32 v198, a84\n"
    "v_accvgpr_read_b32 v199, a85\n"
    "v_mul_f32 v198, v198, v197\n"
    "v_mul_f32 v199, v199, v197\n"
    "v_accvgpr_write_b32 a84, v198\n"
    "v_accvgpr_write_b32 a85, v199\n"
    "v_accvgpr_read_b32 v198, a86\n"
    "v_accvgpr_read_b32 v199, a87\n"
    "v_mul_f32 v198, v198, v197\n"
    "v_mul_f32 v199, v199, v197\n"
    "v_accvgpr_write_b32 a86, v198\n"
    "v_accvgpr_write_b32 a87, v199\n"
    "v_accvgpr_read_b32 v198, a100\n"
    "v_accvgpr_read_b32 v199, a101\n"
    "v_mul_f32 v198, v198, v197\n"
    "v_mul_f32 v199, v199, v197\n"
    "v_accvgpr_write_b32 a100, v198\n"
    "v_accvgpr_write_b32 a101, v199\n"
    "v_accvgpr_read_b32 v198, a102\n"
    "v_accvgpr_read_b32 v199, a103\n"
    "v_mul_f32 v198, v198, v197\n"
    "v_mul_f32 v199, v199, v197\n"
    "v_accvgpr_write_b32 a102, v198\n"
    "v_accvgpr_write_b32 a103, v199\n"
    "v_accvgpr_read_b32 v198, a116\n"
    "v_accvgpr_read_b32 v199, a117\n"
    "v_mul_f32 v198, v198, v197\n"
    "v_mul_f32 v199, v199, v197\n"
    "v_accvgpr_write_b32 a116, v198\n"
    "v_accvgpr_write_b32 a117, v199\n"
    "v_accvgpr_read_b32 v198, a118\n"
    "v_accvgpr_read_b32 v199, a119\n"
    "v_mul_f32 v198, v198, v197\n"
    "v_mul_f32 v199, v199, v197\n"
    "v_accvgpr_write_b32 a118, v198\n"
    "v_accvgpr_write_b32 a119, v199\n"
    "v_accvgpr_read_b32 v198, a132\n"
    "v_accvgpr_read_b32 v199, a133\n"
    "v_mul_f32 v198, v198, v197\n"
    "v_mul_f32 v199, v199, v197\n"
    "v_accvgpr_write_b32 a132, v198\n"
    "v_accvgpr_write_b32 a133, v199\n"
    "v_accvgpr_read_b32 v198, a134\n"
    "v_accvgpr_read_b32 v199, a135\n"
    "v_mul_f32 v198, v198, v197\n"
    "v_mul_f32 v199, v199, v197\n"
    "v_accvgpr_write_b32 a134, v198\n"
    "v_accvgpr_write_b32 a135, v199\n"
    "v_accvgpr_read_b32 v198, a148\n"
    "v_accvgpr_read_b32 v199, a149\n"
    "v_mul_f32 v198, v198, v197\n"
    "v_mul_f32 v199, v199, v197\n"
    "v_accvgpr_write_b32 a148, v198\n"
    "v_accvgpr_write_b32 a149, v199\n"
    "v_accvgpr_read_b32 v198, a150\n"
    "v_accvgpr_read_b32 v199, a151\n"
    "v_mul_f32 v198, v198, v197\n"
    "v_mul_f32 v199, v199, v197\n"
    "v_accvgpr_write_b32 a150, v198\n"
    "v_accvgpr_write_b32 a151, v199\n"
    "v_accvgpr_read_b32 v198, a164\n"
    "v_accvgpr_read_b32 v199, a165\n"
    "v_mul_f32 v198, v198, v197\n"
    "v_mul_f32 v199, v199, v197\n"
    "v_accvgpr_write_b32 a164, v198\n"
    "v_accvgpr_write_b32 a165, v199\n"
    "v_accvgpr_read_b32 v198, a166\n"
    "v_accvgpr_read_b32 v199, a167\n"
    "v_mul_f32 v198, v198, v197\n"
    "v_mul_f32 v199, v199, v197\n"
    "v_accvgpr_write_b32 a166, v198\n"
    "v_accvgpr_write_b32 a167, v199\n"
    "v_accvgpr_read_b32 v198, a180\n"
    "v_accvgpr_read_b32 v199, a181\n"
    "v_mul_f32 v198, v198, v197\n"
    "v_mul_f32 v199, v199, v197\n"
    "v_accvgpr_write_b32 a180, v198\n"
    "v_accvgpr_write_b32 a181, v199\n"
    "v_accvgpr_read_b32 v198, a182\n"
    "v_accvgpr_read_b32 v199, a183\n"
    "v_mul_f32 v198, v198, v197\n"
    "v_mul_f32 v199, v199, v197\n"
    "v_accvgpr_write_b32 a182, v198\n"
    "v_accvgpr_write_b32 a183, v199\n"
    "v_max3_f32 v196, v40, v41, v42\n"
    "v_max3_f32 v196, v196, v43, v56\n"
    "v_max3_f32 v196, v196, v57, v58\n"
    "v_max3_f32 v196, v196, v59, v72\n"
    "v_max3_f32 v196, v196, v73, v74\n"
    "v_max3_f32 v196, v196, v75, v88\n"
    "v_max3_f32 v196, v196, v89, v90\n"
    "v_max_f32 v196, v196, v91\n"
    "v_mov_b32 v197, v196\n"
    "s_nop 1\n"
    "v_permlane32_swap_b32 v196, v197\n"
    "v_max_f32 v196, v196, v197\n"
    "v_mov_b32 v197, v196\n"
    "s_nop 1\n"
    "v_permlane16_swap_b32 v196, v197\n"
    "v_max_f32 v196, v196, v197\n"
    "v_max_f32 v196, v196, v104\n"
    "v_sub_f32 v197, v104, v196\n"
    "v_mov_b32 v104, v196\n"
    "v_exp_f32 v197, v197\n"
    "v_sub_f32 v40, v40, v196\n"
    "v_sub_f32 v41, v41, v196\n"
    "v_sub_f32 v42, v42, v196\n"
    "v_sub_f32 v43, v43, v196\n"
    "v_sub_f32 v56, v56, v196\n"
    "v_sub_f32 v57, v57, v196\n"
    "v_sub_f32 v58, v58, v196\n"
    "v_sub_f32 v59, v59, v196\n"
    "v_sub_f32 v72, v72, v196\n"
    "v_sub_f32 v73, v73, v196\n"
    "v_sub_f32 v74, v74, v196\n"
    "v_sub_f32 v75, v75, v196\n"
    "v_sub_f32 v88, v88, v196\n"
    "v_sub_f32 v89, v89, v196\n"
    "v_sub_f32 v90, v90, v196\n"
    "v_sub_f32 v91, v91, v196\n"
    "v_mul_f32 v120, v120, v197\n"
    "v_mul_f32 v121, v121, v197\n"
    "v_mul_f32 v122, v122, v197\n"
    "v_mul_f32 v123, v123, v197\n"
    "v_accvgpr_read_b32 v198, a8\n"
    "v_accvgpr_read_b32 v199, a9\n"
    "v_mul_f32 v198, v198, v197\n"
    "v_mul_f32 v199, v199, v197\n"
    "v_accvgpr_write_b32 a8, v198\n"
    "v_accvgpr_write_b32 a9, v199\n"
    "v_accvgpr_read_b32 v198, a10\n"
    "v_accvgpr_read_b32 v199, a11\n"
    "v_mul_f32 v198, v198, v197\n"
    "v_mul_f32 v199, v199, v197\n"
    "v_accvgpr_write_b32 a10, v198\n"
    "v_accvgpr_write_b32 a11, v199\n"
    "v_accvgpr_read_b32 v198, a24\n"
    "v_accvgpr_read_b32 v199, a25\n"
    "v_mul_f32 v198, v198, v197\n"
    "v_mul_f32 v199, v199, v197\n"
    "v_accvgpr_write_b32 a24, v198\n"
    "v_accvgpr_write_b32 a25, v199\n"
    "v_accvgpr_read_b32 v198, a26\n"
    "v_accvgpr_read_b32 v199, a27\n"
    "v_mul_f32 v198, v198, v197\n"
    "v_mul_f32 v199, v199, v197\n"
    "v_accvgpr_write_b32 a26, v198\n"
    "v_accvgpr_write_b32 a27, v199\n"
    "v_accvgpr_read_b32 v198, a40\n"
    "v_accvgpr_read_b32 v199, a41\n"
    "v_mul_f32 v198, v198, v197\n"
    "v_mul_f32 v199, v199, v197\n"
    "v_accvgpr_write_b32 a40, v198\n"
    "v_accvgpr_write_b32 a41, v199\n"
    "v_accvgpr_read_b32 v198, a42\n"
    "v_accvgpr_read_b32 v199, a43\n"
    "v_mul_f32 v198, v198, v197\n"
    "v_mul_f32 v199, v199, v197\n"
    "v_accvgpr_write_b32 a42, v198\n"
    "v_accvgpr_write_b32 a43, v199\n"
    "v_accvgpr_read_b32 v198, a56\n"
    "v_accvgpr_read_b32 v199, a57\n"
    "v_mul_f32 v198, v198, v197\n"
    "v_mul_f32 v199, v199, v197\n"
    "v_accvgpr_write_b32 a56, v198\n"
    "v_accvgpr_write_b32 a57, v199\n"
    "v_accvgpr_read_b32 v198, a58\n"
    "v_accvgpr_read_b32 v199, a59\n"
    "v_mul_f32 v198, v198, v197\n"
    "v_mul_f32 v199, v199, v197\n"
    "v_accvgpr_write_b32 a58, v198\n"
    "v_accvgpr_write_b32 a59, v199\n"
    "v_accvgpr_read_b32 v198, a72\n"
    "v_accvgpr_read_b32 v199, a73\n"
    "v_mul_f32 v198, v198, v197\n"
    "v_mul_f32 v199, v199, v197\n"
    "v_accvgpr_write_b32 a72, v198\n"
    "v_accvgpr_write_b32 a73, v199\n"
    "v_accvgpr_read_b32 v198, a74\n"
    "v_accvgpr_read_b32 v199, a75\n"
    "v_mul_f32 v198, v198, v197\n"
    "v_mul_f32 v199, v199, v197\n"
    "v_accvgpr_write_b32 a74, v198\n"
    "v_accvgpr_write_b32 a75, v199\n"
    "v_accvgpr_read_b32 v198, a88\n"
    "v_accvgpr_read_b32 v199, a89\n"
    "v_mul_f32 v198, v198, v197\n"
    "v_mul_f32 v199, v199, v197\n"
    "v_accvgpr_write_b32 a88, v198\n"
    "v_accvgpr_write_b32 a89, v199\n"
    "v_accvgpr_read_b32 v198, a90\n"
    "v_accvgpr_read_b32 v199, a91\n"
    "v_mul_f32 v198, v198, v197\n"
    "v_mul_f32 v199, v199, v197\n"
    "v_accvgpr_write_b32 a90, v198\n"
    "v_accvgpr_write_b32 a91, v199\n"
    "v_accvgpr_read_b32 v198, a104\n"
    "v_accvgpr_read_b32 v199, a105\n"
    "v_mul_f32 v198, v198, v197\n"
    "v_mul_f32 v199, v199, v197\n"
    "v_accvgpr_write_b32 a104, v198\n"
    "v_accvgpr_write_b32 a105, v199\n"
    "v_accvgpr_read_b32 v198, a106\n"
    "v_accvgpr_read_b32 v199, a107\n"
    "v_mul_f32 v198, v198, v197\n"
    "v_mul_f32 v199, v199, v197\n"
    "v_accvgpr_write_b32 a106, v198\n"
    "v_accvgpr_write_b32 a107, v199\n"
    "v_accvgpr_read_b32 v198, a120\n"
    "v_accvgpr_read_b32 v199, a121\n"
    "v_mul_f32 v198, v198, v197\n"
    "v_mul_f32 v199, v199, v197\n"
    "v_accvgpr_write_b32 a120, v198\n"
    "v_accvgpr_write_b32 a121, v199\n"
    "v_accvgpr_read_b32 v198, a122\n"
    "v_accvgpr_read_b32 v199, a123\n"
    "v_mul_f32 v198, v198, v197\n"
    "v_mul_f32 v199, v199, v197\n"
    "v_accvgpr_write_b32 a122, v198\n"
    "v_accvgpr_write_b32 a123, v199\n"
    "v_accvgpr_read_b32 v198, a136\n"
    "v_accvgpr_read_b32 v199, a137\n"
    "v_mul_f32 v198, v198, v197\n"
    "v_mul_f32 v199, v199, v197\n"
    "v_accvgpr_write_b32 a136, v198\n"
    "v_accvgpr_write_b32 a137, v199\n"
    "v_accvgpr_read_b32 v198, a138\n"
    "v_accvgpr_read_b32 v199, a139\n"
    "v_mul_f32 v198, v198, v197\n"
    "v_mul_f32 v199, v199, v197\n"
    "v_accvgpr_write_b32 a138, v198\n"
    "v_accvgpr_write_b32 a139, v199\n"
    "v_accvgpr_read_b32 v198, a152\n"
    "v_accvgpr_read_b32 v199, a153\n"
    "v_mul_f32 v198, v198, v197\n"
    "v_mul_f32 v199, v199, v197\n"
    "v_accvgpr_write_b32 a152, v198\n"
    "v_accvgpr_write_b32 a153, v199\n"
    "v_accvgpr_read_b32 v198, a154\n"
    "v_accvgpr_read_b32 v199, a155\n"
    "v_mul_f32 v198, v198, v197\n"
    "v_mul_f32 v199, v199, v197\n"
    "v_accvgpr_write_b32 a154, v198\n"
    "v_accvgpr_write_b32 a155, v199\n"
    "v_accvgpr_read_b32 v198, a168\n"
    "v_accvgpr_read_b32 v199, a169\n"
    "v_mul_f32 v198, v198, v197\n"
    "v_mul_f32 v199, v199, v197\n"
    "v_accvgpr_write_b32 a168, v198\n"
    "v_accvgpr_write_b32 a169, v199\n"
    "v_accvgpr_read_b32 v198, a170\n"
    "v_accvgpr_read_b32 v199, a171\n"
    "v_mul_f32 v198, v198, v197\n"
    "v_mul_f32 v199, v199, v197\n"
    "v_accvgpr_write_b32 a170, v198\n"
    "v_accvgpr_write_b32 a171, v199\n"
    "v_accvgpr_read_b32 v198, a184\n"
    "v_accvgpr_read_b32 v199, a185\n"
    "v_mul_f32 v198, v198, v197\n"
    "v_mul_f32 v199, v199, v197\n"
    "v_accvgpr_write_b32 a184, v198\n"
    "v_accvgpr_write_b32 a185, v199\n"
    "v_accvgpr_read_b32 v198, a186\n"
    "v_accvgpr_read_b32 v199, a187\n"
    "v_mul_f32 v198, v198, v197\n"
    "v_mul_f32 v199, v199, v197\n"
    "v_accvgpr_write_b32 a186, v198\n"
    "v_accvgpr_write_b32 a187, v199\n"
    "v_max3_f32 v196, v44, v45, v46\n"
    "v_max3_f32 v196, v196, v47, v60\n"
    "v_max3_f32 v196, v196, v61, v62\n"
    "v_max3_f32 v196, v196, v63, v76\n"
    "v_max3_f32 v196, v196, v77, v78\n"
    "v_max3_f32 v196, v196, v79, v92\n"
    "v_max3_f32 v196, v196, v93, v94\n"
    "v_max_f32 v196, v196, v95\n"
    "v_mov_b32 v197, v196\n"
    "s_nop 1\n"
    "v_permlane32_swap_b32 v196, v197\n"
    "v_max_f32 v196, v196, v197\n"
    "v_mov_b32 v197, v196\n"
    "s_nop 1\n"
    "v_permlane16_swap_b32 v196, v197\n"
    "v_max_f32 v196, v196, v197\n"
    "v_max_f32 v196, v196, v108\n"
    "v_sub_f32 v197, v108, v196\n"
    "v_mov_b32 v108, v196\n"
    "v_exp_f32 v197, v197\n"
    "v_sub_f32 v44, v44, v196\n"
    "v_sub_f32 v45, v45, v196\n"
    "v_sub_f32 v46, v46, v196\n"
    "v_sub_f32 v47, v47, v196\n"
    "v_sub_f32 v60, v60, v196\n"
    "v_sub_f32 v61, v61, v196\n"
    "v_sub_f32 v62, v62, v196\n"
    "v_sub_f32 v63, v63, v196\n"
    "v_sub_f32 v76, v76, v196\n"
    "v_sub_f32 v77, v77, v196\n"
    "v_sub_f32 v78, v78, v196\n"
    "v_sub_f32 v79, v79, v196\n"
    "v_sub_f32 v92, v92, v196\n"
    "v_sub_f32 v93, v93, v196\n"
    "v_sub_f32 v94, v94, v196\n"
    "v_sub_f32 v95, v95, v196\n"
    "v_mul_f32 v124, v124, v197\n"
    "v_mul_f32 v125, v125, v197\n"
    "v_mul_f32 v126, v126, v197\n"
    "v_mul_f32 v127, v127, v197\n"
    "v_accvgpr_read_b32 v198, a12\n"
    "v_accvgpr_read_b32 v199, a13\n"
    "v_mul_f32 v198, v198, v197\n"
    "v_mul_f32 v199, v199, v197\n"
    "v_accvgpr_write_b32 a12, v198\n"
    "v_accvgpr_write_b32 a13, v199\n"
    "v_accvgpr_read_b32 v198, a14\n"
    "v_accvgpr_read_b32 v199, a15\n"
    "v_mul_f32 v198, v198, v197\n"
    "v_mul_f32 v199, v199, v197\n"
    "v_accvgpr_write_b32 a14, v198\n"
    "v_accvgpr_write_b32 a15, v199\n"
    "v_accvgpr_read_b32 v198, a28\n"
    "v_accvgpr_read_b32 v199, a29\n"
    "v_mul_f32 v198, v198, v197\n"
    "v_mul_f32 v199, v199, v197\n"
    "v_accvgpr_write_b32 a28, v198\n"
    "v_accvgpr_write_b32 a29, v199\n"
    "v_accvgpr_read_b32 v198, a30\n"
    "v_accvgpr_read_b32 v199, a31\n"
    "v_mul_f32 v198, v198, v197\n"
    "v_mul_f32 v199, v199, v197\n"
    "v_accvgpr_write_b32 a30, v198\n"
    "v_accvgpr_write_b32 a31, v199\n"
    "v_accvgpr_read_b32 v198, a44\n"
    "v_accvgpr_read_b32 v199, a45\n"
    "v_mul_f32 v198, v198, v197\n"
    "v_mul_f32 v199, v199, v197\n"
    "v_accvgpr_write_b32 a44, v198\n"
    "v_accvgpr_write_b32 a45, v199\n"
    "v_accvgpr_read_b32 v198, a46\n"
    "v_accvgpr_read_b32 v199, a47\n"
    "v_mul_f32 v198, v198, v197\n"
    "v_mul_f32 v199, v199, v197\n"
    "v_accvgpr_write_b32 a46, v198\n"
    "v_accvgpr_write_b32 a47, v199\n"
    "v_accvgpr_read_b32 v198, a60\n"
    "v_accvgpr_read_b32 v199, a61\n"
    "v_mul_f32 v198, v198, v197\n"
    "v_mul_f32 v199, v199, v197\n"
    "v_accvgpr_write_b32 a60, v198\n"
    "v_accvgpr_write_b32 a61, v199\n"
    "v_accvgpr_read_b32 v198, a62\n"
    "v_accvgpr_read_b32 v199, a63\n"
    "v_mul_f32 v198, v198, v197\n"
    "v_mul_f32 v199, v199, v197\n"
    "v_accvgpr_write_b32 a62, v198\n"
    "v_accvgpr_write_b32 a63, v199\n"
    "v_accvgpr_read_b32 v198, a76\n"
    "v_accvgpr_read_b32 v199, a77\n"
    "v_mul_f32 v198, v198, v197\n"
    "v_mul_f32 v199, v199, v197\n"
    "v_accvgpr_write_b32 a76, v198\n"
    "v_accvgpr_write_b32 a77, v199\n"
    "v_accvgpr_read_b32 v198, a78\n"
    "v_accvgpr_read_b32 v199, a79\n"
    "v_mul_f32 v198, v198, v197\n"
    "v_mul_f32 v199, v199, v197\n"
    "v_accvgpr_write_b32 a78, v198\n"
    "v_accvgpr_write_b32 a79, v199\n"
    "v_accvgpr_read_b32 v198, a92\n"
    "v_accvgpr_read_b32 v199, a93\n"
    "v_mul_f32 v198, v198, v197\n"
    "v_mul_f32 v199, v199, v197\n"
    "v_accvgpr_write_b32 a92, v198\n"
    "v_accvgpr_write_b32 a93, v199\n"
    "v_accvgpr_read_b32 v198, a94\n"
    "v_accvgpr_read_b32 v199, a95\n"
    "v_mul_f32 v198, v198, v197\n"
    "v_mul_f32 v199, v199, v197\n"
    "v_accvgpr_write_b32 a94, v198\n"
    "v_accvgpr_write_b32 a95, v199\n"
    "v_accvgpr_read_b32 v198, a108\n"
    "v_accvgpr_read_b32 v199, a109\n"
    "v_mul_f32 v198, v198, v197\n"
    "v_mul_f32 v199, v199, v197\n"
    "v_accvgpr_write_b32 a108, v198\n"
    "v_accvgpr_write_b32 a109, v199\n"
    "v_accvgpr_read_b32 v198, a110\n"
    "v_accvgpr_read_b32 v199, a111\n"
    "v_mul_f32 v198, v198, v197\n"
    "v_mul_f32 v199, v199, v197\n"
    "v_accvgpr_write_b32 a110, v198\n"
    "v_accvgpr_write_b32 a111, v199\n"
    "v_accvgpr_read_b32 v198, a124\n"
    "v_accvgpr_read_b32 v199, a125\n"
    "v_mul_f32 v198, v198, v197\n"
    "v_mul_f32 v199, v199, v197\n"
    "v_accvgpr_write_b32 a124, v198\n"
    "v_accvgpr_write_b32 a125, v199\n"
    "v_accvgpr_read_b32 v198, a126\n"
    "v_accvgpr_read_b32 v199, a127\n"
    "v_mul_f32 v198, v198, v197\n"
    "v_mul_f32 v199, v199, v197\n"
    "v_accvgpr_write_b32 a126, v198\n"
    "v_accvgpr_write_b32 a127, v199\n"
    "v_accvgpr_read_b32 v198, a140\n"
    "v_accvgpr_read_b32 v199, a141\n"
    "v_mul_f32 v198, v198, v197\n"
    "v_mul_f32 v199, v199, v197\n"
    "v_accvgpr_write_b32 a140, v198\n"
    "v_accvgpr_write_b32 a141, v199\n"
    "v_accvgpr_read_b32 v198, a142\n"
    "v_accvgpr_read_b32 v199, a143\n"
    "v_mul_f32 v198, v198, v197\n"
    "v_mul_f32 v199, v199, v197\n"
    "v_accvgpr_write_b32 a142, v198\n"
    "v_accvgpr_write_b32 a143, v199\n"
    "v_accvgpr_read_b32 v198, a156\n"
    "v_accvgpr_read_b32 v199, a157\n"
    "v_mul_f32 v198, v198, v197\n"
    "v_mul_f32 v199, v199, v197\n"
    "v_accvgpr_write_b32 a156, v198\n"
    "v_accvgpr_write_b32 a157, v199\n"
    "v_accvgpr_read_b32 v198, a158\n"
    "v_accvgpr_read_b32 v199, a159\n"
    "v_mul_f32 v198, v198, v197\n"
    "v_mul_f32 v199, v199, v197\n"
    "v_accvgpr_write_b32 a158, v198\n"
    "v_accvgpr_write_b32 a159, v199\n"
    "v_accvgpr_read_b32 v198, a172\n"
    "v_accvgpr_read_b32 v199, a173\n"
    "v_mul_f32 v198, v198, v197\n"
    "v_mul_f32 v199, v199, v197\n"
    "v_accvgpr_write_b32 a172, v198\n"
    "v_accvgpr_write_b32 a173, v199\n"
    "v_accvgpr_read_b32 v198, a174\n"
    "v_accvgpr_read_b32 v199, a175\n"
    "v_mul_f32 v198, v198, v197\n"
    "v_mul_f32 v199, v199, v197\n"
    "v_accvgpr_write_b32 a174, v198\n"
    "v_accvgpr_write_b32 a175, v199\n"
    "v_accvgpr_read_b32 v198, a188\n"
    "v_accvgpr_read_b32 v199, a189\n"
    "v_mul_f32 v198, v198, v197\n"
    "v_mul_f32 v199, v199, v197\n"
    "v_accvgpr_write_b32 a188, v198\n"
    "v_accvgpr_write_b32 a189, v199\n"
    "v_accvgpr_read_b32 v198, a190\n"
    "v_accvgpr_read_b32 v199, a191\n"
    "v_mul_f32 v198, v198, v197\n"
    "v_mul_f32 v199, v199, v197\n"
    "v_accvgpr_write_b32 a190, v198\n"
    "v_accvgpr_write_b32 a191, v199\n"
    "v_exp_f32 v32, v32\n"
    "v_exp_f32 v33, v33\n"
    "v_exp_f32 v34, v34\n"
    "v_cvt_pk_bf16_f32 v128, v32, v33\n"
    "v_exp_f32 v35, v35\n"
    "v_exp_f32 v36, v36\n"
    "v_cvt_pk_bf16_f32 v129, v34, v35\n"
    "v_exp_f32 v37, v37\n"
    "v_exp_f32 v38, v38\n"
    "v_cvt_pk_bf16_f32 v132, v36, v37\n"
    "v_exp_f32 v39, v39\n"
    "v_exp_f32 v40, v40\n"
    "v_cvt_pk_bf16_f32 v133, v38, v39\n"
    "v_exp_f32 v41, v41\n"
    "v_exp_f32 v42, v42\n"
    "v_cvt_pk_bf16_f32 v136, v40, v41\n"
    "v_exp_f32 v43, v43\n"
    "v_exp_f32 v44, v44\n"
    "v_cvt_pk_bf16_f32 v137, v42, v43\n"
    "v_exp_f32 v45, v45\n"
    "v_exp_f32 v46, v46\n"
    "v_cvt_pk_bf16_f32 v140, v44, v45\n"
    "v_exp_f32 v47, v47\n"
    "v_exp_f32 v48, v48\n"
    "v_cvt_pk_bf16_f32 v141, v46, v47\n"
    "v_exp_f32 v49, v49\n"
    "v_exp_f32 v50, v50\n"
    "v_cvt_pk_bf16_f32 v130, v48, v49\n"
    "v_exp_f32 v51, v51\n"
    "v_exp_f32 v52, v52\n"
    "v_cvt_pk_bf16_f32 v131, v50, v51\n"
    "v_exp_f32 v53, v53\n"
    "v_exp_f32 v54, v54\n"
    "v_cvt_pk_bf16_f32 v134, v52, v53\n"
    "v_exp_f32 v55, v55\n"
    "v_exp_f32 v56, v56\n"
    "v_cvt_pk_bf16_f32 v135, v54, v55\n"
    "v_exp_f32 v57, v57\n"
    "v_exp_f32 v58, v58\n"
    "v_cvt_pk_bf16_f32 v138, v56, v57\n"
    "v_exp_f32 v59, v59\n"
    "v_exp_f32 v60, v60\n"
    "v_cvt_pk_bf16_f32 v139, v58, v59\n"
    "v_exp_f32 v61, v61\n"
    "v_exp_f32 v62, v62\n"
    "v_cvt_pk_bf16_f32 v142, v60, v61\n"
    "v_exp_f32 v63, v63\n"
    "s_nop 0\n"
    "v_cvt_pk_bf16_f32 v143, v62, v63\n"
    "v_exp_f32 v64, v64\n"
    "v_exp_f32 v65, v65\n"
    "v_exp_f32 v66, v66\n"
    "v_cvt_pk_bf16_f32 v144, v64, v65\n"
    "v_exp_f32 v67, v67\n"
    "v_exp_f32 v68, v68\n"
    "v_cvt_pk_bf16_f32 v145, v66, v67\n"
    "v_exp_f32 v69, v69\n"
    "v_exp_f32 v70, v70\n"
    "v_cvt_pk_bf16_f32 v148, v68, v69\n"
    "v_exp_f32 v71, v71\n"
    "v_exp_f32 v72, v72\n"
    "v_cvt_pk_bf16_f32 v149, v70, v71\n"
    "v_exp_f32 v73, v73\n"
    "v_exp_f32 v74, v74\n"
    "v_cvt_pk_bf16_f32 v152, v72, v73\n"
    "v_exp_f32 v75, v75\n"
    "v_exp_f32 v76, v76\n"
    "v_cvt_pk_bf16_f32 v153, v74, v75\n"
    "v_exp_f32 v77, v77\n"
    "v_exp_f32 v78, v78\n"
    "v_cvt_pk_bf16_f32 v156, v76, v77\n"
    "v_exp_f32 v79, v79\n"
    "v_exp_f32 v80, v80\n"
    "v_cvt_pk_bf16_f32 v157, v78, v79\n"
    "v_exp_f32 v81, v81\n"
    "v_exp_f32 v82, v82\n"
    "v_cvt_pk_bf16_f32 v146, v80, v81\n"
    "v_exp_f32 v83, v83\n"
    "v_exp_f32 v84, v84\n"
    "v_cvt_pk_bf16_f32 v147, v82, v83\n"
    "v_exp_f32 v85, v85\n"
    "v_exp_f32 v86, v86\n"
    "v_cvt_pk_bf16_f32 v150, v84, v85\n"
    "v_exp_f32 v87, v87\n"
    "v_exp_f32 v88, v88\n"
    "v_cvt_pk_bf16_f32 v151, v86, v87\n"
    "v_exp_f32 v89, v89\n"
    "v_exp_f32 v90, v90\n"
    "v_cvt_pk_bf16_f32 v154, v88, v89\n"
    "v_exp_f32 v91, v91\n"
    "v_exp_f32 v92, v92\n"
    "v_cvt_pk_bf16_f32 v155, v90, v91\n"
    "v_exp_f32 v93, v93\n"
    "v_exp_f32 v94, v94\n"
    "v_cvt_pk_bf16_f32 v158, v92, v93\n"
    "v_exp_f32 v95, v95\n"
    "s_nop 0\n"
    "v_cvt_pk_bf16_f32 v159, v94, v95\n"
    "ds_read_b128 v[160:163], v205 offset:12288\n"
    "ds_read_b128 v[164:167], v205 offset:13312\n"
    "s_waitcnt lgkmcnt(1)\n"
    "v_mfma_f32_16x16x32_bf16 a[0:3], v[160:163], v[128:131], a[0:3]\n"
    "s_waitcnt lgkmcnt(0)\n"
    "v_mfma_f32_16x16x32_bf16 a[0:3], v[164:167], v[144:147], a[0:3]\n"
    "v_mfma_f32_16x16x32_bf16 a[4:7], v[160:163], v[132:135], a[4:7]\n"
    "v_mfma_f32_16x16x32_bf16 a[4:7], v[164:167], v[148:151], a[4:7]\n"
    "v_mfma_f32_16x16x32_bf16 a[8:11], v[160:163], v[136:139], a[8:11]\n"
    "v_mfma_f32_16x16x32_bf16 a[8:11], v[164:167], v[152:155], a[8:11]\n"
    "v_mfma_f32_16x16x32_bf16 a[12:15], v[160:163], v[140:143], a[12:15]\n"
    "v_mfma_f32_16x16x32_bf16 a[12:15], v[164:167], v[156:159], a[12:15]\n"
    "ds_read_b128 v[168:171], v205 offset:14336\n"
    "ds_read_b128 v[172:175], v205 offset:15360\n"
    "s_waitcnt lgkmcnt(1)\n"
    "v_mfma_f32_16x16x32_bf16 a[28:31], v[168:171], v[140:143], a[28:31]\n"
    "s_waitcnt lgkmcnt(0)\n"
    "v_mfma_f32_16x16x32_bf16 a[28:31], v[172:175], v[156:159], a[28:31]\n"
    "v_mfma_f32_16x16x32_bf16 a[24:27], v[172:175], v[152:155], a[24:27]\n"
    "v_mfma_f32_16x16x32_bf16 a[24:27], v[168:171], v[136:139], a[24:27]\n"
    "v_mfma_f32_16x16x32_bf16 a[20:23], v[168:171], v[132:135], a[20:23]\n"
    "v_mfma_f32_16x16x32_bf16 a[20:23], v[172:175], v[148:151], a[20:23]\n"
    "v_mfma_f32_16x16x32_bf16 a[16:19], v[172:175], v[144:147], a[16:19]\n"
    "v_mfma_f32_16x16x32_bf16 a[16:19], v[168:171], v[128:131], a[16:19]\n"
    "ds_read_b128 v[176:179], v205 offset:16384\n"
    "ds_read_b128 v[180:183], v205 offset:17408\n"
    "s_waitcnt lgkmcnt(1)\n"
    "v_mfma_f32_16x16x32_bf16 a[32:35], v[176:179], v[128:131], a[32:35]\n"
    "s_waitcnt lgkmcnt(0)\n"
    "v_mfma_f32_16x16x32_bf16 a[32:35], v[180:183], v[144:147], a[32:35]\n"
    "v_mfma_f32_16x16x32_bf16 a[36:39], v[180:183], v[148:151], a[36:39]\n"
    "v_mfma_f32_16x16x32_bf16 a[36:39], v[176:179], v[132:135], a[36:39]\n"
    "v_mfma_f32_16x16x32_bf16 a[40:43], v[176:179], v[136:139], a[40:43]\n"
    "v_mfma_f32_16x16x32_bf16 a[40:43], v[180:183], v[152:155], a[40:43]\n"
    "v_mfma_f32_16x16x32_bf16 a[44:47], v[180:183], v[156:159], a[44:47]\n"
    "v_mfma_f32_16x16x32_bf16 a[44:47], v[176:179], v[140:143], a[44:47]\n"
    "ds_read_b128 v[160:163], v205 offset:18432\n"
    "ds_read_b128 v[164:167], v205 offset:19456\n"
    "s_waitcnt lgkmcnt(1)\n"
    "v_mfma_f32_16x16x32_bf16 a[60:63], v[160:163], v[140:143], a[60:63]\n"
    "s_waitcnt lgkmcnt(0)\n"
    "v_mfma_f32_16x16x32_bf16 a[60:63], v[164:167], v[156:159], a[60:63]\n"
    "v_mfma_f32_16x16x32_bf16 a[56:59], v[164:167], v[152:155], a[56:59]\n"
    "v_mfma_f32_16x16x32_bf16 a[56:59], v[160:163], v[136:139], a[56:59]\n"
    "v_mfma_f32_16x16x32_bf16 a[52:55], v[160:163], v[132:135], a[52:55]\n"
    "v_mfma_f32_16x16x32_bf16 a[52:55], v[164:167], v[148:151], a[52:55]\n"
    "v_mfma_f32_16x16x32_bf16 a[48:51], v[164:167], v[144:147], a[48:51]\n"
    "v_mfma_f32_16x16x32_bf16 a[48:51], v[160:163], v[128:131], a[48:51]\n"
    "ds_read_b128 v[168:171], v205 offset:20480\n"
    "ds_read_b128 v[172:175], v205 offset:21504\n"
    "s_waitcnt lgkmcnt(1)\n"
    "v_mfma_f32_16x16x32_bf16 a[64:67], v[168:171], v[128:131], a[64:67]\n"
    "s_waitcnt lgkmcnt(0)\n"
    "v_mfma_f32_16x16x32_bf16 a[64:67], v[172:175], v[144:147], a[64:67]\n"
    "v_mfma_f32_16x16x32_bf16 a[68:71], v[172:175], v[148:151], a[68:71]\n"
    "v_mfma_f32_16x16x32_bf16 a[68:71], v[168:171], v[132:135], a[68:71]\n"
    "v_mfma_f32_16x16x32_bf16 a[72:75], v[168:171], v[136:139], a[72:75]\n"
    "v_mfma_f32_16x16x32_bf16 a[72:75], v[172:175], v[152:155], a[72:75]\n"
    "v_mfma_f32_16x16x32_bf16 a[76:79], v[172:175], v[156:159], a[76:79]\n"
    "v_mfma_f32_16x16x32_bf16 a[76:79], v[168:171], v[140:143], a[76:79]\n"
    "ds_read_b128 v[176:179], v205 offset:22528\n"
    "ds_read_b128 v[180:183], v205 offset:23552\n"
    "s_waitcnt lgkmcnt(1)\n"
    "v_mfma_f32_16x16x32_bf16 a[92:95], v[176:179], v[140:143], a[92:95]\n"
    "s_waitcnt lgkmcnt(0)\n"
    "v_mfma_f32_16x16x32_bf16 a[92:95], v[180:183], v[156:159], a[92:95]\n"
    "v_mfma_f32_16x16x32_bf16 a[88:91], v[180:183], v[152:155], a[88:91]\n"
    "v_mfma_f32_16x16x32_bf16 a[88:91], v[176:179], v[136:139], a[88:91]\n"
    "v_mfma_f32_16x16x32_bf16 a[84:87], v[176:179], v[132:135], a[84:87]\n"
    "v_mfma_f32_16x16x32_bf16 a[84:87], v[180:183], v[148:151], a[84:87]\n"
    "v_mfma_f32_16x16x32_bf16 a[80:83], v[180:183], v[144:147], a[80:83]\n"
    "v_mfma_f32_16x16x32_bf16 a[80:83], v[176:179], v[128:131], a[80:83]\n"
    "ds_read_b128 v[160:163], v205 offset:24576\n"
    "ds_read_b128 v[164:167], v205 offset:25600\n"
    "s_waitcnt lgkmcnt(1)\n"
    "v_mfma_f32_16x16x32_bf16 a[96:99], v[160:163], v[128:131], a[96:99]\n"
    "s_waitcnt lgkmcnt(0)\n"
    "v_mfma_f32_16x16x32_bf16 a[96:99], v[164:167], v[144:147], a[96:99]\n"
    "v_mfma_f32_16x16x32_bf16 a[100:103], v[164:167], v[148:151], a[100:103]\n"
    "v_mfma_f32_16x16x32_bf16 a[100:103], v[160:163], v[132:135], a[100:103]\n"
    "v_mfma_f32_16x16x32_bf16 a[104:107], v[160:163], v[136:139], a[104:107]\n"
    "v_mfma_f32_16x16x32_bf16 a[104:107], v[164:167], v[152:155], a[104:107]\n"
    "v_mfma_f32_16x16x32_bf16 a[108:111], v[164:167], v[156:159], a[108:111]\n"
    "v_mfma_f32_16x16x32_bf16 a[108:111], v[160:163], v[140:143], a[108:111]\n"
    "ds_read_b128 v[168:171], v205 offset:26624\n"
    "ds_read_b128 v[172:175], v205 offset:27648\n"
    "s_waitcnt lgkmcnt(1)\n"
    "v_mfma_f32_16x16x32_bf16 a[124:127], v[168:171], v[140:143], a[124:127]\n"
    "s_waitcnt lgkmcnt(0)\n"
    "v_mfma_f32_16x16x32_bf16 a[124:127], v[172:175], v[156:159], a[124:127]\n"
    "v_mfma_f32_16x16x32_bf16 a[120:123], v[172:175], v[152:155], a[120:123]\n"
    "v_mfma_f32_16x16x32_bf16 a[120:123], v[168:171], v[136:139], a[120:123]\n"
    "v_mfma_f32_16x16x32_bf16 a[116:119], v[168:171], v[132:135], a[116:119]\n"
    "v_mfma_f32_16x16x32_bf16 a[116:119], v[172:175], v[148:151], a[116:119]\n"
    "v_mfma_f32_16x16x32_bf16 a[112:115], v[172:175], v[144:147], a[112:115]\n"
    "v_mfma_f32_16x16x32_bf16 a[112:115], v[168:171], v[128:131], a[112:115]\n"
    "ds_read_b128 v[176:179], v205 offset:28672\n"
    "ds_read_b128 v[180:183], v205 offset:29696\n"
    "s_waitcnt lgkmcnt(1)\n"
    "v_mfma_f32_16x16x32_bf16 a[128:131], v[176:179], v[128:131], a[128:131]\n"
    "s_waitcnt lgkmcnt(0)\n"
    "v_mfma_f32_16x16x32_bf16 a[128:131], v[180:183], v[144:147], a[128:131]\n"
    "v_mfma_f32_16x16x32_bf16 a[132:135], v[180:183], v[148:151], a[132:135]\n"
    "v_mfma_f32_16x16x32_bf16 a[132:135], v[176:179], v[132:135], a[132:135]\n"
    "v_mfma_f32_16x16x32_bf16 a[136:139], v[176:179], v[136:139], a[136:139]\n"
    "v_mfma_f32_16x16x32_bf16 a[136:139], v[180:183], v[152:155], a[136:139]\n"
    "v_mfma_f32_16x16x32_bf16 a[140:143], v[180:183], v[156:159], a[140:143]\n"
    "v_mfma_f32_16x16x32_bf16 a[140:143], v[176:179], v[140:143], a[140:143]\n"
    "ds_read_b128 v[160:163], v205 offset:30720\n"
    "ds_read_b128 v[164:167], v205 offset:31744\n"
    "s_waitcnt lgkmcnt(1)\n"
    "v_mfma_f32_16x16x32_bf16 a[156:159], v[160:163], v[140:143], a[156:159]\n"
    "s_waitcnt lgkmcnt(0)\n"
    "v_mfma_f32_16x16x32_bf16 a[156:159], v[164:167], v[156:159], a[156:159]\n"
    "v_mfma_f32_16x16x32_bf16 a[152:155], v[164:167], v[152:155], a[152:155]\n"
    "v_mfma_f32_16x16x32_bf16 a[152:155], v[160:163], v[136:139], a[152:155]\n"
    "v_mfma_f32_16x16x32_bf16 a[148:151], v[160:163], v[132:135], a[148:151]\n"
    "v_mfma_f32_16x16x32_bf16 a[148:151], v[164:167], v[148:151], a[148:151]\n"
    "v_mfma_f32_16x16x32_bf16 a[144:147], v[164:167], v[144:147], a[144:147]\n"
    "v_mfma_f32_16x16x32_bf16 a[144:147], v[160:163], v[128:131], a[144:147]\n"
    "ds_read_b128 v[168:171], v205 offset:32768\n"
    "ds_read_b128 v[172:175], v205 offset:33792\n"
    "s_waitcnt lgkmcnt(1)\n"
    "v_mfma_f32_16x16x32_bf16 a[160:163], v[168:171], v[128:131], a[160:163]\n"
    "s_waitcnt lgkmcnt(0)\n"
    "v_mfma_f32_16x16x32_bf16 a[160:163], v[172:175], v[144:147], a[160:163]\n"
    "v_mfma_f32_16x16x32_bf16 a[164:167], v[172:175], v[148:151], a[164:167]\n"
    "v_mfma_f32_16x16x32_bf16 a[164:167], v[168:171], v[132:135], a[164:167]\n"
    "v_mfma_f32_16x16x32_bf16 a[168:171], v[168:171], v[136:139], a[168:171]\n"
    "v_mfma_f32_16x16x32_bf16 a[168:171], v[172:175], v[152:155], a[168:171]\n"
    "v_mfma_f32_16x16x32_bf16 a[172:175], v[172:175], v[156:159], a[172:175]\n"
    "v_mfma_f32_16x16x32_bf16 a[172:175], v[168:171], v[140:143], a[172:175]\n"
    "ds_read_b128 v[176:179], v205 offset:34816\n"
    "ds_read_b128 v[180:183], v205 offset:35840\n"
    "s_waitcnt lgkmcnt(1)\n"
    "v_mfma_f32_16x16x32_bf16 a[188:191], v[176:179], v[140:143], a[188:191]\n"
    "s_waitcnt lgkmcnt(0)\n"
    "v_mfma_f32_16x16x32_bf16 a[188:191], v[180:183], v[156:159], a[188:191]\n"
    "v_mfma_f32_16x16x32_bf16 a[184:187], v[176:179], v[136:139], a[184:187]\n"
    "v_mfma_f32_16x16x32_bf16 a[184:187], v[180:183], v[152:155], a[184:187]\n"
    "v_mfma_f32_16x16x32_bf16 a[180:183], v[176:179], v[132:135], a[180:183]\n"
    "v_mfma_f32_16x16x32_bf16 a[180:183], v[180:183], v[148:151], a[180:183]\n"
    "v_mfma_f32_16x16x32_bf16 a[176:179], v[176:179], v[128:131], a[176:179]\n"
    "v_mfma_f32_16x16x32_bf16 a[176:179], v[180:183], v[144:147], a[176:179]\n"
    "v_mfma_f32_16x16x32_bf16 v[112:115], v[192:195], v[128:131], v[112:115]\n"
    "v_mfma_f32_16x16x32_bf16 v[112:115], v[192:195], v[144:147], v[112:115]\n"
    "v_mfma_f32_16x16x32_bf16 v[116:119], v[192:195], v[132:135], v[116:119]\n"
    "v_mfma_f32_16x16x32_bf16 v[116:119], v[192:195], v[148:151], v[116:119]\n"
    "v_mfma_f32_16x16x32_bf16 v[120:123], v[192:195], v[136:139], v[120:123]\n"
    "v_mfma_f32_16x16x32_bf16 v[120:123], v[192:195], v[152:155], v[120:123]\n"
    "v_mfma_f32_16x16x32_bf16 v[124:127], v[192:195], v[140:143], v[124:127]\n"
    "v_mfma_f32_16x16x32_bf16 v[124:127], v[192:195], v[156:159], v[124:127]\n"
    "s_nop 15\n"
    "s_add_u32 s63, s63, 0x6000\n"
    "s_cmp_lt_u32 s63, 0xc0000\n"
    "s_cbranch_scc1 LSLOOP\n"
    "s_branch LEPI\n"
    "LEPI:\n"
    "s_lshl_b32 s76, s56, 3\n"
    "s_add_u32 s76, s76, s57\n"
    "s_mul_i32 s76, s76, 0x18000\n"
    "s_add_u32 s76, s76, s88\n"
    "s_add_u32 s28, s24, s76\n"
    "s_addc_u32 s29, s25, 0\n"
    "s_and_b32 s29, s29, 0xffff\n"
    "s_mov_b32 s30, 0x6000\n"
    "s_mov_b32 s31, 0x20000\n"
    "s_xor_b32 s94, s70, 0x80000000\n"
    "s_xor_b32 s95, s70, 0x80000000\n"
    "s_mov_b32 s62, 0x1800\n"
    "s_mov_b32 s63, 0x3000\n"
    "s_mov_b32 s64, 0x4800\n"
    "v_and_b32 v198, 15, v231\n"
    "v_mul_u32_u24 v205, 0x180, v198\n"
    "v_lshrrev_b32 v199, 4, v231\n"
    "v_lshl_add_u32 v205, v199, 4, v205\n"
    "v_mul_f32 v222, s68, v112\n"
    "v_rcp_f32 v222, v222\n"
    "v_mul_f32 v224, s68, v116\n"
    "v_rcp_f32 v224, v224\n"
    "v_mul_f32 v226, s68, v120\n"
    "v_rcp_f32 v226, v226\n"
    "v_mul_f32 v228, s68, v124\n"
    "v_rcp_f32 v228, v228\n"
    "v_mov_b32 v223, v222\n"
    "v_mov_b32 v225, v224\n"
    "v_mov_b32 v227, v226\n"
    "v_mov_b32 v229, v228\n"
    "v_accvgpr_read_b32 v96, a0\n"
    "v_accvgpr_read_b32 v97, a1\n"
    "v_accvgpr_read_b32 v98, a2\n"
    "v_accvgpr_read_b32 v99, a3\n"
    "v_pk_fma_f32 v[96:97], v[96:97], v[222:223], s[94:95]\n"
    "v_pk_fma_f32 v[98:99], v[98:99], v[222:223], s[94:95]\n"
    "v_cvt_pk_bf16_f32 v32, v96, v97\n"
    "v_cvt_pk_bf16_f32 v33, v98, v99\n"
    "v_accvgpr_read_b32 v100, a16\n"
    "v_accvgpr_read_b32 v101, a17\n"
    "v_accvgpr_read_b32 v102, a18\n"
    "v_accvgpr_read_b32 v103, a19\n"
    "v_pk_fma_f32 v[100:101], v[100:101], v[222:223], s[94:95]\n"
    "v_pk_fma_f32 v[102:103], v[102:103], v[222:223], s[94:95]\n"
    "v_cvt_pk_bf16_f32 v34, v100, v101\n"
    "v_cvt_pk_bf16_f32 v35, v102, v103\n"
    "buffer_store_dwordx4 v[32:35], v205, s[28:31], 0 offen sc0 sc1\n"
    "v_accvgpr_read_b32 v104, a32\n"
    "v_accvgpr_read_b32 v105, a33\n"
    "v_accvgpr_read_b32 v106, a34\n"
    "v_accvgpr_read_b32 v107, a35\n"
    "v_pk_fma_f32 v[104:105], v[104:105], v[222:223], s[94:95]\n"
    "v_pk_fma_f32 v[106:107], v[106:107], v[222:223], s[94:95]\n"
    "v_cvt_pk_bf16_f32 v36, v104, v105\n"
    "v_cvt_pk_bf16_f32 v37, v106, v107\n"
    "v_accvgpr_read_b32 v108, a48\n"
    "v_accvgpr_read_b32 v109, a49\n"
    "v_accvgpr_read_b32 v110, a50\n"
    "v_accvgpr_read_b32 v111, a51\n"
    "v_pk_fma_f32 v[108:109], v[108:109], v[222:223], s[94:95]\n"
    "v_pk_fma_f32 v[110:111], v[110:111], v[222:223], s[94:95]\n"
    "v_cvt_pk_bf16_f32 v38, v108, v109\n"
    "v_cvt_pk_bf16_f32 v39, v110, v111\n"
    "buffer_store_dwordx4 v[36:39], v205, s[28:31], 0 offen offset:64 sc0 sc1\n"
    "v_accvgpr_read_b32 v96, a64\n"
    "v_accvgpr_read_b32 v97, a65\n"
    "v_accvgpr_read_b32 v98, a66\n"
    "v_accvgpr_read_b32 v99, a67\n"
    "v_pk_fma_f32 v[96:97], v[96:97], v[222:223], s[94:95]\n"
    "v_pk_fma_f32 v[98:99], v[98:99], v[222:223], s[94:95]\n"
    "v_cvt_pk_bf16_f32 v40, v96, v97\n"
    "v_cvt_pk_bf16_f32 v41, v98, v99\n"
    "v_accvgpr_read_b32 v100, a80\n"
    "v_accvgpr_read_b32 v101, a81\n"
    "v_accvgpr_read_b32 v102, a82\n"
    "v_accvgpr_read_b32 v103, a83\n"
    "v_pk_fma_f32 v[100:101], v[100:101], v[222:223], s[94:95]\n"
    "v_pk_fma_f32 v[102:103], v[102:103], v[222:223], s[94:95]\n"
    "v_cvt_pk_bf16_f32 v42, v100, v101\n"
    "v_cvt_pk_bf16_f32 v43, v102, v103\n"
    "buffer_store_dwordx4 v[40:43], v205, s[28:31], 0 offen offset:128 sc0 sc1\n"
    "v_accvgpr_read_b32 v104, a96\n"
    "v_accvgpr_read_b32 v105, a97\n"
    "v_accvgpr_read_b32 v106, a98\n"
    "v_accvgpr_read_b32 v107, a99\n"
    "v_pk_fma_f32 v[104:105], v[104:105], v[222:223], s[94:95]\n"
    "v_pk_fma_f32 v[106:107], v[106:107], v[222:223], s[94:95]\n"
    "v_cvt_pk_bf16_f32 v44, v104, v105\n"
    "v_cvt_pk_bf16_f32 v45, v106, v107\n"
    "v_accvgpr_read_b32 v108, a112\n"
    "v_accvgpr_read_b32 v109, a113\n"
    "v_accvgpr_read_b32 v110, a114\n"
    "v_accvgpr_read_b32 v111, a115\n"
    "v_pk_fma_f32 v[108:109], v[108:109], v[222:223], s[94:95]\n"
    "v_pk_fma_f32 v[110:111], v[110:111], v[222:223], s[94:95]\n"
    "v_cvt_pk_bf16_f32 v46, v108, v109\n"
    "v_cvt_pk_bf16_f32 v47, v110, v111\n"
    "buffer_store_dwordx4 v[44:47], v205, s[28:31], 0 offen offset:192 sc0 sc1\n"
    "v_accvgpr_read_b32 v96, a128\n"
    "v_accvgpr_read_b32 v97, a129\n"
    "v_accvgpr_read_b32 v98, a130\n"
    "v_accvgpr_read_b32 v99, a131\n"
    "v_pk_fma_f32 v[96:97], v[96:97], v[222:223], s[94:95]\n"
    "v_pk_fma_f32 v[98:99], v[98:99], v[222:223], s[94:95]\n"
    "v_cvt_pk_bf16_f32 v48, v96, v97\n"
    "v_cvt_pk_bf16_f32 v49, v98, v99\n"
    "v_accvgpr_read_b32 v100, a144\n"
    "v_accvgpr_read_b32 v101, a145\n"
    "v_accvgpr_read_b32 v102, a146\n"
    "v_accvgpr_read_b32 v103, a147\n"
    "v_pk_fma_f32 v[100:101], v[100:101], v[222:223], s[94:95]\n"
    "v_pk_fma_f32 v[102:103], v[102:103], v[222:223], s[94:95]\n"
    "v_cvt_pk_bf16_f32 v50, v100, v101\n"
    "v_cvt_pk_bf16_f32 v51, v102, v103\n"
    "buffer_store_dwordx4 v[48:51], v205, s[28:31], 0 offen offset:256 sc0 sc1\n"
    "v_accvgpr_read_b32 v104, a160\n"
    "v_accvgpr_read_b32 v105, a161\n"
    "v_accvgpr_read_b32 v106, a162\n"
    "v_accvgpr_read_b32 v107, a163\n"
    "v_pk_fma_f32 v[104:105], v[104:105], v[222:223], s[94:95]\n"
    "v_pk_fma_f32 v[106:107], v[106:107], v[222:223], s[94:95]\n"
    "v_cvt_pk_bf16_f32 v52, v104, v105\n"
    "v_cvt_pk_bf16_f32 v53, v106, v107\n"
    "v_accvgpr_read_b32 v108, a176\n"
    "v_accvgpr_read_b32 v109, a177\n"
    "v_accvgpr_read_b32 v110, a178\n"
    "v_accvgpr_read_b32 v111, a179\n"
    "v_pk_fma_f32 v[108:109], v[108:109], v[222:223], s[94:95]\n"
    "v_pk_fma_f32 v[110:111], v[110:111], v[222:223], s[94:95]\n"
    "v_cvt_pk_bf16_f32 v54, v108, v109\n"
    "v_cvt_pk_bf16_f32 v55, v110, v111\n"
    "buffer_store_dwordx4 v[52:55], v205, s[28:31], 0 offen offset:320 sc0 sc1\n"
    "v_accvgpr_read_b32 v96, a4\n"
    "v_accvgpr_read_b32 v97, a5\n"
    "v_accvgpr_read_b32 v98, a6\n"
    "v_accvgpr_read_b32 v99, a7\n"
    "v_pk_fma_f32 v[96:97], v[96:97], v[224:225], s[94:95]\n"
    "v_pk_fma_f32 v[98:99], v[98:99], v[224:225], s[94:95]\n"
    "v_cvt_pk_bf16_f32 v56, v96, v97\n"
    "v_cvt_pk_bf16_f32 v57, v98, v99\n"
    "v_accvgpr_read_b32 v100, a20\n"
    "v_accvgpr_read_b32 v101, a21\n"
    "v_accvgpr_read_b32 v102, a22\n"
    "v_accvgpr_read_b32 v103, a23\n"
    "v_pk_fma_f32 v[100:101], v[100:101], v[224:225], s[94:95]\n"
    "v_pk_fma_f32 v[102:103], v[102:103], v[224:225], s[94:95]\n"
    "v_cvt_pk_bf16_f32 v58, v100, v101\n"
    "v_cvt_pk_bf16_f32 v59, v102, v103\n"
    "buffer_store_dwordx4 v[56:59], v205, s[28:31], s62 offen sc0 sc1\n"
    "v_accvgpr_read_b32 v104, a36\n"
    "v_accvgpr_read_b32 v105, a37\n"
    "v_accvgpr_read_b32 v106, a38\n"
    "v_accvgpr_read_b32 v107, a39\n"
    "v_pk_fma_f32 v[104:105], v[104:105], v[224:225], s[94:95]\n"
    "v_pk_fma_f32 v[106:107], v[106:107], v[224:225], s[94:95]\n"
    "v_cvt_pk_bf16_f32 v60, v104, v105\n"
    "v_cvt_pk_bf16_f32 v61, v106, v107\n"
    "v_accvgpr_read_b32 v108, a52\n"
    "v_accvgpr_read_b32 v109, a53\n"
    "v_accvgpr_read_b32 v110, a54\n"
    "v_accvgpr_read_b32 v111, a55\n"
    "v_pk_fma_f32 v[108:109], v[108:109], v[224:225], s[94:95]\n"
    "v_pk_fma_f32 v[110:111], v[110:111], v[224:225], s[94:95]\n"
    "v_cvt_pk_bf16_f32 v62, v108, v109\n"
    "v_cvt_pk_bf16_f32 v63, v110, v111\n"
    "buffer_store_dwordx4 v[60:63], v205, s[28:31], s62 offen offset:64 sc0 sc1\n"
    "v_accvgpr_read_b32 v96, a68\n"
    "v_accvgpr_read_b32 v97, a69\n"
    "v_accvgpr_read_b32 v98, a70\n"
    "v_accvgpr_read_b32 v99, a71\n"
    "v_pk_fma_f32 v[96:97], v[96:97], v[224:225], s[94:95]\n"
    "v_pk_fma_f32 v[98:99], v[98:99], v[224:225], s[94:95]\n"
    "v_cvt_pk_bf16_f32 v32, v96, v97\n"
    "v_cvt_pk_bf16_f32 v33, v98, v99\n"
    "v_accvgpr_read_b32 v100, a84\n"
    "v_accvgpr_read_b32 v101, a85\n"
    "v_accvgpr_read_b32 v102, a86\n"
    "v_accvgpr_read_b32 v103, a87\n"
    "v_pk_fma_f32 v[100:101], v[100:101], v[224:225], s[94:95]\n"
    "v_pk_fma_f32 v[102:103], v[102:103], v[224:225], s[94:95]\n"
    "v_cvt_pk_bf16_f32 v34, v100, v101\n"
    "v_cvt_pk_bf16_f32 v35, v102, v103\n"
    "buffer_store_dwordx4 v[32:35], v205, s[28:31], s62 offen offset:128 sc0 sc1\n"
    "v_accvgpr_read_b32 v104, a100\n"
    "v_accvgpr_read_b32 v105, a101\n"
    "v_accvgpr_read_b32 v106, a102\n"
    "v_accvgpr_read_b32 v107, a103\n"
    "v_pk_fma_f32 v[104:105], v[104:105], v[224:225], s[94:95]\n"
    "v_pk_fma_f32 v[106:107], v[106:107], v[224:225], s[94:95]\n"
    "v_cvt_pk_bf16_f32 v36, v104, v105\n"
    "v_cvt_pk_bf16_f32 v37, v106, v107\n"
    "v_accvgpr_read_b32 v108, a116\n"
    "v_accvgpr_read_b32 v109, a117\n"
    "v_accvgpr_read_b32 v110, a118\n"
    "v_accvgpr_read_b32 v111, a119\n"
    "v_pk_fma_f32 v[108:109], v[108:109], v[224:225], s[94:95]\n"
    "v_pk_fma_f32 v[110:111], v[110:111], v[224:225], s[94:95]\n"
    "v_cvt_pk_bf16_f32 v38, v108, v109\n"
    "v_cvt_pk_bf16_f32 v39, v110, v111\n"
    "buffer_store_dwordx4 v[36:39], v205, s[28:31], s62 offen offset:192 sc0 sc1\n"
    "v_accvgpr_read_b32 v96, a132\n"
    "v_accvgpr_read_b32 v97, a133\n"
    "v_accvgpr_read_b32 v98, a134\n"
    "v_accvgpr_read_b32 v99, a135\n"
    "v_pk_fma_f32 v[96:97], v[96:97], v[224:225], s[94:95]\n"
    "v_pk_fma_f32 v[98:99], v[98:99], v[224:225], s[94:95]\n"
    "v_cvt_pk_bf16_f32 v40, v96, v97\n"
    "v_cvt_pk_bf16_f32 v41, v98, v99\n"
    "v_accvgpr_read_b32 v100, a148\n"
    "v_accvgpr_read_b32 v101, a149\n"
    "v_accvgpr_read_b32 v102, a150\n"
    "v_accvgpr_read_b32 v103, a151\n"
    "v_pk_fma_f32 v[100:101], v[100:101], v[224:225], s[94:95]\n"
    "v_pk_fma_f32 v[102:103], v[102:103], v[224:225], s[94:95]\n"
    "v_cvt_pk_bf16_f32 v42, v100, v101\n"
    "v_cvt_pk_bf16_f32 v43, v102, v103\n"
    "buffer_store_dwordx4 v[40:43], v205, s[28:31], s62 offen offset:256 sc0 sc1\n"
    "v_accvgpr_read_b32 v104, a164\n"
    "v_accvgpr_read_b32 v105, a165\n"
    "v_accvgpr_read_b32 v106, a166\n"
    "v_accvgpr_read_b32 v107, a167\n"
    "v_pk_fma_f32 v[104:105], v[104:105], v[224:225], s[94:95]\n"
    "v_pk_fma_f32 v[106:107], v[106:107], v[224:225], s[94:95]\n"
    "v_cvt_pk_bf16_f32 v44, v104, v105\n"
    "v_cvt_pk_bf16_f32 v45, v106, v107\n"
    "v_accvgpr_read_b32 v108, a180\n"
    "v_accvgpr_read_b32 v109, a181\n"
    "v_accvgpr_read_b32 v110, a182\n"
    "v_accvgpr_read_b32 v111, a183\n"
    "v_pk_fma_f32 v[108:109], v[108:109], v[224:225], s[94:95]\n"
    "v_pk_fma_f32 v[110:111], v[110:111], v[224:225], s[94:95]\n"
    "v_cvt_pk_bf16_f32 v46, v108, v109\n"
    "v_cvt_pk_bf16_f32 v47, v110, v111\n"
    "buffer_store_dwordx4 v[44:47], v205, s[28:31], s62 offen offset:320 sc0 sc1\n"
    "v_accvgpr_read_b32 v96, a8\n"
    "v_accvgpr_read_b32 v97, a9\n"
    "v_accvgpr_read_b32 v98, a10\n"
    "v_accvgpr_read_b32 v99, a11\n"
    "v_pk_fma_f32 v[96:97], v[96:97], v[226:227], s[94:95]\n"
    "v_pk_fma_f32 v[98:99], v[98:99], v[226:227], s[94:95]\n"
    "v_cvt_pk_bf16_f32 v48, v96, v97\n"
    "v_cvt_pk_bf16_f32 v49, v98, v99\n"
    "v_accvgpr_read_b32 v100, a24\n"
    "v_accvgpr_read_b32 v101, a25\n"
    "v_accvgpr_read_b32 v102, a26\n"
    "v_accvgpr_read_b32 v103, a27\n"
    "v_pk_fma_f32 v[100:101], v[100:101], v[226:227], s[94:95]\n"
    "v_pk_fma_f32 v[102:103], v[102:103], v[226:227], s[94:95]\n"
    "v_cvt_pk_bf16_f32 v50, v100, v101\n"
    "v_cvt_pk_bf16_f32 v51, v102, v103\n"
    "buffer_store_dwordx4 v[48:51], v205, s[28:31], s63 offen sc0 sc1\n"
    "v_accvgpr_read_b32 v104, a40\n"
    "v_accvgpr_read_b32 v105, a41\n"
    "v_accvgpr_read_b32 v106, a42\n"
    "v_accvgpr_read_b32 v107, a43\n"
    "v_pk_fma_f32 v[104:105], v[104:105], v[226:227], s[94:95]\n"
    "v_pk_fma_f32 v[106:107], v[106:107], v[226:227], s[94:95]\n"
    "v_cvt_pk_bf16_f32 v52, v104, v105\n"
    "v_cvt_pk_bf16_f32 v53, v106, v107\n"
    "v_accvgpr_read_b32 v108, a56\n"
    "v_accvgpr_read_b32 v109, a57\n"
    "v_accvgpr_read_b32 v110, a58\n"
    "v_accvgpr_read_b32 v111, a59\n"
    "v_pk_fma_f32 v[108:109], v[108:109], v[226:227], s[94:95]\n"
    "v_pk_fma_f32 v[110:111], v[110:111], v[226:227], s[94:95]\n"
    "v_cvt_pk_bf16_f32 v54, v108, v109\n"
    "v_cvt_pk_bf16_f32 v55, v110, v111\n"
    "buffer_store_dwordx4 v[52:55], v205, s[28:31], s63 offen offset:64 sc0 sc1\n"
    "v_accvgpr_read_b32 v96, a72\n"
    "v_accvgpr_read_b32 v97, a73\n"
    "v_accvgpr_read_b32 v98, a74\n"
    "v_accvgpr_read_b32 v99, a75\n"
    "v_pk_fma_f32 v[96:97], v[96:97], v[226:227], s[94:95]\n"
    "v_pk_fma_f32 v[98:99], v[98:99], v[226:227], s[94:95]\n"
    "v_cvt_pk_bf16_f32 v56, v96, v97\n"
    "v_cvt_pk_bf16_f32 v57, v98, v99\n"
    "v_accvgpr_read_b32 v100, a88\n"
    "v_accvgpr_read_b32 v101, a89\n"
    "v_accvgpr_read_b32 v102, a90\n"
    "v_accvgpr_read_b32 v103, a91\n"
    "v_pk_fma_f32 v[100:101], v[100:101], v[226:227], s[94:95]\n"
    "v_pk_fma_f32 v[102:103], v[102:103], v[226:227], s[94:95]\n"
    "v_cvt_pk_bf16_f32 v58, v100, v101\n"
    "v_cvt_pk_bf16_f32 v59, v102, v103\n"
    "buffer_store_dwordx4 v[56:59], v205, s[28:31], s63 offen offset:128 sc0 sc1\n"
    "v_accvgpr_read_b32 v104, a104\n"
    "v_accvgpr_read_b32 v105, a105\n"
    "v_accvgpr_read_b32 v106, a106\n"
    "v_accvgpr_read_b32 v107, a107\n"
    "v_pk_fma_f32 v[104:105], v[104:105], v[226:227], s[94:95]\n"
    "v_pk_fma_f32 v[106:107], v[106:107], v[226:227], s[94:95]\n"
    "v_cvt_pk_bf16_f32 v60, v104, v105\n"
    "v_cvt_pk_bf16_f32 v61, v106, v107\n"
    "v_accvgpr_read_b32 v108, a120\n"
    "v_accvgpr_read_b32 v109, a121\n"
    "v_accvgpr_read_b32 v110, a122\n"
    "v_accvgpr_read_b32 v111, a123\n"
    "v_pk_fma_f32 v[108:109], v[108:109], v[226:227], s[94:95]\n"
    "v_pk_fma_f32 v[110:111], v[110:111], v[226:227], s[94:95]\n"
    "v_cvt_pk_bf16_f32 v62, v108, v109\n"
    "v_cvt_pk_bf16_f32 v63, v110, v111\n"
    "buffer_store_dwordx4 v[60:63], v205, s[28:31], s63 offen offset:192 sc0 sc1\n"
    "v_accvgpr_read_b32 v96, a136\n"
    "v_accvgpr_read_b32 v97, a137\n"
    "v_accvgpr_read_b32 v98, a138\n"
    "v_accvgpr_read_b32 v99, a139\n"
    "v_pk_fma_f32 v[96:97], v[96:97], v[226:227], s[94:95]\n"
    "v_pk_fma_f32 v[98:99], v[98:99], v[226:227], s[94:95]\n"
    "v_cvt_pk_bf16_f32 v32, v96, v97\n"
    "v_cvt_pk_bf16_f32 v33, v98, v99\n"
    "v_accvgpr_read_b32 v100, a152\n"
    "v_accvgpr_read_b32 v101, a153\n"
    "v_accvgpr_read_b32 v102, a154\n"
    "v_accvgpr_read_b32 v103, a155\n"
    "v_pk_fma_f32 v[100:101], v[100:101], v[226:227], s[94:95]\n"
    "v_pk_fma_f32 v[102:103], v[102:103], v[226:227], s[94:95]\n"
    "v_cvt_pk_bf16_f32 v34, v100, v101\n"
    "v_cvt_pk_bf16_f32 v35, v102, v103\n"
    "buffer_store_dwordx4 v[32:35], v205, s[28:31], s63 offen offset:256 sc0 sc1\n"
    "v_accvgpr_read_b32 v104, a168\n"
    "v_accvgpr_read_b32 v105, a169\n"
    "v_accvgpr_read_b32 v106, a170\n"
    "v_accvgpr_read_b32 v107, a171\n"
    "v_pk_fma_f32 v[104:105], v[104:105], v[226:227], s[94:95]\n"
    "v_pk_fma_f32 v[106:107], v[106:107], v[226:227], s[94:95]\n"
    "v_cvt_pk_bf16_f32 v36, v104, v105\n"
    "v_cvt_pk_bf16_f32 v37, v106, v107\n"
    "v_accvgpr_read_b32 v108, a184\n"
    "v_accvgpr_read_b32 v109, a185\n"
    "v_accvgpr_read_b32 v110, a186\n"
    "v_accvgpr_read_b32 v111, a187\n"
    "v_pk_fma_f32 v[108:109], v[108:109], v[226:227], s[94:95]\n"
    "v_pk_fma_f32 v[110:111], v[110:111], v[226:227], s[94:95]\n"
    "v_cvt_pk_bf16_f32 v38, v108, v109\n"
    "v_cvt_pk_bf16_f32 v39, v110, v111\n"
    "buffer_store_dwordx4 v[36:39], v205, s[28:31], s63 offen offset:320 sc0 sc1\n"
    "v_accvgpr_read_b32 v96, a12\n"
    "v_accvgpr_read_b32 v97, a13\n"
    "v_accvgpr_read_b32 v98, a14\n"
    "v_accvgpr_read_b32 v99, a15\n"
    "v_pk_fma_f32 v[96:97], v[96:97], v[228:229], s[94:95]\n"
    "v_pk_fma_f32 v[98:99], v[98:99], v[228:229], s[94:95]\n"
    "v_cvt_pk_bf16_f32 v40, v96, v97\n"
    "v_cvt_pk_bf16_f32 v41, v98, v99\n"
    "v_accvgpr_read_b32 v100, a28\n"
    "v_accvgpr_read_b32 v101, a29\n"
    "v_accvgpr_read_b32 v102, a30\n"
    "v_accvgpr_read_b32 v103, a31\n"
    "v_pk_fma_f32 v[100:101], v[100:101], v[228:229], s[94:95]\n"
    "v_pk_fma_f32 v[102:103], v[102:103], v[228:229], s[94:95]\n"
    "v_cvt_pk_bf16_f32 v42, v100, v101\n"
    "v_cvt_pk_bf16_f32 v43, v102, v103\n"
    "buffer_store_dwordx4 v[40:43], v205, s[28:31], s64 offen sc0 sc1\n"
    "v_accvgpr_read_b32 v104, a44\n"
    "v_accvgpr_read_b32 v105, a45\n"
    "v_accvgpr_read_b32 v106, a46\n"
    "v_accvgpr_read_b32 v107, a47\n"
    "v_pk_fma_f32 v[104:105], v[104:105], v[228:229], s[94:95]\n"
    "v_pk_fma_f32 v[106:107], v[106:107], v[228:229], s[94:95]\n"
    "v_cvt_pk_bf16_f32 v44, v104, v105\n"
    "v_cvt_pk_bf16_f32 v45, v106, v107\n"
    "v_accvgpr_read_b32 v108, a60\n"
    "v_accvgpr_read_b32 v109, a61\n"
    "v_accvgpr_read_b32 v110, a62\n"
    "v_accvgpr_read_b32 v111, a63\n"
    "v_pk_fma_f32 v[108:109], v[108:109], v[228:229], s[94:95]\n"
    "v_pk_fma_f32 v[110:111], v[110:111], v[228:229], s[94:95]\n"
    "v_cvt_pk_bf16_f32 v46, v108, v109\n"
    "v_cvt_pk_bf16_f32 v47, v110, v111\n"
    "buffer_store_dwordx4 v[44:47], v205, s[28:31], s64 offen offset:64 sc0 sc1\n"
    "v_accvgpr_read_b32 v96, a76\n"
    "v_accvgpr_read_b32 v97, a77\n"
    "v_accvgpr_read_b32 v98, a78\n"
    "v_accvgpr_read_b32 v99, a79\n"
    "v_pk_fma_f32 v[96:97], v[96:97], v[228:229], s[94:95]\n"
    "v_pk_fma_f32 v[98:99], v[98:99], v[228:229], s[94:95]\n"
    "v_cvt_pk_bf16_f32 v48, v96, v97\n"
    "v_cvt_pk_bf16_f32 v49, v98, v99\n"
    "v_accvgpr_read_b32 v100, a92\n"
    "v_accvgpr_read_b32 v101, a93\n"
    "v_accvgpr_read_b32 v102, a94\n"
    "v_accvgpr_read_b32 v103, a95\n"
    "v_pk_fma_f32 v[100:101], v[100:101], v[228:229], s[94:95]\n"
    "v_pk_fma_f32 v[102:103], v[102:103], v[228:229], s[94:95]\n"
    "v_cvt_pk_bf16_f32 v50, v100, v101\n"
    "v_cvt_pk_bf16_f32 v51, v102, v103\n"
    "buffer_store_dwordx4 v[48:51], v205, s[28:31], s64 offen offset:128 sc0 sc1\n"
    "v_accvgpr_read_b32 v104, a108\n"
    "v_accvgpr_read_b32 v105, a109\n"
    "v_accvgpr_read_b32 v106, a110\n"
    "v_accvgpr_read_b32 v107, a111\n"
    "v_pk_fma_f32 v[104:105], v[104:105], v[228:229], s[94:95]\n"
    "v_pk_fma_f32 v[106:107], v[106:107], v[228:229], s[94:95]\n"
    "v_cvt_pk_bf16_f32 v52, v104, v105\n"
    "v_cvt_pk_bf16_f32 v53, v106, v107\n"
    "v_accvgpr_read_b32 v108, a124\n"
    "v_accvgpr_read_b32 v109, a125\n"
    "v_accvgpr_read_b32 v110, a126\n"
    "v_accvgpr_read_b32 v111, a127\n"
    "v_pk_fma_f32 v[108:109], v[108:109], v[228:229], s[94:95]\n"
    "v_pk_fma_f32 v[110:111], v[110:111], v[228:229], s[94:95]\n"
    "v_cvt_pk_bf16_f32 v54, v108, v109\n"
    "v_cvt_pk_bf16_f32 v55, v110, v111\n"
    "buffer_store_dwordx4 v[52:55], v205, s[28:31], s64 offen offset:192 sc0 sc1\n"
    "v_accvgpr_read_b32 v96, a140\n"
    "v_accvgpr_read_b32 v97, a141\n"
    "v_accvgpr_read_b32 v98, a142\n"
    "v_accvgpr_read_b32 v99, a143\n"
    "v_pk_fma_f32 v[96:97], v[96:97], v[228:229], s[94:95]\n"
    "v_pk_fma_f32 v[98:99], v[98:99], v[228:229], s[94:95]\n"
    "v_cvt_pk_bf16_f32 v56, v96, v97\n"
    "v_cvt_pk_bf16_f32 v57, v98, v99\n"
    "v_accvgpr_read_b32 v100, a156\n"
    "v_accvgpr_read_b32 v101, a157\n"
    "v_accvgpr_read_b32 v102, a158\n"
    "v_accvgpr_read_b32 v103, a159\n"
    "v_pk_fma_f32 v[100:101], v[100:101], v[228:229], s[94:95]\n"
    "v_pk_fma_f32 v[102:103], v[102:103], v[228:229], s[94:95]\n"
    "v_cvt_pk_bf16_f32 v58, v100, v101\n"
    "v_cvt_pk_bf16_f32 v59, v102, v103\n"
    "buffer_store_dwordx4 v[56:59], v205, s[28:31], s64 offen offset:256 sc0 sc1\n"
    "v_accvgpr_read_b32 v104, a172\n"
    "v_accvgpr_read_b32 v105, a173\n"
    "v_accvgpr_read_b32 v106, a174\n"
    "v_accvgpr_read_b32 v107, a175\n"
    "v_pk_fma_f32 v[104:105], v[104:105], v[228:229], s[94:95]\n"
    "v_pk_fma_f32 v[106:107], v[106:107], v[228:229], s[94:95]\n"
    "v_cvt_pk_bf16_f32 v60, v104, v105\n"
    "v_cvt_pk_bf16_f32 v61, v106, v107\n"
    "v_accvgpr_read_b32 v108, a188\n"
    "v_accvgpr_read_b32 v109, a189\n"
    "v_accvgpr_read_b32 v110, a190\n"
    "v_accvgpr_read_b32 v111, a191\n"
    "v_pk_fma_f32 v[108:109], v[108:109], v[228:229], s[94:95]\n"
    "v_pk_fma_f32 v[110:111], v[110:111], v[228:229], s[94:95]\n"
    "v_cvt_pk_bf16_f32 v62, v108, v109\n"
    "v_cvt_pk_bf16_f32 v63, v110, v111\n"
    "buffer_store_dwordx4 v[60:63], v205, s[28:31], s64 offen offset:320 sc0 sc1\n"
    "s_branch LFIN\n"
    "LEND:\n"
    :
    : "s"(__builtin_amdgcn_kernarg_segment_ptr()), "s"(blockIdx.x), "s"(wid)
    : "v0", "v1", "v2", "v3", "v4", "v5", "v6", "v7", "v8", "v9", "v10", "v11", "v12", "v13", "v14", "v15", "v16", "v17", "v18", "v19", "v20", "v21", "v22", "v23", "v24", "v25", "v26", "v27", "v28", "v29", "v30", "v31", "v32", "v33", "v34", "v35", "v36", "v37", "v38", "v39", "v40", "v41", "v42", "v43", "v44", "v45", "v46", "v47", "v48", "v49", "v50", "v51", "v52", "v53", "v54", "v55", "v56", "v57", "v58", "v59", "v60", "v61", "v62", "v63", "v64", "v65", "v66", "v67", "v68", "v69", "v70", "v71", "v72", "v73", "v74", "v75", "v76", "v77", "v78", "v79", "v80", "v81", "v82", "v83", "v84", "v85", "v86", "v87", "v88", "v89", "v90", "v91", "v92", "v93", "v94", "v95", "v96", "v97", "v98", "v99", "v100", "v101", "v102", "v103", "v104", "v105", "v106", "v107", "v108", "v109", "v110", "v111", "v112", "v113", "v114", "v115", "v116", "v117", "v118", "v119", "v120", "v121", "v122", "v123", "v124", "v125", "v126", "v127", "v128", "v129", "v130", "v131", "v132", "v133", "v134", "v135", "v136", "v137", "v138", "v139", "v140", "v141", "v142", "v143", "v144", "v145", "v146", "v147", "v148", "v149", "v150", "v151", "v152", "v153", "v154", "v155", "v156", "v157", "v158", "v159", "v160", "v161", "v162", "v163", "v164", "v165", "v166", "v167", "v168", "v169", "v170", "v171", "v172", "v173", "v174", "v175", "v176", "v177", "v178", "v179", "v180", "v181", "v182", "v183", "v184", "v185", "v186", "v187", "v188", "v189", "v190", "v191", "v192", "v193", "v194", "v195", "v196", "v197", "v198", "v199", "v200", "v201", "v202", "v203", "v204", "v205", "v206", "v207", "v208", "v209", "v210", "v211", "v212", "v213", "v214", "v215", "v216", "v217", "v218", "v219", "v220", "v221", "v222", "v223", "v224", "v225", "v226", "v227", "v228", "v229", "v230", "v231", "v232", "v233", "v234", "v235", "v236", "v237", "v238", "v239", "v240", "v241", "v242", "v243", "v244", "v245", "v246", "v247", "v248", "v249", "v250", "v251", "v252", "v253", "v254", "v255", "a0", "a1", "a2", "a3", "a4", "a5", "a6", "a7", "a8", "a9", "a10", "a11", "a12", "a13", "a14", "a15", "a16", "a17", "a18", "a19", "a20", "a21", "a22", "a23", "a24", "a25", "a26", "a27", "a28", "a29", "a30", "a31", "a32", "a33", "a34", "a35", "a36", "a37", "a38", "a39", "a40", "a41", "a42", "a43", "a44", "a45", "a46", "a47", "a48", "a49", "a50", "a51", "a52", "a53", "a54", "a55", "a56", "a57", "a58", "a59", "a60", "a61", "a62", "a63", "a64", "a65", "a66", "a67", "a68", "a69", "a70", "a71", "a72", "a73", "a74", "a75", "a76", "a77", "a78", "a79", "a80", "a81", "a82", "a83", "a84", "a85", "a86", "a87", "a88", "a89", "a90", "a91", "a92", "a93", "a94", "a95", "a96", "a97", "a98", "a99", "a100", "a101", "a102", "a103", "a104", "a105", "a106", "a107", "a108", "a109", "a110", "a111", "a112", "a113", "a114", "a115", "a116", "a117", "a118", "a119", "a120", "a121", "a122", "a123", "a124", "a125", "a126", "a127", "a128", "a129", "a130", "a131", "a132", "a133", "a134", "a135", "a136", "a137", "a138", "a139", "a140", "a141", "a142", "a143", "a144", "a145", "a146", "a147", "a148", "a149", "a150", "a151", "a152", "a153", "a154", "a155", "a156", "a157", "a158", "a159", "a160", "a161", "a162", "a163", "a164", "a165", "a166", "a167", "a168", "a169", "a170", "a171", "a172", "a173", "a174", "a175", "a176", "a177", "a178", "a179", "a180", "a181", "a182", "a183", "a184", "a185", "a186", "a187", "a188", "a189", "a190", "a191", "a192", "a193", "a194", "a195", "a196", "a197", "a198", "a199", "a200", "a201", "a202", "a203", "a204", "a205", "a206", "a207", "a208", "a209", "a210", "a211", "a212", "a213", "a214", "a215", "a216", "a217", "a218", "a219", "a220", "a221", "a222", "a223", "a224", "a225", "a226", "a227", "a228", "a229", "a230", "a231", "a232", "a233", "a234", "a235", "a236", "a237", "a238", "a239", "a240", "a241", "a242", "a243", "a244", "a245", "a246", "a247", "a248", "a249", "a250", "a251", "a252", "a253", "a254", "a255", "s8", "s9", "s10", "s11", "s12", "s13", "s14", "s15", "s16", "s17", "s18", "s19", "s20", "s21", "s22", "s23", "s24", "s25", "s26", "s27", "s28", "s29", "s30", "s31", "s32", "s33", "s34", "s35", "s36", "s37", "s38", "s39", "s40", "s41", "s42", "s43", "s44", "s45", "s46", "s47", "s48", "s49", "s50", "s51", "s52", "s53", "s54", "s55", "s56", "s57", "s58", "s59", "s60", "s61", "s62", "s63", "s64", "s65", "s66", "s67", "s68", "s69", "s70", "s71", "s72", "s73", "s74", "s75", "s76", "s77", "s78", "s79", "s80", "s81", "s82", "s83", "s84", "s85", "s86", "s87", "s88", "s89", "s90", "s91", "s92", "s93", "s94", "s95", "s96", "s97", "s98", "s99", "s100", "s101", "vcc", "scc", "memory");
}

extern "C" void kernel_launch(void* const* d_in, const int* in_sizes, int n_in, void* d_out, int out_size, void* d_ws, size_t ws_size,
                              hipStream_t stream) {
  (void)hipFuncSetAttribute((const void*)attn_fused, hipFuncAttributeMaxDynamicSharedMemorySize, 153088);
  attn_fused<<<256, 256, 153088, stream>>>(d_in[0], d_in[1], d_in[2], d_in[3], d_in[4], d_in[5], d_in[6], d_ws, d_out);
}
```

```cpp
#include <hip/hip_runtime.h>

__global__ __launch_bounds__(256, 1) void attn_fused(const void* q, const void* qk, const void* qv, const void* ks, const void* kz,
                                                     const void* vs, const void* vz, void* ws, void* out) {
  const int wid = __builtin_amdgcn_readfirstlane(threadIdx.x >> 6);
  asm volatile(
    "LS0:\n"
    "s_load_dwordx16 s[8:23], %0, 0x0\n"
    "s_mov_b32 s58, %2\n"
    "s_and_b32 s76, %1, 7\n"
    "s_lshr_b32 s77, %1, 3\n"
    "s_and_b32 s78, s77, 3\n"
    "s_lshl_b32 s56, s76, 2\n"
    "s_add_u32 s56, s56, s78\n"
    "s_lshr_b32 s57, s77, 2\n"
    "v_mbcnt_lo_u32_b32 v231, -1, 0\n"
    "v_mbcnt_hi_u32_b32 v231, -1, v231\n"
    "s_getpc_b64 s[44:45]\n"
    "LPC:\n"
    "s_mov_b32 s46, LSLOW-LPC\n"
    "s_mov_b32 s47, 0x20000\n"
    "s_lshr_b32 s76, %1, 3\n"
    "s_lshl_b32 s76, s76, 2\n"
    "s_add_u32 s76, s76, s58\n"
    "s_lshl_b32 s76, s76, 9\n"
    "v_lshlrev_b32 v196, 3, v231\n"
    "buffer_load_dword v251, v196, s[44:47], s76 offen\n"
    "s_mul_i32 s72, s58, 0xc00\n"
    "s_lshr_b32 s86, s57, 2\n"
    "s_mul_i32 s64, s86, 0xc000\n"
    "s_lshl_b32 s86, s86, 3\n"
    "s_and_b32 s79, s57, 3\n"
    "s_add_u32 s86, s86, s79\n"
    "s_lshl_b32 s86, s86, 2\n"
    "s_xor_b32 s76, s57, 4\n"
    "s_and_b32 s77, s76, 6\n"
    "s_mul_i32 s77, s77, 15\n"
    "s_add_u32 s76, s76, s77\n"
    "s_and_b32 s77, s58, 1\n"
    "s_mul_i32 s78, s77, 12\n"
    "s_add_u32 s78, s78, s86\n"
    "s_lshl_b32 s78, s78, 1\n"
    "s_mul_i32 s77, s77, 0xc0\n"
    "s_movk_i32 s90, 0x556\n"
    "s_lshl_b32 s60, s56, 12\n"
    "s_cmp_lt_u32 s58, 2\n"
    "s_cselect_b32 s76, s76, s78\n"
    "s_cmovk_i32 s90, 0xaab\n"
    "s_cselect_b32 s91, 24, 0\n"
    "s_cselect_b32 s92, 1, 10\n"
    "s_cselect_b32 s89, 7, 0\n"
    "s_cselect_b32 s64, s64, 0\n"
    "s_add_u32 s60, s60, s76\n"
    "s_mul_i32 s60, s60, 0x180\n"
    "s_mul_i32 s76, s76, 0x180\n"
    "s_sub_u32 s34, 0x180000, s76\n"
    "s_mov_b32 s35, 0x20000\n"
    "v_add_u32 v144, s77, v231\n"
    "v_add_u32 v145, 64, v144\n"
    "v_add_u32 v146, 64, v145\n"
    "v_mul_u32_u24 v197, s90, v144\n"
    "v_lshrrev_b32 v197, 16, v197\n"
    "v_mul_u32_u24 v198, s92, v197\n"
    "v_lshrrev_b32 v198, 1, v198\n"
    "v_and_b32 v198, s89, v198\n"
    "v_xor_b32 v198, v198, v144\n"
    "v_mul_u32_u24 v197, s91, v197\n"
    "v_add_lshl_u32 v202, v197, v198, 4\n"
    "v_mul_u32_u24 v197, s90, v145\n"
    "v_lshrrev_b32 v197, 16, v197\n"
    "v_mul_u32_u24 v198, s92, v197\n"
    "v_lshrrev_b32 v198, 1, v198\n"
    "v_and_b32 v198, s89, v198\n"
    "v_xor_b32 v198, v198, v144\n"
    "v_mul_u32_u24 v197, s91, v197\n"
    "v_add_lshl_u32 v203, v197, v198, 4\n"
    "v_mul_u32_u24 v197, s90, v146\n"
    "v_lshrrev_b32 v197, 16, v197\n"
    "v_mul_u32_u24 v198, s92, v197\n"
    "v_lshrrev_b32 v198, 1, v198\n"
    "v_and_b32 v198, s89, v198\n"
    "v_xor_b32 v198, v198, v144\n"
    "v_mul_u32_u24 v197, s91, v197\n"
    "v_add_lshl_u32 v204, v197, v198, 4\n"
    "s_waitcnt lgkmcnt(0)\n"
    "s_cmp_lt_u32 s58, 2\n"
    "s_cselect_b64 s[80:81], s[10:11], s[12:13]\n"
    "s_cselect_b64 s[82:83], s[16:17], s[14:15]\n"
    "s_add_u32 s32, s80, s60\n"
    "s_addc_u32 s33, s81, 0\n"
    "s_cmp_eq_u32 s64, 0\n"
    "s_cbranch_scc1 LNOR\n"
    "s_add_u32 m0, s72, 0x12000\n"
    "s_nop 0\n"
    "buffer_load_dwordx4 v202, s[32:35], 0 offen lds\n"
    "buffer_load_dwordx4 v203, s[32:35], 0 offen offset:1024 lds\n"
    "buffer_load_dwordx4 v204, s[32:35], 0 offen offset:2048 lds\n"
    "LNOR:\n"
    "s_add_u32 m0, s72, 0x15000\n"
    "s_nop 0\n"
    "buffer_load_dwordx4 v202, s[32:35], s64 offen lds\n"
    "buffer_load_dwordx4 v203, s[32:35], s64 offen offset:1024 lds\n"
    "buffer_load_dwordx4 v204, s[32:35], s64 offen offset:2048 lds\n"
    "LR0:\n"
    "s_load_dwordx2 s[24:25], %0, 0x40\n"
    "s_mul_i32 s71, s58, 0x1800\n"
    "s_lshr_b32 s87, s57, 2\n"
    "s_mul_i32 s87, s87, 0x6000\n"
    "s_mul_i32 s61, s56, 0x3000\n"
    "s_lshl_b32 s62, s56, 12\n"
    "s_lshl_b32 s59, s56, 12\n"
    "s_mov_b32 s50, 0x3000\n"
    "s_mov_b32 s51, 0x20000\n"
    "s_and_b32 s77, s57, 1\n"
    "s_mul_i32 s77, s77, 0xc0\n"
    "v_mul_u32_u24 v197, 0x1556, v144\n"
    "v_lshrrev_b32 v197, 16, v197\n"
    "v_mul_u32_u24 v196, 12, v197\n"
    "v_sub_u32 v196, v144, v196\n"
    "v_mul_u32_u24 v248, 0x180, v197\n"
    "v_lshl_add_u32 v248, v196, 4, v248\n"
    "v_add_u32 v248, s77, v248\n"
    "s_add_u32 s48, s82, s61\n"
    "s_addc_u32 s49, s83, 0\n"
    "s_add_u32 m0, s72, 0x21000\n"
    "s_nop 0\n"
    "buffer_load_dwordx4 v248, s[48:51], 0 offen lds\n"
    "s_add_u32 s40, s20, s62\n"
    "s_addc_u32 s41, s21, 0\n"
    "s_add_u32 s52, s18, s62\n"
    "s_addc_u32 s53, s19, 0\n"
    "s_mov_b32 s42, 0x1000\n"
    "s_mov_b32 s43, 0x20000\n"
    "s_mov_b32 s54, 0x1000\n"
    "s_mov_b32 s55, 0x20000\n"
    "v_lshlrev_b32 v196, 1, v231\n"
    "buffer_load_ushort v44, v196, s[40:43], 0 offen\n"
    "buffer_load_ushort v45, v196, s[52:55], 0 offen\n"
    "s_lshl_b32 s77, s58, 6\n"
    "v_add_u32 v41, s77, v231\n"
    "v_bfe_u32 v197, v231, 2, 1\n"
    "v_mul_u32_u24 v197, 12, v197\n"
    "v_and_b32 v198, 7, v231\n"
    "v_add3_u32 v197, v197, v198, s86\n"
    "v_lshrrev_b32 v40, 3, v41\n"
    "v_lshlrev_b32 v40, 7, v40\n"
    "v_lshl_add_u32 v40, v197, 1, v40\n"
    "buffer_load_ushort v42, v40, s[40:43], 0 offen\n"
    "buffer_load_ushort v43, v40, s[52:55], 0 offen\n"
    "s_add_u32 s44, s22, s59\n"
    "s_addc_u32 s45, s23, 0\n"
    "s_mov_b32 s46, 0x900\n"
    "s_mov_b32 s47, 0x20000\n"
    "v_mov_b32 v196, 0x40000000\n"
    "s_cmp_eq_u32 s58, 0\n"
    "s_cselect_b32 s77, 64, 0\n"
    "s_cselect_b32 s74, 1, 0\n"
    "v_and_b32 v213, 7, v231\n"
    "v_mul_u32_u24 v213, 0x100, v213\n"
    "v_cmp_gt_u32 vcc, s77, v231\n"
    "v_mov_b32 v197, 0x800\n"
    "s_mul_i32 s77, s57, 0x100\n"
    "v_cndmask_b32 v213, v196, v213, vcc\n"
    "v_cndmask_b32 v197, v196, v197, vcc\n"
    "buffer_load_dword v221, v197, s[44:47], 0 offen sc1\n"
    "v_cmp_gt_u32 vcc, s74, v231\n"
    "v_mov_b32 v214, s77\n"
    "s_mul_i32 s66, s74, 3\n"
    "v_cndmask_b32 v214, v196, v214, vcc\n"
    "s_lshl_b32 s76, s56, 3\n"
    "s_add_u32 s76, s76, s57\n"
    "s_mul_i32 s76, s76, 0x18000\n"
    "s_add_u32 s28, s8, s76\n"
    "s_addc_u32 s29, s9, 0\n"
    "s_mov_b32 s30, 0x18000\n"
    "s_mov_b32 s31, 0x20000\n"
    "s_mul_i32 s88, s58, 0x6000\n"
    "v_mul_u32_u24 v197, 0xaab, v231\n"
    "v_lshrrev_b32 v197, 16, v197\n"
    "v_mul_u32_u24 v196, 24, v197\n"
    "v_sub_u32 v196, v231, v196\n"
    "v_mul_u32_u24 v152, 0x180, v197\n"
    "v_lshrrev_b32 v197, 1, v197\n"
    "v_xor_b32 v196, v196, v197\n"
    "v_lshl_add_u32 v152, v196, 4, v152\n"
    "v_add_u32 v198, 64, v231\n"
    "v_mul_u32_u24 v197, 0xaab, v198\n"
    "v_lshrrev_b32 v197, 16, v197\n"
    "v_mul_u32_u24 v196, 24, v197\n"
    "v_sub_u32 v196, v198, v196\n"
    "v_mul_u32_u24 v153, 0x180, v197\n"
    "v_lshrrev_b32 v197, 1, v197\n"
    "v_xor_b32 v196, v196, v197\n"
    "v_lshl_add_u32 v153, v196, 4, v153\n"
    "v_add_u32 v198, 0x80, v231\n"
    "v_mul_u32_u24 v197, 0xaab, v198\n"
    "v_lshrrev_b32 v197, 16, v197\n"
    "v_mul_u32_u24 v196, 24, v197\n"
    "v_sub_u32 v196, v198, v196\n"
    "v_mul_u32_u24 v154, 0x180, v197\n"
    "v_lshrrev_b32 v197, 1, v197\n"
    "v_xor_b32 v196, v196, v197\n"
    "v_lshl_add_u32 v154, v196, 4, v154\n"
    "s_mov_b32 s76, 0xc00\n"
    "v_xad_u32 v155, v152, 64, s76\n"
    "v_xad_u32 v156, v153, 64, s76\n"
    "v_xad_u32 v157, v154, 64, s76\n"
    "s_add_u32 s78, s88, 0\n"
    "s_add_u32 m0, s71, 0\n"
    "s_nop 0\n"
    "buffer_load_dwordx4 v152, s[28:31], s78 offen nt lds\n"
    "s_add_u32 s78, s88, 0\n"
    "s_add_u32 m0, s71, 0x400\n"
    "s_nop 0\n"
    "buffer_load_dwordx4 v153, s[28:31], s78 offen nt lds\n"
    "s_add_u32 s78, s64, 0xc000\n"
    "s_add_u32 m0, s72, 0x1e000\n"
    "s_nop 0\n"
    "buffer_load_dwordx4 v202, s[32:35], s78 offen lds\n"
    "buffer_load_dwordx4 v203, s[32:35], s78 offen offset:1024 lds\n"
    "buffer_load_dwordx4 v204, s[32:35], s78 offen offset:2048 lds\n"
    "v_and_b32 v46, 15, v231\n"
    "v_lshrrev_b32 v47, 4, v231\n"
    "s_mov_b32 s75, 0x3dd53b95\n"
    "s_min_u32 s78, s58, 2\n"
    "s_lshl_b32 s76, s78, 3\n"
    "v_lshl_add_u32 v197, v47, 1, s76\n"
    "v_lshrrev_b32 v198, 1, v46\n"
    "v_xor_b32 v197, v197, v198\n"
    "v_mul_u32_u24 v205, 0x180, v46\n"
    "v_lshl_add_u32 v205, v197, 4, v205\n"
    "v_add_u32 v205, 0x12000, v205\n"
    "v_xor_b32 v206, 16, v205\n"
    "v_and_b32 v197, 7, v231\n"
    "v_lshl_add_u32 v209, v47, 3, v197\n"
    "s_lshl_b32 s76, s78, 5\n"
    "v_add_lshl_u32 v209, v209, s76, 1\n"
    "v_add_u32 v217, 0x21000, v209\n"
    "s_lshr_b32 s76, s57, 2\n"
    "s_mul_i32 s76, s76, 0xc0\n"
    "s_add_u32 s76, s76, 0x20f40\n"
    "v_add_u32 v209, s76, v209\n"
    "v_lshlrev_b32 v210, 3, v197\n"
    "v_add_u32 v210, 0x24400, v210\n"
    "s_lshr_b32 s76, s57, 1\n"
    "s_xor_b32 s76, s76, 2\n"
    "s_mul_i32 s76, s76, 0x1800\n"
    "s_and_b32 s77, s57, 1\n"
    "s_mul_i32 s77, s77, 0xc00\n"
    "s_add_u32 s76, s76, s77\n"
    "s_lshl_b32 s77, s58, 10\n"
    "s_add_u32 s76, s76, s77\n"
    "s_cmp_eq_u32 s58, 3\n"
    "s_cselect_b32 s76, 0x40000000, s76\n"
    "v_lshl_add_u32 v211, v231, 4, s76\n"
    "s_max_u32 s78, s58, 1\n"
    "s_lshl_b32 s78, s78, 6\n"
    "s_sub_u32 s78, s78, 64\n"
    "v_add_u32 v198, s78, v231\n"
    "v_lshlrev_b32 v207, 2, v198\n"
    "v_add_u32 v207, 0x13800, v207\n"
    "s_lshr_b32 s76, s57, 2\n"
    "s_lshl_b32 s76, s76, 10\n"
    "s_lshl_b32 s77, s79, 8\n"
    "s_add_u32 s76, s76, s77\n"
    "s_cmp_eq_u32 s58, 0\n"
    "s_cselect_b32 s76, 0x40000000, s76\n"
    "v_lshrrev_b32 v197, 5, v198\n"
    "v_lshl_add_u32 v212, v197, 12, s76\n"
    "v_bfe_u32 v197, v198, 2, 1\n"
    "v_lshl_add_u32 v212, v197, 11, v212\n"
    "v_bfe_u32 v197, v198, 3, 2\n"
    "v_lshl_add_u32 v212, v197, 6, v212\n"
    "v_and_b32 v197, 3, v198\n"
    "v_lshl_add_u32 v212, v197, 4, v212\n"
    "v_mov_b32 v222, 0\n"
    "v_mov_b32 v223, 0\n"
    "v_mov_b32 v224, 0\n"
    "v_mov_b32 v225, 0\n"
    "v_mov_b32 v160, 0\n"
    "v_mov_b32 v161, 0\n"
    "v_mov_b32 v162, 0\n"
    "v_mov_b32 v163, 0\n"
    "v_mov_b32 v164, 0\n"
    "v_mov_b32 v165, 0\n"
    "v_mov_b32 v172, 0\n"
    "v_mov_b32 v173, 0\n"
    "v_mov_b32 v174, 0\n"
    "v_mov_b32 v175, 0\n"
    "v_lshlrev_b32 v41, 3, v41\n"
    "v_add_u32 v41, 0x24400, v41\n"
    "s_mul_i32 s76, s56, 0xc0000\n"
    "s_add_u32 s77, s76, 0x40000\n"
    "s_add_u32 s36, s22, s77\n"
    "s_addc_u32 s37, s23, 0\n"
    "s_mov_b32 s38, 0xc0000\n"
    "s_mov_b32 s39, 0x20000\n"
    "s_cmp_eq_u32 s87, 0\n"
    "s_cselect_b32 s62, 0x40000000, 0\n"
    "v_lshlrev_b32 v200, 4, v231\n"
    "v_add_u32 v201, 0xc000, v200\n"
    "v_mul_u32_u24 v198, 0x180, v46\n"
    "v_add_u32 v198, s71, v198\n"
    "v_lshrrev_b32 v197, 1, v46\n"
    "v_xor_b32 v197, v197, v47\n"
    "v_lshl_add_u32 v158, v197, 4, v198\n"
    "v_xor_b32 v159, 64, v158\n"
    "v_and_b32 v197, 3, v231\n"
    "v_cmp_eq_u32 vcc, 0, v197\n"
    "v_mov_b32 v198, 0x3f803f80\n"
    "s_nop 0\n"
    "v_cndmask_b32 v192, 0, v198, vcc\n"
    "v_cndmask_b32 v193, 0, v198, vcc\n"
    "v_cndmask_b32 v194, 0, v198, vcc\n"
    "v_cndmask_b32 v195, 0, v198, vcc\n"
    "s_and_b32 s77, s57, 1\n"
    "s_mul_i32 s77, s77, 0xc0\n"
    "s_sub_u32 s77, s77, 0x400\n"
    "v_mul_u32_u24 v197, 0x1556, v145\n"
    "v_lshrrev_b32 v197, 16, v197\n"
    "v_mul_u32_u24 v196, 12, v197\n"
    "v_sub_u32 v196, v145, v196\n"
    "v_mul_u32_u24 v249, 0x180, v197\n"
    "v_lshl_add_u32 v249, v196, 4, v249\n"
    "v_add_u32 v249, s77, v249\n"
    "s_and_b32 s77, s57, 1\n"
    "s_mul_i32 s77, s77, 0xc0\n"
    "s_sub_u32 s77, s77, 0x800\n"
    "v_mul_u32_u24 v197, 0x1556, v146\n"
    "v_lshrrev_b32 v197, 16, v197\n"
    "v_mul_u32_u24 v196, 12, v197\n"
    "v_sub_u32 v196, v146, v196\n"
    "v_mul_u32_u24 v250, 0x180, v197\n"
    "v_lshl_add_u32 v250, v196, 4, v250\n"
    "v_add_u32 v250, s77, v250\n"
    "s_waitcnt vmcnt(9)\n"
    "v_lshlrev_b32 v44, 16, v44\n"
    "s_waitcnt vmcnt(8)\n"
    "v_lshlrev_b32 v45, 16, v45\n"
    "v_mul_f32 v197, v44, v45\n"
    "s_nop 1\n"
    "v_min_f32_dpp v197, v197, v197 row_ror:8 row_mask:0xf bank_mask:0xf\n"
    "s_mul_i32 s76, s56, 0xc0000\n"
    "s_add_u32 s77, s76, 0x1840000\n"
    "v_min_f32_dpp v197, v197, v197 row_ror:4 row_mask:0xf bank_mask:0xf\n"
    "s_add_u32 s40, s22, s77\n"
    "s_addc_u32 s41, s23, 0\n"
    "v_min_f32_dpp v197, v197, v197 row_ror:2 row_mask:0xf bank_mask:0xf\n"
    "s_mov_b32 s42, 0xc0000\n"
    "s_mov_b32 s43, 0x20000\n"
    "v_min_f32_dpp v197, v197, v197 row_ror:1 row_mask:0xf bank_mask:0xf\n"
    "v_mov_b32 v198, v197\n"
    "s_nop 1\n"
    "v_permlane32_swap_b32 v197, v198\n"
    "v_min_f32 v197, v197, v198\n"
    "v_mov_b32 v198, v197\n"
    "s_nop 1\n"
    "v_permlane16_swap_b32 v197, v198\n"
    "v_min_f32 v197, v197, v198\n"
    "v_rcp_f32 v198, v197\n"
    "v_subrev_u32 v199, 0x37800000, v197\n"
    "v_mul_f32 v197, 0x3fcccccd, v197\n"
    "v_cmp_gt_u32 vcc, 0x10000000, v199\n"
    "v_mul_f32 v198, 0x3f3425ed, v198\n"
    "v_mov_b32 v199, 0x3f800000\n"
    "v_cndmask_b32 v197, 0, v197, vcc\n"
    "v_cndmask_b32 v198, v199, v198, vcc\n"
    "v_readfirstlane_b32 s70, v197\n"
    "v_mul_f32 v197, v197, v198\n"
    "v_readfirstlane_b32 s68, v198\n"
    "v_readfirstlane_b32 s69, v197\n"
    "s_waitcnt vmcnt(7)\n"
    "v_lshlrev_b32 v42, 16, v42\n"
    "s_waitcnt vmcnt(6)\n"
    "v_lshlrev_b32 v43, 16, v43\n"
    "v_mul_f32 v44, s68, v43\n"
    "v_fma_f32 v45, -v42, v44, s69\n"
    "ds_write_b64 v41, v[44:45]\n"
    "s_add_u32 s78, s88, 0\n"
    "s_add_u32 m0, s71, 0x800\n"
    "s_nop 0\n"
    "buffer_load_dwordx4 v154, s[28:31], s78 offen nt lds\n"
    "s_add_u32 s78, s88, 0\n"
    "s_add_u32 m0, s71, 0xc00\n"
    "s_nop 0\n"
    "buffer_load_dwordx4 v155, s[28:31], s78 offen nt lds\n"
    "s_waitcnt lgkmcnt(0)\n"
    "s_barrier\n"
    "ds_read_u16_d16_hi v172, v217 offset:6144\n"
    "ds_read_u16_d16_hi v173, v217\n"
    "ds_read_u16_d16_hi v174, v217 offset:6336\n"
    "ds_read_u16_d16_hi v175, v217 offset:192\n"
    "ds_read_b64 v[228:229], v210\n"
    "ds_read2st64_b32 v[240:241], v207 offset0:48 offset1:51\n"
    "ds_read2st64_b32 v[242:243], v207 offset0:54 offset1:57\n"
    "ds_read2st64_b32 v[244:245], v207 offset0:60 offset1:63\n"
    "ds_read2st64_b32 v[246:247], v207 offset0:66 offset1:69\n"
    "ds_read_u16_d16_hi v223, v209 offset:6144\n"
    "ds_read_u16_d16_hi v222, v209\n"
    "ds_read_b128 v[232:235], v205\n"
    "ds_read_b128 v[236:239], v206\n"
    "ds_read_u16_d16_hi v161, v209 offset:6336\n"
    "ds_read_u16_d16_hi v160, v209 offset:192\n"
    "s_waitcnt lgkmcnt(11)\n"
    "ds_read_b128 v[64:67], v205 offset:12288\n"
    "ds_read_b128 v[68:71], v206 offset:12288\n"
    "v_mul_f32 v197, v172, v173\n"
    "v_mul_f32 v198, v174, v175\n"
    "v_min_f32 v197, v197, v198\n"
    "v_max_f32 v197, 0, v197\n"
    "v_mul_f32 v217, 0x3e3fe8d2, v197\n"
    "s_waitcnt lgkmcnt(7)\n"
    "v_mul_f32 v226, s75, v223\n"
    "s_waitcnt lgkmcnt(6)\n"
    "v_fma_f32 v227, -v222, v226, v217\n"
    "s_waitcnt lgkmcnt(5)\n"
    "v_cvt_f32_i32 v232, v232\n"
    "v_cvt_f32_i32 v233, v233\n"
    "v_cvt_f32_i32 v234, v234\n"
    "v_cvt_f32_i32 v235, v235\n"
    "s_waitcnt lgkmcnt(4)\n"
    "v_cvt_f32_i32 v236, v236\n"
    "v_cvt_f32_i32 v237, v237\n"
    "v_cvt_f32_i32 v238, v238\n"
    "v_cvt_f32_i32 v239, v239\n"
    "v_mul_f32_dpp v232, v226, v232 row_newbcast:0 row_mask:0xf bank_mask:0xf\n"
    "v_mul_f32_dpp v233, v226, v233 row_newbcast:1 row_mask:0xf bank_mask:0xf\n"
    "v_mul_f32_dpp v234, v226, v234 row_newbcast:2 row_mask:0xf bank_mask:0xf\n"
    "v_mul_f32_dpp v235, v226, v235 row_newbcast:3 row_mask:0xf bank_mask:0xf\n"
    "v_mul_f32_dpp v236, v226, v236 row_newbcast:4 row_mask:0xf bank_mask:0xf\n"
    "v_mul_f32_dpp v237, v226, v237 row_newbcast:5 row_mask:0xf bank_mask:0xf\n"
    "v_mul_f32_dpp v238, v226, v238 row_newbcast:6 row_mask:0xf bank_mask:0xf\n"
    "v_mul_f32_dpp v239, v226, v239 row_newbcast:7 row_mask:0xf bank_mask:0xf\n"
    "v_add_f32_dpp v232, v227, v232 row_newbcast:0 row_mask:0xf bank_mask:0xf\n"
    "v_add_f32_dpp v233, v227, v233 row_newbcast:1 row_mask:0xf bank_mask:0xf\n"
    "v_add_f32_dpp v234, v227, v234 row_newbcast:2 row_mask:0xf bank_mask:0xf\n"
    "v_add_f32_dpp v235, v227, v235 row_newbcast:3 row_mask:0xf bank_mask:0xf\n"
    "v_add_f32_dpp v236, v227, v236 row_newbcast:4 row_mask:0xf bank_mask:0xf\n"
    "v_add_f32_dpp v237, v227, v237 row_newbcast:5 row_mask:0xf bank_mask:0xf\n"
    "v_add_f32_dpp v238, v227, v238 row_newbcast:6 row_mask:0xf bank_mask:0xf\n"
    "v_add_f32_dpp v239, v227, v239 row_newbcast:7 row_mask:0xf bank_mask:0xf\n"
    "v_cvt_pk_bf16_f32 v232, v232, v233\n"
    "v_cvt_pk_bf16_f32 v233, v234, v235\n"
    "v_cvt_pk_bf16_f32 v234, v236, v237\n"
    "v_cvt_pk_bf16_f32 v235, v238, v239\n"
    "buffer_store_dwordx4 v[232:235], v211, s[36:39], s62 offen sc1\n"
    "v_cvt_f32_i32 v240, v240\n"
    "v_cvt_f32_i32 v241, v241\n"
    "v_cvt_f32_i32 v242, v242\n"
    "v_cvt_f32_i32 v243, v243\n"
    "v_cvt_f32_i32 v244, v244\n"
    "v_cvt_f32_i32 v245, v245\n"
    "v_cvt_f32_i32 v246, v246\n"
    "v_cvt_f32_i32 v247, v247\n"
    "v_mul_f32_dpp v240, v228, v240 row_newbcast:0 row_mask:0xf bank_mask:0xf\n"
    "v_mul_f32_dpp v241, v228, v241 row_newbcast:1 row_mask:0xf bank_mask:0xf\n"
    "v_mul_f32_dpp v242, v228, v242 row_newbcast:2 row_mask:0xf bank_mask:0xf\n"
    "v_mul_f32_dpp v243, v228, v243 row_newbcast:3 row_mask:0xf bank_mask:0xf\n"
    "v_mul_f32_dpp v244, v228, v244 row_newbcast:4 row_mask:0xf bank_mask:0xf\n"
    "v_mul_f32_dpp v245, v228, v245 row_newbcast:5 row_mask:0xf bank_mask:0xf\n"
    "v_mul_f32_dpp v246, v228, v246 row_newbcast:6 row_mask:0xf bank_mask:0xf\n"
    "v_mul_f32_dpp v247, v228, v247 row_newbcast:7 row_mask:0xf bank_mask:0xf\n"
    "v_add_f32_dpp v240, v229, v240 row_newbcast:0 row_mask:0xf bank_mask:0xf\n"
    "v_add_f32_dpp v241, v229, v241 row_newbcast:1 row_mask:0xf bank_mask:0xf\n"
    "v_add_f32_dpp v242, v229, v242 row_newbcast:2 row_mask:0xf bank_mask:0xf\n"
    "v_add_f32_dpp v243, v229, v243 row_newbcast:3 row_mask:0xf bank_mask:0xf\n"
    "v_add_f32_dpp v244, v229, v244 row_newbcast:4 row_mask:0xf bank_mask:0xf\n"
    "v_add_f32_dpp v245, v229, v245 row_newbcast:5 row_mask:0xf bank_mask:0xf\n"
    "v_add_f32_dpp v246, v229, v246 row_newbcast:6 row_mask:0xf bank_mask:0xf\n"
    "v_add_f32_dpp v247, v229, v247 row_newbcast:7 row_mask:0xf bank_mask:0xf\n"
    "v_cvt_pk_bf16_f32 v240, v240, v241\n"
    "v_cvt_pk_bf16_f32 v241, v242, v243\n"
    "v_cvt_pk_bf16_f32 v242, v244, v245\n"
    "v_cvt_pk_bf16_f32 v243, v246, v247\n"
    "s_mov_b32 s78, 0\n"
    "buffer_store_dwordx4 v[240:243], v212, s[40:43], s78 offen sc1\n"
    "s_waitcnt lgkmcnt(3)\n"
    "v_mul_f32 v166, s75, v161\n"
    "s_waitcnt lgkmcnt(2)\n"
    "v_fma_f32 v167, -v160, v166, v217\n"
    "s_waitcnt lgkmcnt(1)\n"
    "v_cvt_f32_i32 v64, v64\n"
    "v_cvt_f32_i32 v65, v65\n"
    "v_cvt_f32_i32 v66, v66\n"
    "v_cvt_f32_i32 v67, v67\n"
    "s_waitcnt lgkmcnt(0)\n"
    "v_cvt_f32_i32 v68, v68\n"
    "v_cvt_f32_i32 v69, v69\n"
    "v_cvt_f32_i32 v70, v70\n"
    "v_cvt_f32_i32 v71, v71\n"
    "v_mul_f32_dpp v64, v166, v64 row_newbcast:0 row_mask:0xf bank_mask:0xf\n"
    "v_mul_f32_dpp v65, v166, v65 row_newbcast:1 row_mask:0xf bank_mask:0xf\n"
    "v_mul_f32_dpp v66, v166, v66 row_newbcast:2 row_mask:0xf bank_mask:0xf\n"
    "v_mul_f32_dpp v67, v166, v67 row_newbcast:3 row_mask:0xf bank_mask:0xf\n"
    "v_mul_f32_dpp v68, v166, v68 row_newbcast:4 row_mask:0xf bank_mask:0xf\n"
    "v_mul_f32_dpp v69, v166, v69 row_newbcast:5 row_mask:0xf bank_mask:0xf\n"
    "v_mul_f32_dpp v70, v166, v70 row_newbcast:6 row_mask:0xf bank_mask:0xf\n"
    "v_mul_f32_dpp v71, v166, v71 row_newbcast:7 row_mask:0xf bank_mask:0xf\n"
    "v_add_f32_dpp v64, v167, v64 row_newbcast:0 row_mask:0xf bank_mask:0xf\n"
    "v_add_f32_dpp v65, v167, v65 row_newbcast:1 row_mask:0xf bank_mask:0xf\n"
    "v_add_f32_dpp v66, v167, v66 row_newbcast:2 row_mask:0xf bank_mask:0xf\n"
    "v_add_f32_dpp v67, v167, v67 row_newbcast:3 row_mask:0xf bank_mask:0xf\n"
    "v_add_f32_dpp v68, v167, v68 row_newbcast:4 row_mask:0xf bank_mask:0xf\n"
    "v_add_f32_dpp v69, v167, v69 row_newbcast:5 row_mask:0xf bank_mask:0xf\n"
    "v_add_f32_dpp v70, v167, v70 row_newbcast:6 row_mask:0xf bank_mask:0xf\n"
    "v_add_f32_dpp v71, v167, v71 row_newbcast:7 row_mask:0xf bank_mask:0xf\n"
    "v_cvt_pk_bf16_f32 v64, v64, v65\n"
    "v_cvt_pk_bf16_f32 v65, v66, v67\n"
    "v_cvt_pk_bf16_f32 v66, v68, v69\n"
    "v_cvt_pk_bf16_f32 v67, v70, v71\n"
    "s_add_u32 s78, s87, 0\n"
    "buffer_store_dwordx4 v[64:67], v211, s[36:39], s78 offen sc1\n"
    "s_waitcnt vmcnt(10)\n"
    "v_add_u32 v221, s74, v221\n"
    "s_mov_b32 s73, 0\n"
    "v_readfirstlane_b32 s67, v221\n"
    "s_mul_i32 s66, s74, 2\n"
    "s_waitcnt vmcnt(0)\n"
    "s_barrier\n"
    "v_mov_b32 v220, 2\n"
    "buffer_store_dwordx2 v[220:221], v214, s[44:47], 0 offen sc1\n"
    "s_add_u32 s78, s64, 0x18000\n"
    "s_add_u32 m0, s72, 0x1b000\n"
    "s_nop 0\n"
    "buffer_load_dwordx4 v202, s[32:35], s78 offen lds\n"
    "buffer_load_dwordx4 v203, s[32:35], s78 offen offset:1024 lds\n"
    "buffer_load_dwordx4 v204, s[32:35], s78 offen offset:2048 lds\n"
    "s_add_u32 s78, s88, 0\n"
    "s_add_u32 m0, s71, 0x1000\n"
    "s_nop 0\n"
    "buffer_load_dwordx4 v156, s[28:31], s78 offen nt lds\n"
    "s_add_u32 s78, s88, 0\n"
    "s_add_u32 m0, s71, 0x1400\n"
    "s_nop 0\n"
    "buffer_load_dwordx4 v157, s[28:31], s78 offen nt lds\n"
    "s_add_u32 s78, s88, 0x1800\n"
    "s_add_u32 m0, s71, 0x6000\n"
    "s_nop 0\n"
    "buffer_load_dwordx4 v152, s[28:31], s78 offen nt lds\n"
    "s_add_u32 s78, s88, 0x1800\n"
    "s_add_u32 m0, s71, 0x6400\n"
    "s_nop 0\n"
    "buffer_load_dwordx4 v153, s[28:31], s78 offen nt lds\n"
    "s_barrier\n"
    "ds_read_u16_d16_hi v163, v209 offset:6528\n"
    "ds_read_u16_d16_hi v162, v209 offset:384\n"
    "ds_read_b128 v[72:75], v205 offset:49152\n"
    "ds_read_b128 v[76:79], v206 offset:49152\n"
    "ds_read_b64 v[170:171], v210 offset:64\n"
    "ds_read2st64_b32 v[80:81], v207 offset0:192 offset1:195\n"
    "ds_read2st64_b32 v[82:83], v207 offset0:198 offset1:201\n"
    "ds_read2st64_b32 v[84:85], v207 offset0:204 offset1:207\n"
    "ds_read2st64_b32 v[86:87], v207 offset0:210 offset1:213\n"
    "s_waitcnt lgkmcnt(8)\n"
    "v_mul_f32 v168, s75, v163\n"
    "s_waitcnt lgkmcnt(7)\n"
    "v_fma_f32 v169, -v162, v168, v217\n"
    "s_waitcnt lgkmcnt(6)\n"
    "v_cvt_f32_i32 v72, v72\n"
    "v_cvt_f32_i32 v73, v73\n"
    "v_cvt_f32_i32 v74, v74\n"
    "v_cvt_f32_i32 v75, v75\n"
    "s_waitcnt lgkmcnt(5)\n"
    "v_cvt_f32_i32 v76, v76\n"
    "v_cvt_f32_i32 v77, v77\n"
    "v_cvt_f32_i32 v78, v78\n"
    "v_cvt_f32_i32 v79, v79\n"
    "v_mul_f32_dpp v72, v168, v72 row_newbcast:0 row_mask:0xf bank_mask:0xf\n"
    "v_mul_f32_dpp v73, v168, v73 row_newbcast:1 row_mask:0xf bank_mask:0xf\n"
    "v_mul_f32_dpp v74, v168, v74 row_newbcast:2 row_mask:0xf bank_mask:0xf\n"
    "v_mul_f32_dpp v75, v168, v75 row_newbcast:3 row_mask:0xf bank_mask:0xf\n"
    "v_mul_f32_dpp v76, v168, v76 row_newbcast:4 row_mask:0xf bank_mask:0xf\n"
    "v_mul_f32_dpp v77, v168, v77 row_newbcast:5 row_mask:0xf bank_mask:0xf\n"
    "v_mul_f32_dpp v78, v168, v78 row_newbcast:6 row_mask:0xf bank_mask:0xf\n"
    "v_mul_f32_dpp v79, v168, v79 row_newbcast:7 row_mask:0xf bank_mask:0xf\n"
    "v_add_f32_dpp v72, v169, v72 row_newbcast:0 row_mask:0xf bank_mask:0xf\n"
    "v_add_f32_dpp v73, v169, v73 row_newbcast:1 row_mask:0xf bank_mask:0xf\n"
    "v_add_f32_dpp v74, v169, v74 row_newbcast:2 row_mask:0xf bank_mask:0xf\n"
    "v_add_f32_dpp v75, v169, v75 row_newbcast:3 row_mask:0xf bank_mask:0xf\n"
    "v_add_f32_dpp v76, v169, v76 row_newbcast:4 row_mask:0xf bank_mask:0xf\n"
    "v_add_f32_dpp v77, v169, v77 row_newbcast:5 row_mask:0xf bank_mask:0xf\n"
    "v_add_f32_dpp v78, v169, v78 row_newbcast:6 row_mask:0xf bank_mask:0xf\n"
    "v_add_f32_dpp v79, v169, v79 row_newbcast:7 row_mask:0xf bank_mask:0xf\n"
    "v_cvt_pk_bf16_f32 v72, v72, v73\n"
    "v_cvt_pk_bf16_f32 v73, v74, v75\n"
    "v_cvt_pk_bf16_f32 v74, v76, v77\n"
    "v_cvt_pk_bf16_f32 v75, v78, v79\n"
    "s_add_u32 s78, s87, 0x6000\n"
    "buffer_store_dwordx4 v[72:75], v211, s[36:39], s78 offen sc1\n"
    "s_waitcnt lgkmcnt(3)\n"
    "v_cvt_f32_i32 v80, v80\n"
    "v_cvt_f32_i32 v81, v81\n"
    "s_waitcnt lgkmcnt(2)\n"
    "v_cvt_f32_i32 v82, v82\n"
    "v_cvt_f32_i32 v83, v83\n"
    "s_waitcnt lgkmcnt(1)\n"
    "v_cvt_f32_i32 v84, v84\n"
    "v_cvt_f32_i32 v85, v85\n"
    "s_waitcnt lgkmcnt(0)\n"
    "v_cvt_f32_i32 v86, v86\n"
    "v_cvt_f32_i32 v87, v87\n"
    "v_mul_f32_dpp v80, v170, v80 row_newbcast:0 row_mask:0xf bank_mask:0xf\n"
    "v_mul_f32_dpp v81, v170, v81 row_newbcast:1 row_mask:0xf bank_mask:0xf\n"
    "v_mul_f32_dpp v82, v170, v82 row_newbcast:2 row_mask:0xf bank_mask:0xf\n"
    "v_mul_f32_dpp v83, v170, v83 row_newbcast:3 row_mask:0xf bank_mask:0xf\n"
    "v_mul_f32_dpp v84, v170, v84 row_newbcast:4 row_mask:0xf bank_mask:0xf\n"
    "v_mul_f32_dpp v85, v170, v85 row_newbcast:5 row_mask:0xf bank_mask:0xf\n"
    "v_mul_f32_dpp v86, v170, v86 row_newbcast:6 row_mask:0xf bank_mask:0xf\n"
    "v_mul_f32_dpp v87, v170, v87 row_newbcast:7 row_mask:0xf bank_mask:0xf\n"
    "v_add_f32_dpp v80, v171, v80 row_newbcast:0 row_mask:0xf bank_mask:0xf\n"
    "v_add_f32_dpp v81, v171, v81 row_newbcast:1 row_mask:0xf bank_mask:0xf\n"
    "v_add_f32_dpp v82, v171, v82 row_newbcast:2 row_mask:0xf bank_mask:0xf\n"
    "v_add_f32_dpp v83, v171, v83 row_newbcast:3 row_mask:0xf bank_mask:0xf\n"
    "v_add_f32_dpp v84, v171, v84 row_newbcast:4 row_mask:0xf bank_mask:0xf\n"
    "v_add_f32_dpp v85, v171, v85 row_newbcast:5 row_mask:0xf bank_mask:0xf\n"
    "v_add_f32_dpp v86, v171, v86 row_newbcast:6 row_mask:0xf bank_mask:0xf\n"
    "v_add_f32_dpp v87, v171, v87 row_newbcast:7 row_mask:0xf bank_mask:0xf\n"
    "v_cvt_pk_bf16_f32 v80, v80, v81\n"
    "v_cvt_pk_bf16_f32 v81, v82, v83\n"
    "v_cvt_pk_bf16_f32 v82, v84, v85\n"
    "v_cvt_pk_bf16_f32 v83, v86, v87\n"
    "s_mov_b32 s78, 0x6000\n"
    "buffer_store_dwordx4 v[80:83], v212, s[40:43], s78 offen sc1\n"
    "buffer_load_dwordx2 v[218:219], v213, s[44:47], 0 offen sc1\n"
    "s_add_u32 s78, s88, 0x1800\n"
    "s_add_u32 m0, s71, 0x6800\n"
    "s_nop 0\n"
    "buffer_load_dwordx4 v154, s[28:31], s78 offen nt lds\n"
    "s_add_u32 s78, s88, 0x1800\n"
    "s_add_u32 m0, s71, 0x6c00\n"
    "s_nop 0\n"
    "buffer_load_dwordx4 v155, s[28:31], s78 offen nt lds\n"
    "s_add_u32 s78, s88, 0x1800\n"
    "s_add_u32 m0, s71, 0x7000\n"
    "s_nop 0\n"
    "buffer_load_dwordx4 v156, s[28:31], s78 offen nt lds\n"
    "s_add_u32 s78, s88, 0x1800\n"
    "s_add_u32 m0, s71, 0x7400\n"
    "s_nop 0\n"
    "buffer_load_dwordx4 v157, s[28:31], s78 offen nt lds\n"
    "s_waitcnt vmcnt(9)\n"
    "ds_read_b128 a[192:195], v158\n"
    "ds_read_b128 a[196:199], v159\n"
    "ds_read_b128 a[200:203], v158 offset:128\n"
    "ds_read_b128 a[204:207], v159 offset:128\n"
    "ds_read_b128 a[208:211], v158 offset:256\n"
    "ds_read_b128 a[212:215], v159 offset:256\n"
    "s_waitcnt vmcnt(4)\n"
    "v_cmp_ne_u32 s[80:81], s67, v219\n"
    "v_cmp_gt_u32 vcc, s66, v218\n"
    "s_or_b64 vcc, vcc, s[80:81]\n"
    "s_cbranch_vccnz LSPRE0\n"
    "LSPB0:\n"
    "s_waitcnt lgkmcnt(0)\n"
    "s_barrier\n"
    "v_mov_b32 v220, 3\n"
    "buffer_store_dwordx2 v[220:221], v214, s[44:47], 0 offen sc1\n"
    "s_add_u32 m0, s72, 0x1e000\n"
    "s_nop 0\n"
    "buffer_load_dwordx4 v200, s[36:39], s72 offen sc1 lds\n"
    "s_add_u32 m0, s72, 0x1e000\n"
    "s_nop 0\n"
    "buffer_load_dwordx4 v200, s[36:39], s72 offen offset:1024 sc1 lds\n"
    "s_add_u32 m0, s72, 0x1e000\n"
    "s_nop 0\n"
    "buffer_load_dwordx4 v200, s[36:39], s72 offen offset:2048 sc1 lds\n"
    "s_add_u32 s65, s71, 0x3000\n"
    "s_add_u32 m0, s71, 0\n"
    "s_nop 0\n"
    "buffer_load_dwordx4 v200, s[36:39], s65 offen sc1 lds\n"
    "s_add_u32 s65, s71, 0x3400\n"
    "s_add_u32 m0, s71, 0x400\n"
    "s_nop 0\n"
    "buffer_load_dwordx4 v200, s[36:39], s65 offen sc1 lds\n"
    "s_add_u32 s65, s71, 0x3800\n"
    "s_add_u32 m0, s71, 0x800\n"
    "s_nop 0\n"
    "buffer_load_dwordx4 v200, s[36:39], s65 offen sc1 lds\n"
    "s_add_u32 s65, s71, 0x3c00\n"
    "s_add_u32 m0, s71, 0xc00\n"
    "s_nop 0\n"
    "buffer_load_dwordx4 v200, s[36:39], s65 offen sc1 lds\n"
    "s_add_u32 s65, s71, 0x4000\n"
    "s_add_u32 m0, s71, 0x1000\n"
    "s_nop 0\n"
    "buffer_load_dwordx4 v200, s[36:39], s65 offen sc1 lds\n"
    "s_add_u32 s65, s71, 0x4400\n"
    "s_add_u32 m0, s71, 0x1400\n"
    "s_nop 0\n"
    "buffer_load_dwordx4 v200, s[36:39], s65 offen sc1 lds\n"
    "s_add_u32 s65, s71, 0\n"
    "s_add_u32 m0, s71, 0x12000\n"
    "s_nop 0\n"
    "buffer_load_dwordx4 v200, s[40:43], s65 offen sc1 lds\n"
    "s_add_u32 s65, s71, 0x400\n"
    "s_add_u32 m0, s71, 0x12400\n"
    "s_nop 0\n"
    "buffer_load_dwordx4 v200, s[40:43], s65 offen sc1 lds\n"
    "s_add_u32 s65, s71, 0x800\n"
    "s_add_u32 m0, s71, 0x12800\n"
    "s_nop 0\n"
    "buffer_load_dwordx4 v200, s[40:43], s65 offen sc1 lds\n"
    "s_add_u32 s65, s71, 0xc00\n"
    "s_add_u32 m0, s71, 0x12c00\n"
    "s_nop 0\n"
    "buffer_load_dwordx4 v200, s[40:43], s65 offen sc1 lds\n"
    "s_add_u32 s65, s71, 0x1000\n"
    "s_add_u32 m0, s71, 0x13000\n"
    "s_nop 0\n"
    "buffer_load_dwordx4 v200, s[40:43], s65 offen sc1 lds\n"
    "s_add_u32 s65, s71, 0x1400\n"
    "s_add_u32 m0, s71, 0x13400\n"
    "s_nop 0\n"
    "buffer_load_dwordx4 v200, s[40:43], s65 offen sc1 lds\n"
    "s_add_u32 s78, s64, 0x24000\n"
    "s_add_u32 m0, s72, 0x18000\n"
    "s_nop 0\n"
    "buffer_load_dwordx4 v202, s[32:35], s78 offen lds\n"
    "buffer_load_dwordx4 v203, s[32:35], s78 offen offset:1024 lds\n"
    "buffer_load_dwordx4 v204, s[32:35], s78 offen offset:2048 lds\n"
    "s_add_u32 s78, s88, 0x3000\n"
    "s_add_u32 m0, s71, 0xc000\n"
    "s_nop 0\n"
    "buffer_load_dwordx4 v152, s[28:31], s78 offen nt lds\n"
    "s_mul_i32 s66, s74, 3\n"
    "s_waitcnt vmcnt(20)\n"
    "ds_read_b128 a[216:219], v158 offset:24576\n"
    "ds_read_b128 a[220:223], v159 offset:24576\n"
    "ds_read_b128 a[224:227], v158 offset:24704\n"
    "ds_read_b128 a[228:231], v159 offset:24704\n"
    "ds_read_b128 a[232:235], v158 offset:24832\n"
    "ds_read_b128 a[236:239], v159 offset:24832\n"
    "s_add_u32 m0, s72, 0x21000\n"
    "s_nop 0\n"
    "buffer_load_dwordx4 v249, s[48:51], 0 offen offset:1024 lds\n"
    "buffer_load_dwordx2 v[218:219], v213, s[44:47], 0 offen sc1\n"
    "v_add_u32 v255, 0x1e000, v200\n"
    "s_add_u32 s60, s71, 0x9000\n"
    "s_add_u32 s61, s71, 0x6000\n"
    "s_add_u32 s62, s87, 0x12000\n"
    "s_mov_b32 s63, 0x12000\n"
    "s_mov_b32 s59, 0\n"
    "s_mul_i32 s66, s74, 3\n"
    "s_getpc_b64 s[52:53]\n"
    "LPC2:\n"
    "s_add_u32 s52, s52, LTAIL-LPC2\n"
    "s_addc_u32 s53, s53, 0\n"
    "s_mov_b32 s54, LSLOW-LTAIL\n"
    "s_lshr_b32 s91, %1, 3\n"
    "s_mul_i32 s91, s91, 4\n"
    "s_add_u32 s91, s91, s58\n"
    "s_sub_u32 s91, s91, 0\n"
    "s_lshl_b32 s91, s91, 9\n"
    "v_lshlrev_b32 v252, 3, v231\n"
    "s_mul_i32 s90, s58, 0x1000\n"
    "s_add_u32 s90, s90, 0x1000\n"
    "s_sub_u32 s92, s90, s71\n"
    "s_waitcnt vmcnt(12) lgkmcnt(0)\n"
    "s_barrier\n"
    "ds_read_b128 v[160:163], v255\n"
    "ds_read_b128 v[164:167], v255 offset:1024\n"
    "ds_read_b128 v[168:171], v255 offset:2048\n"
    "ds_read_b128 v[172:175], v255 offset:3072\n"
    "ds_read_b128 v[176:179], v255 offset:4096\n"
    "ds_read_b128 v[180:183], v255 offset:5120\n"
    "s_add_u32 s76, s87, 0xc000\n"
    "s_mov_b32 s77, 0xc000\n"
    "s_waitcnt lgkmcnt(4)\n"
    "v_mfma_f32_16x16x32_bf16 v[40:43], v[160:163], a[192:195], 0\n"
    "ds_read_b128 v[184:187], v255 offset:6144\n"
    "ds_read_b64 v[228:229], v210 offset:128\n"
    "v_mfma_f32_16x16x32_bf16 v[40:43], v[164:167], a[196:199], v[40:43]\n"
    "s_add_u32 s78, s88, 0x3000\n"
    "ds_read2st64_b32 v[240:241], v207 offset0:144 offset1:147\n"
    "ds_read2st64_b32 v[242:243], v207 offset0:150 offset1:153\n"
    "v_mfma_f32_16x16x32_bf16 v[44:47], v[160:163], a[216:219], 0\n"
    "s_add_u32 m0, s71, 0xc400\n"
    "ds_read_b128 v[188:191], v255 offset:7168\n"
    "ds_read2st64_b32 v[244:245], v207 offset0:156 offset1:159\n"
    "v_mfma_f32_16x16x32_bf16 v[44:47], v[164:167], a[220:223], v[44:47]\n"
    "s_waitcnt lgkmcnt(8)\n"
    "buffer_load_dwordx4 v153, s[28:31], s78 offen nt lds\n"
    "v_mfma_f32_16x16x32_bf16 v[40:43], v[168:171], a[200:203], v[40:43]\n"
    "ds_read_b128 v[160:163], v255 offset:8192\n"
    "ds_read2st64_b32 v[246:247], v207 offset0:162 offset1:165\n"
    "v_mfma_f32_16x16x32_bf16 v[40:43], v[172:175], a[204:207], v[40:43]\n"
    "ds_read_u16_d16_hi v223, v209 offset:6720\n"
    "ds_read_u16_d16_hi v222, v209 offset:576\n"
    "v_mfma_f32_16x16x32_bf16 v[44:47], v[168:171], a[224:227], v[44:47]\n"
    "ds_read_b128 v[164:167], v255 offset:9216\n"
    "ds_read_b128 v[232:235], v205 offset:36864\n"
    "v_mfma_f32_16x16x32_bf16 v[44:47], v[172:175], a[228:231], v[44:47]\n"
    "s_waitcnt lgkmcnt(12)\n"
    "ds_read_b128 v[236:239], v206 offset:36864\n"
    "v_mfma_f32_16x16x32_bf16 v[40:43], v[176:179], a[208:211], v[40:43]\n"
    "ds_read_b128 v[168:171], v255 offset:10240\n"
    "v_mfma_f32_16x16x32_bf16 v[40:43], v[180:183], a[212:215], v[40:43]\n"
    "v_mfma_f32_16x16x32_bf16 v[44:47], v[176:179], a[232:235], v[44:47]\n"
    "ds_read_b128 v[172:175], v255 offset:11264\n"
    "v_mfma_f32_16x16x32_bf16 v[44:47], v[180:183], a[236:239], v[44:47]\n"
    "s_waitcnt lgkmcnt(10)\n"
    "s_add_u32 s78, s88, 0x3000\n"
    "v_cvt_f32_i32 v240, v240\n"
    "v_mfma_f32_16x16x32_bf16 v[56:59], v[184:187], a[192:195], 0\n"
    "s_add_u32 m0, s71, 0xc800\n"
    "ds_read_b128 v[176:179], v200\n"
    "v_cvt_f32_i32 v241, v241\n"
    "v_mfma_f32_16x16x32_bf16 v[56:59], v[188:191], a[196:199], v[56:59]\n"
    "buffer_load_dwordx4 v154, s[28:31], s78 offen nt lds\n"
    "v_mfma_f32_16x16x32_bf16 v[60:63], v[184:187], a[216:219], 0\n"
    "ds_read_b128 v[180:183], v200 offset:1024\n"
    "v_cvt_f32_i32 v242, v242\n"
    "v_mfma_f32_16x16x32_bf16 v[60:63], v[188:191], a[220:223], v[60:63]\n"
    "s_waitcnt lgkmcnt(6)\n"
    "v_cvt_f32_i32 v243, v243\n"
    "v_cvt_f32_i32 v244, v244\n"
    "v_mfma_f32_16x16x32_bf16 v[56:59], v[160:163], a[200:203], v[56:59]\n"
    "ds_read_b128 v[184:187], v200 offset:2048\n"
    "v_cvt_f32_i32 v245, v245\n"
    "v_mfma_f32_16x16x32_bf16 v[56:59], v[164:167], a[204:207], v[56:59]\n"
    "v_cvt_f32_i32 v246, v246\n"
    "v_mul_f32 v226, s75, v223\n"
    "v_mfma_f32_16x16x32_bf16 v[60:63], v[160:163], a[224:227], v[60:63]\n"
    "ds_read_b128 v[188:191], v200 offset:3072\n"
    "v_cvt_f32_i32 v247, v247\n"
    "v_mfma_f32_16x16x32_bf16 v[60:63], v[164:167], a[228:231], v[60:63]\n"
    "s_waitcnt lgkmcnt(4)\n"
    "v_fma_f32 v227, -v222, v226, v217\n"
    "v_mul_f32_dpp v240, v228, v240 row_newbcast:0 row_mask:0xf bank_mask:0xf\n"
    "v_mfma_f32_16x16x32_bf16 v[56:59], v[168:171], a[208:211], v[56:59]\n"
    "ds_read_b128 v[160:163], v200 offset:4096\n"
    "v_cvt_f32_i32 v232, v232\n"
    "v_mfma_f32_16x16x32_bf16 v[56:59], v[172:175], a[212:215], v[56:59]\n"
    "s_add_u32 s78, s88, 0x3000\n"
    "v_mul_f32_dpp v241, v228, v241 row_newbcast:1 row_mask:0xf bank_mask:0xf\n"
    "v_cvt_f32_i32 v233, v233\n"
    "v_mfma_f32_16x16x32_bf16 v[60:63], v[168:171], a[232:235], v[60:63]\n"
    "s_add_u32 m0, s71, 0xcc00\n"
    "ds_read_b128 v[164:167], v200 offset:5120\n"
    "v_mul_f32_dpp v242, v228, v242 row_newbcast:2 row_mask:0xf bank_mask:0xf\n"
    "v_mfma_f32_16x16x32_bf16 v[60:63], v[172:175], a[236:239], v[60:63]\n"
    "s_waitcnt lgkmcnt(4)\n"
    "buffer_load_dwordx4 v155, s[28:31], s78 offen nt lds\n"
    "v_mfma_f32_16x16x32_bf16 v[64:67], v[176:179], a[192:195], 0\n"
    "ds_read_b128 v[168:171], v200 offset:6144\n"
    "v_cvt_f32_i32 v234, v234\n"
    "v_mul_f32_dpp v243, v228, v243 row_newbcast:3 row_mask:0xf bank_mask:0xf\n"
    "v_cvt_f32_i32 v235, v235\n"
    "v_mfma_f32_16x16x32_bf16 v[64:67], v[180:183], a[196:199], v[64:67]\n"
    "v_mul_f32_dpp v244, v228, v244 row_newbcast:4 row_mask:0xf bank_mask:0xf\n"
    "v_cvt_f32_i32 v236, v236\n"
    "v_mul_f32_dpp v245, v228, v245 row_newbcast:5 row_mask:0xf bank_mask:0xf\n"
    "v_cvt_f32_i32 v237, v237\n"
    "v_mfma_f32_16x16x32_bf16 v[68:71], v[176:179], a[216:219], 0\n"
    "ds_read_b128 v[172:175], v200 offset:7168\n"
    "v_mul_f32_dpp v246, v228, v246 row_newbcast:6 row_mask:0xf bank_mask:0xf\n"
    "v_cvt_f32_i32 v238, v238\n"
    "v_mul_f32_dpp v247, v228, v247 row_newbcast:7 row_mask:0xf bank_mask:0xf\n"
    "v_mfma_f32_16x16x32_bf16 v[68:71], v[180:183], a[220:223], v[68:71]\n"
    "s_waitcnt lgkmcnt(4)\n"
    "v_cvt_f32_i32 v239, v239\n"
    "v_add_f32_dpp v240, v229, v240 row_newbcast:0 row_mask:0xf bank_mask:0xf\n"
    "v_mul_f32_dpp v232, v226, v232 row_newbcast:0 row_mask:0xf bank_mask:0xf\n"
    "v_add_f32_dpp v241, v229, v241 row_newbcast:1 row_mask:0xf bank_mask:0xf\n"
    "v_mfma_f32_16x16x32_bf16 v[64:67], v[184:187], a[200:203], v[64:67]\n"
    "ds_read_b128 v[176:179], v200 offset:8192\n"
    "v_mul_f32_dpp v233, v226, v233 row_newbcast:1 row_mask:0xf bank_mask:0xf\n"
    "v_add_f32_dpp v242, v229, v242 row_newbcast:2 row_mask:0xf bank_mask:0xf\n"
    "v_mul_f32_dpp v234, v226, v234 row_newbcast:2 row_mask:0xf bank_mask:0xf\n"
    "v_mfma_f32_16x16x32_bf16 v[64:67], v[188:191], a[204:207], v[64:67]\n"
    "v_add_f32_dpp v243, v229, v243 row_newbcast:3 row_mask:0xf bank_mask:0xf\n"
    "v_mul_f32_dpp v235, v226, v235 row_newbcast:3 row_mask:0xf bank_mask:0xf\n"
    "v_add_f32_dpp v244, v229, v244 row_newbcast:4 row_mask:0xf bank_mask:0xf\n"
    "v_mul_f32_dpp v236, v226, v236 row_newbcast:4 row_mask:0xf bank_mask:0xf\n"
    "v_mfma_f32_16x16x32_bf16 v[68:71], v[184:187], a[224:227], v[68:71]\n"
    "ds_read_b128 v[180:183], v200 offset:9216\n"
    "v_add_f32_dpp v245, v229, v245 row_newbcast:5 row_mask:0xf bank_mask:0xf\n"
    "v_mul_f32_dpp v237, v226, v237 row_newbcast:5 row_mask:0xf bank_mask:0xf\n"
    "v_add_f32_dpp v246, v229, v246 row_newbcast:6 row_mask:0xf bank_mask:0xf\n"
    "v_mul_f32_dpp v238, v226, v238 row_newbcast:6 row_mask:0xf bank_mask:0xf\n"
    "v_add_f32_dpp v247, v229, v247 row_newbcast:7 row_mask:0xf bank_mask:0xf\n"
    "v_mul_f32_dpp v239, v226, v239 row_newbcast:7 row_mask:0xf bank_mask:0xf\n"
    "v_cvt_pk_bf16_f32 v240, v240, v241\n"
    "v_add_f32_dpp v232, v227, v232 row_newbcast:0 row_mask:0xf bank_mask:0xf\n"
    "v_cvt_pk_bf16_f32 v241, v242, v243\n"
    "v_add_f32_dpp v233, v227, v233 row_newbcast:1 row_mask:0xf bank_mask:0xf\n"
    "v_cvt_pk_bf16_f32 v242, v244, v245\n"
    "v_add_f32_dpp v234, v227, v234 row_newbcast:2 row_mask:0xf bank_mask:0xf\n"
    "v_cvt_pk_bf16_f32 v243, v246, v247\n"
    "v_add_f32_dpp v235, v227, v235 row_newbcast:3 row_mask:0xf bank_mask:0xf\n"
    "v_add_f32_dpp v236, v227, v236 row_newbcast:4 row_mask:0xf bank_mask:0xf\n"
    "v_add_f32_dpp v237, v227, v237 row_newbcast:5 row_mask:0xf bank_mask:0xf\n"
    "v_add_f32_dpp v238, v227, v238 row_newbcast:6 row_mask:0xf bank_mask:0xf\n"
    "v_add_f32_dpp v239, v227, v239 row_newbcast:7 row_mask:0xf bank_mask:0xf\n"
    "v_cvt_pk_bf16_f32 v232, v232, v233\n"
    "v_cvt_pk_bf16_f32 v233, v234, v235\n"
    "v_cvt_pk_bf16_f32 v234, v236, v237\n"
    "v_cvt_pk_bf16_f32 v235, v238, v239\n"
    "v_mfma_f32_16x16x32_bf16 v[68:71], v[188:191], a[228:231], v[68:71]\n"
    "s_waitcnt lgkmcnt(4)\n"
    "s_add_u32 s78, s88, 0x3000\n"
    "v_max3_f32 v196, v40, v41, v42\n"
    "v_max3_f32 v196, v196, v43, v56\n"
    "v_max3_f32 v196, v196, v57, v58\n"
    "buffer_store_dwordx4 v[232:235], v211, s[36:39], s76 offen sc1\n"
    "v_mfma_f32_16x16x32_bf16 v[64:67], v[160:163], a[208:211], v[64:67]\n"
    "s_add_u32 m0, s71, 0xd000\n"
    "ds_read_b128 v[184:187], v200 offset:10240\n"
    "v_max_f32 v196, v196, v59\n"
    "v_max3_f32 v198, v44, v45, v46\n"
    "v_mov_b32 v197, v196\n"
    "v_mfma_f32_16x16x32_bf16 v[64:67], v[164:167], a[212:215], v[64:67]\n"
    "buffer_load_dwordx4 v156, s[28:31], s78 offen nt lds\n"
    "v_max3_f32 v198, v198, v47, v60\n"
    "v_permlane32_swap_b32 v196, v197\n"
    "v_mfma_f32_16x16x32_bf16 v[68:71], v[160:163], a[232:235], v[68:71]\n"
    "ds_read_b128 v[188:191], v200 offset:11264\n"
    "v_max3_f32 v198, v198, v61, v62\n"
    "v_max_f32 v196, v196, v197\n"
    "v_max_f32 v198, v198, v63\n"
    "v_mfma_f32_16x16x32_bf16 v[68:71], v[164:167], a[236:239], v[68:71]\n"
    "s_waitcnt lgkmcnt(4)\n"
    "v_mov_b32 v197, v196\n"
    "v_mov_b32 v199, v198\n"
    "s_nop 0\n"
    "v_permlane16_swap_b32 v196, v197\n"
    "v_permlane32_swap_b32 v198, v199\n"
    "v_mfma_f32_16x16x32_bf16 v[80:83], v[168:171], a[192:195], 0\n"
    "ds_read_b128 v[160:163], v200 offset:12288\n"
    "v_max_f32 v196, v196, v197\n"
    "v_max_f32 v198, v198, v199\n"
    "v_add_f32 v196, 0x42640000, v196\n"
    "v_mfma_f32_16x16x32_bf16 v[80:83], v[172:175], a[196:199], v[80:83]\n"
    "buffer_store_dwordx4 v[240:243], v212, s[40:43], s77 offen sc1\n"
    "v_mov_b32 v199, v198\n"
    "v_sub_f32 v96, 0, v196\n"
    "s_nop 0\n"
    "v_permlane16_swap_b32 v198, v199\n"
    "v_sub_f32 v97, 0, v196\n"
    "v_max_f32 v198, v198, v199\n"
    "v_mfma_f32_16x16x32_bf16 v[84:87], v[168:171], a[216:219], 0\n"
    "ds_read_b128 v[164:167], v200 offset:13312\n"
    "v_sub_f32 v98, 0, v196\n"
    "v_add_f32 v198, 0x42640000, v198\n"
    "v_sub_f32 v99, 0, v196\n"
    "v_mfma_f32_16x16x32_bf16 v[84:87], v[172:175], a[220:223], v[84:87]\n"
    "s_waitcnt lgkmcnt(4)\n"
    "v_sub_f32 v100, 0, v198\n"
    "v_add_f32 v40, v40, v96\n"
    "v_sub_f32 v101, 0, v198\n"
    "v_add_f32 v41, v41, v96\n"
    "v_mfma_f32_16x16x32_bf16 v[80:83], v[176:179], a[200:203], v[80:83]\n"
    "ds_read_b128 v[168:171], v200 offset:14336\n"
    "v_sub_f32 v102, 0, v198\n"
    "v_add_f32 v42, v42, v96\n"
    "v_sub_f32 v103, 0, v198\n"
    "v_mfma_f32_16x16x32_bf16 v[80:83], v[180:183], a[204:207], v[80:83]\n"
    "s_add_u32 s78, s88, 0x3000\n"
    "v_add_f32 v43, v43, v96\n"
    "v_add_f32 v44, v44, v100\n"
    "v_add_f32 v56, v56, v96\n"
    "v_add_f32 v45, v45, v100\n"
    "v_mfma_f32_16x16x32_bf16 v[84:87], v[176:179], a[224:227], v[84:87]\n"
    "s_add_u32 m0, s71, 0xd400\n"
    "ds_read_b128 v[172:175], v200 offset:15360\n"
    "v_add_f32 v57, v57, v96\n"
    "v_add_f32 v46, v46, v100\n"
    "v_add_f32 v58, v58, v96\n"
    "v_mfma_f32_16x16x32_bf16 v[84:87], v[180:183], a[228:231], v[84:87]\n"
    "s_waitcnt lgkmcnt(4)\n"
    "buffer_load_dwordx4 v157, s[28:31], s78 offen nt lds\n"
    "v_add_f32 v47, v47, v100\n"
    "v_add_f32 v59, v59, v96\n"
    "v_mfma_f32_16x16x32_bf16 v[80:83], v[184:187], a[208:211], v[80:83]\n"
    "ds_read_b128 v[176:179], v200 offset:16384\n"
    "v_add_f32 v60, v60, v100\n"
    "v_exp_f32 v40, v40\n"
    "v_mfma_f32_16x16x32_bf16 v[80:83], v[188:191], a[212:215], v[80:83]\n"
    "v_add_f32 v61, v61, v100\n"
    "v_add_f32 v62, v62, v100\n"
    "v_exp_f32 v41, v41\n"
    "v_mfma_f32_16x16x32_bf16 v[84:87], v[184:187], a[232:235], v[84:87]\n"
    "ds_read_b128 v[180:183], v200 offset:17408\n"
    "v_add_f32 v63, v63, v100\n"
    "v_exp_f32 v44, v44\n"
    "v_mfma_f32_16x16x32_bf16 v[84:87], v[188:191], a[236:239], v[84:87]\n"
    "s_waitcnt lgkmcnt(4)\n"
    "v_exp_f32 v42, v42\n"
    "v_exp_f32 v45, v45\n"
    "v_mfma_f32_16x16x32_bf16 v[32:35], v[160:163], a[192:195], v[96:99]\n"
    "ds_read_b128 v[184:187], v200 offset:18432\n"
    "v_exp_f32 v43, v43\n"
    "v_mfma_f32_16x16x32_bf16 v[32:35], v[164:167], a[196:199], v[32:35]\n"
    "v_exp_f32 v46, v46\n"
    "v_exp_f32 v56, v56\n"
    "v_mfma_f32_16x16x32_bf16 v[36:39], v[160:163], a[216:219], v[100:103]\n"
    "ds_read_b128 v[188:191], v200 offset:19456\n"
    "v_exp_f32 v47, v47\n"
    "v_mfma_f32_16x16x32_bf16 v[36:39], v[164:167], a[220:223], v[36:39]\n"
    "s_waitcnt lgkmcnt(4)\n"
    "s_add_u32 s78, s88, 0x4800\n"
    "v_exp_f32 v57, v57\n"
    "v_mfma_f32_16x16x32_bf16 v[32:35], v[168:171], a[200:203], v[32:35]\n"
    "s_add_u32 m0, s71, 0x6000\n"
    "ds_read_b128 v[160:163], v200 offset:20480\n"
    "v_exp_f32 v60, v60\n"
    "v_mfma_f32_16x16x32_bf16 v[32:35], v[172:175], a[204:207], v[32:35]\n"
    "buffer_load_dwordx4 v152, s[28:31], s78 offen nt lds\n"
    "v_exp_f32 v58, v58\n"
    "v_mfma_f32_16x16x32_bf16 v[36:39], v[168:171], a[224:227], v[36:39]\n"
    "ds_read_b128 v[164:167], v200 offset:21504\n"
    "v_exp_f32 v61, v61\n"
    "v_mfma_f32_16x16x32_bf16 v[36:39], v[172:175], a[228:231], v[36:39]\n"
    "s_waitcnt lgkmcnt(4)\n"
    "v_exp_f32 v59, v59\n"
    "v_exp_f32 v62, v62\n"
    "v_mfma_f32_16x16x32_bf16 v[32:35], v[176:179], a[208:211], v[32:35]\n"
    "ds_read_b128 v[168:171], v200 offset:22528\n"
    "v_cvt_pk_bf16_f32 v128, v40, v41\n"
    "v_cvt_pk_bf16_f32 v129, v42, v43\n"
    "v_cvt_pk_bf16_f32 v130, v56, v57\n"
    "v_mfma_f32_16x16x32_bf16 v[32:35], v[180:183], a[212:215], v[32:35]\n"
    "v_exp_f32 v63, v63\n"
    "v_cvt_pk_bf16_f32 v131, v58, v59\n"
    "v_cvt_pk_bf16_f32 v132, v44, v45\n"
    "v_mfma_f32_16x16x32_bf16 v[36:39], v[176:179], a[232:235], v[36:39]\n"
    "ds_read_b128 v[172:175], v200 offset:23552\n"
    "v_add_f32 v64, v64, v96\n"
    "v_cvt_pk_bf16_f32 v133, v46, v47\n"
    "v_add_f32 v65, v65, v96\n"
    "v_mfma_f32_16x16x32_bf16 v[36:39], v[180:183], a[236:239], v[36:39]\n"
    "s_waitcnt lgkmcnt(4)\n"
    "v_cvt_pk_bf16_f32 v134, v60, v61\n"
    "v_add_f32 v66, v66, v96\n"
    "s_waitcnt vmcnt(11)\n"
    "s_barrier\n"
    "v_mfma_f32_16x16x32_bf16 v[48:51], v[184:187], a[192:195], v[96:99]\n"
    "ds_read_b128 v[176:179], v201 offset:24576\n"
    "v_cvt_pk_bf16_f32 v135, v62, v63\n"
    "v_add_f32 v67, v67, v96\n"
    "v_add_f32 v68, v68, v100\n"
    "v_mfma_f32_16x16x32_bf16 v[48:51], v[188:191], a[196:199], v[48:51]\n"
    "s_add_u32 s78, s88, 0x4800\n"
    "v_add_f32 v80, v80, v96\n"
    "v_add_f32 v69, v69, v100\n"
    "v_add_f32 v81, v81, v96\n"
    "v_add_f32 v70, v70, v100\n"
    "v_mfma_f32_16x16x32_bf16 v[52:55], v[184:187], a[216:219], v[100:103]\n"
    "s_add_u32 m0, s71, 0x6400\n"
    "ds_read_b128 v[180:183], v201 offset:25600\n"
    "v_add_f32 v82, v82, v96\n"
    "v_add_f32 v71, v71, v100\n"
    "v_add_f32 v83, v83, v96\n"
    "v_mfma_f32_16x16x32_bf16 v[52:55], v[188:191], a[220:223], v[52:55]\n"
    "s_waitcnt lgkmcnt(4)\n"
    "buffer_load_dwordx4 v153, s[28:31], s78 offen nt lds\n"
    "v_add_f32 v84, v84, v100\n"
    "v_add_f32 v85, v85, v100\n"
    "v_mfma_f32_16x16x32_bf16 v[48:51], v[160:163], a[200:203], v[48:51]\n"
    "ds_read_b128 v[184:187], v201 offset:26624\n"
    "v_exp_f32 v64, v64\n"
    "v_add_f32 v86, v86, v100\n"
    "v_mfma_f32_16x16x32_bf16 v[48:51], v[164:167], a[204:207], v[48:51]\n"
    "v_exp_f32 v65, v65\n"
    "v_add_f32 v87, v87, v100\n"
    "ds_read_b64 v[228:229], v210 offset:192\n"
    "v_mfma_f32_16x16x32_bf16 v[52:55], v[160:163], a[224:227], v[52:55]\n"
    "ds_read_b128 v[188:191], v201 offset:27648\n"
    "v_exp_f32 v68, v68\n"
    "ds_read2st64_b32 v[240:241], v207 offset0:96 offset1:99\n"
    "v_mfma_f32_16x16x32_bf16 v[52:55], v[164:167], a[228:231], v[52:55]\n"
    "s_waitcnt lgkmcnt(6)\n"
    "v_exp_f32 v66, v66\n"
    "v_exp_f32 v69, v69\n"
    "v_mfma_f32_16x16x32_bf16 v[48:51], v[168:171], a[208:211], v[48:51]\n"
    "ds_read_b128 v[160:163], v201 offset:28672\n"
    "v_exp_f32 v67, v67\n"
    "ds_read2st64_b32 v[242:243], v207 offset0:102 offset1:105\n"
    "v_mfma_f32_16x16x32_bf16 v[48:51], v[172:175], a[212:215], v[48:51]\n"
    "v_exp_f32 v70, v70\n"
    "v_exp_f32 v80, v80\n"
    "v_mfma_f32_16x16x32_bf16 v[52:55], v[168:171], a[232:235], v[52:55]\n"
    "ds_read_b128 v[164:167], v201 offset:29696\n"
    "v_exp_f32 v71, v71\n"
    "ds_read2st64_b32 v[244:245], v207 offset0:108 offset1:111\n"
    "v_exp_f32 v81, v81\n"
    "v_exp_f32 v82, v82\n"
    "v_exp_f32 v83, v83\n"
    "v_cvt_pk_bf16_f32 v144, v64, v65\n"
    "v_cvt_pk_bf16_f32 v145, v66, v67\n"
    "v_cvt_pk_bf16_f32 v146, v80, v81\n"
    "v_cvt_pk_bf16_f32 v147, v82, v83\n"
    "v_mfma_f32_16x16x32_bf16 v[52:55], v[172:175], a[236:239], v[52:55]\n"
    "s_waitcnt lgkmcnt(8)\n"
    "s_add_u32 s78, s88, 0x4800\n"
    "v_exp_f32 v84, v84\n"
    "ds_read2st64_b32 v[246:247], v207 offset0:114 offset1:117\n"
    "v_mfma_f32_16x16x32_bf16 a[0:3], v[176:179], v[128:131], 0\n"
    "s_add_u32 m0, s71, 0x6800\n"
    "ds_read_b128 v[168:171], v201 offset:30720\n"
    "ds_read_u16_d16_hi v223, v209 offset:6912\n"
    "v_exp_f32 v85, v85\n"
    "v_exp_f32 v86, v86\n"
    "v_exp_f32 v87, v87\n"
    "v_cvt_pk_bf16_f32 v148, v68, v69\n"
    "v_cvt_pk_bf16_f32 v149, v70, v71\n"
    "v_cvt_pk_bf16_f32 v150, v84, v85\n"
    "v_cvt_pk_bf16_f32 v151, v86, v87\n"
    "v_mfma_f32_16x16x32_bf16 a[0:3], v[180:183], v[144:147], a[0:3]\n"
    "buffer_load_dwordx4 v154, s[28:31], s78 offen nt lds\n"
    "v_mfma_f32_16x16x32_bf16 a[4:7], v[176:179], v[132:135], 0\n"
    "ds_read_b128 v[172:175], v201 offset:31744\n"
    "ds_read_u16_d16_hi v222, v209 offset:768\n"
    "v_mfma_f32_16x16x32_bf16 a[4:7], v[180:183], v[148:151], a[4:7]\n"
    "s_waitcnt lgkmcnt(10)\n"
    "v_exp_f32 v32, v32\n"
    "v_mfma_f32_16x16x32_bf16 a[16:19], v[184:187], v[128:131], 0\n"
    "ds_read_b128 v[176:179], v201 offset:32768\n"
    "ds_read_b128 v[232:235], v205 offset:24576\n"
    "v_mfma_f32_16x16x32_bf16 a[16:19], v[188:191], v[144:147], a[16:19]\n"
    "ds_read_b128 v[236:239], v206 offset:24576\n"
    "v_mfma_f32_16x16x32_bf16 a[20:23], v[184:187], v[132:135], 0\n"
    "ds_read_b128 v[180:183], v201 offset:33792\n"
    "s_waitcnt lgkmcnt(13)\n"
    "v_cvt_f32_i32 v240, v240\n"
    "v_mfma_f32_16x16x32_bf16 a[20:23], v[188:191], v[148:151], a[20:23]\n"
    "s_waitcnt lgkmcnt(10)\n"
    "v_exp_f32 v33, v33\n"
    "v_mfma_f32_16x16x32_bf16 a[32:35], v[160:163], v[128:131], 0\n"
    "ds_read_b128 v[184:187], v201 offset:34816\n"
    "v_cvt_f32_i32 v241, v241\n"
    "v_mfma_f32_16x16x32_bf16 a[32:35], v[164:167], v[144:147], a[32:35]\n"
    "s_add_u32 s78, s88, 0x4800\n"
    "v_exp_f32 v34, v34\n"
    "v_mfma_f32_16x16x32_bf16 a[36:39], v[160:163], v[132:135], 0\n"
    "s_add_u32 m0, s71, 0x6c00\n"
    "ds_read_b128 v[188:191], v201 offset:35840\n"
    "v_cvt_f32_i32 v242, v242\n"
    "v_mfma_f32_16x16x32_bf16 a[36:39], v[164:167], v[148:151], a[36:39]\n"
    "buffer_load_dwordx4 v155, s[28:31], s78 offen nt lds\n"
    "v_mfma_f32_16x16x32_bf16 v[112:115], v[192:195], v[128:131], 0\n"
    "v_exp_f32 v35, v35\n"
    "v_mfma_f32_16x16x32_bf16 v[112:115], v[192:195], v[144:147], v[112:115]\n"
    "s_waitcnt lgkmcnt(7)\n"
    "v_exp_f32 v36, v36\n"
    "v_mfma_f32_16x16x32_bf16 a[48:51], v[168:171], v[128:131], 0\n"
    "ds_read_b128 v[160:163], v201 offset:36864\n"
    "v_cvt_f32_i32 v243, v243\n"
    "v_mfma_f32_16x16x32_bf16 a[48:51], v[172:175], v[144:147], a[48:51]\n"
    "v_cvt_f32_i32 v244, v244\n"
    "v_cvt_f32_i32 v245, v245\n"
    "v_mfma_f32_16x16x32_bf16 a[52:55], v[168:171], v[132:135], 0\n"
    "ds_read_b128 v[164:167], v201 offset:37888\n"
    "v_cvt_f32_i32 v246, v246\n"
    "v_mfma_f32_16x16x32_bf16 a[52:55], v[172:175], v[148:151], a[52:55]\n"
    "v_exp_f32 v37, v37\n"
    "v_mfma_f32_16x16x32_bf16 v[116:119], v[192:195], v[132:135], 0\n"
    "v_exp_f32 v38, v38\n"
    "v_mfma_f32_16x16x32_bf16 v[116:119], v[192:195], v[148:151], v[116:119]\n"
    "s_waitcnt lgkmcnt(4)\n"
    "s_add_u32 s78, s88, 0x4800\n"
    "v_mul_f32 v226, s75, v223\n"
    "v_mfma_f32_16x16x32_bf16 a[64:67], v[176:179], v[128:131], 0\n"
    "s_add_u32 m0, s71, 0x7000\n"
    "ds_read_b128 v[168:171], v201 offset:38912\n"
    "v_cvt_f32_i32 v247, v247\n"
    "v_mfma_f32_16x16x32_bf16 a[64:67], v[180:183], v[144:147], a[64:67]\n"
    "buffer_load_dwordx4 v156, s[28:31], s78 offen nt lds\n"
    "v_mfma_f32_16x16x32_bf16 a[68:71], v[176:179], v[132:135], 0\n"
    "ds_read_b128 v[172:175], v201 offset:39936\n"
    "v_fma_f32 v227, -v222, v226, v217\n"
    "v_mfma_f32_16x16x32_bf16 a[68:71], v[180:183], v[148:151], a[68:71]\n"
    "s_waitcnt lgkmcnt(4)\n"
    "v_mul_f32_dpp v240, v228, v240 row_newbcast:0 row_mask:0xf bank_mask:0xf\n"
    "v_cvt_f32_i32 v232, v232\n"
    "v_mfma_f32_16x16x32_bf16 a[80:83], v[184:187], v[128:131], 0\n"
    "ds_read_b128 v[176:179], v201 offset:40960\n"
    "v_mul_f32_dpp v241, v228, v241 row_newbcast:1 row_mask:0xf bank_mask:0xf\n"
    "v_mfma_f32_16x16x32_bf16 a[80:83], v[188:191], v[144:147], a[80:83]\n"
    "v_exp_f32 v39, v39\n"
    "v_mfma_f32_16x16x32_bf16 a[84:87], v[184:187], v[132:135], 0\n"
    "ds_read_b128 v[180:183], v201 offset:41984\n"
    "v_cvt_f32_i32 v233, v233\n"
    "v_mfma_f32_16x16x32_bf16 a[84:87], v[188:191], v[148:151], a[84:87]\n"
    "s_waitcnt lgkmcnt(4)\n"
    "v_mul_f32_dpp v242, v228, v242 row_newbcast:2 row_mask:0xf bank_mask:0xf\n"
    "v_cvt_f32_i32 v234, v234\n"
    "v_mfma_f32_16x16x32_bf16 a[96:99], v[160:163], v[128:131], 0\n"
    "ds_read_b128 v[184:187], v201 offset:43008\n"
    "v_mul_f32_dpp v243, v228, v243 row_newbcast:3 row_mask:0xf bank_mask:0xf\n"
    "v_mfma_f32_16x16x32_bf16 a[96:99], v[164:167], v[144:147], a[96:99]\n"
    "s_add_u32 s78, s88, 0x4800\n"
    "v_exp_f32 v48, v48\n"
    "v_mfma_f32_16x16x32_bf16 a[100:103], v[160:163], v[132:135], 0\n"
    "s_add_u32 m0, s71, 0x7400\n"
    "ds_read_b128 v[188:191], v201 offset:44032\n"
    "v_cvt_f32_i32 v235, v235\n"
    "v_mfma_f32_16x16x32_bf16 a[100:103], v[164:167], v[148:151], a[100:103]\n"
    "s_waitcnt lgkmcnt(4)\n"
    "buffer_load_dwordx4 v157, s[28:31], s78 offen nt lds\n"
    "v_mfma_f32_16x16x32_bf16 a[112:115], v[168:171], v[128:131], 0\n"
    "ds_read_b128 v[160:163], v201 offset:45056\n"
    "v_mul_f32_dpp v244, v228, v244 row_newbcast:4 row_mask:0xf bank_mask:0xf\n"
    "v_mfma_f32_16x16x32_bf16 a[112:115], v[172:175], v[144:147], a[112:115]\n"
    "v_exp_f32 v49, v49\n"
    "v_mfma_f32_16x16x32_bf16 a[116:119], v[168:171], v[132:135], 0\n"
    "ds_read_b128 v[164:167], v201 offset:46080\n"
    "v_cvt_f32_i32 v236, v236\n"
    "v_mfma_f32_16x16x32_bf16 a[116:119], v[172:175], v[148:151], a[116:119]\n"
    "s_waitcnt lgkmcnt(4)\n"
    "v_mul_f32_dpp v245, v228, v245 row_newbcast:5 row_mask:0xf bank_mask:0xf\n"
    "v_cvt_f32_i32 v237, v237\n"
    "v_mfma_f32_16x16x32_bf16 a[128:131], v[176:179], v[128:131], 0\n"
    "ds_read_b128 v[168:171], v201 offset:47104\n"
    "v_mul_f32_dpp v246, v228, v246 row_newbcast:6 row_mask:0xf bank_mask:0xf\n"
    "v_mfma_f32_16x16x32_bf16 a[128:131], v[180:183], v[144:147], a[128:131]\n"
    "v_exp_f32 v50, v50\n"
    "v_mfma_f32_16x16x32_bf16 a[132:135], v[176:179], v[132:135], 0\n"
    "ds_read_b128 v[172:175], v201 offset:48128\n"
    "v_cvt_f32_i32 v238, v238\n"
    "v_mfma_f32_16x16x32_bf16 a[132:135], v[180:183], v[148:151], a[132:135]\n"
    "s_waitcnt lgkmcnt(4)\n"
    "v_mul_f32_dpp v247, v228, v247 row_newbcast:7 row_mask:0xf bank_mask:0xf\n"
    "v_cvt_f32_i32 v239, v239\n"
    "v_mfma_f32_16x16x32_bf16 a[144:147], v[184:187], v[128:131], 0\n"
    "v_exp_f32 v51, v51\n"
    "v_mfma_f32_16x16x32_bf16 a[144:147], v[188:191], v[144:147], a[144:147]\n"
    "v_add_f32_dpp v240, v229, v240 row_newbcast:0 row_mask:0xf bank_mask:0xf\n"
    "v_mul_f32_dpp v232, v226, v232 row_newbcast:0 row_mask:0xf bank_mask:0xf\n"
    "v_mfma_f32_16x16x32_bf16 a[148:151], v[184:187], v[132:135], 0\n"
    "v_add_f32_dpp v241, v229, v241 row_newbcast:1 row_mask:0xf bank_mask:0xf\n"
    "v_mul_f32_dpp v233, v226, v233 row_newbcast:1 row_mask:0xf bank_mask:0xf\n"
    "v_mfma_f32_16x16x32_bf16 a[148:151], v[188:191], v[148:151], a[148:151]\n"
    "s_waitcnt lgkmcnt(2)\n"
    "v_exp_f32 v52, v52\n"
    "v_mfma_f32_16x16x32_bf16 a[160:163], v[160:163], v[128:131], 0\n"
    "v_add_f32_dpp v242, v229, v242 row_newbcast:2 row_mask:0xf bank_mask:0xf\n"
    "v_mul_f32_dpp v234, v226, v234 row_newbcast:2 row_mask:0xf bank_mask:0xf\n"
    "v_mfma_f32_16x16x32_bf16 a[160:163], v[164:167], v[144:147], a[160:163]\n"
    "v_add_f32_dpp v243, v229, v243 row_newbcast:3 row_mask:0xf bank_mask:0xf\n"
    "v_mul_f32_dpp v235, v226, v235 row_newbcast:3 row_mask:0xf bank_mask:0xf\n"
    "v_mfma_f32_16x16x32_bf16 a[164:167], v[160:163], v[132:135], 0\n"
    "v_exp_f32 v53, v53\n"
    "v_mfma_f32_16x16x32_bf16 a[164:167], v[164:167], v[148:151], a[164:167]\n"
    "s_waitcnt lgkmcnt(0)\n"
    "v_add_f32_dpp v244, v229, v244 row_newbcast:4 row_mask:0xf bank_mask:0xf\n"
    "v_mul_f32_dpp v236, v226, v236 row_newbcast:4 row_mask:0xf bank_mask:0xf\n"
    "v_mfma_f32_16x16x32_bf16 a[176:179], v[168:171], v[128:131], 0\n"
    "v_add_f32_dpp v245, v229, v245 row_newbcast:5 row_mask:0xf bank_mask:0xf\n"
    "v_mul_f32_dpp v237, v226, v237 row_newbcast:5 row_mask:0xf bank_mask:0xf\n"
    "v_mfma_f32_16x16x32_bf16 a[176:179], v[172:175], v[144:147], a[176:179]\n"
    "v_exp_f32 v54, v54\n"
    "v_mfma_f32_16x16x32_bf16 a[180:183], v[168:171], v[132:135], 0\n"
    "v_add_f32_dpp v246, v229, v246 row_newbcast:6 row_mask:0xf bank_mask:0xf\n"
    "v_mul_f32_dpp v238, v226, v238 row_newbcast:6 row_mask:0xf bank_mask:0xf\n"
    "v_add_f32_dpp v247, v229, v247 row_newbcast:7 row_mask:0xf bank_mask:0xf\n"
    "v_exp_f32 v55, v55\n"
    "v_mul_f32_dpp v239, v226, v239 row_newbcast:7 row_mask:0xf bank_mask:0xf\n"
    "v_cvt_pk_bf16_f32 v240, v240, v241\n"
    "v_add_f32_dpp v232, v227, v232 row_newbcast:0 row_mask:0xf bank_mask:0xf\n"
    "v_cvt_pk_bf16_f32 v241, v242, v243\n"
    "v_add_f32_dpp v233, v227, v233 row_newbcast:1 row_mask:0xf bank_mask:0xf\n"
    "v_cvt_pk_bf16_f32 v242, v244, v245\n"
    "v_add_f32_dpp v234, v227, v234 row_newbcast:2 row_mask:0xf bank_mask:0xf\n"
    "v_cvt_pk_bf16_f32 v243, v246, v247\n"
    "v_add_f32_dpp v235, v227, v235 row_newbcast:3 row_mask:0xf bank_mask:0xf\n"
    "v_add_f32_dpp v236, v227, v236 row_newbcast:4 row_mask:0xf bank_mask:0xf\n"
    "v_add_f32_dpp v237, v227, v237 row_newbcast:5 row_mask:0xf bank_mask:0xf\n"
    "v_add_f32_dpp v238, v227, v238 row_newbcast:6 row_mask:0xf bank_mask:0xf\n"
    "v_add_f32_dpp v239, v227, v239 row_newbcast:7 row_mask:0xf bank_mask:0xf\n"
    "v_cvt_pk_bf16_f32 v232, v232, v233\n"
    "v_cvt_pk_bf16_f32 v233, v234, v235\n"
    "v_cvt_pk_bf16_f32 v234, v236, v237\n"
    "v_cvt_pk_bf16_f32 v235, v238, v239\n"
    "v_mfma_f32_16x16x32_bf16 a[180:183], v[172:175], v[148:151], a[180:183]\n"
    "v_add_f32 v113, v113, v32\n"
    "v_add_f32 v114, v114, v33\n"
    "v_add_f32 v113, v113, v34\n"
    "v_add_f32 v114, v114, v35\n"
    "v_add_f32 v117, v117, v36\n"
    "v_add_f32 v118, v118, v37\n"
    "v_add_f32 v117, v117, v38\n"
    "v_add_f32 v118, v118, v39\n"
    "v_add_f32 v113, v113, v48\n"
    "v_add_f32 v114, v114, v49\n"
    "v_add_f32 v113, v113, v50\n"
    "v_add_f32 v114, v114, v51\n"
    "v_add_f32 v117, v117, v52\n"
    "buffer_store_dwordx4 v[232:235], v211, s[36:39], s62 offen sc1\n"
    "buffer_store_dwordx4 v[240:243], v212, s[40:43], s63 offen sc1\n"
    "v_add_f32 v118, v118, v53\n"
    "v_add_f32 v117, v117, v54\n"
    "v_add_f32 v118, v118, v55\n"
    "s_waitcnt vmcnt(8)\n"
    "ds_read_b128 a[240:243], v158 offset:49152\n"
    "ds_read_b128 a[244:247], v159 offset:49152\n"
    "ds_read_b128 a[248:251], v158 offset:49280\n"
    "ds_read_b128 a[252:255], v159 offset:49280\n"
    "ds_read_b128 v[0:3], v158 offset:49408\n"
    "ds_read_b128 v[4:7], v159 offset:49408\n"
    "v_cmp_ne_u32 s[80:81], s67, v219\n"
    "v_cmp_gt_u32 vcc, s66, v218\n"
    "s_or_b64 vcc, vcc, s[80:81]\n"
    "s_cbranch_vccnz LSPRE1\n"
    "LSPB1:\n"
    "v_mov_b32 v220, 4\n"
    "s_add_u32 s66, s66, s74\n"
    "s_waitcnt lgkmcnt(0)\n"
    "s_barrier\n"
    "buffer_store_dwordx2 v[220:221], v214, s[44:47], 0 offen sc1\n"
    "ds_read_b128 v[160:163], v255\n"
    "ds_read_b128 v[164:167], v255 offset:1024\n"
    "ds_read_b128 v[168:171], v255 offset:2048\n"
    "ds_read_b128 v[172:175], v255 offset:3072\n"
    "ds_read_b128 v[176:179], v255 offset:4096\n"
    "ds_read_b128 v[180:183], v255 offset:5120\n"
    "s_waitcnt lgkmcnt(4)\n"
    "v_mfma_f32_16x16x32_bf16 v[44:47], v[160:163], a[240:243], 0\n"
    "ds_read_b128 v[184:187], v255 offset:6144\n"
    "v_mfma_f32_16x16x32_bf16 v[44:47], v[164:167], a[244:247], v[44:47]\n"
    "s_waitcnt lgkmcnt(3)\n"
    "ds_read_b128 v[188:191], v255 offset:7168\n"
    "v_mfma_f32_16x16x32_bf16 v[44:47], v[168:171], a[248:251], v[44:47]\n"
    "s_add_u32 m0, s71, 0xc000\n"
    "ds_read_b128 v[160:163], v255 offset:8192\n"
    "v_mfma_f32_16x16x32_bf16 v[44:47], v[172:175], a[252:255], v[44:47]\n"
    "s_waitcnt lgkmcnt(3)\n"
    "ds_read_b128 v[164:167], v255 offset:9216\n"
    "buffer_load_dwordx4 v200, s[40:43], s61 offen sc1 lds\n"
    "v_mfma_f32_16x16x32_bf16 v[44:47], v[176:179], v[0:3], v[44:47]\n"
    "ds_read_b128 v[168:171], v255 offset:10240\n"
    "v_mfma_f32_16x16x32_bf16 v[44:47], v[180:183], v[4:7], v[44:47]\n"
    "s_waitcnt lgkmcnt(3)\n"
    "ds_read_b128 v[172:175], v255 offset:11264\n"
    "v_mfma_f32_16x16x32_bf16 v[60:63], v[184:187], a[240:243], 0\n"
    "ds_read_b128 v[176:179], v200\n"
    "v_mfma_f32_16x16x32_bf16 v[60:63], v[188:191], a[244:247], v[60:63]\n"
    "s_waitcnt lgkmcnt(3)\n"
    "ds_read_b128 v[180:183], v200 offset:1024\n"
    "v_mfma_f32_16x16x32_bf16 v[60:63], v[160:163], a[248:251], v[60:63]\n"
    "ds_read_b128 v[184:187], v200 offset:2048\n"
    "buffer_load_dwordx4 v200, s[40:43], s61 offen offset:1024 sc1 lds\n"
    "v_mfma_f32_16x16x32_bf16 v[60:63], v[164:167], a[252:255], v[60:63]\n"
    "s_waitcnt lgkmcnt(3)\n"
    "ds_read_b128 v[188:191], v200 offset:3072\n"
    "v_mfma_f32_16x16x32_bf16 v[60:63], v[168:171], v[0:3], v[60:63]\n"
    "ds_read_b128 v[160:163], v200 offset:4096\n"
    "v_mfma_f32_16x16x32_bf16 v[60:63], v[172:175], v[4:7], v[60:63]\n"
    "s_waitcnt lgkmcnt(3)\n"
    "ds_read_b128 v[164:167], v200 offset:5120\n"
    "v_mfma_f32_16x16x32_bf16 v[72:75], v[176:179], a[240:243], 0\n"
    "ds_read_b128 v[168:171], v200 offset:6144\n"
    "v_mfma_f32_16x16x32_bf16 v[72:75], v[180:183], a[244:247], v[72:75]\n"
    "s_waitcnt lgkmcnt(3)\n"
    "ds_read_b128 v[172:175], v200 offset:7168\n"
    "buffer_load_dwordx4 v200, s[40:43], s61 offen offset:2048 sc1 lds\n"
    "v_mfma_f32_16x16x32_bf16 v[72:75], v[184:187], a[248:251], v[72:75]\n"
    "ds_read_b128 v[176:179], v200 offset:8192\n"
    "v_mfma_f32_16x16x32_bf16 v[72:75], v[188:191], a[252:255], v[72:75]\n"
    "s_waitcnt lgkmcnt(3)\n"
    "ds_read_b128 v[180:183], v200 offset:9216\n"
    "v_mfma_f32_16x16x32_bf16 v[72:75], v[160:163], v[0:3], v[72:75]\n"
    "ds_read_b128 v[184:187], v200 offset:10240\n"
    "v_mfma_f32_16x16x32_bf16 v[72:75], v[164:167], v[4:7], v[72:75]\n"
    "s_waitcnt lgkmcnt(3)\n"
    "ds_read_b128 v[188:191], v200 offset:11264\n"
    "v_max3_f32 v196, v44, v45, v46\n"
    "v_max3_f32 v196, v196, v47, v60\n"
    "v_max3_f32 v196, v196, v61, v62\n"
    "v_mfma_f32_16x16x32_bf16 v[88:91], v[168:171], a[240:243], 0\n"
    "ds_read_b128 v[160:163], v200 offset:12288\n"
    "buffer_load_dwordx4 v200, s[40:43], s61 offen offset:3072 sc1 lds\n"
    "v_max_f32 v196, v196, v63\n"
    "v_mfma_f32_16x16x32_bf16 v[88:91], v[172:175], a[244:247], v[88:91]\n"
    "s_waitcnt lgkmcnt(3)\n"
    "ds_read_b128 v[164:167], v200 offset:13312\n"
    "v_mov_b32 v197, v196\n"
    "s_nop 1\n"
    "v_permlane32_swap_b32 v196, v197\n"
    "v_max_f32 v196, v196, v197\n"
    "v_mfma_f32_16x16x32_bf16 v[88:91], v[176:179], a[248:251], v[88:91]\n"
    "ds_read_b128 v[168:171], v200 offset:14336\n"
    "v_mov_b32 v197, v196\n"
    "s_nop 1\n"
    "v_permlane16_swap_b32 v196, v197\n"
    "v_max_f32 v196, v196, v197\n"
    "v_mfma_f32_16x16x32_bf16 v[88:91], v[180:183], a[252:255], v[88:91]\n"
    "s_waitcnt lgkmcnt(3)\n"
    "s_add_u32 s65, s61, 0x1000\n"
    "ds_read_b128 v[172:175], v200 offset:15360\n"
    "v_add_f32 v196, 0x42640000, v196\n"
    "v_sub_f32 v104, 0, v196\n"
    "v_mfma_f32_16x16x32_bf16 v[88:91], v[184:187], v[0:3], v[88:91]\n"
    "s_add_u32 m0, s71, 0xd000\n"
    "ds_read_b128 v[176:179], v200 offset:16384\n"
    "v_sub_f32 v105, 0, v196\n"
    "v_sub_f32 v106, 0, v196\n"
    "v_sub_f32 v107, 0, v196\n"
    "v_mfma_f32_16x16x32_bf16 v[88:91], v[188:191], v[4:7], v[88:91]\n"
    "s_waitcnt lgkmcnt(3)\n"
    "ds_read_b128 v[180:183], v200 offset:17408\n"
    "buffer_load_dwordx4 v200, s[40:43], s65 offen sc1 lds\n"
    "v_add_f32 v44, v44, v104\n"
    "v_mfma_f32_16x16x32_bf16 v[40:43], v[160:163], a[240:243], v[104:107]\n"
    "ds_read_b128 v[184:187], v200 offset:18432\n"
    "v_add_f32 v45, v45, v104\n"
    "v_add_f32 v46, v46, v104\n"
    "v_add_f32 v47, v47, v104\n"
    "v_mfma_f32_16x16x32_bf16 v[40:43], v[164:167], a[244:247], v[40:43]\n"
    "s_waitcnt lgkmcnt(3)\n"
    "ds_read_b128 v[188:191], v200 offset:19456\n"
    "v_add_f32 v60, v60, v104\n"
    "v_add_f32 v61, v61, v104\n"
    "v_add_f32 v62, v62, v104\n"
    "v_mfma_f32_16x16x32_bf16 v[40:43], v[168:171], a[248:251], v[40:43]\n"
    "ds_read_b128 v[160:163], v200 offset:20480\n"
    "v_add_f32 v63, v63, v104\n"
    "v_exp_f32 v44, v44\n"
    "v_mfma_f32_16x16x32_bf16 v[40:43], v[172:175], a[252:255], v[40:43]\n"
    "s_waitcnt lgkmcnt(3)\n"
    "ds_read_b128 v[164:167], v200 offset:21504\n"
    "v_exp_f32 v45, v45\n"
    "v_mfma_f32_16x16x32_bf16 v[40:43], v[176:179], v[0:3], v[40:43]\n"
    "ds_read_b128 v[168:171], v200 offset:22528\n"
    "buffer_load_dwordx4 v200, s[40:43], s65 offen offset:1024 sc1 lds\n"
    "v_mfma_f32_16x16x32_bf16 v[40:43], v[180:183], v[4:7], v[40:43]\n"
    "s_waitcnt lgkmcnt(3)\n"
    "ds_read_b128 v[172:175], v200 offset:23552\n"
    "v_exp_f32 v46, v46\n"
    "v_mfma_f32_16x16x32_bf16 v[56:59], v[184:187], a[240:243], v[104:107]\n"
    "ds_read_b128 v[176:179], v201 offset:24576\n"
    "v_exp_f32 v47, v47\n"
    "v_mfma_f32_16x16x32_bf16 v[56:59], v[188:191], a[244:247], v[56:59]\n"
    "s_waitcnt lgkmcnt(3)\n"
    "s_add_u32 s78, s64, 0x30000\n"
    "ds_read_b128 v[180:183], v201 offset:25600\n"
    "v_exp_f32 v60, v60\n"
    "v_mfma_f32_16x16x32_bf16 v[56:59], v[160:163], a[248:251], v[56:59]\n"
    "s_add_u32 m0, s72, 0x1b000\n"
    "ds_read_b128 v[184:187], v201 offset:26624\n"
    "v_exp_f32 v61, v61\n"
    "v_mfma_f32_16x16x32_bf16 v[56:59], v[164:167], a[252:255], v[56:59]\n"
    "s_waitcnt lgkmcnt(3)\n"
    "ds_read_b128 v[188:191], v201 offset:27648\n"
    "buffer_load_dwordx4 v202, s[32:35], s78 offen lds\n"
    "v_mfma_f32_16x16x32_bf16 v[56:59], v[168:171], v[0:3], v[56:59]\n"
    "ds_read_b128 v[160:163], v201 offset:28672\n"
    "v_exp_f32 v62, v62\n"
    "v_exp_f32 v63, v63\n"
    "v_cvt_pk_bf16_f32 v136, v44, v45\n"
    "v_cvt_pk_bf16_f32 v137, v46, v47\n"
    "v_cvt_pk_bf16_f32 v138, v60, v61\n"
    "v_cvt_pk_bf16_f32 v139, v62, v63\n"
    "v_add_f32 v72, v72, v104\n"
    "v_add_f32 v73, v73, v104\n"
    "v_add_f32 v74, v74, v104\n"
    "v_add_f32 v75, v75, v104\n"
    "v_add_f32 v88, v88, v104\n"
    "v_add_f32 v89, v89, v104\n"
    "v_add_f32 v90, v90, v104\n"
    "v_add_f32 v91, v91, v104\n"
    "v_exp_f32 v72, v72\n"
    "v_exp_f32 v73, v73\n"
    "v_exp_f32 v74, v74\n"
    "v_exp_f32 v75, v75\n"
    "v_exp_f32 v88, v88\n"
    "v_exp_f32 v89, v89\n"
    "v_exp_f32 v90, v90\n"
    "v_exp_f32 v91, v91\n"
    "v_cvt_pk_bf16_f32 v152, v72, v73\n"
    "v_cvt_pk_bf16_f32 v153, v74, v75\n"
    "v_cvt_pk_bf16_f32 v154, v88, v89\n"
    "v_cvt_pk_bf16_f32 v155, v90, v91\n"
    "v_mfma_f32_16x16x32_bf16 v[56:59], v[172:175], v[4:7], v[56:59]\n"
    "s_waitcnt lgkmcnt(3)\n"
    "ds_read_b128 v[164:167], v201 offset:29696\n"
    "v_exp_f32 v40, v40\n"
    "v_mfma_f32_16x16x32_bf16 a[8:11], v[176:179], v[136:139], 0\n"
    "ds_read_b128 v[168:171], v201 offset:30720\n"
    "v_mfma_f32_16x16x32_bf16 a[8:11], v[180:183], v[152:155], a[8:11]\n"
    "s_waitcnt lgkmcnt(3)\n"
    "ds_read_b128 v[172:175], v201 offset:31744\n"
    "v_mfma_f32_16x16x32_bf16 a[24:27], v[184:187], v[136:139], 0\n"
    "ds_read_b128 v[176:179], v201 offset:32768\n"
    "buffer_load_dwordx4 v203, s[32:35], s78 offen offset:1024 lds\n"
    "v_mfma_f32_16x16x32_bf16 a[24:27], v[188:191], v[152:155], a[24:27]\n"
    "s_waitcnt lgkmcnt(3)\n"
    "ds_read_b128 v[180:183], v201 offset:33792\n"
    "v_mfma_f32_16x16x32_bf16 a[40:43], v[160:163], v[136:139], 0\n"
    "ds_read_b128 v[184:187], v201 offset:34816\n"
    "v_mfma_f32_16x16x32_bf16 a[40:43], v[164:167], v[152:155], a[40:43]\n"
    "ds_read_b128 v[188:191], v201 offset:35840\n"
    "v_mfma_f32_16x16x32_bf16 v[120:123], v[192:195], v[136:139], 0\n"
    "v_exp_f32 v41, v41\n"
    "v_mfma_f32_16x16x32_bf16 v[120:123], v[192:195], v[152:155], v[120:123]\n"
    "s_waitcnt lgkmcnt(4)\n"
    "buffer_load_dwordx4 v204, s[32:35], s78 offen offset:2048 lds\n"
    "v_mfma_f32_16x16x32_bf16 a[56:59], v[168:171], v[136:139], 0\n"
    "ds_read_b128 v[160:163], v201 offset:36864\n"
    "v_mfma_f32_16x16x32_bf16 a[56:59], v[172:175], v[152:155], a[56:59]\n"
    "s_waitcnt lgkmcnt(3)\n"
    "ds_read_b128 v[164:167], v201 offset:37888\n"
    "v_mfma_f32_16x16x32_bf16 a[72:75], v[176:179], v[136:139], 0\n"
    "ds_read_b128 v[168:171], v201 offset:38912\n"
    "v_mfma_f32_16x16x32_bf16 a[72:75], v[180:183], v[152:155], a[72:75]\n"
    "s_waitcnt lgkmcnt(3)\n"
    "ds_read_b128 v[172:175], v201 offset:39936\n"
    "v_mfma_f32_16x16x32_bf16 a[88:91], v[184:187], v[136:139], 0\n"
    "ds_read_b128 v[176:179], v201 offset:40960\n"
    "v_mfma_f32_16x16x32_bf16 a[88:91], v[188:191], v[152:155], a[88:91]\n"
    "s_waitcnt lgkmcnt(3)\n"
    "ds_read_b128 v[180:183], v201 offset:41984\n"
    "v_mfma_f32_16x16x32_bf16 a[104:107], v[160:163], v[136:139], 0\n"
    "ds_read_b128 v[184:187], v201 offset:43008\n"
    "v_mfma_f32_16x16x32_bf16 a[104:107], v[164:167], v[152:155], a[104:107]\n"
    "s_waitcnt lgkmcnt(3)\n"
    "ds_read_b128 v[188:191], v201 offset:44032\n"
    "v_mfma_f32_16x16x32_bf16 a[120:123], v[168:171], v[136:139], 0\n"
    "ds_read_b128 v[160:163], v201 offset:45056\n"
    "v_mfma_f32_16x16x32_bf16 a[120:123], v[172:175], v[152:155], a[120:123]\n"
    "s_waitcnt lgkmcnt(3)\n"
    "ds_read_b128 v[164:167], v201 offset:46080\n"
    "v_mfma_f32_16x16x32_bf16 a[136:139], v[176:179], v[136:139], 0\n"
    "ds_read_b128 v[168:171], v201 offset:47104\n"
    "v_mfma_f32_16x16x32_bf16 a[136:139], v[180:183], v[152:155], a[136:139]\n"
    "s_waitcnt lgkmcnt(3)\n"
    "ds_read_b128 v[172:175], v201 offset:48128\n"
    "v_mfma_f32_16x16x32_bf16 a[152:155], v[184:187], v[136:139], 0\n"
    "v_exp_f32 v42, v42\n"
    "v_mfma_f32_16x16x32_bf16 a[152:155], v[188:191], v[152:155], a[152:155]\n"
    "s_waitcnt lgkmcnt(2)\n"
    "v_exp_f32 v43, v43\n"
    "v_mfma_f32_16x16x32_bf16 a[168:171], v[160:163], v[136:139], 0\n"
    "v_exp_f32 v56, v56\n"
    "v_mfma_f32_16x16x32_bf16 a[168:171], v[164:167], v[152:155], a[168:171]\n"
    "s_waitcnt lgkmcnt(0)\n"
    "v_exp_f32 v57, v57\n"
    "v_mfma_f32_16x16x32_bf16 a[184:187], v[168:171], v[136:139], 0\n"
    "v_exp_f32 v58, v58\n"
    "v_exp_f32 v59, v59\n"
    "v_mfma_f32_16x16x32_bf16 a[184:187], v[172:175], v[152:155], a[184:187]\n"
    "v_add_f32 v121, v121, v40\n"
    "v_add_f32 v122, v122, v41\n"
    "v_add_f32 v121, v121, v42\n"
    "v_add_f32 v122, v122, v43\n"
    "v_add_f32 v121, v121, v56\n"
    "v_add_f32 v122, v122, v57\n"
    "s_waitcnt vmcnt(12)\n"
    "ds_read_b128 v[8:11], v158 offset:24576\n"
    "ds_read_b128 v[12:15], v159 offset:24576\n"
    "ds_read_b128 v[16:19], v158 offset:24704\n"
    "ds_read_b128 v[20:23], v159 offset:24704\n"
    "ds_read_b128 v[24:27], v158 offset:24832\n"
    "ds_read_b128 v[28:31], v159 offset:24832\n"
    "s_waitcnt lgkmcnt(0)\n"
    "ds_read_b128 v[160:163], v255\n"
    "ds_read_b128 v[164:167], v255 offset:1024\n"
    "ds_read_b128 v[168:171], v255 offset:2048\n"
    "ds_read_b128 v[172:175], v255 offset:3072\n"
    "ds_read_b128 v[176:179], v255 offset:4096\n"
    "ds_read_b128 v[180:183], v255 offset:5120\n"
    "s_waitcnt lgkmcnt(4)\n"
    "v_mfma_f32_16x16x32_bf16 v[64:67], v[160:163], v[8:11], 0\n"
    "ds_read_b128 v[184:187], v255 offset:6144\n"
    "v_mfma_f32_16x16x32_bf16 v[64:67], v[164:167], v[12:15], v[64:67]\n"
    "s_waitcnt lgkmcnt(3)\n"
    "s_add_u32 m0, s71, 0x6000\n"
    "ds_read_b128 v[188:191], v255 offset:7168\n"
    "v_mfma_f32_16x16x32_bf16 v[64:67], v[168:171], v[16:19], v[64:67]\n"
    "ds_read_b128 v[160:163], v255 offset:8192\n"
    "buffer_load_dwordx4 v200, s[36:39], s60 offen sc1 lds\n"
    "v_mfma_f32_16x16x32_bf16 v[64:67], v[172:175], v[20:23], v[64:67]\n"
    "s_waitcnt lgkmcnt(3)\n"
    "ds_read_b128 v[164:167], v255 offset:9216\n"
    "v_mfma_f32_16x16x32_bf16 v[64:67], v[176:179], v[24:27], v[64:67]\n"
    "ds_read_b128 v[168:171], v255 offset:10240\n"
    "v_mfma_f32_16x16x32_bf16 v[64:67], v[180:183], v[28:31], v[64:67]\n"
    "s_waitcnt lgkmcnt(3)\n"
    "ds_read_b128 v[172:175], v255 offset:11264\n"
    "buffer_load_dwordx4 v200, s[36:39], s60 offen offset:1024 sc1 lds\n"
    "v_mfma_f32_16x16x32_bf16 v[80:83], v[184:187], v[8:11], 0\n"
    "ds_read_b128 v[176:179], v200\n"
    "v_mfma_f32_16x16x32_bf16 v[80:83], v[188:191], v[12:15], v[80:83]\n"
    "s_waitcnt lgkmcnt(3)\n"
    "ds_read_b128 v[180:183], v200 offset:1024\n"
    "v_mfma_f32_16x16x32_bf16 v[80:83], v[160:163], v[16:19], v[80:83]\n"
    "ds_read_b128 v[184:187], v200 offset:2048\n"
    "buffer_load_dwordx4 v200, s[36:39], s60 offen offset:2048 sc1 lds\n"
    "v_mfma_f32_16x16x32_bf16 v[80:83], v[164:167], v[20:23], v[80:83]\n"
    "s_waitcnt lgkmcnt(3)\n"
    "ds_read_b128 v[188:191], v200 offset:3072\n"
    "v_mfma_f32_16x16x32_bf16 v[80:83], v[168:171], v[24:27], v[80:83]\n"
    "ds_read_b128 v[160:163], v200 offset:4096\n"
    "v_mfma_f32_16x16x32_bf16 v[80:83], v[172:175], v[28:31], v[80:83]\n"
    "s_waitcnt lgkmcnt(3)\n"
    "ds_read_b128 v[164:167], v200 offset:5120\n"
    "buffer_load_dwordx4 v200, s[36:39], s60 offen offset:3072 sc1 lds\n"
    "v_mfma_f32_16x16x32_bf16 v[76:79], v[176:179], v[8:11], 0\n"
    "s_add_u32 s65, s60, 0x1000\n"
    "ds_read_b128 v[168:171], v200 offset:6144\n"
    "v_mfma_f32_16x16x32_bf16 v[76:79], v[180:183], v[12:15], v[76:79]\n"
    "s_waitcnt lgkmcnt(3)\n"
    "s_add_u32 m0, s71, 0x7000\n"
    "ds_read_b128 v[172:175], v200 offset:7168\n"
    "v_mfma_f32_16x16x32_bf16 v[76:79], v[184:187], v[16:19], v[76:79]\n"
    "ds_read_b128 v[176:179], v200 offset:8192\n"
    "buffer_load_dwordx4 v200, s[36:39], s65 offen sc1 lds\n"
    "v_mfma_f32_16x16x32_bf16 v[76:79], v[188:191], v[20:23], v[76:79]\n"
    "s_waitcnt lgkmcnt(3)\n"
    "ds_read_b128 v[180:183], v200 offset:9216\n"
    "v_mfma_f32_16x16x32_bf16 v[76:79], v[160:163], v[24:27], v[76:79]\n"
    "ds_read_b128 v[184:187], v200 offset:10240\n"
    "v_mfma_f32_16x16x32_bf16 v[76:79], v[164:167], v[28:31], v[76:79]\n"
    "s_waitcnt lgkmcnt(3)\n"
    "ds_read_b128 v[188:191], v200 offset:11264\n"
    "buffer_load_dwordx4 v200, s[36:39], s65 offen offset:1024 sc1 lds\n"
    "v_max3_f32 v196, v64, v65, v66\n"
    "v_mfma_f32_16x16x32_bf16 v[92:95], v[168:171], v[8:11], 0\n"
    "ds_read_b128 v[160:163], v200 offset:12288\n"
    "v_max3_f32 v196, v196, v67, v80\n"
    "v_max3_f32 v196, v196, v81, v82\n"
    "v_max_f32 v196, v196, v83\n"
    "v_mfma_f32_16x16x32_bf16 v[92:95], v[172:175], v[12:15], v[92:95]\n"
    "s_waitcnt lgkmcnt(3)\n"
    "ds_read_b128 v[164:167], v200 offset:13312\n"
    "v_mov_b32 v197, v196\n"
    "s_nop 1\n"
    "v_permlane32_swap_b32 v196, v197\n"
    "v_max_f32 v196, v196, v197\n"
    "v_mfma_f32_16x16x32_bf16 v[92:95], v[176:179], v[16:19], v[92:95]\n"
    "ds_read_b128 v[168:171], v200 offset:14336\n"
    "v_mov_b32 v197, v196\n"
    "s_nop 1\n"
    "v_permlane16_swap_b32 v196, v197\n"
    "v_max_f32 v196, v196, v197\n"
    "v_mfma_f32_16x16x32_bf16 v[92:95], v[180:183], v[20:23], v[92:95]\n"
    "s_waitcnt lgkmcnt(3)\n"
    "ds_read_b128 v[172:175], v200 offset:15360\n"
    "v_add_f32 v196, 0x42640000, v196\n"
    "v_sub_f32 v108, 0, v196\n"
    "v_sub_f32 v109, 0, v196\n"
    "v_mfma_f32_16x16x32_bf16 v[92:95], v[184:187], v[24:27], v[92:95]\n"
    "ds_read_b128 v[176:179], v200 offset:16384\n"
    "v_sub_f32 v110, 0, v196\n"
    "v_sub_f32 v111, 0, v196\n"
    "v_add_f32 v64, v64, v108\n"
    "v_mfma_f32_16x16x32_bf16 v[92:95], v[188:191], v[28:31], v[92:95]\n"
    "s_waitcnt lgkmcnt(3)\n"
    "ds_read_b128 v[180:183], v200 offset:17408\n"
    "v_add_f32 v65, v65, v108\n"
    "v_add_f32 v66, v66, v108\n"
    "v_add_f32 v67, v67, v108\n"
    "v_mfma_f32_16x16x32_bf16 v[44:47], v[160:163], v[8:11], v[108:111]\n"
    "ds_read_b128 v[184:187], v200 offset:18432\n"
    "v_add_f32 v80, v80, v108\n"
    "v_add_f32 v81, v81, v108\n"
    "v_add_f32 v82, v82, v108\n"
    "v_mfma_f32_16x16x32_bf16 v[44:47], v[164:167], v[12:15], v[44:47]\n"
    "s_waitcnt lgkmcnt(3)\n"
    "ds_read_b128 v[188:191], v200 offset:19456\n"
    "v_add_f32 v83, v83, v108\n"
    "v_exp_f32 v64, v64\n"
    "v_mfma_f32_16x16x32_bf16 v[44:47], v[168:171], v[16:19], v[44:47]\n"
    "ds_read_b128 v[160:163], v200 offset:20480\n"
    "buffer_load_dwordx2 v[218:219], v213, s[44:47], 0 offen sc1\n"
    "v_mfma_f32_16x16x32_bf16 v[44:47], v[172:175], v[20:23], v[44:47]\n"
    "s_waitcnt lgkmcnt(3)\n"
    "ds_read_b128 v[164:167], v200 offset:21504\n"
    "v_exp_f32 v65, v65\n"
    "v_mfma_f32_16x16x32_bf16 v[44:47], v[176:179], v[24:27], v[44:47]\n"
    "ds_read_b128 v[168:171], v200 offset:22528\n"
    "v_exp_f32 v66, v66\n"
    "v_mfma_f32_16x16x32_bf16 v[44:47], v[180:183], v[28:31], v[44:47]\n"
    "s_waitcnt lgkmcnt(3)\n"
    "ds_read_b128 v[172:175], v200 offset:23552\n"
    "v_exp_f32 v67, v67\n"
    "v_mfma_f32_16x16x32_bf16 v[60:63], v[184:187], v[8:11], v[108:111]\n"
    "ds_read_b128 v[176:179], v201 offset:24576\n"
    "v_exp_f32 v80, v80\n"
    "v_mfma_f32_16x16x32_bf16 v[60:63], v[188:191], v[12:15], v[60:63]\n"
    "s_waitcnt lgkmcnt(3)\n"
    "ds_read_b128 v[180:183], v201 offset:25600\n"
    "v_exp_f32 v81, v81\n"
    "v_mfma_f32_16x16x32_bf16 v[60:63], v[160:163], v[16:19], v[60:63]\n"
    "ds_read_b128 v[184:187], v201 offset:26624\n"
    "v_exp_f32 v82, v82\n"
    "v_mfma_f32_16x16x32_bf16 v[60:63], v[164:167], v[20:23], v[60:63]\n"
    "s_waitcnt lgkmcnt(3)\n"
    "ds_read_b128 v[188:191], v201 offset:27648\n"
    "v_exp_f32 v83, v83\n"
    "v_cvt_pk_bf16_f32 v140, v64, v65\n"
    "v_mfma_f32_16x16x32_bf16 v[60:63], v[168:171], v[24:27], v[60:63]\n"
    "ds_read_b128 v[160:163], v201 offset:28672\n"
    "v_cvt_pk_bf16_f32 v141, v66, v67\n"
    "v_cvt_pk_bf16_f32 v142, v80, v81\n"
    "v_cvt_pk_bf16_f32 v143, v82, v83\n"
    "v_add_f32 v76, v76, v108\n"
    "v_add_f32 v77, v77, v108\n"
    "v_add_f32 v78, v78, v108\n"
    "v_add_f32 v79, v79, v108\n"
    "v_add_f32 v92, v92, v108\n"
    "v_add_f32 v93, v93, v108\n"
    "v_add_f32 v94, v94, v108\n"
    "v_add_f32 v95, v95, v108\n"
    "v_exp_f32 v76, v76\n"
    "v_exp_f32 v77, v77\n"
    "v_exp_f32 v78, v78\n"
    "v_exp_f32 v79, v79\n"
    "v_exp_f32 v92, v92\n"
    "v_exp_f32 v93, v93\n"
    "v_exp_f32 v94, v94\n"
    "v_exp_f32 v95, v95\n"
    "v_cvt_pk_bf16_f32 v156, v76, v77\n"
    "v_cvt_pk_bf16_f32 v157, v78, v79\n"
    "v_cvt_pk_bf16_f32 v158, v92, v93\n"
    "v_cvt_pk_bf16_f32 v159, v94, v95\n"
    "v_mfma_f32_16x16x32_bf16 v[60:63], v[172:175], v[28:31], v[60:63]\n"
    "s_waitcnt lgkmcnt(3)\n"
    "ds_read_b128 v[164:167], v201 offset:29696\n"
    "v_exp_f32 v44, v44\n"
    "v_mfma_f32_16x16x32_bf16 a[12:15], v[176:179], v[140:143], 0\n"
    "ds_read_b128 v[168:171], v201 offset:30720\n"
    "v_mfma_f32_16x16x32_bf16 a[12:15], v[180:183], v[156:159], a[12:15]\n"
    "s_waitcnt lgkmcnt(3)\n"
    "ds_read_b128 v[172:175], v201 offset:31744\n"
    "v_mfma_f32_16x16x32_bf16 a[28:31], v[184:187], v[140:143], 0\n"
    "ds_read_b128 v[176:179], v201 offset:32768\n"
    "v_mfma_f32_16x16x32_bf16 a[28:31], v[188:191], v[156:159], a[28:31]\n"
    "s_waitcnt lgkmcnt(3)\n"
    "ds_read_b128 v[180:183], v201 offset:33792\n"
    "v_mfma_f32_16x16x32_bf16 a[44:47], v[160:163], v[140:143], 0\n"
    "ds_read_b128 v[184:187], v201 offset:34816\n"
    "v_mfma_f32_16x16x32_bf16 a[44:47], v[164:167], v[156:159], a[44:47]\n"
    "ds_read_b128 v[188:191], v201 offset:35840\n"
    "v_mfma_f32_16x16x32_bf16 v[124:127], v[192:195], v[140:143], 0\n"
    "v_exp_f32 v45, v45\n"
    "v_mfma_f32_16x16x32_bf16 v[124:127], v[192:195], v[156:159], v[124:127]\n"
    "s_waitcnt lgkmcnt(4)\n"
    "v_exp_f32 v46, v46\n"
    "v_mfma_f32_16x16x32_bf16 a[60:63], v[168:171], v[140:143], 0\n"
    "ds_read_b128 v[160:163], v201 offset:36864\n"
    "v_mfma_f32_16x16x32_bf16 a[60:63], v[172:175], v[156:159], a[60:63]\n"
    "s_waitcnt lgkmcnt(3)\n"
    "ds_read_b128 v[164:167], v201 offset:37888\n"
    "v_mfma_f32_16x16x32_bf16 a[76:79], v[176:179], v[140:143], 0\n"
    "ds_read_b128 v[168:171], v201 offset:38912\n"
    "v_mfma_f32_16x16x32_bf16 a[76:79], v[180:183], v[156:159], a[76:79]\n"
    "s_waitcnt lgkmcnt(3)\n"
    "ds_read_b128 v[172:175], v201 offset:39936\n"
    "v_mfma_f32_16x16x32_bf16 a[92:95], v[184:187], v[140:143], 0\n"
    "ds_read_b128 v[176:179], v201 offset:40960\n"
    "v_mfma_f32_16x16x32_bf16 a[92:95], v[188:191], v[156:159], a[92:95]\n"
    "s_waitcnt lgkmcnt(3)\n"
    "ds_read_b128 v[180:183], v201 offset:41984\n"
    "v_mfma_f32_16x16x32_bf16 a[108:111], v[160:163], v[140:143], 0\n"
    "ds_read_b128 v[184:187], v201 offset:43008\n"
    "v_mfma_f32_16x16x32_bf16 a[108:111], v[164:167], v[156:159], a[108:111]\n"
    "s_waitcnt lgkmcnt(3)\n"
    "ds_read_b128 v[188:191], v201 offset:44032\n"
    "v_mfma_f32_16x16x32_bf16 a[124:127], v[168:171], v[140:143], 0\n"
    "ds_read_b128 v[160:163], v201 offset:45056\n"
    "v_mfma_f32_16x16x32_bf16 a[124:127], v[172:175], v[156:159], a[124:127]\n"
    "s_waitcnt lgkmcnt(3)\n"
    "ds_read_b128 v[164:167], v201 offset:46080\n"
    "v_mfma_f32_16x16x32_bf16 a[140:143], v[176:179], v[140:143], 0\n"
    "ds_read_b128 v[168:171], v201 offset:47104\n"
    "v_mfma_f32_16x16x32_bf16 a[140:143], v[180:183], v[156:159], a[140:143]\n"
    "s_waitcnt lgkmcnt(3)\n"
    "ds_read_b128 v[172:175], v201 offset:48128\n"
    "v_mfma_f32_16x16x32_bf16 a[156:159], v[184:187], v[140:143], 0\n"
    "v_exp_f32 v47, v47\n"
    "v_mfma_f32_16x16x32_bf16 a[156:159], v[188:191], v[156:159], a[156:159]\n"
    "s_waitcnt lgkmcnt(2)\n"
    "v_exp_f32 v60, v60\n"
    "v_mfma_f32_16x16x32_bf16 a[172:175], v[160:163], v[140:143], 0\n"
    "v_exp_f32 v61, v61\n"
    "v_mfma_f32_16x16x32_bf16 a[172:175], v[164:167], v[156:159], a[172:175]\n"
    "s_waitcnt lgkmcnt(0)\n"
    "v_exp_f32 v62, v62\n"
    "v_mfma_f32_16x16x32_bf16 a[188:191], v[168:171], v[140:143], 0\n"
    "v_exp_f32 v63, v63\n"
    "v_mfma_f32_16x16x32_bf16 a[188:191], v[172:175], v[156:159], a[188:191]\n"
    "v_add_f32 v125, v125, v44\n"
    "v_add_f32 v126, v126, v45\n"
    "v_add_f32 v125, v125, v46\n"
    "v_add_f32 v126, v126, v47\n"
    "s_add_u32 s64, s64, 0x3c000\n"
    "s_add_u32 s62, s62, 0x6000\n"
    "s_add_u32 s63, s63, 0x6000\n"
    "s_add_u32 s60, s60, 0x6000\n"
    "s_add_u32 s61, s61, 0x6000\n"
    "v_add_u32 v209, 0x300, v209\n"
    "v_add_u32 v210, 0xc0, v210\n"
    "v_mov_b32 v220, 5\n"
    "s_waitcnt vmcnt(0)\n"
    "v_cmp_ne_u32 s[80:81], s67, v219\n"
    "v_cmp_gt_u32 vcc, s66, v218\n"
    "s_or_b64 vcc, vcc, s[80:81]\n"
    "s_cbranch_vccnz LSPRE2\n"
    "LSPB2:\n"
    "s_barrier\n"
    "s_cmp_eq_u32 s58, 0\n"
    "s_cbranch_scc1 LENT0\n"
    "s_cmp_eq_u32 s58, 1\n"
    "s_cbranch_scc1 LENT1\n"
    "s_cmp_eq_u32 s58, 2\n"
    "s_cbranch_scc1 LENT1\n"
    "LENT2:\n"
    "s_mov_b32 s59, 1\n"
    "s_add_u32 m0, s72, 0x21000\n"
    "s_nop 0\n"
    "buffer_load_dwordx4 v250, s[48:51], 0 offen offset:2048 lds\n"
    "s_add_u32 m0, s72, 0x18000\n"
    "s_nop 0\n"
    "buffer_load_dwordx4 v202, s[32:35], s64 offen lds\n"
    "buffer_load_dwordx4 v203, s[32:35], s64 offen offset:1024 lds\n"
    "buffer_load_dwordx4 v204, s[32:35], s64 offen offset:2048 lds\n"
    "s_add_u32 s64, s64, 0xc000\n"
    "s_mov_b32 s94, 0x3000\n"
    "s_mov_b32 s95, 0x6000\n"
    "s_add_u32 s51, s72, 0x18000\n"
    "v_add_u32 v205, 0x3000, v205\n"
    "v_add_u32 v206, 0x3000, v206\n"
    "v_add_u32 v207, 0x3000, v207\n"
    "v_add_f32 v121, v121, v58\n"
    "v_add_f32 v122, v122, v59\n"
    "ds_read_b128 v[160:163], v200 offset:24576\n"
    "v_add_f32 v125, v125, v60\n"
    "v_add_f32 v126, v126, v61\n"
    "v_add_f32 v125, v125, v62\n"
    "ds_read_b128 v[164:167], v200 offset:25600\n"
    "v_add_f32 v126, v126, v63\n"
    "ds_read_b64 v[228:229], v210 offset:64\n"
    "ds_read2st64_b32 v[240:241], v207 offset0:96 offset1:99\n"
    "ds_read2st64_b32 v[242:243], v207 offset0:102 offset1:105\n"
    "ds_read2st64_b32 v[244:245], v207 offset0:108 offset1:111\n"
    "ds_read2st64_b32 v[246:247], v207 offset0:114 offset1:117\n"
    "ds_read_b128 v[168:171], v200 offset:26624\n"
    "v_cvt_pk_bf16_f32 v140, v44, v45\n"
    "v_cvt_pk_bf16_f32 v141, v46, v47\n"
    "v_cvt_pk_bf16_f32 v136, v40, v41\n"
    "v_cvt_pk_bf16_f32 v137, v42, v43\n"
    "ds_read_b128 v[172:175], v200 offset:27648\n"
    "v_cvt_pk_bf16_f32 v132, v36, v37\n"
    "v_cvt_pk_bf16_f32 v133, v38, v39\n"
    "s_waitcnt lgkmcnt(5)\n"
    "v_cvt_f32_i32 v240, v240\n"
    "v_cvt_f32_i32 v241, v241\n"
    "s_nop 7\n"
    ".p2align 6\n"
    "LLOOP2:\n"
    "v_mfma_f32_16x16x32_bf16 v[64:67], v[160:163], a[192:195], v[96:99]\n"
    "ds_read_b128 v[176:179], v200 offset:28672\n"
    "s_waitcnt lgkmcnt(5)\n"
    "v_cvt_f32_i32 v242, v242\n"
    "v_mfma_f32_16x16x32_bf16 v[64:67], v[164:167], a[196:199], v[64:67]\n"
    "s_waitcnt lgkmcnt(1)\n"
    "v_cvt_f32_i32 v243, v243\n"
    "v_cvt_f32_i32 v244, v244\n"
    "v_mfma_f32_16x16x32_bf16 v[64:67], v[168:171], a[200:203], v[64:67]\n"
    "v_cvt_f32_i32 v245, v245\n"
    "v_cvt_f32_i32 v246, v246\n"
    "v_mfma_f32_16x16x32_bf16 v[68:71], v[168:171], a[224:227], v[100:103]\n"
    "v_cvt_f32_i32 v247, v247\n"
    "v_mul_f32_dpp v240, v228, v240 row_newbcast:0 row_mask:0xf bank_mask:0xf\n"
    "v_mfma_f32_16x16x32_bf16 v[68:71], v[164:167], a[220:223], v[68:71]\n"
    "ds_read_b128 v[180:183], v200 offset:29696\n"
    "v_mul_f32_dpp v241, v228, v241 row_newbcast:1 row_mask:0xf bank_mask:0xf\n"
    "v_mfma_f32_16x16x32_bf16 v[68:71], v[160:163], a[216:219], v[68:71]\n"
    "v_mul_f32_dpp v242, v228, v242 row_newbcast:2 row_mask:0xf bank_mask:0xf\n"
    "v_mul_f32_dpp v243, v228, v243 row_newbcast:3 row_mask:0xf bank_mask:0xf\n"
    "v_mfma_f32_16x16x32_bf16 v[72:75], v[160:163], a[240:243], v[104:107]\n"
    "v_mul_f32_dpp v244, v228, v244 row_newbcast:4 row_mask:0xf bank_mask:0xf\n"
    "v_mul_f32_dpp v245, v228, v245 row_newbcast:5 row_mask:0xf bank_mask:0xf\n"
    "v_mfma_f32_16x16x32_bf16 v[72:75], v[164:167], a[244:247], v[72:75]\n"
    "v_mul_f32_dpp v246, v228, v246 row_newbcast:6 row_mask:0xf bank_mask:0xf\n"
    "v_mul_f32_dpp v247, v228, v247 row_newbcast:7 row_mask:0xf bank_mask:0xf\n"
    "v_mfma_f32_16x16x32_bf16 v[72:75], v[168:171], a[248:251], v[72:75]\n"
    "v_add_f32_dpp v240, v229, v240 row_newbcast:0 row_mask:0xf bank_mask:0xf\n"
    "v_add_f32_dpp v241, v229, v241 row_newbcast:1 row_mask:0xf bank_mask:0xf\n"
    "v_mfma_f32_16x16x32_bf16 v[76:79], v[168:171], v[16:19], v[108:111]\n"
    "v_add_f32_dpp v242, v229, v242 row_newbcast:2 row_mask:0xf bank_mask:0xf\n"
    "v_add_f32_dpp v243, v229, v243 row_newbcast:3 row_mask:0xf bank_mask:0xf\n"
    "v_mfma_f32_16x16x32_bf16 v[76:79], v[164:167], v[12:15], v[76:79]\n"
    "v_add_f32_dpp v244, v229, v244 row_newbcast:4 row_mask:0xf bank_mask:0xf\n"
    "v_add_f32_dpp v245, v229, v245 row_newbcast:5 row_mask:0xf bank_mask:0xf\n"
    "v_mfma_f32_16x16x32_bf16 v[76:79], v[160:163], v[8:11], v[76:79]\n"
    "s_waitcnt lgkmcnt(1)\n"
    "v_add_f32_dpp v246, v229, v246 row_newbcast:6 row_mask:0xf bank_mask:0xf\n"
    "v_add_f32_dpp v247, v229, v247 row_newbcast:7 row_mask:0xf bank_mask:0xf\n"
    "v_mfma_f32_16x16x32_bf16 v[76:79], v[172:175], v[20:23], v[76:79]\n"
    "ds_read_b128 v[160:163], v200 offset:33792\n"
    "v_cvt_pk_bf16_f32 v240, v240, v241\n"
    "v_mfma_f32_16x16x32_bf16 v[76:79], v[176:179], v[24:27], v[76:79]\n"
    "s_waitcnt lgkmcnt(1)\n"
    "v_cvt_pk_bf16_f32 v241, v242, v243\n"
    "v_cvt_pk_bf16_f32 v242, v244, v245\n"
    "v_mfma_f32_16x16x32_bf16 v[76:79], v[180:183], v[28:31], v[76:79]\n"
    "v_cvt_pk_bf16_f32 v243, v246, v247\n"
    "v_cvt_pk_bf16_f32 v128, v32, v33\n"
    "v_mfma_f32_16x16x32_bf16 v[72:75], v[180:183], v[4:7], v[72:75]\n"
    "ds_read_b128 v[164:167], v200 offset:34816\n"
    "v_cvt_pk_bf16_f32 v129, v34, v35\n"
    "v_mfma_f32_16x16x32_bf16 v[72:75], v[176:179], v[0:3], v[72:75]\n"
    "buffer_store_dwordx4 v[240:243], v212, s[40:43], s63 offen sc1\n"
    "v_cvt_pk_bf16_f32 v142, v60, v61\n"
    "v_cvt_pk_bf16_f32 v143, v62, v63\n"
    "v_cvt_pk_bf16_f32 v138, v56, v57\n"
    "v_mfma_f32_16x16x32_bf16 v[72:75], v[172:175], a[252:255], v[72:75]\n"
    "v_cvt_pk_bf16_f32 v139, v58, v59\n"
    "v_cvt_pk_bf16_f32 v134, v52, v53\n"
    "v_mfma_f32_16x16x32_bf16 v[68:71], v[172:175], a[228:231], v[68:71]\n"
    "ds_read_b128 v[168:171], v200 offset:35840\n"
    "v_cvt_pk_bf16_f32 v135, v54, v55\n"
    "v_mfma_f32_16x16x32_bf16 v[68:71], v[176:179], a[232:235], v[68:71]\n"
    "v_cvt_pk_bf16_f32 v130, v48, v49\n"
    "v_cvt_pk_bf16_f32 v131, v50, v51\n"
    "v_mfma_f32_16x16x32_bf16 v[68:71], v[180:183], a[236:239], v[68:71]\n"
    "v_mfma_f32_16x16x32_bf16 v[64:67], v[180:183], a[212:215], v[64:67]\n"
    "v_mfma_f32_16x16x32_bf16 v[64:67], v[176:179], a[208:211], v[64:67]\n"
    "v_mfma_f32_16x16x32_bf16 v[64:67], v[172:175], a[204:207], v[64:67]\n"
    "s_waitcnt lgkmcnt(1)\n"
    "v_mfma_f32_16x16x32_bf16 v[80:83], v[160:163], a[204:207], v[96:99]\n"
    "ds_read_b128 v[172:175], v200 offset:30720\n"
    "v_mfma_f32_16x16x32_bf16 v[80:83], v[164:167], a[208:211], v[80:83]\n"
    "s_waitcnt lgkmcnt(1)\n"
    "v_mfma_f32_16x16x32_bf16 v[80:83], v[168:171], a[212:215], v[80:83]\n"
    "v_mfma_f32_16x16x32_bf16 v[84:87], v[168:171], a[236:239], v[100:103]\n"
    "ds_read_b128 v[176:179], v200 offset:31744\n"
    "v_mfma_f32_16x16x32_bf16 v[84:87], v[164:167], a[232:235], v[84:87]\n"
    "v_exp_f32 v64, v64\n"
    "v_mfma_f32_16x16x32_bf16 v[84:87], v[160:163], a[228:231], v[84:87]\n"
    "v_exp_f32 v65, v65\n"
    "v_mfma_f32_16x16x32_bf16 v[88:91], v[160:163], a[252:255], v[104:107]\n"
    "ds_read_b128 v[180:183], v200 offset:32768\n"
    "v_cvt_pk_bf16_f32 v144, v64, v65\n"
    "v_mfma_f32_16x16x32_bf16 v[88:91], v[164:167], v[0:3], v[88:91]\n"
    "s_add_u32 s65, s60, 0xfffff800\n"
    "v_exp_f32 v66, v66\n"
    "v_mfma_f32_16x16x32_bf16 v[88:91], v[168:171], v[4:7], v[88:91]\n"
    "s_mov_b32 m0, 0x4000\n"
    "v_exp_f32 v67, v67\n"
    "v_mfma_f32_16x16x32_bf16 v[92:95], v[168:171], v[28:31], v[108:111]\n"
    "v_exp_f32 v68, v68\n"
    "v_mfma_f32_16x16x32_bf16 v[92:95], v[164:167], v[24:27], v[92:95]\n"
    "buffer_load_dwordx4 v200, s[36:39], s65 offen sc1 lds\n"
    "v_mfma_f32_16x16x32_bf16 v[92:95], v[160:163], v[20:23], v[92:95]\n"
    "s_waitcnt lgkmcnt(1)\n"
    "v_exp_f32 v69, v69\n"
    "v_mfma_f32_16x16x32_bf16 v[92:95], v[172:175], v[8:11], v[92:95]\n"
    "ds_read_b128 v[160:163], v200 offset:36864\n"
    "v_cvt_pk_bf16_f32 v145, v66, v67\n"
    "v_mfma_f32_16x16x32_bf16 v[92:95], v[176:179], v[12:15], v[92:95]\n"
    "s_waitcnt lgkmcnt(1)\n"
    "v_exp_f32 v70, v70\n"
    "v_mfma_f32_16x16x32_bf16 v[92:95], v[180:183], v[16:19], v[92:95]\n"
    "buffer_load_dwordx4 v200, s[36:39], s65 offen offset:1024 sc1 lds\n"
    "v_mfma_f32_16x16x32_bf16 v[88:91], v[180:183], a[248:251], v[88:91]\n"
    "ds_read_b128 v[164:167], v200 offset:37888\n"
    "v_cvt_pk_bf16_f32 v148, v68, v69\n"
    "v_mfma_f32_16x16x32_bf16 v[88:91], v[176:179], a[244:247], v[88:91]\n"
    "v_exp_f32 v71, v71\n"
    "v_mfma_f32_16x16x32_bf16 v[88:91], v[172:175], a[240:243], v[88:91]\n"
    "v_exp_f32 v72, v72\n"
    "v_mfma_f32_16x16x32_bf16 v[84:87], v[172:175], a[216:219], v[84:87]\n"
    "ds_read_b128 v[168:171], v200 offset:38912\n"
    "v_cvt_pk_bf16_f32 v149, v70, v71\n"
    "v_mfma_f32_16x16x32_bf16 v[84:87], v[176:179], a[220:223], v[84:87]\n"
    "buffer_load_dwordx4 v200, s[36:39], s65 offen offset:2048 sc1 lds\n"
    "v_mfma_f32_16x16x32_bf16 v[84:87], v[180:183], a[224:227], v[84:87]\n"
    "v_exp_f32 v73, v73\n"
    "v_mfma_f32_16x16x32_bf16 v[80:83], v[180:183], a[200:203], v[80:83]\n"
    "v_exp_f32 v74, v74\n"
    "v_mfma_f32_16x16x32_bf16 v[80:83], v[176:179], a[196:199], v[80:83]\n"
    "v_exp_f32 v75, v75\n"
    "v_mfma_f32_16x16x32_bf16 v[80:83], v[172:175], a[192:195], v[80:83]\n"
    "s_waitcnt lgkmcnt(1)\n"
    "buffer_load_dwordx4 v200, s[36:39], s65 offen offset:3072 sc1 lds\n"
    "v_mfma_f32_16x16x32_bf16 v[32:35], v[160:163], a[192:195], v[96:99]\n"
    "ds_read_b128 v[172:175], v200 offset:39936\n"
    "v_cvt_pk_bf16_f32 v152, v72, v73\n"
    "v_mfma_f32_16x16x32_bf16 v[32:35], v[164:167], a[196:199], v[32:35]\n"
    "s_waitcnt lgkmcnt(1)\n"
    "s_add_u32 s65, s60, 0x800\n"
    "v_cvt_pk_bf16_f32 v153, v74, v75\n"
    "v_mfma_f32_16x16x32_bf16 v[32:35], v[168:171], a[200:203], v[32:35]\n"
    "s_mov_b32 m0, 0x5000\n"
    "v_exp_f32 v76, v76\n"
    "v_mfma_f32_16x16x32_bf16 v[36:39], v[168:171], a[224:227], v[100:103]\n"
    "ds_read_b128 v[176:179], v200 offset:40960\n"
    "v_add_f32 v113, v113, v64\n"
    "v_mfma_f32_16x16x32_bf16 v[36:39], v[164:167], a[220:223], v[36:39]\n"
    "buffer_load_dwordx4 v200, s[36:39], s65 offen sc1 lds\n"
    "v_mfma_f32_16x16x32_bf16 v[36:39], v[160:163], a[216:219], v[36:39]\n"
    "v_exp_f32 v77, v77\n"
    "v_mfma_f32_16x16x32_bf16 v[40:43], v[160:163], a[240:243], v[104:107]\n"
    "ds_read_b128 v[180:183], v200 offset:41984\n"
    "v_cvt_pk_bf16_f32 v156, v76, v77\n"
    "v_mfma_f32_16x16x32_bf16 v[40:43], v[164:167], a[244:247], v[40:43]\n"
    "v_exp_f32 v78, v78\n"
    "v_mfma_f32_16x16x32_bf16 v[40:43], v[168:171], a[248:251], v[40:43]\n"
    "buffer_load_dwordx4 v200, s[36:39], s65 offen offset:1024 sc1 lds\n"
    "v_mfma_f32_16x16x32_bf16 v[44:47], v[168:171], v[16:19], v[108:111]\n"
    "v_exp_f32 v79, v79\n"
    "v_mfma_f32_16x16x32_bf16 v[44:47], v[164:167], v[12:15], v[44:47]\n"
    "v_exp_f32 v80, v80\n"
    "v_mfma_f32_16x16x32_bf16 v[44:47], v[160:163], v[8:11], v[44:47]\n"
    "s_waitcnt lgkmcnt(1)\n"
    "v_exp_f32 v81, v81\n"
    "v_mfma_f32_16x16x32_bf16 v[44:47], v[172:175], v[20:23], v[44:47]\n"
    "ds_read_b128 v[160:163], v200 offset:46080\n"
    "v_cvt_pk_bf16_f32 v157, v78, v79\n"
    "v_mfma_f32_16x16x32_bf16 v[44:47], v[176:179], v[24:27], v[44:47]\n"
    "s_waitcnt lgkmcnt(1)\n"
    "buffer_load_dwordx4 v200, s[36:39], s65 offen offset:2048 sc1 lds\n"
    "v_mfma_f32_16x16x32_bf16 v[44:47], v[180:183], v[28:31], v[44:47]\n"
    "v_exp_f32 v82, v82\n"
    "v_mfma_f32_16x16x32_bf16 v[40:43], v[180:183], v[4:7], v[40:43]\n"
    "ds_read_b128 v[164:167], v200 offset:47104\n"
    "v_cvt_pk_bf16_f32 v146, v80, v81\n"
    "v_mfma_f32_16x16x32_bf16 v[40:43], v[176:179], v[0:3], v[40:43]\n"
    "v_exp_f32 v83, v83\n"
    "v_mfma_f32_16x16x32_bf16 v[40:43], v[172:175], a[252:255], v[40:43]\n"
    "buffer_load_dwordx4 v200, s[36:39], s65 offen offset:3072 sc1 lds\n"
    "v_mfma_f32_16x16x32_bf16 v[36:39], v[172:175], a[228:231], v[36:39]\n"
    "s_add_u32 s65, s61, 0xfffff800\n"
    "ds_read_b128 v[168:171], v200 offset:48128\n"
    "v_cvt_pk_bf16_f32 v147, v82, v83\n"
    "v_mfma_f32_16x16x32_bf16 v[36:39], v[176:179], a[232:235], v[36:39]\n"
    "s_mov_b32 m0, 0x16000\n"
    "v_exp_f32 v84, v84\n"
    "v_mfma_f32_16x16x32_bf16 v[36:39], v[180:183], a[236:239], v[36:39]\n"
    "v_exp_f32 v85, v85\n"
    "v_mfma_f32_16x16x32_bf16 v[32:35], v[180:183], a[212:215], v[32:35]\n"
    "buffer_load_dwordx4 v200, s[40:43], s65 offen sc1 lds\n"
    "v_mfma_f32_16x16x32_bf16 v[32:35], v[176:179], a[208:211], v[32:35]\n"
    "v_exp_f32 v86, v86\n"
    "v_mfma_f32_16x16x32_bf16 v[32:35], v[172:175], a[204:207], v[32:35]\n"
    "s_waitcnt lgkmcnt(1)\n"
    "v_exp_f32 v87, v87\n"
    "v_mfma_f32_16x16x32_bf16 v[48:51], v[160:163], a[204:207], v[96:99]\n"
    "ds_read_b128 v[172:175], v200 offset:43008\n"
    "v_cvt_pk_bf16_f32 v150, v84, v85\n"
    "v_mfma_f32_16x16x32_bf16 v[48:51], v[164:167], a[208:211], v[48:51]\n"
    "s_waitcnt lgkmcnt(1)\n"
    "buffer_load_dwordx4 v200, s[40:43], s65 offen offset:1024 sc1 lds\n"
    "v_mfma_f32_16x16x32_bf16 v[48:51], v[168:171], a[212:215], v[48:51]\n"
    "v_exp_f32 v88, v88\n"
    "v_mfma_f32_16x16x32_bf16 v[52:55], v[168:171], a[236:239], v[100:103]\n"
    "ds_read_b128 v[176:179], v200 offset:44032\n"
    "v_cvt_pk_bf16_f32 v151, v86, v87\n"
    "v_mfma_f32_16x16x32_bf16 v[52:55], v[164:167], a[232:235], v[52:55]\n"
    "v_exp_f32 v89, v89\n"
    "v_mfma_f32_16x16x32_bf16 v[52:55], v[160:163], a[228:231], v[52:55]\n"
    "buffer_load_dwordx4 v200, s[40:43], s65 offen offset:2048 sc1 lds\n"
    "v_mfma_f32_16x16x32_bf16 v[56:59], v[160:163], a[252:255], v[104:107]\n"
    "ds_read_b128 v[180:183], v200 offset:45056\n"
    "v_cvt_pk_bf16_f32 v154, v88, v89\n"
    "v_mfma_f32_16x16x32_bf16 v[56:59], v[164:167], v[0:3], v[56:59]\n"
    "v_exp_f32 v90, v90\n"
    "v_mfma_f32_16x16x32_bf16 v[56:59], v[168:171], v[4:7], v[56:59]\n"
    "v_exp_f32 v91, v91\n"
    "v_mfma_f32_16x16x32_bf16 v[60:63], v[168:171], v[28:31], v[108:111]\n"
    "buffer_load_dwordx4 v200, s[40:43], s65 offen offset:3072 sc1 lds\n"
    "v_mfma_f32_16x16x32_bf16 v[60:63], v[164:167], v[24:27], v[60:63]\n"
    "s_add_u32 s65, s61, 0x800\n"
    "v_exp_f32 v92, v92\n"
    "v_mfma_f32_16x16x32_bf16 v[60:63], v[160:163], v[20:23], v[60:63]\n"
    "s_waitcnt lgkmcnt(1)\n"
    "s_mov_b32 m0, 0x17000\n"
    "v_cvt_pk_bf16_f32 v155, v90, v91\n"
    "v_mfma_f32_16x16x32_bf16 v[60:63], v[172:175], v[8:11], v[60:63]\n"
    "ds_read_b128 v[160:163], v201\n"
    "v_add_f32 v114, v114, v65\n"
    "v_mfma_f32_16x16x32_bf16 v[60:63], v[176:179], v[12:15], v[60:63]\n"
    "s_waitcnt lgkmcnt(1)\n"
    "buffer_load_dwordx4 v200, s[40:43], s65 offen sc1 lds\n"
    "v_mfma_f32_16x16x32_bf16 v[60:63], v[180:183], v[16:19], v[60:63]\n"
    "v_exp_f32 v93, v93\n"
    "v_mfma_f32_16x16x32_bf16 v[56:59], v[180:183], a[248:251], v[56:59]\n"
    "ds_read_b128 v[164:167], v201 offset:1024\n"
    "v_cvt_pk_bf16_f32 v158, v92, v93\n"
    "v_mfma_f32_16x16x32_bf16 v[56:59], v[176:179], a[244:247], v[56:59]\n"
    "v_exp_f32 v94, v94\n"
    "v_mfma_f32_16x16x32_bf16 v[56:59], v[172:175], a[240:243], v[56:59]\n"
    "buffer_load_dwordx4 v200, s[40:43], s65 offen offset:1024 sc1 lds\n"
    "v_mfma_f32_16x16x32_bf16 v[52:55], v[172:175], a[216:219], v[52:55]\n"
    "ds_read_b128 v[168:171], v201 offset:2048\n"
    "v_add_f32 v113, v113, v66\n"
    "v_mfma_f32_16x16x32_bf16 v[52:55], v[176:179], a[220:223], v[52:55]\n"
    "v_exp_f32 v95, v95\n"
    "v_mfma_f32_16x16x32_bf16 v[52:55], v[180:183], a[224:227], v[52:55]\n"
    "v_cvt_pk_bf16_f32 v159, v94, v95\n"
    "v_add_f32 v114, v114, v67\n"
    "v_mfma_f32_16x16x32_bf16 v[48:51], v[180:183], a[200:203], v[48:51]\n"
    "buffer_load_dwordx4 v200, s[40:43], s65 offen offset:2048 sc1 lds\n"
    "v_mfma_f32_16x16x32_bf16 v[48:51], v[176:179], a[196:199], v[48:51]\n"
    "v_exp_f32 v32, v32\n"
    "v_mfma_f32_16x16x32_bf16 v[48:51], v[172:175], a[192:195], v[48:51]\n"
    "s_waitcnt lgkmcnt(1)\n"
    "v_exp_f32 v33, v33\n"
    "v_mfma_f32_16x16x32_bf16 a[0:3], v[160:163], v[128:131], a[0:3]\n"
    "ds_read_b128 v[172:175], v201 offset:3072\n"
    "ds_read_b128 v[176:179], v201 offset:4096\n"
    "v_mfma_f32_16x16x32_bf16 a[0:3], v[164:167], v[144:147], a[0:3]\n"
    "buffer_load_dwordx4 v200, s[40:43], s65 offen offset:3072 sc1 lds\n"
    "v_mfma_f32_16x16x32_bf16 a[4:7], v[160:163], v[132:135], a[4:7]\n"
    "v_exp_f32 v34, v34\n"
    "v_mfma_f32_16x16x32_bf16 a[4:7], v[164:167], v[148:151], a[4:7]\n"
    "v_exp_f32 v35, v35\n"
    "v_mfma_f32_16x16x32_bf16 a[8:11], v[160:163], v[136:139], a[8:11]\n"
    "ds_read_b128 v[180:183], v201 offset:5120\n"
    "v_add_f32 v117, v117, v68\n"
    "v_mfma_f32_16x16x32_bf16 a[8:11], v[164:167], v[152:155], a[8:11]\n"
    "v_exp_f32 v36, v36\n"
    "v_mfma_f32_16x16x32_bf16 a[12:15], v[160:163], v[140:143], a[12:15]\n"
    "v_exp_f32 v37, v37\n"
    "v_mfma_f32_16x16x32_bf16 a[12:15], v[164:167], v[156:159], a[12:15]\n"
    "s_waitcnt lgkmcnt(2)\n"
    "v_exp_f32 v38, v38\n"
    "v_mfma_f32_16x16x32_bf16 a[28:31], v[168:171], v[140:143], a[28:31]\n"
    "ds_read_b128 v[160:163], v201 offset:6144\n"
    "v_add_f32 v118, v118, v69\n"
    "v_mfma_f32_16x16x32_bf16 a[28:31], v[172:175], v[156:159], a[28:31]\n"
    "v_exp_f32 v39, v39\n"
    "v_mfma_f32_16x16x32_bf16 a[24:27], v[172:175], v[152:155], a[24:27]\n"
    "v_exp_f32 v40, v40\n"
    "v_mfma_f32_16x16x32_bf16 a[24:27], v[168:171], v[136:139], a[24:27]\n"
    "v_exp_f32 v41, v41\n"
    "v_mfma_f32_16x16x32_bf16 a[20:23], v[168:171], v[132:135], a[20:23]\n"
    "ds_read_b128 v[164:167], v201 offset:7168\n"
    "v_add_f32 v117, v117, v70\n"
    "v_mfma_f32_16x16x32_bf16 a[20:23], v[172:175], v[148:151], a[20:23]\n"
    "v_exp_f32 v42, v42\n"
    "v_mfma_f32_16x16x32_bf16 a[16:19], v[172:175], v[144:147], a[16:19]\n"
    "v_exp_f32 v43, v43\n"
    "v_mfma_f32_16x16x32_bf16 a[16:19], v[168:171], v[128:131], a[16:19]\n"
    "s_waitcnt lgkmcnt(2)\n"
    "v_exp_f32 v44, v44\n"
    "v_mfma_f32_16x16x32_bf16 a[32:35], v[176:179], v[128:131], a[32:35]\n"
    "ds_read_b128 v[168:171], v201 offset:8192\n"
    "v_add_f32 v113, v113, v32\n"
    "v_mfma_f32_16x16x32_bf16 a[32:35], v[180:183], v[144:147], a[32:35]\n"
    "v_exp_f32 v45, v45\n"
    "v_mfma_f32_16x16x32_bf16 a[36:39], v[180:183], v[148:151], a[36:39]\n"
    "v_exp_f32 v46, v46\n"
    "v_mfma_f32_16x16x32_bf16 a[36:39], v[176:179], v[132:135], a[36:39]\n"
    "v_exp_f32 v47, v47\n"
    "v_mfma_f32_16x16x32_bf16 a[40:43], v[176:179], v[136:139], a[40:43]\n"
    "ds_read_b128 v[172:175], v201 offset:9216\n"
    "v_add_f32 v118, v118, v71\n"
    "v_mfma_f32_16x16x32_bf16 a[40:43], v[180:183], v[152:155], a[40:43]\n"
    "v_exp_f32 v48, v48\n"
    "v_mfma_f32_16x16x32_bf16 a[44:47], v[180:183], v[156:159], a[44:47]\n"
    "v_exp_f32 v49, v49\n"
    "v_mfma_f32_16x16x32_bf16 a[44:47], v[176:179], v[140:143], a[44:47]\n"
    "s_waitcnt lgkmcnt(2)\n"
    "v_exp_f32 v50, v50\n"
    "v_mfma_f32_16x16x32_bf16 a[60:63], v[160:163], v[140:143], a[60:63]\n"
    "ds_read_b128 v[176:179], v201 offset:10240\n"
    "v_add_f32 v114, v114, v33\n"
    "v_mfma_f32_16x16x32_bf16 a[60:63], v[164:167], v[156:159], a[60:63]\n"
    "v_exp_f32 v51, v51\n"
    "v_mfma_f32_16x16x32_bf16 a[56:59], v[164:167], v[152:155], a[56:59]\n"
    "v_exp_f32 v52, v52\n"
    "v_mfma_f32_16x16x32_bf16 a[56:59], v[160:163], v[136:139], a[56:59]\n"
    "v_add_f32 v121, v121, v72\n"
    "v_add_f32 v113, v113, v34\n"
    "v_mfma_f32_16x16x32_bf16 a[52:55], v[160:163], v[132:135], a[52:55]\n"
    "ds_read_b128 v[180:183], v201 offset:11264\n"
    "v_add_f32 v122, v122, v73\n"
    "v_mfma_f32_16x16x32_bf16 a[52:55], v[164:167], v[148:151], a[52:55]\n"
    "v_exp_f32 v53, v53\n"
    "v_mfma_f32_16x16x32_bf16 a[48:51], v[164:167], v[144:147], a[48:51]\n"
    "v_add_f32 v114, v114, v35\n"
    "v_add_f32 v121, v121, v74\n"
    "v_mfma_f32_16x16x32_bf16 a[48:51], v[160:163], v[128:131], a[48:51]\n"
    "s_waitcnt lgkmcnt(2)\n"
    "v_add_f32 v117, v117, v36\n"
    "v_add_f32 v122, v122, v75\n"
    "v_mfma_f32_16x16x32_bf16 a[64:67], v[168:171], v[128:131], a[64:67]\n"
    "ds_read_b128 v[160:163], v201 offset:12288\n"
    "v_add_f32 v118, v118, v37\n"
    "v_mfma_f32_16x16x32_bf16 a[64:67], v[172:175], v[144:147], a[64:67]\n"
    "v_exp_f32 v54, v54\n"
    "v_mfma_f32_16x16x32_bf16 a[68:71], v[172:175], v[148:151], a[68:71]\n"
    "v_add_f32 v125, v125, v76\n"
    "v_add_f32 v117, v117, v38\n"
    "v_mfma_f32_16x16x32_bf16 a[68:71], v[168:171], v[132:135], a[68:71]\n"
    "s_add_u32 m0, s51, s95\n"
    "v_exp_f32 v55, v55\n"
    "v_mfma_f32_16x16x32_bf16 a[72:75], v[168:171], v[136:139], a[72:75]\n"
    "ds_read_b128 v[164:167], v201 offset:13312\n"
    "v_add_f32 v126, v126, v77\n"
    "v_mfma_f32_16x16x32_bf16 a[72:75], v[172:175], v[152:155], a[72:75]\n"
    "buffer_load_dwordx4 v202, s[32:35], s64 offen lds\n"
    "v_mfma_f32_16x16x32_bf16 a[76:79], v[172:175], v[156:159], a[76:79]\n"
    "v_add_f32 v118, v118, v39\n"
    "v_add_f32 v125, v125, v78\n"
    "v_mfma_f32_16x16x32_bf16 a[76:79], v[168:171], v[140:143], a[76:79]\n"
    "s_waitcnt lgkmcnt(2)\n"
    "v_add_f32 v121, v121, v40\n"
    "v_add_f32 v126, v126, v79\n"
    "v_mfma_f32_16x16x32_bf16 a[92:95], v[176:179], v[140:143], a[92:95]\n"
    "ds_read_b128 v[168:171], v201 offset:14336\n"
    "v_add_f32 v122, v122, v41\n"
    "v_mfma_f32_16x16x32_bf16 a[92:95], v[180:183], v[156:159], a[92:95]\n"
    "buffer_load_dwordx4 v203, s[32:35], s64 offen offset:1024 lds\n"
    "v_mfma_f32_16x16x32_bf16 a[88:91], v[180:183], v[152:155], a[88:91]\n"
    "v_exp_f32 v56, v56\n"
    "v_mfma_f32_16x16x32_bf16 a[88:91], v[176:179], v[136:139], a[88:91]\n"
    "v_add_f32 v113, v113, v80\n"
    "v_add_f32 v121, v121, v42\n"
    "v_mfma_f32_16x16x32_bf16 a[84:87], v[176:179], v[132:135], a[84:87]\n"
    "ds_read_b128 v[172:175], v201 offset:15360\n"
    "v_add_f32 v114, v114, v81\n"
    "v_mfma_f32_16x16x32_bf16 a[84:87], v[180:183], v[148:151], a[84:87]\n"
    "buffer_load_dwordx4 v204, s[32:35], s64 offen offset:2048 lds\n"
    "v_mfma_f32_16x16x32_bf16 a[80:83], v[180:183], v[144:147], a[80:83]\n"
    "v_exp_f32 v57, v57\n"
    "v_mfma_f32_16x16x32_bf16 a[80:83], v[176:179], v[128:131], a[80:83]\n"
    "s_waitcnt lgkmcnt(2)\n"
    "v_add_f32 v122, v122, v43\n"
    "v_add_f32 v113, v113, v82\n"
    "v_mfma_f32_16x16x32_bf16 a[96:99], v[160:163], v[128:131], a[96:99]\n"
    "s_mov_b32 s95, s94\n"
    "ds_read_b128 v[176:179], v201 offset:16384\n"
    "v_add_f32 v125, v125, v44\n"
    "v_mfma_f32_16x16x32_bf16 a[96:99], v[164:167], v[144:147], a[96:99]\n"
    "s_sub_u32 s94, s94, 0x3000\n"
    "v_exp_f32 v58, v58\n"
    "v_mfma_f32_16x16x32_bf16 a[100:103], v[164:167], v[148:151], a[100:103]\n"
    "s_min_u32 s94, s94, 0x6000\n"
    "v_add_f32 v114, v114, v83\n"
    "v_add_f32 v126, v126, v45\n"
    "v_mfma_f32_16x16x32_bf16 a[100:103], v[160:163], v[132:135], a[100:103]\n"
    "s_sub_u32 s93, s94, s95\n"
    "v_add_f32 v117, v117, v84\n"
    "v_add_f32 v125, v125, v46\n"
    "v_mfma_f32_16x16x32_bf16 a[104:107], v[160:163], v[136:139], a[104:107]\n"
    "ds_read_b128 v[180:183], v201 offset:17408\n"
    "v_add_f32 v118, v118, v85\n"
    "v_mfma_f32_16x16x32_bf16 a[104:107], v[164:167], v[152:155], a[104:107]\n"
    "v_exp_f32 v59, v59\n"
    "v_mfma_f32_16x16x32_bf16 a[108:111], v[164:167], v[156:159], a[108:111]\n"
    "v_add_f32 v126, v126, v47\n"
    "v_add_f32 v117, v117, v86\n"
    "v_mfma_f32_16x16x32_bf16 a[108:111], v[160:163], v[140:143], a[108:111]\n"
    "s_waitcnt lgkmcnt(2)\n"
    "v_add_f32 v113, v113, v48\n"
    "v_add_f32 v118, v118, v87\n"
    "v_mfma_f32_16x16x32_bf16 a[124:127], v[168:171], v[140:143], a[124:127]\n"
    "ds_read_b128 v[160:163], v201 offset:18432\n"
    "v_add_f32 v114, v114, v49\n"
    "v_mfma_f32_16x16x32_bf16 a[124:127], v[172:175], v[156:159], a[124:127]\n"
    "v_exp_f32 v60, v60\n"
    "v_mfma_f32_16x16x32_bf16 a[120:123], v[172:175], v[152:155], a[120:123]\n"
    "v_add_f32 v121, v121, v88\n"
    "v_add_f32 v113, v113, v50\n"
    "v_mfma_f32_16x16x32_bf16 a[120:123], v[168:171], v[136:139], a[120:123]\n"
    "v_exp_f32 v61, v61\n"
    "v_mfma_f32_16x16x32_bf16 a[116:119], v[168:171], v[132:135], a[116:119]\n"
    "ds_read_b128 v[164:167], v201 offset:19456\n"
    "v_add_u32 v210, 0x80, v210\n"
    "v_mfma_f32_16x16x32_bf16 a[116:119], v[172:175], v[148:151], a[116:119]\n"
    "v_add_u32 v207, s93, v207\n"
    "v_add_f32 v122, v122, v89\n"
    "v_mfma_f32_16x16x32_bf16 a[112:115], v[172:175], v[144:147], a[112:115]\n"
    "v_add_f32 v114, v114, v51\n"
    "v_add_f32 v121, v121, v90\n"
    "v_mfma_f32_16x16x32_bf16 a[112:115], v[168:171], v[128:131], a[112:115]\n"
    "s_waitcnt lgkmcnt(2)\n"
    "v_add_f32 v117, v117, v52\n"
    "v_add_f32 v122, v122, v91\n"
    "v_mfma_f32_16x16x32_bf16 a[128:131], v[176:179], v[128:131], a[128:131]\n"
    "ds_read_b128 v[168:171], v201 offset:20480\n"
    "v_add_f32 v118, v118, v53\n"
    "v_mfma_f32_16x16x32_bf16 a[128:131], v[180:183], v[144:147], a[128:131]\n"
    "v_exp_f32 v62, v62\n"
    "v_mfma_f32_16x16x32_bf16 a[132:135], v[180:183], v[148:151], a[132:135]\n"
    "v_add_f32 v125, v125, v92\n"
    "v_add_f32 v117, v117, v54\n"
    "v_mfma_f32_16x16x32_bf16 a[132:135], v[176:179], v[132:135], a[132:135]\n"
    "s_add_u32 s64, s64, 0xc000\n"
    "v_exp_f32 v63, v63\n"
    "v_mfma_f32_16x16x32_bf16 a[136:139], v[176:179], v[136:139], a[136:139]\n"
    "s_add_u32 s62, s62, 0x6000\n"
    "ds_read_b128 v[172:175], v201 offset:21504\n"
    "v_add_f32 v126, v126, v93\n"
    "v_mfma_f32_16x16x32_bf16 a[136:139], v[180:183], v[152:155], a[136:139]\n"
    "s_add_u32 s63, s63, 0x6000\n"
    "v_add_f32 v118, v118, v55\n"
    "v_add_f32 v125, v125, v94\n"
    "v_mfma_f32_16x16x32_bf16 a[140:143], v[180:183], v[156:159], a[140:143]\n"
    "s_add_u32 s60, s60, 0x6000\n"
    "v_add_f32 v121, v121, v56\n"
    "v_add_f32 v126, v126, v95\n"
    "v_mfma_f32_16x16x32_bf16 a[140:143], v[176:179], v[140:143], a[140:143]\n"
    "s_waitcnt lgkmcnt(2)\n"
    "v_add_f32 v122, v122, v57\n"
    "v_mfma_f32_16x16x32_bf16 a[156:159], v[160:163], v[140:143], a[156:159]\n"
    "s_add_u32 s61, s61, 0x6000\n"
    "ds_read_b128 v[176:179], v201 offset:22528\n"
    "v_mfma_f32_16x16x32_bf16 a[156:159], v[164:167], v[156:159], a[156:159]\n"
    "ds_read_b128 v[180:183], v201 offset:23552\n"
    "v_mfma_f32_16x16x32_bf16 a[152:155], v[164:167], v[152:155], a[152:155]\n"
    "s_waitcnt vmcnt(3)\n"
    "v_mfma_f32_16x16x32_bf16 a[152:155], v[160:163], v[136:139], a[152:155]\n"
    "v_mfma_f32_16x16x32_bf16 a[148:151], v[160:163], v[132:135], a[148:151]\n"
    "v_mfma_f32_16x16x32_bf16 a[148:151], v[164:167], v[148:151], a[148:151]\n"
    "v_mfma_f32_16x16x32_bf16 a[144:147], v[164:167], v[144:147], a[144:147]\n"
    "v_mfma_f32_16x16x32_bf16 a[144:147], v[160:163], v[128:131], a[144:147]\n"
    "s_waitcnt lgkmcnt(0)\n"
    "v_mfma_f32_16x16x32_bf16 a[160:163], v[168:171], v[128:131], a[160:163]\n"
    "s_barrier\n"
    "v_mfma_f32_16x16x32_bf16 a[160:163], v[172:175], v[144:147], a[160:163]\n"
    "v_add_f32 v121, v121, v58\n"
    "v_add_f32 v122, v122, v59\n"
    "v_mfma_f32_16x16x32_bf16 a[164:167], v[172:175], v[148:151], a[164:167]\n"
    "ds_read_b128 v[160:163], v200\n"
    "v_add_f32 v125, v125, v60\n"
    "v_mfma_f32_16x16x32_bf16 a[164:167], v[168:171], v[132:135], a[164:167]\n"
    "v_add_f32 v126, v126, v61\n"
    "v_add_f32 v125, v125, v62\n"
    "v_mfma_f32_16x16x32_bf16 a[168:171], v[168:171], v[136:139], a[168:171]\n"
    "ds_read_b128 v[164:167], v200 offset:1024\n"
    "v_add_f32 v126, v126, v63\n"
    "v_mfma_f32_16x16x32_bf16 a[168:171], v[172:175], v[152:155], a[168:171]\n"
    "ds_read_b64 v[228:229], v210\n"
    "ds_read2st64_b32 v[240:241], v207 offset0:96 offset1:99\n"
    "v_mfma_f32_16x16x32_bf16 a[172:175], v[172:175], v[156:159], a[172:175]\n"
    "ds_read2st64_b32 v[242:243], v207 offset0:102 offset1:105\n"
    "ds_read2st64_b32 v[244:245], v207 offset0:108 offset1:111\n"
    "v_mfma_f32_16x16x32_bf16 a[172:175], v[168:171], v[140:143], a[172:175]\n"
    "ds_read2st64_b32 v[246:247], v207 offset0:114 offset1:117\n"
    "v_mfma_f32_16x16x32_bf16 a[188:191], v[176:179], v[140:143], a[188:191]\n"
    "ds_read_b128 v[168:171], v200 offset:2048\n"
    "v_cvt_pk_bf16_f32 v140, v44, v45\n"
    "v_mfma_f32_16x16x32_bf16 a[188:191], v[180:183], v[156:159], a[188:191]\n"
    "v_cvt_pk_bf16_f32 v141, v46, v47\n"
    "v_mfma_f32_16x16x32_bf16 a[184:187], v[176:179], v[136:139], a[184:187]\n"
    "v_cvt_pk_bf16_f32 v136, v40, v41\n"
    "v_cvt_pk_bf16_f32 v137, v42, v43\n"
    "v_mfma_f32_16x16x32_bf16 a[184:187], v[180:183], v[152:155], a[184:187]\n"
    "v_mfma_f32_16x16x32_bf16 a[180:183], v[176:179], v[132:135], a[180:183]\n"
    "ds_read_b128 v[172:175], v200 offset:3072\n"
    "v_cvt_pk_bf16_f32 v132, v36, v37\n"
    "v_mfma_f32_16x16x32_bf16 a[180:183], v[180:183], v[148:151], a[180:183]\n"
    "v_cvt_pk_bf16_f32 v133, v38, v39\n"
    "v_mfma_f32_16x16x32_bf16 a[176:179], v[176:179], v[128:131], a[176:179]\n"
    "s_waitcnt lgkmcnt(5)\n"
    "v_cvt_f32_i32 v240, v240\n"
    "v_cvt_f32_i32 v241, v241\n"
    "v_mfma_f32_16x16x32_bf16 a[176:179], v[180:183], v[144:147], a[176:179]\n"
    "s_waitcnt lgkmcnt(4)\n"
    "v_cvt_f32_i32 v242, v242\n"
    "v_cvt_f32_i32 v243, v243\n"
    "LM02:\n"
    "v_mfma_f32_16x16x32_bf16 v[64:67], v[160:163], a[192:195], v[96:99]\n"
    "ds_read_b128 v[176:179], v200 offset:4096\n"
    "s_waitcnt lgkmcnt(4)\n"
    "v_cvt_f32_i32 v244, v244\n"
    "v_mfma_f32_16x16x32_bf16 v[64:67], v[164:167], a[196:199], v[64:67]\n"
    "s_waitcnt lgkmcnt(1)\n"
    "v_cvt_f32_i32 v245, v245\n"
    "v_cvt_f32_i32 v246, v246\n"
    "v_mfma_f32_16x16x32_bf16 v[64:67], v[168:171], a[200:203], v[64:67]\n"
    "v_cvt_f32_i32 v247, v247\n"
    "v_mul_f32_dpp v240, v228, v240 row_newbcast:0 row_mask:0xf bank_mask:0xf\n"
    "v_mfma_f32_16x16x32_bf16 v[68:71], v[168:171], a[224:227], v[100:103]\n"
    "v_mul_f32_dpp v241, v228, v241 row_newbcast:1 row_mask:0xf bank_mask:0xf\n"
    "v_mul_f32_dpp v242, v228, v242 row_newbcast:2 row_mask:0xf bank_mask:0xf\n"
    "v_mfma_f32_16x16x32_bf16 v[68:71], v[164:167], a[220:223], v[68:71]\n"
    "ds_read_b128 v[180:183], v200 offset:5120\n"
    "v_mul_f32_dpp v243, v228, v243 row_newbcast:3 row_mask:0xf bank_mask:0xf\n"
    "v_mfma_f32_16x16x32_bf16 v[68:71], v[160:163], a[216:219], v[68:71]\n"
    "v_mul_f32_dpp v244, v228, v244 row_newbcast:4 row_mask:0xf bank_mask:0xf\n"
    "v_mul_f32_dpp v245, v228, v245 row_newbcast:5 row_mask:0xf bank_mask:0xf\n"
    "v_mfma_f32_16x16x32_bf16 v[72:75], v[160:163], a[240:243], v[104:107]\n"
    "v_mul_f32_dpp v246, v228, v246 row_newbcast:6 row_mask:0xf bank_mask:0xf\n"
    "v_mul_f32_dpp v247, v228, v247 row_newbcast:7 row_mask:0xf bank_mask:0xf\n"
    "v_mfma_f32_16x16x32_bf16 v[72:75], v[164:167], a[244:247], v[72:75]\n"
    "v_add_f32_dpp v240, v229, v240 row_newbcast:0 row_mask:0xf bank_mask:0xf\n"
    "v_add_f32_dpp v241, v229, v241 row_newbcast:1 row_mask:0xf bank_mask:0xf\n"
    "v_mfma_f32_16x16x32_bf16 v[72:75], v[168:171], a[248:251], v[72:75]\n"
    "v_add_f32_dpp v242, v229, v242 row_newbcast:2 row_mask:0xf bank_mask:0xf\n"
    "v_add_f32_dpp v243, v229, v243 row_newbcast:3 row_mask:0xf bank_mask:0xf\n"
    "v_mfma_f32_16x16x32_bf16 v[76:79], v[168:171], v[16:19], v[108:111]\n"
    "v_add_f32_dpp v244, v229, v244 row_newbcast:4 row_mask:0xf bank_mask:0xf\n"
    "v_add_f32_dpp v245, v229, v245 row_newbcast:5 row_mask:0xf bank_mask:0xf\n"
    "v_mfma_f32_16x16x32_bf16 v[76:79], v[164:167], v[12:15], v[76:79]\n"
    "v_add_f32_dpp v246, v229, v246 row_newbcast:6 row_mask:0xf bank_mask:0xf\n"
    "v_add_f32_dpp v247, v229, v247 row_newbcast:7 row_mask:0xf bank_mask:0xf\n"
    "v_mfma_f32_16x16x32_bf16 v[76:79], v[160:163], v[8:11], v[76:79]\n"
    "s_waitcnt lgkmcnt(1)\n"
    "v_cvt_pk_bf16_f32 v240, v240, v241\n"
    "v_cvt_pk_bf16_f32 v241, v242, v243\n"
    "v_mfma_f32_16x16x32_bf16 v[76:79], v[172:175], v[20:23], v[76:79]\n"
    "ds_read_b128 v[160:163], v200 offset:9216\n"
    "v_cvt_pk_bf16_f32 v242, v244, v245\n"
    "v_mfma_f32_16x16x32_bf16 v[76:79], v[176:179], v[24:27], v[76:79]\n"
    "s_waitcnt lgkmcnt(1)\n"
    "v_cvt_pk_bf16_f32 v243, v246, v247\n"
    "v_cvt_pk_bf16_f32 v128, v32, v33\n"
    "v_mfma_f32_16x16x32_bf16 v[76:79], v[180:183], v[28:31], v[76:79]\n"
    "buffer_store_dwordx4 v[240:243], v212, s[40:43], s63 offen sc1\n"
    "v_cvt_pk_bf16_f32 v129, v34, v35\n"
    "v_cvt_pk_bf16_f32 v142, v60, v61\n"
    "v_cvt_pk_bf16_f32 v143, v62, v63\n"
    "v_mfma_f32_16x16x32_bf16 v[72:75], v[180:183], v[4:7], v[72:75]\n"
    "ds_read_b128 v[164:167], v200 offset:10240\n"
    "v_cvt_pk_bf16_f32 v138, v56, v57\n"
    "v_mfma_f32_16x16x32_bf16 v[72:75], v[176:179], v[0:3], v[72:75]\n"
    "v_cvt_pk_bf16_f32 v139, v58, v59\n"
    "v_cvt_pk_bf16_f32 v134, v52, v53\n"
    "v_mfma_f32_16x16x32_bf16 v[72:75], v[172:175], a[252:255], v[72:75]\n"
    "v_cvt_pk_bf16_f32 v135, v54, v55\n"
    "v_cvt_pk_bf16_f32 v130, v48, v49\n"
    "v_mfma_f32_16x16x32_bf16 v[68:71], v[172:175], a[228:231], v[68:71]\n"
    "ds_read_b128 v[168:171], v200 offset:11264\n"
    "v_cvt_pk_bf16_f32 v131, v50, v51\n"
    "v_mfma_f32_16x16x32_bf16 v[68:71], v[176:179], a[232:235], v[68:71]\n"
    "v_mfma_f32_16x16x32_bf16 v[68:71], v[180:183], a[236:239], v[68:71]\n"
    "v_mfma_f32_16x16x32_bf16 v[64:67], v[180:183], a[212:215], v[64:67]\n"
    "v_mfma_f32_16x16x32_bf16 v[64:67], v[176:179], a[208:211], v[64:67]\n"
    "v_mfma_f32_16x16x32_bf16 v[64:67], v[172:175], a[204:207], v[64:67]\n"
    "s_waitcnt lgkmcnt(1)\n"
    "v_mfma_f32_16x16x32_bf16 v[80:83], v[160:163], a[204:207], v[96:99]\n"
    "ds_read_b128 v[172:175], v200 offset:6144\n"
    "v_mfma_f32_16x16x32_bf16 v[80:83], v[164:167], a[208:211], v[80:83]\n"
    "s_waitcnt lgkmcnt(1)\n"
    "v_mfma_f32_16x16x32_bf16 v[80:83], v[168:171], a[212:215], v[80:83]\n"
    "v_mfma_f32_16x16x32_bf16 v[84:87], v[168:171], a[236:239], v[100:103]\n"
    "ds_read_b128 v[176:179], v200 offset:7168\n"
    "v_mfma_f32_16x16x32_bf16 v[84:87], v[164:167], a[232:235], v[84:87]\n"
    "v_exp_f32 v64, v64\n"
    "v_mfma_f32_16x16x32_bf16 v[84:87], v[160:163], a[228:231], v[84:87]\n"
    "v_exp_f32 v65, v65\n"
    "v_mfma_f32_16x16x32_bf16 v[88:91], v[160:163], a[252:255], v[104:107]\n"
    "ds_read_b128 v[180:183], v200 offset:8192\n"
    "v_cvt_pk_bf16_f32 v144, v64, v65\n"
    "v_mfma_f32_16x16x32_bf16 v[88:91], v[164:167], v[0:3], v[88:91]\n"
    "s_add_u32 s65, s60, 0xfffff800\n"
    "v_exp_f32 v66, v66\n"
    "v_mfma_f32_16x16x32_bf16 v[88:91], v[168:171], v[4:7], v[88:91]\n"
    "s_mov_b32 m0, 0xa000\n"
    "v_exp_f32 v67, v67\n"
    "v_mfma_f32_16x16x32_bf16 v[92:95], v[168:171], v[28:31], v[108:111]\n"
    "v_exp_f32 v68, v68\n"
    "v_mfma_f32_16x16x32_bf16 v[92:95], v[164:167], v[24:27], v[92:95]\n"
    "buffer_load_dwordx4 v200, s[36:39], s65 offen sc1 lds\n"
    "v_mfma_f32_16x16x32_bf16 v[92:95], v[160:163], v[20:23], v[92:95]\n"
    "s_waitcnt lgkmcnt(1)\n"
    "v_exp_f32 v69, v69\n"
    "v_mfma_f32_16x16x32_bf16 v[92:95], v[172:175], v[8:11], v[92:95]\n"
    "ds_read_b128 v[160:163], v200 offset:12288\n"
    "v_cvt_pk_bf16_f32 v145, v66, v67\n"
    "v_mfma_f32_16x16x32_bf16 v[92:95], v[176:179], v[12:15], v[92:95]\n"
    "s_waitcnt lgkmcnt(1)\n"
    "v_exp_f32 v70, v70\n"
    "v_mfma_f32_16x16x32_bf16 v[92:95], v[180:183], v[16:19], v[92:95]\n"
    "buffer_load_dwordx4 v200, s[36:39], s65 offen offset:1024 sc1 lds\n"
    "v_mfma_f32_16x16x32_bf16 v[88:91], v[180:183], a[248:251], v[88:91]\n"
    "ds_read_b128 v[164:167], v200 offset:13312\n"
    "v_cvt_pk_bf16_f32 v148, v68, v69\n"
    "v_mfma_f32_16x16x32_bf16 v[88:91], v[176:179], a[244:247], v[88:91]\n"
    "v_exp_f32 v71, v71\n"
    "v_mfma_f32_16x16x32_bf16 v[88:91], v[172:175], a[240:243], v[88:91]\n"
    "v_exp_f32 v72, v72\n"
    "v_mfma_f32_16x16x32_bf16 v[84:87], v[172:175], a[216:219], v[84:87]\n"
    "ds_read_b128 v[168:171], v200 offset:14336\n"
    "v_cvt_pk_bf16_f32 v149, v70, v71\n"
    "v_mfma_f32_16x16x32_bf16 v[84:87], v[176:179], a[220:223], v[84:87]\n"
    "buffer_load_dwordx4 v200, s[36:39], s65 offen offset:2048 sc1 lds\n"
    "v_mfma_f32_16x16x32_bf16 v[84:87], v[180:183], a[224:227], v[84:87]\n"
    "v_exp_f32 v73, v73\n"
    "v_mfma_f32_16x16x32_bf16 v[80:83], v[180:183], a[200:203], v[80:83]\n"
    "v_exp_f32 v74, v74\n"
    "v_mfma_f32_16x16x32_bf16 v[80:83], v[176:179], a[196:199], v[80:83]\n"
    "v_exp_f32 v75, v75\n"
    "v_mfma_f32_16x16x32_bf16 v[80:83], v[172:175], a[192:195], v[80:83]\n"
    "s_waitcnt lgkmcnt(1)\n"
    "buffer_load_dwordx4 v200, s[36:39], s65 offen offset:3072 sc1 lds\n"
    "v_mfma_f32_16x16x32_bf16 v[32:35], v[160:163], a[192:195], v[96:99]\n"
    "ds_read_b128 v[172:175], v200 offset:15360\n"
    "v_cvt_pk_bf16_f32 v152, v72, v73\n"
    "v_mfma_f32_16x16x32_bf16 v[32:35], v[164:167], a[196:199], v[32:35]\n"
    "s_waitcnt lgkmcnt(1)\n"
    "s_add_u32 s65, s60, 0x800\n"
    "v_cvt_pk_bf16_f32 v153, v74, v75\n"
    "v_mfma_f32_16x16x32_bf16 v[32:35], v[168:171], a[200:203], v[32:35]\n"
    "s_mov_b32 m0, 0xb000\n"
    "v_exp_f32 v76, v76\n"
    "v_mfma_f32_16x16x32_bf16 v[36:39], v[168:171], a[224:227], v[100:103]\n"
    "ds_read_b128 v[176:179], v200 offset:16384\n"
    "v_add_f32 v113, v113, v64\n"
    "v_mfma_f32_16x16x32_bf16 v[36:39], v[164:167], a[220:223], v[36:39]\n"
    "buffer_load_dwordx4 v200, s[36:39], s65 offen sc1 lds\n"
    "v_mfma_f32_16x16x32_bf16 v[36:39], v[160:163], a[216:219], v[36:39]\n"
    "v_exp_f32 v77, v77\n"
    "v_mfma_f32_16x16x32_bf16 v[40:43], v[160:163], a[240:243], v[104:107]\n"
    "ds_read_b128 v[180:183], v200 offset:17408\n"
    "v_cvt_pk_bf16_f32 v156, v76, v77\n"
    "v_mfma_f32_16x16x32_bf16 v[40:43], v[164:167], a[244:247], v[40:43]\n"
    "v_exp_f32 v78, v78\n"
    "v_mfma_f32_16x16x32_bf16 v[40:43], v[168:171], a[248:251], v[40:43]\n"
    "buffer_load_dwordx4 v200, s[36:39], s65 offen offset:1024 sc1 lds\n"
    "v_mfma_f32_16x16x32_bf16 v[44:47], v[168:171], v[16:19], v[108:111]\n"
    "v_exp_f32 v79, v79\n"
    "v_mfma_f32_16x16x32_bf16 v[44:47], v[164:167], v[12:15], v[44:47]\n"
    "v_exp_f32 v80, v80\n"
    "v_mfma_f32_16x16x32_bf16 v[44:47], v[160:163], v[8:11], v[44:47]\n"
    "s_waitcnt lgkmcnt(1)\n"
    "v_exp_f32 v81, v81\n"
    "v_mfma_f32_16x16x32_bf16 v[44:47], v[172:175], v[20:23], v[44:47]\n"
    "ds_read_b128 v[160:163], v200 offset:21504\n"
    "v_cvt_pk_bf16_f32 v157, v78, v79\n"
    "v_mfma_f32_16x16x32_bf16 v[44:47], v[176:179], v[24:27], v[44:47]\n"
    "s_waitcnt lgkmcnt(1)\n"
    "buffer_load_dwordx4 v200, s[36:39], s65 offen offset:2048 sc1 lds\n"
    "v_mfma_f32_16x16x32_bf16 v[44:47], v[180:183], v[28:31], v[44:47]\n"
    "v_exp_f32 v82, v82\n"
    "v_mfma_f32_16x16x32_bf16 v[40:43], v[180:183], v[4:7], v[40:43]\n"
    "ds_read_b128 v[164:167], v200 offset:22528\n"
    "v_cvt_pk_bf16_f32 v146, v80, v81\n"
    "v_mfma_f32_16x16x32_bf16 v[40:43], v[176:179], v[0:3], v[40:43]\n"
    "v_exp_f32 v83, v83\n"
    "v_mfma_f32_16x16x32_bf16 v[40:43], v[172:175], a[252:255], v[40:43]\n"
    "buffer_load_dwordx4 v200, s[36:39], s65 offen offset:3072 sc1 lds\n"
    "v_mfma_f32_16x16x32_bf16 v[36:39], v[172:175], a[228:231], v[36:39]\n"
    "s_add_u32 s65, s61, 0xfffff800\n"
    "ds_read_b128 v[168:171], v200 offset:23552\n"
    "v_cvt_pk_bf16_f32 v147, v82, v83\n"
    "v_mfma_f32_16x16x32_bf16 v[36:39], v[176:179], a[232:235], v[36:39]\n"
    "s_mov_b32 m0, 0x10000\n"
    "v_exp_f32 v84, v84\n"
    "v_mfma_f32_16x16x32_bf16 v[36:39], v[180:183], a[236:239], v[36:39]\n"
    "v_exp_f32 v85, v85\n"
    "v_mfma_f32_16x16x32_bf16 v[32:35], v[180:183], a[212:215], v[32:35]\n"
    "buffer_load_dwordx4 v200, s[40:43], s65 offen sc1 lds\n"
    "v_mfma_f32_16x16x32_bf16 v[32:35], v[176:179], a[208:211], v[32:35]\n"
    "v_exp_f32 v86, v86\n"
    "v_mfma_f32_16x16x32_bf16 v[32:35], v[172:175], a[204:207], v[32:35]\n"
    "s_waitcnt lgkmcnt(1)\n"
    "v_exp_f32 v87, v87\n"
    "v_mfma_f32_16x16x32_bf16 v[48:51], v[160:163], a[204:207], v[96:99]\n"
    "ds_read_b128 v[172:175], v200 offset:18432\n"
    "v_cvt_pk_bf16_f32 v150, v84, v85\n"
    "v_mfma_f32_16x16x32_bf16 v[48:51], v[164:167], a[208:211], v[48:51]\n"
    "s_waitcnt lgkmcnt(1)\n"
    "buffer_load_dwordx4 v200, s[40:43], s65 offen offset:1024 sc1 lds\n"
    "v_mfma_f32_16x16x32_bf16 v[48:51], v[168:171], a[212:215], v[48:51]\n"
    "v_exp_f32 v88, v88\n"
    "v_mfma_f32_16x16x32_bf16 v[52:55], v[168:171], a[236:239], v[100:103]\n"
    "ds_read_b128 v[176:179], v200 offset:19456\n"
    "v_cvt_pk_bf16_f32 v151, v86, v87\n"
    "v_mfma_f32_16x16x32_bf16 v[52:55], v[164:167], a[232:235], v[52:55]\n"
    "v_exp_f32 v89, v89\n"
    "v_mfma_f32_16x16x32_bf16 v[52:55], v[160:163], a[228:231], v[52:55]\n"
    "buffer_load_dwordx4 v200, s[40:43], s65 offen offset:2048 sc1 lds\n"
    "v_mfma_f32_16x16x32_bf16 v[56:59], v[160:163], a[252:255], v[104:107]\n"
    "ds_read_b128 v[180:183], v200 offset:20480\n"
    "v_cvt_pk_bf16_f32 v154, v88, v89\n"
    "v_mfma_f32_16x16x32_bf16 v[56:59], v[164:167], v[0:3], v[56:59]\n"
    "v_exp_f32 v90, v90\n"
    "v_mfma_f32_16x16x32_bf16 v[56:59], v[168:171], v[4:7], v[56:59]\n"
    "v_exp_f32 v91, v91\n"
    "v_mfma_f32_16x16x32_bf16 v[60:63], v[168:171], v[28:31], v[108:111]\n"
    "buffer_load_dwordx4 v200, s[40:43], s65 offen offset:3072 sc1 lds\n"
    "v_mfma_f32_16x16x32_bf16 v[60:63], v[164:167], v[24:27], v[60:63]\n"
    "s_add_u32 s65, s61, 0x800\n"
    "v_exp_f32 v92, v92\n"
    "v_mfma_f32_16x16x32_bf16 v[60:63], v[160:163], v[20:23], v[60:63]\n"
    "s_waitcnt lgkmcnt(1)\n"
    "s_mov_b32 m0, 0x11000\n"
    "v_cvt_pk_bf16_f32 v155, v90, v91\n"
    "v_mfma_f32_16x16x32_bf16 v[60:63], v[172:175], v[8:11], v[60:63]\n"
    "ds_read_b128 v[160:163], v201 offset:24576\n"
    "v_add_f32 v114, v114, v65\n"
    "v_mfma_f32_16x16x32_bf16 v[60:63], v[176:179], v[12:15], v[60:63]\n"
    "s_waitcnt lgkmcnt(1)\n"
    "buffer_load_dwordx4 v200, s[40:43], s65 offen sc1 lds\n"
    "v_mfma_f32_16x16x32_bf16 v[60:63], v[180:183], v[16:19], v[60:63]\n"
    "v_exp_f32 v93, v93\n"
    "v_mfma_f32_16x16x32_bf16 v[56:59], v[180:183], a[248:251], v[56:59]\n"
    "ds_read_b128 v[164:167], v201 offset:25600\n"
    "v_cvt_pk_bf16_f32 v158, v92, v93\n"
    "v_mfma_f32_16x16x32_bf16 v[56:59], v[176:179], a[244:247], v[56:59]\n"
    "v_exp_f32 v94, v94\n"
    "v_mfma_f32_16x16x32_bf16 v[56:59], v[172:175], a[240:243], v[56:59]\n"
    "buffer_load_dwordx4 v200, s[40:43], s65 offen offset:1024 sc1 lds\n"
    "v_mfma_f32_16x16x32_bf16 v[52:55], v[172:175], a[216:219], v[52:55]\n"
    "ds_read_b128 v[168:171], v201 offset:26624\n"
    "v_add_f32 v113, v113, v66\n"
    "v_mfma_f32_16x16x32_bf16 v[52:55], v[176:179], a[220:223], v[52:55]\n"
    "v_exp_f32 v95, v95\n"
    "v_mfma_f32_16x16x32_bf16 v[52:55], v[180:183], a[224:227], v[52:55]\n"
    "v_cvt_pk_bf16_f32 v159, v94, v95\n"
    "v_add_f32 v114, v114, v67\n"
    "v_mfma_f32_16x16x32_bf16 v[48:51], v[180:183], a[200:203], v[48:51]\n"
    "buffer_load_dwordx4 v200, s[40:43], s65 offen offset:2048 sc1 lds\n"
    "v_mfma_f32_16x16x32_bf16 v[48:51], v[176:179], a[196:199], v[48:51]\n"
    "v_exp_f32 v32, v32\n"
    "v_mfma_f32_16x16x32_bf16 v[48:51], v[172:175], a[192:195], v[48:51]\n"
    "s_waitcnt lgkmcnt(1)\n"
    "v_exp_f32 v33, v33\n"
    "v_mfma_f32_16x16x32_bf16 a[0:3], v[160:163], v[128:131], a[0:3]\n"
    "ds_read_b128 v[172:175], v201 offset:27648\n"
    "ds_read_b128 v[176:179], v201 offset:28672\n"
    "v_mfma_f32_16x16x32_bf16 a[0:3], v[164:167], v[144:147], a[0:3]\n"
    "buffer_load_dwordx4 v200, s[40:43], s65 offen offset:3072 sc1 lds\n"
    "v_mfma_f32_16x16x32_bf16 a[4:7], v[160:163], v[132:135], a[4:7]\n"
    "v_exp_f32 v34, v34\n"
    "v_mfma_f32_16x16x32_bf16 a[4:7], v[164:167], v[148:151], a[4:7]\n"
    "v_exp_f32 v35, v35\n"
    "v_mfma_f32_16x16x32_bf16 a[8:11], v[160:163], v[136:139], a[8:11]\n"
    "ds_read_b128 v[180:183], v201 offset:29696\n"
    "v_add_f32 v117, v117, v68\n"
    "v_mfma_f32_16x16x32_bf16 a[8:11], v[164:167], v[152:155], a[8:11]\n"
    "v_exp_f32 v36, v36\n"
    "v_mfma_f32_16x16x32_bf16 a[12:15], v[160:163], v[140:143], a[12:15]\n"
    "v_exp_f32 v37, v37\n"
    "v_mfma_f32_16x16x32_bf16 a[12:15], v[164:167], v[156:159], a[12:15]\n"
    "s_waitcnt lgkmcnt(2)\n"
    "v_exp_f32 v38, v38\n"
    "v_mfma_f32_16x16x32_bf16 a[28:31], v[168:171], v[140:143], a[28:31]\n"
    "ds_read_b128 v[160:163], v201 offset:30720\n"
    "v_add_f32 v118, v118, v69\n"
    "v_mfma_f32_16x16x32_bf16 a[28:31], v[172:175], v[156:159], a[28:31]\n"
    "v_exp_f32 v39, v39\n"
    "v_mfma_f32_16x16x32_bf16 a[24:27], v[172:175], v[152:155], a[24:27]\n"
    "v_exp_f32 v40, v40\n"
    "v_mfma_f32_16x16x32_bf16 a[24:27], v[168:171], v[136:139], a[24:27]\n"
    "v_exp_f32 v41, v41\n"
    "v_mfma_f32_16x16x32_bf16 a[20:23], v[168:171], v[132:135], a[20:23]\n"
    "ds_read_b128 v[164:167], v201 offset:31744\n"
    "v_add_f32 v117, v117, v70\n"
    "v_mfma_f32_16x16x32_bf16 a[20:23], v[172:175], v[148:151], a[20:23]\n"
    "v_exp_f32 v42, v42\n"
    "v_mfma_f32_16x16x32_bf16 a[16:19], v[172:175], v[144:147], a[16:19]\n"
    "v_exp_f32 v43, v43\n"
    "v_mfma_f32_16x16x32_bf16 a[16:19], v[168:171], v[128:131], a[16:19]\n"
    "s_waitcnt lgkmcnt(2)\n"
    "v_exp_f32 v44, v44\n"
    "v_mfma_f32_16x16x32_bf16 a[32:35], v[176:179], v[128:131], a[32:35]\n"
    "ds_read_b128 v[168:171], v201 offset:32768\n"
    "v_add_f32 v113, v113, v32\n"
    "v_mfma_f32_16x16x32_bf16 a[32:35], v[180:183], v[144:147], a[32:35]\n"
    "v_exp_f32 v45, v45\n"
    "v_mfma_f32_16x16x32_bf16 a[36:39], v[180:183], v[148:151], a[36:39]\n"
    "v_exp_f32 v46, v46\n"
    "v_mfma_f32_16x16x32_bf16 a[36:39], v[176:179], v[132:135], a[36:39]\n"
    "v_exp_f32 v47, v47\n"
    "v_mfma_f32_16x16x32_bf16 a[40:43], v[176:179], v[136:139], a[40:43]\n"
    "ds_read_b128 v[172:175], v201 offset:33792\n"
    "v_add_f32 v118, v118, v71\n"
    "v_mfma_f32_16x16x32_bf16 a[40:43], v[180:183], v[152:155], a[40:43]\n"
    "v_exp_f32 v48, v48\n"
    "v_mfma_f32_16x16x32_bf16 a[44:47], v[180:183], v[156:159], a[44:47]\n"
    "v_exp_f32 v49, v49\n"
    "v_mfma_f32_16x16x32_bf16 a[44:47], v[176:179], v[140:143], a[44:47]\n"
    "s_waitcnt lgkmcnt(2)\n"
    "v_exp_f32 v50, v50\n"
    "v_mfma_f32_16x16x32_bf16 a[60:63], v[160:163], v[140:143], a[60:63]\n"
    "ds_read_b128 v[176:179], v201 offset:34816\n"
    "v_add_f32 v114, v114, v33\n"
    "v_mfma_f32_16x16x32_bf16 a[60:63], v[164:167], v[156:159], a[60:63]\n"
    "v_exp_f32 v51, v51\n"
    "v_mfma_f32_16x16x32_bf16 a[56:59], v[164:167], v[152:155], a[56:59]\n"
    "v_exp_f32 v52, v52\n"
    "v_mfma_f32_16x16x32_bf16 a[56:59], v[160:163], v[136:139], a[56:59]\n"
    "v_add_f32 v121, v121, v72\n"
    "v_add_f32 v113, v113, v34\n"
    "v_mfma_f32_16x16x32_bf16 a[52:55], v[160:163], v[132:135], a[52:55]\n"
    "ds_read_b128 v[180:183], v201 offset:35840\n"
    "v_add_f32 v122, v122, v73\n"
    "v_mfma_f32_16x16x32_bf16 a[52:55], v[164:167], v[148:151], a[52:55]\n"
    "v_exp_f32 v53, v53\n"
    "v_mfma_f32_16x16x32_bf16 a[48:51], v[164:167], v[144:147], a[48:51]\n"
    "v_add_f32 v114, v114, v35\n"
    "v_add_f32 v121, v121, v74\n"
    "v_mfma_f32_16x16x32_bf16 a[48:51], v[160:163], v[128:131], a[48:51]\n"
    "s_waitcnt lgkmcnt(2)\n"
    "v_add_f32 v117, v117, v36\n"
    "v_add_f32 v122, v122, v75\n"
    "v_mfma_f32_16x16x32_bf16 a[64:67], v[168:171], v[128:131], a[64:67]\n"
    "ds_read_b128 v[160:163], v201 offset:36864\n"
    "v_add_f32 v118, v118, v37\n"
    "v_mfma_f32_16x16x32_bf16 a[64:67], v[172:175], v[144:147], a[64:67]\n"
    "v_exp_f32 v54, v54\n"
    "v_mfma_f32_16x16x32_bf16 a[68:71], v[172:175], v[148:151], a[68:71]\n"
    "v_add_f32 v125, v125, v76\n"
    "v_add_f32 v117, v117, v38\n"
    "v_mfma_f32_16x16x32_bf16 a[68:71], v[168:171], v[132:135], a[68:71]\n"
    "s_add_u32 m0, s51, s95\n"
    "v_exp_f32 v55, v55\n"
    "v_mfma_f32_16x16x32_bf16 a[72:75], v[168:171], v[136:139], a[72:75]\n"
    "ds_read_b128 v[164:167], v201 offset:37888\n"
    "v_add_f32 v126, v126, v77\n"
    "v_mfma_f32_16x16x32_bf16 a[72:75], v[172:175], v[152:155], a[72:75]\n"
    "buffer_load_dwordx4 v202, s[32:35], s64 offen lds\n"
    "v_mfma_f32_16x16x32_bf16 a[76:79], v[172:175], v[156:159], a[76:79]\n"
    "v_add_f32 v118, v118, v39\n"
    "v_add_f32 v125, v125, v78\n"
    "v_mfma_f32_16x16x32_bf16 a[76:79], v[168:171], v[140:143], a[76:79]\n"
    "s_waitcnt lgkmcnt(2)\n"
    "v_add_f32 v121, v121, v40\n"
    "v_add_f32 v126, v126, v79\n"
    "v_mfma_f32_16x16x32_bf16 a[92:95], v[176:179], v[140:143], a[92:95]\n"
    "ds_read_b128 v[168:171], v201 offset:38912\n"
    "v_add_f32 v122, v122, v41\n"
    "v_mfma_f32_16x16x32_bf16 a[92:95], v[180:183], v[156:159], a[92:95]\n"
    "buffer_load_dwordx4 v203, s[32:35], s64 offen offset:1024 lds\n"
    "v_mfma_f32_16x16x32_bf16 a[88:91], v[180:183], v[152:155], a[88:91]\n"
    "v_exp_f32 v56, v56\n"
    "v_mfma_f32_16x16x32_bf16 a[88:91], v[176:179], v[136:139], a[88:91]\n"
    "v_add_f32 v113, v113, v80\n"
    "v_add_f32 v121, v121, v42\n"
    "v_mfma_f32_16x16x32_bf16 a[84:87], v[176:179], v[132:135], a[84:87]\n"
    "ds_read_b128 v[172:175], v201 offset:39936\n"
    "v_add_f32 v114, v114, v81\n"
    "v_mfma_f32_16x16x32_bf16 a[84:87], v[180:183], v[148:151], a[84:87]\n"
    "buffer_load_dwordx4 v204, s[32:35], s64 offen offset:2048 lds\n"
    "v_mfma_f32_16x16x32_bf16 a[80:83], v[180:183], v[144:147], a[80:83]\n"
    "v_exp_f32 v57, v57\n"
    "v_mfma_f32_16x16x32_bf16 a[80:83], v[176:179], v[128:131], a[80:83]\n"
    "s_waitcnt lgkmcnt(2)\n"
    "v_add_f32 v122, v122, v43\n"
    "v_add_f32 v113, v113, v82\n"
    "v_mfma_f32_16x16x32_bf16 a[96:99], v[160:163], v[128:131], a[96:99]\n"
    "s_mov_b32 s95, s94\n"
    "ds_read_b128 v[176:179], v201 offset:40960\n"
    "v_add_f32 v125, v125, v44\n"
    "v_mfma_f32_16x16x32_bf16 a[96:99], v[164:167], v[144:147], a[96:99]\n"
    "s_sub_u32 s94, s94, 0x3000\n"
    "v_exp_f32 v58, v58\n"
    "v_mfma_f32_16x16x32_bf16 a[100:103], v[164:167], v[148:151], a[100:103]\n"
    "s_min_u32 s94, s94, 0x6000\n"
    "v_add_f32 v114, v114, v83\n"
    "v_add_f32 v126, v126, v45\n"
    "v_mfma_f32_16x16x32_bf16 a[100:103], v[160:163], v[132:135], a[100:103]\n"
    "s_sub_u32 s93, s94, s95\n"
    "v_add_f32 v117, v117, v84\n"
    "v_add_f32 v125, v125, v46\n"
    "v_mfma_f32_16x16x32_bf16 a[104:107], v[160:163], v[136:139], a[104:107]\n"
    "ds_read_b128 v[180:183], v201 offset:41984\n"
    "v_add_f32 v118, v118, v85\n"
    "v_mfma_f32_16x16x32_bf16 a[104:107], v[164:167], v[152:155], a[104:107]\n"
    "v_exp_f32 v59, v59\n"
    "v_mfma_f32_16x16x32_bf16 a[108:111], v[164:167], v[156:159], a[108:111]\n"
    "v_add_f32 v126, v126, v47\n"
    "v_add_f32 v117, v117, v86\n"
    "v_mfma_f32_16x16x32_bf16 a[108:111], v[160:163], v[140:143], a[108:111]\n"
    "s_waitcnt lgkmcnt(2)\n"
    "v_add_f32 v113, v113, v48\n"
    "v_add_f32 v118, v118, v87\n"
    "v_mfma_f32_16x16x32_bf16 a[124:127], v[168:171], v[140:143], a[124:127]\n"
    "ds_read_b128 v[160:163], v201 offset:43008\n"
    "v_add_f32 v114, v114, v49\n"
    "v_mfma_f32_16x16x32_bf16 a[124:127], v[172:175], v[156:159], a[124:127]\n"
    "v_exp_f32 v60, v60\n"
    "v_mfma_f32_16x16x32_bf16 a[120:123], v[172:175], v[152:155], a[120:123]\n"
    "v_add_f32 v121, v121, v88\n"
    "v_add_f32 v113, v113, v50\n"
    "v_mfma_f32_16x16x32_bf16 a[120:123], v[168:171], v[136:139], a[120:123]\n"
    "v_exp_f32 v61, v61\n"
    "v_mfma_f32_16x16x32_bf16 a[116:119], v[168:171], v[132:135], a[116:119]\n"
    "ds_read_b128 v[164:167], v201 offset:44032\n"
    "v_add_u32 v207, s93, v207\n"
    "v_mfma_f32_16x16x32_bf16 a[116:119], v[172:175], v[148:151], a[116:119]\n"
    "v_add_f32 v122, v122, v89\n"
    "v_add_f32 v114, v114, v51\n"
    "v_mfma_f32_16x16x32_bf16 a[112:115], v[172:175], v[144:147], a[112:115]\n"
    "v_add_f32 v121, v121, v90\n"
    "v_add_f32 v117, v117, v52\n"
    "v_mfma_f32_16x16x32_bf16 a[112:115], v[168:171], v[128:131], a[112:115]\n"
    "s_waitcnt lgkmcnt(2)\n"
    "v_exp_f32 v62, v62\n"
    "v_mfma_f32_16x16x32_bf16 a[128:131], v[176:179], v[128:131], a[128:131]\n"
    "ds_read_b128 v[168:171], v201 offset:45056\n"
    "v_add_f32 v122, v122, v91\n"
    "v_mfma_f32_16x16x32_bf16 a[128:131], v[180:183], v[144:147], a[128:131]\n"
    "v_add_f32 v118, v118, v53\n"
    "v_add_f32 v125, v125, v92\n"
    "v_mfma_f32_16x16x32_bf16 a[132:135], v[180:183], v[148:151], a[132:135]\n"
    "v_add_f32 v117, v117, v54\n"
    "v_add_f32 v126, v126, v93\n"
    "v_mfma_f32_16x16x32_bf16 a[132:135], v[176:179], v[132:135], a[132:135]\n"
    "s_add_u32 s64, s64, 0xc000\n"
    "v_exp_f32 v63, v63\n"
    "v_mfma_f32_16x16x32_bf16 a[136:139], v[176:179], v[136:139], a[136:139]\n"
    "s_add_u32 s62, s62, 0x6000\n"
    "ds_read_b128 v[172:175], v201 offset:46080\n"
    "v_add_f32 v118, v118, v55\n"
    "v_mfma_f32_16x16x32_bf16 a[136:139], v[180:183], v[152:155], a[136:139]\n"
    "s_add_u32 s63, s63, 0x6000\n"
    "v_add_f32 v125, v125, v94\n"
    "v_add_f32 v121, v121, v56\n"
    "v_mfma_f32_16x16x32_bf16 a[140:143], v[180:183], v[156:159], a[140:143]\n"
    "s_add_u32 s60, s60, 0x6000\n"
    "v_add_f32 v126, v126, v95\n"
    "v_add_f32 v122, v122, v57\n"
    "v_mfma_f32_16x16x32_bf16 a[140:143], v[176:179], v[140:143], a[140:143]\n"
    "s_waitcnt lgkmcnt(2)\n"
    "v_mfma_f32_16x16x32_bf16 a[156:159], v[160:163], v[140:143], a[156:159]\n"
    "s_add_u32 s61, s61, 0x6000\n"
    "ds_read_b128 v[176:179], v201 offset:47104\n"
    "v_mfma_f32_16x16x32_bf16 a[156:159], v[164:167], v[156:159], a[156:159]\n"
    "ds_read_b128 v[180:183], v201 offset:48128\n"
    "v_mfma_f32_16x16x32_bf16 a[152:155], v[164:167], v[152:155], a[152:155]\n"
    "s_waitcnt vmcnt(3)\n"
    "v_mfma_f32_16x16x32_bf16 a[152:155], v[160:163], v[136:139], a[152:155]\n"
    "v_mfma_f32_16x16x32_bf16 a[148:151], v[160:163], v[132:135], a[148:151]\n"
    "v_mfma_f32_16x16x32_bf16 a[148:151], v[164:167], v[148:151], a[148:151]\n"
    "v_mfma_f32_16x16x32_bf16 a[144:147], v[164:167], v[144:147], a[144:147]\n"
    "v_mfma_f32_16x16x32_bf16 a[144:147], v[160:163], v[128:131], a[144:147]\n"
    "s_waitcnt lgkmcnt(0)\n"
    "v_mfma_f32_16x16x32_bf16 a[160:163], v[168:171], v[128:131], a[160:163]\n"
    "s_barrier\n"
    "LMID2:\n"
    "v_mfma_f32_16x16x32_bf16 a[160:163], v[172:175], v[144:147], a[160:163]\n"
    "v_add_f32 v121, v121, v58\n"
    "v_add_f32 v122, v122, v59\n"
    "v_mfma_f32_16x16x32_bf16 a[164:167], v[172:175], v[148:151], a[164:167]\n"
    "ds_read_b128 v[160:163], v200 offset:24576\n"
    "v_add_f32 v125, v125, v60\n"
    "v_mfma_f32_16x16x32_bf16 a[164:167], v[168:171], v[132:135], a[164:167]\n"
    "v_add_f32 v126, v126, v61\n"
    "v_add_f32 v125, v125, v62\n"
    "v_mfma_f32_16x16x32_bf16 a[168:171], v[168:171], v[136:139], a[168:171]\n"
    "ds_read_b128 v[164:167], v200 offset:25600\n"
    "v_add_f32 v126, v126, v63\n"
    "v_mfma_f32_16x16x32_bf16 a[168:171], v[172:175], v[152:155], a[168:171]\n"
    "ds_read_b64 v[228:229], v210 offset:64\n"
    "ds_read2st64_b32 v[240:241], v207 offset0:96 offset1:99\n"
    "v_mfma_f32_16x16x32_bf16 a[172:175], v[172:175], v[156:159], a[172:175]\n"
    "ds_read2st64_b32 v[242:243], v207 offset0:102 offset1:105\n"
    "ds_read2st64_b32 v[244:245], v207 offset0:108 offset1:111\n"
    "v_mfma_f32_16x16x32_bf16 a[172:175], v[168:171], v[140:143], a[172:175]\n"
    "ds_read2st64_b32 v[246:247], v207 offset0:114 offset1:117\n"
    "v_mfma_f32_16x16x32_bf16 a[188:191], v[176:179], v[140:143], a[188:191]\n"
    "ds_read_b128 v[168:171], v200 offset:26624\n"
    "v_cvt_pk_bf16_f32 v140, v44, v45\n"
    "v_mfma_f32_16x16x32_bf16 a[188:191], v[180:183], v[156:159], a[188:191]\n"
    "v_cvt_pk_bf16_f32 v141, v46, v47\n"
    "v_mfma_f32_16x16x32_bf16 a[184:187], v[176:179], v[136:139], a[184:187]\n"
    "v_cvt_pk_bf16_f32 v136, v40, v41\n"
    "v_cvt_pk_bf16_f32 v137, v42, v43\n"
    "v_mfma_f32_16x16x32_bf16 a[184:187], v[180:183], v[152:155], a[184:187]\n"
    "v_mfma_f32_16x16x32_bf16 a[180:183], v[176:179], v[132:135], a[180:183]\n"
    "s_add_u32 s59, s59, 1\n"
    "ds_read_b128 v[172:175], v200 offset:27648\n"
    "v_cvt_pk_bf16_f32 v132, v36, v37\n"
    "v_mfma_f32_16x16x32_bf16 a[180:183], v[180:183], v[148:151], a[180:183]\n"
    "v_cvt_pk_bf16_f32 v133, v38, v39\n"
    "v_mfma_f32_16x16x32_bf16 a[176:179], v[176:179], v[128:131], a[176:179]\n"
    "s_cmp_lt_u32 s59, 15\n"
    "s_waitcnt lgkmcnt(5)\n"
    "v_cvt_f32_i32 v240, v240\n"
    "v_cvt_f32_i32 v241, v241\n"
    "v_mfma_f32_16x16x32_bf16 a[176:179], v[180:183], v[144:147], a[176:179]\n"
    "s_cbranch_scc1 LLOOP2\n"
    "s_branch LTAIL\n"
    "LENT1:\n"
    "s_mov_b32 s59, 1\n"
    "s_add_u32 m0, s72, 0x21000\n"
    "s_nop 0\n"
    "buffer_load_dwordx4 v250, s[48:51], 0 offen offset:2048 lds\n"
    "s_add_u32 m0, s72, 0x18000\n"
    "s_nop 0\n"
    "buffer_load_dwordx4 v202, s[32:35], s64 offen lds\n"
    "buffer_load_dwordx4 v203, s[32:35], s64 offen offset:1024 lds\n"
    "buffer_load_dwordx4 v204, s[32:35], s64 offen offset:2048 lds\n"
    "s_add_u32 s64, s64, 0xc000\n"
    "s_mov_b32 s94, 0x3000\n"
    "s_mov_b32 s95, 0x6000\n"
    "s_add_u32 s51, s72, 0x18000\n"
    "v_add_u32 v205, 0x3000, v205\n"
    "v_add_u32 v206, 0x3000, v206\n"
    "v_add_u32 v207, 0x3000, v207\n"
    "ds_read_u16_d16_hi v223, v209 offset:6336\n"
    "ds_read_u16_d16_hi v222, v209 offset:192\n"
    "ds_read_b128 v[160:163], v200 offset:24576\n"
    "v_add_f32 v121, v121, v58\n"
    "ds_read_b128 v[232:235], v205 offset:24576\n"
    "v_add_f32 v122, v122, v59\n"
    "ds_read_b128 v[164:167], v200 offset:25600\n"
    "ds_read_b128 v[236:239], v206 offset:24576\n"
    "v_add_f32 v125, v125, v60\n"
    "v_add_f32 v126, v126, v61\n"
    "v_add_f32 v125, v125, v62\n"
    "v_add_f32 v126, v126, v63\n"
    "ds_read_b64 v[228:229], v210 offset:64\n"
    "ds_read2st64_b32 v[240:241], v207 offset0:96 offset1:99\n"
    "ds_read_b128 v[168:171], v200 offset:26624\n"
    "ds_read2st64_b32 v[242:243], v207 offset0:102 offset1:105\n"
    "ds_read2st64_b32 v[244:245], v207 offset0:108 offset1:111\n"
    "ds_read2st64_b32 v[246:247], v207 offset0:114 offset1:117\n"
    "v_cvt_pk_bf16_f32 v140, v44, v45\n"
    "v_cvt_pk_bf16_f32 v141, v46, v47\n"
    "s_waitcnt lgkmcnt(6)\n"
    "v_mul_f32 v226, s75, v223\n"
    "ds_read_b128 v[172:175], v200 offset:27648\n"
    "v_fma_f32 v227, -v222, v226, v217\n"
    "v_cvt_f32_i32 v232, v232\n"
    "v_cvt_f32_i32 v233, v233\n"
    "v_cvt_f32_i32 v234, v234\n"
    "v_cvt_f32_i32 v235, v235\n"
    "s_nop 7\n"
    ".p2align 6\n"
    "LLOOP1:\n"
    "v_mfma_f32_16x16x32_bf16 v[64:67], v[160:163], a[192:195], v[96:99]\n"
    "ds_read_b128 v[176:179], v200 offset:28672\n"
    "v_cvt_f32_i32 v236, v236\n"
    "v_mfma_f32_16x16x32_bf16 v[64:67], v[164:167], a[196:199], v[64:67]\n"
    "s_waitcnt lgkmcnt(1)\n"
    "v_cvt_f32_i32 v237, v237\n"
    "v_cvt_f32_i32 v238, v238\n"
    "v_mfma_f32_16x16x32_bf16 v[64:67], v[168:171], a[200:203], v[64:67]\n"
    "v_cvt_f32_i32 v239, v239\n"
    "v_mul_f32_dpp v232, v226, v232 row_newbcast:0 row_mask:0xf bank_mask:0xf\n"
    "v_mfma_f32_16x16x32_bf16 v[68:71], v[168:171], a[224:227], v[100:103]\n"
    "v_mul_f32_dpp v233, v226, v233 row_newbcast:1 row_mask:0xf bank_mask:0xf\n"
    "v_mul_f32_dpp v234, v226, v234 row_newbcast:2 row_mask:0xf bank_mask:0xf\n"
    "v_mfma_f32_16x16x32_bf16 v[68:71], v[164:167], a[220:223], v[68:71]\n"
    "ds_read_b128 v[180:183], v200 offset:29696\n"
    "v_mul_f32_dpp v235, v226, v235 row_newbcast:3 row_mask:0xf bank_mask:0xf\n"
    "v_mfma_f32_16x16x32_bf16 v[68:71], v[160:163], a[216:219], v[68:71]\n"
    "v_mul_f32_dpp v236, v226, v236 row_newbcast:4 row_mask:0xf bank_mask:0xf\n"
    "v_cvt_f32_i32 v240, v240\n"
    "v_mfma_f32_16x16x32_bf16 v[72:75], v[160:163], a[240:243], v[104:107]\n"
    "v_mul_f32_dpp v237, v226, v237 row_newbcast:5 row_mask:0xf bank_mask:0xf\n"
    "v_cvt_f32_i32 v241, v241\n"
    "v_mfma_f32_16x16x32_bf16 v[72:75], v[164:167], a[244:247], v[72:75]\n"
    "v_mul_f32_dpp v238, v226, v238 row_newbcast:6 row_mask:0xf bank_mask:0xf\n"
    "v_cvt_f32_i32 v242, v242\n"
    "v_mfma_f32_16x16x32_bf16 v[72:75], v[168:171], a[248:251], v[72:75]\n"
    "v_mul_f32_dpp v239, v226, v239 row_newbcast:7 row_mask:0xf bank_mask:0xf\n"
    "v_cvt_f32_i32 v243, v243\n"
    "v_mfma_f32_16x16x32_bf16 v[76:79], v[168:171], v[16:19], v[108:111]\n"
    "v_add_f32_dpp v232, v227, v232 row_newbcast:0 row_mask:0xf bank_mask:0xf\n"
    "v_cvt_f32_i32 v244, v244\n"
    "v_mfma_f32_16x16x32_bf16 v[76:79], v[164:167], v[12:15], v[76:79]\n"
    "v_add_f32_dpp v233, v227, v233 row_newbcast:1 row_mask:0xf bank_mask:0xf\n"
    "v_cvt_f32_i32 v245, v245\n"
    "v_mfma_f32_16x16x32_bf16 v[76:79], v[160:163], v[8:11], v[76:79]\n"
    "s_waitcnt lgkmcnt(1)\n"
    "v_add_f32_dpp v234, v227, v234 row_newbcast:2 row_mask:0xf bank_mask:0xf\n"
    "v_cvt_f32_i32 v246, v246\n"
    "v_mfma_f32_16x16x32_bf16 v[76:79], v[172:175], v[20:23], v[76:79]\n"
    "ds_read_b128 v[160:163], v200 offset:33792\n"
    "v_add_f32_dpp v235, v227, v235 row_newbcast:3 row_mask:0xf bank_mask:0xf\n"
    "v_mfma_f32_16x16x32_bf16 v[76:79], v[176:179], v[24:27], v[76:79]\n"
    "s_waitcnt lgkmcnt(1)\n"
    "v_cvt_f32_i32 v247, v247\n"
    "v_add_f32_dpp v236, v227, v236 row_newbcast:4 row_mask:0xf bank_mask:0xf\n"
    "v_mfma_f32_16x16x32_bf16 v[76:79], v[180:183], v[28:31], v[76:79]\n"
    "v_mul_f32_dpp v240, v228, v240 row_newbcast:0 row_mask:0xf bank_mask:0xf\n"
    "v_add_f32_dpp v237, v227, v237 row_newbcast:5 row_mask:0xf bank_mask:0xf\n"
    "v_mfma_f32_16x16x32_bf16 v[72:75], v[180:183], v[4:7], v[72:75]\n"
    "ds_read_b128 v[164:167], v200 offset:34816\n"
    "v_mul_f32_dpp v241, v228, v241 row_newbcast:1 row_mask:0xf bank_mask:0xf\n"
    "v_mfma_f32_16x16x32_bf16 v[72:75], v[176:179], v[0:3], v[72:75]\n"
    "v_add_f32_dpp v238, v227, v238 row_newbcast:6 row_mask:0xf bank_mask:0xf\n"
    "v_mul_f32_dpp v242, v228, v242 row_newbcast:2 row_mask:0xf bank_mask:0xf\n"
    "v_mfma_f32_16x16x32_bf16 v[72:75], v[172:175], a[252:255], v[72:75]\n"
    "v_add_f32_dpp v239, v227, v239 row_newbcast:7 row_mask:0xf bank_mask:0xf\n"
    "v_mul_f32_dpp v243, v228, v243 row_newbcast:3 row_mask:0xf bank_mask:0xf\n"
    "v_mfma_f32_16x16x32_bf16 v[68:71], v[172:175], a[228:231], v[68:71]\n"
    "ds_read_b128 v[168:171], v200 offset:35840\n"
    "v_cvt_pk_bf16_f32 v232, v232, v233\n"
    "v_mfma_f32_16x16x32_bf16 v[68:71], v[176:179], a[232:235], v[68:71]\n"
    "v_mul_f32_dpp v244, v228, v244 row_newbcast:4 row_mask:0xf bank_mask:0xf\n"
    "v_cvt_pk_bf16_f32 v233, v234, v235\n"
    "v_mfma_f32_16x16x32_bf16 v[68:71], v[180:183], a[236:239], v[68:71]\n"
    "v_mul_f32_dpp v245, v228, v245 row_newbcast:5 row_mask:0xf bank_mask:0xf\n"
    "v_cvt_pk_bf16_f32 v234, v236, v237\n"
    "v_mfma_f32_16x16x32_bf16 v[64:67], v[180:183], a[212:215], v[64:67]\n"
    "v_mul_f32_dpp v246, v228, v246 row_newbcast:6 row_mask:0xf bank_mask:0xf\n"
    "v_cvt_pk_bf16_f32 v235, v238, v239\n"
    "v_mfma_f32_16x16x32_bf16 v[64:67], v[176:179], a[208:211], v[64:67]\n"
    "v_mul_f32_dpp v247, v228, v247 row_newbcast:7 row_mask:0xf bank_mask:0xf\n"
    "v_add_f32_dpp v240, v229, v240 row_newbcast:0 row_mask:0xf bank_mask:0xf\n"
    "v_mfma_f32_16x16x32_bf16 v[64:67], v[172:175], a[204:207], v[64:67]\n"
    "s_waitcnt lgkmcnt(1)\n"
    "v_add_f32_dpp v241, v229, v241 row_newbcast:1 row_mask:0xf bank_mask:0xf\n"
    "v_add_f32_dpp v242, v229, v242 row_newbcast:2 row_mask:0xf bank_mask:0xf\n"
    "v_mfma_f32_16x16x32_bf16 v[80:83], v[160:163], a[204:207], v[96:99]\n"
    "ds_read_b128 v[172:175], v200 offset:30720\n"
    "v_add_f32_dpp v243, v229, v243 row_newbcast:3 row_mask:0xf bank_mask:0xf\n"
    "v_mfma_f32_16x16x32_bf16 v[80:83], v[164:167], a[208:211], v[80:83]\n"
    "s_waitcnt lgkmcnt(1)\n"
    "buffer_store_dwordx4 v[232:235], v211, s[36:39], s62 offen sc1\n"
    "v_add_f32_dpp v244, v229, v244 row_newbcast:4 row_mask:0xf bank_mask:0xf\n"
    "v_add_f32_dpp v245, v229, v245 row_newbcast:5 row_mask:0xf bank_mask:0xf\n"
    "v_add_f32_dpp v246, v229, v246 row_newbcast:6 row_mask:0xf bank_mask:0xf\n"
    "v_mfma_f32_16x16x32_bf16 v[80:83], v[168:171], a[212:215], v[80:83]\n"
    "v_add_f32_dpp v247, v229, v247 row_newbcast:7 row_mask:0xf bank_mask:0xf\n"
    "v_cvt_pk_bf16_f32 v240, v240, v241\n"
    "v_mfma_f32_16x16x32_bf16 v[84:87], v[168:171], a[236:239], v[100:103]\n"
    "ds_read_b128 v[176:179], v200 offset:31744\n"
    "v_cvt_pk_bf16_f32 v241, v242, v243\n"
    "v_mfma_f32_16x16x32_bf16 v[84:87], v[164:167], a[232:235], v[84:87]\n"
    "v_cvt_pk_bf16_f32 v242, v244, v245\n"
    "v_cvt_pk_bf16_f32 v243, v246, v247\n"
    "v_mfma_f32_16x16x32_bf16 v[84:87], v[160:163], a[228:231], v[84:87]\n"
    "buffer_store_dwordx4 v[240:243], v212, s[40:43], s63 offen sc1\n"
    "v_cvt_pk_bf16_f32 v136, v40, v41\n"
    "v_cvt_pk_bf16_f32 v137, v42, v43\n"
    "v_cvt_pk_bf16_f32 v132, v36, v37\n"
    "v_mfma_f32_16x16x32_bf16 v[88:91], v[160:163], a[252:255], v[104:107]\n"
    "ds_read_b128 v[180:183], v200 offset:32768\n"
    "v_cvt_pk_bf16_f32 v133, v38, v39\n"
    "v_mfma_f32_16x16x32_bf16 v[88:91], v[164:167], v[0:3], v[88:91]\n"
    "v_cvt_pk_bf16_f32 v128, v32, v33\n"
    "v_cvt_pk_bf16_f32 v129, v34, v35\n"
    "v_mfma_f32_16x16x32_bf16 v[88:91], v[168:171], v[4:7], v[88:91]\n"
    "v_exp_f32 v64, v64\n"
    "v_mfma_f32_16x16x32_bf16 v[92:95], v[168:171], v[28:31], v[108:111]\n"
    "v_exp_f32 v65, v65\n"
    "v_mfma_f32_16x16x32_bf16 v[92:95], v[164:167], v[24:27], v[92:95]\n"
    "v_exp_f32 v66, v66\n"
    "v_mfma_f32_16x16x32_bf16 v[92:95], v[160:163], v[20:23], v[92:95]\n"
    "s_waitcnt lgkmcnt(1)\n"
    "v_exp_f32 v67, v67\n"
    "v_mfma_f32_16x16x32_bf16 v[92:95], v[172:175], v[8:11], v[92:95]\n"
    "ds_read_b128 v[160:163], v200 offset:36864\n"
    "v_cvt_pk_bf16_f32 v142, v60, v61\n"
    "v_mfma_f32_16x16x32_bf16 v[92:95], v[176:179], v[12:15], v[92:95]\n"
    "s_waitcnt lgkmcnt(1)\n"
    "v_exp_f32 v68, v68\n"
    "v_mfma_f32_16x16x32_bf16 v[92:95], v[180:183], v[16:19], v[92:95]\n"
    "v_exp_f32 v69, v69\n"
    "v_mfma_f32_16x16x32_bf16 v[88:91], v[180:183], a[248:251], v[88:91]\n"
    "ds_read_b128 v[164:167], v200 offset:37888\n"
    "v_cvt_pk_bf16_f32 v143, v62, v63\n"
    "v_mfma_f32_16x16x32_bf16 v[88:91], v[176:179], a[244:247], v[88:91]\n"
    "v_exp_f32 v70, v70\n"
    "v_mfma_f32_16x16x32_bf16 v[88:91], v[172:175], a[240:243], v[88:91]\n"
    "v_exp_f32 v71, v71\n"
    "v_mfma_f32_16x16x32_bf16 v[84:87], v[172:175], a[216:219], v[84:87]\n"
    "ds_read_b128 v[168:171], v200 offset:38912\n"
    "v_cvt_pk_bf16_f32 v138, v56, v57\n"
    "v_mfma_f32_16x16x32_bf16 v[84:87], v[176:179], a[220:223], v[84:87]\n"
    "v_cvt_pk_bf16_f32 v139, v58, v59\n"
    "v_cvt_pk_bf16_f32 v134, v52, v53\n"
    "v_mfma_f32_16x16x32_bf16 v[84:87], v[180:183], a[224:227], v[84:87]\n"
    "v_exp_f32 v72, v72\n"
    "v_mfma_f32_16x16x32_bf16 v[80:83], v[180:183], a[200:203], v[80:83]\n"
    "v_cvt_pk_bf16_f32 v135, v54, v55\n"
    "v_cvt_pk_bf16_f32 v130, v48, v49\n"
    "v_mfma_f32_16x16x32_bf16 v[80:83], v[176:179], a[196:199], v[80:83]\n"
    "v_exp_f32 v73, v73\n"
    "v_mfma_f32_16x16x32_bf16 v[80:83], v[172:175], a[192:195], v[80:83]\n"
    "s_waitcnt lgkmcnt(1)\n"
    "v_cvt_pk_bf16_f32 v131, v50, v51\n"
    "v_cvt_pk_bf16_f32 v144, v64, v65\n"
    "v_mfma_f32_16x16x32_bf16 v[32:35], v[160:163], a[192:195], v[96:99]\n"
    "ds_read_b128 v[172:175], v200 offset:39936\n"
    "v_cvt_pk_bf16_f32 v145, v66, v67\n"
    "v_mfma_f32_16x16x32_bf16 v[32:35], v[164:167], a[196:199], v[32:35]\n"
    "s_waitcnt lgkmcnt(1)\n"
    "s_add_u32 s65, s60, s92\n"
    "v_cvt_pk_bf16_f32 v148, v68, v69\n"
    "v_mfma_f32_16x16x32_bf16 v[32:35], v[168:171], a[200:203], v[32:35]\n"
    "s_add_u32 m0, s90, 0\n"
    "v_exp_f32 v74, v74\n"
    "v_mfma_f32_16x16x32_bf16 v[36:39], v[168:171], a[224:227], v[100:103]\n"
    "ds_read_b128 v[176:179], v200 offset:40960\n"
    "v_cvt_pk_bf16_f32 v149, v70, v71\n"
    "v_mfma_f32_16x16x32_bf16 v[36:39], v[164:167], a[220:223], v[36:39]\n"
    "buffer_load_dwordx4 v200, s[36:39], s65 offen sc1 lds\n"
    "v_mfma_f32_16x16x32_bf16 v[36:39], v[160:163], a[216:219], v[36:39]\n"
    "v_exp_f32 v75, v75\n"
    "v_mfma_f32_16x16x32_bf16 v[40:43], v[160:163], a[240:243], v[104:107]\n"
    "ds_read_b128 v[180:183], v200 offset:41984\n"
    "v_cvt_pk_bf16_f32 v152, v72, v73\n"
    "v_mfma_f32_16x16x32_bf16 v[40:43], v[164:167], a[244:247], v[40:43]\n"
    "v_exp_f32 v76, v76\n"
    "v_mfma_f32_16x16x32_bf16 v[40:43], v[168:171], a[248:251], v[40:43]\n"
    "buffer_load_dwordx4 v200, s[36:39], s65 offen offset:1024 sc1 lds\n"
    "v_mfma_f32_16x16x32_bf16 v[44:47], v[168:171], v[16:19], v[108:111]\n"
    "v_exp_f32 v77, v77\n"
    "v_mfma_f32_16x16x32_bf16 v[44:47], v[164:167], v[12:15], v[44:47]\n"
    "v_exp_f32 v78, v78\n"
    "v_mfma_f32_16x16x32_bf16 v[44:47], v[160:163], v[8:11], v[44:47]\n"
    "s_waitcnt lgkmcnt(1)\n"
    "v_exp_f32 v79, v79\n"
    "v_mfma_f32_16x16x32_bf16 v[44:47], v[172:175], v[20:23], v[44:47]\n"
    "ds_read_b128 v[160:163], v200 offset:46080\n"
    "v_cvt_pk_bf16_f32 v153, v74, v75\n"
    "v_mfma_f32_16x16x32_bf16 v[44:47], v[176:179], v[24:27], v[44:47]\n"
    "s_waitcnt lgkmcnt(1)\n"
    "buffer_load_dwordx4 v200, s[36:39], s65 offen offset:2048 sc1 lds\n"
    "v_mfma_f32_16x16x32_bf16 v[44:47], v[180:183], v[28:31], v[44:47]\n"
    "v_exp_f32 v80, v80\n"
    "v_mfma_f32_16x16x32_bf16 v[40:43], v[180:183], v[4:7], v[40:43]\n"
    "ds_read_b128 v[164:167], v200 offset:47104\n"
    "v_cvt_pk_bf16_f32 v156, v76, v77\n"
    "v_mfma_f32_16x16x32_bf16 v[40:43], v[176:179], v[0:3], v[40:43]\n"
    "v_exp_f32 v81, v81\n"
    "v_mfma_f32_16x16x32_bf16 v[40:43], v[172:175], a[252:255], v[40:43]\n"
    "buffer_load_dwordx4 v200, s[36:39], s65 offen offset:3072 sc1 lds\n"
    "v_mfma_f32_16x16x32_bf16 v[36:39], v[172:175], a[228:231], v[36:39]\n"
    "s_add_u32 s65, s61, s92\n"
    "ds_read_b128 v[168:171], v200 offset:48128\n"
    "v_cvt_pk_bf16_f32 v157, v78, v79\n"
    "v_mfma_f32_16x16x32_bf16 v[36:39], v[176:179], a[232:235], v[36:39]\n"
    "s_add_u32 m0, s90, 0x12000\n"
    "v_exp_f32 v82, v82\n"
    "v_mfma_f32_16x16x32_bf16 v[36:39], v[180:183], a[236:239], v[36:39]\n"
    "v_exp_f32 v83, v83\n"
    "v_mfma_f32_16x16x32_bf16 v[32:35], v[180:183], a[212:215], v[32:35]\n"
    "buffer_load_dwordx4 v200, s[40:43], s65 offen sc1 lds\n"
    "v_mfma_f32_16x16x32_bf16 v[32:35], v[176:179], a[208:211], v[32:35]\n"
    "v_exp_f32 v84, v84\n"
    "v_mfma_f32_16x16x32_bf16 v[32:35], v[172:175], a[204:207], v[32:35]\n"
    "s_waitcnt lgkmcnt(1)\n"
    "v_exp_f32 v85, v85\n"
    "v_mfma_f32_16x16x32_bf16 v[48:51], v[160:163], a[204:207], v[96:99]\n"
    "ds_read_b128 v[172:175], v200 offset:43008\n"
    "v_cvt_pk_bf16_f32 v146, v80, v81\n"
    "v_mfma_f32_16x16x32_bf16 v[48:51], v[164:167], a[208:211], v[48:51]\n"
    "s_waitcnt lgkmcnt(1)\n"
    "buffer_load_dwordx4 v200, s[40:43], s65 offen offset:1024 sc1 lds\n"
    "v_mfma_f32_16x16x32_bf16 v[48:51], v[168:171], a[212:215], v[48:51]\n"
    "v_exp_f32 v86, v86\n"
    "v_mfma_f32_16x16x32_bf16 v[52:55], v[168:171], a[236:239], v[100:103]\n"
    "ds_read_b128 v[176:179], v200 offset:44032\n"
    "v_cvt_pk_bf16_f32 v147, v82, v83\n"
    "v_mfma_f32_16x16x32_bf16 v[52:55], v[164:167], a[232:235], v[52:55]\n"
    "v_exp_f32 v87, v87\n"
    "v_mfma_f32_16x16x32_bf16 v[52:55], v[160:163], a[228:231], v[52:55]\n"
    "buffer_load_dwordx4 v200, s[40:43], s65 offen offset:2048 sc1 lds\n"
    "v_mfma_f32_16x16x32_bf16 v[56:59], v[160:163], a[252:255], v[104:107]\n"
    "ds_read_b128 v[180:183], v200 offset:45056\n"
    "v_cvt_pk_bf16_f32 v150, v84, v85\n"
    "v_mfma_f32_16x16x32_bf16 v[56:59], v[164:167], v[0:3], v[56:59]\n"
    "v_exp_f32 v88, v88\n"
    "v_mfma_f32_16x16x32_bf16 v[56:59], v[168:171], v[4:7], v[56:59]\n"
    "v_exp_f32 v89, v89\n"
    "v_mfma_f32_16x16x32_bf16 v[60:63], v[168:171], v[28:31], v[108:111]\n"
    "buffer_load_dwordx4 v200, s[40:43], s65 offen offset:3072 sc1 lds\n"
    "v_mfma_f32_16x16x32_bf16 v[60:63], v[164:167], v[24:27], v[60:63]\n"
    "v_exp_f32 v90, v90\n"
    "v_mfma_f32_16x16x32_bf16 v[60:63], v[160:163], v[20:23], v[60:63]\n"
    "s_waitcnt lgkmcnt(1)\n"
    "v_exp_f32 v91, v91\n"
    "v_mfma_f32_16x16x32_bf16 v[60:63], v[172:175], v[8:11], v[60:63]\n"
    "ds_read_b128 v[160:163], v201\n"
    "v_cvt_pk_bf16_f32 v151, v86, v87\n"
    "v_mfma_f32_16x16x32_bf16 v[60:63], v[176:179], v[12:15], v[60:63]\n"
    "s_waitcnt lgkmcnt(1)\n"
    "v_exp_f32 v92, v92\n"
    "v_mfma_f32_16x16x32_bf16 v[60:63], v[180:183], v[16:19], v[60:63]\n"
    "v_exp_f32 v93, v93\n"
    "v_mfma_f32_16x16x32_bf16 v[56:59], v[180:183], a[248:251], v[56:59]\n"
    "ds_read_b128 v[164:167], v201 offset:1024\n"
    "v_cvt_pk_bf16_f32 v154, v88, v89\n"
    "v_mfma_f32_16x16x32_bf16 v[56:59], v[176:179], a[244:247], v[56:59]\n"
    "v_exp_f32 v94, v94\n"
    "v_mfma_f32_16x16x32_bf16 v[56:59], v[172:175], a[240:243], v[56:59]\n"
    "v_cvt_pk_bf16_f32 v155, v90, v91\n"
    "v_cvt_pk_bf16_f32 v158, v92, v93\n"
    "v_mfma_f32_16x16x32_bf16 v[52:55], v[172:175], a[216:219], v[52:55]\n"
    "ds_read_b128 v[168:171], v201 offset:2048\n"
    "v_add_f32 v113, v113, v64\n"
    "v_mfma_f32_16x16x32_bf16 v[52:55], v[176:179], a[220:223], v[52:55]\n"
    "v_exp_f32 v95, v95\n"
    "v_mfma_f32_16x16x32_bf16 v[52:55], v[180:183], a[224:227], v[52:55]\n"
    "v_cvt_pk_bf16_f32 v159, v94, v95\n"
    "v_add_f32 v114, v114, v65\n"
    "v_mfma_f32_16x16x32_bf16 v[48:51], v[180:183], a[200:203], v[48:51]\n"
    "v_exp_f32 v32, v32\n"
    "v_mfma_f32_16x16x32_bf16 v[48:51], v[176:179], a[196:199], v[48:51]\n"
    "v_exp_f32 v33, v33\n"
    "v_mfma_f32_16x16x32_bf16 v[48:51], v[172:175], a[192:195], v[48:51]\n"
    "s_waitcnt lgkmcnt(1)\n"
    "v_exp_f32 v34, v34\n"
    "v_mfma_f32_16x16x32_bf16 a[0:3], v[160:163], v[128:131], a[0:3]\n"
    "ds_read_b128 v[172:175], v201 offset:3072\n"
    "ds_read_b128 v[176:179], v201 offset:4096\n"
    "v_mfma_f32_16x16x32_bf16 a[0:3], v[164:167], v[144:147], a[0:3]\n"
    "v_exp_f32 v35, v35\n"
    "v_mfma_f32_16x16x32_bf16 a[4:7], v[160:163], v[132:135], a[4:7]\n"
    "v_exp_f32 v36, v36\n"
    "v_mfma_f32_16x16x32_bf16 a[4:7], v[164:167], v[148:151], a[4:7]\n"
    "v_exp_f32 v37, v37\n"
    "v_mfma_f32_16x16x32_bf16 a[8:11], v[160:163], v[136:139], a[8:11]\n"
    "ds_read_b128 v[180:183], v201 offset:5120\n"
    "v_add_f32 v113, v113, v66\n"
    "v_mfma_f32_16x16x32_bf16 a[8:11], v[164:167], v[152:155], a[8:11]\n"
    "v_exp_f32 v38, v38\n"
    "v_mfma_f32_16x16x32_bf16 a[12:15], v[160:163], v[140:143], a[12:15]\n"
    "v_exp_f32 v39, v39\n"
    "v_mfma_f32_16x16x32_bf16 a[12:15], v[164:167], v[156:159], a[12:15]\n"
    "s_waitcnt lgkmcnt(2)\n"
    "v_exp_f32 v40, v40\n"
    "v_mfma_f32_16x16x32_bf16 a[28:31], v[168:171], v[140:143], a[28:31]\n"
    "ds_read_b128 v[160:163], v201 offset:6144\n"
    "v_add_f32 v114, v114, v67\n"
    "v_mfma_f32_16x16x32_bf16 a[28:31], v[172:175], v[156:159], a[28:31]\n"
    "v_exp_f32 v41, v41\n"
    "v_mfma_f32_16x16x32_bf16 a[24:27], v[172:175], v[152:155], a[24:27]\n"
    "v_exp_f32 v42, v42\n"
    "v_mfma_f32_16x16x32_bf16 a[24:27], v[168:171], v[136:139], a[24:27]\n"
    "v_exp_f32 v43, v43\n"
    "v_mfma_f32_16x16x32_bf16 a[20:23], v[168:171], v[132:135], a[20:23]\n"
    "ds_read_b128 v[164:167], v201 offset:7168\n"
    "v_add_f32 v117, v117, v68\n"
    "v_mfma_f32_16x16x32_bf16 a[20:23], v[172:175], v[148:151], a[20:23]\n"
    "v_exp_f32 v44, v44\n"
    "v_mfma_f32_16x16x32_bf16 a[16:19], v[172:175], v[144:147], a[16:19]\n"
    "v_exp_f32 v45, v45\n"
    "v_mfma_f32_16x16x32_bf16 a[16:19], v[168:171], v[128:131], a[16:19]\n"
    "s_waitcnt lgkmcnt(2)\n"
    "v_exp_f32 v46, v46\n"
    "v_mfma_f32_16x16x32_bf16 a[32:35], v[176:179], v[128:131], a[32:35]\n"
    "ds_read_b128 v[168:171], v201 offset:8192\n"
    "v_add_f32 v118, v118, v69\n"
    "v_mfma_f32_16x16x32_bf16 a[32:35], v[180:183], v[144:147], a[32:35]\n"
    "v_exp_f32 v47, v47\n"
    "v_mfma_f32_16x16x32_bf16 a[36:39], v[180:183], v[148:151], a[36:39]\n"
    "v_exp_f32 v48, v48\n"
    "v_mfma_f32_16x16x32_bf16 a[36:39], v[176:179], v[132:135], a[36:39]\n"
    "v_exp_f32 v49, v49\n"
    "v_mfma_f32_16x16x32_bf16 a[40:43], v[176:179], v[136:139], a[40:43]\n"
    "ds_read_b128 v[172:175], v201 offset:9216\n"
    "v_add_f32 v117, v117, v70\n"
    "v_mfma_f32_16x16x32_bf16 a[40:43], v[180:183], v[152:155], a[40:43]\n"
    "v_exp_f32 v50, v50\n"
    "v_mfma_f32_16x16x32_bf16 a[44:47], v[180:183], v[156:159], a[44:47]\n"
    "v_exp_f32 v51, v51\n"
    "v_mfma_f32_16x16x32_bf16 a[44:47], v[176:179], v[140:143], a[44:47]\n"
    "s_waitcnt lgkmcnt(2)\n"
    "v_add_f32 v113, v113, v32\n"
    "v_add_f32 v118, v118, v71\n"
    "v_mfma_f32_16x16x32_bf16 a[60:63], v[160:163], v[140:143], a[60:63]\n"
    "ds_read_b128 v[176:179], v201 offset:10240\n"
    "v_add_f32 v114, v114, v33\n"
    "v_mfma_f32_16x16x32_bf16 a[60:63], v[164:167], v[156:159], a[60:63]\n"
    "v_exp_f32 v52, v52\n"
    "v_mfma_f32_16x16x32_bf16 a[56:59], v[164:167], v[152:155], a[56:59]\n"
    "v_add_f32 v121, v121, v72\n"
    "v_add_f32 v113, v113, v34\n"
    "v_mfma_f32_16x16x32_bf16 a[56:59], v[160:163], v[136:139], a[56:59]\n"
    "v_exp_f32 v53, v53\n"
    "v_mfma_f32_16x16x32_bf16 a[52:55], v[160:163], v[132:135], a[52:55]\n"
    "ds_read_b128 v[180:183], v201 offset:11264\n"
    "v_add_f32 v122, v122, v73\n"
    "v_mfma_f32_16x16x32_bf16 a[52:55], v[164:167], v[148:151], a[52:55]\n"
    "v_add_f32 v114, v114, v35\n"
    "v_add_f32 v121, v121, v74\n"
    "v_mfma_f32_16x16x32_bf16 a[48:51], v[164:167], v[144:147], a[48:51]\n"
    "v_add_f32 v117, v117, v36\n"
    "v_add_f32 v122, v122, v75\n"
    "v_mfma_f32_16x16x32_bf16 a[48:51], v[160:163], v[128:131], a[48:51]\n"
    "s_waitcnt lgkmcnt(2)\n"
    "v_exp_f32 v54, v54\n"
    "v_mfma_f32_16x16x32_bf16 a[64:67], v[168:171], v[128:131], a[64:67]\n"
    "ds_read_b128 v[160:163], v201 offset:12288\n"
    "v_add_f32 v118, v118, v37\n"
    "v_mfma_f32_16x16x32_bf16 a[64:67], v[172:175], v[144:147], a[64:67]\n"
    "v_add_f32 v125, v125, v76\n"
    "v_add_f32 v117, v117, v38\n"
    "v_mfma_f32_16x16x32_bf16 a[68:71], v[172:175], v[148:151], a[68:71]\n"
    "v_exp_f32 v55, v55\n"
    "v_mfma_f32_16x16x32_bf16 a[68:71], v[168:171], v[132:135], a[68:71]\n"
    "s_add_u32 m0, s51, s95\n"
    "v_add_f32 v126, v126, v77\n"
    "v_add_f32 v118, v118, v39\n"
    "v_mfma_f32_16x16x32_bf16 a[72:75], v[168:171], v[136:139], a[72:75]\n"
    "ds_read_b128 v[164:167], v201 offset:13312\n"
    "v_add_f32 v125, v125, v78\n"
    "v_mfma_f32_16x16x32_bf16 a[72:75], v[172:175], v[152:155], a[72:75]\n"
    "buffer_load_dwordx4 v202, s[32:35], s64 offen lds\n"
    "v_mfma_f32_16x16x32_bf16 a[76:79], v[172:175], v[156:159], a[76:79]\n"
    "v_add_f32 v121, v121, v40\n"
    "v_add_f32 v126, v126, v79\n"
    "v_mfma_f32_16x16x32_bf16 a[76:79], v[168:171], v[140:143], a[76:79]\n"
    "s_waitcnt lgkmcnt(2)\n"
    "v_exp_f32 v56, v56\n"
    "v_mfma_f32_16x16x32_bf16 a[92:95], v[176:179], v[140:143], a[92:95]\n"
    "ds_read_b128 v[168:171], v201 offset:14336\n"
    "v_add_f32 v122, v122, v41\n"
    "v_mfma_f32_16x16x32_bf16 a[92:95], v[180:183], v[156:159], a[92:95]\n"
    "buffer_load_dwordx4 v203, s[32:35], s64 offen offset:1024 lds\n"
    "v_mfma_f32_16x16x32_bf16 a[88:91], v[180:183], v[152:155], a[88:91]\n"
    "v_add_f32 v113, v113, v80\n"
    "v_add_f32 v121, v121, v42\n"
    "v_mfma_f32_16x16x32_bf16 a[88:91], v[176:179], v[136:139], a[88:91]\n"
    "v_exp_f32 v57, v57\n"
    "v_mfma_f32_16x16x32_bf16 a[84:87], v[176:179], v[132:135], a[84:87]\n"
    "ds_read_b128 v[172:175], v201 offset:15360\n"
    "v_add_f32 v114, v114, v81\n"
    "v_mfma_f32_16x16x32_bf16 a[84:87], v[180:183], v[148:151], a[84:87]\n"
    "buffer_load_dwordx4 v204, s[32:35], s64 offen offset:2048 lds\n"
    "v_mfma_f32_16x16x32_bf16 a[80:83], v[180:183], v[144:147], a[80:83]\n"
    "v_add_f32 v122, v122, v43\n"
    "v_add_f32 v113, v113, v82\n"
    "v_mfma_f32_16x16x32_bf16 a[80:83], v[176:179], v[128:131], a[80:83]\n"
    "s_waitcnt lgkmcnt(2)\n"
    "v_add_f32 v125, v125, v44\n"
    "v_add_f32 v114, v114, v83\n"
    "v_mfma_f32_16x16x32_bf16 a[96:99], v[160:163], v[128:131], a[96:99]\n"
    "s_mov_b32 s95, s94\n"
    "ds_read_b128 v[176:179], v201 offset:16384\n"
    "v_add_f32 v126, v126, v45\n"
    "v_mfma_f32_16x16x32_bf16 a[96:99], v[164:167], v[144:147], a[96:99]\n"
    "s_sub_u32 s94, s94, 0x3000\n"
    "v_exp_f32 v58, v58\n"
    "v_mfma_f32_16x16x32_bf16 a[100:103], v[164:167], v[148:151], a[100:103]\n"
    "s_min_u32 s94, s94, 0x6000\n"
    "v_add_f32 v117, v117, v84\n"
    "v_add_f32 v125, v125, v46\n"
    "v_mfma_f32_16x16x32_bf16 a[100:103], v[160:163], v[132:135], a[100:103]\n"
    "s_sub_u32 s93, s94, s95\n"
    "v_exp_f32 v59, v59\n"
    "v_mfma_f32_16x16x32_bf16 a[104:107], v[160:163], v[136:139], a[104:107]\n"
    "ds_read_b128 v[180:183], v201 offset:17408\n"
    "v_add_f32 v118, v118, v85\n"
    "v_mfma_f32_16x16x32_bf16 a[104:107], v[164:167], v[152:155], a[104:107]\n"
    "v_add_f32 v126, v126, v47\n"
    "v_add_f32 v117, v117, v86\n"
    "v_mfma_f32_16x16x32_bf16 a[108:111], v[164:167], v[156:159], a[108:111]\n"
    "v_add_f32 v113, v113, v48\n"
    "v_add_u32 v205, s93, v205\n"
    "v_mfma_f32_16x16x32_bf16 a[108:111], v[160:163], v[140:143], a[108:111]\n"
    "s_waitcnt lgkmcnt(2)\n"
    "v_exp_f32 v60, v60\n"
    "v_mfma_f32_16x16x32_bf16 a[124:127], v[168:171], v[140:143], a[124:127]\n"
    "ds_read_b128 v[160:163], v201 offset:18432\n"
    "v_add_f32 v118, v118, v87\n"
    "v_mfma_f32_16x16x32_bf16 a[124:127], v[172:175], v[156:159], a[124:127]\n"
    "v_add_f32 v114, v114, v49\n"
    "v_add_u32 v209, 0x180, v209\n"
    "v_mfma_f32_16x16x32_bf16 a[120:123], v[172:175], v[152:155], a[120:123]\n"
    "v_add_u32 v206, s93, v206\n"
    "v_add_f32 v121, v121, v88\n"
    "v_mfma_f32_16x16x32_bf16 a[120:123], v[168:171], v[136:139], a[120:123]\n"
    "v_add_f32 v113, v113, v50\n"
    "v_add_u32 v210, 0x80, v210\n"
    "v_mfma_f32_16x16x32_bf16 a[116:119], v[168:171], v[132:135], a[116:119]\n"
    "ds_read_b128 v[164:167], v201 offset:19456\n"
    "v_add_u32 v207, s93, v207\n"
    "v_mfma_f32_16x16x32_bf16 a[116:119], v[172:175], v[148:151], a[116:119]\n"
    "v_exp_f32 v61, v61\n"
    "v_mfma_f32_16x16x32_bf16 a[112:115], v[172:175], v[144:147], a[112:115]\n"
    "v_add_f32 v122, v122, v89\n"
    "v_add_f32 v114, v114, v51\n"
    "v_mfma_f32_16x16x32_bf16 a[112:115], v[168:171], v[128:131], a[112:115]\n"
    "s_waitcnt lgkmcnt(2)\n"
    "v_add_f32 v121, v121, v90\n"
    "v_add_f32 v117, v117, v52\n"
    "v_mfma_f32_16x16x32_bf16 a[128:131], v[176:179], v[128:131], a[128:131]\n"
    "ds_read_b128 v[168:171], v201 offset:20480\n"
    "v_add_f32 v122, v122, v91\n"
    "v_mfma_f32_16x16x32_bf16 a[128:131], v[180:183], v[144:147], a[128:131]\n"
    "v_exp_f32 v62, v62\n"
    "v_mfma_f32_16x16x32_bf16 a[132:135], v[180:183], v[148:151], a[132:135]\n"
    "v_add_f32 v118, v118, v53\n"
    "v_add_f32 v125, v125, v92\n"
    "v_mfma_f32_16x16x32_bf16 a[132:135], v[176:179], v[132:135], a[132:135]\n"
    "s_add_u32 s64, s64, 0xc000\n"
    "v_add_f32 v117, v117, v54\n"
    "v_add_f32 v126, v126, v93\n"
    "v_mfma_f32_16x16x32_bf16 a[136:139], v[176:179], v[136:139], a[136:139]\n"
    "s_add_u32 s62, s62, 0x6000\n"
    "ds_read_b128 v[172:175], v201 offset:21504\n"
    "v_add_f32 v118, v118, v55\n"
    "v_mfma_f32_16x16x32_bf16 a[136:139], v[180:183], v[152:155], a[136:139]\n"
    "s_add_u32 s63, s63, 0x6000\n"
    "v_exp_f32 v63, v63\n"
    "v_mfma_f32_16x16x32_bf16 a[140:143], v[180:183], v[156:159], a[140:143]\n"
    "s_add_u32 s60, s60, 0x6000\n"
    "v_add_f32 v125, v125, v94\n"
    "v_add_f32 v121, v121, v56\n"
    "v_mfma_f32_16x16x32_bf16 a[140:143], v[176:179], v[140:143], a[140:143]\n"
    "s_waitcnt lgkmcnt(2)\n"
    "v_add_f32 v126, v126, v95\n"
    "v_add_f32 v122, v122, v57\n"
    "v_mfma_f32_16x16x32_bf16 a[156:159], v[160:163], v[140:143], a[156:159]\n"
    "s_add_u32 s61, s61, 0x6000\n"
    "ds_read_b128 v[176:179], v201 offset:22528\n"
    "v_mfma_f32_16x16x32_bf16 a[156:159], v[164:167], v[156:159], a[156:159]\n"
    "ds_read_b128 v[180:183], v201 offset:23552\n"
    "v_mfma_f32_16x16x32_bf16 a[152:155], v[164:167], v[152:155], a[152:155]\n"
    "s_waitcnt vmcnt(3)\n"
    "v_mfma_f32_16x16x32_bf16 a[152:155], v[160:163], v[136:139], a[152:155]\n"
    "v_mfma_f32_16x16x32_bf16 a[148:151], v[160:163], v[132:135], a[148:151]\n"
    "v_mfma_f32_16x16x32_bf16 a[148:151], v[164:167], v[148:151], a[148:151]\n"
    "v_mfma_f32_16x16x32_bf16 a[144:147], v[164:167], v[144:147], a[144:147]\n"
    "v_mfma_f32_16x16x32_bf16 a[144:147], v[160:163], v[128:131], a[144:147]\n"
    "s_waitcnt lgkmcnt(0)\n"
    "v_mfma_f32_16x16x32_bf16 a[160:163], v[168:171], v[128:131], a[160:163]\n"
    "s_barrier\n"
    "v_mfma_f32_16x16x32_bf16 a[160:163], v[172:175], v[144:147], a[160:163]\n"
    "ds_read_u16_d16_hi v223, v209 offset:6144\n"
    "ds_read_u16_d16_hi v222, v209\n"
    "v_mfma_f32_16x16x32_bf16 a[164:167], v[172:175], v[148:151], a[164:167]\n"
    "ds_read_b128 v[160:163], v200\n"
    "v_add_f32 v121, v121, v58\n"
    "v_mfma_f32_16x16x32_bf16 a[164:167], v[168:171], v[132:135], a[164:167]\n"
    "ds_read_b128 v[232:235], v205 offset:24576\n"
    "v_add_f32 v122, v122, v59\n"
    "v_mfma_f32_16x16x32_bf16 a[168:171], v[168:171], v[136:139], a[168:171]\n"
    "ds_read_b128 v[164:167], v200 offset:1024\n"
    "ds_read_b128 v[236:239], v206 offset:24576\n"
    "v_mfma_f32_16x16x32_bf16 a[168:171], v[172:175], v[152:155], a[168:171]\n"
    "v_add_f32 v125, v125, v60\n"
    "v_add_f32 v126, v126, v61\n"
    "v_mfma_f32_16x16x32_bf16 a[172:175], v[172:175], v[156:159], a[172:175]\n"
    "v_add_f32 v125, v125, v62\n"
    "v_add_f32 v126, v126, v63\n"
    "v_mfma_f32_16x16x32_bf16 a[172:175], v[168:171], v[140:143], a[172:175]\n"
    "ds_read_b64 v[228:229], v210\n"
    "ds_read2st64_b32 v[240:241], v207 offset0:96 offset1:99\n"
    "v_mfma_f32_16x16x32_bf16 a[188:191], v[176:179], v[140:143], a[188:191]\n"
    "ds_read_b128 v[168:171], v200 offset:2048\n"
    "ds_read2st64_b32 v[242:243], v207 offset0:102 offset1:105\n"
    "v_mfma_f32_16x16x32_bf16 a[188:191], v[180:183], v[156:159], a[188:191]\n"
    "ds_read2st64_b32 v[244:245], v207 offset0:108 offset1:111\n"
    "ds_read2st64_b32 v[246:247], v207 offset0:114 offset1:117\n"
    "v_mfma_f32_16x16x32_bf16 a[184:187], v[176:179], v[136:139], a[184:187]\n"
    "v_cvt_pk_bf16_f32 v140, v44, v45\n"
    "v_cvt_pk_bf16_f32 v141, v46, v47\n"
    "v_mfma_f32_16x16x32_bf16 a[184:187], v[180:183], v[152:155], a[184:187]\n"
    "s_waitcnt lgkmcnt(6)\n"
    "v_mul_f32 v226, s75, v223\n"
    "v_mfma_f32_16x16x32_bf16 a[180:183], v[176:179], v[132:135], a[180:183]\n"
    "ds_read_b128 v[172:175], v200 offset:3072\n"
    "v_fma_f32 v227, -v222, v226, v217\n"
    "v_mfma_f32_16x16x32_bf16 a[180:183], v[180:183], v[148:151], a[180:183]\n"
    "v_cvt_f32_i32 v232, v232\n"
    "v_cvt_f32_i32 v233, v233\n"
    "v_mfma_f32_16x16x32_bf16 a[176:179], v[176:179], v[128:131], a[176:179]\n"
    "v_cvt_f32_i32 v234, v234\n"
    "v_cvt_f32_i32 v235, v235\n"
    "v_mfma_f32_16x16x32_bf16 a[176:179], v[180:183], v[144:147], a[176:179]\n"
    "v_cvt_f32_i32 v236, v236\n"
    "v_cvt_f32_i32 v237, v237\n"
    "LM01:\n"
    "v_mfma_f32_16x16x32_bf16 v[64:67], v[160:163], a[192:195], v[96:99]\n"
    "ds_read_b128 v[176:179], v200 offset:4096\n"
    "v_cvt_f32_i32 v238, v238\n"
    "v_mfma_f32_16x16x32_bf16 v[64:67], v[164:167], a[196:199], v[64:67]\n"
    "s_waitcnt lgkmcnt(1)\n"
    "v_cvt_f32_i32 v239, v239\n"
    "v_mul_f32_dpp v232, v226, v232 row_newbcast:0 row_mask:0xf bank_mask:0xf\n"
    "v_mfma_f32_16x16x32_bf16 v[64:67], v[168:171], a[200:203], v[64:67]\n"
    "v_mul_f32_dpp v233, v226, v233 row_newbcast:1 row_mask:0xf bank_mask:0xf\n"
    "v_mul_f32_dpp v234, v226, v234 row_newbcast:2 row_mask:0xf bank_mask:0xf\n"
    "v_mfma_f32_16x16x32_bf16 v[68:71], v[168:171], a[224:227], v[100:103]\n"
    "v_mul_f32_dpp v235, v226, v235 row_newbcast:3 row_mask:0xf bank_mask:0xf\n"
    "v_mul_f32_dpp v236, v226, v236 row_newbcast:4 row_mask:0xf bank_mask:0xf\n"
    "v_mfma_f32_16x16x32_bf16 v[68:71], v[164:167], a[220:223], v[68:71]\n"
    "ds_read_b128 v[180:183], v200 offset:5120\n"
    "v_cvt_f32_i32 v240, v240\n"
    "v_mfma_f32_16x16x32_bf16 v[68:71], v[160:163], a[216:219], v[68:71]\n"
    "v_mul_f32_dpp v237, v226, v237 row_newbcast:5 row_mask:0xf bank_mask:0xf\n"
    "v_cvt_f32_i32 v241, v241\n"
    "v_mfma_f32_16x16x32_bf16 v[72:75], v[160:163], a[240:243], v[104:107]\n"
    "v_mul_f32_dpp v238, v226, v238 row_newbcast:6 row_mask:0xf bank_mask:0xf\n"
    "v_cvt_f32_i32 v242, v242\n"
    "v_mfma_f32_16x16x32_bf16 v[72:75], v[164:167], a[244:247], v[72:75]\n"
    "v_mul_f32_dpp v239, v226, v239 row_newbcast:7 row_mask:0xf bank_mask:0xf\n"
    "v_cvt_f32_i32 v243, v243\n"
    "v_mfma_f32_16x16x32_bf16 v[72:75], v[168:171], a[248:251], v[72:75]\n"
    "v_add_f32_dpp v232, v227, v232 row_newbcast:0 row_mask:0xf bank_mask:0xf\n"
    "v_cvt_f32_i32 v244, v244\n"
    "v_mfma_f32_16x16x32_bf16 v[76:79], v[168:171], v[16:19], v[108:111]\n"
    "v_add_f32_dpp v233, v227, v233 row_newbcast:1 row_mask:0xf bank_mask:0xf\n"
    "v_cvt_f32_i32 v245, v245\n"
    "v_mfma_f32_16x16x32_bf16 v[76:79], v[164:167], v[12:15], v[76:79]\n"
    "v_add_f32_dpp v234, v227, v234 row_newbcast:2 row_mask:0xf bank_mask:0xf\n"
    "v_cvt_f32_i32 v246, v246\n"
    "v_mfma_f32_16x16x32_bf16 v[76:79], v[160:163], v[8:11], v[76:79]\n"
    "s_waitcnt lgkmcnt(1)\n"
    "v_add_f32_dpp v235, v227, v235 row_newbcast:3 row_mask:0xf bank_mask:0xf\n"
    "v_cvt_f32_i32 v247, v247\n"
    "v_mfma_f32_16x16x32_bf16 v[76:79], v[172:175], v[20:23], v[76:79]\n"
    "ds_read_b128 v[160:163], v200 offset:9216\n"
    "v_add_f32_dpp v236, v227, v236 row_newbcast:4 row_mask:0xf bank_mask:0xf\n"
    "v_mfma_f32_16x16x32_bf16 v[76:79], v[176:179], v[24:27], v[76:79]\n"
    "s_waitcnt lgkmcnt(1)\n"
    "v_mul_f32_dpp v240, v228, v240 row_newbcast:0 row_mask:0xf bank_mask:0xf\n"
    "v_add_f32_dpp v237, v227, v237 row_newbcast:5 row_mask:0xf bank_mask:0xf\n"
    "v_mfma_f32_16x16x32_bf16 v[76:79], v[180:183], v[28:31], v[76:79]\n"
    "v_mul_f32_dpp v241, v228, v241 row_newbcast:1 row_mask:0xf bank_mask:0xf\n"
    "v_add_f32_dpp v238, v227, v238 row_newbcast:6 row_mask:0xf bank_mask:0xf\n"
    "v_mfma_f32_16x16x32_bf16 v[72:75], v[180:183], v[4:7], v[72:75]\n"
    "ds_read_b128 v[164:167], v200 offset:10240\n"
    "v_mul_f32_dpp v242, v228, v242 row_newbcast:2 row_mask:0xf bank_mask:0xf\n"
    "v_mfma_f32_16x16x32_bf16 v[72:75], v[176:179], v[0:3], v[72:75]\n"
    "v_add_f32_dpp v239, v227, v239 row_newbcast:7 row_mask:0xf bank_mask:0xf\n"
    "v_mul_f32_dpp v243, v228, v243 row_newbcast:3 row_mask:0xf bank_mask:0xf\n"
    "v_mfma_f32_16x16x32_bf16 v[72:75], v[172:175], a[252:255], v[72:75]\n"
    "v_cvt_pk_bf16_f32 v232, v232, v233\n"
    "v_mul_f32_dpp v244, v228, v244 row_newbcast:4 row_mask:0xf bank_mask:0xf\n"
    "v_mfma_f32_16x16x32_bf16 v[68:71], v[172:175], a[228:231], v[68:71]\n"
    "ds_read_b128 v[168:171], v200 offset:11264\n"
    "v_cvt_pk_bf16_f32 v233, v234, v235\n"
    "v_mfma_f32_16x16x32_bf16 v[68:71], v[176:179], a[232:235], v[68:71]\n"
    "v_mul_f32_dpp v245, v228, v245 row_newbcast:5 row_mask:0xf bank_mask:0xf\n"
    "v_cvt_pk_bf16_f32 v234, v236, v237\n"
    "v_mfma_f32_16x16x32_bf16 v[68:71], v[180:183], a[236:239], v[68:71]\n"
    "v_mul_f32_dpp v246, v228, v246 row_newbcast:6 row_mask:0xf bank_mask:0xf\n"
    "v_cvt_pk_bf16_f32 v235, v238, v239\n"
    "v_mfma_f32_16x16x32_bf16 v[64:67], v[180:183], a[212:215], v[64:67]\n"
    "v_mul_f32_dpp v247, v228, v247 row_newbcast:7 row_mask:0xf bank_mask:0xf\n"
    "v_add_f32_dpp v240, v229, v240 row_newbcast:0 row_mask:0xf bank_mask:0xf\n"
    "v_mfma_f32_16x16x32_bf16 v[64:67], v[176:179], a[208:211], v[64:67]\n"
    "v_add_f32_dpp v241, v229, v241 row_newbcast:1 row_mask:0xf bank_mask:0xf\n"
    "v_add_f32_dpp v242, v229, v242 row_newbcast:2 row_mask:0xf bank_mask:0xf\n"
    "v_mfma_f32_16x16x32_bf16 v[64:67], v[172:175], a[204:207], v[64:67]\n"
    "s_waitcnt lgkmcnt(1)\n"
    "buffer_store_dwordx4 v[232:235], v211, s[36:39], s62 offen sc1\n"
    "v_add_f32_dpp v243, v229, v243 row_newbcast:3 row_mask:0xf bank_mask:0xf\n"
    "v_add_f32_dpp v244, v229, v244 row_newbcast:4 row_mask:0xf bank_mask:0xf\n"
    "v_add_f32_dpp v245, v229, v245 row_newbcast:5 row_mask:0xf bank_mask:0xf\n"
    "v_mfma_f32_16x16x32_bf16 v[80:83], v[160:163], a[204:207], v[96:99]\n"
    "ds_read_b128 v[172:175], v200 offset:6144\n"
    "v_add_f32_dpp v246, v229, v246 row_newbcast:6 row_mask:0xf bank_mask:0xf\n"
    "v_mfma_f32_16x16x32_bf16 v[80:83], v[164:167], a[208:211], v[80:83]\n"
    "s_waitcnt lgkmcnt(1)\n"
    "v_add_f32_dpp v247, v229, v247 row_newbcast:7 row_mask:0xf bank_mask:0xf\n"
    "v_cvt_pk_bf16_f32 v240, v240, v241\n"
    "v_mfma_f32_16x16x32_bf16 v[80:83], v[168:171], a[212:215], v[80:83]\n"
    "v_cvt_pk_bf16_f32 v241, v242, v243\n"
    "v_cvt_pk_bf16_f32 v242, v244, v245\n"
    "v_mfma_f32_16x16x32_bf16 v[84:87], v[168:171], a[236:239], v[100:103]\n"
    "ds_read_b128 v[176:179], v200 offset:7168\n"
    "v_cvt_pk_bf16_f32 v243, v246, v247\n"
    "v_mfma_f32_16x16x32_bf16 v[84:87], v[164:167], a[232:235], v[84:87]\n"
    "buffer_store_dwordx4 v[240:243], v212, s[40:43], s63 offen sc1\n"
    "v_cvt_pk_bf16_f32 v136, v40, v41\n"
    "v_cvt_pk_bf16_f32 v137, v42, v43\n"
    "v_cvt_pk_bf16_f32 v132, v36, v37\n"
    "v_mfma_f32_16x16x32_bf16 v[84:87], v[160:163], a[228:231], v[84:87]\n"
    "v_cvt_pk_bf16_f32 v133, v38, v39\n"
    "v_cvt_pk_bf16_f32 v128, v32, v33\n"
    "v_mfma_f32_16x16x32_bf16 v[88:91], v[160:163], a[252:255], v[104:107]\n"
    "ds_read_b128 v[180:183], v200 offset:8192\n"
    "v_cvt_pk_bf16_f32 v129, v34, v35\n"
    "v_mfma_f32_16x16x32_bf16 v[88:91], v[164:167], v[0:3], v[88:91]\n"
    "v_exp_f32 v64, v64\n"
    "v_mfma_f32_16x16x32_bf16 v[88:91], v[168:171], v[4:7], v[88:91]\n"
    "v_exp_f32 v65, v65\n"
    "v_mfma_f32_16x16x32_bf16 v[92:95], v[168:171], v[28:31], v[108:111]\n"
    "v_exp_f32 v66, v66\n"
    "v_mfma_f32_16x16x32_bf16 v[92:95], v[164:167], v[24:27], v[92:95]\n"
    "v_exp_f32 v67, v67\n"
    "v_mfma_f32_16x16x32_bf16 v[92:95], v[160:163], v[20:23], v[92:95]\n"
    "s_waitcnt lgkmcnt(1)\n"
    "v_exp_f32 v68, v68\n"
    "v_mfma_f32_16x16x32_bf16 v[92:95], v[172:175], v[8:11], v[92:95]\n"
    "ds_read_b128 v[160:163], v200 offset:12288\n"
    "v_cvt_pk_bf16_f32 v142, v60, v61\n"
    "v_mfma_f32_16x16x32_bf16 v[92:95], v[176:179], v[12:15], v[92:95]\n"
    "s_waitcnt lgkmcnt(1)\n"
    "v_exp_f32 v69, v69\n"
    "v_mfma_f32_16x16x32_bf16 v[92:95], v[180:183], v[16:19], v[92:95]\n"
    "v_exp_f32 v70, v70\n"
    "v_mfma_f32_16x16x32_bf16 v[88:91], v[180:183], a[248:251], v[88:91]\n"
    "ds_read_b128 v[164:167], v200 offset:13312\n"
    "v_cvt_pk_bf16_f32 v143, v62, v63\n"
    "v_mfma_f32_16x16x32_bf16 v[88:91], v[176:179], a[244:247], v[88:91]\n"
    "v_exp_f32 v71, v71\n"
    "v_mfma_f32_16x16x32_bf16 v[88:91], v[172:175], a[240:243], v[88:91]\n"
    "v_cvt_pk_bf16_f32 v138, v56, v57\n"
    "v_cvt_pk_bf16_f32 v139, v58, v59\n"
    "v_mfma_f32_16x16x32_bf16 v[84:87], v[172:175], a[216:219], v[84:87]\n"
    "ds_read_b128 v[168:171], v200 offset:14336\n"
    "v_cvt_pk_bf16_f32 v134, v52, v53\n"
    "v_mfma_f32_16x16x32_bf16 v[84:87], v[176:179], a[220:223], v[84:87]\n"
    "v_exp_f32 v72, v72\n"
    "v_mfma_f32_16x16x32_bf16 v[84:87], v[180:183], a[224:227], v[84:87]\n"
    "v_cvt_pk_bf16_f32 v135, v54, v55\n"
    "v_cvt_pk_bf16_f32 v130, v48, v49\n"
    "v_mfma_f32_16x16x32_bf16 v[80:83], v[180:183], a[200:203], v[80:83]\n"
    "v_exp_f32 v73, v73\n"
    "v_mfma_f32_16x16x32_bf16 v[80:83], v[176:179], a[196:199], v[80:83]\n"
    "v_cvt_pk_bf16_f32 v131, v50, v51\n"
    "v_cvt_pk_bf16_f32 v144, v64, v65\n"
    "v_mfma_f32_16x16x32_bf16 v[80:83], v[172:175], a[192:195], v[80:83]\n"
    "s_waitcnt lgkmcnt(1)\n"
    "v_exp_f32 v74, v74\n"
    "v_mfma_f32_16x16x32_bf16 v[32:35], v[160:163], a[192:195], v[96:99]\n"
    "ds_read_b128 v[172:175], v200 offset:15360\n"
    "v_cvt_pk_bf16_f32 v145, v66, v67\n"
    "v_mfma_f32_16x16x32_bf16 v[32:35], v[164:167], a[196:199], v[32:35]\n"
    "s_waitcnt lgkmcnt(1)\n"
    "s_add_u32 s65, s60, s92\n"
    "v_cvt_pk_bf16_f32 v148, v68, v69\n"
    "v_mfma_f32_16x16x32_bf16 v[32:35], v[168:171], a[200:203], v[32:35]\n"
    "s_add_u32 m0, s90, 0x6000\n"
    "v_exp_f32 v75, v75\n"
    "v_mfma_f32_16x16x32_bf16 v[36:39], v[168:171], a[224:227], v[100:103]\n"
    "ds_read_b128 v[176:179], v200 offset:16384\n"
    "v_cvt_pk_bf16_f32 v149, v70, v71\n"
    "v_mfma_f32_16x16x32_bf16 v[36:39], v[164:167], a[220:223], v[36:39]\n"
    "buffer_load_dwordx4 v200, s[36:39], s65 offen sc1 lds\n"
    "v_mfma_f32_16x16x32_bf16 v[36:39], v[160:163], a[216:219], v[36:39]\n"
    "v_exp_f32 v76, v76\n"
    "v_mfma_f32_16x16x32_bf16 v[40:43], v[160:163], a[240:243], v[104:107]\n"
    "ds_read_b128 v[180:183], v200 offset:17408\n"
    "v_cvt_pk_bf16_f32 v152, v72, v73\n"
    "v_mfma_f32_16x16x32_bf16 v[40:43], v[164:167], a[244:247], v[40:43]\n"
    "v_exp_f32 v77, v77\n"
    "v_mfma_f32_16x16x32_bf16 v[40:43], v[168:171], a[248:251], v[40:43]\n"
    "buffer_load_dwordx4 v200, s[36:39], s65 offen offset:1024 sc1 lds\n"
    "v_mfma_f32_16x16x32_bf16 v[44:47], v[168:171], v[16:19], v[108:111]\n"
    "v_exp_f32 v78, v78\n"
    "v_mfma_f32_16x16x32_bf16 v[44:47], v[164:167], v[12:15], v[44:47]\n"
    "v_exp_f32 v79, v79\n"
    "v_mfma_f32_16x16x32_bf16 v[44:47], v[160:163], v[8:11], v[44:47]\n"
    "s_waitcnt lgkmcnt(1)\n"
    "v_exp_f32 v80, v80\n"
    "v_mfma_f32_16x16x32_bf16 v[44:47], v[172:175], v[20:23], v[44:47]\n"
    "ds_read_b128 v[160:163], v200 offset:21504\n"
    "v_cvt_pk_bf16_f32 v153, v74, v75\n"
    "v_mfma_f32_16x16x32_bf16 v[44:47], v[176:179], v[24:27], v[44:47]\n"
    "s_waitcnt lgkmcnt(1)\n"
    "buffer_load_dwordx4 v200, s[36:39], s65 offen offset:2048 sc1 lds\n"
    "v_mfma_f32_16x16x32_bf16 v[44:47], v[180:183], v[28:31], v[44:47]\n"
    "v_exp_f32 v81, v81\n"
    "v_mfma_f32_16x16x32_bf16 v[40:43], v[180:183], v[4:7], v[40:43]\n"
    "ds_read_b128 v[164:167], v200 offset:22528\n"
    "v_cvt_pk_bf16_f32 v156, v76, v77\n"
    "v_mfma_f32_16x16x32_bf16 v[40:43], v[176:179], v[0:3], v[40:43]\n"
    "v_exp_f32 v82, v82\n"
    "v_mfma_f32_16x16x32_bf16 v[40:43], v[172:175], a[252:255], v[40:43]\n"
    "buffer_load_dwordx4 v200, s[36:39], s65 offen offset:3072 sc1 lds\n"
    "v_mfma_f32_16x16x32_bf16 v[36:39], v[172:175], a[228:231], v[36:39]\n"
    "s_add_u32 s65, s61, s92\n"
    "ds_read_b128 v[168:171], v200 offset:23552\n"
    "v_cvt_pk_bf16_f32 v157, v78, v79\n"
    "v_mfma_f32_16x16x32_bf16 v[36:39], v[176:179], a[232:235], v[36:39]\n"
    "s_add_u32 m0, s90, 0xc000\n"
    "v_exp_f32 v83, v83\n"
    "v_mfma_f32_16x16x32_bf16 v[36:39], v[180:183], a[236:239], v[36:39]\n"
    "v_exp_f32 v84, v84\n"
    "v_mfma_f32_16x16x32_bf16 v[32:35], v[180:183], a[212:215], v[32:35]\n"
    "buffer_load_dwordx4 v200, s[40:43], s65 offen sc1 lds\n"
    "v_mfma_f32_16x16x32_bf16 v[32:35], v[176:179], a[208:211], v[32:35]\n"
    "v_exp_f32 v85, v85\n"
    "v_mfma_f32_16x16x32_bf16 v[32:35], v[172:175], a[204:207], v[32:35]\n"
    "s_waitcnt lgkmcnt(1)\n"
    "v_exp_f32 v86, v86\n"
    "v_mfma_f32_16x16x32_bf16 v[48:51], v[160:163], a[204:207], v[96:99]\n"
    "ds_read_b128 v[172:175], v200 offset:18432\n"
    "v_cvt_pk_bf16_f32 v146, v80, v81\n"
    "v_mfma_f32_16x16x32_bf16 v[48:51], v[164:167], a[208:211], v[48:51]\n"
    "s_waitcnt lgkmcnt(1)\n"
    "buffer_load_dwordx4 v200, s[40:43], s65 offen offset:1024 sc1 lds\n"
    "v_mfma_f32_16x16x32_bf16 v[48:51], v[168:171], a[212:215], v[48:51]\n"
    "v_exp_f32 v87, v87\n"
    "v_mfma_f32_16x16x32_bf16 v[52:55], v[168:171], a[236:239], v[100:103]\n"
    "ds_read_b128 v[176:179], v200 offset:19456\n"
    "v_cvt_pk_bf16_f32 v147, v82, v83\n"
    "v_mfma_f32_16x16x32_bf16 v[52:55], v[164:167], a[232:235], v[52:55]\n"
    "v_exp_f32 v88, v88\n"
    "v_mfma_f32_16x16x32_bf16 v[52:55], v[160:163], a[228:231], v[52:55]\n"
    "buffer_load_dwordx4 v200, s[40:43], s65 offen offset:2048 sc1 lds\n"
    "v_mfma_f32_16x16x32_bf16 v[56:59], v[160:163], a[252:255], v[104:107]\n"
    "ds_read_b128 v[180:183], v200 offset:20480\n"
    "v_cvt_pk_bf16_f32 v150, v84, v85\n"
    "v_mfma_f32_16x16x32_bf16 v[56:59], v[164:167], v[0:3], v[56:59]\n"
    "v_exp_f32 v89, v89\n"
    "v_mfma_f32_16x16x32_bf16 v[56:59], v[168:171], v[4:7], v[56:59]\n"
    "v_exp_f32 v90, v90\n"
    "v_mfma_f32_16x16x32_bf16 v[60:63], v[168:171], v[28:31], v[108:111]\n"
    "buffer_load_dwordx4 v200, s[40:43], s65 offen offset:3072 sc1 lds\n"
    "v_mfma_f32_16x16x32_bf16 v[60:63], v[164:167], v[24:27], v[60:63]\n"
    "v_exp_f32 v91, v91\n"
    "v_mfma_f32_16x16x32_bf16 v[60:63], v[160:163], v[20:23], v[60:63]\n"
    "s_waitcnt lgkmcnt(1)\n"
    "v_exp_f32 v92, v92\n"
    "v_mfma_f32_16x16x32_bf16 v[60:63], v[172:175], v[8:11], v[60:63]\n"
    "ds_read_b128 v[160:163], v201 offset:24576\n"
    "v_cvt_pk_bf16_f32 v151, v86, v87\n"
    "v_mfma_f32_16x16x32_bf16 v[60:63], v[176:179], v[12:15], v[60:63]\n"
    "s_waitcnt lgkmcnt(1)\n"
    "v_exp_f32 v93, v93\n"
    "v_mfma_f32_16x16x32_bf16 v[60:63], v[180:183], v[16:19], v[60:63]\n"
    "v_exp_f32 v94, v94\n"
    "v_mfma_f32_16x16x32_bf16 v[56:59], v[180:183], a[248:251], v[56:59]\n"
    "ds_read_b128 v[164:167], v201 offset:25600\n"
    "v_cvt_pk_bf16_f32 v154, v88, v89\n"
    "v_mfma_f32_16x16x32_bf16 v[56:59], v[176:179], a[244:247], v[56:59]\n"
    "v_cvt_pk_bf16_f32 v155, v90, v91\n"
    "v_cvt_pk_bf16_f32 v158, v92, v93\n"
    "v_mfma_f32_16x16x32_bf16 v[56:59], v[172:175], a[240:243], v[56:59]\n"
    "v_exp_f32 v95, v95\n"
    "v_mfma_f32_16x16x32_bf16 v[52:55], v[172:175], a[216:219], v[52:55]\n"
    "ds_read_b128 v[168:171], v201 offset:26624\n"
    "v_cvt_pk_bf16_f32 v159, v94, v95\n"
    "v_mfma_f32_16x16x32_bf16 v[52:55], v[176:179], a[220:223], v[52:55]\n"
    "v_exp_f32 v32, v32\n"
    "v_mfma_f32_16x16x32_bf16 v[52:55], v[180:183], a[224:227], v[52:55]\n"
    "v_exp_f32 v33, v33\n"
    "v_mfma_f32_16x16x32_bf16 v[48:51], v[180:183], a[200:203], v[48:51]\n"
    "v_exp_f32 v34, v34\n"
    "v_mfma_f32_16x16x32_bf16 v[48:51], v[176:179], a[196:199], v[48:51]\n"
    "v_exp_f32 v35, v35\n"
    "v_mfma_f32_16x16x32_bf16 v[48:51], v[172:175], a[192:195], v[48:51]\n"
    "s_waitcnt lgkmcnt(1)\n"
    "v_exp_f32 v36, v36\n"
    "v_mfma_f32_16x16x32_bf16 a[0:3], v[160:163], v[128:131], a[0:3]\n"
    "ds_read_b128 v[172:175], v201 offset:27648\n"
    "ds_read_b128 v[176:179], v201 offset:28672\n"
    "v_mfma_f32_16x16x32_bf16 a[0:3], v[164:167], v[144:147], a[0:3]\n"
    "v_exp_f32 v37, v37\n"
    "v_mfma_f32_16x16x32_bf16 a[4:7], v[160:163], v[132:135], a[4:7]\n"
    "v_exp_f32 v38, v38\n"
    "v_mfma_f32_16x16x32_bf16 a[4:7], v[164:167], v[148:151], a[4:7]\n"
    "v_exp_f32 v39, v39\n"
    "v_mfma_f32_16x16x32_bf16 a[8:11], v[160:163], v[136:139], a[8:11]\n"
    "ds_read_b128 v[180:183], v201 offset:29696\n"
    "v_add_f32 v113, v113, v64\n"
    "v_mfma_f32_16x16x32_bf16 a[8:11], v[164:167], v[152:155], a[8:11]\n"
    "v_exp_f32 v40, v40\n"
    "v_mfma_f32_16x16x32_bf16 a[12:15], v[160:163], v[140:143], a[12:15]\n"
    "v_exp_f32 v41, v41\n"
    "v_mfma_f32_16x16x32_bf16 a[12:15], v[164:167], v[156:159], a[12:15]\n"
    "s_waitcnt lgkmcnt(2)\n"
    "v_exp_f32 v42, v42\n"
    "v_mfma_f32_16x16x32_bf16 a[28:31], v[168:171], v[140:143], a[28:31]\n"
    "ds_read_b128 v[160:163], v201 offset:30720\n"
    "v_add_f32 v114, v114, v65\n"
    "v_mfma_f32_16x16x32_bf16 a[28:31], v[172:175], v[156:159], a[28:31]\n"
    "v_exp_f32 v43, v43\n"
    "v_mfma_f32_16x16x32_bf16 a[24:27], v[172:175], v[152:155], a[24:27]\n"
    "v_exp_f32 v44, v44\n"
    "v_mfma_f32_16x16x32_bf16 a[24:27], v[168:171], v[136:139], a[24:27]\n"
    "v_exp_f32 v45, v45\n"
    "v_mfma_f32_16x16x32_bf16 a[20:23], v[168:171], v[132:135], a[20:23]\n"
    "ds_read_b128 v[164:167], v201 offset:31744\n"
    "v_add_f32 v113, v113, v66\n"
    "v_mfma_f32_16x16x32_bf16 a[20:23], v[172:175], v[148:151], a[20:23]\n"
    "v_exp_f32 v46, v46\n"
    "v_mfma_f32_16x16x32_bf16 a[16:19], v[172:175], v[144:147], a[16:19]\n"
    "v_exp_f32 v47, v47\n"
    "v_mfma_f32_16x16x32_bf16 a[16:19], v[168:171], v[128:131], a[16:19]\n"
    "s_waitcnt lgkmcnt(2)\n"
    "v_exp_f32 v48, v48\n"
    "v_mfma_f32_16x16x32_bf16 a[32:35], v[176:179], v[128:131], a[32:35]\n"
    "ds_read_b128 v[168:171], v201 offset:32768\n"
    "v_add_f32 v114, v114, v67\n"
    "v_mfma_f32_16x16x32_bf16 a[32:35], v[180:183], v[144:147], a[32:35]\n"
    "v_exp_f32 v49, v49\n"
    "v_mfma_f32_16x16x32_bf16 a[36:39], v[180:183], v[148:151], a[36:39]\n"
    "v_exp_f32 v50, v50\n"
    "v_mfma_f32_16x16x32_bf16 a[36:39], v[176:179], v[132:135], a[36:39]\n"
    "v_add_f32 v117, v117, v68\n"
    "v_add_f32 v118, v118, v69\n"
    "v_mfma_f32_16x16x32_bf16 a[40:43], v[176:179], v[136:139], a[40:43]\n"
    "ds_read_b128 v[172:175], v201 offset:33792\n"
    "v_add_f32 v117, v117, v70\n"
    "v_mfma_f32_16x16x32_bf16 a[40:43], v[180:183], v[152:155], a[40:43]\n"
    "v_exp_f32 v51, v51\n"
    "v_mfma_f32_16x16x32_bf16 a[44:47], v[180:183], v[156:159], a[44:47]\n"
    "v_add_f32 v113, v113, v32\n"
    "v_add_f32 v118, v118, v71\n"
    "v_mfma_f32_16x16x32_bf16 a[44:47], v[176:179], v[140:143], a[44:47]\n"
    "s_waitcnt lgkmcnt(2)\n"
    "v_exp_f32 v52, v52\n"
    "v_mfma_f32_16x16x32_bf16 a[60:63], v[160:163], v[140:143], a[60:63]\n"
    "ds_read_b128 v[176:179], v201 offset:34816\n"
    "v_add_f32 v114, v114, v33\n"
    "v_mfma_f32_16x16x32_bf16 a[60:63], v[164:167], v[156:159], a[60:63]\n"
    "v_add_f32 v121, v121, v72\n"
    "v_add_f32 v113, v113, v34\n"
    "v_mfma_f32_16x16x32_bf16 a[56:59], v[164:167], v[152:155], a[56:59]\n"
    "v_exp_f32 v53, v53\n"
    "v_mfma_f32_16x16x32_bf16 a[56:59], v[160:163], v[136:139], a[56:59]\n"
    "v_add_f32 v122, v122, v73\n"
    "v_add_f32 v114, v114, v35\n"
    "v_mfma_f32_16x16x32_bf16 a[52:55], v[160:163], v[132:135], a[52:55]\n"
    "ds_read_b128 v[180:183], v201 offset:35840\n"
    "v_add_f32 v121, v121, v74\n"
    "v_mfma_f32_16x16x32_bf16 a[52:55], v[164:167], v[148:151], a[52:55]\n"
    "v_add_f32 v117, v117, v36\n"
    "v_add_f32 v122, v122, v75\n"
    "v_mfma_f32_16x16x32_bf16 a[48:51], v[164:167], v[144:147], a[48:51]\n"
    "v_exp_f32 v54, v54\n"
    "v_mfma_f32_16x16x32_bf16 a[48:51], v[160:163], v[128:131], a[48:51]\n"
    "s_waitcnt lgkmcnt(2)\n"
    "v_add_f32 v118, v118, v37\n"
    "v_add_f32 v125, v125, v76\n"
    "v_mfma_f32_16x16x32_bf16 a[64:67], v[168:171], v[128:131], a[64:67]\n"
    "ds_read_b128 v[160:163], v201 offset:36864\n"
    "v_add_f32 v117, v117, v38\n"
    "v_mfma_f32_16x16x32_bf16 a[64:67], v[172:175], v[144:147], a[64:67]\n"
    "v_exp_f32 v55, v55\n"
    "v_mfma_f32_16x16x32_bf16 a[68:71], v[172:175], v[148:151], a[68:71]\n"
    "v_add_f32 v126, v126, v77\n"
    "v_add_f32 v118, v118, v39\n"
    "v_mfma_f32_16x16x32_bf16 a[68:71], v[168:171], v[132:135], a[68:71]\n"
    "s_add_u32 m0, s51, s95\n"
    "v_add_f32 v125, v125, v78\n"
    "v_add_f32 v121, v121, v40\n"
    "v_mfma_f32_16x16x32_bf16 a[72:75], v[168:171], v[136:139], a[72:75]\n"
    "ds_read_b128 v[164:167], v201 offset:37888\n"
    "v_add_f32 v126, v126, v79\n"
    "v_mfma_f32_16x16x32_bf16 a[72:75], v[172:175], v[152:155], a[72:75]\n"
    "buffer_load_dwordx4 v202, s[32:35], s64 offen lds\n"
    "v_mfma_f32_16x16x32_bf16 a[76:79], v[172:175], v[156:159], a[76:79]\n"
    "v_exp_f32 v56, v56\n"
    "v_mfma_f32_16x16x32_bf16 a[76:79], v[168:171], v[140:143], a[76:79]\n"
    "s_waitcnt lgkmcnt(2)\n"
    "v_add_f32 v122, v122, v41\n"
    "v_add_f32 v113, v113, v80\n"
    "v_mfma_f32_16x16x32_bf16 a[92:95], v[176:179], v[140:143], a[92:95]\n"
    "ds_read_b128 v[168:171], v201 offset:38912\n"
    "v_add_f32 v121, v121, v42\n"
    "v_mfma_f32_16x16x32_bf16 a[92:95], v[180:183], v[156:159], a[92:95]\n"
    "buffer_load_dwordx4 v203, s[32:35], s64 offen offset:1024 lds\n"
    "v_mfma_f32_16x16x32_bf16 a[88:91], v[180:183], v[152:155], a[88:91]\n"
    "v_exp_f32 v57, v57\n"
    "v_mfma_f32_16x16x32_bf16 a[88:91], v[176:179], v[136:139], a[88:91]\n"
    "v_add_f32 v114, v114, v81\n"
    "v_add_f32 v122, v122, v43\n"
    "v_mfma_f32_16x16x32_bf16 a[84:87], v[176:179], v[132:135], a[84:87]\n"
    "ds_read_b128 v[172:175], v201 offset:39936\n"
    "v_add_f32 v113, v113, v82\n"
    "v_mfma_f32_16x16x32_bf16 a[84:87], v[180:183], v[148:151], a[84:87]\n"
    "buffer_load_dwordx4 v204, s[32:35], s64 offen offset:2048 lds\n"
    "v_mfma_f32_16x16x32_bf16 a[80:83], v[180:183], v[144:147], a[80:83]\n"
    "v_add_f32 v125, v125, v44\n"
    "v_add_f32 v114, v114, v83\n"
    "v_mfma_f32_16x16x32_bf16 a[80:83], v[176:179], v[128:131], a[80:83]\n"
    "s_waitcnt lgkmcnt(2)\n"
    "v_exp_f32 v58, v58\n"
    "v_mfma_f32_16x16x32_bf16 a[96:99], v[160:163], v[128:131], a[96:99]\n"
    "s_mov_b32 s95, s94\n"
    "ds_read_b128 v[176:179], v201 offset:40960\n"
    "v_add_f32 v126, v126, v45\n"
    "v_mfma_f32_16x16x32_bf16 a[96:99], v[164:167], v[144:147], a[96:99]\n"
    "s_sub_u32 s94, s94, 0x3000\n"
    "v_add_f32 v117, v117, v84\n"
    "v_add_f32 v125, v125, v46\n"
    "v_mfma_f32_16x16x32_bf16 a[100:103], v[164:167], v[148:151], a[100:103]\n"
    "s_min_u32 s94, s94, 0x6000\n"
    "v_exp_f32 v59, v59\n"
    "v_mfma_f32_16x16x32_bf16 a[100:103], v[160:163], v[132:135], a[100:103]\n"
    "s_sub_u32 s93, s94, s95\n"
    "v_add_f32 v118, v118, v85\n"
    "v_add_f32 v126, v126, v47\n"
    "v_mfma_f32_16x16x32_bf16 a[104:107], v[160:163], v[136:139], a[104:107]\n"
    "ds_read_b128 v[180:183], v201 offset:41984\n"
    "v_add_f32 v117, v117, v86\n"
    "v_mfma_f32_16x16x32_bf16 a[104:107], v[164:167], v[152:155], a[104:107]\n"
    "v_add_f32 v113, v113, v48\n"
    "v_add_u32 v205, s93, v205\n"
    "v_mfma_f32_16x16x32_bf16 a[108:111], v[164:167], v[156:159], a[108:111]\n"
    "v_exp_f32 v60, v60\n"
    "v_mfma_f32_16x16x32_bf16 a[108:111], v[160:163], v[140:143], a[108:111]\n"
    "s_waitcnt lgkmcnt(2)\n"
    "v_add_f32 v118, v118, v87\n"
    "v_add_f32 v114, v114, v49\n"
    "v_mfma_f32_16x16x32_bf16 a[124:127], v[168:171], v[140:143], a[124:127]\n"
    "ds_read_b128 v[160:163], v201 offset:43008\n"
    "v_add_u32 v206, s93, v206\n"
    "v_mfma_f32_16x16x32_bf16 a[124:127], v[172:175], v[156:159], a[124:127]\n"
    "v_add_f32 v121, v121, v88\n"
    "v_add_f32 v113, v113, v50\n"
    "v_mfma_f32_16x16x32_bf16 a[120:123], v[172:175], v[152:155], a[120:123]\n"
    "v_exp_f32 v61, v61\n"
    "v_mfma_f32_16x16x32_bf16 a[120:123], v[168:171], v[136:139], a[120:123]\n"
    "v_add_u32 v207, s93, v207\n"
    "v_add_f32 v122, v122, v89\n"
    "v_mfma_f32_16x16x32_bf16 a[116:119], v[168:171], v[132:135], a[116:119]\n"
    "ds_read_b128 v[164:167], v201 offset:44032\n"
    "v_add_f32 v114, v114, v51\n"
    "v_mfma_f32_16x16x32_bf16 a[116:119], v[172:175], v[148:151], a[116:119]\n"
    "v_add_f32 v121, v121, v90\n"
    "v_add_f32 v117, v117, v52\n"
    "v_mfma_f32_16x16x32_bf16 a[112:115], v[172:175], v[144:147], a[112:115]\n"
    "v_exp_f32 v62, v62\n"
    "v_mfma_f32_16x16x32_bf16 a[112:115], v[168:171], v[128:131], a[112:115]\n"
    "s_waitcnt lgkmcnt(2)\n"
    "v_add_f32 v122, v122, v91\n"
    "v_add_f32 v118, v118, v53\n"
    "v_mfma_f32_16x16x32_bf16 a[128:131], v[176:179], v[128:131], a[128:131]\n"
    "ds_read_b128 v[168:171], v201 offset:45056\n"
    "v_add_f32 v125, v125, v92\n"
    "v_mfma_f32_16x16x32_bf16 a[128:131], v[180:183], v[144:147], a[128:131]\n"
    "v_add_f32 v117, v117, v54\n"
    "v_add_f32 v126, v126, v93\n"
    "v_mfma_f32_16x16x32_bf16 a[132:135], v[180:183], v[148:151], a[132:135]\n"
    "v_exp_f32 v63, v63\n"
    "v_mfma_f32_16x16x32_bf16 a[132:135], v[176:179], v[132:135], a[132:135]\n"
    "s_add_u32 s64, s64, 0xc000\n"
    "v_add_f32 v118, v118, v55\n"
    "v_add_f32 v125, v125, v94\n"
    "v_mfma_f32_16x16x32_bf16 a[136:139], v[176:179], v[136:139], a[136:139]\n"
    "s_add_u32 s62, s62, 0x6000\n"
    "ds_read_b128 v[172:175], v201 offset:46080\n"
    "v_add_f32 v121, v121, v56\n"
    "v_mfma_f32_16x16x32_bf16 a[136:139], v[180:183], v[152:155], a[136:139]\n"
    "s_add_u32 s63, s63, 0x6000\n"
    "v_add_f32 v126, v126, v95\n"
    "v_add_f32 v122, v122, v57\n"
    "v_mfma_f32_16x16x32_bf16 a[140:143], v[180:183], v[156:159], a[140:143]\n"
    "s_add_u32 s60, s60, 0x6000\n"
    "v_mfma_f32_16x16x32_bf16 a[140:143], v[176:179], v[140:143], a[140:143]\n"
    "s_waitcnt lgkmcnt(2)\n"
    "v_mfma_f32_16x16x32_bf16 a[156:159], v[160:163], v[140:143], a[156:159]\n"
    "s_add_u32 s61, s61, 0x6000\n"
    "ds_read_b128 v[176:179], v201 offset:47104\n"
    "v_mfma_f32_16x16x32_bf16 a[156:159], v[164:167], v[156:159], a[156:159]\n"
    "ds_read_b128 v[180:183], v201 offset:48128\n"
    "v_mfma_f32_16x16x32_bf16 a[152:155], v[164:167], v[152:155], a[152:155]\n"
    "s_waitcnt vmcnt(3)\n"
    "v_mfma_f32_16x16x32_bf16 a[152:155], v[160:163], v[136:139], a[152:155]\n"
    "v_mfma_f32_16x16x32_bf16 a[148:151], v[160:163], v[132:135], a[148:151]\n"
    "v_mfma_f32_16x16x32_bf16 a[148:151], v[164:167], v[148:151], a[148:151]\n"
    "v_mfma_f32_16x16x32_bf16 a[144:147], v[164:167], v[144:147], a[144:147]\n"
    "v_mfma_f32_16x16x32_bf16 a[144:147], v[160:163], v[128:131], a[144:147]\n"
    "s_waitcnt lgkmcnt(0)\n"
    "v_mfma_f32_16x16x32_bf16 a[160:163], v[168:171], v[128:131], a[160:163]\n"
    "s_barrier\n"
    "LMID1:\n"
    "v_mfma_f32_16x16x32_bf16 a[160:163], v[172:175], v[144:147], a[160:163]\n"
    "ds_read_u16_d16_hi v223, v209 offset:6336\n"
    "ds_read_u16_d16_hi v222, v209 offset:192\n"
    "v_mfma_f32_16x16x32_bf16 a[164:167], v[172:175], v[148:151], a[164:167]\n"
    "ds_read_b128 v[160:163], v200 offset:24576\n"
    "v_add_f32 v121, v121, v58\n"
    "v_mfma_f32_16x16x32_bf16 a[164:167], v[168:171], v[132:135], a[164:167]\n"
    "ds_read_b128 v[232:235], v205 offset:24576\n"
    "v_add_f32 v122, v122, v59\n"
    "v_mfma_f32_16x16x32_bf16 a[168:171], v[168:171], v[136:139], a[168:171]\n"
    "ds_read_b128 v[164:167], v200 offset:25600\n"
    "ds_read_b128 v[236:239], v206 offset:24576\n"
    "v_mfma_f32_16x16x32_bf16 a[168:171], v[172:175], v[152:155], a[168:171]\n"
    "v_add_f32 v125, v125, v60\n"
    "v_add_f32 v126, v126, v61\n"
    "v_mfma_f32_16x16x32_bf16 a[172:175], v[172:175], v[156:159], a[172:175]\n"
    "v_add_f32 v125, v125, v62\n"
    "v_add_f32 v126, v126, v63\n"
    "v_mfma_f32_16x16x32_bf16 a[172:175], v[168:171], v[140:143], a[172:175]\n"
    "ds_read_b64 v[228:229], v210 offset:64\n"
    "ds_read2st64_b32 v[240:241], v207 offset0:96 offset1:99\n"
    "v_mfma_f32_16x16x32_bf16 a[188:191], v[176:179], v[140:143], a[188:191]\n"
    "ds_read_b128 v[168:171], v200 offset:26624\n"
    "ds_read2st64_b32 v[242:243], v207 offset0:102 offset1:105\n"
    "v_mfma_f32_16x16x32_bf16 a[188:191], v[180:183], v[156:159], a[188:191]\n"
    "ds_read2st64_b32 v[244:245], v207 offset0:108 offset1:111\n"
    "ds_read2st64_b32 v[246:247], v207 offset0:114 offset1:117\n"
    "v_mfma_f32_16x16x32_bf16 a[184:187], v[176:179], v[136:139], a[184:187]\n"
    "v_cvt_pk_bf16_f32 v140, v44, v45\n"
    "v_cvt_pk_bf16_f32 v141, v46, v47\n"
    "v_mfma_f32_16x16x32_bf16 a[184:187], v[180:183], v[152:155], a[184:187]\n"
    "s_waitcnt lgkmcnt(6)\n"
    "v_mul_f32 v226, s75, v223\n"
    "v_mfma_f32_16x16x32_bf16 a[180:183], v[176:179], v[132:135], a[180:183]\n"
    "s_add_u32 s59, s59, 1\n"
    "ds_read_b128 v[172:175], v200 offset:27648\n"
    "v_fma_f32 v227, -v222, v226, v217\n"
    "v_mfma_f32_16x16x32_bf16 a[180:183], v[180:183], v[148:151], a[180:183]\n"
    "v_cvt_f32_i32 v232, v232\n"
    "v_cvt_f32_i32 v233, v233\n"
    "v_mfma_f32_16x16x32_bf16 a[176:179], v[176:179], v[128:131], a[176:179]\n"
    "s_cmp_lt_u32 s59, 15\n"
    "v_cvt_f32_i32 v234, v234\n"
    "v_cvt_f32_i32 v235, v235\n"
    "v_mfma_f32_16x16x32_bf16 a[176:179], v[180:183], v[144:147], a[176:179]\n"
    "s_cbranch_scc1 LLOOP1\n"
    "s_branch LTAIL\n"
    "LENT0:\n"
    "s_mov_b32 s59, 1\n"
    "s_add_u32 m0, s72, 0x21000\n"
    "s_nop 0\n"
    "buffer_load_dwordx4 v250, s[48:51], 0 offen offset:2048 lds\n"
    "s_add_u32 m0, s72, 0x18000\n"
    "s_nop 0\n"
    "buffer_load_dwordx4 v202, s[32:35], s64 offen lds\n"
    "buffer_load_dwordx4 v203, s[32:35], s64 offen offset:1024 lds\n"
    "buffer_load_dwordx4 v204, s[32:35], s64 offen offset:2048 lds\n"
    "s_add_u32 s64, s64, 0xc000\n"
    "s_mov_b32 s94, 0x3000\n"
    "s_mov_b32 s95, 0x6000\n"
    "s_add_u32 s51, s72, 0x18000\n"
    "v_add_u32 v205, 0x3000, v205\n"
    "v_add_u32 v206, 0x3000, v206\n"
    "v_add_u32 v207, 0x3000, v207\n"
    "buffer_store_dwordx2 v[220:221], v214, s[44:47], 0 offen sc1\n"
    "ds_read_b128 v[160:163], v200 offset:24576\n"
    "ds_read_u16_d16_hi v223, v209 offset:6336\n"
    "ds_read_u16_d16_hi v222, v209 offset:192\n"
    "v_add_f32 v121, v121, v58\n"
    "ds_read_b128 v[164:167], v200 offset:25600\n"
    "ds_read_b128 v[232:235], v205 offset:24576\n"
    "v_add_f32 v122, v122, v59\n"
    "ds_read_b128 v[236:239], v206 offset:24576\n"
    "v_add_f32 v125, v125, v60\n"
    "v_add_f32 v126, v126, v61\n"
    "v_add_f32 v125, v125, v62\n"
    "v_add_f32 v126, v126, v63\n"
    "ds_read_b128 v[168:171], v200 offset:26624\n"
    "v_cvt_pk_bf16_f32 v140, v44, v45\n"
    "v_cvt_pk_bf16_f32 v141, v46, v47\n"
    "v_cvt_pk_bf16_f32 v136, v40, v41\n"
    "v_cvt_pk_bf16_f32 v137, v42, v43\n"
    "ds_read_b128 v[172:175], v200 offset:27648\n"
    "s_waitcnt lgkmcnt(2)\n"
    "v_mul_f32 v226, s75, v223\n"
    "v_fma_f32 v227, -v222, v226, v217\n"
    "v_cvt_f32_i32 v232, v232\n"
    "v_cvt_f32_i32 v233, v233\n"
    "s_nop 7\n"
    ".p2align 6\n"
    "LLOOP:\n"
    "v_mfma_f32_16x16x32_bf16 v[64:67], v[160:163], a[192:195], v[96:99]\n"
    "ds_read_b128 v[176:179], v200 offset:28672\n"
    "v_cvt_f32_i32 v234, v234\n"
    "v_mfma_f32_16x16x32_bf16 v[64:67], v[164:167], a[196:199], v[64:67]\n"
    "s_waitcnt lgkmcnt(1)\n"
    "v_cvt_f32_i32 v235, v235\n"
    "v_cvt_f32_i32 v236, v236\n"
    "v_mfma_f32_16x16x32_bf16 v[64:67], v[168:171], a[200:203], v[64:67]\n"
    "v_cvt_f32_i32 v237, v237\n"
    "v_cvt_f32_i32 v238, v238\n"
    "v_mfma_f32_16x16x32_bf16 v[68:71], v[168:171], a[224:227], v[100:103]\n"
    "v_cvt_f32_i32 v239, v239\n"
    "v_mul_f32_dpp v232, v226, v232 row_newbcast:0 row_mask:0xf bank_mask:0xf\n"
    "v_mfma_f32_16x16x32_bf16 v[68:71], v[164:167], a[220:223], v[68:71]\n"
    "ds_read_b128 v[180:183], v200 offset:29696\n"
    "v_mul_f32_dpp v233, v226, v233 row_newbcast:1 row_mask:0xf bank_mask:0xf\n"
    "v_mfma_f32_16x16x32_bf16 v[68:71], v[160:163], a[216:219], v[68:71]\n"
    "v_mul_f32_dpp v234, v226, v234 row_newbcast:2 row_mask:0xf bank_mask:0xf\n"
    "v_mul_f32_dpp v235, v226, v235 row_newbcast:3 row_mask:0xf bank_mask:0xf\n"
    "v_mfma_f32_16x16x32_bf16 v[72:75], v[160:163], a[240:243], v[104:107]\n"
    "v_mul_f32_dpp v236, v226, v236 row_newbcast:4 row_mask:0xf bank_mask:0xf\n"
    "v_mul_f32_dpp v237, v226, v237 row_newbcast:5 row_mask:0xf bank_mask:0xf\n"
    "v_mfma_f32_16x16x32_bf16 v[72:75], v[164:167], a[244:247], v[72:75]\n"
    "v_mul_f32_dpp v238, v226, v238 row_newbcast:6 row_mask:0xf bank_mask:0xf\n"
    "v_mul_f32_dpp v239, v226, v239 row_newbcast:7 row_mask:0xf bank_mask:0xf\n"
    "v_mfma_f32_16x16x32_bf16 v[72:75], v[168:171], a[248:251], v[72:75]\n"
    "v_add_f32_dpp v232, v227, v232 row_newbcast:0 row_mask:0xf bank_mask:0xf\n"
    "v_add_f32_dpp v233, v227, v233 row_newbcast:1 row_mask:0xf bank_mask:0xf\n"
    "v_mfma_f32_16x16x32_bf16 v[76:79], v[168:171], v[16:19], v[108:111]\n"
    "v_add_f32_dpp v234, v227, v234 row_newbcast:2 row_mask:0xf bank_mask:0xf\n"
    "v_add_f32_dpp v235, v227, v235 row_newbcast:3 row_mask:0xf bank_mask:0xf\n"
    "v_mfma_f32_16x16x32_bf16 v[76:79], v[164:167], v[12:15], v[76:79]\n"
    "v_add_f32_dpp v236, v227, v236 row_newbcast:4 row_mask:0xf bank_mask:0xf\n"
    "v_add_f32_dpp v237, v227, v237 row_newbcast:5 row_mask:0xf bank_mask:0xf\n"
    "v_mfma_f32_16x16x32_bf16 v[76:79], v[160:163], v[8:11], v[76:79]\n"
    "s_waitcnt lgkmcnt(1)\n"
    "v_add_f32_dpp v238, v227, v238 row_newbcast:6 row_mask:0xf bank_mask:0xf\n"
    "v_add_f32_dpp v239, v227, v239 row_newbcast:7 row_mask:0xf bank_mask:0xf\n"
    "v_mfma_f32_16x16x32_bf16 v[76:79], v[172:175], v[20:23], v[76:79]\n"
    "ds_read_b128 v[160:163], v200 offset:33792\n"
    "v_cvt_pk_bf16_f32 v232, v232, v233\n"
    "v_mfma_f32_16x16x32_bf16 v[76:79], v[176:179], v[24:27], v[76:79]\n"
    "s_waitcnt lgkmcnt(1)\n"
    "v_cvt_pk_bf16_f32 v233, v234, v235\n"
    "v_cvt_pk_bf16_f32 v234, v236, v237\n"
    "v_mfma_f32_16x16x32_bf16 v[76:79], v[180:183], v[28:31], v[76:79]\n"
    "v_cvt_pk_bf16_f32 v235, v238, v239\n"
    "v_cvt_pk_bf16_f32 v132, v36, v37\n"
    "v_mfma_f32_16x16x32_bf16 v[72:75], v[180:183], v[4:7], v[72:75]\n"
    "ds_read_b128 v[164:167], v200 offset:34816\n"
    "v_cvt_pk_bf16_f32 v133, v38, v39\n"
    "v_mfma_f32_16x16x32_bf16 v[72:75], v[176:179], v[0:3], v[72:75]\n"
    "v_cvt_pk_bf16_f32 v128, v32, v33\n"
    "v_cvt_pk_bf16_f32 v129, v34, v35\n"
    "v_mfma_f32_16x16x32_bf16 v[72:75], v[172:175], a[252:255], v[72:75]\n"
    "v_cvt_pk_bf16_f32 v142, v60, v61\n"
    "v_cvt_pk_bf16_f32 v143, v62, v63\n"
    "v_mfma_f32_16x16x32_bf16 v[68:71], v[172:175], a[228:231], v[68:71]\n"
    "ds_read_b128 v[168:171], v200 offset:35840\n"
    "v_cvt_pk_bf16_f32 v138, v56, v57\n"
    "v_mfma_f32_16x16x32_bf16 v[68:71], v[176:179], a[232:235], v[68:71]\n"
    "v_cvt_pk_bf16_f32 v139, v58, v59\n"
    "v_cvt_pk_bf16_f32 v134, v52, v53\n"
    "v_mfma_f32_16x16x32_bf16 v[68:71], v[180:183], a[236:239], v[68:71]\n"
    "v_cvt_pk_bf16_f32 v135, v54, v55\n"
    "v_cvt_pk_bf16_f32 v130, v48, v49\n"
    "v_mfma_f32_16x16x32_bf16 v[64:67], v[180:183], a[212:215], v[64:67]\n"
    "v_cvt_pk_bf16_f32 v131, v50, v51\n"
    "v_mfma_f32_16x16x32_bf16 v[64:67], v[176:179], a[208:211], v[64:67]\n"
    "buffer_store_dwordx4 v[232:235], v211, s[36:39], s62 offen sc1\n"
    "v_mfma_f32_16x16x32_bf16 v[64:67], v[172:175], a[204:207], v[64:67]\n"
    "s_waitcnt lgkmcnt(1)\n"
    "v_mfma_f32_16x16x32_bf16 v[80:83], v[160:163], a[204:207], v[96:99]\n"
    "ds_read_b128 v[172:175], v200 offset:30720\n"
    "v_mfma_f32_16x16x32_bf16 v[80:83], v[164:167], a[208:211], v[80:83]\n"
    "s_waitcnt lgkmcnt(1)\n"
    "v_mfma_f32_16x16x32_bf16 v[80:83], v[168:171], a[212:215], v[80:83]\n"
    "v_mfma_f32_16x16x32_bf16 v[84:87], v[168:171], a[236:239], v[100:103]\n"
    "ds_read_b128 v[176:179], v200 offset:31744\n"
    "v_mfma_f32_16x16x32_bf16 v[84:87], v[164:167], a[232:235], v[84:87]\n"
    "v_exp_f32 v64, v64\n"
    "v_mfma_f32_16x16x32_bf16 v[84:87], v[160:163], a[228:231], v[84:87]\n"
    "v_exp_f32 v65, v65\n"
    "v_mfma_f32_16x16x32_bf16 v[88:91], v[160:163], a[252:255], v[104:107]\n"
    "ds_read_b128 v[180:183], v200 offset:32768\n"
    "v_cvt_pk_bf16_f32 v144, v64, v65\n"
    "v_mfma_f32_16x16x32_bf16 v[88:91], v[164:167], v[0:3], v[88:91]\n"
    "s_add_u32 s65, s60, 0\n"
    "v_exp_f32 v66, v66\n"
    "v_mfma_f32_16x16x32_bf16 v[88:91], v[168:171], v[4:7], v[88:91]\n"
    "s_mov_b32 m0, 0\n"
    "v_exp_f32 v67, v67\n"
    "v_mfma_f32_16x16x32_bf16 v[92:95], v[168:171], v[28:31], v[108:111]\n"
    "v_exp_f32 v68, v68\n"
    "v_mfma_f32_16x16x32_bf16 v[92:95], v[164:167], v[24:27], v[92:95]\n"
    "buffer_load_dwordx4 v200, s[36:39], s65 offen sc1 lds\n"
    "v_mfma_f32_16x16x32_bf16 v[92:95], v[160:163], v[20:23], v[92:95]\n"
    "s_waitcnt lgkmcnt(1)\n"
    "v_exp_f32 v69, v69\n"
    "v_mfma_f32_16x16x32_bf16 v[92:95], v[172:175], v[8:11], v[92:95]\n"
    "ds_read_b128 v[160:163], v200 offset:36864\n"
    "v_cvt_pk_bf16_f32 v145, v66, v67\n"
    "v_mfma_f32_16x16x32_bf16 v[92:95], v[176:179], v[12:15], v[92:95]\n"
    "s_waitcnt lgkmcnt(1)\n"
    "v_exp_f32 v70, v70\n"
    "v_mfma_f32_16x16x32_bf16 v[92:95], v[180:183], v[16:19], v[92:95]\n"
    "buffer_load_dwordx4 v200, s[36:39], s65 offen offset:1024 sc1 lds\n"
    "v_mfma_f32_16x16x32_bf16 v[88:91], v[180:183], a[248:251], v[88:91]\n"
    "ds_read_b128 v[164:167], v200 offset:37888\n"
    "v_cvt_pk_bf16_f32 v148, v68, v69\n"
    "v_mfma_f32_16x16x32_bf16 v[88:91], v[176:179], a[244:247], v[88:91]\n"
    "v_exp_f32 v71, v71\n"
    "v_mfma_f32_16x16x32_bf16 v[88:91], v[172:175], a[240:243], v[88:91]\n"
    "v_exp_f32 v72, v72\n"
    "v_mfma_f32_16x16x32_bf16 v[84:87], v[172:175], a[216:219], v[84:87]\n"
    "ds_read_b128 v[168:171], v200 offset:38912\n"
    "v_cvt_pk_bf16_f32 v149, v70, v71\n"
    "v_mfma_f32_16x16x32_bf16 v[84:87], v[176:179], a[220:223], v[84:87]\n"
    "buffer_load_dwordx4 v200, s[36:39], s65 offen offset:2048 sc1 lds\n"
    "v_mfma_f32_16x16x32_bf16 v[84:87], v[180:183], a[224:227], v[84:87]\n"
    "v_exp_f32 v73, v73\n"
    "v_mfma_f32_16x16x32_bf16 v[80:83], v[180:183], a[200:203], v[80:83]\n"
    "v_exp_f32 v74, v74\n"
    "v_mfma_f32_16x16x32_bf16 v[80:83], v[176:179], a[196:199], v[80:83]\n"
    "v_exp_f32 v75, v75\n"
    "v_mfma_f32_16x16x32_bf16 v[80:83], v[172:175], a[192:195], v[80:83]\n"
    "s_waitcnt lgkmcnt(1)\n"
    "buffer_load_dwordx4 v200, s[36:39], s65 offen offset:3072 sc1 lds\n"
    "v_mfma_f32_16x16x32_bf16 v[32:35], v[160:163], a[192:195], v[96:99]\n"
    "ds_read_b128 v[172:175], v200 offset:39936\n"
    "v_cvt_pk_bf16_f32 v152, v72, v73\n"
    "v_mfma_f32_16x16x32_bf16 v[32:35], v[164:167], a[196:199], v[32:35]\n"
    "s_waitcnt lgkmcnt(1)\n"
    "s_add_u32 s65, s60, 0x1000\n"
    "v_cvt_pk_bf16_f32 v153, v74, v75\n"
    "v_mfma_f32_16x16x32_bf16 v[32:35], v[168:171], a[200:203], v[32:35]\n"
    "s_mov_b32 m0, 0x1000\n"
    "v_exp_f32 v76, v76\n"
    "v_mfma_f32_16x16x32_bf16 v[36:39], v[168:171], a[224:227], v[100:103]\n"
    "ds_read_b128 v[176:179], v200 offset:40960\n"
    "v_add_f32 v113, v113, v64\n"
    "v_mfma_f32_16x16x32_bf16 v[36:39], v[164:167], a[220:223], v[36:39]\n"
    "buffer_load_dwordx4 v200, s[36:39], s65 offen sc1 lds\n"
    "v_mfma_f32_16x16x32_bf16 v[36:39], v[160:163], a[216:219], v[36:39]\n"
    "v_exp_f32 v77, v77\n"
    "v_mfma_f32_16x16x32_bf16 v[40:43], v[160:163], a[240:243], v[104:107]\n"
    "ds_read_b128 v[180:183], v200 offset:41984\n"
    "v_cvt_pk_bf16_f32 v156, v76, v77\n"
    "v_mfma_f32_16x16x32_bf16 v[40:43], v[164:167], a[244:247], v[40:43]\n"
    "v_exp_f32 v78, v78\n"
    "v_mfma_f32_16x16x32_bf16 v[40:43], v[168:171], a[248:251], v[40:43]\n"
    "buffer_load_dwordx4 v200, s[36:39], s65 offen offset:1024 sc1 lds\n"
    "v_mfma_f32_16x16x32_bf16 v[44:47], v[168:171], v[16:19], v[108:111]\n"
    "v_exp_f32 v79, v79\n"
    "v_mfma_f32_16x16x32_bf16 v[44:47], v[164:167], v[12:15], v[44:47]\n"
    "v_exp_f32 v80, v80\n"
    "v_mfma_f32_16x16x32_bf16 v[44:47], v[160:163], v[8:11], v[44:47]\n"
    "s_waitcnt lgkmcnt(1)\n"
    "v_exp_f32 v81, v81\n"
    "v_mfma_f32_16x16x32_bf16 v[44:47], v[172:175], v[20:23], v[44:47]\n"
    "ds_read_b128 v[160:163], v200 offset:46080\n"
    "v_cvt_pk_bf16_f32 v157, v78, v79\n"
    "v_mfma_f32_16x16x32_bf16 v[44:47], v[176:179], v[24:27], v[44:47]\n"
    "s_waitcnt lgkmcnt(1)\n"
    "buffer_load_dwordx4 v200, s[36:39], s65 offen offset:2048 sc1 lds\n"
    "v_mfma_f32_16x16x32_bf16 v[44:47], v[180:183], v[28:31], v[44:47]\n"
    "v_exp_f32 v82, v82\n"
    "v_mfma_f32_16x16x32_bf16 v[40:43], v[180:183], v[4:7], v[40:43]\n"
    "ds_read_b128 v[164:167], v200 offset:47104\n"
    "v_cvt_pk_bf16_f32 v146, v80, v81\n"
    "v_mfma_f32_16x16x32_bf16 v[40:43], v[176:179], v[0:3], v[40:43]\n"
    "v_exp_f32 v83, v83\n"
    "v_mfma_f32_16x16x32_bf16 v[40:43], v[172:175], a[252:255], v[40:43]\n"
    "buffer_load_dwordx4 v200, s[36:39], s65 offen offset:3072 sc1 lds\n"
    "v_mfma_f32_16x16x32_bf16 v[36:39], v[172:175], a[228:231], v[36:39]\n"
    "s_add_u32 s65, s61, 0\n"
    "ds_read_b128 v[168:171], v200 offset:48128\n"
    "v_cvt_pk_bf16_f32 v147, v82, v83\n"
    "v_mfma_f32_16x16x32_bf16 v[36:39], v[176:179], a[232:235], v[36:39]\n"
    "s_mov_b32 m0, 0x12000\n"
    "v_exp_f32 v84, v84\n"
    "v_mfma_f32_16x16x32_bf16 v[36:39], v[180:183], a[236:239], v[36:39]\n"
    "v_exp_f32 v85, v85\n"
    "v_mfma_f32_16x16x32_bf16 v[32:35], v[180:183], a[212:215], v[32:35]\n"
    "buffer_load_dwordx4 v200, s[40:43], s65 offen sc1 lds\n"
    "v_mfma_f32_16x16x32_bf16 v[32:35], v[176:179], a[208:211], v[32:35]\n"
    "v_exp_f32 v86, v86\n"
    "v_mfma_f32_16x16x32_bf16 v[32:35], v[172:175], a[204:207], v[32:35]\n"
    "s_waitcnt lgkmcnt(1)\n"
    "v_exp_f32 v87, v87\n"
    "v_mfma_f32_16x16x32_bf16 v[48:51], v[160:163], a[204:207], v[96:99]\n"
    "ds_read_b128 v[172:175], v200 offset:43008\n"
    "v_cvt_pk_bf16_f32 v150, v84, v85\n"
    "v_mfma_f32_16x16x32_bf16 v[48:51], v[164:167], a[208:211], v[48:51]\n"
    "s_waitcnt lgkmcnt(1)\n"
    "buffer_load_dwordx4 v200, s[40:43], s65 offen offset:1024 sc1 lds\n"
    "v_mfma_f32_16x16x32_bf16 v[48:51], v[168:171], a[212:215], v[48:51]\n"
    "v_exp_f32 v88, v88\n"
    "v_mfma_f32_16x16x32_bf16 v[52:55], v[168:171], a[236:239], v[100:103]\n"
    "ds_read_b128 v[176:179], v200 offset:44032\n"
    "v_cvt_pk_bf16_f32 v151, v86, v87\n"
    "v_mfma_f32_16x16x32_bf16 v[52:55], v[164:167], a[232:235], v[52:55]\n"
    "v_exp_f32 v89, v89\n"
    "v_mfma_f32_16x16x32_bf16 v[52:55], v[160:163], a[228:231], v[52:55]\n"
    "buffer_load_dwordx4 v200, s[40:43], s65 offen offset:2048 sc1 lds\n"
    "v_mfma_f32_16x16x32_bf16 v[56:59], v[160:163], a[252:255], v[104:107]\n"
    "ds_read_b128 v[180:183], v200 offset:45056\n"
    "v_cvt_pk_bf16_f32 v154, v88, v89\n"
    "v_mfma_f32_16x16x32_bf16 v[56:59], v[164:167], v[0:3], v[56:59]\n"
    "v_exp_f32 v90, v90\n"
    "v_mfma_f32_16x16x32_bf16 v[56:59], v[168:171], v[4:7], v[56:59]\n"
    "v_exp_f32 v91, v91\n"
    "v_mfma_f32_16x16x32_bf16 v[60:63], v[168:171], v[28:31], v[108:111]\n"
    "buffer_load_dwordx4 v200, s[40:43], s65 offen offset:3072 sc1 lds\n"
    "v_mfma_f32_16x16x32_bf16 v[60:63], v[164:167], v[24:27], v[60:63]\n"
    "s_add_u32 s65, s61, 0x1000\n"
    "v_exp_f32 v92, v92\n"
    "v_mfma_f32_16x16x32_bf16 v[60:63], v[160:163], v[20:23], v[60:63]\n"
    "s_waitcnt lgkmcnt(1)\n"
    "s_mov_b32 m0, 0x13000\n"
    "v_cvt_pk_bf16_f32 v155, v90, v91\n"
    "v_mfma_f32_16x16x32_bf16 v[60:63], v[172:175], v[8:11], v[60:63]\n"
    "ds_read_b128 v[160:163], v201\n"
    "v_add_f32 v114, v114, v65\n"
    "v_mfma_f32_16x16x32_bf16 v[60:63], v[176:179], v[12:15], v[60:63]\n"
    "s_waitcnt lgkmcnt(1)\n"
    "buffer_load_dwordx4 v200, s[40:43], s65 offen sc1 lds\n"
    "v_mfma_f32_16x16x32_bf16 v[60:63], v[180:183], v[16:19], v[60:63]\n"
    "v_exp_f32 v93, v93\n"
    "v_mfma_f32_16x16x32_bf16 v[56:59], v[180:183], a[248:251], v[56:59]\n"
    "ds_read_b128 v[164:167], v201 offset:1024\n"
    "v_cvt_pk_bf16_f32 v158, v92, v93\n"
    "v_mfma_f32_16x16x32_bf16 v[56:59], v[176:179], a[244:247], v[56:59]\n"
    "v_exp_f32 v94, v94\n"
    "v_mfma_f32_16x16x32_bf16 v[56:59], v[172:175], a[240:243], v[56:59]\n"
    "buffer_load_dwordx4 v200, s[40:43], s65 offen offset:1024 sc1 lds\n"
    "v_mfma_f32_16x16x32_bf16 v[52:55], v[172:175], a[216:219], v[52:55]\n"
    "ds_read_b128 v[168:171], v201 offset:2048\n"
    "v_add_f32 v113, v113, v66\n"
    "v_mfma_f32_16x16x32_bf16 v[52:55], v[176:179], a[220:223], v[52:55]\n"
    "v_exp_f32 v95, v95\n"
    "v_mfma_f32_16x16x32_bf16 v[52:55], v[180:183], a[224:227], v[52:55]\n"
    "v_cvt_pk_bf16_f32 v159, v94, v95\n"
    "v_add_f32 v114, v114, v67\n"
    "v_mfma_f32_16x16x32_bf16 v[48:51], v[180:183], a[200:203], v[48:51]\n"
    "buffer_load_dwordx4 v200, s[40:43], s65 offen offset:2048 sc1 lds\n"
    "v_mfma_f32_16x16x32_bf16 v[48:51], v[176:179], a[196:199], v[48:51]\n"
    "v_exp_f32 v32, v32\n"
    "v_mfma_f32_16x16x32_bf16 v[48:51], v[172:175], a[192:195], v[48:51]\n"
    "s_waitcnt lgkmcnt(1)\n"
    "v_exp_f32 v33, v33\n"
    "v_mfma_f32_16x16x32_bf16 a[0:3], v[160:163], v[128:131], a[0:3]\n"
    "ds_read_b128 v[172:175], v201 offset:3072\n"
    "ds_read_b128 v[176:179], v201 offset:4096\n"
    "v_mfma_f32_16x16x32_bf16 a[0:3], v[164:167], v[144:147], a[0:3]\n"
    "buffer_load_dwordx4 v200, s[40:43], s65 offen offset:3072 sc1 lds\n"
    "v_mfma_f32_16x16x32_bf16 a[4:7], v[160:163], v[132:135], a[4:7]\n"
    "v_exp_f32 v34, v34\n"
    "v_mfma_f32_16x16x32_bf16 a[4:7], v[164:167], v[148:151], a[4:7]\n"
    "v_exp_f32 v35, v35\n"
    "v_mfma_f32_16x16x32_bf16 a[8:11], v[160:163], v[136:139], a[8:11]\n"
    "ds_read_b128 v[180:183], v201 offset:5120\n"
    "v_add_f32 v117, v117, v68\n"
    "v_mfma_f32_16x16x32_bf16 a[8:11], v[164:167], v[152:155], a[8:11]\n"
    "v_exp_f32 v36, v36\n"
    "v_mfma_f32_16x16x32_bf16 a[12:15], v[160:163], v[140:143], a[12:15]\n"
    "v_exp_f32 v37, v37\n"
    "v_mfma_f32_16x16x32_bf16 a[12:15], v[164:167], v[156:159], a[12:15]\n"
    "s_waitcnt lgkmcnt(2)\n"
    "v_exp_f32 v38, v38\n"
    "v_mfma_f32_16x16x32_bf16 a[28:31], v[168:171], v[140:143], a[28:31]\n"
    "ds_read_b128 v[160:163], v201 offset:6144\n"
    "v_add_f32 v118, v118, v69\n"
    "v_mfma_f32_16x16x32_bf16 a[28:31], v[172:175], v[156:159], a[28:31]\n"
    "v_exp_f32 v39, v39\n"
    "v_mfma_f32_16x16x32_bf16 a[24:27], v[172:175], v[152:155], a[24:27]\n"
    "v_exp_f32 v40, v40\n"
    "v_mfma_f32_16x16x32_bf16 a[24:27], v[168:171], v[136:139], a[24:27]\n"
    "v_exp_f32 v41, v41\n"
    "v_mfma_f32_16x16x32_bf16 a[20:23], v[168:171], v[132:135], a[20:23]\n"
    "ds_read_b128 v[164:167], v201 offset:7168\n"
    "v_add_f32 v117, v117, v70\n"
    "v_mfma_f32_16x16x32_bf16 a[20:23], v[172:175], v[148:151], a[20:23]\n"
    "v_exp_f32 v42, v42\n"
    "v_mfma_f32_16x16x32_bf16 a[16:19], v[172:175], v[144:147], a[16:19]\n"
    "v_exp_f32 v43, v43\n"
    "v_mfma_f32_16x16x32_bf16 a[16:19], v[168:171], v[128:131], a[16:19]\n"
    "s_waitcnt lgkmcnt(2)\n"
    "v_exp_f32 v44, v44\n"
    "v_mfma_f32_16x16x32_bf16 a[32:35], v[176:179], v[128:131], a[32:35]\n"
    "ds_read_b128 v[168:171], v201 offset:8192\n"
    "v_add_f32 v113, v113, v32\n"
    "v_mfma_f32_16x16x32_bf16 a[32:35], v[180:183], v[144:147], a[32:35]\n"
    "buffer_load_dwordx2 v[218:219], v213, s[44:47], 0 offen sc1\n"
    "v_mfma_f32_16x16x32_bf16 a[36:39], v[180:183], v[148:151], a[36:39]\n"
    "v_exp_f32 v45, v45\n"
    "v_mfma_f32_16x16x32_bf16 a[36:39], v[176:179], v[132:135], a[36:39]\n"
    "v_exp_f32 v46, v46\n"
    "v_mfma_f32_16x16x32_bf16 a[40:43], v[176:179], v[136:139], a[40:43]\n"
    "ds_read_b128 v[172:175], v201 offset:9216\n"
    "v_add_f32 v118, v118, v71\n"
    "v_mfma_f32_16x16x32_bf16 a[40:43], v[180:183], v[152:155], a[40:43]\n"
    "v_exp_f32 v47, v47\n"
    "v_mfma_f32_16x16x32_bf16 a[44:47], v[180:183], v[156:159], a[44:47]\n"
    "v_exp_f32 v48, v48\n"
    "v_mfma_f32_16x16x32_bf16 a[44:47], v[176:179], v[140:143], a[44:47]\n"
    "s_waitcnt lgkmcnt(2)\n"
    "v_exp_f32 v49, v49\n"
    "v_mfma_f32_16x16x32_bf16 a[60:63], v[160:163], v[140:143], a[60:63]\n"
    "ds_read_b128 v[176:179], v201 offset:10240\n"
    "v_add_f32 v114, v114, v33\n"
    "v_mfma_f32_16x16x32_bf16 a[60:63], v[164:167], v[156:159], a[60:63]\n"
    "v_exp_f32 v50, v50\n"
    "v_mfma_f32_16x16x32_bf16 a[56:59], v[164:167], v[152:155], a[56:59]\n"
    "v_exp_f32 v51, v51\n"
    "v_mfma_f32_16x16x32_bf16 a[56:59], v[160:163], v[136:139], a[56:59]\n"
    "v_exp_f32 v52, v52\n"
    "v_mfma_f32_16x16x32_bf16 a[52:55], v[160:163], v[132:135], a[52:55]\n"
    "ds_read_b128 v[180:183], v201 offset:11264\n"
    "v_add_f32 v121, v121, v72\n"
    "v_mfma_f32_16x16x32_bf16 a[52:55], v[164:167], v[148:151], a[52:55]\n"
    "v_add_f32 v113, v113, v34\n"
    "v_add_f32 v122, v122, v73\n"
    "v_mfma_f32_16x16x32_bf16 a[48:51], v[164:167], v[144:147], a[48:51]\n"
    "v_exp_f32 v53, v53\n"
    "v_mfma_f32_16x16x32_bf16 a[48:51], v[160:163], v[128:131], a[48:51]\n"
    "s_waitcnt lgkmcnt(2)\n"
    "v_add_f32 v114, v114, v35\n"
    "v_add_f32 v121, v121, v74\n"
    "v_mfma_f32_16x16x32_bf16 a[64:67], v[168:171], v[128:131], a[64:67]\n"
    "ds_read_b128 v[160:163], v201 offset:12288\n"
    "v_add_f32 v117, v117, v36\n"
    "v_mfma_f32_16x16x32_bf16 a[64:67], v[172:175], v[144:147], a[64:67]\n"
    "v_exp_f32 v54, v54\n"
    "v_mfma_f32_16x16x32_bf16 a[68:71], v[172:175], v[148:151], a[68:71]\n"
    "v_add_f32 v122, v122, v75\n"
    "v_add_f32 v118, v118, v37\n"
    "v_mfma_f32_16x16x32_bf16 a[68:71], v[168:171], v[132:135], a[68:71]\n"
    "s_add_u32 m0, s51, s95\n"
    "v_add_f32 v125, v125, v76\n"
    "v_add_f32 v117, v117, v38\n"
    "v_mfma_f32_16x16x32_bf16 a[72:75], v[168:171], v[136:139], a[72:75]\n"
    "ds_read_b128 v[164:167], v201 offset:13312\n"
    "v_add_f32 v126, v126, v77\n"
    "v_mfma_f32_16x16x32_bf16 a[72:75], v[172:175], v[152:155], a[72:75]\n"
    "buffer_load_dwordx4 v202, s[32:35], s64 offen lds\n"
    "v_mfma_f32_16x16x32_bf16 a[76:79], v[172:175], v[156:159], a[76:79]\n"
    "v_exp_f32 v55, v55\n"
    "v_mfma_f32_16x16x32_bf16 a[76:79], v[168:171], v[140:143], a[76:79]\n"
    "s_waitcnt lgkmcnt(2)\n"
    "v_add_f32 v118, v118, v39\n"
    "v_add_f32 v125, v125, v78\n"
    "v_mfma_f32_16x16x32_bf16 a[92:95], v[176:179], v[140:143], a[92:95]\n"
    "ds_read_b128 v[168:171], v201 offset:14336\n"
    "v_add_f32 v121, v121, v40\n"
    "v_mfma_f32_16x16x32_bf16 a[92:95], v[180:183], v[156:159], a[92:95]\n"
    "buffer_load_dwordx4 v203, s[32:35], s64 offen offset:1024 lds\n"
    "v_mfma_f32_16x16x32_bf16 a[88:91], v[180:183], v[152:155], a[88:91]\n"
    "v_exp_f32 v56, v56\n"
    "v_mfma_f32_16x16x32_bf16 a[88:91], v[176:179], v[136:139], a[88:91]\n"
    "v_add_f32 v126, v126, v79\n"
    "v_add_f32 v122, v122, v41\n"
    "v_mfma_f32_16x16x32_bf16 a[84:87], v[176:179], v[132:135], a[84:87]\n"
    "ds_read_b128 v[172:175], v201 offset:15360\n"
    "v_add_f32 v113, v113, v80\n"
    "v_mfma_f32_16x16x32_bf16 a[84:87], v[180:183], v[148:151], a[84:87]\n"
    "buffer_load_dwordx4 v204, s[32:35], s64 offen offset:2048 lds\n"
    "v_mfma_f32_16x16x32_bf16 a[80:83], v[180:183], v[144:147], a[80:83]\n"
    "v_add_f32 v121, v121, v42\n"
    "v_add_f32 v114, v114, v81\n"
    "v_mfma_f32_16x16x32_bf16 a[80:83], v[176:179], v[128:131], a[80:83]\n"
    "s_waitcnt lgkmcnt(2)\n"
    "v_exp_f32 v57, v57\n"
    "v_mfma_f32_16x16x32_bf16 a[96:99], v[160:163], v[128:131], a[96:99]\n"
    "s_mov_b32 s95, s94\n"
    "ds_read_b128 v[176:179], v201 offset:16384\n"
    "v_add_f32 v122, v122, v43\n"
    "v_mfma_f32_16x16x32_bf16 a[96:99], v[164:167], v[144:147], a[96:99]\n"
    "s_sub_u32 s94, s94, 0x3000\n"
    "v_add_f32 v113, v113, v82\n"
    "v_add_f32 v125, v125, v44\n"
    "v_mfma_f32_16x16x32_bf16 a[100:103], v[164:167], v[148:151], a[100:103]\n"
    "s_min_u32 s94, s94, 0x6000\n"
    "v_exp_f32 v58, v58\n"
    "v_mfma_f32_16x16x32_bf16 a[100:103], v[160:163], v[132:135], a[100:103]\n"
    "s_sub_u32 s93, s94, s95\n"
    "v_add_f32 v114, v114, v83\n"
    "v_add_f32 v126, v126, v45\n"
    "v_mfma_f32_16x16x32_bf16 a[104:107], v[160:163], v[136:139], a[104:107]\n"
    "ds_read_b128 v[180:183], v201 offset:17408\n"
    "v_add_f32 v117, v117, v84\n"
    "v_mfma_f32_16x16x32_bf16 a[104:107], v[164:167], v[152:155], a[104:107]\n"
    "v_add_f32 v125, v125, v46\n"
    "v_add_f32 v118, v118, v85\n"
    "v_mfma_f32_16x16x32_bf16 a[108:111], v[164:167], v[156:159], a[108:111]\n"
    "v_exp_f32 v59, v59\n"
    "v_mfma_f32_16x16x32_bf16 a[108:111], v[160:163], v[140:143], a[108:111]\n"
    "s_waitcnt lgkmcnt(2)\n"
    "v_add_f32 v126, v126, v47\n"
    "v_add_f32 v117, v117, v86\n"
    "v_mfma_f32_16x16x32_bf16 a[124:127], v[168:171], v[140:143], a[124:127]\n"
    "ds_read_b128 v[160:163], v201 offset:18432\n"
    "v_add_f32 v113, v113, v48\n"
    "v_mfma_f32_16x16x32_bf16 a[124:127], v[172:175], v[156:159], a[124:127]\n"
    "v_exp_f32 v60, v60\n"
    "v_mfma_f32_16x16x32_bf16 a[120:123], v[172:175], v[152:155], a[120:123]\n"
    "v_add_f32 v118, v118, v87\n"
    "v_add_f32 v114, v114, v49\n"
    "v_mfma_f32_16x16x32_bf16 a[120:123], v[168:171], v[136:139], a[120:123]\n"
    "v_add_u32 v220, 1, v220\n"
    "v_add_u32 v205, s93, v205\n"
    "v_mfma_f32_16x16x32_bf16 a[116:119], v[168:171], v[132:135], a[116:119]\n"
    "ds_read_b128 v[164:167], v201 offset:19456\n"
    "v_add_f32 v121, v121, v88\n"
    "v_mfma_f32_16x16x32_bf16 a[116:119], v[172:175], v[148:151], a[116:119]\n"
    "v_add_f32 v113, v113, v50\n"
    "v_add_u32 v209, 0x180, v209\n"
    "v_mfma_f32_16x16x32_bf16 a[112:115], v[172:175], v[144:147], a[112:115]\n"
    "v_exp_f32 v61, v61\n"
    "v_mfma_f32_16x16x32_bf16 a[112:115], v[168:171], v[128:131], a[112:115]\n"
    "s_waitcnt lgkmcnt(2)\n"
    "v_add_u32 v206, s93, v206\n"
    "v_add_f32 v122, v122, v89\n"
    "v_mfma_f32_16x16x32_bf16 a[128:131], v[176:179], v[128:131], a[128:131]\n"
    "ds_read_b128 v[168:171], v201 offset:20480\n"
    "v_add_f32 v114, v114, v51\n"
    "v_mfma_f32_16x16x32_bf16 a[128:131], v[180:183], v[144:147], a[128:131]\n"
    "s_add_u32 s64, s64, 0xc000\n"
    "v_add_f32 v121, v121, v90\n"
    "v_add_f32 v117, v117, v52\n"
    "v_mfma_f32_16x16x32_bf16 a[132:135], v[180:183], v[148:151], a[132:135]\n"
    "s_add_u32 s62, s62, 0x6000\n"
    "v_exp_f32 v62, v62\n"
    "v_mfma_f32_16x16x32_bf16 a[132:135], v[176:179], v[132:135], a[132:135]\n"
    "s_add_u32 s63, s63, 0x6000\n"
    "v_add_f32 v122, v122, v91\n"
    "v_add_f32 v118, v118, v53\n"
    "v_mfma_f32_16x16x32_bf16 a[136:139], v[176:179], v[136:139], a[136:139]\n"
    "s_add_u32 s60, s60, 0x6000\n"
    "ds_read_b128 v[172:175], v201 offset:21504\n"
    "v_add_f32 v125, v125, v92\n"
    "v_mfma_f32_16x16x32_bf16 a[136:139], v[180:183], v[152:155], a[136:139]\n"
    "s_add_u32 s61, s61, 0x6000\n"
    "v_add_f32 v117, v117, v54\n"
    "v_add_f32 v126, v126, v93\n"
    "v_mfma_f32_16x16x32_bf16 a[140:143], v[180:183], v[156:159], a[140:143]\n"
    "s_add_u32 s66, s66, s74\n"
    "v_exp_f32 v63, v63\n"
    "v_mfma_f32_16x16x32_bf16 a[140:143], v[176:179], v[140:143], a[140:143]\n"
    "s_waitcnt lgkmcnt(2)\n"
    "v_add_f32 v118, v118, v55\n"
    "v_add_f32 v125, v125, v94\n"
    "v_mfma_f32_16x16x32_bf16 a[156:159], v[160:163], v[140:143], a[156:159]\n"
    "s_min_u32 s66, s66, 33\n"
    "ds_read_b128 v[176:179], v201 offset:22528\n"
    "v_add_f32 v121, v121, v56\n"
    "v_mfma_f32_16x16x32_bf16 a[156:159], v[164:167], v[156:159], a[156:159]\n"
    "ds_read_b128 v[180:183], v201 offset:23552\n"
    "v_add_f32 v126, v126, v95\n"
    "v_mfma_f32_16x16x32_bf16 a[152:155], v[164:167], v[152:155], a[152:155]\n"
    "s_waitcnt vmcnt(3)\n"
    "v_add_f32 v122, v122, v57\n"
    "v_mfma_f32_16x16x32_bf16 a[152:155], v[160:163], v[136:139], a[152:155]\n"
    "v_cmp_ne_u32 s[80:81], s67, v219\n"
    "v_mfma_f32_16x16x32_bf16 a[148:151], v[160:163], v[132:135], a[148:151]\n"
    "v_cmp_gt_u32 vcc, s66, v218\n"
    "v_mfma_f32_16x16x32_bf16 a[148:151], v[164:167], v[148:151], a[148:151]\n"
    "s_or_b64 vcc, vcc, s[80:81]\n"
    "v_mfma_f32_16x16x32_bf16 a[144:147], v[164:167], v[144:147], a[144:147]\n"
    "s_cbranch_vccnz LSPRE4\n"
    "LSPB4:\n"
    "v_mfma_f32_16x16x32_bf16 a[144:147], v[160:163], v[128:131], a[144:147]\n"
    "s_waitcnt lgkmcnt(0)\n"
    "v_mfma_f32_16x16x32_bf16 a[160:163], v[168:171], v[128:131], a[160:163]\n"
    "s_barrier\n"
    "v_mfma_f32_16x16x32_bf16 a[160:163], v[172:175], v[144:147], a[160:163]\n"
    "buffer_store_dwordx2 v[220:221], v214, s[44:47], 0 offen sc1\n"
    "v_mfma_f32_16x16x32_bf16 a[164:167], v[172:175], v[148:151], a[164:167]\n"
    "ds_read_b128 v[160:163], v200\n"
    "ds_read_u16_d16_hi v223, v209 offset:6144\n"
    "v_mfma_f32_16x16x32_bf16 a[164:167], v[168:171], v[132:135], a[164:167]\n"
    "ds_read_u16_d16_hi v222, v209\n"
    "v_add_f32 v121, v121, v58\n"
    "v_mfma_f32_16x16x32_bf16 a[168:171], v[168:171], v[136:139], a[168:171]\n"
    "ds_read_b128 v[164:167], v200 offset:1024\n"
    "ds_read_b128 v[232:235], v205 offset:24576\n"
    "v_mfma_f32_16x16x32_bf16 a[168:171], v[172:175], v[152:155], a[168:171]\n"
    "v_add_f32 v122, v122, v59\n"
    "ds_read_b128 v[236:239], v206 offset:24576\n"
    "v_mfma_f32_16x16x32_bf16 a[172:175], v[172:175], v[156:159], a[172:175]\n"
    "v_add_f32 v125, v125, v60\n"
    "v_add_f32 v126, v126, v61\n"
    "v_mfma_f32_16x16x32_bf16 a[172:175], v[168:171], v[140:143], a[172:175]\n"
    "v_add_f32 v125, v125, v62\n"
    "v_add_f32 v126, v126, v63\n"
    "v_mfma_f32_16x16x32_bf16 a[188:191], v[176:179], v[140:143], a[188:191]\n"
    "ds_read_b128 v[168:171], v200 offset:2048\n"
    "v_cvt_pk_bf16_f32 v140, v44, v45\n"
    "v_mfma_f32_16x16x32_bf16 a[188:191], v[180:183], v[156:159], a[188:191]\n"
    "v_cvt_pk_bf16_f32 v141, v46, v47\n"
    "v_mfma_f32_16x16x32_bf16 a[184:187], v[176:179], v[136:139], a[184:187]\n"
    "v_cvt_pk_bf16_f32 v136, v40, v41\n"
    "v_cvt_pk_bf16_f32 v137, v42, v43\n"
    "v_mfma_f32_16x16x32_bf16 a[184:187], v[180:183], v[152:155], a[184:187]\n"
    "v_mfma_f32_16x16x32_bf16 a[180:183], v[176:179], v[132:135], a[180:183]\n"
    "ds_read_b128 v[172:175], v200 offset:3072\n"
    "s_waitcnt lgkmcnt(2)\n"
    "v_mfma_f32_16x16x32_bf16 a[180:183], v[180:183], v[148:151], a[180:183]\n"
    "v_mul_f32 v226, s75, v223\n"
    "v_fma_f32 v227, -v222, v226, v217\n"
    "v_mfma_f32_16x16x32_bf16 a[176:179], v[176:179], v[128:131], a[176:179]\n"
    "v_cvt_f32_i32 v232, v232\n"
    "v_cvt_f32_i32 v233, v233\n"
    "v_mfma_f32_16x16x32_bf16 a[176:179], v[180:183], v[144:147], a[176:179]\n"
    "v_cvt_f32_i32 v234, v234\n"
    "v_cvt_f32_i32 v235, v235\n"
    "LM0:\n"
    "v_mfma_f32_16x16x32_bf16 v[64:67], v[160:163], a[192:195], v[96:99]\n"
    "ds_read_b128 v[176:179], v200 offset:4096\n"
    "v_cvt_f32_i32 v236, v236\n"
    "v_mfma_f32_16x16x32_bf16 v[64:67], v[164:167], a[196:199], v[64:67]\n"
    "s_waitcnt lgkmcnt(1)\n"
    "v_cvt_f32_i32 v237, v237\n"
    "v_cvt_f32_i32 v238, v238\n"
    "v_mfma_f32_16x16x32_bf16 v[64:67], v[168:171], a[200:203], v[64:67]\n"
    "v_cvt_f32_i32 v239, v239\n"
    "v_mul_f32_dpp v232, v226, v232 row_newbcast:0 row_mask:0xf bank_mask:0xf\n"
    "v_mfma_f32_16x16x32_bf16 v[68:71], v[168:171], a[224:227], v[100:103]\n"
    "v_mul_f32_dpp v233, v226, v233 row_newbcast:1 row_mask:0xf bank_mask:0xf\n"
    "v_mul_f32_dpp v234, v226, v234 row_newbcast:2 row_mask:0xf bank_mask:0xf\n"
    "v_mfma_f32_16x16x32_bf16 v[68:71], v[164:167], a[220:223], v[68:71]\n"
    "ds_read_b128 v[180:183], v200 offset:5120\n"
    "v_mul_f32_dpp v235, v226, v235 row_newbcast:3 row_mask:0xf bank_mask:0xf\n"
    "v_mfma_f32_16x16x32_bf16 v[68:71], v[160:163], a[216:219], v[68:71]\n"
    "v_mul_f32_dpp v236, v226, v236 row_newbcast:4 row_mask:0xf bank_mask:0xf\n"
    "v_mul_f32_dpp v237, v226, v237 row_newbcast:5 row_mask:0xf bank_mask:0xf\n"
    "v_mfma_f32_16x16x32_bf16 v[72:75], v[160:163], a[240:243], v[104:107]\n"
    "v_mul_f32_dpp v238, v226, v238 row_newbcast:6 row_mask:0xf bank_mask:0xf\n"
    "v_mul_f32_dpp v239, v226, v239 row_newbcast:7 row_mask:0xf bank_mask:0xf\n"
    "v_mfma_f32_16x16x32_bf16 v[72:75], v[164:167], a[244:247], v[72:75]\n"
    "v_add_f32_dpp v232, v227, v232 row_newbcast:0 row_mask:0xf bank_mask:0xf\n"
    "v_add_f32_dpp v233, v227, v233 row_newbcast:1 row_mask:0xf bank_mask:0xf\n"
    "v_mfma_f32_16x16x32_bf16 v[72:75], v[168:171], a[248:251], v[72:75]\n"
    "v_add_f32_dpp v234, v227, v234 row_newbcast:2 row_mask:0xf bank_mask:0xf\n"
    "v_add_f32_dpp v235, v227, v235 row_newbcast:3 row_mask:0xf bank_mask:0xf\n"
    "v_mfma_f32_16x16x32_bf16 v[76:79], v[168:171], v[16:19], v[108:111]\n"
    "v_add_f32_dpp v236, v227, v236 row_newbcast:4 row_mask:0xf bank_mask:0xf\n"
    "v_add_f32_dpp v237, v227, v237 row_newbcast:5 row_mask:0xf bank_mask:0xf\n"
    "v_mfma_f32_16x16x32_bf16 v[76:79], v[164:167], v[12:15], v[76:79]\n"
    "v_add_f32_dpp v238, v227, v238 row_newbcast:6 row_mask:0xf bank_mask:0xf\n"
    "v_add_f32_dpp v239, v227, v239 row_newbcast:7 row_mask:0xf bank_mask:0xf\n"
    "v_mfma_f32_16x16x32_bf16 v[76:79], v[160:163], v[8:11], v[76:79]\n"
    "s_waitcnt lgkmcnt(1)\n"
    "v_cvt_pk_bf16_f32 v232, v232, v233\n"
    "v_cvt_pk_bf16_f32 v233, v234, v235\n"
    "v_mfma_f32_16x16x32_bf16 v[76:79], v[172:175], v[20:23], v[76:79]\n"
    "ds_read_b128 v[160:163], v200 offset:9216\n"
    "v_cvt_pk_bf16_f32 v234, v236, v237\n"
    "v_mfma_f32_16x16x32_bf16 v[76:79], v[176:179], v[24:27], v[76:79]\n"
    "s_waitcnt lgkmcnt(1)\n"
    "v_cvt_pk_bf16_f32 v235, v238, v239\n"
    "v_cvt_pk_bf16_f32 v132, v36, v37\n"
    "v_mfma_f32_16x16x32_bf16 v[76:79], v[180:183], v[28:31], v[76:79]\n"
    "buffer_store_dwordx4 v[232:235], v211, s[36:39], s62 offen sc1\n"
    "v_cvt_pk_bf16_f32 v133, v38, v39\n"
    "v_cvt_pk_bf16_f32 v128, v32, v33\n"
    "v_cvt_pk_bf16_f32 v129, v34, v35\n"
    "v_mfma_f32_16x16x32_bf16 v[72:75], v[180:183], v[4:7], v[72:75]\n"
    "ds_read_b128 v[164:167], v200 offset:10240\n"
    "v_cvt_pk_bf16_f32 v142, v60, v61\n"
    "v_mfma_f32_16x16x32_bf16 v[72:75], v[176:179], v[0:3], v[72:75]\n"
    "v_cvt_pk_bf16_f32 v143, v62, v63\n"
    "v_cvt_pk_bf16_f32 v138, v56, v57\n"
    "v_mfma_f32_16x16x32_bf16 v[72:75], v[172:175], a[252:255], v[72:75]\n"
    "v_cvt_pk_bf16_f32 v139, v58, v59\n"
    "v_cvt_pk_bf16_f32 v134, v52, v53\n"
    "v_mfma_f32_16x16x32_bf16 v[68:71], v[172:175], a[228:231], v[68:71]\n"
    "ds_read_b128 v[168:171], v200 offset:11264\n"
    "v_cvt_pk_bf16_f32 v135, v54, v55\n"
    "v_mfma_f32_16x16x32_bf16 v[68:71], v[176:179], a[232:235], v[68:71]\n"
    "v_cvt_pk_bf16_f32 v130, v48, v49\n"
    "v_cvt_pk_bf16_f32 v131, v50, v51\n"
    "v_mfma_f32_16x16x32_bf16 v[68:71], v[180:183], a[236:239], v[68:71]\n"
    "v_mfma_f32_16x16x32_bf16 v[64:67], v[180:183], a[212:215], v[64:67]\n"
    "v_mfma_f32_16x16x32_bf16 v[64:67], v[176:179], a[208:211], v[64:67]\n"
    "v_mfma_f32_16x16x32_bf16 v[64:67], v[172:175], a[204:207], v[64:67]\n"
    "s_waitcnt lgkmcnt(1)\n"
    "v_mfma_f32_16x16x32_bf16 v[80:83], v[160:163], a[204:207], v[96:99]\n"
    "ds_read_b128 v[172:175], v200 offset:6144\n"
    "v_mfma_f32_16x16x32_bf16 v[80:83], v[164:167], a[208:211], v[80:83]\n"
    "s_waitcnt lgkmcnt(1)\n"
    "v_mfma_f32_16x16x32_bf16 v[80:83], v[168:171], a[212:215], v[80:83]\n"
    "v_mfma_f32_16x16x32_bf16 v[84:87], v[168:171], a[236:239], v[100:103]\n"
    "ds_read_b128 v[176:179], v200 offset:7168\n"
    "v_mfma_f32_16x16x32_bf16 v[84:87], v[164:167], a[232:235], v[84:87]\n"
    "v_exp_f32 v64, v64\n"
    "v_mfma_f32_16x16x32_bf16 v[84:87], v[160:163], a[228:231], v[84:87]\n"
    "v_exp_f32 v65, v65\n"
    "v_mfma_f32_16x16x32_bf16 v[88:91], v[160:163], a[252:255], v[104:107]\n"
    "ds_read_b128 v[180:183], v200 offset:8192\n"
    "v_cvt_pk_bf16_f32 v144, v64, v65\n"
    "v_mfma_f32_16x16x32_bf16 v[88:91], v[164:167], v[0:3], v[88:91]\n"
    "s_add_u32 s65, s60, 0\n"
    "v_exp_f32 v66, v66\n"
    "v_mfma_f32_16x16x32_bf16 v[88:91], v[168:171], v[4:7], v[88:91]\n"
    "s_mov_b32 m0, 0x6000\n"
    "v_exp_f32 v67, v67\n"
    "v_mfma_f32_16x16x32_bf16 v[92:95], v[168:171], v[28:31], v[108:111]\n"
    "v_exp_f32 v68, v68\n"
    "v_mfma_f32_16x16x32_bf16 v[92:95], v[164:167], v[24:27], v[92:95]\n"
    "buffer_load_dwordx4 v200, s[36:39], s65 offen sc1 lds\n"
    "v_mfma_f32_16x16x32_bf16 v[92:95], v[160:163], v[20:23], v[92:95]\n"
    "s_waitcnt lgkmcnt(1)\n"
    "v_exp_f32 v69, v69\n"
    "v_mfma_f32_16x16x32_bf16 v[92:95], v[172:175], v[8:11], v[92:95]\n"
    "ds_read_b128 v[160:163], v200 offset:12288\n"
    "v_cvt_pk_bf16_f32 v145, v66, v67\n"
    "v_mfma_f32_16x16x32_bf16 v[92:95], v[176:179], v[12:15], v[92:95]\n"
    "s_waitcnt lgkmcnt(1)\n"
    "v_exp_f32 v70, v70\n"
    "v_mfma_f32_16x16x32_bf16 v[92:95], v[180:183], v[16:19], v[92:95]\n"
    "buffer_load_dwordx4 v200, s[36:39], s65 offen offset:1024 sc1 lds\n"
    "v_mfma_f32_16x16x32_bf16 v[88:91], v[180:183], a[248:251], v[88:91]\n"
    "ds_read_b128 v[164:167], v200 offset:13312\n"
    "v_cvt_pk_bf16_f32 v148, v68, v69\n"
    "v_mfma_f32_16x16x32_bf16 v[88:91], v[176:179], a[244:247], v[88:91]\n"
    "v_exp_f32 v71, v71\n"
    "v_mfma_f32_16x16x32_bf16 v[88:91], v[172:175], a[240:243], v[88:91]\n"
    "v_exp_f32 v72, v72\n"
    "v_mfma_f32_16x16x32_bf16 v[84:87], v[172:175], a[216:219], v[84:87]\n"
    "ds_read_b128 v[168:171], v200 offset:14336\n"
    "v_cvt_pk_bf16_f32 v149, v70, v71\n"
    "v_mfma_f32_16x16x32_bf16 v[84:87], v[176:179], a[220:223], v[84:87]\n"
    "buffer_load_dwordx4 v200, s[36:39], s65 offen offset:2048 sc1 lds\n"
    "v_mfma_f32_16x16x32_bf16 v[84:87], v[180:183], a[224:227], v[84:87]\n"
    "v_exp_f32 v73, v73\n"
    "v_mfma_f32_16x16x32_bf16 v[80:83], v[180:183], a[200:203], v[80:83]\n"
    "v_exp_f32 v74, v74\n"
    "v_mfma_f32_16x16x32_bf16 v[80:83], v[176:179], a[196:199], v[80:83]\n"
    "v_exp_f32 v75, v75\n"
    "v_mfma_f32_16x16x32_bf16 v[80:83], v[172:175], a[192:195], v[80:83]\n"
    "s_waitcnt lgkmcnt(1)\n"
    "buffer_load_dwordx4 v200, s[36:39], s65 offen offset:3072 sc1 lds\n"
    "v_mfma_f32_16x16x32_bf16 v[32:35], v[160:163], a[192:195], v[96:99]\n"
    "ds_read_b128 v[172:175], v200 offset:15360\n"
    "v_cvt_pk_bf16_f32 v152, v72, v73\n"
    "v_mfma_f32_16x16x32_bf16 v[32:35], v[164:167], a[196:199], v[32:35]\n"
    "s_waitcnt lgkmcnt(1)\n"
    "s_add_u32 s65, s60, 0x1000\n"
    "v_cvt_pk_bf16_f32 v153, v74, v75\n"
    "v_mfma_f32_16x16x32_bf16 v[32:35], v[168:171], a[200:203], v[32:35]\n"
    "s_mov_b32 m0, 0x7000\n"
    "v_exp_f32 v76, v76\n"
    "v_mfma_f32_16x16x32_bf16 v[36:39], v[168:171], a[224:227], v[100:103]\n"
    "ds_read_b128 v[176:179], v200 offset:16384\n"
    "v_add_f32 v113, v113, v64\n"
    "v_mfma_f32_16x16x32_bf16 v[36:39], v[164:167], a[220:223], v[36:39]\n"
    "buffer_load_dwordx4 v200, s[36:39], s65 offen sc1 lds\n"
    "v_mfma_f32_16x16x32_bf16 v[36:39], v[160:163], a[216:219], v[36:39]\n"
    "v_exp_f32 v77, v77\n"
    "v_mfma_f32_16x16x32_bf16 v[40:43], v[160:163], a[240:243], v[104:107]\n"
    "ds_read_b128 v[180:183], v200 offset:17408\n"
    "v_cvt_pk_bf16_f32 v156, v76, v77\n"
    "v_mfma_f32_16x16x32_bf16 v[40:43], v[164:167], a[244:247], v[40:43]\n"
    "v_exp_f32 v78, v78\n"
    "v_mfma_f32_16x16x32_bf16 v[40:43], v[168:171], a[248:251], v[40:43]\n"
    "buffer_load_dwordx4 v200, s[36:39], s65 offen offset:1024 sc1 lds\n"
    "v_mfma_f32_16x16x32_bf16 v[44:47], v[168:171], v[16:19], v[108:111]\n"
    "v_exp_f32 v79, v79\n"
    "v_mfma_f32_16x16x32_bf16 v[44:47], v[164:167], v[12:15], v[44:47]\n"
    "v_exp_f32 v80, v80\n"
    "v_mfma_f32_16x16x32_bf16 v[44:47], v[160:163], v[8:11], v[44:47]\n"
    "s_waitcnt lgkmcnt(1)\n"
    "v_exp_f32 v81, v81\n"
    "v_mfma_f32_16x16x32_bf16 v[44:47], v[172:175], v[20:23], v[44:47]\n"
    "ds_read_b128 v[160:163], v200 offset:21504\n"
    "v_cvt_pk_bf16_f32 v157, v78, v79\n"
    "v_mfma_f32_16x16x32_bf16 v[44:47], v[176:179], v[24:27], v[44:47]\n"
    "s_waitcnt lgkmcnt(1)\n"
    "buffer_load_dwordx4 v200, s[36:39], s65 offen offset:2048 sc1 lds\n"
    "v_mfma_f32_16x16x32_bf16 v[44:47], v[180:183], v[28:31], v[44:47]\n"
    "v_exp_f32 v82, v82\n"
    "v_mfma_f32_16x16x32_bf16 v[40:43], v[180:183], v[4:7], v[40:43]\n"
    "ds_read_b128 v[164:167], v200 offset:22528\n"
    "v_cvt_pk_bf16_f32 v146, v80, v81\n"
    "v_mfma_f32_16x16x32_bf16 v[40:43], v[176:179], v[0:3], v[40:43]\n"
    "v_exp_f32 v83, v83\n"
    "v_mfma_f32_16x16x32_bf16 v[40:43], v[172:175], a[252:255], v[40:43]\n"
    "buffer_load_dwordx4 v200, s[36:39], s65 offen offset:3072 sc1 lds\n"
    "v_mfma_f32_16x16x32_bf16 v[36:39], v[172:175], a[228:231], v[36:39]\n"
    "s_add_u32 s65, s61, 0\n"
    "ds_read_b128 v[168:171], v200 offset:23552\n"
    "v_cvt_pk_bf16_f32 v147, v82, v83\n"
    "v_mfma_f32_16x16x32_bf16 v[36:39], v[176:179], a[232:235], v[36:39]\n"
    "s_mov_b32 m0, 0xc000\n"
    "v_exp_f32 v84, v84\n"
    "v_mfma_f32_16x16x32_bf16 v[36:39], v[180:183], a[236:239], v[36:39]\n"
    "v_exp_f32 v85, v85\n"
    "v_mfma_f32_16x16x32_bf16 v[32:35], v[180:183], a[212:215], v[32:35]\n"
    "buffer_load_dwordx4 v200, s[40:43], s65 offen sc1 lds\n"
    "v_mfma_f32_16x16x32_bf16 v[32:35], v[176:179], a[208:211], v[32:35]\n"
    "v_exp_f32 v86, v86\n"
    "v_mfma_f32_16x16x32_bf16 v[32:35], v[172:175], a[204:207], v[32:35]\n"
    "s_waitcnt lgkmcnt(1)\n"
    "v_exp_f32 v87, v87\n"
    "v_mfma_f32_16x16x32_bf16 v[48:51], v[160:163], a[204:207], v[96:99]\n"
    "ds_read_b128 v[172:175], v200 offset:18432\n"
    "v_cvt_pk_bf16_f32 v150, v84, v85\n"
    "v_mfma_f32_16x16x32_bf16 v[48:51], v[164:167], a[208:211], v[48:51]\n"
    "s_waitcnt lgkmcnt(1)\n"
    "buffer_load_dwordx4 v200, s[40:43], s65 offen offset:1024 sc1 lds\n"
    "v_mfma_f32_16x16x32_bf16 v[48:51], v[168:171], a[212:215], v[48:51]\n"
    "v_exp_f32 v88, v88\n"
    "v_mfma_f32_16x16x32_bf16 v[52:55], v[168:171], a[236:239], v[100:103]\n"
    "ds_read_b128 v[176:179], v200 offset:19456\n"
    "v_cvt_pk_bf16_f32 v151, v86, v87\n"
    "v_mfma_f32_16x16x32_bf16 v[52:55], v[164:167], a[232:235], v[52:55]\n"
    "v_exp_f32 v89, v89\n"
    "v_mfma_f32_16x16x32_bf16 v[52:55], v[160:163], a[228:231], v[52:55]\n"
    "buffer_load_dwordx4 v200, s[40:43], s65 offen offset:2048 sc1 lds\n"
    "v_mfma_f32_16x16x32_bf16 v[56:59], v[160:163], a[252:255], v[104:107]\n"
    "ds_read_b128 v[180:183], v200 offset:20480\n"
    "v_cvt_pk_bf16_f32 v154, v88, v89\n"
    "v_mfma_f32_16x16x32_bf16 v[56:59], v[164:167], v[0:3], v[56:59]\n"
    "v_exp_f32 v90, v90\n"
    "v_mfma_f32_16x16x32_bf16 v[56:59], v[168:171], v[4:7], v[56:59]\n"
    "v_exp_f32 v91, v91\n"
    "v_mfma_f32_16x16x32_bf16 v[60:63], v[168:171], v[28:31], v[108:111]\n"
    "buffer_load_dwordx4 v200, s[40:43], s65 offen offset:3072 sc1 lds\n"
    "v_mfma_f32_16x16x32_bf16 v[60:63], v[164:167], v[24:27], v[60:63]\n"
    "s_add_u32 s65, s61, 0x1000\n"
    "v_exp_f32 v92, v92\n"
    "v_mfma_f32_16x16x32_bf16 v[60:63], v[160:163], v[20:23], v[60:63]\n"
    "s_waitcnt lgkmcnt(1)\n"
    "s_mov_b32 m0, 0xd000\n"
    "v_cvt_pk_bf16_f32 v155, v90, v91\n"
    "v_mfma_f32_16x16x32_bf16 v[60:63], v[172:175], v[8:11], v[60:63]\n"
    "ds_read_b128 v[160:163], v201 offset:24576\n"
    "v_add_f32 v114, v114, v65\n"
    "v_mfma_f32_16x16x32_bf16 v[60:63], v[176:179], v[12:15], v[60:63]\n"
    "s_waitcnt lgkmcnt(1)\n"
    "buffer_load_dwordx4 v200, s[40:43], s65 offen sc1 lds\n"
    "v_mfma_f32_16x16x32_bf16 v[60:63], v[180:183], v[16:19], v[60:63]\n"
    "v_exp_f32 v93, v93\n"
    "v_mfma_f32_16x16x32_bf16 v[56:59], v[180:183], a[248:251], v[56:59]\n"
    "ds_read_b128 v[164:167], v201 offset:25600\n"
    "v_cvt_pk_bf16_f32 v158, v92, v93\n"
    "v_mfma_f32_16x16x32_bf16 v[56:59], v[176:179], a[244:247], v[56:59]\n"
    "v_exp_f32 v94, v94\n"
    "v_mfma_f32_16x16x32_bf16 v[56:59], v[172:175], a[240:243], v[56:59]\n"
    "buffer_load_dwordx4 v200, s[40:43], s65 offen offset:1024 sc1 lds\n"
    "v_mfma_f32_16x16x32_bf16 v[52:55], v[172:175], a[216:219], v[52:55]\n"
    "ds_read_b128 v[168:171], v201 offset:26624\n"
    "v_add_f32 v113, v113, v66\n"
    "v_mfma_f32_16x16x32_bf16 v[52:55], v[176:179], a[220:223], v[52:55]\n"
    "v_exp_f32 v95, v95\n"
    "v_mfma_f32_16x16x32_bf16 v[52:55], v[180:183], a[224:227], v[52:55]\n"
    "v_cvt_pk_bf16_f32 v159, v94, v95\n"
    "v_add_f32 v114, v114, v67\n"
    "v_mfma_f32_16x16x32_bf16 v[48:51], v[180:183], a[200:203], v[48:51]\n"
    "buffer_load_dwordx4 v200, s[40:43], s65 offen offset:2048 sc1 lds\n"
    "v_mfma_f32_16x16x32_bf16 v[48:51], v[176:179], a[196:199], v[48:51]\n"
    "v_exp_f32 v32, v32\n"
    "v_mfma_f32_16x16x32_bf16 v[48:51], v[172:175], a[192:195], v[48:51]\n"
    "s_waitcnt lgkmcnt(1)\n"
    "v_exp_f32 v33, v33\n"
    "v_mfma_f32_16x16x32_bf16 a[0:3], v[160:163], v[128:131], a[0:3]\n"
    "ds_read_b128 v[172:175], v201 offset:27648\n"
    "ds_read_b128 v[176:179], v201 offset:28672\n"
    "v_mfma_f32_16x16x32_bf16 a[0:3], v[164:167], v[144:147], a[0:3]\n"
    "buffer_load_dwordx4 v200, s[40:43], s65 offen offset:3072 sc1 lds\n"
    "v_mfma_f32_16x16x32_bf16 a[4:7], v[160:163], v[132:135], a[4:7]\n"
    "v_exp_f32 v34, v34\n"
    "v_mfma_f32_16x16x32_bf16 a[4:7], v[164:167], v[148:151], a[4:7]\n"
    "v_exp_f32 v35, v35\n"
    "v_mfma_f32_16x16x32_bf16 a[8:11], v[160:163], v[136:139], a[8:11]\n"
    "ds_read_b128 v[180:183], v201 offset:29696\n"
    "v_add_f32 v117, v117, v68\n"
    "v_mfma_f32_16x16x32_bf16 a[8:11], v[164:167], v[152:155], a[8:11]\n"
    "v_exp_f32 v36, v36\n"
    "v_mfma_f32_16x16x32_bf16 a[12:15], v[160:163], v[140:143], a[12:15]\n"
    "v_exp_f32 v37, v37\n"
    "v_mfma_f32_16x16x32_bf16 a[12:15], v[164:167], v[156:159], a[12:15]\n"
    "s_waitcnt lgkmcnt(2)\n"
    "v_exp_f32 v38, v38\n"
    "v_mfma_f32_16x16x32_bf16 a[28:31], v[168:171], v[140:143], a[28:31]\n"
    "ds_read_b128 v[160:163], v201 offset:30720\n"
    "v_add_f32 v118, v118, v69\n"
    "v_mfma_f32_16x16x32_bf16 a[28:31], v[172:175], v[156:159], a[28:31]\n"
    "v_exp_f32 v39, v39\n"
    "v_mfma_f32_16x16x32_bf16 a[24:27], v[172:175], v[152:155], a[24:27]\n"
    "v_exp_f32 v40, v40\n"
    "v_mfma_f32_16x16x32_bf16 a[24:27], v[168:171], v[136:139], a[24:27]\n"
    "v_exp_f32 v41, v41\n"
    "v_mfma_f32_16x16x32_bf16 a[20:23], v[168:171], v[132:135], a[20:23]\n"
    "ds_read_b128 v[164:167], v201 offset:31744\n"
    "v_add_f32 v117, v117, v70\n"
    "v_mfma_f32_16x16x32_bf16 a[20:23], v[172:175], v[148:151], a[20:23]\n"
    "v_exp_f32 v42, v42\n"
    "v_mfma_f32_16x16x32_bf16 a[16:19], v[172:175], v[144:147], a[16:19]\n"
    "v_exp_f32 v43, v43\n"
    "v_mfma_f32_16x16x32_bf16 a[16:19], v[168:171], v[128:131], a[16:19]\n"
    "s_waitcnt lgkmcnt(2)\n"
    "v_exp_f32 v44, v44\n"
    "v_mfma_f32_16x16x32_bf16 a[32:35], v[176:179], v[128:131], a[32:35]\n"
    "ds_read_b128 v[168:171], v201 offset:32768\n"
    "v_add_f32 v113, v113, v32\n"
    "v_mfma_f32_16x16x32_bf16 a[32:35], v[180:183], v[144:147], a[32:35]\n"
    "buffer_load_dwordx2 v[218:219], v213, s[44:47], 0 offen sc1\n"
    "v_mfma_f32_16x16x32_bf16 a[36:39], v[180:183], v[148:151], a[36:39]\n"
    "v_exp_f32 v45, v45\n"
    "v_mfma_f32_16x16x32_bf16 a[36:39], v[176:179], v[132:135], a[36:39]\n"
    "v_exp_f32 v46, v46\n"
    "v_mfma_f32_16x16x32_bf16 a[40:43], v[176:179], v[136:139], a[40:43]\n"
    "ds_read_b128 v[172:175], v201 offset:33792\n"
    "v_add_f32 v118, v118, v71\n"
    "v_mfma_f32_16x16x32_bf16 a[40:43], v[180:183], v[152:155], a[40:43]\n"
    "v_exp_f32 v47, v47\n"
    "v_mfma_f32_16x16x32_bf16 a[44:47], v[180:183], v[156:159], a[44:47]\n"
    "v_exp_f32 v48, v48\n"
    "v_mfma_f32_16x16x32_bf16 a[44:47], v[176:179], v[140:143], a[44:47]\n"
    "s_waitcnt lgkmcnt(2)\n"
    "v_exp_f32 v49, v49\n"
    "v_mfma_f32_16x16x32_bf16 a[60:63], v[160:163], v[140:143], a[60:63]\n"
    "ds_read_b128 v[176:179], v201 offset:34816\n"
    "v_add_f32 v114, v114, v33\n"
    "v_mfma_f32_16x16x32_bf16 a[60:63], v[164:167], v[156:159], a[60:63]\n"
    "v_exp_f32 v50, v50\n"
    "v_mfma_f32_16x16x32_bf16 a[56:59], v[164:167], v[152:155], a[56:59]\n"
    "v_exp_f32 v51, v51\n"
    "v_mfma_f32_16x16x32_bf16 a[56:59], v[160:163], v[136:139], a[56:59]\n"
    "v_exp_f32 v52, v52\n"
    "v_mfma_f32_16x16x32_bf16 a[52:55], v[160:163], v[132:135], a[52:55]\n"
    "ds_read_b128 v[180:183], v201 offset:35840\n"
    "v_add_f32 v121, v121, v72\n"
    "v_mfma_f32_16x16x32_bf16 a[52:55], v[164:167], v[148:151], a[52:55]\n"
    "v_add_f32 v113, v113, v34\n"
    "v_add_f32 v122, v122, v73\n"
    "v_mfma_f32_16x16x32_bf16 a[48:51], v[164:167], v[144:147], a[48:51]\n"
    "v_exp_f32 v53, v53\n"
    "v_mfma_f32_16x16x32_bf16 a[48:51], v[160:163], v[128:131], a[48:51]\n"
    "s_waitcnt lgkmcnt(2)\n"
    "v_add_f32 v114, v114, v35\n"
    "v_add_f32 v121, v121, v74\n"
    "v_mfma_f32_16x16x32_bf16 a[64:67], v[168:171], v[128:131], a[64:67]\n"
    "ds_read_b128 v[160:163], v201 offset:36864\n"
    "v_add_f32 v117, v117, v36\n"
    "v_mfma_f32_16x16x32_bf16 a[64:67], v[172:175], v[144:147], a[64:67]\n"
    "v_exp_f32 v54, v54\n"
    "v_mfma_f32_16x16x32_bf16 a[68:71], v[172:175], v[148:151], a[68:71]\n"
    "v_add_f32 v122, v122, v75\n"
    "v_add_f32 v118, v118, v37\n"
    "v_mfma_f32_16x16x32_bf16 a[68:71], v[168:171], v[132:135], a[68:71]\n"
    "s_add_u32 m0, s51, s95\n"
    "v_add_f32 v125, v125, v76\n"
    "v_add_f32 v117, v117, v38\n"
    "v_mfma_f32_16x16x32_bf16 a[72:75], v[168:171], v[136:139], a[72:75]\n"
    "ds_read_b128 v[164:167], v201 offset:37888\n"
    "v_add_f32 v126, v126, v77\n"
    "v_mfma_f32_16x16x32_bf16 a[72:75], v[172:175], v[152:155], a[72:75]\n"
    "buffer_load_dwordx4 v202, s[32:35], s64 offen lds\n"
    "v_mfma_f32_16x16x32_bf16 a[76:79], v[172:175], v[156:159], a[76:79]\n"
    "v_exp_f32 v55, v55\n"
    "v_mfma_f32_16x16x32_bf16 a[76:79], v[168:171], v[140:143], a[76:79]\n"
    "s_waitcnt lgkmcnt(2)\n"
    "v_add_f32 v118, v118, v39\n"
    "v_add_f32 v125, v125, v78\n"
    "v_mfma_f32_16x16x32_bf16 a[92:95], v[176:179], v[140:143], a[92:95]\n"
    "ds_read_b128 v[168:171], v201 offset:38912\n"
    "v_add_f32 v121, v121, v40\n"
    "v_mfma_f32_16x16x32_bf16 a[92:95], v[180:183], v[156:159], a[92:95]\n"
    "buffer_load_dwordx4 v203, s[32:35], s64 offen offset:1024 lds\n"
    "v_mfma_f32_16x16x32_bf16 a[88:91], v[180:183], v[152:155], a[88:91]\n"
    "v_exp_f32 v56, v56\n"
    "v_mfma_f32_16x16x32_bf16 a[88:91], v[176:179], v[136:139], a[88:91]\n"
    "v_add_f32 v126, v126, v79\n"
    "v_add_f32 v122, v122, v41\n"
    "v_mfma_f32_16x16x32_bf16 a[84:87], v[176:179], v[132:135], a[84:87]\n"
    "ds_read_b128 v[172:175], v201 offset:39936\n"
    "v_add_f32 v113, v113, v80\n"
    "v_mfma_f32_16x16x32_bf16 a[84:87], v[180:183], v[148:151], a[84:87]\n"
    "buffer_load_dwordx4 v204, s[32:35], s64 offen offset:2048 lds\n"
    "v_mfma_f32_16x16x32_bf16 a[80:83], v[180:183], v[144:147], a[80:83]\n"
    "v_add_f32 v121, v121, v42\n"
    "v_add_f32 v114, v114, v81\n"
    "v_mfma_f32_16x16x32_bf16 a[80:83], v[176:179], v[128:131], a[80:83]\n"
    "s_waitcnt lgkmcnt(2)\n"
    "v_exp_f32 v57, v57\n"
    "v_mfma_f32_16x16x32_bf16 a[96:99], v[160:163], v[128:131], a[96:99]\n"
    "s_mov_b32 s95, s94\n"
    "ds_read_b128 v[176:179], v201 offset:40960\n"
    "v_add_f32 v122, v122, v43\n"
    "v_mfma_f32_16x16x32_bf16 a[96:99], v[164:167], v[144:147], a[96:99]\n"
    "s_sub_u32 s94, s94, 0x3000\n"
    "v_add_f32 v113, v113, v82\n"
    "v_add_f32 v125, v125, v44\n"
    "v_mfma_f32_16x16x32_bf16 a[100:103], v[164:167], v[148:151], a[100:103]\n"
    "s_min_u32 s94, s94, 0x6000\n"
    "v_exp_f32 v58, v58\n"
    "v_mfma_f32_16x16x32_bf16 a[100:103], v[160:163], v[132:135], a[100:103]\n"
    "s_sub_u32 s93, s94, s95\n"
    "v_add_f32 v114, v114, v83\n"
    "v_add_f32 v126, v126, v45\n"
    "v_mfma_f32_16x16x32_bf16 a[104:107], v[160:163], v[136:139], a[104:107]\n"
    "ds_read_b128 v[180:183], v201 offset:41984\n"
    "v_add_f32 v117, v117, v84\n"
    "v_mfma_f32_16x16x32_bf16 a[104:107], v[164:167], v[152:155], a[104:107]\n"
    "v_add_f32 v125, v125, v46\n"
    "v_add_f32 v118, v118, v85\n"
    "v_mfma_f32_16x16x32_bf16 a[108:111], v[164:167], v[156:159], a[108:111]\n"
    "v_exp_f32 v59, v59\n"
    "v_mfma_f32_16x16x32_bf16 a[108:111], v[160:163], v[140:143], a[108:111]\n"
    "s_waitcnt lgkmcnt(2)\n"
    "v_add_f32 v126, v126, v47\n"
    "v_add_f32 v117, v117, v86\n"
    "v_mfma_f32_16x16x32_bf16 a[124:127], v[168:171], v[140:143], a[124:127]\n"
    "ds_read_b128 v[160:163], v201 offset:43008\n"
    "v_add_f32 v113, v113, v48\n"
    "v_mfma_f32_16x16x32_bf16 a[124:127], v[172:175], v[156:159], a[124:127]\n"
    "v_exp_f32 v60, v60\n"
    "v_mfma_f32_16x16x32_bf16 a[120:123], v[172:175], v[152:155], a[120:123]\n"
    "v_add_f32 v118, v118, v87\n"
    "v_add_f32 v114, v114, v49\n"
    "v_mfma_f32_16x16x32_bf16 a[120:123], v[168:171], v[136:139], a[120:123]\n"
    "v_add_u32 v205, s93, v205\n"
    "v_add_f32 v121, v121, v88\n"
    "v_mfma_f32_16x16x32_bf16 a[116:119], v[168:171], v[132:135], a[116:119]\n"
    "ds_read_b128 v[164:167], v201 offset:44032\n"
    "v_add_f32 v113, v113, v50\n"
    "v_mfma_f32_16x16x32_bf16 a[116:119], v[172:175], v[148:151], a[116:119]\n"
    "v_exp_f32 v61, v61\n"
    "v_mfma_f32_16x16x32_bf16 a[112:115], v[172:175], v[144:147], a[112:115]\n"
    "v_add_u32 v220, 1, v220\n"
    "v_add_u32 v206, s93, v206\n"
    "v_mfma_f32_16x16x32_bf16 a[112:115], v[168:171], v[128:131], a[112:115]\n"
    "s_waitcnt lgkmcnt(2)\n"
    "v_add_f32 v122, v122, v89\n"
    "v_add_f32 v114, v114, v51\n"
    "v_mfma_f32_16x16x32_bf16 a[128:131], v[176:179], v[128:131], a[128:131]\n"
    "ds_read_b128 v[168:171], v201 offset:45056\n"
    "v_add_f32 v121, v121, v90\n"
    "v_mfma_f32_16x16x32_bf16 a[128:131], v[180:183], v[144:147], a[128:131]\n"
    "s_add_u32 s64, s64, 0xc000\n"
    "v_add_f32 v117, v117, v52\n"
    "v_add_f32 v122, v122, v91\n"
    "v_mfma_f32_16x16x32_bf16 a[132:135], v[180:183], v[148:151], a[132:135]\n"
    "s_add_u32 s62, s62, 0x6000\n"
    "v_exp_f32 v62, v62\n"
    "v_mfma_f32_16x16x32_bf16 a[132:135], v[176:179], v[132:135], a[132:135]\n"
    "s_add_u32 s63, s63, 0x6000\n"
    "v_add_f32 v118, v118, v53\n"
    "v_add_f32 v125, v125, v92\n"
    "v_mfma_f32_16x16x32_bf16 a[136:139], v[176:179], v[136:139], a[136:139]\n"
    "s_add_u32 s60, s60, 0x6000\n"
    "ds_read_b128 v[172:175], v201 offset:46080\n"
    "v_add_f32 v117, v117, v54\n"
    "v_mfma_f32_16x16x32_bf16 a[136:139], v[180:183], v[152:155], a[136:139]\n"
    "s_add_u32 s61, s61, 0x6000\n"
    "v_exp_f32 v63, v63\n"
    "v_mfma_f32_16x16x32_bf16 a[140:143], v[180:183], v[156:159], a[140:143]\n"
    "s_add_u32 s66, s66, s74\n"
    "v_add_f32 v126, v126, v93\n"
    "v_add_f32 v118, v118, v55\n"
    "v_mfma_f32_16x16x32_bf16 a[140:143], v[176:179], v[140:143], a[140:143]\n"
    "s_waitcnt lgkmcnt(2)\n"
    "v_add_f32 v125, v125, v94\n"
    "v_add_f32 v121, v121, v56\n"
    "v_mfma_f32_16x16x32_bf16 a[156:159], v[160:163], v[140:143], a[156:159]\n"
    "s_min_u32 s66, s66, 33\n"
    "ds_read_b128 v[176:179], v201 offset:47104\n"
    "v_add_f32 v126, v126, v95\n"
    "v_mfma_f32_16x16x32_bf16 a[156:159], v[164:167], v[156:159], a[156:159]\n"
    "ds_read_b128 v[180:183], v201 offset:48128\n"
    "v_add_f32 v122, v122, v57\n"
    "v_mfma_f32_16x16x32_bf16 a[152:155], v[164:167], v[152:155], a[152:155]\n"
    "s_waitcnt vmcnt(3)\n"
    "v_mfma_f32_16x16x32_bf16 a[152:155], v[160:163], v[136:139], a[152:155]\n"
    "v_cmp_ne_u32 s[80:81], s67, v219\n"
    "v_mfma_f32_16x16x32_bf16 a[148:151], v[160:163], v[132:135], a[148:151]\n"
    "v_cmp_gt_u32 vcc, s66, v218\n"
    "v_mfma_f32_16x16x32_bf16 a[148:151], v[164:167], v[148:151], a[148:151]\n"
    "s_or_b64 vcc, vcc, s[80:81]\n"
    "v_mfma_f32_16x16x32_bf16 a[144:147], v[164:167], v[144:147], a[144:147]\n"
    "s_cbranch_vccnz LSPRE3\n"
    "LSPB3:\n"
    "v_mfma_f32_16x16x32_bf16 a[144:147], v[160:163], v[128:131], a[144:147]\n"
    "s_waitcnt lgkmcnt(0)\n"
    "v_mfma_f32_16x16x32_bf16 a[160:163], v[168:171], v[128:131], a[160:163]\n"
    "s_barrier\n"
    "LMID:\n"
    "v_mfma_f32_16x16x32_bf16 a[160:163], v[172:175], v[144:147], a[160:163]\n"
    "buffer_store_dwordx2 v[220:221], v214, s[44:47], 0 offen sc1\n"
    "v_mfma_f32_16x16x32_bf16 a[164:167], v[172:175], v[148:151], a[164:167]\n"
    "ds_read_b128 v[160:163], v200 offset:24576\n"
    "ds_read_u16_d16_hi v223, v209 offset:6336\n"
    "v_mfma_f32_16x16x32_bf16 a[164:167], v[168:171], v[132:135], a[164:167]\n"
    "ds_read_u16_d16_hi v222, v209 offset:192\n"
    "v_add_f32 v121, v121, v58\n"
    "v_mfma_f32_16x16x32_bf16 a[168:171], v[168:171], v[136:139], a[168:171]\n"
    "ds_read_b128 v[164:167], v200 offset:25600\n"
    "ds_read_b128 v[232:235], v205 offset:24576\n"
    "v_mfma_f32_16x16x32_bf16 a[168:171], v[172:175], v[152:155], a[168:171]\n"
    "v_add_f32 v122, v122, v59\n"
    "ds_read_b128 v[236:239], v206 offset:24576\n"
    "v_mfma_f32_16x16x32_bf16 a[172:175], v[172:175], v[156:159], a[172:175]\n"
    "v_add_f32 v125, v125, v60\n"
    "v_add_f32 v126, v126, v61\n"
    "v_mfma_f32_16x16x32_bf16 a[172:175], v[168:171], v[140:143], a[172:175]\n"
    "v_add_f32 v125, v125, v62\n"
    "v_add_f32 v126, v126, v63\n"
    "v_mfma_f32_16x16x32_bf16 a[188:191], v[176:179], v[140:143], a[188:191]\n"
    "ds_read_b128 v[168:171], v200 offset:26624\n"
    "v_cvt_pk_bf16_f32 v140, v44, v45\n"
    "v_mfma_f32_16x16x32_bf16 a[188:191], v[180:183], v[156:159], a[188:191]\n"
    "v_cvt_pk_bf16_f32 v141, v46, v47\n"
    "v_mfma_f32_16x16x32_bf16 a[184:187], v[176:179], v[136:139], a[184:187]\n"
    "v_cvt_pk_bf16_f32 v136, v40, v41\n"
    "v_cvt_pk_bf16_f32 v137, v42, v43\n"
    "v_mfma_f32_16x16x32_bf16 a[184:187], v[180:183], v[152:155], a[184:187]\n"
    "v_mfma_f32_16x16x32_bf16 a[180:183], v[176:179], v[132:135], a[180:183]\n"
    "s_add_u32 s59, s59, 1\n"
    "ds_read_b128 v[172:175], v200 offset:27648\n"
    "s_waitcnt lgkmcnt(2)\n"
    "v_mfma_f32_16x16x32_bf16 a[180:183], v[180:183], v[148:151], a[180:183]\n"
    "v_mul_f32 v226, s75, v223\n"
    "v_fma_f32 v227, -v222, v226, v217\n"
    "v_mfma_f32_16x16x32_bf16 a[176:179], v[176:179], v[128:131], a[176:179]\n"
    "s_cmp_lt_u32 s59, 15\n"
    "v_cvt_f32_i32 v232, v232\n"
    "v_cvt_f32_i32 v233, v233\n"
    "v_mfma_f32_16x16x32_bf16 a[176:179], v[180:183], v[144:147], a[176:179]\n"
    "s_cbranch_scc1 LLOOP\n"
    "LTAIL:\n"
    "s_waitcnt lgkmcnt(0)\n"
    "s_nop 1\n"
    "buffer_load_dword v251, v252, s[52:55], s91 offen\n"
    "v_mfma_f32_16x16x32_bf16 v[64:67], v[160:163], a[192:195], v[96:99]\n"
    "ds_read_b128 v[176:179], v200 offset:28672\n"
    "v_cvt_pk_bf16_f32 v128, v32, v33\n"
    "v_mfma_f32_16x16x32_bf16 v[64:67], v[164:167], a[196:199], v[64:67]\n"
    "s_add_u32 m0, s71, 0\n"
    "v_cvt_pk_bf16_f32 v129, v34, v35\n"
    "v_cvt_pk_bf16_f32 v132, v36, v37\n"
    "v_mfma_f32_16x16x32_bf16 v[68:71], v[164:167], a[220:223], v[100:103]\n"
    "buffer_load_dwordx4 v200, s[36:39], s60 offen sc1 lds\n"
    "v_mfma_f32_16x16x32_bf16 v[68:71], v[160:163], a[216:219], v[68:71]\n"
    "v_cvt_pk_bf16_f32 v133, v38, v39\n"
    "v_cvt_pk_bf16_f32 v136, v40, v41\n"
    "v_mfma_f32_16x16x32_bf16 v[72:75], v[160:163], a[240:243], v[104:107]\n"
    "ds_read_b128 v[180:183], v200 offset:29696\n"
    "v_cvt_pk_bf16_f32 v137, v42, v43\n"
    "v_mfma_f32_16x16x32_bf16 v[72:75], v[164:167], a[244:247], v[72:75]\n"
    "v_cvt_pk_bf16_f32 v130, v48, v49\n"
    "v_cvt_pk_bf16_f32 v131, v50, v51\n"
    "v_mfma_f32_16x16x32_bf16 v[76:79], v[164:167], v[12:15], v[108:111]\n"
    "buffer_load_dwordx4 v200, s[36:39], s60 offen offset:1024 sc1 lds\n"
    "v_mfma_f32_16x16x32_bf16 v[76:79], v[160:163], v[8:11], v[76:79]\n"
    "v_cvt_pk_bf16_f32 v134, v52, v53\n"
    "v_cvt_pk_bf16_f32 v135, v54, v55\n"
    "v_mfma_f32_16x16x32_bf16 v[76:79], v[168:171], v[16:19], v[76:79]\n"
    "ds_read_b128 v[160:163], v200 offset:34816\n"
    "v_cvt_pk_bf16_f32 v138, v56, v57\n"
    "v_mfma_f32_16x16x32_bf16 v[76:79], v[172:175], v[20:23], v[76:79]\n"
    "v_cvt_pk_bf16_f32 v139, v58, v59\n"
    "v_cvt_pk_bf16_f32 v142, v60, v61\n"
    "v_mfma_f32_16x16x32_bf16 v[72:75], v[172:175], a[252:255], v[72:75]\n"
    "buffer_load_dwordx4 v200, s[36:39], s60 offen offset:2048 sc1 lds\n"
    "v_mfma_f32_16x16x32_bf16 v[72:75], v[168:171], a[248:251], v[72:75]\n"
    "v_cvt_pk_bf16_f32 v143, v62, v63\n"
    "v_add_f32 v113, v113, v114\n"
    "v_mfma_f32_16x16x32_bf16 v[68:71], v[168:171], a[224:227], v[68:71]\n"
    "ds_read_b128 v[164:167], v200 offset:35840\n"
    "v_add_f32 v117, v117, v118\n"
    "v_mfma_f32_16x16x32_bf16 v[68:71], v[172:175], a[228:231], v[68:71]\n"
    "v_add_f32 v121, v121, v122\n"
    "v_add_f32 v125, v125, v126\n"
    "v_mfma_f32_16x16x32_bf16 v[64:67], v[172:175], a[204:207], v[64:67]\n"
    "buffer_load_dwordx4 v200, s[36:39], s60 offen offset:3072 sc1 lds\n"
    "v_mfma_f32_16x16x32_bf16 v[64:67], v[168:171], a[200:203], v[64:67]\n"
    "s_waitcnt lgkmcnt(2)\n"
    "v_mov_b32 v115, v113\n"
    "v_mov_b32 v119, v117\n"
    "v_mfma_f32_16x16x32_bf16 v[64:67], v[176:179], a[208:211], v[64:67]\n"
    "s_add_u32 s65, s60, 0x1000\n"
    "ds_read_b128 v[168:171], v200 offset:32768\n"
    "v_mov_b32 v123, v121\n"
    "v_mfma_f32_16x16x32_bf16 v[64:67], v[180:183], a[212:215], v[64:67]\n"
    "s_add_u32 m0, s71, 0x1000\n"
    "v_mov_b32 v127, v125\n"
    "v_permlane32_swap_b32 v113, v115\n"
    "v_mfma_f32_16x16x32_bf16 v[68:71], v[180:183], a[236:239], v[68:71]\n"
    "buffer_load_dwordx4 v200, s[36:39], s65 offen sc1 lds\n"
    "v_mfma_f32_16x16x32_bf16 v[68:71], v[176:179], a[232:235], v[68:71]\n"
    "v_permlane32_swap_b32 v117, v119\n"
    "v_permlane32_swap_b32 v121, v123\n"
    "v_mfma_f32_16x16x32_bf16 v[72:75], v[176:179], v[0:3], v[72:75]\n"
    "ds_read_b128 v[172:175], v200 offset:33792\n"
    "v_permlane32_swap_b32 v125, v127\n"
    "v_mfma_f32_16x16x32_bf16 v[72:75], v[180:183], v[4:7], v[72:75]\n"
    "v_add_f32 v113, v113, v115\n"
    "v_add_f32 v117, v117, v119\n"
    "v_mfma_f32_16x16x32_bf16 v[76:79], v[180:183], v[28:31], v[76:79]\n"
    "buffer_load_dwordx4 v200, s[36:39], s65 offen offset:1024 sc1 lds\n"
    "v_mfma_f32_16x16x32_bf16 v[76:79], v[176:179], v[24:27], v[76:79]\n"
    "s_waitcnt lgkmcnt(2)\n"
    "v_add_f32 v121, v121, v123\n"
    "v_add_f32 v125, v125, v127\n"
    "v_mfma_f32_16x16x32_bf16 v[92:95], v[160:163], v[24:27], v[108:111]\n"
    "ds_read_b128 v[176:179], v200 offset:30720\n"
    "v_mov_b32 v115, v113\n"
    "v_mfma_f32_16x16x32_bf16 v[92:95], v[164:167], v[28:31], v[92:95]\n"
    "s_add_u32 m0, s71, 0x12000\n"
    "v_exp_f32 v64, v64\n"
    "v_mfma_f32_16x16x32_bf16 v[88:91], v[164:167], v[4:7], v[104:107]\n"
    "buffer_load_dwordx4 v200, s[40:43], s61 offen sc1 lds\n"
    "v_mfma_f32_16x16x32_bf16 v[88:91], v[160:163], v[0:3], v[88:91]\n"
    "v_exp_f32 v65, v65\n"
    "v_mfma_f32_16x16x32_bf16 v[84:87], v[160:163], a[232:235], v[100:103]\n"
    "ds_read_b128 v[180:183], v200 offset:31744\n"
    "v_cvt_pk_bf16_f32 v144, v64, v65\n"
    "v_mfma_f32_16x16x32_bf16 v[84:87], v[164:167], a[236:239], v[84:87]\n"
    "v_exp_f32 v66, v66\n"
    "v_mfma_f32_16x16x32_bf16 v[80:83], v[164:167], a[212:215], v[96:99]\n"
    "buffer_load_dwordx4 v200, s[40:43], s61 offen offset:1024 sc1 lds\n"
    "v_mfma_f32_16x16x32_bf16 v[80:83], v[160:163], a[208:211], v[80:83]\n"
    "s_waitcnt lgkmcnt(2)\n"
    "v_exp_f32 v67, v67\n"
    "v_mfma_f32_16x16x32_bf16 v[80:83], v[168:171], a[200:203], v[80:83]\n"
    "ds_read_b128 v[160:163], v200 offset:36864\n"
    "v_cvt_pk_bf16_f32 v145, v66, v67\n"
    "v_mfma_f32_16x16x32_bf16 v[80:83], v[172:175], a[204:207], v[80:83]\n"
    "v_exp_f32 v68, v68\n"
    "v_mfma_f32_16x16x32_bf16 v[84:87], v[172:175], a[228:231], v[84:87]\n"
    "buffer_load_dwordx4 v200, s[40:43], s61 offen offset:2048 sc1 lds\n"
    "v_mfma_f32_16x16x32_bf16 v[84:87], v[168:171], a[224:227], v[84:87]\n"
    "v_exp_f32 v69, v69\n"
    "v_mfma_f32_16x16x32_bf16 v[88:91], v[168:171], a[248:251], v[88:91]\n"
    "ds_read_b128 v[164:167], v200 offset:37888\n"
    "v_cvt_pk_bf16_f32 v148, v68, v69\n"
    "v_mfma_f32_16x16x32_bf16 v[88:91], v[172:175], a[252:255], v[88:91]\n"
    "v_exp_f32 v70, v70\n"
    "v_mfma_f32_16x16x32_bf16 v[92:95], v[172:175], v[20:23], v[92:95]\n"
    "buffer_load_dwordx4 v200, s[40:43], s61 offen offset:3072 sc1 lds\n"
    "v_mfma_f32_16x16x32_bf16 v[92:95], v[168:171], v[16:19], v[92:95]\n"
    "s_waitcnt lgkmcnt(2)\n"
    "v_exp_f32 v71, v71\n"
    "v_mfma_f32_16x16x32_bf16 v[92:95], v[176:179], v[8:11], v[92:95]\n"
    "s_add_u32 s65, s61, 0x1000\n"
    "ds_read_b128 v[168:171], v200 offset:38912\n"
    "v_cvt_pk_bf16_f32 v149, v70, v71\n"
    "v_mfma_f32_16x16x32_bf16 v[92:95], v[180:183], v[12:15], v[92:95]\n"
    "s_add_u32 m0, s71, 0x13000\n"
    "v_exp_f32 v72, v72\n"
    "v_mfma_f32_16x16x32_bf16 v[88:91], v[180:183], a[244:247], v[88:91]\n"
    "buffer_load_dwordx4 v200, s[40:43], s65 offen sc1 lds\n"
    "v_mfma_f32_16x16x32_bf16 v[88:91], v[176:179], a[240:243], v[88:91]\n"
    "v_exp_f32 v73, v73\n"
    "v_mfma_f32_16x16x32_bf16 v[84:87], v[176:179], a[216:219], v[84:87]\n"
    "ds_read_b128 v[172:175], v200 offset:39936\n"
    "v_cvt_pk_bf16_f32 v152, v72, v73\n"
    "v_mfma_f32_16x16x32_bf16 v[84:87], v[180:183], a[220:223], v[84:87]\n"
    "v_exp_f32 v74, v74\n"
    "v_mfma_f32_16x16x32_bf16 v[80:83], v[180:183], a[196:199], v[80:83]\n"
    "buffer_load_dwordx4 v200, s[40:43], s65 offen offset:1024 sc1 lds\n"
    "v_mfma_f32_16x16x32_bf16 v[80:83], v[176:179], a[192:195], v[80:83]\n"
    "s_waitcnt lgkmcnt(2)\n"
    "v_exp_f32 v75, v75\n"
    "v_mfma_f32_16x16x32_bf16 v[32:35], v[160:163], a[192:195], v[96:99]\n"
    "ds_read_b128 v[176:179], v200 offset:40960\n"
    "v_cvt_pk_bf16_f32 v153, v74, v75\n"
    "v_mfma_f32_16x16x32_bf16 v[32:35], v[164:167], a[196:199], v[32:35]\n"
    "v_exp_f32 v76, v76\n"
    "v_mfma_f32_16x16x32_bf16 v[36:39], v[164:167], a[220:223], v[100:103]\n"
    "v_exp_f32 v77, v77\n"
    "v_mfma_f32_16x16x32_bf16 v[36:39], v[160:163], a[216:219], v[36:39]\n"
    "v_exp_f32 v78, v78\n"
    "v_mfma_f32_16x16x32_bf16 v[40:43], v[160:163], a[240:243], v[104:107]\n"
    "ds_read_b128 v[180:183], v200 offset:41984\n"
    "v_cvt_pk_bf16_f32 v156, v76, v77\n"
    "v_mfma_f32_16x16x32_bf16 v[40:43], v[164:167], a[244:247], v[40:43]\n"
    "v_exp_f32 v79, v79\n"
    "v_mfma_f32_16x16x32_bf16 v[44:47], v[164:167], v[12:15], v[108:111]\n"
    "v_exp_f32 v80, v80\n"
    "v_mfma_f32_16x16x32_bf16 v[44:47], v[160:163], v[8:11], v[44:47]\n"
    "s_waitcnt lgkmcnt(2)\n"
    "v_exp_f32 v81, v81\n"
    "v_mfma_f32_16x16x32_bf16 v[44:47], v[168:171], v[16:19], v[44:47]\n"
    "ds_read_b128 v[160:163], v200 offset:47104\n"
    "v_cvt_pk_bf16_f32 v157, v78, v79\n"
    "v_mfma_f32_16x16x32_bf16 v[44:47], v[172:175], v[20:23], v[44:47]\n"
    "v_exp_f32 v82, v82\n"
    "v_mfma_f32_16x16x32_bf16 v[40:43], v[172:175], a[252:255], v[40:43]\n"
    "v_exp_f32 v83, v83\n"
    "v_mfma_f32_16x16x32_bf16 v[40:43], v[168:171], a[248:251], v[40:43]\n"
    "v_exp_f32 v84, v84\n"
    "v_mfma_f32_16x16x32_bf16 v[36:39], v[168:171], a[224:227], v[36:39]\n"
    "ds_read_b128 v[164:167], v200 offset:48128\n"
    "v_cvt_pk_bf16_f32 v146, v80, v81\n"
    "v_mfma_f32_16x16x32_bf16 v[36:39], v[172:175], a[228:231], v[36:39]\n"
    "v_exp_f32 v85, v85\n"
    "v_mfma_f32_16x16x32_bf16 v[32:35], v[172:175], a[204:207], v[32:35]\n"
    "v_exp_f32 v86, v86\n"
    "v_mfma_f32_16x16x32_bf16 v[32:35], v[168:171], a[200:203], v[32:35]\n"
    "s_waitcnt lgkmcnt(2)\n"
    "v_exp_f32 v87, v87\n"
    "v_mfma_f32_16x16x32_bf16 v[32:35], v[176:179], a[208:211], v[32:35]\n"
    "ds_read_b128 v[168:171], v200 offset:45056\n"
    "v_cvt_pk_bf16_f32 v147, v82, v83\n"
    "v_mfma_f32_16x16x32_bf16 v[32:35], v[180:183], a[212:215], v[32:35]\n"
    "v_exp_f32 v88, v88\n"
    "v_mfma_f32_16x16x32_bf16 v[36:39], v[180:183], a[236:239], v[36:39]\n"
    "v_exp_f32 v89, v89\n"
    "v_mfma_f32_16x16x32_bf16 v[36:39], v[176:179], a[232:235], v[36:39]\n"
    "v_exp_f32 v90, v90\n"
    "v_mfma_f32_16x16x32_bf16 v[40:43], v[176:179], v[0:3], v[40:43]\n"
    "ds_read_b128 v[172:175], v200 offset:46080\n"
    "v_cvt_pk_bf16_f32 v150, v84, v85\n"
    "v_mfma_f32_16x16x32_bf16 v[40:43], v[180:183], v[4:7], v[40:43]\n"
    "v_exp_f32 v91, v91\n"
    "v_mfma_f32_16x16x32_bf16 v[44:47], v[180:183], v[28:31], v[44:47]\n"
    "v_exp_f32 v92, v92\n"
    "v_mfma_f32_16x16x32_bf16 v[44:47], v[176:179], v[24:27], v[44:47]\n"
    "s_waitcnt lgkmcnt(2)\n"
    "v_exp_f32 v93, v93\n"
    "v_mfma_f32_16x16x32_bf16 v[60:63], v[160:163], v[24:27], v[108:111]\n"
    "ds_read_b128 v[176:179], v200 offset:43008\n"
    "v_cvt_pk_bf16_f32 v151, v86, v87\n"
    "v_mfma_f32_16x16x32_bf16 v[60:63], v[164:167], v[28:31], v[60:63]\n"
    "v_exp_f32 v94, v94\n"
    "v_mfma_f32_16x16x32_bf16 v[56:59], v[164:167], v[4:7], v[104:107]\n"
    "v_exp_f32 v95, v95\n"
    "v_mfma_f32_16x16x32_bf16 v[56:59], v[160:163], v[0:3], v[56:59]\n"
    "v_exp_f32 v32, v32\n"
    "v_mfma_f32_16x16x32_bf16 v[52:55], v[160:163], a[232:235], v[100:103]\n"
    "ds_read_b128 v[180:183], v200 offset:44032\n"
    "v_cvt_pk_bf16_f32 v154, v88, v89\n"
    "v_mfma_f32_16x16x32_bf16 v[52:55], v[164:167], a[236:239], v[52:55]\n"
    "v_exp_f32 v33, v33\n"
    "v_mfma_f32_16x16x32_bf16 v[48:51], v[164:167], a[212:215], v[96:99]\n"
    "s_waitcnt vmcnt(6)\n"
    "v_exp_f32 v34, v34\n"
    "v_mfma_f32_16x16x32_bf16 v[48:51], v[160:163], a[208:211], v[48:51]\n"
    "s_waitcnt lgkmcnt(2)\n"
    "v_exp_f32 v35, v35\n"
    "v_mfma_f32_16x16x32_bf16 v[48:51], v[168:171], a[200:203], v[48:51]\n"
    "s_barrier\n"
    "v_exp_f32 v36, v36\n"
    "v_mfma_f32_16x16x32_bf16 v[48:51], v[172:175], a[204:207], v[48:51]\n"
    "ds_read_b128 v[160:163], v200\n"
    "v_cvt_pk_bf16_f32 v155, v90, v91\n"
    "v_mfma_f32_16x16x32_bf16 v[52:55], v[172:175], a[228:231], v[52:55]\n"
    "v_exp_f32 v37, v37\n"
    "v_mfma_f32_16x16x32_bf16 v[52:55], v[168:171], a[224:227], v[52:55]\n"
    "v_exp_f32 v38, v38\n"
    "v_mfma_f32_16x16x32_bf16 v[56:59], v[168:171], a[248:251], v[56:59]\n"
    "ds_read_b128 v[164:167], v200 offset:1024\n"
    "v_cvt_pk_bf16_f32 v158, v92, v93\n"
    "v_mfma_f32_16x16x32_bf16 v[56:59], v[172:175], a[252:255], v[56:59]\n"
    "buffer_load_dwordx2 v[218:219], v213, s[44:47], 0 offen sc1\n"
    "v_mfma_f32_16x16x32_bf16 v[60:63], v[172:175], v[20:23], v[60:63]\n"
    "v_exp_f32 v39, v39\n"
    "v_mfma_f32_16x16x32_bf16 v[60:63], v[168:171], v[16:19], v[60:63]\n"
    "s_waitcnt lgkmcnt(2)\n"
    "v_exp_f32 v40, v40\n"
    "v_mfma_f32_16x16x32_bf16 v[60:63], v[176:179], v[8:11], v[60:63]\n"
    "ds_read_b128 v[168:171], v200 offset:2048\n"
    "v_cvt_pk_bf16_f32 v159, v94, v95\n"
    "v_mfma_f32_16x16x32_bf16 v[60:63], v[180:183], v[12:15], v[60:63]\n"
    "v_exp_f32 v41, v41\n"
    "v_mfma_f32_16x16x32_bf16 v[56:59], v[180:183], a[244:247], v[56:59]\n"
    "v_exp_f32 v42, v42\n"
    "v_mfma_f32_16x16x32_bf16 v[56:59], v[176:179], a[240:243], v[56:59]\n"
    "v_exp_f32 v43, v43\n"
    "v_mfma_f32_16x16x32_bf16 v[52:55], v[176:179], a[216:219], v[52:55]\n"
    "ds_read_b128 v[172:175], v200 offset:3072\n"
    "v_cvt_pk_bf16_f32 v32, v32, v33\n"
    "v_mfma_f32_16x16x32_bf16 v[52:55], v[180:183], a[220:223], v[52:55]\n"
    "v_exp_f32 v44, v44\n"
    "v_mfma_f32_16x16x32_bf16 v[48:51], v[180:183], a[196:199], v[48:51]\n"
    "v_exp_f32 v45, v45\n"
    "v_mfma_f32_16x16x32_bf16 v[48:51], v[176:179], a[192:195], v[48:51]\n"
    "s_waitcnt lgkmcnt(2)\n"
    "v_exp_f32 v46, v46\n"
    "v_mfma_f32_16x16x32_bf16 v[64:67], v[160:163], a[192:195], v[96:99]\n"
    "ds_read_b128 v[176:179], v200 offset:4096\n"
    "v_cvt_pk_bf16_f32 v33, v34, v35\n"
    "v_mfma_f32_16x16x32_bf16 v[64:67], v[164:167], a[196:199], v[64:67]\n"
    "v_exp_f32 v47, v47\n"
    "v_mfma_f32_16x16x32_bf16 v[68:71], v[164:167], a[220:223], v[100:103]\n"
    "v_cvt_pk_bf16_f32 v36, v36, v37\n"
    "v_cvt_pk_bf16_f32 v37, v38, v39\n"
    "v_mfma_f32_16x16x32_bf16 v[68:71], v[160:163], a[216:219], v[68:71]\n"
    "v_cvt_pk_bf16_f32 v40, v40, v41\n"
    "v_cvt_pk_bf16_f32 v41, v42, v43\n"
    "v_mfma_f32_16x16x32_bf16 v[72:75], v[160:163], a[240:243], v[104:107]\n"
    "ds_read_b128 v[180:183], v200 offset:5120\n"
    "v_cvt_pk_bf16_f32 v44, v44, v45\n"
    "v_mfma_f32_16x16x32_bf16 v[72:75], v[164:167], a[244:247], v[72:75]\n"
    "v_cvt_pk_bf16_f32 v45, v46, v47\n"
    "v_mov_b32 v119, v117\n"
    "v_mfma_f32_16x16x32_bf16 v[76:79], v[164:167], v[12:15], v[108:111]\n"
    "v_exp_f32 v48, v48\n"
    "v_mfma_f32_16x16x32_bf16 v[76:79], v[160:163], v[8:11], v[76:79]\n"
    "s_waitcnt lgkmcnt(2)\n"
    "v_exp_f32 v49, v49\n"
    "v_mfma_f32_16x16x32_bf16 v[76:79], v[168:171], v[16:19], v[76:79]\n"
    "ds_read_b128 v[160:163], v200 offset:10240\n"
    "v_cvt_pk_bf16_f32 v34, v48, v49\n"
    "v_mfma_f32_16x16x32_bf16 v[76:79], v[172:175], v[20:23], v[76:79]\n"
    "v_exp_f32 v50, v50\n"
    "v_mfma_f32_16x16x32_bf16 v[72:75], v[172:175], a[252:255], v[72:75]\n"
    "v_exp_f32 v51, v51\n"
    "v_mfma_f32_16x16x32_bf16 v[72:75], v[168:171], a[248:251], v[72:75]\n"
    "v_exp_f32 v52, v52\n"
    "v_mfma_f32_16x16x32_bf16 v[68:71], v[168:171], a[224:227], v[68:71]\n"
    "ds_read_b128 v[164:167], v200 offset:11264\n"
    "v_cvt_pk_bf16_f32 v35, v50, v51\n"
    "v_mfma_f32_16x16x32_bf16 v[68:71], v[172:175], a[228:231], v[68:71]\n"
    "v_exp_f32 v53, v53\n"
    "v_mfma_f32_16x16x32_bf16 v[64:67], v[172:175], a[204:207], v[64:67]\n"
    "v_exp_f32 v54, v54\n"
    "v_mfma_f32_16x16x32_bf16 v[64:67], v[168:171], a[200:203], v[64:67]\n"
    "s_waitcnt lgkmcnt(2)\n"
    "v_exp_f32 v55, v55\n"
    "v_mfma_f32_16x16x32_bf16 v[64:67], v[176:179], a[208:211], v[64:67]\n"
    "s_add_u32 s66, s66, s74\n"
    "ds_read_b128 v[168:171], v200 offset:8192\n"
    "v_cvt_pk_bf16_f32 v38, v52, v53\n"
    "v_mfma_f32_16x16x32_bf16 v[64:67], v[180:183], a[212:215], v[64:67]\n"
    "s_add_u32 s60, s72, 0xbd000\n"
    "v_exp_f32 v56, v56\n"
    "v_mfma_f32_16x16x32_bf16 v[68:71], v[180:183], a[236:239], v[68:71]\n"
    "s_add_u32 s61, s71, 0xba000\n"
    "v_exp_f32 v57, v57\n"
    "v_mfma_f32_16x16x32_bf16 v[68:71], v[176:179], a[232:235], v[68:71]\n"
    "v_exp_f32 v58, v58\n"
    "v_mfma_f32_16x16x32_bf16 v[72:75], v[176:179], v[0:3], v[72:75]\n"
    "ds_read_b128 v[172:175], v200 offset:9216\n"
    "v_cvt_pk_bf16_f32 v39, v54, v55\n"
    "v_mfma_f32_16x16x32_bf16 v[72:75], v[180:183], v[4:7], v[72:75]\n"
    "v_exp_f32 v59, v59\n"
    "v_mfma_f32_16x16x32_bf16 v[76:79], v[180:183], v[28:31], v[76:79]\n"
    "s_waitcnt vmcnt(0)\n"
    "v_cmp_ne_u32 s[80:81], s67, v219\n"
    "v_cvt_pk_bf16_f32 v42, v56, v57\n"
    "v_mfma_f32_16x16x32_bf16 v[76:79], v[176:179], v[24:27], v[76:79]\n"
    "s_waitcnt lgkmcnt(2)\n"
    "v_cmp_gt_u32 vcc, s66, v218\n"
    "v_cvt_pk_bf16_f32 v43, v58, v59\n"
    "v_mfma_f32_16x16x32_bf16 v[92:95], v[160:163], v[24:27], v[108:111]\n"
    "ds_read_b128 v[176:179], v200 offset:6144\n"
    "v_mov_b32 v123, v121\n"
    "v_mfma_f32_16x16x32_bf16 v[92:95], v[164:167], v[28:31], v[92:95]\n"
    "s_or_b64 vcc, vcc, s[80:81]\n"
    "v_exp_f32 v64, v64\n"
    "v_mfma_f32_16x16x32_bf16 v[88:91], v[164:167], v[4:7], v[104:107]\n"
    "s_cbranch_vccnz LSPRE5\n"
    "LSPB5:\n"
    "v_exp_f32 v65, v65\n"
    "v_mfma_f32_16x16x32_bf16 v[88:91], v[160:163], v[0:3], v[88:91]\n"
    "v_exp_f32 v66, v66\n"
    "v_mfma_f32_16x16x32_bf16 v[84:87], v[160:163], a[232:235], v[100:103]\n"
    "s_barrier\n"
    "ds_read_b128 v[180:183], v200 offset:7168\n"
    "v_cvt_pk_bf16_f32 v64, v64, v65\n"
    "v_mfma_f32_16x16x32_bf16 v[84:87], v[164:167], a[236:239], v[84:87]\n"
    "s_add_u32 m0, s72, 0x6000\n"
    "v_exp_f32 v67, v67\n"
    "v_mfma_f32_16x16x32_bf16 v[80:83], v[164:167], a[212:215], v[96:99]\n"
    "buffer_load_dwordx4 v200, s[36:39], s60 offen sc1 lds\n"
    "v_mfma_f32_16x16x32_bf16 v[80:83], v[160:163], a[208:211], v[80:83]\n"
    "s_waitcnt lgkmcnt(2)\n"
    "v_exp_f32 v68, v68\n"
    "v_mfma_f32_16x16x32_bf16 v[80:83], v[168:171], a[200:203], v[80:83]\n"
    "ds_read_b128 v[160:163], v200 offset:12288\n"
    "v_cvt_pk_bf16_f32 v65, v66, v67\n"
    "v_mfma_f32_16x16x32_bf16 v[80:83], v[172:175], a[204:207], v[80:83]\n"
    "buffer_load_dwordx4 v200, s[36:39], s60 offen offset:1024 sc1 lds\n"
    "v_mfma_f32_16x16x32_bf16 v[84:87], v[172:175], a[228:231], v[84:87]\n"
    "v_exp_f32 v69, v69\n"
    "v_mfma_f32_16x16x32_bf16 v[84:87], v[168:171], a[224:227], v[84:87]\n"
    "buffer_load_dwordx4 v200, s[36:39], s60 offen offset:2048 sc1 lds\n"
    "v_mfma_f32_16x16x32_bf16 v[88:91], v[168:171], a[248:251], v[88:91]\n"
    "ds_read_b128 v[164:167], v200 offset:13312\n"
    "v_cvt_pk_bf16_f32 v68, v68, v69\n"
    "v_mfma_f32_16x16x32_bf16 v[88:91], v[172:175], a[252:255], v[88:91]\n"
    "v_exp_f32 v60, v60\n"
    "v_mfma_f32_16x16x32_bf16 v[92:95], v[172:175], v[20:23], v[92:95]\n"
    "s_add_u32 m0, s71, 0x18000\n"
    "v_exp_f32 v70, v70\n"
    "v_mfma_f32_16x16x32_bf16 v[92:95], v[168:171], v[16:19], v[92:95]\n"
    "s_waitcnt lgkmcnt(2)\n"
    "buffer_load_dwordx4 v200, s[40:43], s61 offen sc1 lds\n"
    "v_mfma_f32_16x16x32_bf16 v[92:95], v[176:179], v[8:11], v[92:95]\n"
    "ds_read_b128 v[168:171], v200 offset:14336\n"
    "v_mov_b32 v127, v125\n"
    "v_mfma_f32_16x16x32_bf16 v[92:95], v[180:183], v[12:15], v[92:95]\n"
    "v_exp_f32 v61, v61\n"
    "v_mfma_f32_16x16x32_bf16 v[88:91], v[180:183], a[244:247], v[88:91]\n"
    "v_exp_f32 v71, v71\n"
    "v_mfma_f32_16x16x32_bf16 v[88:91], v[176:179], a[240:243], v[88:91]\n"
    "v_exp_f32 v62, v62\n"
    "v_mfma_f32_16x16x32_bf16 v[84:87], v[176:179], a[216:219], v[84:87]\n"
    "ds_read_b128 v[172:175], v200 offset:15360\n"
    "v_cvt_pk_bf16_f32 v46, v60, v61\n"
    "v_mfma_f32_16x16x32_bf16 v[84:87], v[180:183], a[220:223], v[84:87]\n"
    "v_exp_f32 v72, v72\n"
    "v_mfma_f32_16x16x32_bf16 v[80:83], v[180:183], a[196:199], v[80:83]\n"
    "v_exp_f32 v63, v63\n"
    "v_mfma_f32_16x16x32_bf16 v[80:83], v[176:179], a[192:195], v[80:83]\n"
    "s_waitcnt lgkmcnt(2)\n"
    "buffer_load_dwordx4 v200, s[40:43], s61 offen offset:1024 sc1 lds\n"
    "v_mfma_f32_16x16x32_bf16 v[232:235], v[160:163], a[192:195], v[96:99]\n"
    "ds_read_b128 v[176:179], v200 offset:16384\n"
    "v_cvt_pk_bf16_f32 v47, v62, v63\n"
    "v_mfma_f32_16x16x32_bf16 v[232:235], v[164:167], a[196:199], v[232:235]\n"
    "v_exp_f32 v73, v73\n"
    "v_mfma_f32_16x16x32_bf16 v[236:239], v[164:167], a[220:223], v[100:103]\n"
    "v_exp_f32 v74, v74\n"
    "v_mfma_f32_16x16x32_bf16 v[236:239], v[160:163], a[216:219], v[236:239]\n"
    "v_exp_f32 v75, v75\n"
    "v_mfma_f32_16x16x32_bf16 v[240:243], v[160:163], a[240:243], v[104:107]\n"
    "ds_read_b128 v[180:183], v200 offset:17408\n"
    "v_cvt_pk_bf16_f32 v69, v70, v71\n"
    "v_mfma_f32_16x16x32_bf16 v[240:243], v[164:167], a[244:247], v[240:243]\n"
    "v_exp_f32 v76, v76\n"
    "v_mfma_f32_16x16x32_bf16 v[244:247], v[164:167], v[12:15], v[108:111]\n"
    "v_exp_f32 v77, v77\n"
    "v_mfma_f32_16x16x32_bf16 v[244:247], v[160:163], v[8:11], v[244:247]\n"
    "s_waitcnt lgkmcnt(2)\n"
    "buffer_load_dwordx4 v200, s[40:43], s61 offen offset:2048 sc1 lds\n"
    "v_mfma_f32_16x16x32_bf16 v[244:247], v[168:171], v[16:19], v[244:247]\n"
    "ds_read_b128 v[160:163], v200 offset:22528\n"
    "v_cvt_pk_bf16_f32 v72, v72, v73\n"
    "v_mfma_f32_16x16x32_bf16 v[244:247], v[172:175], v[20:23], v[244:247]\n"
    "v_exp_f32 v78, v78\n"
    "v_mfma_f32_16x16x32_bf16 v[240:243], v[172:175], a[252:255], v[240:243]\n"
    "v_exp_f32 v79, v79\n"
    "v_mfma_f32_16x16x32_bf16 v[240:243], v[168:171], a[248:251], v[240:243]\n"
    "v_exp_f32 v80, v80\n"
    "v_mfma_f32_16x16x32_bf16 v[236:239], v[168:171], a[224:227], v[236:239]\n"
    "ds_read_b128 v[164:167], v200 offset:23552\n"
    "v_cvt_pk_bf16_f32 v73, v74, v75\n"
    "v_mfma_f32_16x16x32_bf16 v[236:239], v[172:175], a[228:231], v[236:239]\n"
    "v_exp_f32 v81, v81\n"
    "v_mfma_f32_16x16x32_bf16 v[232:235], v[172:175], a[204:207], v[232:235]\n"
    "v_exp_f32 v82, v82\n"
    "v_mfma_f32_16x16x32_bf16 v[232:235], v[168:171], a[200:203], v[232:235]\n"
    "s_waitcnt lgkmcnt(2)\n"
    "buffer_load_dwordx4 v200, s[40:43], s61 offen offset:3072 sc1 lds\n"
    "v_mfma_f32_16x16x32_bf16 v[232:235], v[176:179], a[208:211], v[232:235]\n"
    "ds_read_b128 v[168:171], v200 offset:20480\n"
    "v_cvt_pk_bf16_f32 v76, v76, v77\n"
    "v_mfma_f32_16x16x32_bf16 v[232:235], v[180:183], a[212:215], v[232:235]\n"
    "v_exp_f32 v83, v83\n"
    "v_mfma_f32_16x16x32_bf16 v[236:239], v[180:183], a[236:239], v[236:239]\n"
    "v_exp_f32 v84, v84\n"
    "v_mfma_f32_16x16x32_bf16 v[236:239], v[176:179], a[232:235], v[236:239]\n"
    "v_exp_f32 v85, v85\n"
    "v_mfma_f32_16x16x32_bf16 v[240:243], v[176:179], v[0:3], v[240:243]\n"
    "ds_read_b128 v[172:175], v200 offset:21504\n"
    "v_cvt_pk_bf16_f32 v77, v78, v79\n"
    "v_mfma_f32_16x16x32_bf16 v[240:243], v[180:183], v[4:7], v[240:243]\n"
    "s_add_u32 s65, s61, 0x1000\n"
    "v_exp_f32 v86, v86\n"
    "v_mfma_f32_16x16x32_bf16 v[244:247], v[180:183], v[28:31], v[244:247]\n"
    "s_add_u32 m0, s71, 0x19000\n"
    "v_exp_f32 v87, v87\n"
    "v_mfma_f32_16x16x32_bf16 v[244:247], v[176:179], v[24:27], v[244:247]\n"
    "s_waitcnt lgkmcnt(2)\n"
    "buffer_load_dwordx4 v200, s[40:43], s65 offen sc1 lds\n"
    "v_mfma_f32_16x16x32_bf16 v[60:63], v[160:163], v[24:27], v[108:111]\n"
    "ds_read_b128 v[176:179], v200 offset:18432\n"
    "v_cvt_pk_bf16_f32 v66, v80, v81\n"
    "v_mfma_f32_16x16x32_bf16 v[60:63], v[164:167], v[28:31], v[60:63]\n"
    "v_exp_f32 v88, v88\n"
    "v_mfma_f32_16x16x32_bf16 v[56:59], v[164:167], v[4:7], v[104:107]\n"
    "v_exp_f32 v89, v89\n"
    "v_mfma_f32_16x16x32_bf16 v[56:59], v[160:163], v[0:3], v[56:59]\n"
    "v_exp_f32 v90, v90\n"
    "v_mfma_f32_16x16x32_bf16 v[52:55], v[160:163], a[232:235], v[100:103]\n"
    "ds_read_b128 v[180:183], v200 offset:19456\n"
    "v_cvt_pk_bf16_f32 v67, v82, v83\n"
    "v_mfma_f32_16x16x32_bf16 v[52:55], v[164:167], a[236:239], v[52:55]\n"
    "v_exp_f32 v91, v91\n"
    "v_mfma_f32_16x16x32_bf16 v[48:51], v[164:167], a[212:215], v[96:99]\n"
    "s_waitcnt vmcnt(5)\n"
    "v_exp_f32 v92, v92\n"
    "v_mfma_f32_16x16x32_bf16 v[48:51], v[160:163], a[208:211], v[48:51]\n"
    "s_waitcnt lgkmcnt(2)\n"
    "buffer_load_dwordx4 v200, s[40:43], s65 offen offset:1024 sc1 lds\n"
    "v_mfma_f32_16x16x32_bf16 v[48:51], v[168:171], a[200:203], v[48:51]\n"
    "s_barrier\n"
    "v_exp_f32 v93, v93\n"
    "v_mfma_f32_16x16x32_bf16 v[48:51], v[172:175], a[204:207], v[48:51]\n"
    "ds_read_b128 v[160:163], v200 offset:24576\n"
    "v_cvt_pk_bf16_f32 v70, v84, v85\n"
    "v_mfma_f32_16x16x32_bf16 v[52:55], v[172:175], a[228:231], v[52:55]\n"
    "v_exp_f32 v94, v94\n"
    "v_mfma_f32_16x16x32_bf16 v[52:55], v[168:171], a[224:227], v[52:55]\n"
    "v_exp_f32 v95, v95\n"
    "v_mfma_f32_16x16x32_bf16 v[56:59], v[168:171], a[248:251], v[56:59]\n"
    "ds_read_b128 v[164:167], v200 offset:25600\n"
    "v_cvt_pk_bf16_f32 v71, v86, v87\n"
    "v_mfma_f32_16x16x32_bf16 v[56:59], v[172:175], a[252:255], v[56:59]\n"
    "v_exp_f32 v232, v232\n"
    "v_mfma_f32_16x16x32_bf16 v[60:63], v[172:175], v[20:23], v[60:63]\n"
    "v_exp_f32 v233, v233\n"
    "v_mfma_f32_16x16x32_bf16 v[60:63], v[168:171], v[16:19], v[60:63]\n"
    "s_waitcnt lgkmcnt(2)\n"
    "v_exp_f32 v234, v234\n"
    "v_mfma_f32_16x16x32_bf16 v[60:63], v[176:179], v[8:11], v[60:63]\n"
    "s_lshl_b32 s76, s56, 3\n"
    "ds_read_b128 v[168:171], v200 offset:26624\n"
    "v_cvt_pk_bf16_f32 v74, v88, v89\n"
    "v_mfma_f32_16x16x32_bf16 v[60:63], v[180:183], v[12:15], v[60:63]\n"
    "s_add_u32 s76, s76, s57\n"
    "v_exp_f32 v235, v235\n"
    "v_mfma_f32_16x16x32_bf16 v[56:59], v[180:183], a[244:247], v[56:59]\n"
    "s_mul_i32 s76, s76, 0x18000\n"
    "v_exp_f32 v236, v236\n"
    "v_mfma_f32_16x16x32_bf16 v[56:59], v[176:179], a[240:243], v[56:59]\n"
    "s_add_u32 s76, s76, s88\n"
    "v_exp_f32 v237, v237\n"
    "v_mfma_f32_16x16x32_bf16 v[52:55], v[176:179], a[216:219], v[52:55]\n"
    "s_add_u32 s28, s24, s76\n"
    "ds_read_b128 v[172:175], v200 offset:27648\n"
    "v_cvt_pk_bf16_f32 v75, v90, v91\n"
    "v_mfma_f32_16x16x32_bf16 v[52:55], v[180:183], a[220:223], v[52:55]\n"
    "s_addc_u32 s29, s25, 0\n"
    "v_exp_f32 v238, v238\n"
    "v_mfma_f32_16x16x32_bf16 v[48:51], v[180:183], a[196:199], v[48:51]\n"
    "s_and_b32 s29, s29, 0xffff\n"
    "v_exp_f32 v239, v239\n"
    "v_mfma_f32_16x16x32_bf16 v[48:51], v[176:179], a[192:195], v[48:51]\n"
    "s_waitcnt lgkmcnt(2)\n"
    "v_exp_f32 v240, v240\n"
    "v_mfma_f32_16x16x32_bf16 v[80:83], v[160:163], a[192:195], v[96:99]\n"
    "s_mov_b32 s30, 0x6000\n"
    "ds_read_b128 v[176:179], v200 offset:28672\n"
    "v_cvt_pk_bf16_f32 v78, v92, v93\n"
    "v_mfma_f32_16x16x32_bf16 v[80:83], v[164:167], a[196:199], v[80:83]\n"
    "s_mov_b32 s31, 0x20000\n"
    "v_cvt_pk_bf16_f32 v79, v94, v95\n"
    "v_permlane16_swap_b32 v113, v115\n"
    "v_mfma_f32_16x16x32_bf16 v[84:87], v[164:167], a[220:223], v[100:103]\n"
    "s_xor_b32 s94, s70, 0x80000000\n"
    "v_exp_f32 v241, v241\n"
    "v_mfma_f32_16x16x32_bf16 v[84:87], v[160:163], a[216:219], v[84:87]\n"
    "s_xor_b32 s95, s70, 0x80000000\n"
    "v_exp_f32 v242, v242\n"
    "v_mfma_f32_16x16x32_bf16 v[88:91], v[160:163], a[240:243], v[104:107]\n"
    "s_mov_b32 s62, 0x1800\n"
    "ds_read_b128 v[180:183], v200 offset:29696\n"
    "v_permlane16_swap_b32 v117, v119\n"
    "v_mfma_f32_16x16x32_bf16 v[88:91], v[164:167], a[244:247], v[88:91]\n"
    "s_mov_b32 s63, 0x3000\n"
    "v_exp_f32 v243, v243\n"
    "v_mfma_f32_16x16x32_bf16 v[92:95], v[164:167], v[12:15], v[108:111]\n"
    "s_mov_b32 s64, 0x4800\n"
    "v_exp_f32 v244, v244\n"
    "v_mfma_f32_16x16x32_bf16 v[92:95], v[160:163], v[8:11], v[92:95]\n"
    "s_waitcnt lgkmcnt(2)\n"
    "v_exp_f32 v245, v245\n"
    "v_mfma_f32_16x16x32_bf16 v[92:95], v[168:171], v[16:19], v[92:95]\n"
    "ds_read_b128 v[160:163], v200 offset:34816\n"
    "v_permlane16_swap_b32 v121, v123\n"
    "v_mfma_f32_16x16x32_bf16 v[92:95], v[172:175], v[20:23], v[92:95]\n"
    "v_exp_f32 v246, v246\n"
    "v_mfma_f32_16x16x32_bf16 v[88:91], v[172:175], a[252:255], v[88:91]\n"
    "v_exp_f32 v247, v247\n"
    "v_mfma_f32_16x16x32_bf16 v[88:91], v[168:171], a[248:251], v[88:91]\n"
    "v_exp_f32 v48, v48\n"
    "v_mfma_f32_16x16x32_bf16 v[84:87], v[168:171], a[224:227], v[84:87]\n"
    "ds_read_b128 v[164:167], v200 offset:35840\n"
    "v_permlane16_swap_b32 v125, v127\n"
    "v_mfma_f32_16x16x32_bf16 v[84:87], v[172:175], a[228:231], v[84:87]\n"
    "v_exp_f32 v49, v49\n"
    "v_mfma_f32_16x16x32_bf16 v[80:83], v[172:175], a[204:207], v[80:83]\n"
    "v_exp_f32 v50, v50\n"
    "v_mfma_f32_16x16x32_bf16 v[80:83], v[168:171], a[200:203], v[80:83]\n"
    "s_waitcnt lgkmcnt(2)\n"
    "v_exp_f32 v51, v51\n"
    "v_mfma_f32_16x16x32_bf16 v[80:83], v[176:179], a[208:211], v[80:83]\n"
    "ds_read_b128 v[168:171], v200 offset:32768\n"
    "v_add_f32 v113, v113, v115\n"
    "v_mfma_f32_16x16x32_bf16 v[80:83], v[180:183], a[212:215], v[80:83]\n"
    "v_exp_f32 v52, v52\n"
    "v_mfma_f32_16x16x32_bf16 v[84:87], v[180:183], a[236:239], v[84:87]\n"
    "v_exp_f32 v53, v53\n"
    "v_mfma_f32_16x16x32_bf16 v[84:87], v[176:179], a[232:235], v[84:87]\n"
    "v_exp_f32 v54, v54\n"
    "v_mfma_f32_16x16x32_bf16 v[88:91], v[176:179], v[0:3], v[88:91]\n"
    "ds_read_b128 v[172:175], v200 offset:33792\n"
    "v_add_f32 v117, v117, v119\n"
    "v_mfma_f32_16x16x32_bf16 v[88:91], v[180:183], v[4:7], v[88:91]\n"
    "v_add_f32 v121, v121, v123\n"
    "v_add_f32 v125, v125, v127\n"
    "v_mfma_f32_16x16x32_bf16 v[92:95], v[180:183], v[28:31], v[92:95]\n"
    "v_exp_f32 v55, v55\n"
    "v_mfma_f32_16x16x32_bf16 v[92:95], v[176:179], v[24:27], v[92:95]\n"
    "s_waitcnt lgkmcnt(2)\n"
    "v_add_f32 v112, v112, v113\n"
    "v_add_f32 v116, v116, v117\n"
    "v_mfma_f32_16x16x32_bf16 v[252:255], v[160:163], v[24:27], v[108:111]\n"
    "ds_read_b128 v[176:179], v200 offset:30720\n"
    "v_add_f32 v120, v120, v121\n"
    "v_mfma_f32_16x16x32_bf16 v[252:255], v[164:167], v[28:31], v[252:255]\n"
    "v_add_f32 v124, v124, v125\n"
    "v_and_b32 v198, 15, v231\n"
    "v_mfma_f32_16x16x32_bf16 v[248:251], v[164:167], v[4:7], v[104:107]\n"
    "v_mul_u32_u24 v205, 0x180, v198\n"
    "v_lshrrev_b32 v199, 4, v231\n"
    "v_mfma_f32_16x16x32_bf16 v[248:251], v[160:163], v[0:3], v[248:251]\n"
    "v_lshl_add_u32 v205, v199, 4, v205\n"
    "v_add_u32 v207, 0x18000, v200\n"
    "v_mfma_f32_16x16x32_bf16 v[188:191], v[160:163], a[232:235], v[100:103]\n"
    "ds_read_b128 v[180:183], v200 offset:31744\n"
    "v_cvt_pk_bf16_f32 v232, v232, v233\n"
    "v_mfma_f32_16x16x32_bf16 v[188:191], v[164:167], a[236:239], v[188:191]\n"
    "v_exp_f32 v56, v56\n"
    "v_mfma_f32_16x16x32_bf16 v[184:187], v[164:167], a[212:215], v[96:99]\n"
    "v_exp_f32 v57, v57\n"
    "v_mfma_f32_16x16x32_bf16 v[184:187], v[160:163], a[208:211], v[184:187]\n"
    "s_waitcnt lgkmcnt(2)\n"
    "v_exp_f32 v58, v58\n"
    "v_mfma_f32_16x16x32_bf16 v[184:187], v[168:171], a[200:203], v[184:187]\n"
    "ds_read_b128 v[160:163], v201\n"
    "v_cvt_pk_bf16_f32 v233, v234, v235\n"
    "v_mfma_f32_16x16x32_bf16 v[184:187], v[172:175], a[204:207], v[184:187]\n"
    "v_exp_f32 v59, v59\n"
    "v_mfma_f32_16x16x32_bf16 v[188:191], v[172:175], a[228:231], v[188:191]\n"
    "v_exp_f32 v60, v60\n"
    "v_mfma_f32_16x16x32_bf16 v[188:191], v[168:171], a[224:227], v[188:191]\n"
    "v_exp_f32 v61, v61\n"
    "v_mfma_f32_16x16x32_bf16 v[248:251], v[168:171], a[248:251], v[248:251]\n"
    "ds_read_b128 v[164:167], v201 offset:1024\n"
    "v_cvt_pk_bf16_f32 v236, v236, v237\n"
    "v_mfma_f32_16x16x32_bf16 v[248:251], v[172:175], a[252:255], v[248:251]\n"
    "v_exp_f32 v62, v62\n"
    "v_mfma_f32_16x16x32_bf16 v[252:255], v[172:175], v[20:23], v[252:255]\n"
    "v_exp_f32 v63, v63\n"
    "v_mfma_f32_16x16x32_bf16 v[252:255], v[168:171], v[16:19], v[252:255]\n"
    "s_waitcnt lgkmcnt(2)\n"
    "v_exp_f32 v80, v80\n"
    "v_mfma_f32_16x16x32_bf16 v[252:255], v[176:179], v[8:11], v[252:255]\n"
    "ds_read_b128 v[168:171], v201 offset:24576\n"
    "v_cvt_pk_bf16_f32 v237, v238, v239\n"
    "v_mfma_f32_16x16x32_bf16 v[252:255], v[180:183], v[12:15], v[252:255]\n"
    "v_exp_f32 v81, v81\n"
    "v_mfma_f32_16x16x32_bf16 v[248:251], v[180:183], a[244:247], v[248:251]\n"
    "v_exp_f32 v82, v82\n"
    "v_mfma_f32_16x16x32_bf16 v[248:251], v[176:179], a[240:243], v[248:251]\n"
    "v_exp_f32 v83, v83\n"
    "v_mfma_f32_16x16x32_bf16 v[188:191], v[176:179], a[216:219], v[188:191]\n"
    "ds_read_b128 v[172:175], v201 offset:25600\n"
    "v_cvt_pk_bf16_f32 v240, v240, v241\n"
    "v_mfma_f32_16x16x32_bf16 v[188:191], v[180:183], a[220:223], v[188:191]\n"
    "v_exp_f32 v84, v84\n"
    "v_mfma_f32_16x16x32_bf16 v[184:187], v[180:183], a[196:199], v[184:187]\n"
    "v_exp_f32 v85, v85\n"
    "v_mfma_f32_16x16x32_bf16 v[184:187], v[176:179], a[192:195], v[184:187]\n"
    "s_waitcnt lgkmcnt(2)\n"
    "v_exp_f32 v86, v86\n"
    "v_mfma_f32_16x16x32_bf16 a[0:3], v[160:163], v[128:131], a[0:3]\n"
    "ds_read_b128 v[176:179], v201 offset:2048\n"
    "v_cvt_pk_bf16_f32 v241, v242, v243\n"
    "v_mfma_f32_16x16x32_bf16 a[0:3], v[164:167], v[144:147], a[0:3]\n"
    "v_exp_f32 v87, v87\n"
    "v_mfma_f32_16x16x32_bf16 a[4:7], v[160:163], v[132:135], a[4:7]\n"
    "v_exp_f32 v88, v88\n"
    "v_mfma_f32_16x16x32_bf16 a[4:7], v[164:167], v[148:151], a[4:7]\n"
    "v_exp_f32 v89, v89\n"
    "v_mfma_f32_16x16x32_bf16 a[8:11], v[160:163], v[136:139], a[8:11]\n"
    "ds_read_b128 v[180:183], v201 offset:3072\n"
    "v_cvt_pk_bf16_f32 v244, v244, v245\n"
    "v_mfma_f32_16x16x32_bf16 a[8:11], v[164:167], v[152:155], a[8:11]\n"
    "v_cvt_pk_bf16_f32 v245, v246, v247\n"
    "v_cvt_pk_bf16_f32 v234, v48, v49\n"
    "v_mfma_f32_16x16x32_bf16 a[12:15], v[160:163], v[140:143], a[12:15]\n"
    "v_cvt_pk_bf16_f32 v235, v50, v51\n"
    "v_cvt_pk_bf16_f32 v238, v52, v53\n"
    "v_mfma_f32_16x16x32_bf16 a[12:15], v[164:167], v[156:159], a[12:15]\n"
    "v_cvt_pk_bf16_f32 v239, v54, v55\n"
    "v_cvt_pk_bf16_f32 v242, v56, v57\n"
    "v_mfma_f32_16x16x32_bf16 v[112:115], v[192:195], v[144:147], v[112:115]\n"
    "v_exp_f32 v90, v90\n"
    "v_mfma_f32_16x16x32_bf16 v[112:115], v[192:195], v[32:35], v[112:115]\n"
    "v_cvt_pk_bf16_f32 v243, v58, v59\n"
    "v_cvt_pk_bf16_f32 v246, v60, v61\n"
    "v_mfma_f32_16x16x32_bf16 v[112:115], v[192:195], v[64:67], v[112:115]\n"
    "v_cvt_pk_bf16_f32 v247, v62, v63\n"
    "v_cvt_pk_bf16_f32 v80, v80, v81\n"
    "v_mfma_f32_16x16x32_bf16 v[116:119], v[192:195], v[148:151], v[116:119]\n"
    "v_exp_f32 v91, v91\n"
    "v_mfma_f32_16x16x32_bf16 v[116:119], v[192:195], v[36:39], v[116:119]\n"
    "v_exp_f32 v92, v92\n"
    "v_mfma_f32_16x16x32_bf16 v[116:119], v[192:195], v[68:71], v[116:119]\n"
    "s_waitcnt lgkmcnt(2)\n"
    "v_exp_f32 v93, v93\n"
    "v_mfma_f32_16x16x32_bf16 a[0:3], v[168:171], v[32:35], a[0:3]\n"
    "ds_read_b128 v[160:163], v201 offset:26624\n"
    "v_cvt_pk_bf16_f32 v81, v82, v83\n"
    "v_mfma_f32_16x16x32_bf16 a[0:3], v[172:175], v[64:67], a[0:3]\n"
    "v_exp_f32 v94, v94\n"
    "v_mfma_f32_16x16x32_bf16 a[4:7], v[168:171], v[36:39], a[4:7]\n"
    "v_exp_f32 v95, v95\n"
    "v_mfma_f32_16x16x32_bf16 a[4:7], v[172:175], v[68:71], a[4:7]\n"
    "v_exp_f32 v184, v184\n"
    "v_mfma_f32_16x16x32_bf16 a[8:11], v[168:171], v[40:43], a[8:11]\n"
    "ds_read_b128 v[164:167], v201 offset:27648\n"
    "v_cvt_pk_bf16_f32 v84, v84, v85\n"
    "v_mfma_f32_16x16x32_bf16 a[8:11], v[172:175], v[72:75], a[8:11]\n"
    "v_exp_f32 v185, v185\n"
    "v_mfma_f32_16x16x32_bf16 a[12:15], v[168:171], v[44:47], a[12:15]\n"
    "v_exp_f32 v186, v186\n"
    "v_mfma_f32_16x16x32_bf16 a[12:15], v[172:175], v[76:79], a[12:15]\n"
    "v_exp_f32 v187, v187\n"
    "v_mfma_f32_16x16x32_bf16 v[120:123], v[192:195], v[152:155], v[120:123]\n"
    "v_exp_f32 v188, v188\n"
    "v_mfma_f32_16x16x32_bf16 v[120:123], v[192:195], v[40:43], v[120:123]\n"
    "v_exp_f32 v189, v189\n"
    "v_mfma_f32_16x16x32_bf16 v[120:123], v[192:195], v[72:75], v[120:123]\n"
    "v_exp_f32 v190, v190\n"
    "v_mfma_f32_16x16x32_bf16 v[124:127], v[192:195], v[156:159], v[124:127]\n"
    "v_exp_f32 v191, v191\n"
    "v_mfma_f32_16x16x32_bf16 v[124:127], v[192:195], v[44:47], v[124:127]\n"
    "s_waitcnt vmcnt(0)\n"
    "v_exp_f32 v248, v248\n"
    "v_mfma_f32_16x16x32_bf16 v[124:127], v[192:195], v[76:79], v[124:127]\n"
    "s_waitcnt lgkmcnt(2)\n"
    "v_exp_f32 v249, v249\n"
    "v_mfma_f32_16x16x32_bf16 a[16:19], v[176:179], v[128:131], a[16:19]\n"
    "s_barrier\n"
    "v_exp_f32 v250, v250\n"
    "v_mfma_f32_16x16x32_bf16 a[16:19], v[180:183], v[144:147], a[16:19]\n"
    "ds_read_b128 v[168:171], v207\n"
    "v_cvt_pk_bf16_f32 v85, v86, v87\n"
    "v_mfma_f32_16x16x32_bf16 a[20:23], v[176:179], v[132:135], a[20:23]\n"
    "v_exp_f32 v251, v251\n"
    "v_mfma_f32_16x16x32_bf16 a[20:23], v[180:183], v[148:151], a[20:23]\n"
    "v_exp_f32 v252, v252\n"
    "v_mfma_f32_16x16x32_bf16 a[24:27], v[176:179], v[136:139], a[24:27]\n"
    "ds_read_b128 v[172:175], v207 offset:1024\n"
    "v_cvt_pk_bf16_f32 v88, v88, v89\n"
    "v_mfma_f32_16x16x32_bf16 a[24:27], v[180:183], v[152:155], a[24:27]\n"
    "v_exp_f32 v253, v253\n"
    "v_mfma_f32_16x16x32_bf16 a[28:31], v[176:179], v[140:143], a[28:31]\n"
    "v_exp_f32 v254, v254\n"
    "v_mfma_f32_16x16x32_bf16 a[28:31], v[180:183], v[156:159], a[28:31]\n"
    "s_waitcnt lgkmcnt(2)\n"
    "v_cvt_pk_bf16_f32 v89, v90, v91\n"
    "v_cvt_pk_bf16_f32 v92, v92, v93\n"
    "v_mfma_f32_16x16x32_bf16 a[16:19], v[160:163], v[32:35], a[16:19]\n"
    "ds_read_b128 v[176:179], v207 offset:2048\n"
    "v_cvt_pk_bf16_f32 v93, v94, v95\n"
    "v_mfma_f32_16x16x32_bf16 a[16:19], v[164:167], v[64:67], a[16:19]\n"
    "ds_read_b128 v[180:183], v207 offset:3072\n"
    "v_cvt_pk_bf16_f32 v82, v184, v185\n"
    "v_mfma_f32_16x16x32_bf16 a[20:23], v[160:163], v[36:39], a[20:23]\n"
    "v_cvt_pk_bf16_f32 v83, v186, v187\n"
    "v_cvt_pk_bf16_f32 v86, v188, v189\n"
    "v_mfma_f32_16x16x32_bf16 a[20:23], v[164:167], v[68:71], a[20:23]\n"
    "v_exp_f32 v255, v255\n"
    "v_mfma_f32_16x16x32_bf16 a[24:27], v[160:163], v[40:43], a[24:27]\n"
    "v_cvt_pk_bf16_f32 v87, v190, v191\n"
    "v_cvt_pk_bf16_f32 v90, v248, v249\n"
    "v_mfma_f32_16x16x32_bf16 a[24:27], v[164:167], v[72:75], a[24:27]\n"
    "v_cvt_pk_bf16_f32 v91, v250, v251\n"
    "v_cvt_pk_bf16_f32 v94, v252, v253\n"
    "v_mfma_f32_16x16x32_bf16 a[28:31], v[160:163], v[44:47], a[28:31]\n"
    "v_cvt_pk_bf16_f32 v95, v254, v255\n"
    "v_mfma_f32_16x16x32_bf16 a[28:31], v[164:167], v[76:79], a[28:31]\n"
    "v_mfma_f32_16x16x32_bf16 v[112:115], v[192:195], v[232:235], v[112:115]\n"
    "ds_read_b128 v[160:163], v201 offset:4096\n"
    "v_mfma_f32_16x16x32_bf16 v[112:115], v[192:195], v[80:83], v[112:115]\n"
    "s_waitcnt lgkmcnt(3)\n"
    "v_mfma_f32_16x16x32_bf16 a[0:3], v[168:171], v[232:235], a[0:3]\n"
    "v_mfma_f32_16x16x32_bf16 a[0:3], v[172:175], v[80:83], a[0:3]\n"
    "s_waitcnt lgkmcnt(1)\n"
    "v_mfma_f32_16x16x32_bf16 a[16:19], v[176:179], v[232:235], a[16:19]\n"
    "ds_read_b128 v[164:167], v201 offset:5120\n"
    "v_mfma_f32_16x16x32_bf16 a[16:19], v[180:183], v[80:83], a[16:19]\n"
    "v_mfma_f32_16x16x32_bf16 v[116:119], v[192:195], v[236:239], v[116:119]\n"
    "v_mul_f32 v222, s68, v112\n"
    "v_rcp_f32 v222, v222\n"
    "v_mfma_f32_16x16x32_bf16 v[116:119], v[192:195], v[84:87], v[116:119]\n"
    "v_accvgpr_read_b32 v96, a0\n"
    "v_accvgpr_read_b32 v97, a1\n"
    "v_accvgpr_read_b32 v98, a2\n"
    "v_mfma_f32_16x16x32_bf16 a[4:7], v[168:171], v[236:239], a[4:7]\n"
    "v_accvgpr_read_b32 v99, a3\n"
    "v_fma_f32 v96, v96, v222, s94\n"
    "v_fma_f32 v97, v97, v222, s94\n"
    "v_mfma_f32_16x16x32_bf16 a[4:7], v[172:175], v[84:87], a[4:7]\n"
    "v_fma_f32 v98, v98, v222, s94\n"
    "v_fma_f32 v99, v99, v222, s94\n"
    "v_cvt_pk_bf16_f32 v48, v96, v97\n"
    "v_mfma_f32_16x16x32_bf16 a[20:23], v[176:179], v[236:239], a[20:23]\n"
    "v_cvt_pk_bf16_f32 v49, v98, v99\n"
    "v_accvgpr_read_b32 v100, a16\n"
    "v_accvgpr_read_b32 v101, a17\n"
    "v_mfma_f32_16x16x32_bf16 a[20:23], v[180:183], v[84:87], a[20:23]\n"
    "v_accvgpr_read_b32 v102, a18\n"
    "v_accvgpr_read_b32 v103, a19\n"
    "v_fma_f32 v100, v100, v222, s94\n"
    "v_mfma_f32_16x16x32_bf16 v[120:123], v[192:195], v[240:243], v[120:123]\n"
    "v_fma_f32 v101, v101, v222, s94\n"
    "v_fma_f32 v102, v102, v222, s94\n"
    "v_fma_f32 v103, v103, v222, s94\n"
    "v_mfma_f32_16x16x32_bf16 v[120:123], v[192:195], v[88:91], v[120:123]\n"
    "v_cvt_pk_bf16_f32 v50, v100, v101\n"
    "v_cvt_pk_bf16_f32 v51, v102, v103\n"
    "v_mfma_f32_16x16x32_bf16 a[8:11], v[168:171], v[240:243], a[8:11]\n"
    "buffer_store_dwordx4 v[48:51], v205, s[28:31], 0 offen sc0 sc1\n"
    "v_mul_f32 v224, s68, v116\n"
    "v_rcp_f32 v224, v224\n"
    "v_accvgpr_read_b32 v104, a4\n"
    "v_mfma_f32_16x16x32_bf16 a[8:11], v[172:175], v[88:91], a[8:11]\n"
    "v_accvgpr_read_b32 v105, a5\n"
    "v_accvgpr_read_b32 v106, a6\n"
    "v_accvgpr_read_b32 v107, a7\n"
    "v_mfma_f32_16x16x32_bf16 a[24:27], v[176:179], v[240:243], a[24:27]\n"
    "v_fma_f32 v104, v104, v224, s94\n"
    "v_fma_f32 v105, v105, v224, s94\n"
    "v_fma_f32 v106, v106, v224, s94\n"
    "v_mfma_f32_16x16x32_bf16 a[24:27], v[180:183], v[88:91], a[24:27]\n"
    "v_fma_f32 v107, v107, v224, s94\n"
    "v_cvt_pk_bf16_f32 v52, v104, v105\n"
    "v_cvt_pk_bf16_f32 v53, v106, v107\n"
    "v_mfma_f32_16x16x32_bf16 v[124:127], v[192:195], v[244:247], v[124:127]\n"
    "v_accvgpr_read_b32 v108, a20\n"
    "v_accvgpr_read_b32 v109, a21\n"
    "v_accvgpr_read_b32 v110, a22\n"
    "v_mfma_f32_16x16x32_bf16 v[124:127], v[192:195], v[92:95], v[124:127]\n"
    "v_accvgpr_read_b32 v111, a23\n"
    "v_fma_f32 v108, v108, v224, s94\n"
    "v_fma_f32 v109, v109, v224, s94\n"
    "v_mfma_f32_16x16x32_bf16 a[12:15], v[168:171], v[244:247], a[12:15]\n"
    "v_fma_f32 v110, v110, v224, s94\n"
    "v_fma_f32 v111, v111, v224, s94\n"
    "v_mfma_f32_16x16x32_bf16 a[12:15], v[172:175], v[92:95], a[12:15]\n"
    "v_cvt_pk_bf16_f32 v54, v108, v109\n"
    "v_cvt_pk_bf16_f32 v55, v110, v111\n"
    "v_mfma_f32_16x16x32_bf16 a[28:31], v[176:179], v[244:247], a[28:31]\n"
    "ds_read_b128 v[168:171], v201 offset:28672\n"
    "v_mfma_f32_16x16x32_bf16 a[28:31], v[180:183], v[92:95], a[28:31]\n"
    "s_waitcnt lgkmcnt(1)\n"
    "ds_read_b128 v[172:175], v201 offset:29696\n"
    "v_mfma_f32_16x16x32_bf16 a[32:35], v[160:163], v[128:131], a[32:35]\n"
    "ds_read_b128 v[176:179], v207 offset:4096\n"
    "v_mfma_f32_16x16x32_bf16 a[32:35], v[164:167], v[144:147], a[32:35]\n"
    "buffer_store_dwordx4 v[52:55], v205, s[28:31], s62 offen sc0 sc1\n"
    "v_mul_f32 v226, s68, v120\n"
    "v_rcp_f32 v226, v226\n"
    "v_mfma_f32_16x16x32_bf16 a[36:39], v[160:163], v[132:135], a[36:39]\n"
    "v_accvgpr_read_b32 v96, a8\n"
    "v_accvgpr_read_b32 v97, a9\n"
    "v_mfma_f32_16x16x32_bf16 a[36:39], v[164:167], v[148:151], a[36:39]\n"
    "v_accvgpr_read_b32 v98, a10\n"
    "v_accvgpr_read_b32 v99, a11\n"
    "v_mfma_f32_16x16x32_bf16 a[40:43], v[160:163], v[136:139], a[40:43]\n"
    "ds_read_b128 v[180:183], v207 offset:5120\n"
    "v_fma_f32 v96, v96, v226, s94\n"
    "v_mfma_f32_16x16x32_bf16 a[40:43], v[164:167], v[152:155], a[40:43]\n"
    "v_fma_f32 v97, v97, v226, s94\n"
    "v_fma_f32 v98, v98, v226, s94\n"
    "v_mfma_f32_16x16x32_bf16 a[44:47], v[160:163], v[140:143], a[44:47]\n"
    "v_fma_f32 v99, v99, v226, s94\n"
    "v_cvt_pk_bf16_f32 v56, v96, v97\n"
    "v_mfma_f32_16x16x32_bf16 a[44:47], v[164:167], v[156:159], a[44:47]\n"
    "s_waitcnt lgkmcnt(2)\n"
    "v_cvt_pk_bf16_f32 v57, v98, v99\n"
    "v_accvgpr_read_b32 v100, a24\n"
    "v_mfma_f32_16x16x32_bf16 a[44:47], v[168:171], v[44:47], a[44:47]\n"
    "ds_read_b128 v[160:163], v201 offset:6144\n"
    "v_accvgpr_read_b32 v101, a25\n"
    "v_mfma_f32_16x16x32_bf16 a[44:47], v[172:175], v[76:79], a[44:47]\n"
    "v_accvgpr_read_b32 v102, a26\n"
    "v_accvgpr_read_b32 v103, a27\n"
    "v_mfma_f32_16x16x32_bf16 a[40:43], v[168:171], v[40:43], a[40:43]\n"
    "v_fma_f32 v100, v100, v226, s94\n"
    "v_fma_f32 v101, v101, v226, s94\n"
    "v_mfma_f32_16x16x32_bf16 a[40:43], v[172:175], v[72:75], a[40:43]\n"
    "v_fma_f32 v102, v102, v226, s94\n"
    "v_fma_f32 v103, v103, v226, s94\n"
    "v_mfma_f32_16x16x32_bf16 a[36:39], v[168:171], v[36:39], a[36:39]\n"
    "ds_read_b128 v[164:167], v201 offset:7168\n"
    "v_cvt_pk_bf16_f32 v58, v100, v101\n"
    "v_mfma_f32_16x16x32_bf16 a[36:39], v[172:175], v[68:71], a[36:39]\n"
    "v_cvt_pk_bf16_f32 v59, v102, v103\n"
    "v_mfma_f32_16x16x32_bf16 a[32:35], v[168:171], v[32:35], a[32:35]\n"
    "buffer_store_dwordx4 v[56:59], v205, s[28:31], s63 offen sc0 sc1\n"
    "v_mul_f32 v228, s68, v124\n"
    "v_rcp_f32 v228, v228\n"
    "v_mfma_f32_16x16x32_bf16 a[32:35], v[172:175], v[64:67], a[32:35]\n"
    "s_waitcnt lgkmcnt(2)\n"
    "v_accvgpr_read_b32 v104, a12\n"
    "v_accvgpr_read_b32 v105, a13\n"
    "v_mfma_f32_16x16x32_bf16 a[32:35], v[176:179], v[232:235], a[32:35]\n"
    "ds_read_b128 v[168:171], v201 offset:30720\n"
    "v_accvgpr_read_b32 v106, a14\n"
    "v_mfma_f32_16x16x32_bf16 a[32:35], v[180:183], v[80:83], a[32:35]\n"
    "v_accvgpr_read_b32 v107, a15\n"
    "v_fma_f32 v104, v104, v228, s94\n"
    "v_mfma_f32_16x16x32_bf16 a[36:39], v[176:179], v[236:239], a[36:39]\n"
    "v_fma_f32 v105, v105, v228, s94\n"
    "v_fma_f32 v106, v106, v228, s94\n"
    "v_mfma_f32_16x16x32_bf16 a[36:39], v[180:183], v[84:87], a[36:39]\n"
    "v_fma_f32 v107, v107, v228, s94\n"
    "v_cvt_pk_bf16_f32 v60, v104, v105\n"
    "v_mfma_f32_16x16x32_bf16 a[40:43], v[176:179], v[240:243], a[40:43]\n"
    "ds_read_b128 v[172:175], v201 offset:31744\n"
    "v_cvt_pk_bf16_f32 v61, v106, v107\n"
    "v_mfma_f32_16x16x32_bf16 a[40:43], v[180:183], v[88:91], a[40:43]\n"
    "v_accvgpr_read_b32 v108, a28\n"
    "v_accvgpr_read_b32 v109, a29\n"
    "v_mfma_f32_16x16x32_bf16 a[44:47], v[176:179], v[244:247], a[44:47]\n"
    "v_accvgpr_read_b32 v110, a30\n"
    "v_accvgpr_read_b32 v111, a31\n"
    "v_mfma_f32_16x16x32_bf16 a[44:47], v[180:183], v[92:95], a[44:47]\n"
    "s_waitcnt lgkmcnt(2)\n"
    "v_fma_f32 v108, v108, v228, s94\n"
    "v_fma_f32 v109, v109, v228, s94\n"
    "v_mfma_f32_16x16x32_bf16 a[48:51], v[160:163], v[128:131], a[48:51]\n"
    "ds_read_b128 v[176:179], v207 offset:6144\n"
    "v_fma_f32 v110, v110, v228, s94\n"
    "v_mfma_f32_16x16x32_bf16 a[48:51], v[164:167], v[144:147], a[48:51]\n"
    "v_fma_f32 v111, v111, v228, s94\n"
    "v_cvt_pk_bf16_f32 v62, v108, v109\n"
    "v_mfma_f32_16x16x32_bf16 a[52:55], v[160:163], v[132:135], a[52:55]\n"
    "v_cvt_pk_bf16_f32 v63, v110, v111\n"
    "v_mfma_f32_16x16x32_bf16 a[52:55], v[164:167], v[148:151], a[52:55]\n"
    "buffer_store_dwordx4 v[60:63], v205, s[28:31], s64 offen sc0 sc1\n"
    "v_accvgpr_read_b32 v96, a32\n"
    "v_accvgpr_read_b32 v97, a33\n"
    "v_accvgpr_read_b32 v98, a34\n"
    "v_mfma_f32_16x16x32_bf16 a[56:59], v[160:163], v[136:139], a[56:59]\n"
    "ds_read_b128 v[180:183], v207 offset:7168\n"
    "v_accvgpr_read_b32 v99, a35\n"
    "v_mfma_f32_16x16x32_bf16 a[56:59], v[164:167], v[152:155], a[56:59]\n"
    "v_fma_f32 v96, v96, v222, s94\n"
    "v_fma_f32 v97, v97, v222, s94\n"
    "v_mfma_f32_16x16x32_bf16 a[60:63], v[160:163], v[140:143], a[60:63]\n"
    "v_fma_f32 v98, v98, v222, s94\n"
    "v_fma_f32 v99, v99, v222, s94\n"
    "v_mfma_f32_16x16x32_bf16 a[60:63], v[164:167], v[156:159], a[60:63]\n"
    "s_waitcnt lgkmcnt(2)\n"
    "v_cvt_pk_bf16_f32 v184, v96, v97\n"
    "v_cvt_pk_bf16_f32 v185, v98, v99\n"
    "v_mfma_f32_16x16x32_bf16 a[60:63], v[168:171], v[44:47], a[60:63]\n"
    "ds_read_b128 v[160:163], v201 offset:8192\n"
    "v_accvgpr_read_b32 v100, a36\n"
    "v_mfma_f32_16x16x32_bf16 a[60:63], v[172:175], v[76:79], a[60:63]\n"
    "v_accvgpr_read_b32 v101, a37\n"
    "v_accvgpr_read_b32 v102, a38\n"
    "v_mfma_f32_16x16x32_bf16 a[56:59], v[168:171], v[40:43], a[56:59]\n"
    "v_accvgpr_read_b32 v103, a39\n"
    "v_fma_f32 v100, v100, v224, s94\n"
    "v_mfma_f32_16x16x32_bf16 a[56:59], v[172:175], v[72:75], a[56:59]\n"
    "v_fma_f32 v101, v101, v224, s94\n"
    "v_fma_f32 v102, v102, v224, s94\n"
    "v_mfma_f32_16x16x32_bf16 a[52:55], v[168:171], v[36:39], a[52:55]\n"
    "ds_read_b128 v[164:167], v201 offset:9216\n"
    "v_fma_f32 v103, v103, v224, s94\n"
    "v_mfma_f32_16x16x32_bf16 a[52:55], v[172:175], v[68:71], a[52:55]\n"
    "v_cvt_pk_bf16_f32 v188, v100, v101\n"
    "v_cvt_pk_bf16_f32 v189, v102, v103\n"
    "v_mfma_f32_16x16x32_bf16 a[48:51], v[168:171], v[32:35], a[48:51]\n"
    "v_accvgpr_read_b32 v104, a40\n"
    "v_accvgpr_read_b32 v105, a41\n"
    "v_mfma_f32_16x16x32_bf16 a[48:51], v[172:175], v[64:67], a[48:51]\n"
    "s_waitcnt lgkmcnt(2)\n"
    "v_accvgpr_read_b32 v106, a42\n"
    "v_accvgpr_read_b32 v107, a43\n"
    "v_mfma_f32_16x16x32_bf16 a[48:51], v[176:179], v[232:235], a[48:51]\n"
    "ds_read_b128 v[168:171], v201 offset:32768\n"
    "v_fma_f32 v104, v104, v226, s94\n"
    "v_mfma_f32_16x16x32_bf16 a[48:51], v[180:183], v[80:83], a[48:51]\n"
    "v_fma_f32 v105, v105, v226, s94\n"
    "v_fma_f32 v106, v106, v226, s94\n"
    "v_mfma_f32_16x16x32_bf16 a[52:55], v[176:179], v[236:239], a[52:55]\n"
    "v_fma_f32 v107, v107, v226, s94\n"
    "v_cvt_pk_bf16_f32 v248, v104, v105\n"
    "v_mfma_f32_16x16x32_bf16 a[52:55], v[180:183], v[84:87], a[52:55]\n"
    "v_cvt_pk_bf16_f32 v249, v106, v107\n"
    "v_accvgpr_read_b32 v108, a44\n"
    "v_mfma_f32_16x16x32_bf16 a[56:59], v[176:179], v[240:243], a[56:59]\n"
    "ds_read_b128 v[172:175], v201 offset:33792\n"
    "v_accvgpr_read_b32 v109, a45\n"
    "v_mfma_f32_16x16x32_bf16 a[56:59], v[180:183], v[88:91], a[56:59]\n"
    "v_accvgpr_read_b32 v110, a46\n"
    "v_accvgpr_read_b32 v111, a47\n"
    "v_mfma_f32_16x16x32_bf16 a[60:63], v[176:179], v[244:247], a[60:63]\n"
    "v_fma_f32 v108, v108, v228, s94\n"
    "v_fma_f32 v109, v109, v228, s94\n"
    "v_mfma_f32_16x16x32_bf16 a[60:63], v[180:183], v[92:95], a[60:63]\n"
    "s_waitcnt lgkmcnt(2)\n"
    "v_fma_f32 v110, v110, v228, s94\n"
    "v_fma_f32 v111, v111, v228, s94\n"
    "v_mfma_f32_16x16x32_bf16 a[64:67], v[160:163], v[128:131], a[64:67]\n"
    "ds_read_b128 v[176:179], v207 offset:8192\n"
    "v_cvt_pk_bf16_f32 v252, v108, v109\n"
    "v_mfma_f32_16x16x32_bf16 a[64:67], v[164:167], v[144:147], a[64:67]\n"
    "v_cvt_pk_bf16_f32 v253, v110, v111\n"
    "v_accvgpr_read_b32 v96, a48\n"
    "v_mfma_f32_16x16x32_bf16 a[68:71], v[160:163], v[132:135], a[68:71]\n"
    "v_accvgpr_read_b32 v97, a49\n"
    "v_accvgpr_read_b32 v98, a50\n"
    "v_mfma_f32_16x16x32_bf16 a[68:71], v[164:167], v[148:151], a[68:71]\n"
    "v_accvgpr_read_b32 v99, a51\n"
    "v_fma_f32 v96, v96, v222, s94\n"
    "v_mfma_f32_16x16x32_bf16 a[72:75], v[160:163], v[136:139], a[72:75]\n"
    "ds_read_b128 v[180:183], v207 offset:9216\n"
    "v_fma_f32 v97, v97, v222, s94\n"
    "v_mfma_f32_16x16x32_bf16 a[72:75], v[164:167], v[152:155], a[72:75]\n"
    "v_fma_f32 v98, v98, v222, s94\n"
    "v_fma_f32 v99, v99, v222, s94\n"
    "v_mfma_f32_16x16x32_bf16 a[76:79], v[160:163], v[140:143], a[76:79]\n"
    "v_cvt_pk_bf16_f32 v186, v96, v97\n"
    "v_cvt_pk_bf16_f32 v187, v98, v99\n"
    "v_mfma_f32_16x16x32_bf16 a[76:79], v[164:167], v[156:159], a[76:79]\n"
    "s_waitcnt lgkmcnt(2)\n"
    "buffer_store_dwordx4 v[184:187], v205, s[28:31], 0 offen offset:64 sc0 sc1\n"
    "v_accvgpr_read_b32 v100, a52\n"
    "v_accvgpr_read_b32 v101, a53\n"
    "v_accvgpr_read_b32 v102, a54\n"
    "v_mfma_f32_16x16x32_bf16 a[76:79], v[168:171], v[44:47], a[76:79]\n"
    "ds_read_b128 v[160:163], v201 offset:10240\n"
    "v_accvgpr_read_b32 v103, a55\n"
    "v_mfma_f32_16x16x32_bf16 a[76:79], v[172:175], v[76:79], a[76:79]\n"
    "v_fma_f32 v100, v100, v224, s94\n"
    "v_fma_f32 v101, v101, v224, s94\n"
    "v_mfma_f32_16x16x32_bf16 a[72:75], v[168:171], v[40:43], a[72:75]\n"
    "v_fma_f32 v102, v102, v224, s94\n"
    "v_fma_f32 v103, v103, v224, s94\n"
    "v_mfma_f32_16x16x32_bf16 a[72:75], v[172:175], v[72:75], a[72:75]\n"
    "v_cvt_pk_bf16_f32 v190, v100, v101\n"
    "v_cvt_pk_bf16_f32 v191, v102, v103\n"
    "v_mfma_f32_16x16x32_bf16 a[68:71], v[168:171], v[36:39], a[68:71]\n"
    "ds_read_b128 v[164:167], v201 offset:11264\n"
    "v_mfma_f32_16x16x32_bf16 a[68:71], v[172:175], v[68:71], a[68:71]\n"
    "buffer_store_dwordx4 v[188:191], v205, s[28:31], s62 offen offset:64 sc0 sc1\n"
    "v_accvgpr_read_b32 v104, a56\n"
    "v_accvgpr_read_b32 v105, a57\n"
    "v_accvgpr_read_b32 v106, a58\n"
    "v_mfma_f32_16x16x32_bf16 a[64:67], v[168:171], v[32:35], a[64:67]\n"
    "v_accvgpr_read_b32 v107, a59\n"
    "v_fma_f32 v104, v104, v226, s94\n"
    "v_mfma_f32_16x16x32_bf16 a[64:67], v[172:175], v[64:67], a[64:67]\n"
    "s_waitcnt lgkmcnt(2)\n"
    "v_fma_f32 v105, v105, v226, s94\n"
    "v_fma_f32 v106, v106, v226, s94\n"
    "v_mfma_f32_16x16x32_bf16 a[64:67], v[176:179], v[232:235], a[64:67]\n"
    "ds_read_b128 v[168:171], v201 offset:34816\n"
    "v_fma_f32 v107, v107, v226, s94\n"
    "v_mfma_f32_16x16x32_bf16 a[64:67], v[180:183], v[80:83], a[64:67]\n"
    "v_cvt_pk_bf16_f32 v250, v104, v105\n"
    "v_cvt_pk_bf16_f32 v251, v106, v107\n"
    "v_mfma_f32_16x16x32_bf16 a[68:71], v[176:179], v[236:239], a[68:71]\n"
    "buffer_store_dwordx4 v[248:251], v205, s[28:31], s63 offen offset:64 sc0 sc1\n"
    "v_accvgpr_read_b32 v108, a60\n"
    "v_accvgpr_read_b32 v109, a61\n"
    "v_accvgpr_read_b32 v110, a62\n"
    "v_mfma_f32_16x16x32_bf16 a[68:71], v[180:183], v[84:87], a[68:71]\n"
    "v_accvgpr_read_b32 v111, a63\n"
    "v_fma_f32 v108, v108, v228, s94\n"
    "v_mfma_f32_16x16x32_bf16 a[72:75], v[176:179], v[240:243], a[72:75]\n"
    "ds_read_b128 v[172:175], v201 offset:35840\n"
    "v_fma_f32 v109, v109, v228, s94\n"
    "v_mfma_f32_16x16x32_bf16 a[72:75], v[180:183], v[88:91], a[72:75]\n"
    "v_fma_f32 v110, v110, v228, s94\n"
    "v_fma_f32 v111, v111, v228, s94\n"
    "v_mfma_f32_16x16x32_bf16 a[76:79], v[176:179], v[244:247], a[76:79]\n"
    "v_cvt_pk_bf16_f32 v254, v108, v109\n"
    "v_cvt_pk_bf16_f32 v255, v110, v111\n"
    "v_mfma_f32_16x16x32_bf16 a[76:79], v[180:183], v[92:95], a[76:79]\n"
    "s_waitcnt lgkmcnt(2)\n"
    "buffer_store_dwordx4 v[252:255], v205, s[28:31], s64 offen offset:64 sc0 sc1\n"
    "v_accvgpr_read_b32 v96, a64\n"
    "v_accvgpr_read_b32 v97, a65\n"
    "v_accvgpr_read_b32 v98, a66\n"
    "v_mfma_f32_16x16x32_bf16 a[80:83], v[160:163], v[128:131], a[80:83]\n"
    "ds_read_b128 v[176:179], v207 offset:10240\n"
    "v_accvgpr_read_b32 v99, a67\n"
    "v_mfma_f32_16x16x32_bf16 a[80:83], v[164:167], v[144:147], a[80:83]\n"
    "v_fma_f32 v96, v96, v222, s94\n"
    "v_fma_f32 v97, v97, v222, s94\n"
    "v_mfma_f32_16x16x32_bf16 a[84:87], v[160:163], v[132:135], a[84:87]\n"
    "v_fma_f32 v98, v98, v222, s94\n"
    "v_fma_f32 v99, v99, v222, s94\n"
    "v_mfma_f32_16x16x32_bf16 a[84:87], v[164:167], v[148:151], a[84:87]\n"
    "v_cvt_pk_bf16_f32 v48, v96, v97\n"
    "v_cvt_pk_bf16_f32 v49, v98, v99\n"
    "v_mfma_f32_16x16x32_bf16 a[88:91], v[160:163], v[136:139], a[88:91]\n"
    "ds_read_b128 v[180:183], v207 offset:11264\n"
    "v_accvgpr_read_b32 v100, a68\n"
    "v_mfma_f32_16x16x32_bf16 a[88:91], v[164:167], v[152:155], a[88:91]\n"
    "v_accvgpr_read_b32 v101, a69\n"
    "v_accvgpr_read_b32 v102, a70\n"
    "v_mfma_f32_16x16x32_bf16 a[92:95], v[160:163], v[140:143], a[92:95]\n"
    "v_accvgpr_read_b32 v103, a71\n"
    "v_fma_f32 v100, v100, v224, s94\n"
    "v_mfma_f32_16x16x32_bf16 a[92:95], v[164:167], v[156:159], a[92:95]\n"
    "s_waitcnt lgkmcnt(2)\n"
    "v_fma_f32 v101, v101, v224, s94\n"
    "v_fma_f32 v102, v102, v224, s94\n"
    "v_mfma_f32_16x16x32_bf16 a[92:95], v[168:171], v[44:47], a[92:95]\n"
    "ds_read_b128 v[160:163], v201 offset:12288\n"
    "v_fma_f32 v103, v103, v224, s94\n"
    "v_mfma_f32_16x16x32_bf16 a[92:95], v[172:175], v[76:79], a[92:95]\n"
    "v_cvt_pk_bf16_f32 v52, v100, v101\n"
    "v_cvt_pk_bf16_f32 v53, v102, v103\n"
    "v_mfma_f32_16x16x32_bf16 a[88:91], v[168:171], v[40:43], a[88:91]\n"
    "v_accvgpr_read_b32 v104, a72\n"
    "v_accvgpr_read_b32 v105, a73\n"
    "v_mfma_f32_16x16x32_bf16 a[88:91], v[172:175], v[72:75], a[88:91]\n"
    "v_accvgpr_read_b32 v106, a74\n"
    "v_accvgpr_read_b32 v107, a75\n"
    "v_mfma_f32_16x16x32_bf16 a[84:87], v[168:171], v[36:39], a[84:87]\n"
    "ds_read_b128 v[164:167], v201 offset:13312\n"
    "v_fma_f32 v104, v104, v226, s94\n"
    "v_mfma_f32_16x16x32_bf16 a[84:87], v[172:175], v[68:71], a[84:87]\n"
    "v_fma_f32 v105, v105, v226, s94\n"
    "v_fma_f32 v106, v106, v226, s94\n"
    "v_mfma_f32_16x16x32_bf16 a[80:83], v[168:171], v[32:35], a[80:83]\n"
    "v_fma_f32 v107, v107, v226, s94\n"
    "v_cvt_pk_bf16_f32 v56, v104, v105\n"
    "v_mfma_f32_16x16x32_bf16 a[80:83], v[172:175], v[64:67], a[80:83]\n"
    "s_waitcnt lgkmcnt(2)\n"
    "v_cvt_pk_bf16_f32 v57, v106, v107\n"
    "v_accvgpr_read_b32 v108, a76\n"
    "v_mfma_f32_16x16x32_bf16 a[80:83], v[176:179], v[232:235], a[80:83]\n"
    "ds_read_b128 v[168:171], v201 offset:36864\n"
    "v_accvgpr_read_b32 v109, a77\n"
    "v_mfma_f32_16x16x32_bf16 a[80:83], v[180:183], v[80:83], a[80:83]\n"
    "v_accvgpr_read_b32 v110, a78\n"
    "v_accvgpr_read_b32 v111, a79\n"
    "v_mfma_f32_16x16x32_bf16 a[84:87], v[176:179], v[236:239], a[84:87]\n"
    "v_fma_f32 v108, v108, v228, s94\n"
    "v_fma_f32 v109, v109, v228, s94\n"
    "v_mfma_f32_16x16x32_bf16 a[84:87], v[180:183], v[84:87], a[84:87]\n"
    "v_fma_f32 v110, v110, v228, s94\n"
    "v_fma_f32 v111, v111, v228, s94\n"
    "v_mfma_f32_16x16x32_bf16 a[88:91], v[176:179], v[240:243], a[88:91]\n"
    "ds_read_b128 v[172:175], v201 offset:37888\n"
    "v_cvt_pk_bf16_f32 v60, v108, v109\n"
    "v_mfma_f32_16x16x32_bf16 a[88:91], v[180:183], v[88:91], a[88:91]\n"
    "v_cvt_pk_bf16_f32 v61, v110, v111\n"
    "v_mfma_f32_16x16x32_bf16 a[92:95], v[176:179], v[244:247], a[92:95]\n"
    "v_mfma_f32_16x16x32_bf16 a[92:95], v[180:183], v[92:95], a[92:95]\n"
    "s_waitcnt lgkmcnt(2)\n"
    "v_accvgpr_read_b32 v96, a80\n"
    "v_accvgpr_read_b32 v97, a81\n"
    "v_mfma_f32_16x16x32_bf16 a[96:99], v[160:163], v[128:131], a[96:99]\n"
    "ds_read_b128 v[176:179], v207 offset:12288\n"
    "v_accvgpr_read_b32 v98, a82\n"
    "v_mfma_f32_16x16x32_bf16 a[96:99], v[164:167], v[144:147], a[96:99]\n"
    "v_accvgpr_read_b32 v99, a83\n"
    "v_fma_f32 v96, v96, v222, s94\n"
    "v_mfma_f32_16x16x32_bf16 a[100:103], v[160:163], v[132:135], a[100:103]\n"
    "v_fma_f32 v97, v97, v222, s94\n"
    "v_fma_f32 v98, v98, v222, s94\n"
    "v_mfma_f32_16x16x32_bf16 a[100:103], v[164:167], v[148:151], a[100:103]\n"
    "v_fma_f32 v99, v99, v222, s94\n"
    "v_cvt_pk_bf16_f32 v50, v96, v97\n"
    "v_mfma_f32_16x16x32_bf16 a[104:107], v[160:163], v[136:139], a[104:107]\n"
    "ds_read_b128 v[180:183], v207 offset:13312\n"
    "v_cvt_pk_bf16_f32 v51, v98, v99\n"
    "v_mfma_f32_16x16x32_bf16 a[104:107], v[164:167], v[152:155], a[104:107]\n"
    "buffer_store_dwordx4 v[48:51], v205, s[28:31], 0 offen offset:128 sc0 sc1\n"
    "v_accvgpr_read_b32 v100, a84\n"
    "v_accvgpr_read_b32 v101, a85\n"
    "v_accvgpr_read_b32 v102, a86\n"
    "v_mfma_f32_16x16x32_bf16 a[108:111], v[160:163], v[140:143], a[108:111]\n"
    "v_accvgpr_read_b32 v103, a87\n"
    "v_fma_f32 v100, v100, v224, s94\n"
    "v_mfma_f32_16x16x32_bf16 a[108:111], v[164:167], v[156:159], a[108:111]\n"
    "s_waitcnt lgkmcnt(2)\n"
    "s_cmp_eq_u32 s57, 0\n"
    "s_cselect_b32 s77, s74, 0\n"
    "v_mov_b32 v196, 0x40000000\n"
    "v_mov_b32 v197, 0x800\n"
    "v_cmp_gt_u32 vcc, s77, v231\n"
    "v_mov_b32 v198, 0\n"
    "v_mov_b32 v198, 0\n"
    "v_cndmask_b32 v197, v196, v197, vcc\n"
    "buffer_store_dword v221, v197, s[44:47], 0 offen sc1\n"
    "v_mfma_f32_16x16x32_bf16 a[108:111], v[168:171], v[44:47], a[108:111]\n"
    "ds_read_b128 v[160:163], v201 offset:14336\n"
    "v_fma_f32 v101, v101, v224, s94\n"
    "v_mfma_f32_16x16x32_bf16 a[108:111], v[172:175], v[76:79], a[108:111]\n"
    "v_fma_f32 v102, v102, v224, s94\n"
    "v_fma_f32 v103, v103, v224, s94\n"
    "v_mfma_f32_16x16x32_bf16 a[104:107], v[168:171], v[40:43], a[104:107]\n"
    "v_cvt_pk_bf16_f32 v54, v100, v101\n"
    "v_cvt_pk_bf16_f32 v55, v102, v103\n"
    "v_mfma_f32_16x16x32_bf16 a[104:107], v[172:175], v[72:75], a[104:107]\n"
    "buffer_store_dwordx4 v[52:55], v205, s[28:31], s62 offen offset:128 sc0 sc1\n"
    "v_accvgpr_read_b32 v104, a88\n"
    "v_accvgpr_read_b32 v105, a89\n"
    "v_accvgpr_read_b32 v106, a90\n"
    "v_mfma_f32_16x16x32_bf16 a[100:103], v[168:171], v[36:39], a[100:103]\n"
    "ds_read_b128 v[164:167], v201 offset:15360\n"
    "v_accvgpr_read_b32 v107, a91\n"
    "v_mfma_f32_16x16x32_bf16 a[100:103], v[172:175], v[68:71], a[100:103]\n"
    "v_fma_f32 v104, v104, v226, s94\n"
    "v_fma_f32 v105, v105, v226, s94\n"
    "v_mfma_f32_16x16x32_bf16 a[96:99], v[168:171], v[32:35], a[96:99]\n"
    "v_fma_f32 v106, v106, v226, s94\n"
    "v_fma_f32 v107, v107, v226, s94\n"
    "v_mfma_f32_16x16x32_bf16 a[96:99], v[172:175], v[64:67], a[96:99]\n"
    "s_waitcnt lgkmcnt(2)\n"
    "v_cvt_pk_bf16_f32 v58, v104, v105\n"
    "v_cvt_pk_bf16_f32 v59, v106, v107\n"
    "v_mfma_f32_16x16x32_bf16 a[96:99], v[176:179], v[232:235], a[96:99]\n"
    "ds_read_b128 v[168:171], v201 offset:38912\n"
    "v_mfma_f32_16x16x32_bf16 a[96:99], v[180:183], v[80:83], a[96:99]\n"
    "buffer_store_dwordx4 v[56:59], v205, s[28:31], s63 offen offset:128 sc0 sc1\n"
    "v_accvgpr_read_b32 v108, a92\n"
    "v_accvgpr_read_b32 v109, a93\n"
    "v_accvgpr_read_b32 v110, a94\n"
    "v_mfma_f32_16x16x32_bf16 a[100:103], v[176:179], v[236:239], a[100:103]\n"
    "v_accvgpr_read_b32 v111, a95\n"
    "v_fma_f32 v108, v108, v228, s94\n"
    "v_mfma_f32_16x16x32_bf16 a[100:103], v[180:183], v[84:87], a[100:103]\n"
    "v_fma_f32 v109, v109, v228, s94\n"
    "v_fma_f32 v110, v110, v228, s94\n"
    "v_mfma_f32_16x16x32_bf16 a[104:107], v[176:179], v[240:243], a[104:107]\n"
    "ds_read_b128 v[172:175], v201 offset:39936\n"
    "v_fma_f32 v111, v111, v228, s94\n"
    "v_mfma_f32_16x16x32_bf16 a[104:107], v[180:183], v[88:91], a[104:107]\n"
    "v_cvt_pk_bf16_f32 v62, v108, v109\n"
    "v_cvt_pk_bf16_f32 v63, v110, v111\n"
    "v_mfma_f32_16x16x32_bf16 a[108:111], v[176:179], v[244:247], a[108:111]\n"
    "buffer_store_dwordx4 v[60:63], v205, s[28:31], s64 offen offset:128 sc0 sc1\n"
    "v_mfma_f32_16x16x32_bf16 a[108:111], v[180:183], v[92:95], a[108:111]\n"
    "s_waitcnt lgkmcnt(2)\n"
    "v_accvgpr_read_b32 v96, a96\n"
    "v_accvgpr_read_b32 v97, a97\n"
    "v_mfma_f32_16x16x32_bf16 a[112:115], v[160:163], v[128:131], a[112:115]\n"
    "ds_read_b128 v[176:179], v207 offset:14336\n"
    "v_accvgpr_read_b32 v98, a98\n"
    "v_mfma_f32_16x16x32_bf16 a[112:115], v[164:167], v[144:147], a[112:115]\n"
    "v_accvgpr_read_b32 v99, a99\n"
    "v_fma_f32 v96, v96, v222, s94\n"
    "v_mfma_f32_16x16x32_bf16 a[116:119], v[160:163], v[132:135], a[116:119]\n"
    "v_fma_f32 v97, v97, v222, s94\n"
    "v_fma_f32 v98, v98, v222, s94\n"
    "v_mfma_f32_16x16x32_bf16 a[116:119], v[164:167], v[148:151], a[116:119]\n"
    "v_fma_f32 v99, v99, v222, s94\n"
    "v_cvt_pk_bf16_f32 v184, v96, v97\n"
    "v_mfma_f32_16x16x32_bf16 a[120:123], v[160:163], v[136:139], a[120:123]\n"
    "ds_read_b128 v[180:183], v207 offset:15360\n"
    "v_cvt_pk_bf16_f32 v185, v98, v99\n"
    "v_mfma_f32_16x16x32_bf16 a[120:123], v[164:167], v[152:155], a[120:123]\n"
    "v_accvgpr_read_b32 v100, a100\n"
    "v_accvgpr_read_b32 v101, a101\n"
    "v_mfma_f32_16x16x32_bf16 a[124:127], v[160:163], v[140:143], a[124:127]\n"
    "v_accvgpr_read_b32 v102, a102\n"
    "v_accvgpr_read_b32 v103, a103\n"
    "v_mfma_f32_16x16x32_bf16 a[124:127], v[164:167], v[156:159], a[124:127]\n"
    "s_waitcnt lgkmcnt(2)\n"
    "v_fma_f32 v100, v100, v224, s94\n"
    "v_fma_f32 v101, v101, v224, s94\n"
    "v_mfma_f32_16x16x32_bf16 a[124:127], v[168:171], v[44:47], a[124:127]\n"
    "ds_read_b128 v[160:163], v201 offset:16384\n"
    "v_fma_f32 v102, v102, v224, s94\n"
    "v_mfma_f32_16x16x32_bf16 a[124:127], v[172:175], v[76:79], a[124:127]\n"
    "v_fma_f32 v103, v103, v224, s94\n"
    "v_cvt_pk_bf16_f32 v188, v100, v101\n"
    "v_mfma_f32_16x16x32_bf16 a[120:123], v[168:171], v[40:43], a[120:123]\n"
    "v_cvt_pk_bf16_f32 v189, v102, v103\n"
    "v_accvgpr_read_b32 v104, a104\n"
    "v_mfma_f32_16x16x32_bf16 a[120:123], v[172:175], v[72:75], a[120:123]\n"
    "v_accvgpr_read_b32 v105, a105\n"
    "v_accvgpr_read_b32 v106, a106\n"
    "v_mfma_f32_16x16x32_bf16 a[116:119], v[168:171], v[36:39], a[116:119]\n"
    "ds_read_b128 v[164:167], v201 offset:17408\n"
    "v_accvgpr_read_b32 v107, a107\n"
    "v_mfma_f32_16x16x32_bf16 a[116:119], v[172:175], v[68:71], a[116:119]\n"
    "v_fma_f32 v104, v104, v226, s94\n"
    "v_fma_f32 v105, v105, v226, s94\n"
    "v_mfma_f32_16x16x32_bf16 a[112:115], v[168:171], v[32:35], a[112:115]\n"
    "v_fma_f32 v106, v106, v226, s94\n"
    "v_fma_f32 v107, v107, v226, s94\n"
    "v_mfma_f32_16x16x32_bf16 a[112:115], v[172:175], v[64:67], a[112:115]\n"
    "s_waitcnt lgkmcnt(2)\n"
    "v_cvt_pk_bf16_f32 v248, v104, v105\n"
    "v_cvt_pk_bf16_f32 v249, v106, v107\n"
    "v_mfma_f32_16x16x32_bf16 a[112:115], v[176:179], v[232:235], a[112:115]\n"
    "ds_read_b128 v[168:171], v201 offset:40960\n"
    "v_accvgpr_read_b32 v108, a108\n"
    "v_mfma_f32_16x16x32_bf16 a[112:115], v[180:183], v[80:83], a[112:115]\n"
    "v_accvgpr_read_b32 v109, a109\n"
    "v_accvgpr_read_b32 v110, a110\n"
    "v_mfma_f32_16x16x32_bf16 a[116:119], v[176:179], v[236:239], a[116:119]\n"
    "v_accvgpr_read_b32 v111, a111\n"
    "v_fma_f32 v108, v108, v228, s94\n"
    "v_mfma_f32_16x16x32_bf16 a[116:119], v[180:183], v[84:87], a[116:119]\n"
    "v_fma_f32 v109, v109, v228, s94\n"
    "v_fma_f32 v110, v110, v228, s94\n"
    "v_mfma_f32_16x16x32_bf16 a[120:123], v[176:179], v[240:243], a[120:123]\n"
    "ds_read_b128 v[172:175], v201 offset:41984\n"
    "v_fma_f32 v111, v111, v228, s94\n"
    "v_mfma_f32_16x16x32_bf16 a[120:123], v[180:183], v[88:91], a[120:123]\n"
    "v_cvt_pk_bf16_f32 v252, v108, v109\n"
    "v_cvt_pk_bf16_f32 v253, v110, v111\n"
    "v_mfma_f32_16x16x32_bf16 a[124:127], v[176:179], v[244:247], a[124:127]\n"
    "v_mfma_f32_16x16x32_bf16 a[124:127], v[180:183], v[92:95], a[124:127]\n"
    "s_waitcnt lgkmcnt(2)\n"
    "v_accvgpr_read_b32 v96, a112\n"
    "v_accvgpr_read_b32 v97, a113\n"
    "v_mfma_f32_16x16x32_bf16 a[128:131], v[160:163], v[128:131], a[128:131]\n"
    "ds_read_b128 v[176:179], v207 offset:16384\n"
    "v_accvgpr_read_b32 v98, a114\n"
    "v_mfma_f32_16x16x32_bf16 a[128:131], v[164:167], v[144:147], a[128:131]\n"
    "v_accvgpr_read_b32 v99, a115\n"
    "v_fma_f32 v96, v96, v222, s94\n"
    "v_mfma_f32_16x16x32_bf16 a[132:135], v[160:163], v[132:135], a[132:135]\n"
    "v_fma_f32 v97, v97, v222, s94\n"
    "v_fma_f32 v98, v98, v222, s94\n"
    "v_mfma_f32_16x16x32_bf16 a[132:135], v[164:167], v[148:151], a[132:135]\n"
    "v_fma_f32 v99, v99, v222, s94\n"
    "v_cvt_pk_bf16_f32 v186, v96, v97\n"
    "v_mfma_f32_16x16x32_bf16 a[136:139], v[160:163], v[136:139], a[136:139]\n"
    "ds_read_b128 v[180:183], v207 offset:17408\n"
    "v_cvt_pk_bf16_f32 v187, v98, v99\n"
    "v_mfma_f32_16x16x32_bf16 a[136:139], v[164:167], v[152:155], a[136:139]\n"
    "buffer_store_dwordx4 v[184:187], v205, s[28:31], 0 offen offset:192 sc0 sc1\n"
    "v_accvgpr_read_b32 v100, a116\n"
    "v_accvgpr_read_b32 v101, a117\n"
    "v_accvgpr_read_b32 v102, a118\n"
    "v_mfma_f32_16x16x32_bf16 a[140:143], v[160:163], v[140:143], a[140:143]\n"
    "v_accvgpr_read_b32 v103, a119\n"
    "v_fma_f32 v100, v100, v224, s94\n"
    "v_mfma_f32_16x16x32_bf16 a[140:143], v[164:167], v[156:159], a[140:143]\n"
    "s_waitcnt lgkmcnt(2)\n"
    "v_fma_f32 v101, v101, v224, s94\n"
    "v_fma_f32 v102, v102, v224, s94\n"
    "v_mfma_f32_16x16x32_bf16 a[140:143], v[168:171], v[44:47], a[140:143]\n"
    "ds_read_b128 v[160:163], v201 offset:18432\n"
    "v_fma_f32 v103, v103, v224, s94\n"
    "v_mfma_f32_16x16x32_bf16 a[140:143], v[172:175], v[76:79], a[140:143]\n"
    "v_cvt_pk_bf16_f32 v190, v100, v101\n"
    "v_cvt_pk_bf16_f32 v191, v102, v103\n"
    "v_mfma_f32_16x16x32_bf16 a[136:139], v[168:171], v[40:43], a[136:139]\n"
    "buffer_store_dwordx4 v[188:191], v205, s[28:31], s62 offen offset:192 sc0 sc1\n"
    "v_accvgpr_read_b32 v104, a120\n"
    "v_accvgpr_read_b32 v105, a121\n"
    "v_accvgpr_read_b32 v106, a122\n"
    "v_mfma_f32_16x16x32_bf16 a[136:139], v[172:175], v[72:75], a[136:139]\n"
    "v_accvgpr_read_b32 v107, a123\n"
    "v_fma_f32 v104, v104, v226, s94\n"
    "v_mfma_f32_16x16x32_bf16 a[132:135], v[168:171], v[36:39], a[132:135]\n"
    "ds_read_b128 v[164:167], v201 offset:19456\n"
    "v_fma_f32 v105, v105, v226, s94\n"
    "v_mfma_f32_16x16x32_bf16 a[132:135], v[172:175], v[68:71], a[132:135]\n"
    "v_fma_f32 v106, v106, v226, s94\n"
    "v_fma_f32 v107, v107, v226, s94\n"
    "v_mfma_f32_16x16x32_bf16 a[128:131], v[168:171], v[32:35], a[128:131]\n"
    "v_cvt_pk_bf16_f32 v250, v104, v105\n"
    "v_cvt_pk_bf16_f32 v251, v106, v107\n"
    "v_mfma_f32_16x16x32_bf16 a[128:131], v[172:175], v[64:67], a[128:131]\n"
    "s_waitcnt lgkmcnt(2)\n"
    "buffer_store_dwordx4 v[248:251], v205, s[28:31], s63 offen offset:192 sc0 sc1\n"
    "v_accvgpr_read_b32 v108, a124\n"
    "v_accvgpr_read_b32 v109, a125\n"
    "v_accvgpr_read_b32 v110, a126\n"
    "v_mfma_f32_16x16x32_bf16 a[128:131], v[176:179], v[232:235], a[128:131]\n"
    "ds_read_b128 v[168:171], v201 offset:43008\n"
    "v_accvgpr_read_b32 v111, a127\n"
    "v_mfma_f32_16x16x32_bf16 a[128:131], v[180:183], v[80:83], a[128:131]\n"
    "v_fma_f32 v108, v108, v228, s94\n"
    "v_fma_f32 v109, v109, v228, s94\n"
    "v_mfma_f32_16x16x32_bf16 a[132:135], v[176:179], v[236:239], a[132:135]\n"
    "v_fma_f32 v110, v110, v228, s94\n"
    "v_fma_f32 v111, v111, v228, s94\n"
    "v_mfma_f32_16x16x32_bf16 a[132:135], v[180:183], v[84:87], a[132:135]\n"
    "v_cvt_pk_bf16_f32 v254, v108, v109\n"
    "v_cvt_pk_bf16_f32 v255, v110, v111\n"
    "v_mfma_f32_16x16x32_bf16 a[136:139], v[176:179], v[240:243], a[136:139]\n"
    "ds_read_b128 v[172:175], v201 offset:44032\n"
    "v_mfma_f32_16x16x32_bf16 a[136:139], v[180:183], v[88:91], a[136:139]\n"
    "buffer_store_dwordx4 v[252:255], v205, s[28:31], s64 offen offset:192 sc0 sc1\n"
    "v_mfma_f32_16x16x32_bf16 a[140:143], v[176:179], v[244:247], a[140:143]\n"
    "v_mfma_f32_16x16x32_bf16 a[140:143], v[180:183], v[92:95], a[140:143]\n"
    "s_waitcnt lgkmcnt(2)\n"
    "v_accvgpr_read_b32 v96, a128\n"
    "v_accvgpr_read_b32 v97, a129\n"
    "v_mfma_f32_16x16x32_bf16 a[144:147], v[160:163], v[128:131], a[144:147]\n"
    "ds_read_b128 v[176:179], v207 offset:18432\n"
    "v_accvgpr_read_b32 v98, a130\n"
    "v_mfma_f32_16x16x32_bf16 a[144:147], v[164:167], v[144:147], a[144:147]\n"
    "v_accvgpr_read_b32 v99, a131\n"
    "v_fma_f32 v96, v96, v222, s94\n"
    "v_mfma_f32_16x16x32_bf16 a[148:151], v[160:163], v[132:135], a[148:151]\n"
    "v_fma_f32 v97, v97, v222, s94\n"
    "v_fma_f32 v98, v98, v222, s94\n"
    "v_mfma_f32_16x16x32_bf16 a[148:151], v[164:167], v[148:151], a[148:151]\n"
    "v_fma_f32 v99, v99, v222, s94\n"
    "v_cvt_pk_bf16_f32 v48, v96, v97\n"
    "v_mfma_f32_16x16x32_bf16 a[152:155], v[160:163], v[136:139], a[152:155]\n"
    "ds_read_b128 v[180:183], v207 offset:19456\n"
    "v_cvt_pk_bf16_f32 v49, v98, v99\n"
    "v_mfma_f32_16x16x32_bf16 a[152:155], v[164:167], v[152:155], a[152:155]\n"
    "v_accvgpr_read_b32 v100, a132\n"
    "v_accvgpr_read_b32 v101, a133\n"
    "v_mfma_f32_16x16x32_bf16 a[156:159], v[160:163], v[140:143], a[156:159]\n"
    "v_accvgpr_read_b32 v102, a134\n"
    "v_accvgpr_read_b32 v103, a135\n"
    "v_mfma_f32_16x16x32_bf16 a[156:159], v[164:167], v[156:159], a[156:159]\n"
    "s_waitcnt lgkmcnt(2)\n"
    "v_fma_f32 v100, v100, v224, s94\n"
    "v_fma_f32 v101, v101, v224, s94\n"
    "v_mfma_f32_16x16x32_bf16 a[156:159], v[168:171], v[44:47], a[156:159]\n"
    "ds_read_b128 v[160:163], v201 offset:20480\n"
    "v_fma_f32 v102, v102, v224, s94\n"
    "v_mfma_f32_16x16x32_bf16 a[156:159], v[172:175], v[76:79], a[156:159]\n"
    "v_fma_f32 v103, v103, v224, s94\n"
    "v_cvt_pk_bf16_f32 v52, v100, v101\n"
    "v_mfma_f32_16x16x32_bf16 a[152:155], v[168:171], v[40:43], a[152:155]\n"
    "v_cvt_pk_bf16_f32 v53, v102, v103\n"
    "v_accvgpr_read_b32 v104, a136\n"
    "v_mfma_f32_16x16x32_bf16 a[152:155], v[172:175], v[72:75], a[152:155]\n"
    "v_accvgpr_read_b32 v105, a137\n"
    "v_accvgpr_read_b32 v106, a138\n"
    "v_mfma_f32_16x16x32_bf16 a[148:151], v[168:171], v[36:39], a[148:151]\n"
    "ds_read_b128 v[164:167], v201 offset:21504\n"
    "v_accvgpr_read_b32 v107, a139\n"
    "v_mfma_f32_16x16x32_bf16 a[148:151], v[172:175], v[68:71], a[148:151]\n"
    "v_fma_f32 v104, v104, v226, s94\n"
    "v_fma_f32 v105, v105, v226, s94\n"
    "v_mfma_f32_16x16x32_bf16 a[144:147], v[168:171], v[32:35], a[144:147]\n"
    "v_fma_f32 v106, v106, v226, s94\n"
    "v_fma_f32 v107, v107, v226, s94\n"
    "v_mfma_f32_16x16x32_bf16 a[144:147], v[172:175], v[64:67], a[144:147]\n"
    "s_waitcnt lgkmcnt(2)\n"
    "v_cvt_pk_bf16_f32 v56, v104, v105\n"
    "v_cvt_pk_bf16_f32 v57, v106, v107\n"
    "v_mfma_f32_16x16x32_bf16 a[144:147], v[176:179], v[232:235], a[144:147]\n"
    "ds_read_b128 v[168:171], v201 offset:45056\n"
    "v_accvgpr_read_b32 v108, a140\n"
    "v_mfma_f32_16x16x32_bf16 a[144:147], v[180:183], v[80:83], a[144:147]\n"
    "v_accvgpr_read_b32 v109, a141\n"
    "v_accvgpr_read_b32 v110, a142\n"
    "v_mfma_f32_16x16x32_bf16 a[148:151], v[176:179], v[236:239], a[148:151]\n"
    "v_accvgpr_read_b32 v111, a143\n"
    "v_fma_f32 v108, v108, v228, s94\n"
    "v_mfma_f32_16x16x32_bf16 a[148:151], v[180:183], v[84:87], a[148:151]\n"
    "v_fma_f32 v109, v109, v228, s94\n"
    "v_fma_f32 v110, v110, v228, s94\n"
    "v_mfma_f32_16x16x32_bf16 a[152:155], v[176:179], v[240:243], a[152:155]\n"
    "ds_read_b128 v[172:175], v201 offset:46080\n"
    "v_fma_f32 v111, v111, v228, s94\n"
    "v_mfma_f32_16x16x32_bf16 a[152:155], v[180:183], v[88:91], a[152:155]\n"
    "v_cvt_pk_bf16_f32 v60, v108, v109\n"
    "v_cvt_pk_bf16_f32 v61, v110, v111\n"
    "v_mfma_f32_16x16x32_bf16 a[156:159], v[176:179], v[244:247], a[156:159]\n"
    "v_mfma_f32_16x16x32_bf16 a[156:159], v[180:183], v[92:95], a[156:159]\n"
    "s_waitcnt lgkmcnt(2)\n"
    "v_accvgpr_read_b32 v96, a144\n"
    "v_accvgpr_read_b32 v97, a145\n"
    "v_mfma_f32_16x16x32_bf16 a[160:163], v[160:163], v[128:131], a[160:163]\n"
    "ds_read_b128 v[176:179], v207 offset:20480\n"
    "v_accvgpr_read_b32 v98, a146\n"
    "v_mfma_f32_16x16x32_bf16 a[160:163], v[164:167], v[144:147], a[160:163]\n"
    "v_accvgpr_read_b32 v99, a147\n"
    "v_fma_f32 v96, v96, v222, s94\n"
    "v_mfma_f32_16x16x32_bf16 a[164:167], v[160:163], v[132:135], a[164:167]\n"
    "v_fma_f32 v97, v97, v222, s94\n"
    "v_fma_f32 v98, v98, v222, s94\n"
    "v_mfma_f32_16x16x32_bf16 a[164:167], v[164:167], v[148:151], a[164:167]\n"
    "v_fma_f32 v99, v99, v222, s94\n"
    "v_cvt_pk_bf16_f32 v50, v96, v97\n"
    "v_mfma_f32_16x16x32_bf16 a[168:171], v[160:163], v[136:139], a[168:171]\n"
    "ds_read_b128 v[180:183], v207 offset:21504\n"
    "v_cvt_pk_bf16_f32 v51, v98, v99\n"
    "v_mfma_f32_16x16x32_bf16 a[168:171], v[164:167], v[152:155], a[168:171]\n"
    "buffer_store_dwordx4 v[48:51], v205, s[28:31], 0 offen offset:256 sc0 sc1\n"
    "v_accvgpr_read_b32 v100, a148\n"
    "v_accvgpr_read_b32 v101, a149\n"
    "v_accvgpr_read_b32 v102, a150\n"
    "v_mfma_f32_16x16x32_bf16 a[172:175], v[160:163], v[140:143], a[172:175]\n"
    "v_accvgpr_read_b32 v103, a151\n"
    "v_fma_f32 v100, v100, v224, s94\n"
    "v_mfma_f32_16x16x32_bf16 a[172:175], v[164:167], v[156:159], a[172:175]\n"
    "s_waitcnt lgkmcnt(2)\n"
    "v_fma_f32 v101, v101, v224, s94\n"
    "v_fma_f32 v102, v102, v224, s94\n"
    "v_mfma_f32_16x16x32_bf16 a[172:175], v[168:171], v[44:47], a[172:175]\n"
    "ds_read_b128 v[160:163], v201 offset:22528\n"
    "v_fma_f32 v103, v103, v224, s94\n"
    "v_mfma_f32_16x16x32_bf16 a[172:175], v[172:175], v[76:79], a[172:175]\n"
    "v_cvt_pk_bf16_f32 v54, v100, v101\n"
    "v_cvt_pk_bf16_f32 v55, v102, v103\n"
    "v_mfma_f32_16x16x32_bf16 a[168:171], v[168:171], v[40:43], a[168:171]\n"
    "buffer_store_dwordx4 v[52:55], v205, s[28:31], s62 offen offset:256 sc0 sc1\n"
    "v_accvgpr_read_b32 v104, a152\n"
    "v_accvgpr_read_b32 v105, a153\n"
    "v_accvgpr_read_b32 v106, a154\n"
    "v_mfma_f32_16x16x32_bf16 a[168:171], v[172:175], v[72:75], a[168:171]\n"
    "v_accvgpr_read_b32 v107, a155\n"
    "v_fma_f32 v104, v104, v226, s94\n"
    "v_mfma_f32_16x16x32_bf16 a[164:167], v[168:171], v[36:39], a[164:167]\n"
    "ds_read_b128 v[164:167], v201 offset:23552\n"
    "v_fma_f32 v105, v105, v226, s94\n"
    "v_mfma_f32_16x16x32_bf16 a[164:167], v[172:175], v[68:71], a[164:167]\n"
    "v_fma_f32 v106, v106, v226, s94\n"
    "v_fma_f32 v107, v107, v226, s94\n"
    "v_mfma_f32_16x16x32_bf16 a[160:163], v[168:171], v[32:35], a[160:163]\n"
    "v_cvt_pk_bf16_f32 v58, v104, v105\n"
    "v_cvt_pk_bf16_f32 v59, v106, v107\n"
    "v_mfma_f32_16x16x32_bf16 a[160:163], v[172:175], v[64:67], a[160:163]\n"
    "s_waitcnt lgkmcnt(2)\n"
    "buffer_store_dwordx4 v[56:59], v205, s[28:31], s63 offen offset:256 sc0 sc1\n"
    "v_accvgpr_read_b32 v108, a156\n"
    "v_accvgpr_read_b32 v109, a157\n"
    "v_accvgpr_read_b32 v110, a158\n"
    "v_mfma_f32_16x16x32_bf16 a[160:163], v[176:179], v[232:235], a[160:163]\n"
    "ds_read_b128 v[168:171], v201 offset:47104\n"
    "v_accvgpr_read_b32 v111, a159\n"
    "v_mfma_f32_16x16x32_bf16 a[160:163], v[180:183], v[80:83], a[160:163]\n"
    "v_fma_f32 v108, v108, v228, s94\n"
    "v_fma_f32 v109, v109, v228, s94\n"
    "v_mfma_f32_16x16x32_bf16 a[164:167], v[176:179], v[236:239], a[164:167]\n"
    "v_fma_f32 v110, v110, v228, s94\n"
    "v_fma_f32 v111, v111, v228, s94\n"
    "v_mfma_f32_16x16x32_bf16 a[164:167], v[180:183], v[84:87], a[164:167]\n"
    "v_cvt_pk_bf16_f32 v62, v108, v109\n"
    "v_cvt_pk_bf16_f32 v63, v110, v111\n"
    "v_mfma_f32_16x16x32_bf16 a[168:171], v[176:179], v[240:243], a[168:171]\n"
    "ds_read_b128 v[172:175], v201 offset:48128\n"
    "v_mfma_f32_16x16x32_bf16 a[168:171], v[180:183], v[88:91], a[168:171]\n"
    "buffer_store_dwordx4 v[60:63], v205, s[28:31], s64 offen offset:256 sc0 sc1\n"
    "v_mfma_f32_16x16x32_bf16 a[172:175], v[176:179], v[244:247], a[172:175]\n"
    "v_mfma_f32_16x16x32_bf16 a[172:175], v[180:183], v[92:95], a[172:175]\n"
    "s_waitcnt lgkmcnt(2)\n"
    "v_accvgpr_read_b32 v96, a160\n"
    "v_accvgpr_read_b32 v97, a161\n"
    "v_mfma_f32_16x16x32_bf16 a[176:179], v[160:163], v[128:131], a[176:179]\n"
    "ds_read_b128 v[176:179], v207 offset:22528\n"
    "v_accvgpr_read_b32 v98, a162\n"
    "v_mfma_f32_16x16x32_bf16 a[176:179], v[164:167], v[144:147], a[176:179]\n"
    "v_accvgpr_read_b32 v99, a163\n"
    "v_fma_f32 v96, v96, v222, s94\n"
    "v_mfma_f32_16x16x32_bf16 a[180:183], v[160:163], v[132:135], a[180:183]\n"
    "v_fma_f32 v97, v97, v222, s94\n"
    "v_fma_f32 v98, v98, v222, s94\n"
    "v_mfma_f32_16x16x32_bf16 a[180:183], v[164:167], v[148:151], a[180:183]\n"
    "v_fma_f32 v99, v99, v222, s94\n"
    "v_cvt_pk_bf16_f32 v184, v96, v97\n"
    "v_mfma_f32_16x16x32_bf16 a[184:187], v[160:163], v[136:139], a[184:187]\n"
    "ds_read_b128 v[180:183], v207 offset:23552\n"
    "v_cvt_pk_bf16_f32 v185, v98, v99\n"
    "v_mfma_f32_16x16x32_bf16 a[184:187], v[164:167], v[152:155], a[184:187]\n"
    "v_accvgpr_read_b32 v100, a164\n"
    "v_accvgpr_read_b32 v101, a165\n"
    "v_mfma_f32_16x16x32_bf16 a[188:191], v[160:163], v[140:143], a[188:191]\n"
    "v_accvgpr_read_b32 v102, a166\n"
    "v_accvgpr_read_b32 v103, a167\n"
    "v_mfma_f32_16x16x32_bf16 a[188:191], v[164:167], v[156:159], a[188:191]\n"
    "s_waitcnt lgkmcnt(2)\n"
    "v_fma_f32 v100, v100, v224, s94\n"
    "v_fma_f32 v101, v101, v224, s94\n"
    "v_mfma_f32_16x16x32_bf16 a[188:191], v[168:171], v[44:47], a[188:191]\n"
    "v_fma_f32 v102, v102, v224, s94\n"
    "v_fma_f32 v103, v103, v224, s94\n"
    "v_mfma_f32_16x16x32_bf16 a[188:191], v[172:175], v[76:79], a[188:191]\n"
    "v_cvt_pk_bf16_f32 v188, v100, v101\n"
    "v_cvt_pk_bf16_f32 v189, v102, v103\n"
    "v_mfma_f32_16x16x32_bf16 a[184:187], v[168:171], v[40:43], a[184:187]\n"
    "v_accvgpr_read_b32 v104, a168\n"
    "v_accvgpr_read_b32 v105, a169\n"
    "v_mfma_f32_16x16x32_bf16 a[184:187], v[172:175], v[72:75], a[184:187]\n"
    "v_accvgpr_read_b32 v106, a170\n"
    "v_accvgpr_read_b32 v107, a171\n"
    "v_mfma_f32_16x16x32_bf16 a[180:183], v[168:171], v[36:39], a[180:183]\n"
    "v_fma_f32 v104, v104, v226, s94\n"
    "v_fma_f32 v105, v105, v226, s94\n"
    "v_mfma_f32_16x16x32_bf16 a[180:183], v[172:175], v[68:71], a[180:183]\n"
    "v_fma_f32 v106, v106, v226, s94\n"
    "v_fma_f32 v107, v107, v226, s94\n"
    "v_mfma_f32_16x16x32_bf16 a[176:179], v[168:171], v[32:35], a[176:179]\n"
    "v_cvt_pk_bf16_f32 v248, v104, v105\n"
    "v_cvt_pk_bf16_f32 v249, v106, v107\n"
    "v_mfma_f32_16x16x32_bf16 a[176:179], v[172:175], v[64:67], a[176:179]\n"
    "s_waitcnt lgkmcnt(0)\n"
    "v_accvgpr_read_b32 v108, a172\n"
    "v_accvgpr_read_b32 v109, a173\n"
    "v_mfma_f32_16x16x32_bf16 a[176:179], v[176:179], v[232:235], a[176:179]\n"
    "v_accvgpr_read_b32 v110, a174\n"
    "v_accvgpr_read_b32 v111, a175\n"
    "v_mfma_f32_16x16x32_bf16 a[176:179], v[180:183], v[80:83], a[176:179]\n"
    "v_fma_f32 v108, v108, v228, s94\n"
    "v_fma_f32 v109, v109, v228, s94\n"
    "v_mfma_f32_16x16x32_bf16 a[180:183], v[176:179], v[236:239], a[180:183]\n"
    "v_fma_f32 v110, v110, v228, s94\n"
    "v_fma_f32 v111, v111, v228, s94\n"
    "v_mfma_f32_16x16x32_bf16 a[180:183], v[180:183], v[84:87], a[180:183]\n"
    "v_cvt_pk_bf16_f32 v252, v108, v109\n"
    "v_cvt_pk_bf16_f32 v253, v110, v111\n"
    "v_mfma_f32_16x16x32_bf16 a[184:187], v[176:179], v[240:243], a[184:187]\n"
    "v_mfma_f32_16x16x32_bf16 a[184:187], v[180:183], v[88:91], a[184:187]\n"
    "v_mfma_f32_16x16x32_bf16 a[188:191], v[176:179], v[244:247], a[188:191]\n"
    "v_mfma_f32_16x16x32_bf16 a[188:191], v[180:183], v[92:95], a[188:191]\n"
    "v_accvgpr_read_b32 v96, a176\n"
    "v_accvgpr_read_b32 v97, a177\n"
    "v_accvgpr_read_b32 v98, a178\n"
    "v_accvgpr_read_b32 v99, a179\n"
    "v_fma_f32 v96, v96, v222, s94\n"
    "v_fma_f32 v97, v97, v222, s94\n"
    "v_fma_f32 v98, v98, v222, s94\n"
    "v_fma_f32 v99, v99, v222, s94\n"
    "v_cvt_pk_bf16_f32 v186, v96, v97\n"
    "v_cvt_pk_bf16_f32 v187, v98, v99\n"
    "buffer_store_dwordx4 v[184:187], v205, s[28:31], 0 offen offset:320 sc0 sc1\n"
    "v_accvgpr_read_b32 v100, a180\n"
    "v_accvgpr_read_b32 v101, a181\n"
    "v_accvgpr_read_b32 v102, a182\n"
    "v_accvgpr_read_b32 v103, a183\n"
    "v_fma_f32 v100, v100, v224, s94\n"
    "v_fma_f32 v101, v101, v224, s94\n"
    "v_fma_f32 v102, v102, v224, s94\n"
    "v_fma_f32 v103, v103, v224, s94\n"
    "v_cvt_pk_bf16_f32 v190, v100, v101\n"
    "v_cvt_pk_bf16_f32 v191, v102, v103\n"
    "buffer_store_dwordx4 v[188:191], v205, s[28:31], s62 offen offset:320 sc0 sc1\n"
    "v_accvgpr_read_b32 v104, a184\n"
    "v_accvgpr_read_b32 v105, a185\n"
    "v_accvgpr_read_b32 v106, a186\n"
    "v_accvgpr_read_b32 v107, a187\n"
    "v_fma_f32 v104, v104, v226, s94\n"
    "v_fma_f32 v105, v105, v226, s94\n"
    "v_fma_f32 v106, v106, v226, s94\n"
    "v_fma_f32 v107, v107, v226, s94\n"
    "v_cvt_pk_bf16_f32 v250, v104, v105\n"
    "v_cvt_pk_bf16_f32 v251, v106, v107\n"
    "buffer_store_dwordx4 v[248:251], v205, s[28:31], s63 offen offset:320 sc0 sc1\n"
    "v_accvgpr_read_b32 v108, a188\n"
    "v_accvgpr_read_b32 v109, a189\n"
    "v_accvgpr_read_b32 v110, a190\n"
    "v_accvgpr_read_b32 v111, a191\n"
    "v_fma_f32 v108, v108, v228, s94\n"
    "v_fma_f32 v109, v109, v228, s94\n"
    "v_fma_f32 v110, v110, v228, s94\n"
    "v_fma_f32 v111, v111, v228, s94\n"
    "v_cvt_pk_bf16_f32 v254, v108, v109\n"
    "v_cvt_pk_bf16_f32 v255, v110, v111\n"
    "buffer_store_dwordx4 v[252:255], v205, s[28:31], s64 offen offset:320 sc0 sc1\n"
    "v_add_f32 v196, v112, v116\n"
    "v_add_f32 v197, v120, v124\n"
    "v_add_f32 v196, v196, v197\n"
    "v_cmp_ngt_f32 vcc, 0x45800000, v196\n"
    "s_barrier\n"
    "s_cbranch_vccnz LSLOW\n"
    "LFIN:\n"
    "s_branch LEND\n"
    "LSPRE0:\n"
    "LSPIN0:\n"
    "s_sleep 1\n"
    "buffer_load_dwordx2 v[218:219], v213, s[44:47], 0 offen sc1\n"
    "s_waitcnt vmcnt(0)\n"
    "s_add_u32 s73, s73, 1\n"
    "s_cmp_gt_u32 s73, 200000\n"
    "s_cbranch_scc1 LSPB0\n"
    "v_cmp_ne_u32 s[80:81], s67, v219\n"
    "v_cmp_gt_u32 vcc, s66, v218\n"
    "s_or_b64 vcc, vcc, s[80:81]\n"
    "s_cbranch_vccnz LSPIN0\n"
    "s_branch LSPB0\n"
    "LSPRE1:\n"
    "LSPIN1:\n"
    "s_sleep 1\n"
    "buffer_load_dwordx2 v[218:219], v213, s[44:47], 0 offen sc1\n"
    "s_waitcnt vmcnt(0)\n"
    "s_add_u32 s73, s73, 1\n"
    "s_cmp_gt_u32 s73, 200000\n"
    "s_cbranch_scc1 LSPB1\n"
    "v_cmp_ne_u32 s[80:81], s67, v219\n"
    "v_cmp_gt_u32 vcc, s66, v218\n"
    "s_or_b64 vcc, vcc, s[80:81]\n"
    "s_cbranch_vccnz LSPIN1\n"
    "s_branch LSPB1\n"
    "LSPRE2:\n"
    "LSPIN2:\n"
    "s_sleep 1\n"
    "buffer_load_dwordx2 v[218:219], v213, s[44:47], 0 offen sc1\n"
    "s_waitcnt vmcnt(0)\n"
    "s_add_u32 s73, s73, 1\n"
    "s_cmp_gt_u32 s73, 200000\n"
    "s_cbranch_scc1 LSPB2\n"
    "v_cmp_ne_u32 s[80:81], s67, v219\n"
    "v_cmp_gt_u32 vcc, s66, v218\n"
    "s_or_b64 vcc, vcc, s[80:81]\n"
    "s_cbranch_vccnz LSPIN2\n"
    "s_branch LSPB2\n"
    "LSPRE3:\n"
    "LSPIN3:\n"
    "s_sleep 1\n"
    "buffer_load_dwordx2 v[218:219], v213, s[44:47], 0 offen sc1\n"
    "s_waitcnt vmcnt(0)\n"
    "s_add_u32 s73, s73, 1\n"
    "s_cmp_gt_u32 s73, 200000\n"
    "s_cbranch_scc1 LSPB3\n"
    "v_cmp_ne_u32 s[80:81], s67, v219\n"
    "v_cmp_gt_u32 vcc, s66, v218\n"
    "s_or_b64 vcc, vcc, s[80:81]\n"
    "s_cbranch_vccnz LSPIN3\n"
    "s_branch LSPB3\n"
    "LSPRE4:\n"
    "LSPIN4:\n"
    "s_sleep 1\n"
    "buffer_load_dwordx2 v[218:219], v213, s[44:47], 0 offen sc1\n"
    "s_waitcnt vmcnt(0)\n"
    "s_add_u32 s73, s73, 1\n"
    "s_cmp_gt_u32 s73, 200000\n"
    "s_cbranch_scc1 LSPB4\n"
    "v_cmp_ne_u32 s[80:81], s67, v219\n"
    "v_cmp_gt_u32 vcc, s66, v218\n"
    "s_or_b64 vcc, vcc, s[80:81]\n"
    "s_cbranch_vccnz LSPIN4\n"
    "s_branch LSPB4\n"
    "LSPRE5:\n"
    "LSPIN5:\n"
    "s_sleep 1\n"
    "buffer_load_dwordx2 v[218:219], v213, s[44:47], 0 offen sc1\n"
    "s_waitcnt vmcnt(0)\n"
    "s_add_u32 s73, s73, 1\n"
    "s_cmp_gt_u32 s73, 200000\n"
    "s_cbranch_scc1 LSPB5\n"
    "v_cmp_ne_u32 s[80:81], s67, v219\n"
    "v_cmp_gt_u32 vcc, s66, v218\n"
    "s_or_b64 vcc, vcc, s[80:81]\n"
    "s_cbranch_vccnz LSPIN5\n"
    "s_branch LSPB5\n"
    "LSLOW:\n"
    "s_waitcnt vmcnt(0)\n"
    "s_nop 15\n"
    "s_mul_i32 s62, s58, 0x9000\n"
    "v_add_u32 v205, s62, v200\n"
    "s_mov_b32 s63, 0\n"
    "v_mov_b32 v128, 0\n"
    "v_mov_b32 v129, 0\n"
    "v_mov_b32 v130, 0\n"
    "v_mov_b32 v131, 0\n"
    "v_mov_b32 v96, 0xff61b1e6\n"
    "v_mov_b32 v100, 0xff61b1e6\n"
    "v_mov_b32 v104, 0xff61b1e6\n"
    "v_mov_b32 v108, 0xff61b1e6\n"
    "v_mfma_f32_16x16x32_bf16 a[0:3], v[128:131], v[128:131], 0\n"
    "v_mfma_f32_16x16x32_bf16 a[4:7], v[128:131], v[128:131], 0\n"
    "v_mfma_f32_16x16x32_bf16 a[8:11], v[128:131], v[128:131], 0\n"
    "v_mfma_f32_16x16x32_bf16 a[12:15], v[128:131], v[128:131], 0\n"
    "v_mfma_f32_16x16x32_bf16 a[16:19], v[128:131], v[128:131], 0\n"
    "v_mfma_f32_16x16x32_bf16 a[20:23], v[128:131], v[128:131], 0\n"
    "v_mfma_f32_16x16x32_bf16 a[24:27], v[128:131], v[128:131], 0\n"
    "v_mfma_f32_16x16x32_bf16 a[28:31], v[128:131], v[128:131], 0\n"
    "v_mfma_f32_16x16x32_bf16 a[32:35], v[128:131], v[128:131], 0\n"
    "v_mfma_f32_16x16x32_bf16 a[36:39], v[128:131], v[128:131], 0\n"
    "v_mfma_f32_16x16x32_bf16 a[40:43], v[128:131], v[128:131], 0\n"
    "v_mfma_f32_16x16x32_bf16 a[44:47], v[128:131], v[128:131], 0\n"
    "v_mfma_f32_16x16x32_bf16 a[48:51], v[128:131], v[128:131], 0\n"
    "v_mfma_f32_16x16x32_bf16 a[52:55], v[128:131], v[128:131], 0\n"
    "v_mfma_f32_16x16x32_bf16 a[56:59], v[128:131], v[128:131], 0\n"
    "v_mfma_f32_16x16x32_bf16 a[60:63], v[128:131], v[128:131], 0\n"
    "v_mfma_f32_16x16x32_bf16 a[64:67], v[128:131], v[128:131], 0\n"
    "v_mfma_f32_16x16x32_bf16 a[68:71], v[128:131], v[128:131], 0\n"
    "v_mfma_f32_16x16x32_bf16 a[72:75], v[128:131], v[128:131], 0\n"
    "v_mfma_f32_16x16x32_bf16 a[76:79], v[128:131], v[128:131], 0\n"
    "v_mfma_f32_16x16x32_bf16 a[80:83], v[128:131], v[128:131], 0\n"
    "v_mfma_f32_16x16x32_bf16 a[84:87], v[128:131], v[128:131], 0\n"
    "v_mfma_f32_16x16x32_bf16 a[88:91], v[128:131], v[128:131], 0\n"
    "v_mfma_f32_16x16x32_bf16 a[92:95], v[128:131], v[128:131], 0\n"
    "v_mfma_f32_16x16x32_bf16 a[96:99], v[128:131], v[128:131], 0\n"
    "v_mfma_f32_16x16x32_bf16 a[100:103], v[128:131], v[128:131], 0\n"
    "v_mfma_f32_16x16x32_bf16 a[104:107], v[128:131], v[128:131], 0\n"
    "v_mfma_f32_16x16x32_bf16 a[108:111], v[128:131], v[128:131], 0\n"
    "v_mfma_f32_16x16x32_bf16 a[112:115], v[128:131], v[128:131], 0\n"
    "v_mfma_f32_16x16x32_bf16 a[116:119], v[128:131], v[128:131], 0\n"
    "v_mfma_f32_16x16x32_bf16 a[120:123], v[128:131], v[128:131], 0\n"
    "v_mfma_f32_16x16x32_bf16 a[124:127], v[128:131], v[128:131], 0\n"
    "v_mfma_f32_16x16x32_bf16 a[128:131], v[128:131], v[128:131], 0\n"
    "v_mfma_f32_16x16x32_bf16 a[132:135], v[128:131], v[128:131], 0\n"
    "v_mfma_f32_16x16x32_bf16 a[136:139], v[128:131], v[128:131], 0\n"
    "v_mfma_f32_16x16x32_bf16 a[140:143], v[128:131], v[128:131], 0\n"
    "v_mfma_f32_16x16x32_bf16 a[144:147], v[128:131], v[128:131], 0\n"
    "v_mfma_f32_16x16x32_bf16 a[148:151], v[128:131], v[128:131], 0\n"
    "v_mfma_f32_16x16x32_bf16 a[152:155], v[128:131], v[128:131], 0\n"
    "v_mfma_f32_16x16x32_bf16 a[156:159], v[128:131], v[128:131], 0\n"
    "v_mfma_f32_16x16x32_bf16 a[160:163], v[128:131], v[128:131], 0\n"
    "v_mfma_f32_16x16x32_bf16 a[164:167], v[128:131], v[128:131], 0\n"
    "v_mfma_f32_16x16x32_bf16 a[168:171], v[128:131], v[128:131], 0\n"
    "v_mfma_f32_16x16x32_bf16 a[172:175], v[128:131], v[128:131], 0\n"
    "v_mfma_f32_16x16x32_bf16 a[176:179], v[128:131], v[128:131], 0\n"
    "v_mfma_f32_16x16x32_bf16 a[180:183], v[128:131], v[128:131], 0\n"
    "v_mfma_f32_16x16x32_bf16 a[184:187], v[128:131], v[128:131], 0\n"
    "v_mfma_f32_16x16x32_bf16 a[188:191], v[128:131], v[128:131], 0\n"
    "v_mfma_f32_16x16x32_bf16 v[112:115], v[128:131], v[128:131], 0\n"
    "v_mfma_f32_16x16x32_bf16 v[116:119], v[128:131], v[128:131], 0\n"
    "v_mfma_f32_16x16x32_bf16 v[120:123], v[128:131], v[128:131], 0\n"
    "v_mfma_f32_16x16x32_bf16 v[124:127], v[128:131], v[128:131], 0\n"
    "s_nop 15\n"
    "LSLOOP:\n"
    "s_add_u32 s78, s63, 0\n"
    "s_add_u32 m0, s62, 0\n"
    "s_nop 0\n"
    "buffer_load_dwordx4 v200, s[36:39], s78 offen sc1 lds\n"
    "buffer_load_dwordx4 v200, s[36:39], s78 offen offset:1024 sc1 lds\n"
    "buffer_load_dwordx4 v200, s[36:39], s78 offen offset:2048 sc1 lds\n"
    "buffer_load_dwordx4 v200, s[36:39], s78 offen offset:3072 sc1 lds\n"
    "s_add_u32 s78, s63, 0x1000\n"
    "s_add_u32 m0, s62, 0x1000\n"
    "s_nop 0\n"
    "buffer_load_dwordx4 v200, s[36:39], s78 offen sc1 lds\n"
    "buffer_load_dwordx4 v200, s[36:39], s78 offen offset:1024 sc1 lds\n"
    "buffer_load_dwordx4 v200, s[36:39], s78 offen offset:2048 sc1 lds\n"
    "buffer_load_dwordx4 v200, s[36:39], s78 offen offset:3072 sc1 lds\n"
    "s_add_u32 s78, s63, 0x2000\n"
    "s_add_u32 m0, s62, 0x2000\n"
    "s_nop 0\n"
    "buffer_load_dwordx4 v200, s[36:39], s78 offen sc1 lds\n"
    "buffer_load_dwordx4 v200, s[36:39], s78 offen offset:1024 sc1 lds\n"
    "buffer_load_dwordx4 v200, s[36:39], s78 offen offset:2048 sc1 lds\n"
    "buffer_load_dwordx4 v200, s[36:39], s78 offen offset:3072 sc1 lds\n"
    "s_add_u32 s78, s63, 0\n"
    "s_add_u32 m0, s62, 0x3000\n"
    "s_nop 0\n"
    "buffer_load_dwordx4 v200, s[40:43], s78 offen sc1 lds\n"
    "buffer_load_dwordx4 v200, s[40:43], s78 offen offset:1024 sc1 lds\n"
    "buffer_load_dwordx4 v200, s[40:43], s78 offen offset:2048 sc1 lds\n"
    "buffer_load_dwordx4 v200, s[40:43], s78 offen offset:3072 sc1 lds\n"
    "s_add_u32 s78, s63, 0x1000\n"
    "s_add_u32 m0, s62, 0x4000\n"
    "s_nop 0\n"
    "buffer_load_dwordx4 v200, s[40:43], s78 offen sc1 lds\n"
    "buffer_load_dwordx4 v200, s[40:43], s78 offen offset:1024 sc1 lds\n"
    "buffer_load_dwordx4 v200, s[40:43], s78 offen offset:2048 sc1 lds\n"
    "buffer_load_dwordx4 v200, s[40:43], s78 offen offset:3072 sc1 lds\n"
    "s_add_u32 s78, s63, 0x2000\n"
    "s_add_u32 m0, s62, 0x5000\n"
    "s_nop 0\n"
    "buffer_load_dwordx4 v200, s[40:43], s78 offen sc1 lds\n"
    "buffer_load_dwordx4 v200, s[40:43], s78 offen offset:1024 sc1 lds\n"
    "buffer_load_dwordx4 v200, s[40:43], s78 offen offset:2048 sc1 lds\n"
    "buffer_load_dwordx4 v200, s[40:43], s78 offen offset:3072 sc1 lds\n"
    "s_add_u32 s78, s63, 0x3000\n"
    "s_add_u32 m0, s62, 0x6000\n"
    "s_nop 0\n"
    "buffer_load_dwordx4 v200, s[40:43], s78 offen sc1 lds\n"
    "buffer_load_dwordx4 v200, s[40:43], s78 offen offset:1024 sc1 lds\n"
    "buffer_load_dwordx4 v200, s[40:43], s78 offen offset:2048 sc1 lds\n"
    "buffer_load_dwordx4 v200, s[40:43], s78 offen offset:3072 sc1 lds\n"
    "s_add_u32 s78, s63, 0x4000\n"
    "s_add_u32 m0, s62, 0x7000\n"
    "s_nop 0\n"
    "buffer_load_dwordx4 v200, s[40:43], s78 offen sc1 lds\n"
    "buffer_load_dwordx4 v200, s[40:43], s78 offen offset:1024 sc1 lds\n"
    "buffer_load_dwordx4 v200, s[40:43], s78 offen offset:2048 sc1 lds\n"
    "buffer_load_dwordx4 v200, s[40:43], s78 offen offset:3072 sc1 lds\n"
    "s_add_u32 s78, s63, 0x5000\n"
    "s_add_u32 m0, s62, 0x8000\n"
    "s_nop 0\n"
    "buffer_load_dwordx4 v200, s[40:43], s78 offen sc1 lds\n"
    "buffer_load_dwordx4 v200, s[40:43], s78 offen offset:1024 sc1 lds\n"
    "buffer_load_dwordx4 v200, s[40:43], s78 offen offset:2048 sc1 lds\n"
    "buffer_load_dwordx4 v200, s[40:43], s78 offen offset:3072 sc1 lds\n"
    "s_waitcnt vmcnt(0)\n"
    "ds_read_b128 v[160:163], v205\n"
    "ds_read_b128 v[164:167], v205 offset:1024\n"
    "s_waitcnt lgkmcnt(1)\n"
    "v_mfma_f32_16x16x32_bf16 v[32:35], v[160:163], a[192:195], 0\n"
    "s_waitcnt lgkmcnt(0)\n"
    "v_mfma_f32_16x16x32_bf16 v[32:35], v[164:167], a[196:199], v[32:35]\n"
    "v_mfma_f32_16x16x32_bf16 v[36:39], v[164:167], a[220:223], 0\n"
    "v_mfma_f32_16x16x32_bf16 v[36:39], v[160:163], a[216:219], v[36:39]\n"
    "v_mfma_f32_16x16x32_bf16 v[40:43], v[160:163], a[240:243], 0\n"
    "v_mfma_f32_16x16x32_bf16 v[40:43], v[164:167], a[244:247], v[40:43]\n"
    "v_mfma_f32_16x16x32_bf16 v[44:47], v[164:167], v[12:15], 0\n"
    "v_mfma_f32_16x16x32_bf16 v[44:47], v[160:163], v[8:11], v[44:47]\n"
    "ds_read_b128 v[168:171], v205 offset:2048\n"
    "ds_read_b128 v[172:175], v205 offset:3072\n"
    "s_waitcnt lgkmcnt(1)\n"
    "v_mfma_f32_16x16x32_bf16 v[44:47], v[168:171], v[16:19], v[44:47]\n"
    "s_waitcnt lgkmcnt(0)\n"
    "v_mfma_f32_16x16x32_bf16 v[44:47], v[172:175], v[20:23], v[44:47]\n"
    "v_mfma_f32_16x16x32_bf16 v[40:43], v[172:175], a[252:255], v[40:43]\n"
    "v_mfma_f32_16x16x32_bf16 v[40:43], v[168:171], a[248:251], v[40:43]\n"
    "v_mfma_f32_16x16x32_bf16 v[36:39], v[168:171], a[224:227], v[36:39]\n"
    "v_mfma_f32_16x16x32_bf16 v[36:39], v[172:175], a[228:231], v[36:39]\n"
    "v_mfma_f32_16x16x32_bf16 v[32:35], v[172:175], a[204:207], v[32:35]\n"
    "v_mfma_f32_16x16x32_bf16 v[32:35], v[168:171], a[200:203], v[32:35]\n"
    "ds_read_b128 v[176:179], v205 offset:4096\n"
    "ds_read_b128 v[180:183], v205 offset:5120\n"
    "s_waitcnt lgkmcnt(1)\n"
    "v_mfma_f32_16x16x32_bf16 v[32:35], v[176:179], a[208:211], v[32:35]\n"
    "s_waitcnt lgkmcnt(0)\n"
    "v_mfma_f32_16x16x32_bf16 v[32:35], v[180:183], a[212:215], v[32:35]\n"
    "v_mfma_f32_16x16x32_bf16 v[36:39], v[180:183], a[236:239], v[36:39]\n"
    "v_mfma_f32_16x16x32_bf16 v[36:39], v[176:179], a[232:235], v[36:39]\n"
    "v_mfma_f32_16x16x32_bf16 v[40:43], v[176:179], v[0:3], v[40:43]\n"
    "v_mfma_f32_16x16x32_bf16 v[40:43], v[180:183], v[4:7], v[40:43]\n"
    "v_mfma_f32_16x16x32_bf16 v[44:47], v[180:183], v[28:31], v[44:47]\n"
    "v_mfma_f32_16x16x32_bf16 v[44:47], v[176:179], v[24:27], v[44:47]\n"
    "ds_read_b128 v[160:163], v205 offset:10240\n"
    "ds_read_b128 v[164:167], v205 offset:11264\n"
    "s_waitcnt lgkmcnt(1)\n"
    "v_mfma_f32_16x16x32_bf16 v[60:63], v[160:163], v[24:27], 0\n"
    "s_waitcnt lgkmcnt(0)\n"
    "v_mfma_f32_16x16x32_bf16 v[60:63], v[164:167], v[28:31], v[60:63]\n"
    "v_mfma_f32_16x16x32_bf16 v[56:59], v[164:167], v[4:7], 0\n"
    "v_mfma_f32_16x16x32_bf16 v[56:59], v[160:163], v[0:3], v[56:59]\n"
    "v_mfma_f32_16x16x32_bf16 v[52:55], v[160:163], a[232:235], 0\n"
    "v_mfma_f32_16x16x32_bf16 v[52:55], v[164:167], a[236:239], v[52:55]\n"
    "v_mfma_f32_16x16x32_bf16 v[48:51], v[164:167], a[212:215], 0\n"
    "v_mfma_f32_16x16x32_bf16 v[48:51], v[160:163], a[208:211], v[48:51]\n"
    "ds_read_b128 v[168:171], v205 offset:8192\n"
    "ds_read_b128 v[172:175], v205 offset:9216\n"
    "s_waitcnt lgkmcnt(1)\n"
    "v_mfma_f32_16x16x32_bf16 v[48:51], v[168:171], a[200:203], v[48:51]\n"
    "s_waitcnt lgkmcnt(0)\n"
    "v_mfma_f32_16x16x32_bf16 v[48:51], v[172:175], a[204:207], v[48:51]\n"
    "v_mfma_f32_16x16x32_bf16 v[52:55], v[172:175], a[228:231], v[52:55]\n"
    "v_mfma_f32_16x16x32_bf16 v[52:55], v[168:171], a[224:227], v[52:55]\n"
    "v_mfma_f32_16x16x32_bf16 v[56:59], v[168:171], a[248:251], v[56:59]\n"
    "v_mfma_f32_16x16x32_bf16 v[56:59], v[172:175], a[252:255], v[56:59]\n"
    "v_mfma_f32_16x16x32_bf16 v[60:63], v[172:175], v[20:23], v[60:63]\n"
    "v_mfma_f32_16x16x32_bf16 v[60:63], v[168:171], v[16:19], v[60:63]\n"
    "ds_read_b128 v[176:179], v205 offset:6144\n"
    "ds_read_b128 v[180:183], v205 offset:7168\n"
    "s_waitcnt lgkmcnt(1)\n"
    "v_mfma_f32_16x16x32_bf16 v[60:63], v[176:179], v[8:11], v[60:63]\n"
    "s_waitcnt lgkmcnt(0)\n"
    "v_mfma_f32_16x16x32_bf16 v[60:63], v[180:183], v[12:15], v[60:63]\n"
    "v_mfma_f32_16x16x32_bf16 v[56:59], v[180:183], a[244:247], v[56:59]\n"
    "v_mfma_f32_16x16x32_bf16 v[56:59], v[176:179], a[240:243], v[56:59]\n"
    "v_mfma_f32_16x16x32_bf16 v[52:55], v[176:179], a[216:219], v[52:55]\n"
    "v_mfma_f32_16x16x32_bf16 v[52:55], v[180:183], a[220:223], v[52:55]\n"
    "v_mfma_f32_16x16x32_bf16 v[48:51], v[180:183], a[196:199], v[48:51]\n"
    "v_mfma_f32_16x16x32_bf16 v[48:51], v[176:179], a[192:195], v[48:51]\n"
    "s_add_u32 s78, s63, 0x3000\n"
    "s_add_u32 m0, s62, 0\n"
    "s_nop 0\n"
    "buffer_load_dwordx4 v200, s[36:39], s78 offen sc1 lds\n"
    "buffer_load_dwordx4 v200, s[36:39], s78 offen offset:1024 sc1 lds\n"
    "buffer_load_dwordx4 v200, s[36:39], s78 offen offset:2048 sc1 lds\n"
    "buffer_load_dwordx4 v200, s[36:39], s78 offen offset:3072 sc1 lds\n"
    "s_add_u32 s78, s63, 0x4000\n"
    "s_add_u32 m0, s62, 0x1000\n"
    "s_nop 0\n"
    "buffer_load_dwordx4 v200, s[36:39], s78 offen sc1 lds\n"
    "buffer_load_dwordx4 v200, s[36:39], s78 offen offset:1024 sc1 lds\n"
    "buffer_load_dwordx4 v200, s[36:39], s78 offen offset:2048 sc1 lds\n"
    "buffer_load_dwordx4 v200, s[36:39], s78 offen offset:3072 sc1 lds\n"
    "s_add_u32 s78, s63, 0x5000\n"
    "s_add_u32 m0, s62, 0x2000\n"
    "s_nop 0\n"
    "buffer_load_dwordx4 v200, s[36:39], s78 offen sc1 lds\n"
    "buffer_load_dwordx4 v200, s[36:39], s78 offen offset:1024 sc1 lds\n"
    "buffer_load_dwordx4 v200, s[36:39], s78 offen offset:2048 sc1 lds\n"
    "buffer_load_dwordx4 v200, s[36:39], s78 offen offset:3072 sc1 lds\n"
    "s_waitcnt vmcnt(0)\n"
    "ds_read_b128 v[160:163], v205\n"
    "ds_read_b128 v[164:167], v205 offset:1024\n"
    "s_waitcnt lgkmcnt(1)\n"
    "v_mfma_f32_16x16x32_bf16 v[64:67], v[160:163], a[192:195], 0\n"
    "s_waitcnt lgkmcnt(0)\n"
    "v_mfma_f32_16x16x32_bf16 v[64:67], v[164:167], a[196:199], v[64:67]\n"
    "v_mfma_f32_16x16x32_bf16 v[68:71], v[164:167], a[220:223], 0\n"
    "v_mfma_f32_16x16x32_bf16 v[68:71], v[160:163], a[216:219], v[68:71]\n"
    "v_mfma_f32_16x16x32_bf16 v[72:75], v[160:163], a[240:243], 0\n"
    "v_mfma_f32_16x16x32_bf16 v[72:75], v[164:167], a[244:247], v[72:75]\n"
    "v_mfma_f32_16x16x32_bf16 v[76:79], v[164:167], v[12:15], 0\n"
    "v_mfma_f32_16x16x32_bf16 v[76:79], v[160:163], v[8:11], v[76:79]\n"
    "ds_read_b128 v[168:171], v205 offset:2048\n"
    "ds_read_b128 v[172:175], v205 offset:3072\n"
    "s_waitcnt lgkmcnt(1)\n"
    "v_mfma_f32_16x16x32_bf16 v[76:79], v[168:171], v[16:19], v[76:79]\n"
    "s_waitcnt lgkmcnt(0)\n"
    "v_mfma_f32_16x16x32_bf16 v[76:79], v[172:175], v[20:23], v[76:79]\n"
    "v_mfma_f32_16x16x32_bf16 v[72:75], v[172:175], a[252:255], v[72:75]\n"
    "v_mfma_f32_16x16x32_bf16 v[72:75], v[168:171], a[248:251], v[72:75]\n"
    "v_mfma_f32_16x16x32_bf16 v[68:71], v[168:171], a[224:227], v[68:71]\n"
    "v_mfma_f32_16x16x32_bf16 v[68:71], v[172:175], a[228:231], v[68:71]\n"
    "v_mfma_f32_16x16x32_bf16 v[64:67], v[172:175], a[204:207], v[64:67]\n"
    "v_mfma_f32_16x16x32_bf16 v[64:67], v[168:171], a[200:203], v[64:67]\n"
    "ds_read_b128 v[176:179], v205 offset:4096\n"
    "ds_read_b128 v[180:183], v205 offset:5120\n"
    "s_waitcnt lgkmcnt(1)\n"
    "v_mfma_f32_16x16x32_bf16 v[64:67], v[176:179], a[208:211], v[64:67]\n"
    "s_waitcnt lgkmcnt(0)\n"
    "v_mfma_f32_16x16x32_bf16 v[64:67], v[180:183], a[212:215], v[64:67]\n"
    "v_mfma_f32_16x16x32_bf16 v[68:71], v[180:183], a[236:239], v[68:71]\n"
    "v_mfma_f32_16x16x32_bf16 v[68:71], v[176:179], a[232:235], v[68:71]\n"
    "v_mfma_f32_16x16x32_bf16 v[72:75], v[176:179], v[0:3], v[72:75]\n"
    "v_mfma_f32_16x16x32_bf16 v[72:75], v[180:183], v[4:7], v[72:75]\n"
    "v_mfma_f32_16x16x32_bf16 v[76:79], v[180:183], v[28:31], v[76:79]\n"
    "v_mfma_f32_16x16x32_bf16 v[76:79], v[176:179], v[24:27], v[76:79]\n"
    "ds_read_b128 v[160:163], v205 offset:10240\n"
    "ds_read_b128 v[164:167], v205 offset:11264\n"
    "s_waitcnt lgkmcnt(1)\n"
    "v_mfma_f32_16x16x32_bf16 v[92:95], v[160:163], v[24:27], 0\n"
    "s_waitcnt lgkmcnt(0)\n"
    "v_mfma_f32_16x16x32_bf16 v[92:95], v[164:167], v[28:31], v[92:95]\n"
    "v_mfma_f32_16x16x32_bf16 v[88:91], v[164:167], v[4:7], 0\n"
    "v_mfma_f32_16x16x32_bf16 v[88:91], v[160:163], v[0:3], v[88:91]\n"
    "v_mfma_f32_16x16x32_bf16 v[84:87], v[160:163], a[232:235], 0\n"
    "v_mfma_f32_16x16x32_bf16 v[84:87], v[164:167], a[236:239], v[84:87]\n"
    "v_mfma_f32_16x16x32_bf16 v[80:83], v[164:167], a[212:215], 0\n"
    "v_mfma_f32_16x16x32_bf16 v[80:83], v[160:163], a[208:211], v[80:83]\n"
    "ds_read_b128 v[168:171], v205 offset:8192\n"
    "ds_read_b128 v[172:175], v205 offset:9216\n"
    "s_waitcnt lgkmcnt(1)\n"
    "v_mfma_f32_16x16x32_bf16 v[80:83], v[168:171], a[200:203], v[80:83]\n"
    "s_waitcnt lgkmcnt(0)\n"
    "v_mfma_f32_16x16x32_bf16 v[80:83], v[172:175], a[204:207], v[80:83]\n"
    "v_mfma_f32_16x16x32_bf16 v[84:87], v[172:175], a[228:231], v[84:87]\n"
    "v_mfma_f32_16x16x32_bf16 v[84:87], v[168:171], a[224:227], v[84:87]\n"
    "v_mfma_f32_16x16x32_bf16 v[88:91], v[168:171], a[248:251], v[88:91]\n"
    "v_mfma_f32_16x16x32_bf16 v[88:91], v[172:175], a[252:255], v[88:91]\n"
    "v_mfma_f32_16x16x32_bf16 v[92:95], v[172:175], v[20:23], v[92:95]\n"
    "v_mfma_f32_16x16x32_bf16 v[92:95], v[168:171], v[16:19], v[92:95]\n"
    "ds_read_b128 v[176:179], v205 offset:6144\n"
    "ds_read_b128 v[180:183], v205 offset:7168\n"
    "s_waitcnt lgkmcnt(1)\n"
    "v_mfma_f32_16x16x32_bf16 v[92:95], v[176:179], v[8:11], v[92:95]\n"
    "s_waitcnt lgkmcnt(0)\n"
    "v_mfma_f32_16x16x32_bf16 v[92:95], v[180:183], v[12:15], v[92:95]\n"
    "v_mfma_f32_16x16x32_bf16 v[88:91], v[180:183], a[244:247], v[88:91]\n"
    "v_mfma_f32_16x16x32_bf16 v[88:91], v[176:179], a[240:243], v[88:91]\n"
    "v_mfma_f32_16x16x32_bf16 v[84:87], v[176:179], a[216:219], v[84:87]\n"
    "v_mfma_f32_16x16x32_bf16 v[84:87], v[180:183], a[220:223], v[84:87]\n"
    "v_mfma_f32_16x16x32_bf16 v[80:83], v[180:183], a[196:199], v[80:83]\n"
    "v_mfma_f32_16x16x32_bf16 v[80:83], v[176:179], a[192:195], v[80:83]\n"
    "v_max3_f32 v196, v32, v33, v34\n"
    "v_max3_f32 v196, v196, v35, v48\n"
    "v_max3_f32 v196, v196, v49, v50\n"
    "v_max3_f32 v196, v196, v51, v64\n"
    "v_max3_f32 v196, v196, v65, v66\n"
    "s_nop 2\n"
    "v_max3_f32 v196, v196, v67, v80\n"
    "v_max3_f32 v196, v196, v81, v82\n"
    "v_max_f32 v196, v196, v83\n"
    "v_mov_b32 v197, v196\n"
    "s_nop 1\n"
    "v_permlane32_swap_b32 v196, v197\n"
    "v_max_f32 v196, v196, v197\n"
    "v_mov_b32 v197, v196\n"
    "s_nop 1\n"
    "v_permlane16_swap_b32 v196, v197\n"
    "v_max_f32 v196, v196, v197\n"
    "v_max_f32 v196, v196, v96\n"
    "v_sub_f32 v197, v96, v196\n"
    "v_mov_b32 v96, v196\n"
    "v_exp_f32 v197, v197\n"
    "v_sub_f32 v32, v32, v196\n"
    "v_sub_f32 v33, v33, v196\n"
    "v_sub_f32 v34, v34, v196\n"
    "v_sub_f32 v35, v35, v196\n"
    "v_sub_f32 v48, v48, v196\n"
    "v_sub_f32 v49, v49, v196\n"
    "v_sub_f32 v50, v50, v196\n"
    "v_sub_f32 v51, v51, v196\n"
    "v_sub_f32 v64, v64, v196\n"
    "v_sub_f32 v65, v65, v196\n"
    "v_sub_f32 v66, v66, v196\n"
    "v_sub_f32 v67, v67, v196\n"
    "v_sub_f32 v80, v80, v196\n"
    "v_sub_f32 v81, v81, v196\n"
    "v_sub_f32 v82, v82, v196\n"
    "v_sub_f32 v83, v83, v196\n"
    "v_mul_f32 v112, v112, v197\n"
    "v_mul_f32 v113, v113, v197\n"
    "v_mul_f32 v114, v114, v197\n"
    "v_mul_f32 v115, v115, v197\n"
    "v_accvgpr_read_b32 v198, a0\n"
    "v_accvgpr_read_b32 v199, a1\n"
    "v_mul_f32 v198, v198, v197\n"
    "v_mul_f32 v199, v199, v197\n"
    "v_accvgpr_write_b32 a0, v198\n"
    "v_accvgpr_write_b32 a1, v199\n"
    "v_accvgpr_read_b32 v198, a2\n"
    "v_accvgpr_read_b32 v199, a3\n"
    "v_mul_f32 v198, v198, v197\n"
    "v_mul_f32 v199, v199, v197\n"
    "v_accvgpr_write_b32 a2, v198\n"
    "v_accvgpr_write_b32 a3, v199\n"
    "v_accvgpr_read_b32 v198, a16\n"
    "v_accvgpr_read_b32 v199, a17\n"
    "v_mul_f32 v198, v198, v197\n"
    "v_mul_f32 v199, v199, v197\n"
    "v_accvgpr_write_b32 a16, v198\n"
    "v_accvgpr_write_b32 a17, v199\n"
    "v_accvgpr_read_b32 v198, a18\n"
    "v_accvgpr_read_b32 v199, a19\n"
    "v_mul_f32 v198, v198, v197\n"
    "v_mul_f32 v199, v199, v197\n"
    "v_accvgpr_write_b32 a18, v198\n"
    "v_accvgpr_write_b32 a19, v199\n"
    "v_accvgpr_read_b32 v198, a32\n"
    "v_accvgpr_read_b32 v199, a33\n"
    "v_mul_f32 v198, v198, v197\n"
    "v_mul_f32 v199, v199, v197\n"
    "v_accvgpr_write_b32 a32, v198\n"
    "v_accvgpr_write_b32 a33, v199\n"
    "v_accvgpr_read_b32 v198, a34\n"
    "v_accvgpr_read_b32 v199, a35\n"
    "v_mul_f32 v198, v198, v197\n"
    "v_mul_f32 v199, v199, v197\n"
    "v_accvgpr_write_b32 a34, v198\n"
    "v_accvgpr_write_b32 a35, v199\n"
    "v_accvgpr_read_b32 v198, a48\n"
    "v_accvgpr_read_b32 v199, a49\n"
    "v_mul_f32 v198, v198, v197\n"
    "v_mul_f32 v199, v199, v197\n"
    "v_accvgpr_write_b32 a48, v198\n"
    "v_accvgpr_write_b32 a49, v199\n"
    "v_accvgpr_read_b32 v198, a50\n"
    "v_accvgpr_read_b32 v199, a51\n"
    "v_mul_f32 v198, v198, v197\n"
    "v_mul_f32 v199, v199, v197\n"
    "v_accvgpr_write_b32 a50, v198\n"
    "v_accvgpr_write_b32 a51, v199\n"
    "v_accvgpr_read_b32 v198, a64\n"
    "v_accvgpr_read_b32 v199, a65\n"
    "v_mul_f32 v198, v198, v197\n"
    "v_mul_f32 v199, v199, v197\n"
    "v_accvgpr_write_b32 a64, v198\n"
    "v_accvgpr_write_b32 a65, v199\n"
    "v_accvgpr_read_b32 v198, a66\n"
    "v_accvgpr_read_b32 v199, a67\n"
    "v_mul_f32 v198, v198, v197\n"
    "v_mul_f32 v199, v199, v197\n"
    "v_accvgpr_write_b32 a66, v198\n"
    "v_accvgpr_write_b32 a67, v199\n"
    "v_accvgpr_read_b32 v198, a80\n"
    "v_accvgpr_read_b32 v199, a81\n"
    "v_mul_f32 v198, v198, v197\n"
    "v_mul_f32 v199, v199, v197\n"
    "v_accvgpr_write_b32 a80, v198\n"
    "v_accvgpr_write_b32 a81, v199\n"
    "v_accvgpr_read_b32 v198, a82\n"
    "v_accvgpr_read_b32 v199, a83\n"
    "v_mul_f32 v198, v198, v197\n"
    "v_mul_f32 v199, v199, v197\n"
    "v_accvgpr_write_b32 a82, v198\n"
    "v_accvgpr_write_b32 a83, v199\n"
    "v_accvgpr_read_b32 v198, a96\n"
    "v_accvgpr_read_b32 v199, a97\n"
    "v_mul_f32 v198, v198, v197\n"
    "v_mul_f32 v199, v199, v197\n"
    "v_accvgpr_write_b32 a96, v198\n"
    "v_accvgpr_write_b32 a97, v199\n"
    "v_accvgpr_read_b32 v198, a98\n"
    "v_accvgpr_read_b32 v199, a99\n"
    "v_mul_f32 v198, v198, v197\n"
    "v_mul_f32 v199, v199, v197\n"
    "v_accvgpr_write_b32 a98, v198\n"
    "v_accvgpr_write_b32 a99, v199\n"
    "v_accvgpr_read_b32 v198, a112\n"
    "v_accvgpr_read_b32 v199, a113\n"
    "v_mul_f32 v198, v198, v197\n"
    "v_mul_f32 v199, v199, v197\n"
    "v_accvgpr_write_b32 a112, v198\n"
    "v_accvgpr_write_b32 a113, v199\n"
    "v_accvgpr_read_b32 v198, a114\n"
    "v_accvgpr_read_b32 v199, a115\n"
    "v_mul_f32 v198, v198, v197\n"
    "v_mul_f32 v199, v199, v197\n"
    "v_accvgpr_write_b32 a114, v198\n"
    "v_accvgpr_write_b32 a115, v199\n"
    "v_accvgpr_read_b32 v198, a128\n"
    "v_accvgpr_read_b32 v199, a129\n"
    "v_mul_f32 v198, v198, v197\n"
    "v_mul_f32 v199, v199, v197\n"
    "v_accvgpr_write_b32 a128, v198\n"
    "v_accvgpr_write_b32 a129, v199\n"
    "v_accvgpr_read_b32 v198, a130\n"
    "v_accvgpr_read_b32 v199, a131\n"
    "v_mul_f32 v198, v198, v197\n"
    "v_mul_f32 v199, v199, v197\n"
    "v_accvgpr_write_b32 a130, v198\n"
    "v_accvgpr_write_b32 a131, v199\n"
    "v_accvgpr_read_b32 v198, a144\n"
    "v_accvgpr_read_b32 v199, a145\n"
    "v_mul_f32 v198, v198, v197\n"
    "v_mul_f32 v199, v199, v197\n"
    "v_accvgpr_write_b32 a144, v198\n"
    "v_accvgpr_write_b32 a145, v199\n"
    "v_accvgpr_read_b32 v198, a146\n"
    "v_accvgpr_read_b32 v199, a147\n"
    "v_mul_f32 v198, v198, v197\n"
    "v_mul_f32 v199, v199, v197\n"
    "v_accvgpr_write_b32 a146, v198\n"
    "v_accvgpr_write_b32 a147, v199\n"
    "v_accvgpr_read_b32 v198, a160\n"
    "v_accvgpr_read_b32 v199, a161\n"
    "v_mul_f32 v198, v198, v197\n"
    "v_mul_f32 v199, v199, v197\n"
    "v_accvgpr_write_b32 a160, v198\n"
    "v_accvgpr_write_b32 a161, v199\n"
    "v_accvgpr_read_b32 v198, a162\n"
    "v_accvgpr_read_b32 v199, a163\n"
    "v_mul_f32 v198, v198, v197\n"
    "v_mul_f32 v199, v199, v197\n"
    "v_accvgpr_write_b32 a162, v198\n"
    "v_accvgpr_write_b32 a163, v199\n"
    "v_accvgpr_read_b32 v198, a176\n"
    "v_accvgpr_read_b32 v199, a177\n"
    "v_mul_f32 v198, v198, v197\n"
    "v_mul_f32 v199, v199, v197\n"
    "v_accvgpr_write_b32 a176, v198\n"
    "v_accvgpr_write_b32 a177, v199\n"
    "v_accvgpr_read_b32 v198, a178\n"
    "v_accvgpr_read_b32 v199, a179\n"
    "v_mul_f32 v198, v198, v197\n"
    "v_mul_f32 v199, v199, v197\n"
    "v_accvgpr_write_b32 a178, v198\n"
    "v_accvgpr_write_b32 a179, v199\n"
    "v_max3_f32 v196, v36, v37, v38\n"
    "v_max3_f32 v196, v196, v39, v52\n"
    "v_max3_f32 v196, v196, v53, v54\n"
    "v_max3_f32 v196, v196, v55, v68\n"
    "v_max3_f32 v196, v196, v69, v70\n"
    "v_max3_f32 v196, v196, v71, v84\n"
    "v_max3_f32 v196, v196, v85, v86\n"
    "v_max_f32 v196, v196, v87\n"
    "v_mov_b32 v197, v196\n"
    "s_nop 1\n"
    "v_permlane32_swap_b32 v196, v197\n"
    "v_max_f32 v196, v196, v197\n"
    "v_mov_b32 v197, v196\n"
    "s_nop 1\n"
    "v_permlane16_swap_b32 v196, v197\n"
    "v_max_f32 v196, v196, v197\n"
    "v_max_f32 v196, v196, v100\n"
    "v_sub_f32 v197, v100, v196\n"
    "v_mov_b32 v100, v196\n"
    "v_exp_f32 v197, v197\n"
    "v_sub_f32 v36, v36, v196\n"
    "v_sub_f32 v37, v37, v196\n"
    "v_sub_f32 v38, v38, v196\n"
    "v_sub_f32 v39, v39, v196\n"
    "v_sub_f32 v52, v52, v196\n"
    "v_sub_f32 v53, v53, v196\n"
    "v_sub_f32 v54, v54, v196\n"
    "v_sub_f32 v55, v55, v196\n"
    "v_sub_f32 v68, v68, v196\n"
    "v_sub_f32 v69, v69, v196\n"
    "v_sub_f32 v70, v70, v196\n"
    "v_sub_f32 v71, v71, v196\n"
    "v_sub_f32 v84, v84, v196\n"
    "v_sub_f32 v85, v85, v196\n"
    "v_sub_f32 v86, v86, v196\n"
    "v_sub_f32 v87, v87, v196\n"
    "v_mul_f32 v116, v116, v197\n"
    "v_mul_f32 v117, v117, v197\n"
    "v_mul_f32 v118, v118, v197\n"
    "v_mul_f32 v119, v119, v197\n"
    "v_accvgpr_read_b32 v198, a4\n"
    "v_accvgpr_read_b32 v199, a5\n"
    "v_mul_f32 v198, v198, v197\n"
    "v_mul_f32 v199, v199, v197\n"
    "v_accvgpr_write_b32 a4, v198\n"
    "v_accvgpr_write_b32 a5, v199\n"
    "v_accvgpr_read_b32 v198, a6\n"
    "v_accvgpr_read_b32 v199, a7\n"
    "v_mul_f32 v198, v198, v197\n"
    "v_mul_f32 v199, v199, v197\n"
    "v_accvgpr_write_b32 a6, v198\n"
    "v_accvgpr_write_b32 a7, v199\n"
    "v_accvgpr_read_b32 v198, a20\n"
    "v_accvgpr_read_b32 v199, a21\n"
    "v_mul_f32 v198, v198, v197\n"
    "v_mul_f32 v199, v199, v197\n"
    "v_accvgpr_write_b32 a20, v198\n"
    "v_accvgpr_write_b32 a21, v199\n"
    "v_accvgpr_read_b32 v198, a22\n"
    "v_accvgpr_read_b32 v199, a23\n"
    "v_mul_f32 v198, v198, v197\n"
    "v_mul_f32 v199, v199, v197\n"
    "v_accvgpr_write_b32 a22, v198\n"
    "v_accvgpr_write_b32 a23, v199\n"
    "v_accvgpr_read_b32 v198, a36\n"
    "v_accvgpr_read_b32 v199, a37\n"
    "v_mul_f32 v198, v198, v197\n"
    "v_mul_f32 v199, v199, v197\n"
    "v_accvgpr_write_b32 a36, v198\n"
    "v_accvgpr_write_b32 a37, v199\n"
    "v_accvgpr_read_b32 v198, a38\n"
    "v_accvgpr_read_b32 v199, a39\n"
    "v_mul_f32 v198, v198, v197\n"
    "v_mul_f32 v199, v199, v197\n"
    "v_accvgpr_write_b32 a38, v198\n"
    "v_accvgpr_write_b32 a39, v199\n"
    "v_accvgpr_read_b32 v198, a52\n"
    "v_accvgpr_read_b32 v199, a53\n"
    "v_mul_f32 v198, v198, v197\n"
    "v_mul_f32 v199, v199, v197\n"
    "v_accvgpr_write_b32 a52, v198\n"
    "v_accvgpr_write_b32 a53, v199\n"
    "v_accvgpr_read_b32 v198, a54\n"
    "v_accvgpr_read_b32 v199, a55\n"
    "v_mul_f32 v198, v198, v197\n"
    "v_mul_f32 v199, v199, v197\n"
    "v_accvgpr_write_b32 a54, v198\n"
    "v_accvgpr_write_b32 a55, v199\n"
    "v_accvgpr_read_b32 v198, a68\n"
    "v_accvgpr_read_b32 v199, a69\n"
    "v_mul_f32 v198, v198, v197\n"
    "v_mul_f32 v199, v199, v197\n"
    "v_accvgpr_write_b32 a68, v198\n"
    "v_accvgpr_write_b32 a69, v199\n"
    "v_accvgpr_read_b32 v198, a70\n"
    "v_accvgpr_read_b32 v199, a71\n"
    "v_mul_f32 v198, v198, v197\n"
    "v_mul_f32 v199, v199, v197\n"
    "v_accvgpr_write_b32 a70, v198\n"
    "v_accvgpr_write_b32 a71, v199\n"
    "v_accvgpr_read_b32 v198, a84\n"
    "v_accvgpr_read_b32 v199, a85\n"
    "v_mul_f32 v198, v198, v197\n"
    "v_mul_f32 v199, v199, v197\n"
    "v_accvgpr_write_b32 a84, v198\n"
    "v_accvgpr_write_b32 a85, v199\n"
    "v_accvgpr_read_b32 v198, a86\n"
    "v_accvgpr_read_b32 v199, a87\n"
    "v_mul_f32 v198, v198, v197\n"
    "v_mul_f32 v199, v199, v197\n"
    "v_accvgpr_write_b32 a86, v198\n"
    "v_accvgpr_write_b32 a87, v199\n"
    "v_accvgpr_read_b32 v198, a100\n"
    "v_accvgpr_read_b32 v199, a101\n"
    "v_mul_f32 v198, v198, v197\n"
    "v_mul_f32 v199, v199, v197\n"
    "v_accvgpr_write_b32 a100, v198\n"
    "v_accvgpr_write_b32 a101, v199\n"
    "v_accvgpr_read_b32 v198, a102\n"
    "v_accvgpr_read_b32 v199, a103\n"
    "v_mul_f32 v198, v198, v197\n"
    "v_mul_f32 v199, v199, v197\n"
    "v_accvgpr_write_b32 a102, v198\n"
    "v_accvgpr_write_b32 a103, v199\n"
    "v_accvgpr_read_b32 v198, a116\n"
    "v_accvgpr_read_b32 v199, a117\n"
    "v_mul_f32 v198, v198, v197\n"
    "v_mul_f32 v199, v199, v197\n"
    "v_accvgpr_write_b32 a116, v198\n"
    "v_accvgpr_write_b32 a117, v199\n"
    "v_accvgpr_read_b32 v198, a118\n"
    "v_accvgpr_read_b32 v199, a119\n"
    "v_mul_f32 v198, v198, v197\n"
    "v_mul_f32 v199, v199, v197\n"
    "v_accvgpr_write_b32 a118, v198\n"
    "v_accvgpr_write_b32 a119, v199\n"
    "v_accvgpr_read_b32 v198, a132\n"
    "v_accvgpr_read_b32 v199, a133\n"
    "v_mul_f32 v198, v198, v197\n"
    "v_mul_f32 v199, v199, v197\n"
    "v_accvgpr_write_b32 a132, v198\n"
    "v_accvgpr_write_b32 a133, v199\n"
    "v_accvgpr_read_b32 v198, a134\n"
    "v_accvgpr_read_b32 v199, a135\n"
    "v_mul_f32 v198, v198, v197\n"
    "v_mul_f32 v199, v199, v197\n"
    "v_accvgpr_write_b32 a134, v198\n"
    "v_accvgpr_write_b32 a135, v199\n"
    "v_accvgpr_read_b32 v198, a148\n"
    "v_accvgpr_read_b32 v199, a149\n"
    "v_mul_f32 v198, v198, v197\n"
    "v_mul_f32 v199, v199, v197\n"
    "v_accvgpr_write_b32 a148, v198\n"
    "v_accvgpr_write_b32 a149, v199\n"
    "v_accvgpr_read_b32 v198, a150\n"
    "v_accvgpr_read_b32 v199, a151\n"
    "v_mul_f32 v198, v198, v197\n"
    "v_mul_f32 v199, v199, v197\n"
    "v_accvgpr_write_b32 a150, v198\n"
    "v_accvgpr_write_b32 a151, v199\n"
    "v_accvgpr_read_b32 v198, a164\n"
    "v_accvgpr_read_b32 v199, a165\n"
    "v_mul_f32 v198, v198, v197\n"
    "v_mul_f32 v199, v199, v197\n"
    "v_accvgpr_write_b32 a164, v198\n"
    "v_accvgpr_write_b32 a165, v199\n"
    "v_accvgpr_read_b32 v198, a166\n"
    "v_accvgpr_read_b32 v199, a167\n"
    "v_mul_f32 v198, v198, v197\n"
    "v_mul_f32 v199, v199, v197\n"
    "v_accvgpr_write_b32 a166, v198\n"
    "v_accvgpr_write_b32 a167, v199\n"
    "v_accvgpr_read_b32 v198, a180\n"
    "v_accvgpr_read_b32 v199, a181\n"
    "v_mul_f32 v198, v198, v197\n"
    "v_mul_f32 v199, v199, v197\n"
    "v_accvgpr_write_b32 a180, v198\n"
    "v_accvgpr_write_b32 a181, v199\n"
    "v_accvgpr_read_b32 v198, a182\n"
    "v_accvgpr_read_b32 v199, a183\n"
    "v_mul_f32 v198, v198, v197\n"
    "v_mul_f32 v199, v199, v197\n"
    "v_accvgpr_write_b32 a182, v198\n"
    "v_accvgpr_write_b32 a183, v199\n"
    "v_max3_f32 v196, v40, v41, v42\n"
    "v_max3_f32 v196, v196, v43, v56\n"
    "v_max3_f32 v196, v196, v57, v58\n"
    "v_max3_f32 v196, v196, v59, v72\n"
    "v_max3_f32 v196, v196, v73, v74\n"
    "v_max3_f32 v196, v196, v75, v88\n"
    "v_max3_f32 v196, v196, v89, v90\n"
    "v_max_f32 v196, v196, v91\n"
    "v_mov_b32 v197, v196\n"
    "s_nop 1\n"
    "v_permlane32_swap_b32 v196, v197\n"
    "v_max_f32 v196, v196, v197\n"
    "v_mov_b32 v197, v196\n"
    "s_nop 1\n"
    "v_permlane16_swap_b32 v196, v197\n"
    "v_max_f32 v196, v196, v197\n"
    "v_max_f32 v196, v196, v104\n"
    "v_sub_f32 v197, v104, v196\n"
    "v_mov_b32 v104, v196\n"
    "v_exp_f32 v197, v197\n"
    "v_sub_f32 v40, v40, v196\n"
    "v_sub_f32 v41, v41, v196\n"
    "v_sub_f32 v42, v42, v196\n"
    "v_sub_f32 v43, v43, v196\n"
    "v_sub_f32 v56, v56, v196\n"
    "v_sub_f32 v57, v57, v196\n"
    "v_sub_f32 v58, v58, v196\n"
    "v_sub_f32 v59, v59, v196\n"
    "v_sub_f32 v72, v72, v196\n"
    "v_sub_f32 v73, v73, v196\n"
    "v_sub_f32 v74, v74, v196\n"
    "v_sub_f32 v75, v75, v196\n"
    "v_sub_f32 v88, v88, v196\n"
    "v_sub_f32 v89, v89, v196\n"
    "v_sub_f32 v90, v90, v196\n"
    "v_sub_f32 v91, v91, v196\n"
    "v_mul_f32 v120, v120, v197\n"
    "v_mul_f32 v121, v121, v197\n"
    "v_mul_f32 v122, v122, v197\n"
    "v_mul_f32 v123, v123, v197\n"
    "v_accvgpr_read_b32 v198, a8\n"
    "v_accvgpr_read_b32 v199, a9\n"
    "v_mul_f32 v198, v198, v197\n"
    "v_mul_f32 v199, v199, v197\n"
    "v_accvgpr_write_b32 a8, v198\n"
    "v_accvgpr_write_b32 a9, v199\n"
    "v_accvgpr_read_b32 v198, a10\n"
    "v_accvgpr_read_b32 v199, a11\n"
    "v_mul_f32 v198, v198, v197\n"
    "v_mul_f32 v199, v199, v197\n"
    "v_accvgpr_write_b32 a10, v198\n"
    "v_accvgpr_write_b32 a11, v199\n"
    "v_accvgpr_read_b32 v198, a24\n"
    "v_accvgpr_read_b32 v199, a25\n"
    "v_mul_f32 v198, v198, v197\n"
    "v_mul_f32 v199, v199, v197\n"
    "v_accvgpr_write_b32 a24, v198\n"
    "v_accvgpr_write_b32 a25, v199\n"
    "v_accvgpr_read_b32 v198, a26\n"
    "v_accvgpr_read_b32 v199, a27\n"
    "v_mul_f32 v198, v198, v197\n"
    "v_mul_f32 v199, v199, v197\n"
    "v_accvgpr_write_b32 a26, v198\n"
    "v_accvgpr_write_b32 a27, v199\n"
    "v_accvgpr_read_b32 v198, a40\n"
    "v_accvgpr_read_b32 v199, a41\n"
    "v_mul_f32 v198, v198, v197\n"
    "v_mul_f32 v199, v199, v197\n"
    "v_accvgpr_write_b32 a40, v198\n"
    "v_accvgpr_write_b32 a41, v199\n"
    "v_accvgpr_read_b32 v198, a42\n"
    "v_accvgpr_read_b32 v199, a43\n"
    "v_mul_f32 v198, v198, v197\n"
    "v_mul_f32 v199, v199, v197\n"
    "v_accvgpr_write_b32 a42, v198\n"
    "v_accvgpr_write_b32 a43, v199\n"
    "v_accvgpr_read_b32 v198, a56\n"
    "v_accvgpr_read_b32 v199, a57\n"
    "v_mul_f32 v198, v198, v197\n"
    "v_mul_f32 v199, v199, v197\n"
    "v_accvgpr_write_b32 a56, v198\n"
    "v_accvgpr_write_b32 a57, v199\n"
    "v_accvgpr_read_b32 v198, a58\n"
    "v_accvgpr_read_b32 v199, a59\n"
    "v_mul_f32 v198, v198, v197\n"
    "v_mul_f32 v199, v199, v197\n"
    "v_accvgpr_write_b32 a58, v198\n"
    "v_accvgpr_write_b32 a59, v199\n"
    "v_accvgpr_read_b32 v198, a72\n"
    "v_accvgpr_read_b32 v199, a73\n"
    "v_mul_f32 v198, v198, v197\n"
    "v_mul_f32 v199, v199, v197\n"
    "v_accvgpr_write_b32 a72, v198\n"
    "v_accvgpr_write_b32 a73, v199\n"
    "v_accvgpr_read_b32 v198, a74\n"
    "v_accvgpr_read_b32 v199, a75\n"
    "v_mul_f32 v198, v198, v197\n"
    "v_mul_f32 v199, v199, v197\n"
    "v_accvgpr_write_b32 a74, v198\n"
    "v_accvgpr_write_b32 a75, v199\n"
    "v_accvgpr_read_b32 v198, a88\n"
    "v_accvgpr_read_b32 v199, a89\n"
    "v_mul_f32 v198, v198, v197\n"
    "v_mul_f32 v199, v199, v197\n"
    "v_accvgpr_write_b32 a88, v198\n"
    "v_accvgpr_write_b32 a89, v199\n"
    "v_accvgpr_read_b32 v198, a90\n"
    "v_accvgpr_read_b32 v199, a91\n"
    "v_mul_f32 v198, v198, v197\n"
    "v_mul_f32 v199, v199, v197\n"
    "v_accvgpr_write_b32 a90, v198\n"
    "v_accvgpr_write_b32 a91, v199\n"
    "v_accvgpr_read_b32 v198, a104\n"
    "v_accvgpr_read_b32 v199, a105\n"
    "v_mul_f32 v198, v198, v197\n"
    "v_mul_f32 v199, v199, v197\n"
    "v_accvgpr_write_b32 a104, v198\n"
    "v_accvgpr_write_b32 a105, v199\n"
    "v_accvgpr_read_b32 v198, a106\n"
    "v_accvgpr_read_b32 v199, a107\n"
    "v_mul_f32 v198, v198, v197\n"
    "v_mul_f32 v199, v199, v197\n"
    "v_accvgpr_write_b32 a106, v198\n"
    "v_accvgpr_write_b32 a107, v199\n"
    "v_accvgpr_read_b32 v198, a120\n"
    "v_accvgpr_read_b32 v199, a121\n"
    "v_mul_f32 v198, v198, v197\n"
    "v_mul_f32 v199, v199, v197\n"
    "v_accvgpr_write_b32 a120, v198\n"
    "v_accvgpr_write_b32 a121, v199\n"
    "v_accvgpr_read_b32 v198, a122\n"
    "v_accvgpr_read_b32 v199, a123\n"
    "v_mul_f32 v198, v198, v197\n"
    "v_mul_f32 v199, v199, v197\n"
    "v_accvgpr_write_b32 a122, v198\n"
    "v_accvgpr_write_b32 a123, v199\n"
    "v_accvgpr_read_b32 v198, a136\n"
    "v_accvgpr_read_b32 v199, a137\n"
    "v_mul_f32 v198, v198, v197\n"
    "v_mul_f32 v199, v199, v197\n"
    "v_accvgpr_write_b32 a136, v198\n"
    "v_accvgpr_write_b32 a137, v199\n"
    "v_accvgpr_read_b32 v198, a138\n"
    "v_accvgpr_read_b32 v199, a139\n"
    "v_mul_f32 v198, v198, v197\n"
    "v_mul_f32 v199, v199, v197\n"
    "v_accvgpr_write_b32 a138, v198\n"
    "v_accvgpr_write_b32 a139, v199\n"
    "v_accvgpr_read_b32 v198, a152\n"
    "v_accvgpr_read_b32 v199, a153\n"
    "v_mul_f32 v198, v198, v197\n"
    "v_mul_f32 v199, v199, v197\n"
    "v_accvgpr_write_b32 a152, v198\n"
    "v_accvgpr_write_b32 a153, v199\n"
    "v_accvgpr_read_b32 v198, a154\n"
    "v_accvgpr_read_b32 v199, a155\n"
    "v_mul_f32 v198, v198, v197\n"
    "v_mul_f32 v199, v199, v197\n"
    "v_accvgpr_write_b32 a154, v198\n"
    "v_accvgpr_write_b32 a155, v199\n"
    "v_accvgpr_read_b32 v198, a168\n"
    "v_accvgpr_read_b32 v199, a169\n"
    "v_mul_f32 v198, v198, v197\n"
    "v_mul_f32 v199, v199, v197\n"
    "v_accvgpr_write_b32 a168, v198\n"
    "v_accvgpr_write_b32 a169, v199\n"
    "v_accvgpr_read_b32 v198, a170\n"
    "v_accvgpr_read_b32 v199, a171\n"
    "v_mul_f32 v198, v198, v197\n"
    "v_mul_f32 v199, v199, v197\n"
    "v_accvgpr_write_b32 a170, v198\n"
    "v_accvgpr_write_b32 a171, v199\n"
    "v_accvgpr_read_b32 v198, a184\n"
    "v_accvgpr_read_b32 v199, a185\n"
    "v_mul_f32 v198, v198, v197\n"
    "v_mul_f32 v199, v199, v197\n"
    "v_accvgpr_write_b32 a184, v198\n"
    "v_accvgpr_write_b32 a185, v199\n"
    "v_accvgpr_read_b32 v198, a186\n"
    "v_accvgpr_read_b32 v199, a187\n"
    "v_mul_f32 v198, v198, v197\n"
    "v_mul_f32 v199, v199, v197\n"
    "v_accvgpr_write_b32 a186, v198\n"
    "v_accvgpr_write_b32 a187, v199\n"
    "v_max3_f32 v196, v44, v45, v46\n"
    "v_max3_f32 v196, v196, v47, v60\n"
    "v_max3_f32 v196, v196, v61, v62\n"
    "v_max3_f32 v196, v196, v63, v76\n"
    "v_max3_f32 v196, v196, v77, v78\n"
    "v_max3_f32 v196, v196, v79, v92\n"
    "v_max3_f32 v196, v196, v93, v94\n"
    "v_max_f32 v196, v196, v95\n"
    "v_mov_b32 v197, v196\n"
    "s_nop 1\n"
    "v_permlane32_swap_b32 v196, v197\n"
    "v_max_f32 v196, v196, v197\n"
    "v_mov_b32 v197, v196\n"
    "s_nop 1\n"
    "v_permlane16_swap_b32 v196, v197\n"
    "v_max_f32 v196, v196, v197\n"
    "v_max_f32 v196, v196, v108\n"
    "v_sub_f32 v197, v108, v196\n"
    "v_mov_b32 v108, v196\n"
    "v_exp_f32 v197, v197\n"
    "v_sub_f32 v44, v44, v196\n"
    "v_sub_f32 v45, v45, v196\n"
    "v_sub_f32 v46, v46, v196\n"
    "v_sub_f32 v47, v47, v196\n"
    "v_sub_f32 v60, v60, v196\n"
    "v_sub_f32 v61, v61, v196\n"
    "v_sub_f32 v62, v62, v196\n"
    "v_sub_f32 v63, v63, v196\n"
    "v_sub_f32 v76, v76, v196\n"
    "v_sub_f32 v77, v77, v196\n"
    "v_sub_f32 v78, v78, v196\n"
    "v_sub_f32 v79, v79, v196\n"
    "v_sub_f32 v92, v92, v196\n"
    "v_sub_f32 v93, v93, v196\n"
    "v_sub_f32 v94, v94, v196\n"
    "v_sub_f32 v95, v95, v196\n"
    "v_mul_f32 v124, v124, v197\n"
    "v_mul_f32 v125, v125, v197\n"
    "v_mul_f32 v126, v126, v197\n"
    "v_mul_f32 v127, v127, v197\n"
    "v_accvgpr_read_b32 v198, a12\n"
    "v_accvgpr_read_b32 v199, a13\n"
    "v_mul_f32 v198, v198, v197\n"
    "v_mul_f32 v199, v199, v197\n"
    "v_accvgpr_write_b32 a12, v198\n"
    "v_accvgpr_write_b32 a13, v199\n"
    "v_accvgpr_read_b32 v198, a14\n"
    "v_accvgpr_read_b32 v199, a15\n"
    "v_mul_f32 v198, v198, v197\n"
    "v_mul_f32 v199, v199, v197\n"
    "v_accvgpr_write_b32 a14, v198\n"
    "v_accvgpr_write_b32 a15, v199\n"
    "v_accvgpr_read_b32 v198, a28\n"
    "v_accvgpr_read_b32 v199, a29\n"
    "v_mul_f32 v198, v198, v197\n"
    "v_mul_f32 v199, v199, v197\n"
    "v_accvgpr_write_b32 a28, v198\n"
    "v_accvgpr_write_b32 a29, v199\n"
    "v_accvgpr_read_b32 v198, a30\n"
    "v_accvgpr_read_b32 v199, a31\n"
    "v_mul_f32 v198, v198, v197\n"
    "v_mul_f32 v199, v199, v197\n"
    "v_accvgpr_write_b32 a30, v198\n"
    "v_accvgpr_write_b32 a31, v199\n"
    "v_accvgpr_read_b32 v198, a44\n"
    "v_accvgpr_read_b32 v199, a45\n"
    "v_mul_f32 v198, v198, v197\n"
    "v_mul_f32 v199, v199, v197\n"
    "v_accvgpr_write_b32 a44, v198\n"
    "v_accvgpr_write_b32 a45, v199\n"
    "v_accvgpr_read_b32 v198, a46\n"
    "v_accvgpr_read_b32 v199, a47\n"
    "v_mul_f32 v198, v198, v197\n"
    "v_mul_f32 v199, v199, v197\n"
    "v_accvgpr_write_b32 a46, v198\n"
    "v_accvgpr_write_b32 a47, v199\n"
    "v_accvgpr_read_b32 v198, a60\n"
    "v_accvgpr_read_b32 v199, a61\n"
    "v_mul_f32 v198, v198, v197\n"
    "v_mul_f32 v199, v199, v197\n"
    "v_accvgpr_write_b32 a60, v198\n"
    "v_accvgpr_write_b32 a61, v199\n"
    "v_accvgpr_read_b32 v198, a62\n"
    "v_accvgpr_read_b32 v199, a63\n"
    "v_mul_f32 v198, v198, v197\n"
    "v_mul_f32 v199, v199, v197\n"
    "v_accvgpr_write_b32 a62, v198\n"
    "v_accvgpr_write_b32 a63, v199\n"
    "v_accvgpr_read_b32 v198, a76\n"
    "v_accvgpr_read_b32 v199, a77\n"
    "v_mul_f32 v198, v198, v197\n"
    "v_mul_f32 v199, v199, v197\n"
    "v_accvgpr_write_b32 a76, v198\n"
    "v_accvgpr_write_b32 a77, v199\n"
    "v_accvgpr_read_b32 v198, a78\n"
    "v_accvgpr_read_b32 v199, a79\n"
    "v_mul_f32 v198, v198, v197\n"
    "v_mul_f32 v199, v199, v197\n"
    "v_accvgpr_write_b32 a78, v198\n"
    "v_accvgpr_write_b32 a79, v199\n"
    "v_accvgpr_read_b32 v198, a92\n"
    "v_accvgpr_read_b32 v199, a93\n"
    "v_mul_f32 v198, v198, v197\n"
    "v_mul_f32 v199, v199, v197\n"
    "v_accvgpr_write_b32 a92, v198\n"
    "v_accvgpr_write_b32 a93, v199\n"
    "v_accvgpr_read_b32 v198, a94\n"
    "v_accvgpr_read_b32 v199, a95\n"
    "v_mul_f32 v198, v198, v197\n"
    "v_mul_f32 v199, v199, v197\n"
    "v_accvgpr_write_b32 a94, v198\n"
    "v_accvgpr_write_b32 a95, v199\n"
    "v_accvgpr_read_b32 v198, a108\n"
    "v_accvgpr_read_b32 v199, a109\n"
    "v_mul_f32 v198, v198, v197\n"
    "v_mul_f32 v199, v199, v197\n"
    "v_accvgpr_write_b32 a108, v198\n"
    "v_accvgpr_write_b32 a109, v199\n"
    "v_accvgpr_read_b32 v198, a110\n"
    "v_accvgpr_read_b32 v199, a111\n"
    "v_mul_f32 v198, v198, v197\n"
    "v_mul_f32 v199, v199, v197\n"
    "v_accvgpr_write_b32 a110, v198\n"
    "v_accvgpr_write_b32 a111, v199\n"
    "v_accvgpr_read_b32 v198, a124\n"
    "v_accvgpr_read_b32 v199, a125\n"
    "v_mul_f32 v198, v198, v197\n"
    "v_mul_f32 v199, v199, v197\n"
    "v_accvgpr_write_b32 a124, v198\n"
    "v_accvgpr_write_b32 a125, v199\n"
    "v_accvgpr_read_b32 v198, a126\n"
    "v_accvgpr_read_b32 v199, a127\n"
    "v_mul_f32 v198, v198, v197\n"
    "v_mul_f32 v199, v199, v197\n"
    "v_accvgpr_write_b32 a126, v198\n"
    "v_accvgpr_write_b32 a127, v199\n"
    "v_accvgpr_read_b32 v198, a140\n"
    "v_accvgpr_read_b32 v199, a141\n"
    "v_mul_f32 v198, v198, v197\n"
    "v_mul_f32 v199, v199, v197\n"
    "v_accvgpr_write_b32 a140, v198\n"
    "v_accvgpr_write_b32 a141, v199\n"
    "v_accvgpr_read_b32 v198, a142\n"
    "v_accvgpr_read_b32 v199, a143\n"
    "v_mul_f32 v198, v198, v197\n"
    "v_mul_f32 v199, v199, v197\n"
    "v_accvgpr_write_b32 a142, v198\n"
    "v_accvgpr_write_b32 a143, v199\n"
    "v_accvgpr_read_b32 v198, a156\n"
    "v_accvgpr_read_b32 v199, a157\n"
    "v_mul_f32 v198, v198, v197\n"
    "v_mul_f32 v199, v199, v197\n"
    "v_accvgpr_write_b32 a156, v198\n"
    "v_accvgpr_write_b32 a157, v199\n"
    "v_accvgpr_read_b32 v198, a158\n"
    "v_accvgpr_read_b32 v199, a159\n"
    "v_mul_f32 v198, v198, v197\n"
    "v_mul_f32 v199, v199, v197\n"
    "v_accvgpr_write_b32 a158, v198\n"
    "v_accvgpr_write_b32 a159, v199\n"
    "v_accvgpr_read_b32 v198, a172\n"
    "v_accvgpr_read_b32 v199, a173\n"
    "v_mul_f32 v198, v198, v197\n"
    "v_mul_f32 v199, v199, v197\n"
    "v_accvgpr_write_b32 a172, v198\n"
    "v_accvgpr_write_b32 a173, v199\n"
    "v_accvgpr_read_b32 v198, a174\n"
    "v_accvgpr_read_b32 v199, a175\n"
    "v_mul_f32 v198, v198, v197\n"
    "v_mul_f32 v199, v199, v197\n"
    "v_accvgpr_write_b32 a174, v198\n"
    "v_accvgpr_write_b32 a175, v199\n"
    "v_accvgpr_read_b32 v198, a188\n"
    "v_accvgpr_read_b32 v199, a189\n"
    "v_mul_f32 v198, v198, v197\n"
    "v_mul_f32 v199, v199, v197\n"
    "v_accvgpr_write_b32 a188, v198\n"
    "v_accvgpr_write_b32 a189, v199\n"
    "v_accvgpr_read_b32 v198, a190\n"
    "v_accvgpr_read_b32 v199, a191\n"
    "v_mul_f32 v198, v198, v197\n"
    "v_mul_f32 v199, v199, v197\n"
    "v_accvgpr_write_b32 a190, v198\n"
    "v_accvgpr_write_b32 a191, v199\n"
    "v_exp_f32 v32, v32\n"
    "v_exp_f32 v33, v33\n"
    "v_exp_f32 v34, v34\n"
    "v_cvt_pk_bf16_f32 v128, v32, v33\n"
    "v_exp_f32 v35, v35\n"
    "v_exp_f32 v36, v36\n"
    "v_cvt_pk_bf16_f32 v129, v34, v35\n"
    "v_exp_f32 v37, v37\n"
    "v_exp_f32 v38, v38\n"
    "v_cvt_pk_bf16_f32 v132, v36, v37\n"
    "v_exp_f32 v39, v39\n"
    "v_exp_f32 v40, v40\n"
    "v_cvt_pk_bf16_f32 v133, v38, v39\n"
    "v_exp_f32 v41, v41\n"
    "v_exp_f32 v42, v42\n"
    "v_cvt_pk_bf16_f32 v136, v40, v41\n"
    "v_exp_f32 v43, v43\n"
    "v_exp_f32 v44, v44\n"
    "v_cvt_pk_bf16_f32 v137, v42, v43\n"
    "v_exp_f32 v45, v45\n"
    "v_exp_f32 v46, v46\n"
    "v_cvt_pk_bf16_f32 v140, v44, v45\n"
    "v_exp_f32 v47, v47\n"
    "v_exp_f32 v48, v48\n"
    "v_cvt_pk_bf16_f32 v141, v46, v47\n"
    "v_exp_f32 v49, v49\n"
    "v_exp_f32 v50, v50\n"
    "v_cvt_pk_bf16_f32 v130, v48, v49\n"
    "v_exp_f32 v51, v51\n"
    "v_exp_f32 v52, v52\n"
    "v_cvt_pk_bf16_f32 v131, v50, v51\n"
    "v_exp_f32 v53, v53\n"
    "v_exp_f32 v54, v54\n"
    "v_cvt_pk_bf16_f32 v134, v52, v53\n"
    "v_exp_f32 v55, v55\n"
    "v_exp_f32 v56, v56\n"
    "v_cvt_pk_bf16_f32 v135, v54, v55\n"
    "v_exp_f32 v57, v57\n"
    "v_exp_f32 v58, v58\n"
    "v_cvt_pk_bf16_f32 v138, v56, v57\n"
    "v_exp_f32 v59, v59\n"
    "v_exp_f32 v60, v60\n"
    "v_cvt_pk_bf16_f32 v139, v58, v59\n"
    "v_exp_f32 v61, v61\n"
    "v_exp_f32 v62, v62\n"
    "v_cvt_pk_bf16_f32 v142, v60, v61\n"
    "v_exp_f32 v63, v63\n"
    "s_nop 0\n"
    "v_cvt_pk_bf16_f32 v143, v62, v63\n"
    "v_exp_f32 v64, v64\n"
    "v_exp_f32 v65, v65\n"
    "v_exp_f32 v66, v66\n"
    "v_cvt_pk_bf16_f32 v144, v64, v65\n"
    "v_exp_f32 v67, v67\n"
    "v_exp_f32 v68, v68\n"
    "v_cvt_pk_bf16_f32 v145, v66, v67\n"
    "v_exp_f32 v69, v69\n"
    "v_exp_f32 v70, v70\n"
    "v_cvt_pk_bf16_f32 v148, v68, v69\n"
    "v_exp_f32 v71, v71\n"
    "v_exp_f32 v72, v72\n"
    "v_cvt_pk_bf16_f32 v149, v70, v71\n"
    "v_exp_f32 v73, v73\n"
    "v_exp_f32 v74, v74\n"
    "v_cvt_pk_bf16_f32 v152, v72, v73\n"
    "v_exp_f32 v75, v75\n"
    "v_exp_f32 v76, v76\n"
    "v_cvt_pk_bf16_f32 v153, v74, v75\n"
    "v_exp_f32 v77, v77\n"
    "v_exp_f32 v78, v78\n"
    "v_cvt_pk_bf16_f32 v156, v76, v77\n"
    "v_exp_f32 v79, v79\n"
    "v_exp_f32 v80, v80\n"
    "v_cvt_pk_bf16_f32 v157, v78, v79\n"
    "v_exp_f32 v81, v81\n"
    "v_exp_f32 v82, v82\n"
    "v_cvt_pk_bf16_f32 v146, v80, v81\n"
    "v_exp_f32 v83, v83\n"
    "v_exp_f32 v84, v84\n"
    "v_cvt_pk_bf16_f32 v147, v82, v83\n"
    "v_exp_f32 v85, v85\n"
    "v_exp_f32 v86, v86\n"
    "v_cvt_pk_bf16_f32 v150, v84, v85\n"
    "v_exp_f32 v87, v87\n"
    "v_exp_f32 v88, v88\n"
    "v_cvt_pk_bf16_f32 v151, v86, v87\n"
    "v_exp_f32 v89, v89\n"
    "v_exp_f32 v90, v90\n"
    "v_cvt_pk_bf16_f32 v154, v88, v89\n"
    "v_exp_f32 v91, v91\n"
    "v_exp_f32 v92, v92\n"
    "v_cvt_pk_bf16_f32 v155, v90, v91\n"
    "v_exp_f32 v93, v93\n"
    "v_exp_f32 v94, v94\n"
    "v_cvt_pk_bf16_f32 v158, v92, v93\n"
    "v_exp_f32 v95, v95\n"
    "s_nop 0\n"
    "v_cvt_pk_bf16_f32 v159, v94, v95\n"
    "ds_read_b128 v[160:163], v205 offset:12288\n"
    "ds_read_b128 v[164:167], v205 offset:13312\n"
    "s_waitcnt lgkmcnt(1)\n"
    "v_mfma_f32_16x16x32_bf16 a[0:3], v[160:163], v[128:131], a[0:3]\n"
    "s_waitcnt lgkmcnt(0)\n"
    "v_mfma_f32_16x16x32_bf16 a[0:3], v[164:167], v[144:147], a[0:3]\n"
    "v_mfma_f32_16x16x32_bf16 a[4:7], v[160:163], v[132:135], a[4:7]\n"
    "v_mfma_f32_16x16x32_bf16 a[4:7], v[164:167], v[148:151], a[4:7]\n"
    "v_mfma_f32_16x16x32_bf16 a[8:11], v[160:163], v[136:139], a[8:11]\n"
    "v_mfma_f32_16x16x32_bf16 a[8:11], v[164:167], v[152:155], a[8:11]\n"
    "v_mfma_f32_16x16x32_bf16 a[12:15], v[160:163], v[140:143], a[12:15]\n"
    "v_mfma_f32_16x16x32_bf16 a[12:15], v[164:167], v[156:159], a[12:15]\n"
    "ds_read_b128 v[168:171], v205 offset:14336\n"
    "ds_read_b128 v[172:175], v205 offset:15360\n"
    "s_waitcnt lgkmcnt(1)\n"
    "v_mfma_f32_16x16x32_bf16 a[28:31], v[168:171], v[140:143], a[28:31]\n"
    "s_waitcnt lgkmcnt(0)\n"
    "v_mfma_f32_16x16x32_bf16 a[28:31], v[172:175], v[156:159], a[28:31]\n"
    "v_mfma_f32_16x16x32_bf16 a[24:27], v[172:175], v[152:155], a[24:27]\n"
    "v_mfma_f32_16x16x32_bf16 a[24:27], v[168:171], v[136:139], a[24:27]\n"
    "v_mfma_f32_16x16x32_bf16 a[20:23], v[168:171], v[132:135], a[20:23]\n"
    "v_mfma_f32_16x16x32_bf16 a[20:23], v[172:175], v[148:151], a[20:23]\n"
    "v_mfma_f32_16x16x32_bf16 a[16:19], v[172:175], v[144:147], a[16:19]\n"
    "v_mfma_f32_16x16x32_bf16 a[16:19], v[168:171], v[128:131], a[16:19]\n"
    "ds_read_b128 v[176:179], v205 offset:16384\n"
    "ds_read_b128 v[180:183], v205 offset:17408\n"
    "s_waitcnt lgkmcnt(1)\n"
    "v_mfma_f32_16x16x32_bf16 a[32:35], v[176:179], v[128:131], a[32:35]\n"
    "s_waitcnt lgkmcnt(0)\n"
    "v_mfma_f32_16x16x32_bf16 a[32:35], v[180:183], v[144:147], a[32:35]\n"
    "v_mfma_f32_16x16x32_bf16 a[36:39], v[180:183], v[148:151], a[36:39]\n"
    "v_mfma_f32_16x16x32_bf16 a[36:39], v[176:179], v[132:135], a[36:39]\n"
    "v_mfma_f32_16x16x32_bf16 a[40:43], v[176:179], v[136:139], a[40:43]\n"
    "v_mfma_f32_16x16x32_bf16 a[40:43], v[180:183], v[152:155], a[40:43]\n"
    "v_mfma_f32_16x16x32_bf16 a[44:47], v[180:183], v[156:159], a[44:47]\n"
    "v_mfma_f32_16x16x32_bf16 a[44:47], v[176:179], v[140:143], a[44:47]\n"
    "ds_read_b128 v[160:163], v205 offset:18432\n"
    "ds_read_b128 v[164:167], v205 offset:19456\n"
    "s_waitcnt lgkmcnt(1)\n"
    "v_mfma_f32_16x16x32_bf16 a[60:63], v[160:163], v[140:143], a[60:63]\n"
    "s_waitcnt lgkmcnt(0)\n"
    "v_mfma_f32_16x16x32_bf16 a[60:63], v[164:167], v[156:159], a[60:63]\n"
    "v_mfma_f32_16x16x32_bf16 a[56:59], v[164:167], v[152:155], a[56:59]\n"
    "v_mfma_f32_16x16x32_bf16 a[56:59], v[160:163], v[136:139], a[56:59]\n"
    "v_mfma_f32_16x16x32_bf16 a[52:55], v[160:163], v[132:135], a[52:55]\n"
    "v_mfma_f32_16x16x32_bf16 a[52:55], v[164:167], v[148:151], a[52:55]\n"
    "v_mfma_f32_16x16x32_bf16 a[48:51], v[164:167], v[144:147], a[48:51]\n"
    "v_mfma_f32_16x16x32_bf16 a[48:51], v[160:163], v[128:131], a[48:51]\n"
    "ds_read_b128 v[168:171], v205 offset:20480\n"
    "ds_read_b128 v[172:175], v205 offset:21504\n"
    "s_waitcnt lgkmcnt(1)\n"
    "v_mfma_f32_16x16x32_bf16 a[64:67], v[168:171], v[128:131], a[64:67]\n"
    "s_waitcnt lgkmcnt(0)\n"
    "v_mfma_f32_16x16x32_bf16 a[64:67], v[172:175], v[144:147], a[64:67]\n"
    "v_mfma_f32_16x16x32_bf16 a[68:71], v[172:175], v[148:151], a[68:71]\n"
    "v_mfma_f32_16x16x32_bf16 a[68:71], v[168:171], v[132:135], a[68:71]\n"
    "v_mfma_f32_16x16x32_bf16 a[72:75], v[168:171], v[136:139], a[72:75]\n"
    "v_mfma_f32_16x16x32_bf16 a[72:75], v[172:175], v[152:155], a[72:75]\n"
    "v_mfma_f32_16x16x32_bf16 a[76:79], v[172:175], v[156:159], a[76:79]\n"
    "v_mfma_f32_16x16x32_bf16 a[76:79], v[168:171], v[140:143], a[76:79]\n"
    "ds_read_b128 v[176:179], v205 offset:22528\n"
    "ds_read_b128 v[180:183], v205 offset:23552\n"
    "s_waitcnt lgkmcnt(1)\n"
    "v_mfma_f32_16x16x32_bf16 a[92:95], v[176:179], v[140:143], a[92:95]\n"
    "s_waitcnt lgkmcnt(0)\n"
    "v_mfma_f32_16x16x32_bf16 a[92:95], v[180:183], v[156:159], a[92:95]\n"
    "v_mfma_f32_16x16x32_bf16 a[88:91], v[180:183], v[152:155], a[88:91]\n"
    "v_mfma_f32_16x16x32_bf16 a[88:91], v[176:179], v[136:139], a[88:91]\n"
    "v_mfma_f32_16x16x32_bf16 a[84:87], v[176:179], v[132:135], a[84:87]\n"
    "v_mfma_f32_16x16x32_bf16 a[84:87], v[180:183], v[148:151], a[84:87]\n"
    "v_mfma_f32_16x16x32_bf16 a[80:83], v[180:183], v[144:147], a[80:83]\n"
    "v_mfma_f32_16x16x32_bf16 a[80:83], v[176:179], v[128:131], a[80:83]\n"
    "ds_read_b128 v[160:163], v205 offset:24576\n"
    "ds_read_b128 v[164:167], v205 offset:25600\n"
    "s_waitcnt lgkmcnt(1)\n"
    "v_mfma_f32_16x16x32_bf16 a[96:99], v[160:163], v[128:131], a[96:99]\n"
    "s_waitcnt lgkmcnt(0)\n"
    "v_mfma_f32_16x16x32_bf16 a[96:99], v[164:167], v[144:147], a[96:99]\n"
    "v_mfma_f32_16x16x32_bf16 a[100:103], v[164:167], v[148:151], a[100:103]\n"
    "v_mfma_f32_16x16x32_bf16 a[100:103], v[160:163], v[132:135], a[100:103]\n"
    "v_mfma_f32_16x16x32_bf16 a[104:107], v[160:163], v[136:139], a[104:107]\n"
    "v_mfma_f32_16x16x32_bf16 a[104:107], v[164:167], v[152:155], a[104:107]\n"
    "v_mfma_f32_16x16x32_bf16 a[108:111], v[164:167], v[156:159], a[108:111]\n"
    "v_mfma_f32_16x16x32_bf16 a[108:111], v[160:163], v[140:143], a[108:111]\n"
    "ds_read_b128 v[168:171], v205 offset:26624\n"
    "ds_read_b128 v[172:175], v205 offset:27648\n"
    "s_waitcnt lgkmcnt(1)\n"
    "v_mfma_f32_16x16x32_bf16 a[124:127], v[168:171], v[140:143], a[124:127]\n"
    "s_waitcnt lgkmcnt(0)\n"
    "v_mfma_f32_16x16x32_bf16 a[124:127], v[172:175], v[156:159], a[124:127]\n"
    "v_mfma_f32_16x16x32_bf16 a[120:123], v[172:175], v[152:155], a[120:123]\n"
    "v_mfma_f32_16x16x32_bf16 a[120:123], v[168:171], v[136:139], a[120:123]\n"
    "v_mfma_f32_16x16x32_bf16 a[116:119], v[168:171], v[132:135], a[116:119]\n"
    "v_mfma_f32_16x16x32_bf16 a[116:119], v[172:175], v[148:151], a[116:119]\n"
    "v_mfma_f32_16x16x32_bf16 a[112:115], v[172:175], v[144:147], a[112:115]\n"
    "v_mfma_f32_16x16x32_bf16 a[112:115], v[168:171], v[128:131], a[112:115]\n"
    "ds_read_b128 v[176:179], v205 offset:28672\n"
    "ds_read_b128 v[180:183], v205 offset:29696\n"
    "s_waitcnt lgkmcnt(1)\n"
    "v_mfma_f32_16x16x32_bf16 a[128:131], v[176:179], v[128:131], a[128:131]\n"
    "s_waitcnt lgkmcnt(0)\n"
    "v_mfma_f32_16x16x32_bf16 a[128:131], v[180:183], v[144:147], a[128:131]\n"
    "v_mfma_f32_16x16x32_bf16 a[132:135], v[180:183], v[148:151], a[132:135]\n"
    "v_mfma_f32_16x16x32_bf16 a[132:135], v[176:179], v[132:135], a[132:135]\n"
    "v_mfma_f32_16x16x32_bf16 a[136:139], v[176:179], v[136:139], a[136:139]\n"
    "v_mfma_f32_16x16x32_bf16 a[136:139], v[180:183], v[152:155], a[136:139]\n"
    "v_mfma_f32_16x16x32_bf16 a[140:143], v[180:183], v[156:159], a[140:143]\n"
    "v_mfma_f32_16x16x32_bf16 a[140:143], v[176:179], v[140:143], a[140:143]\n"
    "ds_read_b128 v[160:163], v205 offset:30720\n"
    "ds_read_b128 v[164:167], v205 offset:31744\n"
    "s_waitcnt lgkmcnt(1)\n"
    "v_mfma_f32_16x16x32_bf16 a[156:159], v[160:163], v[140:143], a[156:159]\n"
    "s_waitcnt lgkmcnt(0)\n"
    "v_mfma_f32_16x16x32_bf16 a[156:159], v[164:167], v[156:159], a[156:159]\n"
    "v_mfma_f32_16x16x32_bf16 a[152:155], v[164:167], v[152:155], a[152:155]\n"
    "v_mfma_f32_16x16x32_bf16 a[152:155], v[160:163], v[136:139], a[152:155]\n"
    "v_mfma_f32_16x16x32_bf16 a[148:151], v[160:163], v[132:135], a[148:151]\n"
    "v_mfma_f32_16x16x32_bf16 a[148:151], v[164:167], v[148:151], a[148:151]\n"
    "v_mfma_f32_16x16x32_bf16 a[144:147], v[164:167], v[144:147], a[144:147]\n"
    "v_mfma_f32_16x16x32_bf16 a[144:147], v[160:163], v[128:131], a[144:147]\n"
    "ds_read_b128 v[168:171], v205 offset:32768\n"
    "ds_read_b128 v[172:175], v205 offset:33792\n"
    "s_waitcnt lgkmcnt(1)\n"
    "v_mfma_f32_16x16x32_bf16 a[160:163], v[168:171], v[128:131], a[160:163]\n"
    "s_waitcnt lgkmcnt(0)\n"
    "v_mfma_f32_16x16x32_bf16 a[160:163], v[172:175], v[144:147], a[160:163]\n"
    "v_mfma_f32_16x16x32_bf16 a[164:167], v[172:175], v[148:151], a[164:167]\n"
    "v_mfma_f32_16x16x32_bf16 a[164:167], v[168:171], v[132:135], a[164:167]\n"
    "v_mfma_f32_16x16x32_bf16 a[168:171], v[168:171], v[136:139], a[168:171]\n"
    "v_mfma_f32_16x16x32_bf16 a[168:171], v[172:175], v[152:155], a[168:171]\n"
    "v_mfma_f32_16x16x32_bf16 a[172:175], v[172:175], v[156:159], a[172:175]\n"
    "v_mfma_f32_16x16x32_bf16 a[172:175], v[168:171], v[140:143], a[172:175]\n"
    "ds_read_b128 v[176:179], v205 offset:34816\n"
    "ds_read_b128 v[180:183], v205 offset:35840\n"
    "s_waitcnt lgkmcnt(1)\n"
    "v_mfma_f32_16x16x32_bf16 a[188:191], v[176:179], v[140:143], a[188:191]\n"
    "s_waitcnt lgkmcnt(0)\n"
    "v_mfma_f32_16x16x32_bf16 a[188:191], v[180:183], v[156:159], a[188:191]\n"
    "v_mfma_f32_16x16x32_bf16 a[184:187], v[176:179], v[136:139], a[184:187]\n"
    "v_mfma_f32_16x16x32_bf16 a[184:187], v[180:183], v[152:155], a[184:187]\n"
    "v_mfma_f32_16x16x32_bf16 a[180:183], v[176:179], v[132:135], a[180:183]\n"
    "v_mfma_f32_16x16x32_bf16 a[180:183], v[180:183], v[148:151], a[180:183]\n"
    "v_mfma_f32_16x16x32_bf16 a[176:179], v[176:179], v[128:131], a[176:179]\n"
    "v_mfma_f32_16x16x32_bf16 a[176:179], v[180:183], v[144:147], a[176:179]\n"
    "v_mfma_f32_16x16x32_bf16 v[112:115], v[192:195], v[128:131], v[112:115]\n"
    "v_mfma_f32_16x16x32_bf16 v[112:115], v[192:195], v[144:147], v[112:115]\n"
    "v_mfma_f32_16x16x32_bf16 v[116:119], v[192:195], v[132:135], v[116:119]\n"
    "v_mfma_f32_16x16x32_bf16 v[116:119], v[192:195], v[148:151], v[116:119]\n"
    "v_mfma_f32_16x16x32_bf16 v[120:123], v[192:195], v[136:139], v[120:123]\n"
    "v_mfma_f32_16x16x32_bf16 v[120:123], v[192:195], v[152:155], v[120:123]\n"
    "v_mfma_f32_16x16x32_bf16 v[124:127], v[192:195], v[140:143], v[124:127]\n"
    "v_mfma_f32_16x16x32_bf16 v[124:127], v[192:195], v[156:159], v[124:127]\n"
    "s_nop 15\n"
    "s_add_u32 s63, s63, 0x6000\n"
    "s_cmp_lt_u32 s63, 0xc0000\n"
    "s_cbranch_scc1 LSLOOP\n"
    "s_branch LEPI\n"
    "LEPI:\n"
    "s_lshl_b32 s76, s56, 3\n"
    "s_add_u32 s76, s76, s57\n"
    "s_mul_i32 s76, s76, 0x18000\n"
    "s_add_u32 s76, s76, s88\n"
    "s_add_u32 s28, s24, s76\n"
    "s_addc_u32 s29, s25, 0\n"
    "s_and_b32 s29, s29, 0xffff\n"
    "s_mov_b32 s30, 0x6000\n"
    "s_mov_b32 s31, 0x20000\n"
    "s_xor_b32 s94, s70, 0x80000000\n"
    "s_xor_b32 s95, s70, 0x80000000\n"
    "s_mov_b32 s62, 0x1800\n"
    "s_mov_b32 s63, 0x3000\n"
    "s_mov_b32 s64, 0x4800\n"
    "v_and_b32 v198, 15, v231\n"
    "v_mul_u32_u24 v205, 0x180, v198\n"
    "v_lshrrev_b32 v199, 4, v231\n"
    "v_lshl_add_u32 v205, v199, 4, v205\n"
    "v_mul_f32 v222, s68, v112\n"
    "v_rcp_f32 v222, v222\n"
    "v_mul_f32 v224, s68, v116\n"
    "v_rcp_f32 v224, v224\n"
    "v_mul_f32 v226, s68, v120\n"
    "v_rcp_f32 v226, v226\n"
    "v_mul_f32 v228, s68, v124\n"
    "v_rcp_f32 v228, v228\n"
    "v_mov_b32 v223, v222\n"
    "v_mov_b32 v225, v224\n"
    "v_mov_b32 v227, v226\n"
    "v_mov_b32 v229, v228\n"
    "v_accvgpr_read_b32 v96, a0\n"
    "v_accvgpr_read_b32 v97, a1\n"
    "v_accvgpr_read_b32 v98, a2\n"
    "v_accvgpr_read_b32 v99, a3\n"
    "v_pk_fma_f32 v[96:97], v[96:97], v[222:223], s[94:95]\n"
    "v_pk_fma_f32 v[98:99], v[98:99], v[222:223], s[94:95]\n"
    "v_cvt_pk_bf16_f32 v32, v96, v97\n"
    "v_cvt_pk_bf16_f32 v33, v98, v99\n"
    "v_accvgpr_read_b32 v100, a16\n"
    "v_accvgpr_read_b32 v101, a17\n"
    "v_accvgpr_read_b32 v102, a18\n"
    "v_accvgpr_read_b32 v103, a19\n"
    "v_pk_fma_f32 v[100:101], v[100:101], v[222:223], s[94:95]\n"
    "v_pk_fma_f32 v[102:103], v[102:103], v[222:223], s[94:95]\n"
    "v_cvt_pk_bf16_f32 v34, v100, v101\n"
    "v_cvt_pk_bf16_f32 v35, v102, v103\n"
    "buffer_store_dwordx4 v[32:35], v205, s[28:31], 0 offen sc0 sc1\n"
    "v_accvgpr_read_b32 v104, a32\n"
    "v_accvgpr_read_b32 v105, a33\n"
    "v_accvgpr_read_b32 v106, a34\n"
    "v_accvgpr_read_b32 v107, a35\n"
    "v_pk_fma_f32 v[104:105], v[104:105], v[222:223], s[94:95]\n"
    "v_pk_fma_f32 v[106:107], v[106:107], v[222:223], s[94:95]\n"
    "v_cvt_pk_bf16_f32 v36, v104, v105\n"
    "v_cvt_pk_bf16_f32 v37, v106, v107\n"
    "v_accvgpr_read_b32 v108, a48\n"
    "v_accvgpr_read_b32 v109, a49\n"
    "v_accvgpr_read_b32 v110, a50\n"
    "v_accvgpr_read_b32 v111, a51\n"
    "v_pk_fma_f32 v[108:109], v[108:109], v[222:223], s[94:95]\n"
    "v_pk_fma_f32 v[110:111], v[110:111], v[222:223], s[94:95]\n"
    "v_cvt_pk_bf16_f32 v38, v108, v109\n"
    "v_cvt_pk_bf16_f32 v39, v110, v111\n"
    "buffer_store_dwordx4 v[36:39], v205, s[28:31], 0 offen offset:64 sc0 sc1\n"
    "v_accvgpr_read_b32 v96, a64\n"
    "v_accvgpr_read_b32 v97, a65\n"
    "v_accvgpr_read_b32 v98, a66\n"
    "v_accvgpr_read_b32 v99, a67\n"
    "v_pk_fma_f32 v[96:97], v[96:97], v[222:223], s[94:95]\n"
    "v_pk_fma_f32 v[98:99], v[98:99], v[222:223], s[94:95]\n"
    "v_cvt_pk_bf16_f32 v40, v96, v97\n"
    "v_cvt_pk_bf16_f32 v41, v98, v99\n"
    "v_accvgpr_read_b32 v100, a80\n"
    "v_accvgpr_read_b32 v101, a81\n"
    "v_accvgpr_read_b32 v102, a82\n"
    "v_accvgpr_read_b32 v103, a83\n"
    "v_pk_fma_f32 v[100:101], v[100:101], v[222:223], s[94:95]\n"
    "v_pk_fma_f32 v[102:103], v[102:103], v[222:223], s[94:95]\n"
    "v_cvt_pk_bf16_f32 v42, v100, v101\n"
    "v_cvt_pk_bf16_f32 v43, v102, v103\n"
    "buffer_store_dwordx4 v[40:43], v205, s[28:31], 0 offen offset:128 sc0 sc1\n"
    "v_accvgpr_read_b32 v104, a96\n"
    "v_accvgpr_read_b32 v105, a97\n"
    "v_accvgpr_read_b32 v106, a98\n"
    "v_accvgpr_read_b32 v107, a99\n"
    "v_pk_fma_f32 v[104:105], v[104:105], v[222:223], s[94:95]\n"
    "v_pk_fma_f32 v[106:107], v[106:107], v[222:223], s[94:95]\n"
    "v_cvt_pk_bf16_f32 v44, v104, v105\n"
    "v_cvt_pk_bf16_f32 v45, v106, v107\n"
    "v_accvgpr_read_b32 v108, a112\n"
    "v_accvgpr_read_b32 v109, a113\n"
    "v_accvgpr_read_b32 v110, a114\n"
    "v_accvgpr_read_b32 v111, a115\n"
    "v_pk_fma_f32 v[108:109], v[108:109], v[222:223], s[94:95]\n"
    "v_pk_fma_f32 v[110:111], v[110:111], v[222:223], s[94:95]\n"
    "v_cvt_pk_bf16_f32 v46, v108, v109\n"
    "v_cvt_pk_bf16_f32 v47, v110, v111\n"
    "buffer_store_dwordx4 v[44:47], v205, s[28:31], 0 offen offset:192 sc0 sc1\n"
    "v_accvgpr_read_b32 v96, a128\n"
    "v_accvgpr_read_b32 v97, a129\n"
    "v_accvgpr_read_b32 v98, a130\n"
    "v_accvgpr_read_b32 v99, a131\n"
    "v_pk_fma_f32 v[96:97], v[96:97], v[222:223], s[94:95]\n"
    "v_pk_fma_f32 v[98:99], v[98:99], v[222:223], s[94:95]\n"
    "v_cvt_pk_bf16_f32 v48, v96, v97\n"
    "v_cvt_pk_bf16_f32 v49, v98, v99\n"
    "v_accvgpr_read_b32 v100, a144\n"
    "v_accvgpr_read_b32 v101, a145\n"
    "v_accvgpr_read_b32 v102, a146\n"
    "v_accvgpr_read_b32 v103, a147\n"
    "v_pk_fma_f32 v[100:101], v[100:101], v[222:223], s[94:95]\n"
    "v_pk_fma_f32 v[102:103], v[102:103], v[222:223], s[94:95]\n"
    "v_cvt_pk_bf16_f32 v50, v100, v101\n"
    "v_cvt_pk_bf16_f32 v51, v102, v103\n"
    "buffer_store_dwordx4 v[48:51], v205, s[28:31], 0 offen offset:256 sc0 sc1\n"
    "v_accvgpr_read_b32 v104, a160\n"
    "v_accvgpr_read_b32 v105, a161\n"
    "v_accvgpr_read_b32 v106, a162\n"
    "v_accvgpr_read_b32 v107, a163\n"
    "v_pk_fma_f32 v[104:105], v[104:105], v[222:223], s[94:95]\n"
    "v_pk_fma_f32 v[106:107], v[106:107], v[222:223], s[94:95]\n"
    "v_cvt_pk_bf16_f32 v52, v104, v105\n"
    "v_cvt_pk_bf16_f32 v53, v106, v107\n"
    "v_accvgpr_read_b32 v108, a176\n"
    "v_accvgpr_read_b32 v109, a177\n"
    "v_accvgpr_read_b32 v110, a178\n"
    "v_accvgpr_read_b32 v111, a179\n"
    "v_pk_fma_f32 v[108:109], v[108:109], v[222:223], s[94:95]\n"
    "v_pk_fma_f32 v[110:111], v[110:111], v[222:223], s[94:95]\n"
    "v_cvt_pk_bf16_f32 v54, v108, v109\n"
    "v_cvt_pk_bf16_f32 v55, v110, v111\n"
    "buffer_store_dwordx4 v[52:55], v205, s[28:31], 0 offen offset:320 sc0 sc1\n"
    "v_accvgpr_read_b32 v96, a4\n"
    "v_accvgpr_read_b32 v97, a5\n"
    "v_accvgpr_read_b32 v98, a6\n"
    "v_accvgpr_read_b32 v99, a7\n"
    "v_pk_fma_f32 v[96:97], v[96:97], v[224:225], s[94:95]\n"
    "v_pk_fma_f32 v[98:99], v[98:99], v[224:225], s[94:95]\n"
    "v_cvt_pk_bf16_f32 v56, v96, v97\n"
    "v_cvt_pk_bf16_f32 v57, v98, v99\n"
    "v_accvgpr_read_b32 v100, a20\n"
    "v_accvgpr_read_b32 v101, a21\n"
    "v_accvgpr_read_b32 v102, a22\n"
    "v_accvgpr_read_b32 v103, a23\n"
    "v_pk_fma_f32 v[100:101], v[100:101], v[224:225], s[94:95]\n"
    "v_pk_fma_f32 v[102:103], v[102:103], v[224:225], s[94:95]\n"
    "v_cvt_pk_bf16_f32 v58, v100, v101\n"
    "v_cvt_pk_bf16_f32 v59, v102, v103\n"
    "buffer_store_dwordx4 v[56:59], v205, s[28:31], s62 offen sc0 sc1\n"
    "v_accvgpr_read_b32 v104, a36\n"
    "v_accvgpr_read_b32 v105, a37\n"
    "v_accvgpr_read_b32 v106, a38\n"
    "v_accvgpr_read_b32 v107, a39\n"
    "v_pk_fma_f32 v[104:105], v[104:105], v[224:225], s[94:95]\n"
    "v_pk_fma_f32 v[106:107], v[106:107], v[224:225], s[94:95]\n"
    "v_cvt_pk_bf16_f32 v60, v104, v105\n"
    "v_cvt_pk_bf16_f32 v61, v106, v107\n"
    "v_accvgpr_read_b32 v108, a52\n"
    "v_accvgpr_read_b32 v109, a53\n"
    "v_accvgpr_read_b32 v110, a54\n"
    "v_accvgpr_read_b32 v111, a55\n"
    "v_pk_fma_f32 v[108:109], v[108:109], v[224:225], s[94:95]\n"
    "v_pk_fma_f32 v[110:111], v[110:111], v[224:225], s[94:95]\n"
    "v_cvt_pk_bf16_f32 v62, v108, v109\n"
    "v_cvt_pk_bf16_f32 v63, v110, v111\n"
    "buffer_store_dwordx4 v[60:63], v205, s[28:31], s62 offen offset:64 sc0 sc1\n"
    "v_accvgpr_read_b32 v96, a68\n"
    "v_accvgpr_read_b32 v97, a69\n"
    "v_accvgpr_read_b32 v98, a70\n"
    "v_accvgpr_read_b32 v99, a71\n"
    "v_pk_fma_f32 v[96:97], v[96:97], v[224:225], s[94:95]\n"
    "v_pk_fma_f32 v[98:99], v[98:99], v[224:225], s[94:95]\n"
    "v_cvt_pk_bf16_f32 v32, v96, v97\n"
    "v_cvt_pk_bf16_f32 v33, v98, v99\n"
    "v_accvgpr_read_b32 v100, a84\n"
    "v_accvgpr_read_b32 v101, a85\n"
    "v_accvgpr_read_b32 v102, a86\n"
    "v_accvgpr_read_b32 v103, a87\n"
    "v_pk_fma_f32 v[100:101], v[100:101], v[224:225], s[94:95]\n"
    "v_pk_fma_f32 v[102:103], v[102:103], v[224:225], s[94:95]\n"
    "v_cvt_pk_bf16_f32 v34, v100, v101\n"
    "v_cvt_pk_bf16_f32 v35, v102, v103\n"
    "buffer_store_dwordx4 v[32:35], v205, s[28:31], s62 offen offset:128 sc0 sc1\n"
    "v_accvgpr_read_b32 v104, a100\n"
    "v_accvgpr_read_b32 v105, a101\n"
    "v_accvgpr_read_b32 v106, a102\n"
    "v_accvgpr_read_b32 v107, a103\n"
    "v_pk_fma_f32 v[104:105], v[104:105], v[224:225], s[94:95]\n"
    "v_pk_fma_f32 v[106:107], v[106:107], v[224:225], s[94:95]\n"
    "v_cvt_pk_bf16_f32 v36, v104, v105\n"
    "v_cvt_pk_bf16_f32 v37, v106, v107\n"
    "v_accvgpr_read_b32 v108, a116\n"
    "v_accvgpr_read_b32 v109, a117\n"
    "v_accvgpr_read_b32 v110, a118\n"
    "v_accvgpr_read_b32 v111, a119\n"
    "v_pk_fma_f32 v[108:109], v[108:109], v[224:225], s[94:95]\n"
    "v_pk_fma_f32 v[110:111], v[110:111], v[224:225], s[94:95]\n"
    "v_cvt_pk_bf16_f32 v38, v108, v109\n"
    "v_cvt_pk_bf16_f32 v39, v110, v111\n"
    "buffer_store_dwordx4 v[36:39], v205, s[28:31], s62 offen offset:192 sc0 sc1\n"
    "v_accvgpr_read_b32 v96, a132\n"
    "v_accvgpr_read_b32 v97, a133\n"
    "v_accvgpr_read_b32 v98, a134\n"
    "v_accvgpr_read_b32 v99, a135\n"
    "v_pk_fma_f32 v[96:97], v[96:97], v[224:225], s[94:95]\n"
    "v_pk_fma_f32 v[98:99], v[98:99], v[224:225], s[94:95]\n"
    "v_cvt_pk_bf16_f32 v40, v96, v97\n"
    "v_cvt_pk_bf16_f32 v41, v98, v99\n"
    "v_accvgpr_read_b32 v100, a148\n"
    "v_accvgpr_read_b32 v101, a149\n"
    "v_accvgpr_read_b32 v102, a150\n"
    "v_accvgpr_read_b32 v103, a151\n"
    "v_pk_fma_f32 v[100:101], v[100:101], v[224:225], s[94:95]\n"
    "v_pk_fma_f32 v[102:103], v[102:103], v[224:225], s[94:95]\n"
    "v_cvt_pk_bf16_f32 v42, v100, v101\n"
    "v_cvt_pk_bf16_f32 v43, v102, v103\n"
    "buffer_store_dwordx4 v[40:43], v205, s[28:31], s62 offen offset:256 sc0 sc1\n"
    "v_accvgpr_read_b32 v104, a164\n"
    "v_accvgpr_read_b32 v105, a165\n"
    "v_accvgpr_read_b32 v106, a166\n"
    "v_accvgpr_read_b32 v107, a167\n"
    "v_pk_fma_f32 v[104:105], v[104:105], v[224:225], s[94:95]\n"
    "v_pk_fma_f32 v[106:107], v[106:107], v[224:225], s[94:95]\n"
    "v_cvt_pk_bf16_f32 v44, v104, v105\n"
    "v_cvt_pk_bf16_f32 v45, v106, v107\n"
    "v_accvgpr_read_b32 v108, a180\n"
    "v_accvgpr_read_b32 v109, a181\n"
    "v_accvgpr_read_b32 v110, a182\n"
    "v_accvgpr_read_b32 v111, a183\n"
    "v_pk_fma_f32 v[108:109], v[108:109], v[224:225], s[94:95]\n"
    "v_pk_fma_f32 v[110:111], v[110:111], v[224:225], s[94:95]\n"
    "v_cvt_pk_bf16_f32 v46, v108, v109\n"
    "v_cvt_pk_bf16_f32 v47, v110, v111\n"
    "buffer_store_dwordx4 v[44:47], v205, s[28:31], s62 offen offset:320 sc0 sc1\n"
    "v_accvgpr_read_b32 v96, a8\n"
    "v_accvgpr_read_b32 v97, a9\n"
    "v_accvgpr_read_b32 v98, a10\n"
    "v_accvgpr_read_b32 v99, a11\n"
    "v_pk_fma_f32 v[96:97], v[96:97], v[226:227], s[94:95]\n"
    "v_pk_fma_f32 v[98:99], v[98:99], v[226:227], s[94:95]\n"
    "v_cvt_pk_bf16_f32 v48, v96, v97\n"
    "v_cvt_pk_bf16_f32 v49, v98, v99\n"
    "v_accvgpr_read_b32 v100, a24\n"
    "v_accvgpr_read_b32 v101, a25\n"
    "v_accvgpr_read_b32 v102, a26\n"
    "v_accvgpr_read_b32 v103, a27\n"
    "v_pk_fma_f32 v[100:101], v[100:101], v[226:227], s[94:95]\n"
    "v_pk_fma_f32 v[102:103], v[102:103], v[226:227], s[94:95]\n"
    "v_cvt_pk_bf16_f32 v50, v100, v101\n"
    "v_cvt_pk_bf16_f32 v51, v102, v103\n"
    "buffer_store_dwordx4 v[48:51], v205, s[28:31], s63 offen sc0 sc1\n"
    "v_accvgpr_read_b32 v104, a40\n"
    "v_accvgpr_read_b32 v105, a41\n"
    "v_accvgpr_read_b32 v106, a42\n"
    "v_accvgpr_read_b32 v107, a43\n"
    "v_pk_fma_f32 v[104:105], v[104:105], v[226:227], s[94:95]\n"
    "v_pk_fma_f32 v[106:107], v[106:107], v[226:227], s[94:95]\n"
    "v_cvt_pk_bf16_f32 v52, v104, v105\n"
    "v_cvt_pk_bf16_f32 v53, v106, v107\n"
    "v_accvgpr_read_b32 v108, a56\n"
    "v_accvgpr_read_b32 v109, a57\n"
    "v_accvgpr_read_b32 v110, a58\n"
    "v_accvgpr_read_b32 v111, a59\n"
    "v_pk_fma_f32 v[108:109], v[108:109], v[226:227], s[94:95]\n"
    "v_pk_fma_f32 v[110:111], v[110:111], v[226:227], s[94:95]\n"
    "v_cvt_pk_bf16_f32 v54, v108, v109\n"
    "v_cvt_pk_bf16_f32 v55, v110, v111\n"
    "buffer_store_dwordx4 v[52:55], v205, s[28:31], s63 offen offset:64 sc0 sc1\n"
    "v_accvgpr_read_b32 v96, a72\n"
    "v_accvgpr_read_b32 v97, a73\n"
    "v_accvgpr_read_b32 v98, a74\n"
    "v_accvgpr_read_b32 v99, a75\n"
    "v_pk_fma_f32 v[96:97], v[96:97], v[226:227], s[94:95]\n"
    "v_pk_fma_f32 v[98:99], v[98:99], v[226:227], s[94:95]\n"
    "v_cvt_pk_bf16_f32 v56, v96, v97\n"
    "v_cvt_pk_bf16_f32 v57, v98, v99\n"
    "v_accvgpr_read_b32 v100, a88\n"
    "v_accvgpr_read_b32 v101, a89\n"
    "v_accvgpr_read_b32 v102, a90\n"
    "v_accvgpr_read_b32 v103, a91\n"
    "v_pk_fma_f32 v[100:101], v[100:101], v[226:227], s[94:95]\n"
    "v_pk_fma_f32 v[102:103], v[102:103], v[226:227], s[94:95]\n"
    "v_cvt_pk_bf16_f32 v58, v100, v101\n"
    "v_cvt_pk_bf16_f32 v59, v102, v103\n"
    "buffer_store_dwordx4 v[56:59], v205, s[28:31], s63 offen offset:128 sc0 sc1\n"
    "v_accvgpr_read_b32 v104, a104\n"
    "v_accvgpr_read_b32 v105, a105\n"
    "v_accvgpr_read_b32 v106, a106\n"
    "v_accvgpr_read_b32 v107, a107\n"
    "v_pk_fma_f32 v[104:105], v[104:105], v[226:227], s[94:95]\n"
    "v_pk_fma_f32 v[106:107], v[106:107], v[226:227], s[94:95]\n"
    "v_cvt_pk_bf16_f32 v60, v104, v105\n"
    "v_cvt_pk_bf16_f32 v61, v106, v107\n"
    "v_accvgpr_read_b32 v108, a120\n"
    "v_accvgpr_read_b32 v109, a121\n"
    "v_accvgpr_read_b32 v110, a122\n"
    "v_accvgpr_read_b32 v111, a123\n"
    "v_pk_fma_f32 v[108:109], v[108:109], v[226:227], s[94:95]\n"
    "v_pk_fma_f32 v[110:111], v[110:111], v[226:227], s[94:95]\n"
    "v_cvt_pk_bf16_f32 v62, v108, v109\n"
    "v_cvt_pk_bf16_f32 v63, v110, v111\n"
    "buffer_store_dwordx4 v[60:63], v205, s[28:31], s63 offen offset:192 sc0 sc1\n"
    "v_accvgpr_read_b32 v96, a136\n"
    "v_accvgpr_read_b32 v97, a137\n"
    "v_accvgpr_read_b32 v98, a138\n"
    "v_accvgpr_read_b32 v99, a139\n"
    "v_pk_fma_f32 v[96:97], v[96:97], v[226:227], s[94:95]\n"
    "v_pk_fma_f32 v[98:99], v[98:99], v[226:227], s[94:95]\n"
    "v_cvt_pk_bf16_f32 v32, v96, v97\n"
    "v_cvt_pk_bf16_f32 v33, v98, v99\n"
    "v_accvgpr_read_b32 v100, a152\n"
    "v_accvgpr_read_b32 v101, a153\n"
    "v_accvgpr_read_b32 v102, a154\n"
    "v_accvgpr_read_b32 v103, a155\n"
    "v_pk_fma_f32 v[100:101], v[100:101], v[226:227], s[94:95]\n"
    "v_pk_fma_f32 v[102:103], v[102:103], v[226:227], s[94:95]\n"
    "v_cvt_pk_bf16_f32 v34, v100, v101\n"
    "v_cvt_pk_bf16_f32 v35, v102, v103\n"
    "buffer_store_dwordx4 v[32:35], v205, s[28:31], s63 offen offset:256 sc0 sc1\n"
    "v_accvgpr_read_b32 v104, a168\n"
    "v_accvgpr_read_b32 v105, a169\n"
    "v_accvgpr_read_b32 v106, a170\n"
    "v_accvgpr_read_b32 v107, a171\n"
    "v_pk_fma_f32 v[104:105], v[104:105], v[226:227], s[94:95]\n"
    "v_pk_fma_f32 v[106:107], v[106:107], v[226:227], s[94:95]\n"
    "v_cvt_pk_bf16_f32 v36, v104, v105\n"
    "v_cvt_pk_bf16_f32 v37, v106, v107\n"
    "v_accvgpr_read_b32 v108, a184\n"
    "v_accvgpr_read_b32 v109, a185\n"
    "v_accvgpr_read_b32 v110, a186\n"
    "v_accvgpr_read_b32 v111, a187\n"
    "v_pk_fma_f32 v[108:109], v[108:109], v[226:227], s[94:95]\n"
    "v_pk_fma_f32 v[110:111], v[110:111], v[226:227], s[94:95]\n"
    "v_cvt_pk_bf16_f32 v38, v108, v109\n"
    "v_cvt_pk_bf16_f32 v39, v110, v111\n"
    "buffer_store_dwordx4 v[36:39], v205, s[28:31], s63 offen offset:320 sc0 sc1\n"
    "v_accvgpr_read_b32 v96, a12\n"
    "v_accvgpr_read_b32 v97, a13\n"
    "v_accvgpr_read_b32 v98, a14\n"
    "v_accvgpr_read_b32 v99, a15\n"
    "v_pk_fma_f32 v[96:97], v[96:97], v[228:229], s[94:95]\n"
    "v_pk_fma_f32 v[98:99], v[98:99], v[228:229], s[94:95]\n"
    "v_cvt_pk_bf16_f32 v40, v96, v97\n"
    "v_cvt_pk_bf16_f32 v41, v98, v99\n"
    "v_accvgpr_read_b32 v100, a28\n"
    "v_accvgpr_read_b32 v101, a29\n"
    "v_accvgpr_read_b32 v102, a30\n"
    "v_accvgpr_read_b32 v103, a31\n"
    "v_pk_fma_f32 v[100:101], v[100:101], v[228:229], s[94:95]\n"
    "v_pk_fma_f32 v[102:103], v[102:103], v[228:229], s[94:95]\n"
    "v_cvt_pk_bf16_f32 v42, v100, v101\n"
    "v_cvt_pk_bf16_f32 v43, v102, v103\n"
    "buffer_store_dwordx4 v[40:43], v205, s[28:31], s64 offen sc0 sc1\n"
    "v_accvgpr_read_b32 v104, a44\n"
    "v_accvgpr_read_b32 v105, a45\n"
    "v_accvgpr_read_b32 v106, a46\n"
    "v_accvgpr_read_b32 v107, a47\n"
    "v_pk_fma_f32 v[104:105], v[104:105], v[228:229], s[94:95]\n"
    "v_pk_fma_f32 v[106:107], v[106:107], v[228:229], s[94:95]\n"
    "v_cvt_pk_bf16_f32 v44, v104, v105\n"
    "v_cvt_pk_bf16_f32 v45, v106, v107\n"
    "v_accvgpr_read_b32 v108, a60\n"
    "v_accvgpr_read_b32 v109, a61\n"
    "v_accvgpr_read_b32 v110, a62\n"
    "v_accvgpr_read_b32 v111, a63\n"
    "v_pk_fma_f32 v[108:109], v[108:109], v[228:229], s[94:95]\n"
    "v_pk_fma_f32 v[110:111], v[110:111], v[228:229], s[94:95]\n"
    "v_cvt_pk_bf16_f32 v46, v108, v109\n"
    "v_cvt_pk_bf16_f32 v47, v110, v111\n"
    "buffer_store_dwordx4 v[44:47], v205, s[28:31], s64 offen offset:64 sc0 sc1\n"
    "v_accvgpr_read_b32 v96, a76\n"
    "v_accvgpr_read_b32 v97, a77\n"
    "v_accvgpr_read_b32 v98, a78\n"
    "v_accvgpr_read_b32 v99, a79\n"
    "v_pk_fma_f32 v[96:97], v[96:97], v[228:229], s[94:95]\n"
    "v_pk_fma_f32 v[98:99], v[98:99], v[228:229], s[94:95]\n"
    "v_cvt_pk_bf16_f32 v48, v96, v97\n"
    "v_cvt_pk_bf16_f32 v49, v98, v99\n"
    "v_accvgpr_read_b32 v100, a92\n"
    "v_accvgpr_read_b32 v101, a93\n"
    "v_accvgpr_read_b32 v102, a94\n"
    "v_accvgpr_read_b32 v103, a95\n"
    "v_pk_fma_f32 v[100:101], v[100:101], v[228:229], s[94:95]\n"
    "v_pk_fma_f32 v[102:103], v[102:103], v[228:229], s[94:95]\n"
    "v_cvt_pk_bf16_f32 v50, v100, v101\n"
    "v_cvt_pk_bf16_f32 v51, v102, v103\n"
    "buffer_store_dwordx4 v[48:51], v205, s[28:31], s64 offen offset:128 sc0 sc1\n"
    "v_accvgpr_read_b32 v104, a108\n"
    "v_accvgpr_read_b32 v105, a109\n"
    "v_accvgpr_read_b32 v106, a110\n"
    "v_accvgpr_read_b32 v107, a111\n"
    "v_pk_fma_f32 v[104:105], v[104:105], v[228:229], s[94:95]\n"
    "v_pk_fma_f32 v[106:107], v[106:107], v[228:229], s[94:95]\n"
    "v_cvt_pk_bf16_f32 v52, v104, v105\n"
    "v_cvt_pk_bf16_f32 v53, v106, v107\n"
    "v_accvgpr_read_b32 v108, a124\n"
    "v_accvgpr_read_b32 v109, a125\n"
    "v_accvgpr_read_b32 v110, a126\n"
    "v_accvgpr_read_b32 v111, a127\n"
    "v_pk_fma_f32 v[108:109], v[108:109], v[228:229], s[94:95]\n"
    "v_pk_fma_f32 v[110:111], v[110:111], v[228:229], s[94:95]\n"
    "v_cvt_pk_bf16_f32 v54, v108, v109\n"
    "v_cvt_pk_bf16_f32 v55, v110, v111\n"
    "buffer_store_dwordx4 v[52:55], v205, s[28:31], s64 offen offset:192 sc0 sc1\n"
    "v_accvgpr_read_b32 v96, a140\n"
    "v_accvgpr_read_b32 v97, a141\n"
    "v_accvgpr_read_b32 v98, a142\n"
    "v_accvgpr_read_b32 v99, a143\n"
    "v_pk_fma_f32 v[96:97], v[96:97], v[228:229], s[94:95]\n"
    "v_pk_fma_f32 v[98:99], v[98:99], v[228:229], s[94:95]\n"
    "v_cvt_pk_bf16_f32 v56, v96, v97\n"
    "v_cvt_pk_bf16_f32 v57, v98, v99\n"
    "v_accvgpr_read_b32 v100, a156\n"
    "v_accvgpr_read_b32 v101, a157\n"
    "v_accvgpr_read_b32 v102, a158\n"
    "v_accvgpr_read_b32 v103, a159\n"
    "v_pk_fma_f32 v[100:101], v[100:101], v[228:229], s[94:95]\n"
    "v_pk_fma_f32 v[102:103], v[102:103], v[228:229], s[94:95]\n"
    "v_cvt_pk_bf16_f32 v58, v100, v101\n"
    "v_cvt_pk_bf16_f32 v59, v102, v103\n"
    "buffer_store_dwordx4 v[56:59], v205, s[28:31], s64 offen offset:256 sc0 sc1\n"
    "v_accvgpr_read_b32 v104, a172\n"
    "v_accvgpr_read_b32 v105, a173\n"
    "v_accvgpr_read_b32 v106, a174\n"
    "v_accvgpr_read_b32 v107, a175\n"
    "v_pk_fma_f32 v[104:105], v[104:105], v[228:229], s[94:95]\n"
    "v_pk_fma_f32 v[106:107], v[106:107], v[228:229], s[94:95]\n"
    "v_cvt_pk_bf16_f32 v60, v104, v105\n"
    "v_cvt_pk_bf16_f32 v61, v106, v107\n"
    "v_accvgpr_read_b32 v108, a188\n"
    "v_accvgpr_read_b32 v109, a189\n"
    "v_accvgpr_read_b32 v110, a190\n"
    "v_accvgpr_read_b32 v111, a191\n"
    "v_pk_fma_f32 v[108:109], v[108:109], v[228:229], s[94:95]\n"
    "v_pk_fma_f32 v[110:111], v[110:111], v[228:229], s[94:95]\n"
    "v_cvt_pk_bf16_f32 v62, v108, v109\n"
    "v_cvt_pk_bf16_f32 v63, v110, v111\n"
    "buffer_store_dwordx4 v[60:63], v205, s[28:31], s64 offen offset:320 sc0 sc1\n"
    "s_branch LFIN\n"
    "LEND:\n"
    :
    : "s"(__builtin_amdgcn_kernarg_segment_ptr()), "s"(blockIdx.x), "s"(wid)
    : "v0", "v1", "v2", "v3", "v4", "v5", "v6", "v7", "v8", "v9", "v10", "v11", "v12", "v13", "v14", "v15", "v16", "v17", "v18", "v19", "v20", "v21", "v22", "v23", "v24", "v25", "v26", "v27", "v28", "v29", "v30", "v31", "v32", "v33", "v34", "v35", "v36", "v37", "v38", "v39", "v40", "v41", "v42", "v43", "v44", "v45", "v46", "v47", "v48", "v49", "v50", "v51", "v52", "v53", "v54", "v55", "v56", "v57", "v58", "v59", "v60", "v61", "v62", "v63", "v64", "v65", "v66", "v67", "v68", "v69", "v70", "v71", "v72", "v73", "v74", "v75", "v76", "v77", "v78", "v79", "v80", "v81", "v82", "v83", "v84", "v85", "v86", "v87", "v88", "v89", "v90", "v91", "v92", "v93", "v94", "v95", "v96", "v97", "v98", "v99", "v100", "v101", "v102", "v103", "v104", "v105", "v106", "v107", "v108", "v109", "v110", "v111", "v112", "v113", "v114", "v115", "v116", "v117", "v118", "v119", "v120", "v121", "v122", "v123", "v124", "v125", "v126", "v127", "v128", "v129", "v130", "v131", "v132", "v133", "v134", "v135", "v136", "v137", "v138", "v139", "v140", "v141", "v142", "v143", "v144", "v145", "v146", "v147", "v148", "v149", "v150", "v151", "v152", "v153", "v154", "v155", "v156", "v157", "v158", "v159", "v160", "v161", "v162", "v163", "v164", "v165", "v166", "v167", "v168", "v169", "v170", "v171", "v172", "v173", "v174", "v175", "v176", "v177", "v178", "v179", "v180", "v181", "v182", "v183", "v184", "v185", "v186", "v187", "v188", "v189", "v190", "v191", "v192", "v193", "v194", "v195", "v196", "v197", "v198", "v199", "v200", "v201", "v202", "v203", "v204", "v205", "v206", "v207", "v208", "v209", "v210", "v211", "v212", "v213", "v214", "v215", "v216", "v217", "v218", "v219", "v220", "v221", "v222", "v223", "v224", "v225", "v226", "v227", "v228", "v229", "v230", "v231", "v232", "v233", "v234", "v235", "v236", "v237", "v238", "v239", "v240", "v241", "v242", "v243", "v244", "v245", "v246", "v247", "v248", "v249", "v250", "v251", "v252", "v253", "v254", "v255", "a0", "a1", "a2", "a3", "a4", "a5", "a6", "a7", "a8", "a9", "a10", "a11", "a12", "a13", "a14", "a15", "a16", "a17", "a18", "a19", "a20", "a21", "a22", "a23", "a24", "a25", "a26", "a27", "a28", "a29", "a30", "a31", "a32", "a33", "a34", "a35", "a36", "a37", "a38", "a39", "a40", "a41", "a42", "a43", "a44", "a45", "a46", "a47", "a48", "a49", "a50", "a51", "a52", "a53", "a54", "a55", "a56", "a57", "a58", "a59", "a60", "a61", "a62", "a63", "a64", "a65", "a66", "a67", "a68", "a69", "a70", "a71", "a72", "a73", "a74", "a75", "a76", "a77", "a78", "a79", "a80", "a81", "a82", "a83", "a84", "a85", "a86", "a87", "a88", "a89", "a90", "a91", "a92", "a93", "a94", "a95", "a96", "a97", "a98", "a99", "a100", "a101", "a102", "a103", "a104", "a105", "a106", "a107", "a108", "a109", "a110", "a111", "a112", "a113", "a114", "a115", "a116", "a117", "a118", "a119", "a120", "a121", "a122", "a123", "a124", "a125", "a126", "a127", "a128", "a129", "a130", "a131", "a132", "a133", "a134", "a135", "a136", "a137", "a138", "a139", "a140", "a141", "a142", "a143", "a144", "a145", "a146", "a147", "a148", "a149", "a150", "a151", "a152", "a153", "a154", "a155", "a156", "a157", "a158", "a159", "a160", "a161", "a162", "a163", "a164", "a165", "a166", "a167", "a168", "a169", "a170", "a171", "a172", "a173", "a174", "a175", "a176", "a177", "a178", "a179", "a180", "a181", "a182", "a183", "a184", "a185", "a186", "a187", "a188", "a189", "a190", "a191", "a192", "a193", "a194", "a195", "a196", "a197", "a198", "a199", "a200", "a201", "a202", "a203", "a204", "a205", "a206", "a207", "a208", "a209", "a210", "a211", "a212", "a213", "a214", "a215", "a216", "a217", "a218", "a219", "a220", "a221", "a222", "a223", "a224", "a225", "a226", "a227", "a228", "a229", "a230", "a231", "a232", "a233", "a234", "a235", "a236", "a237", "a238", "a239", "a240", "a241", "a242", "a243", "a244", "a245", "a246", "a247", "a248", "a249", "a250", "a251", "a252", "a253", "a254", "a255", "s8", "s9", "s10", "s11", "s12", "s13", "s14", "s15", "s16", "s17", "s18", "s19", "s20", "s21", "s22", "s23", "s24", "s25", "s26", "s27", "s28", "s29", "s30", "s31", "s32", "s33", "s34", "s35", "s36", "s37", "s38", "s39", "s40", "s41", "s42", "s43", "s44", "s45", "s46", "s47", "s48", "s49", "s50", "s51", "s52", "s53", "s54", "s55", "s56", "s57", "s58", "s59", "s60", "s61", "s62", "s63", "s64", "s65", "s66", "s67", "s68", "s69", "s70", "s71", "s72", "s73", "s74", "s75", "s76", "s77", "s78", "s79", "s80", "s81", "s82", "s83", "s84", "s85", "s86", "s87", "s88", "s89", "s90", "s91", "s92", "s93", "s94", "s95", "s96", "s97", "s98", "s99", "s100", "s101", "vcc", "scc", "memory");
}

extern "C" void kernel_launch(void* const* d_in, const int* in_sizes, int n_in, void* d_out, int out_size, void* d_ws, size_t ws_size,
                              hipStream_t stream) {
  (void)hipFuncSetAttribute((const void*)attn_fused, hipFuncAttributeMaxDynamicSharedMemorySize, 153088);
  attn_fused<<<256, 256, 153088, stream>>>(d_in[0], d_in[1], d_in[2], d_in[3], d_in[4], d_in[5], d_in[6], d_ws, d_out);
}
```

```cpp
#include <hip/hip_runtime.h>

__global__ __launch_bounds__(256, 1) void attn_fused(const void* q, const void* qk, const void* qv, const void* ks, const void* kz,
                                                     const void* vs, const void* vz, void* ws, void* out) {
  const int wid = __builtin_amdgcn_readfirstlane(threadIdx.x >> 6);
  asm volatile(
    "LS0:\n"
    "s_load_dwordx16 s[8:23], %0, 0x0\n"
    "s_mov_b32 s58, %2\n"
    "s_and_b32 s76, %1, 7\n"
    "s_lshr_b32 s77, %1, 3\n"
    "s_and_b32 s78, s77, 3\n"
    "s_lshl_b32 s56, s76, 2\n"
    "s_add_u32 s56, s56, s78\n"
    "s_lshr_b32 s57, s77, 2\n"
    "v_mbcnt_lo_u32_b32 v231, -1, 0\n"
    "v_mbcnt_hi_u32_b32 v231, -1, v231\n"
    "s_getpc_b64 s[44:45]\n"
    "LPC:\n"
    "s_mov_b32 s46, LSLOW-LPC\n"
    "s_mov_b32 s47, 0x20000\n"
    "s_lshr_b32 s76, %1, 3\n"
    "s_lshl_b32 s76, s76, 2\n"
    "s_add_u32 s76, s76, s58\n"
    "s_lshl_b32 s76, s76, 9\n"
    "v_lshlrev_b32 v196, 3, v231\n"
    "buffer_load_dword v251, v196, s[44:47], s76 offen\n"
    "s_mul_i32 s72, s58, 0xc00\n"
    "s_lshr_b32 s86, s57, 2\n"
    "s_mul_i32 s64, s86, 0xc000\n"
    "s_lshl_b32 s86, s86, 3\n"
    "s_and_b32 s79, s57, 3\n"
    "s_add_u32 s86, s86, s79\n"
    "s_lshl_b32 s86, s86, 2\n"
    "s_xor_b32 s76, s57, 4\n"
    "s_and_b32 s77, s76, 6\n"
    "s_mul_i32 s77, s77, 15\n"
    "s_add_u32 s76, s76, s77\n"
    "s_and_b32 s77, s58, 1\n"
    "s_mul_i32 s78, s77, 12\n"
    "s_add_u32 s78, s78, s86\n"
    "s_lshl_b32 s78, s78, 1\n"
    "s_mul_i32 s77, s77, 0xc0\n"
    "s_movk_i32 s90, 0x556\n"
    "s_lshl_b32 s60, s56, 12\n"
    "s_cmp_lt_u32 s58, 2\n"
    "s_cselect_b32 s76, s76, s78\n"
    "s_cmovk_i32 s90, 0xaab\n"
    "s_cselect_b32 s91, 24, 0\n"
    "s_cselect_b32 s92, 1, 10\n"
    "s_cselect_b32 s89, 7, 0\n"
    "s_cselect_b32 s64, s64, 0\n"
    "s_add_u32 s60, s60, s76\n"
    "s_mul_i32 s60, s60, 0x180\n"
    "s_mul_i32 s76, s76, 0x180\n"
    "s_sub_u32 s34, 0x180000, s76\n"
    "s_mov_b32 s35, 0x20000\n"
    "v_add_u32 v144, s77, v231\n"
    "v_add_u32 v145, 64, v144\n"
    "v_add_u32 v146, 64, v145\n"
    "v_mul_u32_u24 v197, s90, v144\n"
    "v_lshrrev_b32 v197, 16, v197\n"
    "v_mul_u32_u24 v198, s92, v197\n"
    "v_lshrrev_b32 v198, 1, v198\n"
    "v_and_b32 v198, s89, v198\n"
    "v_xor_b32 v198, v198, v144\n"
    "v_mul_u32_u24 v197, s91, v197\n"
    "v_add_lshl_u32 v202, v197, v198, 4\n"
    "v_mul_u32_u24 v197, s90, v145\n"
    "v_lshrrev_b32 v197, 16, v197\n"
    "v_mul_u32_u24 v198, s92, v197\n"
    "v_lshrrev_b32 v198, 1, v198\n"
    "v_and_b32 v198, s89, v198\n"
    "v_xor_b32 v198, v198, v144\n"
    "v_mul_u32_u24 v197, s91, v197\n"
    "v_add_lshl_u32 v203, v197, v198, 4\n"
    "v_mul_u32_u24 v197, s90, v146\n"
    "v_lshrrev_b32 v197, 16, v197\n"
    "v_mul_u32_u24 v198, s92, v197\n"
    "v_lshrrev_b32 v198, 1, v198\n"
    "v_and_b32 v198, s89, v198\n"
    "v_xor_b32 v198, v198, v144\n"
    "v_mul_u32_u24 v197, s91, v197\n"
    "v_add_lshl_u32 v204, v197, v198, 4\n"
    "s_waitcnt lgkmcnt(0)\n"
    "s_cmp_lt_u32 s58, 2\n"
    "s_cselect_b64 s[80:81], s[10:11], s[12:13]\n"
    "s_cselect_b64 s[82:83], s[16:17], s[14:15]\n"
    "s_add_u32 s32, s80, s60\n"
    "s_addc_u32 s33, s81, 0\n"
    "s_cmp_eq_u32 s64, 0\n"
    "s_cbranch_scc1 LNOR\n"
    "s_add_u32 m0, s72, 0x12000\n"
    "s_nop 0\n"
    "buffer_load_dwordx4 v202, s[32:35], 0 offen lds\n"
    "buffer_load_dwordx4 v203, s[32:35], 0 offen offset:1024 lds\n"
    "buffer_load_dwordx4 v204, s[32:35], 0 offen offset:2048 lds\n"
    "LNOR:\n"
    "s_add_u32 m0, s72, 0x15000\n"
    "s_nop 0\n"
    "buffer_load_dwordx4 v202, s[32:35], s64 offen lds\n"
    "buffer_load_dwordx4 v203, s[32:35], s64 offen offset:1024 lds\n"
    "buffer_load_dwordx4 v204, s[32:35], s64 offen offset:2048 lds\n"
    "LR0:\n"
    "s_load_dwordx2 s[24:25], %0, 0x40\n"
    "s_mul_i32 s71, s58, 0x1800\n"
    "s_lshr_b32 s87, s57, 2\n"
    "s_mul_i32 s87, s87, 0x6000\n"
    "s_mul_i32 s61, s56, 0x3000\n"
    "s_lshl_b32 s62, s56, 12\n"
    "s_lshl_b32 s59, s56, 12\n"
    "s_mov_b32 s50, 0x3000\n"
    "s_mov_b32 s51, 0x20000\n"
    "s_and_b32 s77, s57, 1\n"
    "s_mul_i32 s77, s77, 0xc0\n"
    "v_mul_u32_u24 v197, 0x1556, v144\n"
    "v_lshrrev_b32 v197, 16, v197\n"
    "v_mul_u32_u24 v196, 12, v197\n"
    "v_sub_u32 v196, v144, v196\n"
    "v_mul_u32_u24 v248, 0x180, v197\n"
    "v_lshl_add_u32 v248, v196, 4, v248\n"
    "v_add_u32 v248, s77, v248\n"
    "s_add_u32 s48, s82, s61\n"
    "s_addc_u32 s49, s83, 0\n"
    "s_add_u32 m0, s72, 0x21000\n"
    "s_nop 0\n"
    "buffer_load_dwordx4 v248, s[48:51], 0 offen lds\n"
    "s_add_u32 s40, s20, s62\n"
    "s_addc_u32 s41, s21, 0\n"
    "s_add_u32 s52, s18, s62\n"
    "s_addc_u32 s53, s19, 0\n"
    "s_mov_b32 s42, 0x1000\n"
    "s_mov_b32 s43, 0x20000\n"
    "s_mov_b32 s54, 0x1000\n"
    "s_mov_b32 s55, 0x20000\n"
    "v_lshlrev_b32 v196, 1, v231\n"
    "buffer_load_ushort v44, v196, s[40:43], 0 offen\n"
    "buffer_load_ushort v45, v196, s[52:55], 0 offen\n"
    "s_lshl_b32 s77, s58, 6\n"
    "v_add_u32 v41, s77, v231\n"
    "v_bfe_u32 v197, v231, 2, 1\n"
    "v_mul_u32_u24 v197, 12, v197\n"
    "v_and_b32 v198, 7, v231\n"
    "v_add3_u32 v197, v197, v198, s86\n"
    "v_lshrrev_b32 v40, 3, v41\n"
    "v_lshlrev_b32 v40, 7, v40\n"
    "v_lshl_add_u32 v40, v197, 1, v40\n"
    "buffer_load_ushort v42, v40, s[40:43], 0 offen\n"
    "buffer_load_ushort v43, v40, s[52:55], 0 offen\n"
    "s_add_u32 s44, s22, s59\n"
    "s_addc_u32 s45, s23, 0\n"
    "s_mov_b32 s46, 0x900\n"
    "s_mov_b32 s47, 0x20000\n"
    "v_mov_b32 v196, 0x40000000\n"
    "s_cmp_eq_u32 s58, 0\n"
    "s_cselect_b32 s77, 64, 0\n"
    "s_cselect_b32 s74, 1, 0\n"
    "v_and_b32 v213, 7, v231\n"
    "v_mul_u32_u24 v213, 0x100, v213\n"
    "v_cmp_gt_u32 vcc, s77, v231\n"
    "v_mov_b32 v197, 0x800\n"
    "s_mul_i32 s77, s57, 0x100\n"
    "v_cndmask_b32 v213, v196, v213, vcc\n"
    "v_cndmask_b32 v197, v196, v197, vcc\n"
    "buffer_load_dword v221, v197, s[44:47], 0 offen sc1\n"
    "v_cmp_gt_u32 vcc, s74, v231\n"
    "v_mov_b32 v214, s77\n"
    "s_mul_i32 s66, s74, 3\n"
    "v_cndmask_b32 v214, v196, v214, vcc\n"
    "s_lshl_b32 s76, s56, 3\n"
    "s_add_u32 s76, s76, s57\n"
    "s_mul_i32 s76, s76, 0x18000\n"
    "s_add_u32 s28, s8, s76\n"
    "s_addc_u32 s29, s9, 0\n"
    "s_mov_b32 s30, 0x18000\n"
    "s_mov_b32 s31, 0x20000\n"
    "s_mul_i32 s88, s58, 0x6000\n"
    "v_mul_u32_u24 v197, 0xaab, v231\n"
    "v_lshrrev_b32 v197, 16, v197\n"
    "v_mul_u32_u24 v196, 24, v197\n"
    "v_sub_u32 v196, v231, v196\n"
    "v_mul_u32_u24 v152, 0x180, v197\n"
    "v_lshrrev_b32 v197, 1, v197\n"
    "v_xor_b32 v196, v196, v197\n"
    "v_lshl_add_u32 v152, v196, 4, v152\n"
    "v_add_u32 v198, 64, v231\n"
    "v_mul_u32_u24 v197, 0xaab, v198\n"
    "v_lshrrev_b32 v197, 16, v197\n"
    "v_mul_u32_u24 v196, 24, v197\n"
    "v_sub_u32 v196, v198, v196\n"
    "v_mul_u32_u24 v153, 0x180, v197\n"
    "v_lshrrev_b32 v197, 1, v197\n"
    "v_xor_b32 v196, v196, v197\n"
    "v_lshl_add_u32 v153, v196, 4, v153\n"
    "v_add_u32 v198, 0x80, v231\n"
    "v_mul_u32_u24 v197, 0xaab, v198\n"
    "v_lshrrev_b32 v197, 16, v197\n"
    "v_mul_u32_u24 v196, 24, v197\n"
    "v_sub_u32 v196, v198, v196\n"
    "v_mul_u32_u24 v154, 0x180, v197\n"
    "v_lshrrev_b32 v197, 1, v197\n"
    "v_xor_b32 v196, v196, v197\n"
    "v_lshl_add_u32 v154, v196, 4, v154\n"
    "s_mov_b32 s76, 0xc00\n"
    "v_xad_u32 v155, v152, 64, s76\n"
    "v_xad_u32 v156, v153, 64, s76\n"
    "v_xad_u32 v157, v154, 64, s76\n"
    "s_add_u32 s78, s88, 0\n"
    "s_add_u32 m0, s71, 0\n"
    "s_nop 0\n"
    "buffer_load_dwordx4 v152, s[28:31], s78 offen nt lds\n"
    "s_add_u32 s78, s88, 0\n"
    "s_add_u32 m0, s71, 0x400\n"
    "s_nop 0\n"
    "buffer_load_dwordx4 v153, s[28:31], s78 offen nt lds\n"
    "s_add_u32 s78, s64, 0xc000\n"
    "s_add_u32 m0, s72, 0x1e000\n"
    "s_nop 0\n"
    "buffer_load_dwordx4 v202, s[32:35], s78 offen lds\n"
    "buffer_load_dwordx4 v203, s[32:35], s78 offen offset:1024 lds\n"
    "buffer_load_dwordx4 v204, s[32:35], s78 offen offset:2048 lds\n"
    "v_and_b32 v46, 15, v231\n"
    "v_lshrrev_b32 v47, 4, v231\n"
    "s_mov_b32 s75, 0x3dd53b95\n"
    "s_min_u32 s78, s58, 2\n"
    "s_lshl_b32 s76, s78, 3\n"
    "v_lshl_add_u32 v197, v47, 1, s76\n"
    "v_lshrrev_b32 v198, 1, v46\n"
    "v_xor_b32 v197, v197, v198\n"
    "v_mul_u32_u24 v205, 0x180, v46\n"
    "v_lshl_add_u32 v205, v197, 4, v205\n"
    "v_add_u32 v205, 0x12000, v205\n"
    "v_xor_b32 v206, 16, v205\n"
    "v_and_b32 v197, 7, v231\n"
    "v_lshl_add_u32 v209, v47, 3, v197\n"
    "s_lshl_b32 s76, s78, 5\n"
    "v_add_lshl_u32 v209, v209, s76, 1\n"
    "v_add_u32 v217, 0x21000, v209\n"
    "s_lshr_b32 s76, s57, 2\n"
    "s_mul_i32 s76, s76, 0xc0\n"
    "s_add_u32 s76, s76, 0x20f40\n"
    "v_add_u32 v209, s76, v209\n"
    "v_lshlrev_b32 v210, 3, v197\n"
    "v_add_u32 v210, 0x24400, v210\n"
    "s_lshr_b32 s76, s57, 1\n"
    "s_xor_b32 s76, s76, 2\n"
    "s_mul_i32 s76, s76, 0x1800\n"
    "s_and_b32 s77, s57, 1\n"
    "s_mul_i32 s77, s77, 0xc00\n"
    "s_add_u32 s76, s76, s77\n"
    "s_lshl_b32 s77, s58, 10\n"
    "s_add_u32 s76, s76, s77\n"
    "s_cmp_eq_u32 s58, 3\n"
    "s_cselect_b32 s76, 0x40000000, s76\n"
    "v_lshl_add_u32 v211, v231, 4, s76\n"
    "s_max_u32 s78, s58, 1\n"
    "s_lshl_b32 s78, s78, 6\n"
    "s_sub_u32 s78, s78, 64\n"
    "v_add_u32 v198, s78, v231\n"
    "v_lshlrev_b32 v207, 2, v198\n"
    "v_add_u32 v207, 0x13800, v207\n"
    "s_lshr_b32 s76, s57, 2\n"
    "s_lshl_b32 s76, s76, 10\n"
    "s_lshl_b32 s77, s79, 8\n"
    "s_add_u32 s76, s76, s77\n"
    "s_cmp_eq_u32 s58, 0\n"
    "s_cselect_b32 s76, 0x40000000, s76\n"
    "v_lshrrev_b32 v197, 5, v198\n"
    "v_lshl_add_u32 v212, v197, 12, s76\n"
    "v_bfe_u32 v197, v198, 2, 1\n"
    "v_lshl_add_u32 v212, v197, 11, v212\n"
    "v_bfe_u32 v197, v198, 3, 2\n"
    "v_lshl_add_u32 v212, v197, 6, v212\n"
    "v_and_b32 v197, 3, v198\n"
    "v_lshl_add_u32 v212, v197, 4, v212\n"
    "v_mov_b32 v222, 0\n"
    "v_mov_b32 v223, 0\n"
    "v_mov_b32 v224, 0\n"
    "v_mov_b32 v225, 0\n"
    "v_mov_b32 v160, 0\n"
    "v_mov_b32 v161, 0\n"
    "v_mov_b32 v162, 0\n"
    "v_mov_b32 v163, 0\n"
    "v_mov_b32 v164, 0\n"
    "v_mov_b32 v165, 0\n"
    "v_mov_b32 v172, 0\n"
    "v_mov_b32 v173, 0\n"
    "v_mov_b32 v174, 0\n"
    "v_mov_b32 v175, 0\n"
    "v_lshlrev_b32 v41, 3, v41\n"
    "v_add_u32 v41, 0x24400, v41\n"
    "s_mul_i32 s76, s56, 0xc0000\n"
    "s_add_u32 s77, s76, 0x40000\n"
    "s_add_u32 s36, s22, s77\n"
    "s_addc_u32 s37, s23, 0\n"
    "s_mov_b32 s38, 0xc0000\n"
    "s_mov_b32 s39, 0x20000\n"
    "s_cmp_eq_u32 s87, 0\n"
    "s_cselect_b32 s62, 0x40000000, 0\n"
    "v_lshlrev_b32 v200, 4, v231\n"
    "v_add_u32 v201, 0xc000, v200\n"
    "v_mul_u32_u24 v198, 0x180, v46\n"
    "v_add_u32 v198, s71, v198\n"
    "v_lshrrev_b32 v197, 1, v46\n"
    "v_xor_b32 v197, v197, v47\n"
    "v_lshl_add_u32 v158, v197, 4, v198\n"
    "v_xor_b32 v159, 64, v158\n"
    "v_and_b32 v197, 3, v231\n"
    "v_cmp_eq_u32 vcc, 0, v197\n"
    "v_mov_b32 v198, 0x3f803f80\n"
    "s_nop 0\n"
    "v_cndmask_b32 v192, 0, v198, vcc\n"
    "v_cndmask_b32 v193, 0, v198, vcc\n"
    "v_cndmask_b32 v194, 0, v198, vcc\n"
    "v_cndmask_b32 v195, 0, v198, vcc\n"
    "s_and_b32 s77, s57, 1\n"
    "s_mul_i32 s77, s77, 0xc0\n"
    "s_sub_u32 s77, s77, 0x400\n"
    "v_mul_u32_u24 v197, 0x1556, v145\n"
    "v_lshrrev_b32 v197, 16, v197\n"
    "v_mul_u32_u24 v196, 12, v197\n"
    "v_sub_u32 v196, v145, v196\n"
    "v_mul_u32_u24 v249, 0x180, v197\n"
    "v_lshl_add_u32 v249, v196, 4, v249\n"
    "v_add_u32 v249, s77, v249\n"
    "s_and_b32 s77, s57, 1\n"
    "s_mul_i32 s77, s77, 0xc0\n"
    "s_sub_u32 s77, s77, 0x800\n"
    "v_mul_u32_u24 v197, 0x1556, v146\n"
    "v_lshrrev_b32 v197, 16, v197\n"
    "v_mul_u32_u24 v196, 12, v197\n"
    "v_sub_u32 v196, v146, v196\n"
    "v_mul_u32_u24 v250, 0x180, v197\n"
    "v_lshl_add_u32 v250, v196, 4, v250\n"
    "v_add_u32 v250, s77, v250\n"
    "s_waitcnt vmcnt(9)\n"
    "v_lshlrev_b32 v44, 16, v44\n"
    "s_waitcnt vmcnt(8)\n"
    "v_lshlrev_b32 v45, 16, v45\n"
    "v_mul_f32 v197, v44, v45\n"
    "s_nop 1\n"
    "v_min_f32_dpp v197, v197, v197 row_ror:8 row_mask:0xf bank_mask:0xf\n"
    "s_mul_i32 s76, s56, 0xc0000\n"
    "s_add_u32 s77, s76, 0x1840000\n"
    "v_min_f32_dpp v197, v197, v197 row_ror:4 row_mask:0xf bank_mask:0xf\n"
    "s_add_u32 s40, s22, s77\n"
    "s_addc_u32 s41, s23, 0\n"
    "v_min_f32_dpp v197, v197, v197 row_ror:2 row_mask:0xf bank_mask:0xf\n"
    "s_mov_b32 s42, 0xc0000\n"
    "s_mov_b32 s43, 0x20000\n"
    "v_min_f32_dpp v197, v197, v197 row_ror:1 row_mask:0xf bank_mask:0xf\n"
    "v_mov_b32 v198, v197\n"
    "s_nop 1\n"
    "v_permlane32_swap_b32 v197, v198\n"
    "v_min_f32 v197, v197, v198\n"
    "v_mov_b32 v198, v197\n"
    "s_nop 1\n"
    "v_permlane16_swap_b32 v197, v198\n"
    "v_min_f32 v197, v197, v198\n"
    "v_rcp_f32 v198, v197\n"
    "v_subrev_u32 v199, 0x37800000, v197\n"
    "v_mul_f32 v197, 0x3fcccccd, v197\n"
    "v_cmp_gt_u32 vcc, 0x10000000, v199\n"
    "v_mul_f32 v198, 0x3f3425ed, v198\n"
    "v_mov_b32 v199, 0x3f800000\n"
    "v_cndmask_b32 v197, 0, v197, vcc\n"
    "v_cndmask_b32 v198, v199, v198, vcc\n"
    "v_readfirstlane_b32 s70, v197\n"
    "v_mul_f32 v197, v197, v198\n"
    "v_readfirstlane_b32 s68, v198\n"
    "v_readfirstlane_b32 s69, v197\n"
    "s_waitcnt vmcnt(7)\n"
    "v_lshlrev_b32 v42, 16, v42\n"
    "s_waitcnt vmcnt(6)\n"
    "v_lshlrev_b32 v43, 16, v43\n"
    "v_mul_f32 v44, s68, v43\n"
    "v_fma_f32 v45, -v42, v44, s69\n"
    "ds_write_b64 v41, v[44:45]\n"
    "s_add_u32 s78, s88, 0\n"
    "s_add_u32 m0, s71, 0x800\n"
    "s_nop 0\n"
    "buffer_load_dwordx4 v154, s[28:31], s78 offen nt lds\n"
    "s_add_u32 s78, s88, 0\n"
    "s_add_u32 m0, s71, 0xc00\n"
    "s_nop 0\n"
    "buffer_load_dwordx4 v155, s[28:31], s78 offen nt lds\n"
    "s_waitcnt lgkmcnt(0)\n"
    "s_barrier\n"
    "ds_read_u16_d16_hi v172, v217 offset:6144\n"
    "ds_read_u16_d16_hi v173, v217\n"
    "ds_read_u16_d16_hi v174, v217 offset:6336\n"
    "ds_read_u16_d16_hi v175, v217 offset:192\n"
    "ds_read_b64 v[228:229], v210\n"
    "ds_read2st64_b32 v[240:241], v207 offset0:48 offset1:51\n"
    "ds_read2st64_b32 v[242:243], v207 offset0:54 offset1:57\n"
    "ds_read2st64_b32 v[244:245], v207 offset0:60 offset1:63\n"
    "ds_read2st64_b32 v[246:247], v207 offset0:66 offset1:69\n"
    "ds_read_u16_d16_hi v223, v209 offset:6144\n"
    "ds_read_u16_d16_hi v222, v209\n"
    "ds_read_b128 v[232:235], v205\n"
    "ds_read_b128 v[236:239], v206\n"
    "ds_read_u16_d16_hi v161, v209 offset:6336\n"
    "ds_read_u16_d16_hi v160, v209 offset:192\n"
    "s_waitcnt lgkmcnt(11)\n"
    "ds_read_b128 v[64:67], v205 offset:12288\n"
    "ds_read_b128 v[68:71], v206 offset:12288\n"
    "v_mul_f32 v197, v172, v173\n"
    "v_mul_f32 v198, v174, v175\n"
    "v_min_f32 v197, v197, v198\n"
    "v_max_f32 v197, 0, v197\n"
    "v_mul_f32 v217, 0x3e3fe8d2, v197\n"
    "s_waitcnt lgkmcnt(7)\n"
    "v_mul_f32 v226, s75, v223\n"
    "s_waitcnt lgkmcnt(6)\n"
    "v_fma_f32 v227, -v222, v226, v217\n"
    "s_waitcnt lgkmcnt(5)\n"
    "v_cvt_f32_i32 v232, v232\n"
    "v_cvt_f32_i32 v233, v233\n"
    "v_cvt_f32_i32 v234, v234\n"
    "v_cvt_f32_i32 v235, v235\n"
    "s_waitcnt lgkmcnt(4)\n"
    "v_cvt_f32_i32 v236, v236\n"
    "v_cvt_f32_i32 v237, v237\n"
    "v_cvt_f32_i32 v238, v238\n"
    "v_cvt_f32_i32 v239, v239\n"
    "v_mul_f32_dpp v232, v226, v232 row_newbcast:0 row_mask:0xf bank_mask:0xf\n"
    "v_mul_f32_dpp v233, v226, v233 row_newbcast:1 row_mask:0xf bank_mask:0xf\n"
    "v_mul_f32_dpp v234, v226, v234 row_newbcast:2 row_mask:0xf bank_mask:0xf\n"
    "v_mul_f32_dpp v235, v226, v235 row_newbcast:3 row_mask:0xf bank_mask:0xf\n"
    "v_mul_f32_dpp v236, v226, v236 row_newbcast:4 row_mask:0xf bank_mask:0xf\n"
    "v_mul_f32_dpp v237, v226, v237 row_newbcast:5 row_mask:0xf bank_mask:0xf\n"
    "v_mul_f32_dpp v238, v226, v238 row_newbcast:6 row_mask:0xf bank_mask:0xf\n"
    "v_mul_f32_dpp v239, v226, v239 row_newbcast:7 row_mask:0xf bank_mask:0xf\n"
    "v_add_f32_dpp v232, v227, v232 row_newbcast:0 row_mask:0xf bank_mask:0xf\n"
    "v_add_f32_dpp v233, v227, v233 row_newbcast:1 row_mask:0xf bank_mask:0xf\n"
    "v_add_f32_dpp v234, v227, v234 row_newbcast:2 row_mask:0xf bank_mask:0xf\n"
    "v_add_f32_dpp v235, v227, v235 row_newbcast:3 row_mask:0xf bank_mask:0xf\n"
    "v_add_f32_dpp v236, v227, v236 row_newbcast:4 row_mask:0xf bank_mask:0xf\n"
    "v_add_f32_dpp v237, v227, v237 row_newbcast:5 row_mask:0xf bank_mask:0xf\n"
    "v_add_f32_dpp v238, v227, v238 row_newbcast:6 row_mask:0xf bank_mask:0xf\n"
    "v_add_f32_dpp v239, v227, v239 row_newbcast:7 row_mask:0xf bank_mask:0xf\n"
    "v_cvt_pk_bf16_f32 v232, v232, v233\n"
    "v_cvt_pk_bf16_f32 v233, v234, v235\n"
    "v_cvt_pk_bf16_f32 v234, v236, v237\n"
    "v_cvt_pk_bf16_f32 v235, v238, v239\n"
    "buffer_store_dwordx4 v[232:235], v211, s[36:39], s62 offen sc1\n"
    "v_cvt_f32_i32 v240, v240\n"
    "v_cvt_f32_i32 v241, v241\n"
    "v_cvt_f32_i32 v242, v242\n"
    "v_cvt_f32_i32 v243, v243\n"
    "v_cvt_f32_i32 v244, v244\n"
    "v_cvt_f32_i32 v245, v245\n"
    "v_cvt_f32_i32 v246, v246\n"
    "v_cvt_f32_i32 v247, v247\n"
    "v_mul_f32_dpp v240, v228, v240 row_newbcast:0 row_mask:0xf bank_mask:0xf\n"
    "v_mul_f32_dpp v241, v228, v241 row_newbcast:1 row_mask:0xf bank_mask:0xf\n"
    "v_mul_f32_dpp v242, v228, v242 row_newbcast:2 row_mask:0xf bank_mask:0xf\n"
    "v_mul_f32_dpp v243, v228, v243 row_newbcast:3 row_mask:0xf bank_mask:0xf\n"
    "v_mul_f32_dpp v244, v228, v244 row_newbcast:4 row_mask:0xf bank_mask:0xf\n"
    "v_mul_f32_dpp v245, v228, v245 row_newbcast:5 row_mask:0xf bank_mask:0xf\n"
    "v_mul_f32_dpp v246, v228, v246 row_newbcast:6 row_mask:0xf bank_mask:0xf\n"
    "v_mul_f32_dpp v247, v228, v247 row_newbcast:7 row_mask:0xf bank_mask:0xf\n"
    "v_add_f32_dpp v240, v229, v240 row_newbcast:0 row_mask:0xf bank_mask:0xf\n"
    "v_add_f32_dpp v241, v229, v241 row_newbcast:1 row_mask:0xf bank_mask:0xf\n"
    "v_add_f32_dpp v242, v229, v242 row_newbcast:2 row_mask:0xf bank_mask:0xf\n"
    "v_add_f32_dpp v243, v229, v243 row_newbcast:3 row_mask:0xf bank_mask:0xf\n"
    "v_add_f32_dpp v244, v229, v244 row_newbcast:4 row_mask:0xf bank_mask:0xf\n"
    "v_add_f32_dpp v245, v229, v245 row_newbcast:5 row_mask:0xf bank_mask:0xf\n"
    "v_add_f32_dpp v246, v229, v246 row_newbcast:6 row_mask:0xf bank_mask:0xf\n"
    "v_add_f32_dpp v247, v229, v247 row_newbcast:7 row_mask:0xf bank_mask:0xf\n"
    "v_cvt_pk_bf16_f32 v240, v240, v241\n"
    "v_cvt_pk_bf16_f32 v241, v242, v243\n"
    "v_cvt_pk_bf16_f32 v242, v244, v245\n"
    "v_cvt_pk_bf16_f32 v243, v246, v247\n"
    "s_mov_b32 s78, 0\n"
    "buffer_store_dwordx4 v[240:243], v212, s[40:43], s78 offen sc1\n"
    "s_waitcnt lgkmcnt(3)\n"
    "v_mul_f32 v166, s75, v161\n"
    "s_waitcnt lgkmcnt(2)\n"
    "v_fma_f32 v167, -v160, v166, v217\n"
    "s_waitcnt lgkmcnt(1)\n"
    "v_cvt_f32_i32 v64, v64\n"
    "v_cvt_f32_i32 v65, v65\n"
    "v_cvt_f32_i32 v66, v66\n"
    "v_cvt_f32_i32 v67, v67\n"
    "s_waitcnt lgkmcnt(0)\n"
    "v_cvt_f32_i32 v68, v68\n"
    "v_cvt_f32_i32 v69, v69\n"
    "v_cvt_f32_i32 v70, v70\n"
    "v_cvt_f32_i32 v71, v71\n"
    "v_mul_f32_dpp v64, v166, v64 row_newbcast:0 row_mask:0xf bank_mask:0xf\n"
    "v_mul_f32_dpp v65, v166, v65 row_newbcast:1 row_mask:0xf bank_mask:0xf\n"
    "v_mul_f32_dpp v66, v166, v66 row_newbcast:2 row_mask:0xf bank_mask:0xf\n"
    "v_mul_f32_dpp v67, v166, v67 row_newbcast:3 row_mask:0xf bank_mask:0xf\n"
    "v_mul_f32_dpp v68, v166, v68 row_newbcast:4 row_mask:0xf bank_mask:0xf\n"
    "v_mul_f32_dpp v69, v166, v69 row_newbcast:5 row_mask:0xf bank_mask:0xf\n"
    "v_mul_f32_dpp v70, v166, v70 row_newbcast:6 row_mask:0xf bank_mask:0xf\n"
    "v_mul_f32_dpp v71, v166, v71 row_newbcast:7 row_mask:0xf bank_mask:0xf\n"
    "v_add_f32_dpp v64, v167, v64 row_newbcast:0 row_mask:0xf bank_mask:0xf\n"
    "v_add_f32_dpp v65, v167, v65 row_newbcast:1 row_mask:0xf bank_mask:0xf\n"
    "v_add_f32_dpp v66, v167, v66 row_newbcast:2 row_mask:0xf bank_mask:0xf\n"
    "v_add_f32_dpp v67, v167, v67 row_newbcast:3 row_mask:0xf bank_mask:0xf\n"
    "v_add_f32_dpp v68, v167, v68 row_newbcast:4 row_mask:0xf bank_mask:0xf\n"
    "v_add_f32_dpp v69, v167, v69 row_newbcast:5 row_mask:0xf bank_mask:0xf\n"
    "v_add_f32_dpp v70, v167, v70 row_newbcast:6 row_mask:0xf bank_mask:0xf\n"
    "v_add_f32_dpp v71, v167, v71 row_newbcast:7 row_mask:0xf bank_mask:0xf\n"
    "v_cvt_pk_bf16_f32 v64, v64, v65\n"
    "v_cvt_pk_bf16_f32 v65, v66, v67\n"
    "v_cvt_pk_bf16_f32 v66, v68, v69\n"
    "v_cvt_pk_bf16_f32 v67, v70, v71\n"
    "s_add_u32 s78, s87, 0\n"
    "buffer_store_dwordx4 v[64:67], v211, s[36:39], s78 offen sc1\n"
    "s_waitcnt vmcnt(10)\n"
    "v_add_u32 v221, s74, v221\n"
    "s_mov_b32 s73, 0\n"
    "v_readfirstlane_b32 s67, v221\n"
    "s_mul_i32 s66, s74, 2\n"
    "s_waitcnt vmcnt(0)\n"
    "s_barrier\n"
    "v_mov_b32 v220, 2\n"
    "buffer_store_dwordx2 v[220:221], v214, s[44:47], 0 offen sc1\n"
    "s_add_u32 s78, s64, 0x18000\n"
    "s_add_u32 m0, s72, 0x1b000\n"
    "s_nop 0\n"
    "buffer_load_dwordx4 v202, s[32:35], s78 offen lds\n"
    "buffer_load_dwordx4 v203, s[32:35], s78 offen offset:1024 lds\n"
    "buffer_load_dwordx4 v204, s[32:35], s78 offen offset:2048 lds\n"
    "s_add_u32 s78, s88, 0\n"
    "s_add_u32 m0, s71, 0x1000\n"
    "s_nop 0\n"
    "buffer_load_dwordx4 v156, s[28:31], s78 offen nt lds\n"
    "s_add_u32 s78, s88, 0\n"
    "s_add_u32 m0, s71, 0x1400\n"
    "s_nop 0\n"
    "buffer_load_dwordx4 v157, s[28:31], s78 offen nt lds\n"
    "s_add_u32 s78, s88, 0x1800\n"
    "s_add_u32 m0, s71, 0x6000\n"
    "s_nop 0\n"
    "buffer_load_dwordx4 v152, s[28:31], s78 offen nt lds\n"
    "s_add_u32 s78, s88, 0x1800\n"
    "s_add_u32 m0, s71, 0x6400\n"
    "s_nop 0\n"
    "buffer_load_dwordx4 v153, s[28:31], s78 offen nt lds\n"
    "s_barrier\n"
    "ds_read_u16_d16_hi v163, v209 offset:6528\n"
    "ds_read_u16_d16_hi v162, v209 offset:384\n"
    "ds_read_b128 v[72:75], v205 offset:49152\n"
    "ds_read_b128 v[76:79], v206 offset:49152\n"
    "ds_read_b64 v[170:171], v210 offset:64\n"
    "ds_read2st64_b32 v[80:81], v207 offset0:192 offset1:195\n"
    "ds_read2st64_b32 v[82:83], v207 offset0:198 offset1:201\n"
    "ds_read2st64_b32 v[84:85], v207 offset0:204 offset1:207\n"
    "ds_read2st64_b32 v[86:87], v207 offset0:210 offset1:213\n"
    "s_waitcnt lgkmcnt(8)\n"
    "v_mul_f32 v168, s75, v163\n"
    "s_waitcnt lgkmcnt(7)\n"
    "v_fma_f32 v169, -v162, v168, v217\n"
    "s_waitcnt lgkmcnt(6)\n"
    "v_cvt_f32_i32 v72, v72\n"
    "v_cvt_f32_i32 v73, v73\n"
    "v_cvt_f32_i32 v74, v74\n"
    "v_cvt_f32_i32 v75, v75\n"
    "s_waitcnt lgkmcnt(5)\n"
    "v_cvt_f32_i32 v76, v76\n"
    "v_cvt_f32_i32 v77, v77\n"
    "v_cvt_f32_i32 v78, v78\n"
    "v_cvt_f32_i32 v79, v79\n"
    "v_mul_f32_dpp v72, v168, v72 row_newbcast:0 row_mask:0xf bank_mask:0xf\n"
    "v_mul_f32_dpp v73, v168, v73 row_newbcast:1 row_mask:0xf bank_mask:0xf\n"
    "v_mul_f32_dpp v74, v168, v74 row_newbcast:2 row_mask:0xf bank_mask:0xf\n"
    "v_mul_f32_dpp v75, v168, v75 row_newbcast:3 row_mask:0xf bank_mask:0xf\n"
    "v_mul_f32_dpp v76, v168, v76 row_newbcast:4 row_mask:0xf bank_mask:0xf\n"
    "v_mul_f32_dpp v77, v168, v77 row_newbcast:5 row_mask:0xf bank_mask:0xf\n"
    "v_mul_f32_dpp v78, v168, v78 row_newbcast:6 row_mask:0xf bank_mask:0xf\n"
    "v_mul_f32_dpp v79, v168, v79 row_newbcast:7 row_mask:0xf bank_mask:0xf\n"
    "v_add_f32_dpp v72, v169, v72 row_newbcast:0 row_mask:0xf bank_mask:0xf\n"
    "v_add_f32_dpp v73, v169, v73 row_newbcast:1 row_mask:0xf bank_mask:0xf\n"
    "v_add_f32_dpp v74, v169, v74 row_newbcast:2 row_mask:0xf bank_mask:0xf\n"
    "v_add_f32_dpp v75, v169, v75 row_newbcast:3 row_mask:0xf bank_mask:0xf\n"
    "v_add_f32_dpp v76, v169, v76 row_newbcast:4 row_mask:0xf bank_mask:0xf\n"
    "v_add_f32_dpp v77, v169, v77 row_newbcast:5 row_mask:0xf bank_mask:0xf\n"
    "v_add_f32_dpp v78, v169, v78 row_newbcast:6 row_mask:0xf bank_mask:0xf\n"
    "v_add_f32_dpp v79, v169, v79 row_newbcast:7 row_mask:0xf bank_mask:0xf\n"
    "v_cvt_pk_bf16_f32 v72, v72, v73\n"
    "v_cvt_pk_bf16_f32 v73, v74, v75\n"
    "v_cvt_pk_bf16_f32 v74, v76, v77\n"
    "v_cvt_pk_bf16_f32 v75, v78, v79\n"
    "s_add_u32 s78, s87, 0x6000\n"
    "buffer_store_dwordx4 v[72:75], v211, s[36:39], s78 offen sc1\n"
    "s_waitcnt lgkmcnt(3)\n"
    "v_cvt_f32_i32 v80, v80\n"
    "v_cvt_f32_i32 v81, v81\n"
    "s_waitcnt lgkmcnt(2)\n"
    "v_cvt_f32_i32 v82, v82\n"
    "v_cvt_f32_i32 v83, v83\n"
    "s_waitcnt lgkmcnt(1)\n"
    "v_cvt_f32_i32 v84, v84\n"
    "v_cvt_f32_i32 v85, v85\n"
    "s_waitcnt lgkmcnt(0)\n"
    "v_cvt_f32_i32 v86, v86\n"
    "v_cvt_f32_i32 v87, v87\n"
    "v_mul_f32_dpp v80, v170, v80 row_newbcast:0 row_mask:0xf bank_mask:0xf\n"
    "v_mul_f32_dpp v81, v170, v81 row_newbcast:1 row_mask:0xf bank_mask:0xf\n"
    "v_mul_f32_dpp v82, v170, v82 row_newbcast:2 row_mask:0xf bank_mask:0xf\n"
    "v_mul_f32_dpp v83, v170, v83 row_newbcast:3 row_mask:0xf bank_mask:0xf\n"
    "v_mul_f32_dpp v84, v170, v84 row_newbcast:4 row_mask:0xf bank_mask:0xf\n"
    "v_mul_f32_dpp v85, v170, v85 row_newbcast:5 row_mask:0xf bank_mask:0xf\n"
    "v_mul_f32_dpp v86, v170, v86 row_newbcast:6 row_mask:0xf bank_mask:0xf\n"
    "v_mul_f32_dpp v87, v170, v87 row_newbcast:7 row_mask:0xf bank_mask:0xf\n"
    "v_add_f32_dpp v80, v171, v80 row_newbcast:0 row_mask:0xf bank_mask:0xf\n"
    "v_add_f32_dpp v81, v171, v81 row_newbcast:1 row_mask:0xf bank_mask:0xf\n"
    "v_add_f32_dpp v82, v171, v82 row_newbcast:2 row_mask:0xf bank_mask:0xf\n"
    "v_add_f32_dpp v83, v171, v83 row_newbcast:3 row_mask:0xf bank_mask:0xf\n"
    "v_add_f32_dpp v84, v171, v84 row_newbcast:4 row_mask:0xf bank_mask:0xf\n"
    "v_add_f32_dpp v85, v171, v85 row_newbcast:5 row_mask:0xf bank_mask:0xf\n"
    "v_add_f32_dpp v86, v171, v86 row_newbcast:6 row_mask:0xf bank_mask:0xf\n"
    "v_add_f32_dpp v87, v171, v87 row_newbcast:7 row_mask:0xf bank_mask:0xf\n"
    "v_cvt_pk_bf16_f32 v80, v80, v81\n"
    "v_cvt_pk_bf16_f32 v81, v82, v83\n"
    "v_cvt_pk_bf16_f32 v82, v84, v85\n"
    "v_cvt_pk_bf16_f32 v83, v86, v87\n"
    "s_mov_b32 s78, 0x6000\n"
    "buffer_store_dwordx4 v[80:83], v212, s[40:43], s78 offen sc1\n"
    "buffer_load_dwordx2 v[218:219], v213, s[44:47], 0 offen sc1\n"
    "s_getreg_b32 s96, hwreg(HW_REG_HW_ID, 8, 1)\n"
    "s_lshl_b32 s96, s96, 2\n"
    "s_add_u32 s96, s96, s58\n"
    "s_mov_b32 s97, 0\n"
    "s_cmp_eq_u32 s96, 0\n"
    "s_cselect_b32 s97, LDF1-LDC0, s97\n"
    "s_cmp_eq_u32 s96, 1\n"
    "s_cselect_b32 s97, LDF4-LDC0, s97\n"
    "s_cmp_eq_u32 s96, 2\n"
    "s_cselect_b32 s97, LDF7-LDC0, s97\n"
    "s_cmp_eq_u32 s96, 3\n"
    "s_cselect_b32 s97, LDF9-LDC0, s97\n"
    "s_cmp_eq_u32 s96, 4\n"
    "s_cselect_b32 s97, LDF12-LDC0, s97\n"
    "s_cmp_eq_u32 s96, 5\n"
    "s_cselect_b32 s97, LDF13-LDC0, s97\n"
    "s_cmp_eq_u32 s96, 6\n"
    "s_cselect_b32 s97, LDF14-LDC0, s97\n"
    "s_cmp_eq_u32 s96, 7\n"
    "s_cselect_b32 s97, LDF15-LDC0, s97\n"
    "s_cmp_eq_u32 s97, 0\n"
    "s_cbranch_scc1 LDX0\n"
    "s_getpc_b64 s[98:99]\n"
    "LDC0:\n"
    "s_add_u32 s98, s98, s97\n"
    "s_addc_u32 s99, s99, 0\n"
    "s_mov_b32 s26, s64\n"
    "s_mov_b32 s27, s66\n"
    "s_mov_b64 s[90:91], s[28:29]\n"
    "s_mov_b32 s89, s30\n"
    "s_mov_b64 exec, 0\n"
    "s_setvskip -1, 0\n"
    "s_swappc_b64 s[100:101], s[98:99]\n"
    "s_setvskip 0, 0\n"
    "s_mov_b64 exec, -1\n"
    "s_mov_b32 s64, s26\n"
    "s_mov_b32 s66, s27\n"
    "s_mov_b64 s[28:29], s[90:91]\n"
    "s_mov_b32 s30, s89\n"
    "LDX0:\n"
    "s_add_u32 s78, s88, 0x1800\n"
    "s_add_u32 m0, s71, 0x6800\n"
    "s_nop 0\n"
    "buffer_load_dwordx4 v154, s[28:31], s78 offen nt lds\n"
    "s_add_u32 s78, s88, 0x1800\n"
    "s_add_u32 m0, s71, 0x6c00\n"
    "s_nop 0\n"
    "buffer_load_dwordx4 v155, s[28:31], s78 offen nt lds\n"
    "s_add_u32 s78, s88, 0x1800\n"
    "s_add_u32 m0, s71, 0x7000\n"
    "s_nop 0\n"
    "buffer_load_dwordx4 v156, s[28:31], s78 offen nt lds\n"
    "s_add_u32 s78, s88, 0x1800\n"
    "s_add_u32 m0, s71, 0x7400\n"
    "s_nop 0\n"
    "buffer_load_dwordx4 v157, s[28:31], s78 offen nt lds\n"
    "s_waitcnt vmcnt(9)\n"
    "ds_read_b128 a[192:195], v158\n"
    "ds_read_b128 a[196:199], v159\n"
    "ds_read_b128 a[200:203], v158 offset:128\n"
    "ds_read_b128 a[204:207], v159 offset:128\n"
    "ds_read_b128 a[208:211], v158 offset:256\n"
    "ds_read_b128 a[212:215], v159 offset:256\n"
    "s_waitcnt vmcnt(4)\n"
    "v_cmp_ne_u32 s[80:81], s67, v219\n"
    "v_cmp_gt_u32 vcc, s66, v218\n"
    "s_or_b64 vcc, vcc, s[80:81]\n"
    "s_cbranch_vccnz LSPRE0\n"
    "LSPB0:\n"
    "s_waitcnt lgkmcnt(0)\n"
    "s_barrier\n"
    "v_mov_b32 v220, 3\n"
    "buffer_store_dwordx2 v[220:221], v214, s[44:47], 0 offen sc1\n"
    "s_add_u32 m0, s72, 0x1e000\n"
    "s_nop 0\n"
    "buffer_load_dwordx4 v200, s[36:39], s72 offen sc1 lds\n"
    "s_add_u32 m0, s72, 0x1e000\n"
    "s_nop 0\n"
    "buffer_load_dwordx4 v200, s[36:39], s72 offen offset:1024 sc1 lds\n"
    "s_add_u32 m0, s72, 0x1e000\n"
    "s_nop 0\n"
    "buffer_load_dwordx4 v200, s[36:39], s72 offen offset:2048 sc1 lds\n"
    "s_add_u32 s65, s71, 0x3000\n"
    "s_add_u32 m0, s71, 0\n"
    "s_nop 0\n"
    "buffer_load_dwordx4 v200, s[36:39], s65 offen sc1 lds\n"
    "s_add_u32 s65, s71, 0x3400\n"
    "s_add_u32 m0, s71, 0x400\n"
    "s_nop 0\n"
    "buffer_load_dwordx4 v200, s[36:39], s65 offen sc1 lds\n"
    "s_add_u32 s65, s71, 0x3800\n"
    "s_add_u32 m0, s71, 0x800\n"
    "s_nop 0\n"
    "buffer_load_dwordx4 v200, s[36:39], s65 offen sc1 lds\n"
    "s_add_u32 s65, s71, 0x3c00\n"
    "s_add_u32 m0, s71, 0xc00\n"
    "s_nop 0\n"
    "buffer_load_dwordx4 v200, s[36:39], s65 offen sc1 lds\n"
    "s_add_u32 s65, s71, 0x4000\n"
    "s_add_u32 m0, s71, 0x1000\n"
    "s_nop 0\n"
    "buffer_load_dwordx4 v200, s[36:39], s65 offen sc1 lds\n"
    "s_add_u32 s65, s71, 0x4400\n"
    "s_add_u32 m0, s71, 0x1400\n"
    "s_nop 0\n"
    "buffer_load_dwordx4 v200, s[36:39], s65 offen sc1 lds\n"
    "s_add_u32 s65, s71, 0\n"
    "s_add_u32 m0, s71, 0x12000\n"
    "s_nop 0\n"
    "buffer_load_dwordx4 v200, s[40:43], s65 offen sc1 lds\n"
    "s_add_u32 s65, s71, 0x400\n"
    "s_add_u32 m0, s71, 0x12400\n"
    "s_nop 0\n"
    "buffer_load_dwordx4 v200, s[40:43], s65 offen sc1 lds\n"
    "s_add_u32 s65, s71, 0x800\n"
    "s_add_u32 m0, s71, 0x12800\n"
    "s_nop 0\n"
    "buffer_load_dwordx4 v200, s[40:43], s65 offen sc1 lds\n"
    "s_add_u32 s65, s71, 0xc00\n"
    "s_add_u32 m0, s71, 0x12c00\n"
    "s_nop 0\n"
    "buffer_load_dwordx4 v200, s[40:43], s65 offen sc1 lds\n"
    "s_add_u32 s65, s71, 0x1000\n"
    "s_add_u32 m0, s71, 0x13000\n"
    "s_nop 0\n"
    "buffer_load_dwordx4 v200, s[40:43], s65 offen sc1 lds\n"
    "s_add_u32 s65, s71, 0x1400\n"
    "s_add_u32 m0, s71, 0x13400\n"
    "s_nop 0\n"
    "buffer_load_dwordx4 v200, s[40:43], s65 offen sc1 lds\n"
    "s_add_u32 s78, s64, 0x24000\n"
    "s_add_u32 m0, s72, 0x18000\n"
    "s_nop 0\n"
    "buffer_load_dwordx4 v202, s[32:35], s78 offen lds\n"
    "buffer_load_dwordx4 v203, s[32:35], s78 offen offset:1024 lds\n"
    "buffer_load_dwordx4 v204, s[32:35], s78 offen offset:2048 lds\n"
    "s_add_u32 s78, s88, 0x3000\n"
    "s_add_u32 m0, s71, 0xc000\n"
    "s_nop 0\n"
    "buffer_load_dwordx4 v152, s[28:31], s78 offen nt lds\n"
    "s_mul_i32 s66, s74, 3\n"
    "s_waitcnt vmcnt(20)\n"
    "ds_read_b128 a[216:219], v158 offset:24576\n"
    "ds_read_b128 a[220:223], v159 offset:24576\n"
    "ds_read_b128 a[224:227], v158 offset:24704\n"
    "ds_read_b128 a[228:231], v159 offset:24704\n"
    "ds_read_b128 a[232:235], v158 offset:24832\n"
    "ds_read_b128 a[236:239], v159 offset:24832\n"
    "s_add_u32 m0, s72, 0x21000\n"
    "s_nop 0\n"
    "buffer_load_dwordx4 v249, s[48:51], 0 offen offset:1024 lds\n"
    "buffer_load_dwordx2 v[218:219], v213, s[44:47], 0 offen sc1\n"
    "v_add_u32 v255, 0x1e000, v200\n"
    "s_add_u32 s60, s71, 0x9000\n"
    "s_add_u32 s61, s71, 0x6000\n"
    "s_add_u32 s62, s87, 0x12000\n"
    "s_mov_b32 s63, 0x12000\n"
    "s_mov_b32 s59, 0\n"
    "s_mul_i32 s66, s74, 3\n"
    "s_getpc_b64 s[52:53]\n"
    "LPC2:\n"
    "s_add_u32 s52, s52, LTAIL-LPC2\n"
    "s_addc_u32 s53, s53, 0\n"
    "s_mov_b32 s54, LSLOW-LTAIL\n"
    "s_lshr_b32 s91, %1, 3\n"
    "s_mul_i32 s91, s91, 4\n"
    "s_add_u32 s91, s91, s58\n"
    "s_sub_u32 s91, s91, 0\n"
    "s_lshl_b32 s91, s91, 9\n"
    "s_mul_i32 s90, s58, 0x1000\n"
    "s_add_u32 s90, s90, 0x1000\n"
    "s_sub_u32 s92, s90, s71\n"
    "s_waitcnt vmcnt(12) lgkmcnt(0)\n"
    "s_barrier\n"
    "ds_read_b128 v[160:163], v255\n"
    "ds_read_b128 v[164:167], v255 offset:1024\n"
    "ds_read_b128 v[168:171], v255 offset:2048\n"
    "ds_read_b128 v[172:175], v255 offset:3072\n"
    "ds_read_b128 v[176:179], v255 offset:4096\n"
    "ds_read_b128 v[180:183], v255 offset:5120\n"
    "s_add_u32 s76, s87, 0xc000\n"
    "s_mov_b32 s77, 0xc000\n"
    "s_waitcnt lgkmcnt(4)\n"
    "v_mfma_f32_16x16x32_bf16 v[40:43], v[160:163], a[192:195], 0\n"
    "ds_read_b128 v[184:187], v255 offset:6144\n"
    "ds_read_b64 v[228:229], v210 offset:128\n"
    "v_mfma_f32_16x16x32_bf16 v[40:43], v[164:167], a[196:199], v[40:43]\n"
    "s_add_u32 s78, s88, 0x3000\n"
    "ds_read2st64_b32 v[240:241], v207 offset0:144 offset1:147\n"
    "ds_read2st64_b32 v[242:243], v207 offset0:150 offset1:153\n"
    "v_mfma_f32_16x16x32_bf16 v[44:47], v[160:163], a[216:219], 0\n"
    "s_add_u32 m0, s71, 0xc400\n"
    "ds_read_b128 v[188:191], v255 offset:7168\n"
    "ds_read2st64_b32 v[244:245], v207 offset0:156 offset1:159\n"
    "v_mfma_f32_16x16x32_bf16 v[44:47], v[164:167], a[220:223], v[44:47]\n"
    "s_waitcnt lgkmcnt(8)\n"
    "buffer_load_dwordx4 v153, s[28:31], s78 offen nt lds\n"
    "v_mfma_f32_16x16x32_bf16 v[40:43], v[168:171], a[200:203], v[40:43]\n"
    "ds_read_b128 v[160:163], v255 offset:8192\n"
    "ds_read2st64_b32 v[246:247], v207 offset0:162 offset1:165\n"
    "v_mfma_f32_16x16x32_bf16 v[40:43], v[172:175], a[204:207], v[40:43]\n"
    "ds_read_u16_d16_hi v223, v209 offset:6720\n"
    "ds_read_u16_d16_hi v222, v209 offset:576\n"
    "v_mfma_f32_16x16x32_bf16 v[44:47], v[168:171], a[224:227], v[44:47]\n"
    "ds_read_b128 v[164:167], v255 offset:9216\n"
    "ds_read_b128 v[232:235], v205 offset:36864\n"
    "v_mfma_f32_16x16x32_bf16 v[44:47], v[172:175], a[228:231], v[44:47]\n"
    "s_waitcnt lgkmcnt(12)\n"
    "ds_read_b128 v[236:239], v206 offset:36864\n"
    "v_mfma_f32_16x16x32_bf16 v[40:43], v[176:179], a[208:211], v[40:43]\n"
    "ds_read_b128 v[168:171], v255 offset:10240\n"
    "v_mfma_f32_16x16x32_bf16 v[40:43], v[180:183], a[212:215], v[40:43]\n"
    "v_mfma_f32_16x16x32_bf16 v[44:47], v[176:179], a[232:235], v[44:47]\n"
    "ds_read_b128 v[172:175], v255 offset:11264\n"
    "v_mfma_f32_16x16x32_bf16 v[44:47], v[180:183], a[236:239], v[44:47]\n"
    "s_waitcnt lgkmcnt(10)\n"
    "s_add_u32 s78, s88, 0x3000\n"
    "v_cvt_f32_i32 v240, v240\n"
    "v_mfma_f32_16x16x32_bf16 v[56:59], v[184:187], a[192:195], 0\n"
    "s_add_u32 m0, s71, 0xc800\n"
    "ds_read_b128 v[176:179], v200\n"
    "v_cvt_f32_i32 v241, v241\n"
    "v_mfma_f32_16x16x32_bf16 v[56:59], v[188:191], a[196:199], v[56:59]\n"
    "buffer_load_dwordx4 v154, s[28:31], s78 offen nt lds\n"
    "v_mfma_f32_16x16x32_bf16 v[60:63], v[184:187], a[216:219], 0\n"
    "ds_read_b128 v[180:183], v200 offset:1024\n"
    "v_cvt_f32_i32 v242, v242\n"
    "v_mfma_f32_16x16x32_bf16 v[60:63], v[188:191], a[220:223], v[60:63]\n"
    "s_waitcnt lgkmcnt(6)\n"
    "v_cvt_f32_i32 v243, v243\n"
    "v_cvt_f32_i32 v244, v244\n"
    "v_mfma_f32_16x16x32_bf16 v[56:59], v[160:163], a[200:203], v[56:59]\n"
    "ds_read_b128 v[184:187], v200 offset:2048\n"
    "v_cvt_f32_i32 v245, v245\n"
    "v_mfma_f32_16x16x32_bf16 v[56:59], v[164:167], a[204:207], v[56:59]\n"
    "v_cvt_f32_i32 v246, v246\n"
    "v_mul_f32 v226, s75, v223\n"
    "v_mfma_f32_16x16x32_bf16 v[60:63], v[160:163], a[224:227], v[60:63]\n"
    "ds_read_b128 v[188:191], v200 offset:3072\n"
    "v_cvt_f32_i32 v247, v247\n"
    "v_mfma_f32_16x16x32_bf16 v[60:63], v[164:167], a[228:231], v[60:63]\n"
    "s_waitcnt lgkmcnt(4)\n"
    "v_fma_f32 v227, -v222, v226, v217\n"
    "v_mul_f32_dpp v240, v228, v240 row_newbcast:0 row_mask:0xf bank_mask:0xf\n"
    "v_mfma_f32_16x16x32_bf16 v[56:59], v[168:171], a[208:211], v[56:59]\n"
    "ds_read_b128 v[160:163], v200 offset:4096\n"
    "v_cvt_f32_i32 v232, v232\n"
    "v_mfma_f32_16x16x32_bf16 v[56:59], v[172:175], a[212:215], v[56:59]\n"
    "s_add_u32 s78, s88, 0x3000\n"
    "v_mul_f32_dpp v241, v228, v241 row_newbcast:1 row_mask:0xf bank_mask:0xf\n"
    "v_cvt_f32_i32 v233, v233\n"
    "v_mfma_f32_16x16x32_bf16 v[60:63], v[168:171], a[232:235], v[60:63]\n"
    "s_add_u32 m0, s71, 0xcc00\n"
    "ds_read_b128 v[164:167], v200 offset:5120\n"
    "v_mul_f32_dpp v242, v228, v242 row_newbcast:2 row_mask:0xf bank_mask:0xf\n"
    "v_mfma_f32_16x16x32_bf16 v[60:63], v[172:175], a[236:239], v[60:63]\n"
    "s_waitcnt lgkmcnt(4)\n"
    "buffer_load_dwordx4 v155, s[28:31], s78 offen nt lds\n"
    "v_mfma_f32_16x16x32_bf16 v[64:67], v[176:179], a[192:195], 0\n"
    "ds_read_b128 v[168:171], v200 offset:6144\n"
    "v_cvt_f32_i32 v234, v234\n"
    "v_mul_f32_dpp v243, v228, v243 row_newbcast:3 row_mask:0xf bank_mask:0xf\n"
    "v_cvt_f32_i32 v235, v235\n"
    "v_mfma_f32_16x16x32_bf16 v[64:67], v[180:183], a[196:199], v[64:67]\n"
    "v_mul_f32_dpp v244, v228, v244 row_newbcast:4 row_mask:0xf bank_mask:0xf\n"
    "v_cvt_f32_i32 v236, v236\n"
    "v_mul_f32_dpp v245, v228, v245 row_newbcast:5 row_mask:0xf bank_mask:0xf\n"
    "v_cvt_f32_i32 v237, v237\n"
    "v_mfma_f32_16x16x32_bf16 v[68:71], v[176:179], a[216:219], 0\n"
    "ds_read_b128 v[172:175], v200 offset:7168\n"
    "v_mul_f32_dpp v246, v228, v246 row_newbcast:6 row_mask:0xf bank_mask:0xf\n"
    "v_cvt_f32_i32 v238, v238\n"
    "v_mul_f32_dpp v247, v228, v247 row_newbcast:7 row_mask:0xf bank_mask:0xf\n"
    "v_mfma_f32_16x16x32_bf16 v[68:71], v[180:183], a[220:223], v[68:71]\n"
    "s_waitcnt lgkmcnt(4)\n"
    "v_cvt_f32_i32 v239, v239\n"
    "v_add_f32_dpp v240, v229, v240 row_newbcast:0 row_mask:0xf bank_mask:0xf\n"
    "v_mul_f32_dpp v232, v226, v232 row_newbcast:0 row_mask:0xf bank_mask:0xf\n"
    "v_add_f32_dpp v241, v229, v241 row_newbcast:1 row_mask:0xf bank_mask:0xf\n"
    "v_mfma_f32_16x16x32_bf16 v[64:67], v[184:187], a[200:203], v[64:67]\n"
    "ds_read_b128 v[176:179], v200 offset:8192\n"
    "v_mul_f32_dpp v233, v226, v233 row_newbcast:1 row_mask:0xf bank_mask:0xf\n"
    "v_add_f32_dpp v242, v229, v242 row_newbcast:2 row_mask:0xf bank_mask:0xf\n"
    "v_mul_f32_dpp v234, v226, v234 row_newbcast:2 row_mask:0xf bank_mask:0xf\n"
    "v_mfma_f32_16x16x32_bf16 v[64:67], v[188:191], a[204:207], v[64:67]\n"
    "v_add_f32_dpp v243, v229, v243 row_newbcast:3 row_mask:0xf bank_mask:0xf\n"
    "v_mul_f32_dpp v235, v226, v235 row_newbcast:3 row_mask:0xf bank_mask:0xf\n"
    "v_add_f32_dpp v244, v229, v244 row_newbcast:4 row_mask:0xf bank_mask:0xf\n"
    "v_mul_f32_dpp v236, v226, v236 row_newbcast:4 row_mask:0xf bank_mask:0xf\n"
    "v_mfma_f32_16x16x32_bf16 v[68:71], v[184:187], a[224:227], v[68:71]\n"
    "ds_read_b128 v[180:183], v200 offset:9216\n"
    "v_add_f32_dpp v245, v229, v245 row_newbcast:5 row_mask:0xf bank_mask:0xf\n"
    "v_mul_f32_dpp v237, v226, v237 row_newbcast:5 row_mask:0xf bank_mask:0xf\n"
    "v_add_f32_dpp v246, v229, v246 row_newbcast:6 row_mask:0xf bank_mask:0xf\n"
    "v_mul_f32_dpp v238, v226, v238 row_newbcast:6 row_mask:0xf bank_mask:0xf\n"
    "v_add_f32_dpp v247, v229, v247 row_newbcast:7 row_mask:0xf bank_mask:0xf\n"
    "v_mul_f32_dpp v239, v226, v239 row_newbcast:7 row_mask:0xf bank_mask:0xf\n"
    "v_cvt_pk_bf16_f32 v240, v240, v241\n"
    "v_add_f32_dpp v232, v227, v232 row_newbcast:0 row_mask:0xf bank_mask:0xf\n"
    "v_cvt_pk_bf16_f32 v241, v242, v243\n"
    "v_add_f32_dpp v233, v227, v233 row_newbcast:1 row_mask:0xf bank_mask:0xf\n"
    "v_cvt_pk_bf16_f32 v242, v244, v245\n"
    "v_add_f32_dpp v234, v227, v234 row_newbcast:2 row_mask:0xf bank_mask:0xf\n"
    "v_cvt_pk_bf16_f32 v243, v246, v247\n"
    "v_add_f32_dpp v235, v227, v235 row_newbcast:3 row_mask:0xf bank_mask:0xf\n"
    "v_add_f32_dpp v236, v227, v236 row_newbcast:4 row_mask:0xf bank_mask:0xf\n"
    "v_add_f32_dpp v237, v227, v237 row_newbcast:5 row_mask:0xf bank_mask:0xf\n"
    "v_add_f32_dpp v238, v227, v238 row_newbcast:6 row_mask:0xf bank_mask:0xf\n"
    "v_add_f32_dpp v239, v227, v239 row_newbcast:7 row_mask:0xf bank_mask:0xf\n"
    "v_cvt_pk_bf16_f32 v232, v232, v233\n"
    "v_cvt_pk_bf16_f32 v233, v234, v235\n"
    "v_cvt_pk_bf16_f32 v234, v236, v237\n"
    "v_cvt_pk_bf16_f32 v235, v238, v239\n"
    "v_mfma_f32_16x16x32_bf16 v[68:71], v[188:191], a[228:231], v[68:71]\n"
    "s_waitcnt lgkmcnt(4)\n"
    "s_add_u32 s78, s88, 0x3000\n"
    "v_max3_f32 v196, v40, v41, v42\n"
    "v_max3_f32 v196, v196, v43, v56\n"
    "v_max3_f32 v196, v196, v57, v58\n"
    "buffer_store_dwordx4 v[232:235], v211, s[36:39], s76 offen sc1\n"
    "v_mfma_f32_16x16x32_bf16 v[64:67], v[160:163], a[208:211], v[64:67]\n"
    "s_add_u32 m0, s71, 0xd000\n"
    "ds_read_b128 v[184:187], v200 offset:10240\n"
    "v_max_f32 v196, v196, v59\n"
    "v_max3_f32 v198, v44, v45, v46\n"
    "v_mov_b32 v197, v196\n"
    "v_mfma_f32_16x16x32_bf16 v[64:67], v[164:167], a[212:215], v[64:67]\n"
    "buffer_load_dwordx4 v156, s[28:31], s78 offen nt lds\n"
    "v_max3_f32 v198, v198, v47, v60\n"
    "v_permlane32_swap_b32 v196, v197\n"
    "v_mfma_f32_16x16x32_bf16 v[68:71], v[160:163], a[232:235], v[68:71]\n"
    "ds_read_b128 v[188:191], v200 offset:11264\n"
    "v_max3_f32 v198, v198, v61, v62\n"
    "v_max_f32 v196, v196, v197\n"
    "v_max_f32 v198, v198, v63\n"
    "v_mfma_f32_16x16x32_bf16 v[68:71], v[164:167], a[236:239], v[68:71]\n"
    "s_waitcnt lgkmcnt(4)\n"
    "v_mov_b32 v197, v196\n"
    "v_mov_b32 v199, v198\n"
    "s_nop 0\n"
    "v_permlane16_swap_b32 v196, v197\n"
    "v_permlane32_swap_b32 v198, v199\n"
    "v_mfma_f32_16x16x32_bf16 v[80:83], v[168:171], a[192:195], 0\n"
    "ds_read_b128 v[160:163], v200 offset:12288\n"
    "v_max_f32 v196, v196, v197\n"
    "v_max_f32 v198, v198, v199\n"
    "v_add_f32 v196, 0x42640000, v196\n"
    "v_mfma_f32_16x16x32_bf16 v[80:83], v[172:175], a[196:199], v[80:83]\n"
    "buffer_store_dwordx4 v[240:243], v212, s[40:43], s77 offen sc1\n"
    "v_mov_b32 v199, v198\n"
    "v_sub_f32 v96, 0, v196\n"
    "s_nop 0\n"
    "v_permlane16_swap_b32 v198, v199\n"
    "v_sub_f32 v97, 0, v196\n"
    "v_max_f32 v198, v198, v199\n"
    "v_mfma_f32_16x16x32_bf16 v[84:87], v[168:171], a[216:219], 0\n"
    "ds_read_b128 v[164:167], v200 offset:13312\n"
    "v_sub_f32 v98, 0, v196\n"
    "v_add_f32 v198, 0x42640000, v198\n"
    "v_sub_f32 v99, 0, v196\n"
    "v_mfma_f32_16x16x32_bf16 v[84:87], v[172:175], a[220:223], v[84:87]\n"
    "s_waitcnt lgkmcnt(4)\n"
    "v_sub_f32 v100, 0, v198\n"
    "v_add_f32 v40, v40, v96\n"
    "v_sub_f32 v101, 0, v198\n"
    "v_add_f32 v41, v41, v96\n"
    "v_mfma_f32_16x16x32_bf16 v[80:83], v[176:179], a[200:203], v[80:83]\n"
    "ds_read_b128 v[168:171], v200 offset:14336\n"
    "v_sub_f32 v102, 0, v198\n"
    "v_add_f32 v42, v42, v96\n"
    "v_sub_f32 v103, 0, v198\n"
    "v_mfma_f32_16x16x32_bf16 v[80:83], v[180:183], a[204:207], v[80:83]\n"
    "s_add_u32 s78, s88, 0x3000\n"
    "v_add_f32 v43, v43, v96\n"
    "v_add_f32 v44, v44, v100\n"
    "v_add_f32 v56, v56, v96\n"
    "v_add_f32 v45, v45, v100\n"
    "v_mfma_f32_16x16x32_bf16 v[84:87], v[176:179], a[224:227], v[84:87]\n"
    "s_add_u32 m0, s71, 0xd400\n"
    "ds_read_b128 v[172:175], v200 offset:15360\n"
    "v_add_f32 v57, v57, v96\n"
    "v_add_f32 v46, v46, v100\n"
    "v_add_f32 v58, v58, v96\n"
    "v_mfma_f32_16x16x32_bf16 v[84:87], v[180:183], a[228:231], v[84:87]\n"
    "s_waitcnt lgkmcnt(4)\n"
    "buffer_load_dwordx4 v157, s[28:31], s78 offen nt lds\n"
    "v_add_f32 v47, v47, v100\n"
    "v_add_f32 v59, v59, v96\n"
    "v_mfma_f32_16x16x32_bf16 v[80:83], v[184:187], a[208:211], v[80:83]\n"
    "ds_read_b128 v[176:179], v200 offset:16384\n"
    "v_add_f32 v60, v60, v100\n"
    "v_exp_f32 v40, v40\n"
    "v_mfma_f32_16x16x32_bf16 v[80:83], v[188:191], a[212:215], v[80:83]\n"
    "v_add_f32 v61, v61, v100\n"
    "v_add_f32 v62, v62, v100\n"
    "v_exp_f32 v41, v41\n"
    "v_mfma_f32_16x16x32_bf16 v[84:87], v[184:187], a[232:235], v[84:87]\n"
    "ds_read_b128 v[180:183], v200 offset:17408\n"
    "v_add_f32 v63, v63, v100\n"
    "v_exp_f32 v44, v44\n"
    "v_mfma_f32_16x16x32_bf16 v[84:87], v[188:191], a[236:239], v[84:87]\n"
    "s_waitcnt lgkmcnt(4)\n"
    "v_exp_f32 v42, v42\n"
    "v_exp_f32 v45, v45\n"
    "v_mfma_f32_16x16x32_bf16 v[32:35], v[160:163], a[192:195], v[96:99]\n"
    "ds_read_b128 v[184:187], v200 offset:18432\n"
    "v_exp_f32 v43, v43\n"
    "v_mfma_f32_16x16x32_bf16 v[32:35], v[164:167], a[196:199], v[32:35]\n"
    "v_exp_f32 v46, v46\n"
    "v_exp_f32 v56, v56\n"
    "v_mfma_f32_16x16x32_bf16 v[36:39], v[160:163], a[216:219], v[100:103]\n"
    "ds_read_b128 v[188:191], v200 offset:19456\n"
    "v_exp_f32 v47, v47\n"
    "v_mfma_f32_16x16x32_bf16 v[36:39], v[164:167], a[220:223], v[36:39]\n"
    "s_waitcnt lgkmcnt(4)\n"
    "s_add_u32 s78, s88, 0x4800\n"
    "v_exp_f32 v57, v57\n"
    "v_mfma_f32_16x16x32_bf16 v[32:35], v[168:171], a[200:203], v[32:35]\n"
    "s_add_u32 m0, s71, 0x6000\n"
    "ds_read_b128 v[160:163], v200 offset:20480\n"
    "v_exp_f32 v60, v60\n"
    "v_mfma_f32_16x16x32_bf16 v[32:35], v[172:175], a[204:207], v[32:35]\n"
    "buffer_load_dwordx4 v152, s[28:31], s78 offen nt lds\n"
    "v_exp_f32 v58, v58\n"
    "v_mfma_f32_16x16x32_bf16 v[36:39], v[168:171], a[224:227], v[36:39]\n"
    "ds_read_b128 v[164:167], v200 offset:21504\n"
    "v_exp_f32 v61, v61\n"
    "v_mfma_f32_16x16x32_bf16 v[36:39], v[172:175], a[228:231], v[36:39]\n"
    "s_waitcnt lgkmcnt(4)\n"
    "v_exp_f32 v59, v59\n"
    "v_exp_f32 v62, v62\n"
    "v_mfma_f32_16x16x32_bf16 v[32:35], v[176:179], a[208:211], v[32:35]\n"
    "ds_read_b128 v[168:171], v200 offset:22528\n"
    "v_cvt_pk_bf16_f32 v128, v40, v41\n"
    "v_cvt_pk_bf16_f32 v129, v42, v43\n"
    "v_cvt_pk_bf16_f32 v130, v56, v57\n"
    "v_mfma_f32_16x16x32_bf16 v[32:35], v[180:183], a[212:215], v[32:35]\n"
    "v_exp_f32 v63, v63\n"
    "v_cvt_pk_bf16_f32 v131, v58, v59\n"
    "v_cvt_pk_bf16_f32 v132, v44, v45\n"
    "v_mfma_f32_16x16x32_bf16 v[36:39], v[176:179], a[232:235], v[36:39]\n"
    "ds_read_b128 v[172:175], v200 offset:23552\n"
    "v_add_f32 v64, v64, v96\n"
    "v_cvt_pk_bf16_f32 v133, v46, v47\n"
    "v_add_f32 v65, v65, v96\n"
    "v_mfma_f32_16x16x32_bf16 v[36:39], v[180:183], a[236:239], v[36:39]\n"
    "s_waitcnt lgkmcnt(4)\n"
    "v_cvt_pk_bf16_f32 v134, v60, v61\n"
    "v_add_f32 v66, v66, v96\n"
    "s_waitcnt vmcnt(11)\n"
    "s_barrier\n"
    "v_mfma_f32_16x16x32_bf16 v[48:51], v[184:187], a[192:195], v[96:99]\n"
    "ds_read_b128 v[176:179], v201 offset:24576\n"
    "v_cvt_pk_bf16_f32 v135, v62, v63\n"
    "v_add_f32 v67, v67, v96\n"
    "v_add_f32 v68, v68, v100\n"
    "v_mfma_f32_16x16x32_bf16 v[48:51], v[188:191], a[196:199], v[48:51]\n"
    "s_add_u32 s78, s88, 0x4800\n"
    "v_add_f32 v80, v80, v96\n"
    "v_add_f32 v69, v69, v100\n"
    "v_add_f32 v81, v81, v96\n"
    "v_add_f32 v70, v70, v100\n"
    "v_mfma_f32_16x16x32_bf16 v[52:55], v[184:187], a[216:219], v[100:103]\n"
    "s_add_u32 m0, s71, 0x6400\n"
    "ds_read_b128 v[180:183], v201 offset:25600\n"
    "v_add_f32 v82, v82, v96\n"
    "v_add_f32 v71, v71, v100\n"
    "v_add_f32 v83, v83, v96\n"
    "v_mfma_f32_16x16x32_bf16 v[52:55], v[188:191], a[220:223], v[52:55]\n"
    "s_waitcnt lgkmcnt(4)\n"
    "buffer_load_dwordx4 v153, s[28:31], s78 offen nt lds\n"
    "v_add_f32 v84, v84, v100\n"
    "v_add_f32 v85, v85, v100\n"
    "v_mfma_f32_16x16x32_bf16 v[48:51], v[160:163], a[200:203], v[48:51]\n"
    "ds_read_b128 v[184:187], v201 offset:26624\n"
    "v_exp_f32 v64, v64\n"
    "v_add_f32 v86, v86, v100\n"
    "v_mfma_f32_16x16x32_bf16 v[48:51], v[164:167], a[204:207], v[48:51]\n"
    "v_exp_f32 v65, v65\n"
    "v_add_f32 v87, v87, v100\n"
    "ds_read_b64 v[228:229], v210 offset:192\n"
    "v_mfma_f32_16x16x32_bf16 v[52:55], v[160:163], a[224:227], v[52:55]\n"
    "ds_read_b128 v[188:191], v201 offset:27648\n"
    "v_exp_f32 v68, v68\n"
    "ds_read2st64_b32 v[240:241], v207 offset0:96 offset1:99\n"
    "v_mfma_f32_16x16x32_bf16 v[52:55], v[164:167], a[228:231], v[52:55]\n"
    "s_waitcnt lgkmcnt(6)\n"
    "v_exp_f32 v66, v66\n"
    "v_exp_f32 v69, v69\n"
    "v_mfma_f32_16x16x32_bf16 v[48:51], v[168:171], a[208:211], v[48:51]\n"
    "ds_read_b128 v[160:163], v201 offset:28672\n"
    "v_exp_f32 v67, v67\n"
    "ds_read2st64_b32 v[242:243], v207 offset0:102 offset1:105\n"
    "v_mfma_f32_16x16x32_bf16 v[48:51], v[172:175], a[212:215], v[48:51]\n"
    "v_exp_f32 v70, v70\n"
    "v_exp_f32 v80, v80\n"
    "v_mfma_f32_16x16x32_bf16 v[52:55], v[168:171], a[232:235], v[52:55]\n"
    "ds_read_b128 v[164:167], v201 offset:29696\n"
    "v_exp_f32 v71, v71\n"
    "ds_read2st64_b32 v[244:245], v207 offset0:108 offset1:111\n"
    "v_exp_f32 v81, v81\n"
    "v_exp_f32 v82, v82\n"
    "v_exp_f32 v83, v83\n"
    "v_cvt_pk_bf16_f32 v144, v64, v65\n"
    "v_cvt_pk_bf16_f32 v145, v66, v67\n"
    "v_cvt_pk_bf16_f32 v146, v80, v81\n"
    "v_cvt_pk_bf16_f32 v147, v82, v83\n"
    "v_mfma_f32_16x16x32_bf16 v[52:55], v[172:175], a[236:239], v[52:55]\n"
    "s_waitcnt lgkmcnt(8)\n"
    "s_add_u32 s78, s88, 0x4800\n"
    "v_exp_f32 v84, v84\n"
    "ds_read2st64_b32 v[246:247], v207 offset0:114 offset1:117\n"
    "v_mfma_f32_16x16x32_bf16 a[0:3], v[176:179], v[128:131], 0\n"
    "s_add_u32 m0, s71, 0x6800\n"
    "ds_read_b128 v[168:171], v201 offset:30720\n"
    "ds_read_u16_d16_hi v223, v209 offset:6912\n"
    "v_exp_f32 v85, v85\n"
    "v_exp_f32 v86, v86\n"
    "v_exp_f32 v87, v87\n"
    "v_cvt_pk_bf16_f32 v148, v68, v69\n"
    "v_cvt_pk_bf16_f32 v149, v70, v71\n"
    "v_cvt_pk_bf16_f32 v150, v84, v85\n"
    "v_cvt_pk_bf16_f32 v151, v86, v87\n"
    "v_mfma_f32_16x16x32_bf16 a[0:3], v[180:183], v[144:147], a[0:3]\n"
    "buffer_load_dwordx4 v154, s[28:31], s78 offen nt lds\n"
    "v_mfma_f32_16x16x32_bf16 a[4:7], v[176:179], v[132:135], 0\n"
    "ds_read_b128 v[172:175], v201 offset:31744\n"
    "ds_read_u16_d16_hi v222, v209 offset:768\n"
    "v_mfma_f32_16x16x32_bf16 a[4:7], v[180:183], v[148:151], a[4:7]\n"
    "s_waitcnt lgkmcnt(10)\n"
    "v_exp_f32 v32, v32\n"
    "v_mfma_f32_16x16x32_bf16 a[16:19], v[184:187], v[128:131], 0\n"
    "ds_read_b128 v[176:179], v201 offset:32768\n"
    "ds_read_b128 v[232:235], v205 offset:24576\n"
    "v_mfma_f32_16x16x32_bf16 a[16:19], v[188:191], v[144:147], a[16:19]\n"
    "ds_read_b128 v[236:239], v206 offset:24576\n"
    "v_mfma_f32_16x16x32_bf16 a[20:23], v[184:187], v[132:135], 0\n"
    "ds_read_b128 v[180:183], v201 offset:33792\n"
    "s_waitcnt lgkmcnt(13)\n"
    "v_cvt_f32_i32 v240, v240\n"
    "v_mfma_f32_16x16x32_bf16 a[20:23], v[188:191], v[148:151], a[20:23]\n"
    "s_waitcnt lgkmcnt(10)\n"
    "v_exp_f32 v33, v33\n"
    "v_mfma_f32_16x16x32_bf16 a[32:35], v[160:163], v[128:131], 0\n"
    "ds_read_b128 v[184:187], v201 offset:34816\n"
    "v_cvt_f32_i32 v241, v241\n"
    "v_mfma_f32_16x16x32_bf16 a[32:35], v[164:167], v[144:147], a[32:35]\n"
    "s_add_u32 s78, s88, 0x4800\n"
    "v_exp_f32 v34, v34\n"
    "v_mfma_f32_16x16x32_bf16 a[36:39], v[160:163], v[132:135], 0\n"
    "s_add_u32 m0, s71, 0x6c00\n"
    "ds_read_b128 v[188:191], v201 offset:35840\n"
    "v_cvt_f32_i32 v242, v242\n"
    "v_mfma_f32_16x16x32_bf16 a[36:39], v[164:167], v[148:151], a[36:39]\n"
    "buffer_load_dwordx4 v155, s[28:31], s78 offen nt lds\n"
    "v_mfma_f32_16x16x32_bf16 v[112:115], v[192:195], v[128:131], 0\n"
    "v_exp_f32 v35, v35\n"
    "v_mfma_f32_16x16x32_bf16 v[112:115], v[192:195], v[144:147], v[112:115]\n"
    "s_waitcnt lgkmcnt(7)\n"
    "v_exp_f32 v36, v36\n"
    "v_mfma_f32_16x16x32_bf16 a[48:51], v[168:171], v[128:131], 0\n"
    "ds_read_b128 v[160:163], v201 offset:36864\n"
    "v_cvt_f32_i32 v243, v243\n"
    "v_mfma_f32_16x16x32_bf16 a[48:51], v[172:175], v[144:147], a[48:51]\n"
    "v_cvt_f32_i32 v244, v244\n"
    "v_cvt_f32_i32 v245, v245\n"
    "v_mfma_f32_16x16x32_bf16 a[52:55], v[168:171], v[132:135], 0\n"
    "ds_read_b128 v[164:167], v201 offset:37888\n"
    "v_cvt_f32_i32 v246, v246\n"
    "v_mfma_f32_16x16x32_bf16 a[52:55], v[172:175], v[148:151], a[52:55]\n"
    "v_exp_f32 v37, v37\n"
    "v_mfma_f32_16x16x32_bf16 v[116:119], v[192:195], v[132:135], 0\n"
    "v_exp_f32 v38, v38\n"
    "v_mfma_f32_16x16x32_bf16 v[116:119], v[192:195], v[148:151], v[116:119]\n"
    "s_waitcnt lgkmcnt(4)\n"
    "s_add_u32 s78, s88, 0x4800\n"
    "v_mul_f32 v226, s75, v223\n"
    "v_mfma_f32_16x16x32_bf16 a[64:67], v[176:179], v[128:131], 0\n"
    "s_add_u32 m0, s71, 0x7000\n"
    "ds_read_b128 v[168:171], v201 offset:38912\n"
    "v_cvt_f32_i32 v247, v247\n"
    "v_mfma_f32_16x16x32_bf16 a[64:67], v[180:183], v[144:147], a[64:67]\n"
    "buffer_load_dwordx4 v156, s[28:31], s78 offen nt lds\n"
    "v_mfma_f32_16x16x32_bf16 a[68:71], v[176:179], v[132:135], 0\n"
    "ds_read_b128 v[172:175], v201 offset:39936\n"
    "v_fma_f32 v227, -v222, v226, v217\n"
    "v_mfma_f32_16x16x32_bf16 a[68:71], v[180:183], v[148:151], a[68:71]\n"
    "s_waitcnt lgkmcnt(4)\n"
    "v_mul_f32_dpp v240, v228, v240 row_newbcast:0 row_mask:0xf bank_mask:0xf\n"
    "v_cvt_f32_i32 v232, v232\n"
    "v_mfma_f32_16x16x32_bf16 a[80:83], v[184:187], v[128:131], 0\n"
    "ds_read_b128 v[176:179], v201 offset:40960\n"
    "v_mul_f32_dpp v241, v228, v241 row_newbcast:1 row_mask:0xf bank_mask:0xf\n"
    "v_mfma_f32_16x16x32_bf16 a[80:83], v[188:191], v[144:147], a[80:83]\n"
    "v_exp_f32 v39, v39\n"
    "v_mfma_f32_16x16x32_bf16 a[84:87], v[184:187], v[132:135], 0\n"
    "ds_read_b128 v[180:183], v201 offset:41984\n"
    "v_cvt_f32_i32 v233, v233\n"
    "v_mfma_f32_16x16x32_bf16 a[84:87], v[188:191], v[148:151], a[84:87]\n"
    "s_waitcnt lgkmcnt(4)\n"
    "v_mul_f32_dpp v242, v228, v242 row_newbcast:2 row_mask:0xf bank_mask:0xf\n"
    "v_cvt_f32_i32 v234, v234\n"
    "v_mfma_f32_16x16x32_bf16 a[96:99], v[160:163], v[128:131], 0\n"
    "ds_read_b128 v[184:187], v201 offset:43008\n"
    "v_mul_f32_dpp v243, v228, v243 row_newbcast:3 row_mask:0xf bank_mask:0xf\n"
    "v_mfma_f32_16x16x32_bf16 a[96:99], v[164:167], v[144:147], a[96:99]\n"
    "s_add_u32 s78, s88, 0x4800\n"
    "v_exp_f32 v48, v48\n"
    "v_mfma_f32_16x16x32_bf16 a[100:103], v[160:163], v[132:135], 0\n"
    "s_add_u32 m0, s71, 0x7400\n"
    "ds_read_b128 v[188:191], v201 offset:44032\n"
    "v_cvt_f32_i32 v235, v235\n"
    "v_mfma_f32_16x16x32_bf16 a[100:103], v[164:167], v[148:151], a[100:103]\n"
    "s_waitcnt lgkmcnt(4)\n"
    "buffer_load_dwordx4 v157, s[28:31], s78 offen nt lds\n"
    "v_mfma_f32_16x16x32_bf16 a[112:115], v[168:171], v[128:131], 0\n"
    "ds_read_b128 v[160:163], v201 offset:45056\n"
    "v_mul_f32_dpp v244, v228, v244 row_newbcast:4 row_mask:0xf bank_mask:0xf\n"
    "v_mfma_f32_16x16x32_bf16 a[112:115], v[172:175], v[144:147], a[112:115]\n"
    "v_exp_f32 v49, v49\n"
    "v_mfma_f32_16x16x32_bf16 a[116:119], v[168:171], v[132:135], 0\n"
    "ds_read_b128 v[164:167], v201 offset:46080\n"
    "v_cvt_f32_i32 v236, v236\n"
    "v_mfma_f32_16x16x32_bf16 a[116:119], v[172:175], v[148:151], a[116:119]\n"
    "s_waitcnt lgkmcnt(4)\n"
    "v_mul_f32_dpp v245, v228, v245 row_newbcast:5 row_mask:0xf bank_mask:0xf\n"
    "v_cvt_f32_i32 v237, v237\n"
    "v_mfma_f32_16x16x32_bf16 a[128:131], v[176:179], v[128:131], 0\n"
    "ds_read_b128 v[168:171], v201 offset:47104\n"
    "v_mul_f32_dpp v246, v228, v246 row_newbcast:6 row_mask:0xf bank_mask:0xf\n"
    "v_mfma_f32_16x16x32_bf16 a[128:131], v[180:183], v[144:147], a[128:131]\n"
    "v_exp_f32 v50, v50\n"
    "v_mfma_f32_16x16x32_bf16 a[132:135], v[176:179], v[132:135], 0\n"
    "ds_read_b128 v[172:175], v201 offset:48128\n"
    "v_cvt_f32_i32 v238, v238\n"
    "v_mfma_f32_16x16x32_bf16 a[132:135], v[180:183], v[148:151], a[132:135]\n"
    "s_waitcnt lgkmcnt(4)\n"
    "v_mul_f32_dpp v247, v228, v247 row_newbcast:7 row_mask:0xf bank_mask:0xf\n"
    "v_cvt_f32_i32 v239, v239\n"
    "v_mfma_f32_16x16x32_bf16 a[144:147], v[184:187], v[128:131], 0\n"
    "v_exp_f32 v51, v51\n"
    "v_mfma_f32_16x16x32_bf16 a[144:147], v[188:191], v[144:147], a[144:147]\n"
    "v_add_f32_dpp v240, v229, v240 row_newbcast:0 row_mask:0xf bank_mask:0xf\n"
    "v_mul_f32_dpp v232, v226, v232 row_newbcast:0 row_mask:0xf bank_mask:0xf\n"
    "v_mfma_f32_16x16x32_bf16 a[148:151], v[184:187], v[132:135], 0\n"
    "v_add_f32_dpp v241, v229, v241 row_newbcast:1 row_mask:0xf bank_mask:0xf\n"
    "v_mul_f32_dpp v233, v226, v233 row_newbcast:1 row_mask:0xf bank_mask:0xf\n"
    "v_mfma_f32_16x16x32_bf16 a[148:151], v[188:191], v[148:151], a[148:151]\n"
    "s_waitcnt lgkmcnt(2)\n"
    "v_exp_f32 v52, v52\n"
    "v_mfma_f32_16x16x32_bf16 a[160:163], v[160:163], v[128:131], 0\n"
    "v_add_f32_dpp v242, v229, v242 row_newbcast:2 row_mask:0xf bank_mask:0xf\n"
    "v_mul_f32_dpp v234, v226, v234 row_newbcast:2 row_mask:0xf bank_mask:0xf\n"
    "v_mfma_f32_16x16x32_bf16 a[160:163], v[164:167], v[144:147], a[160:163]\n"
    "v_add_f32_dpp v243, v229, v243 row_newbcast:3 row_mask:0xf bank_mask:0xf\n"
    "v_mul_f32_dpp v235, v226, v235 row_newbcast:3 row_mask:0xf bank_mask:0xf\n"
    "v_mfma_f32_16x16x32_bf16 a[164:167], v[160:163], v[132:135], 0\n"
    "v_exp_f32 v53, v53\n"
    "v_mfma_f32_16x16x32_bf16 a[164:167], v[164:167], v[148:151], a[164:167]\n"
    "s_waitcnt lgkmcnt(0)\n"
    "v_add_f32_dpp v244, v229, v244 row_newbcast:4 row_mask:0xf bank_mask:0xf\n"
    "v_mul_f32_dpp v236, v226, v236 row_newbcast:4 row_mask:0xf bank_mask:0xf\n"
    "v_mfma_f32_16x16x32_bf16 a[176:179], v[168:171], v[128:131], 0\n"
    "v_add_f32_dpp v245, v229, v245 row_newbcast:5 row_mask:0xf bank_mask:0xf\n"
    "v_mul_f32_dpp v237, v226, v237 row_newbcast:5 row_mask:0xf bank_mask:0xf\n"
    "v_mfma_f32_16x16x32_bf16 a[176:179], v[172:175], v[144:147], a[176:179]\n"
    "v_exp_f32 v54, v54\n"
    "v_mfma_f32_16x16x32_bf16 a[180:183], v[168:171], v[132:135], 0\n"
    "v_add_f32_dpp v246, v229, v246 row_newbcast:6 row_mask:0xf bank_mask:0xf\n"
    "v_mul_f32_dpp v238, v226, v238 row_newbcast:6 row_mask:0xf bank_mask:0xf\n"
    "v_add_f32_dpp v247, v229, v247 row_newbcast:7 row_mask:0xf bank_mask:0xf\n"
    "v_exp_f32 v55, v55\n"
    "v_mul_f32_dpp v239, v226, v239 row_newbcast:7 row_mask:0xf bank_mask:0xf\n"
    "v_cvt_pk_bf16_f32 v240, v240, v241\n"
    "v_add_f32_dpp v232, v227, v232 row_newbcast:0 row_mask:0xf bank_mask:0xf\n"
    "v_cvt_pk_bf16_f32 v241, v242, v243\n"
    "v_add_f32_dpp v233, v227, v233 row_newbcast:1 row_mask:0xf bank_mask:0xf\n"
    "v_cvt_pk_bf16_f32 v242, v244, v245\n"
    "v_add_f32_dpp v234, v227, v234 row_newbcast:2 row_mask:0xf bank_mask:0xf\n"
    "v_cvt_pk_bf16_f32 v243, v246, v247\n"
    "v_add_f32_dpp v235, v227, v235 row_newbcast:3 row_mask:0xf bank_mask:0xf\n"
    "v_add_f32_dpp v236, v227, v236 row_newbcast:4 row_mask:0xf bank_mask:0xf\n"
    "v_add_f32_dpp v237, v227, v237 row_newbcast:5 row_mask:0xf bank_mask:0xf\n"
    "v_add_f32_dpp v238, v227, v238 row_newbcast:6 row_mask:0xf bank_mask:0xf\n"
    "v_add_f32_dpp v239, v227, v239 row_newbcast:7 row_mask:0xf bank_mask:0xf\n"
    "v_cvt_pk_bf16_f32 v232, v232, v233\n"
    "v_cvt_pk_bf16_f32 v233, v234, v235\n"
    "v_cvt_pk_bf16_f32 v234, v236, v237\n"
    "v_cvt_pk_bf16_f32 v235, v238, v239\n"
    "v_mfma_f32_16x16x32_bf16 a[180:183], v[172:175], v[148:151], a[180:183]\n"
    "v_add_f32 v113, v113, v32\n"
    "v_add_f32 v114, v114, v33\n"
    "v_add_f32 v113, v113, v34\n"
    "v_add_f32 v114, v114, v35\n"
    "v_add_f32 v117, v117, v36\n"
    "v_add_f32 v118, v118, v37\n"
    "v_add_f32 v117, v117, v38\n"
    "v_add_f32 v118, v118, v39\n"
    "v_add_f32 v113, v113, v48\n"
    "v_add_f32 v114, v114, v49\n"
    "v_add_f32 v113, v113, v50\n"
    "v_add_f32 v114, v114, v51\n"
    "v_add_f32 v117, v117, v52\n"
    "buffer_store_dwordx4 v[232:235], v211, s[36:39], s62 offen sc1\n"
    "buffer_store_dwordx4 v[240:243], v212, s[40:43], s63 offen sc1\n"
    "v_add_f32 v118, v118, v53\n"
    "v_add_f32 v117, v117, v54\n"
    "v_add_f32 v118, v118, v55\n"
    "s_waitcnt vmcnt(8)\n"
    "ds_read_b128 a[240:243], v158 offset:49152\n"
    "ds_read_b128 a[244:247], v159 offset:49152\n"
    "ds_read_b128 a[248:251], v158 offset:49280\n"
    "ds_read_b128 a[252:255], v159 offset:49280\n"
    "ds_read_b128 v[0:3], v158 offset:49408\n"
    "ds_read_b128 v[4:7], v159 offset:49408\n"
    "v_cmp_ne_u32 s[80:81], s67, v219\n"
    "v_cmp_gt_u32 vcc, s66, v218\n"
    "s_or_b64 vcc, vcc, s[80:81]\n"
    "s_cbranch_vccnz LSPRE1\n"
    "LSPB1:\n"
    "v_mov_b32 v220, 4\n"
    "s_add_u32 s66, s66, s74\n"
    "s_waitcnt lgkmcnt(0)\n"
    "s_barrier\n"
    "buffer_store_dwordx2 v[220:221], v214, s[44:47], 0 offen sc1\n"
    "ds_read_b128 v[160:163], v255\n"
    "ds_read_b128 v[164:167], v255 offset:1024\n"
    "ds_read_b128 v[168:171], v255 offset:2048\n"
    "ds_read_b128 v[172:175], v255 offset:3072\n"
    "ds_read_b128 v[176:179], v255 offset:4096\n"
    "ds_read_b128 v[180:183], v255 offset:5120\n"
    "s_waitcnt lgkmcnt(4)\n"
    "v_mfma_f32_16x16x32_bf16 v[44:47], v[160:163], a[240:243], 0\n"
    "ds_read_b128 v[184:187], v255 offset:6144\n"
    "v_mfma_f32_16x16x32_bf16 v[44:47], v[164:167], a[244:247], v[44:47]\n"
    "s_waitcnt lgkmcnt(3)\n"
    "ds_read_b128 v[188:191], v255 offset:7168\n"
    "v_mfma_f32_16x16x32_bf16 v[44:47], v[168:171], a[248:251], v[44:47]\n"
    "s_add_u32 m0, s71, 0xc000\n"
    "ds_read_b128 v[160:163], v255 offset:8192\n"
    "v_mfma_f32_16x16x32_bf16 v[44:47], v[172:175], a[252:255], v[44:47]\n"
    "s_waitcnt lgkmcnt(3)\n"
    "ds_read_b128 v[164:167], v255 offset:9216\n"
    "buffer_load_dwordx4 v200, s[40:43], s61 offen sc1 lds\n"
    "v_mfma_f32_16x16x32_bf16 v[44:47], v[176:179], v[0:3], v[44:47]\n"
    "ds_read_b128 v[168:171], v255 offset:10240\n"
    "v_mfma_f32_16x16x32_bf16 v[44:47], v[180:183], v[4:7], v[44:47]\n"
    "s_waitcnt lgkmcnt(3)\n"
    "ds_read_b128 v[172:175], v255 offset:11264\n"
    "v_mfma_f32_16x16x32_bf16 v[60:63], v[184:187], a[240:243], 0\n"
    "ds_read_b128 v[176:179], v200\n"
    "v_mfma_f32_16x16x32_bf16 v[60:63], v[188:191], a[244:247], v[60:63]\n"
    "s_waitcnt lgkmcnt(3)\n"
    "ds_read_b128 v[180:183], v200 offset:1024\n"
    "v_mfma_f32_16x16x32_bf16 v[60:63], v[160:163], a[248:251], v[60:63]\n"
    "ds_read_b128 v[184:187], v200 offset:2048\n"
    "buffer_load_dwordx4 v200, s[40:43], s61 offen offset:1024 sc1 lds\n"
    "v_mfma_f32_16x16x32_bf16 v[60:63], v[164:167], a[252:255], v[60:63]\n"
    "s_waitcnt lgkmcnt(3)\n"
    "ds_read_b128 v[188:191], v200 offset:3072\n"
    "v_mfma_f32_16x16x32_bf16 v[60:63], v[168:171], v[0:3], v[60:63]\n"
    "ds_read_b128 v[160:163], v200 offset:4096\n"
    "v_mfma_f32_16x16x32_bf16 v[60:63], v[172:175], v[4:7], v[60:63]\n"
    "s_waitcnt lgkmcnt(3)\n"
    "ds_read_b128 v[164:167], v200 offset:5120\n"
    "v_mfma_f32_16x16x32_bf16 v[72:75], v[176:179], a[240:243], 0\n"
    "ds_read_b128 v[168:171], v200 offset:6144\n"
    "v_mfma_f32_16x16x32_bf16 v[72:75], v[180:183], a[244:247], v[72:75]\n"
    "s_waitcnt lgkmcnt(3)\n"
    "ds_read_b128 v[172:175], v200 offset:7168\n"
    "buffer_load_dwordx4 v200, s[40:43], s61 offen offset:2048 sc1 lds\n"
    "v_mfma_f32_16x16x32_bf16 v[72:75], v[184:187], a[248:251], v[72:75]\n"
    "ds_read_b128 v[176:179], v200 offset:8192\n"
    "v_mfma_f32_16x16x32_bf16 v[72:75], v[188:191], a[252:255], v[72:75]\n"
    "s_waitcnt lgkmcnt(3)\n"
    "ds_read_b128 v[180:183], v200 offset:9216\n"
    "v_mfma_f32_16x16x32_bf16 v[72:75], v[160:163], v[0:3], v[72:75]\n"
    "ds_read_b128 v[184:187], v200 offset:10240\n"
    "v_mfma_f32_16x16x32_bf16 v[72:75], v[164:167], v[4:7], v[72:75]\n"
    "s_waitcnt lgkmcnt(3)\n"
    "ds_read_b128 v[188:191], v200 offset:11264\n"
    "v_max3_f32 v196, v44, v45, v46\n"
    "v_max3_f32 v196, v196, v47, v60\n"
    "v_max3_f32 v196, v196, v61, v62\n"
    "v_mfma_f32_16x16x32_bf16 v[88:91], v[168:171], a[240:243], 0\n"
    "ds_read_b128 v[160:163], v200 offset:12288\n"
    "buffer_load_dwordx4 v200, s[40:43], s61 offen offset:3072 sc1 lds\n"
    "v_max_f32 v196, v196, v63\n"
    "v_mfma_f32_16x16x32_bf16 v[88:91], v[172:175], a[244:247], v[88:91]\n"
    "s_waitcnt lgkmcnt(3)\n"
    "ds_read_b128 v[164:167], v200 offset:13312\n"
    "v_mov_b32 v197, v196\n"
    "s_nop 1\n"
    "v_permlane32_swap_b32 v196, v197\n"
    "v_max_f32 v196, v196, v197\n"
    "v_mfma_f32_16x16x32_bf16 v[88:91], v[176:179], a[248:251], v[88:91]\n"
    "ds_read_b128 v[168:171], v200 offset:14336\n"
    "v_mov_b32 v197, v196\n"
    "s_nop 1\n"
    "v_permlane16_swap_b32 v196, v197\n"
    "v_max_f32 v196, v196, v197\n"
    "v_mfma_f32_16x16x32_bf16 v[88:91], v[180:183], a[252:255], v[88:91]\n"
    "s_waitcnt lgkmcnt(3)\n"
    "s_add_u32 s65, s61, 0x1000\n"
    "ds_read_b128 v[172:175], v200 offset:15360\n"
    "v_add_f32 v196, 0x42640000, v196\n"
    "v_sub_f32 v104, 0, v196\n"
    "v_mfma_f32_16x16x32_bf16 v[88:91], v[184:187], v[0:3], v[88:91]\n"
    "s_add_u32 m0, s71, 0xd000\n"
    "ds_read_b128 v[176:179], v200 offset:16384\n"
    "v_sub_f32 v105, 0, v196\n"
    "v_sub_f32 v106, 0, v196\n"
    "v_sub_f32 v107, 0, v196\n"
    "v_mfma_f32_16x16x32_bf16 v[88:91], v[188:191], v[4:7], v[88:91]\n"
    "s_waitcnt lgkmcnt(3)\n"
    "ds_read_b128 v[180:183], v200 offset:17408\n"
    "buffer_load_dwordx4 v200, s[40:43], s65 offen sc1 lds\n"
    "v_add_f32 v44, v44, v104\n"
    "v_mfma_f32_16x16x32_bf16 v[40:43], v[160:163], a[240:243], v[104:107]\n"
    "ds_read_b128 v[184:187], v200 offset:18432\n"
    "v_add_f32 v45, v45, v104\n"
    "v_add_f32 v46, v46, v104\n"
    "v_add_f32 v47, v47, v104\n"
    "v_mfma_f32_16x16x32_bf16 v[40:43], v[164:167], a[244:247], v[40:43]\n"
    "s_waitcnt lgkmcnt(3)\n"
    "ds_read_b128 v[188:191], v200 offset:19456\n"
    "v_add_f32 v60, v60, v104\n"
    "v_add_f32 v61, v61, v104\n"
    "v_add_f32 v62, v62, v104\n"
    "v_mfma_f32_16x16x32_bf16 v[40:43], v[168:171], a[248:251], v[40:43]\n"
    "ds_read_b128 v[160:163], v200 offset:20480\n"
    "v_add_f32 v63, v63, v104\n"
    "v_exp_f32 v44, v44\n"
    "v_mfma_f32_16x16x32_bf16 v[40:43], v[172:175], a[252:255], v[40:43]\n"
    "s_waitcnt lgkmcnt(3)\n"
    "ds_read_b128 v[164:167], v200 offset:21504\n"
    "v_exp_f32 v45, v45\n"
    "v_mfma_f32_16x16x32_bf16 v[40:43], v[176:179], v[0:3], v[40:43]\n"
    "ds_read_b128 v[168:171], v200 offset:22528\n"
    "buffer_load_dwordx4 v200, s[40:43], s65 offen offset:1024 sc1 lds\n"
    "v_mfma_f32_16x16x32_bf16 v[40:43], v[180:183], v[4:7], v[40:43]\n"
    "s_waitcnt lgkmcnt(3)\n"
    "ds_read_b128 v[172:175], v200 offset:23552\n"
    "v_exp_f32 v46, v46\n"
    "v_mfma_f32_16x16x32_bf16 v[56:59], v[184:187], a[240:243], v[104:107]\n"
    "ds_read_b128 v[176:179], v201 offset:24576\n"
    "v_exp_f32 v47, v47\n"
    "v_mfma_f32_16x16x32_bf16 v[56:59], v[188:191], a[244:247], v[56:59]\n"
    "s_waitcnt lgkmcnt(3)\n"
    "s_add_u32 s78, s64, 0x30000\n"
    "ds_read_b128 v[180:183], v201 offset:25600\n"
    "v_exp_f32 v60, v60\n"
    "v_mfma_f32_16x16x32_bf16 v[56:59], v[160:163], a[248:251], v[56:59]\n"
    "s_add_u32 m0, s72, 0x1b000\n"
    "ds_read_b128 v[184:187], v201 offset:26624\n"
    "v_exp_f32 v61, v61\n"
    "v_mfma_f32_16x16x32_bf16 v[56:59], v[164:167], a[252:255], v[56:59]\n"
    "s_waitcnt lgkmcnt(3)\n"
    "ds_read_b128 v[188:191], v201 offset:27648\n"
    "buffer_load_dwordx4 v202, s[32:35], s78 offen lds\n"
    "v_mfma_f32_16x16x32_bf16 v[56:59], v[168:171], v[0:3], v[56:59]\n"
    "ds_read_b128 v[160:163], v201 offset:28672\n"
    "v_exp_f32 v62, v62\n"
    "v_exp_f32 v63, v63\n"
    "v_cvt_pk_bf16_f32 v136, v44, v45\n"
    "v_cvt_pk_bf16_f32 v137, v46, v47\n"
    "v_cvt_pk_bf16_f32 v138, v60, v61\n"
    "v_cvt_pk_bf16_f32 v139, v62, v63\n"
    "v_add_f32 v72, v72, v104\n"
    "v_add_f32 v73, v73, v104\n"
    "v_add_f32 v74, v74, v104\n"
    "v_add_f32 v75, v75, v104\n"
    "v_add_f32 v88, v88, v104\n"
    "v_add_f32 v89, v89, v104\n"
    "v_add_f32 v90, v90, v104\n"
    "v_add_f32 v91, v91, v104\n"
    "v_exp_f32 v72, v72\n"
    "v_exp_f32 v73, v73\n"
    "v_exp_f32 v74, v74\n"
    "v_exp_f32 v75, v75\n"
    "v_exp_f32 v88, v88\n"
    "v_exp_f32 v89, v89\n"
    "v_exp_f32 v90, v90\n"
    "v_exp_f32 v91, v91\n"
    "v_cvt_pk_bf16_f32 v152, v72, v73\n"
    "v_cvt_pk_bf16_f32 v153, v74, v75\n"
    "v_cvt_pk_bf16_f32 v154, v88, v89\n"
    "v_cvt_pk_bf16_f32 v155, v90, v91\n"
    "v_mfma_f32_16x16x32_bf16 v[56:59], v[172:175], v[4:7], v[56:59]\n"
    "s_waitcnt lgkmcnt(3)\n"
    "ds_read_b128 v[164:167], v201 offset:29696\n"
    "v_exp_f32 v40, v40\n"
    "v_mfma_f32_16x16x32_bf16 a[8:11], v[176:179], v[136:139], 0\n"
    "ds_read_b128 v[168:171], v201 offset:30720\n"
    "v_mfma_f32_16x16x32_bf16 a[8:11], v[180:183], v[152:155], a[8:11]\n"
    "s_waitcnt lgkmcnt(3)\n"
    "ds_read_b128 v[172:175], v201 offset:31744\n"
    "v_mfma_f32_16x16x32_bf16 a[24:27], v[184:187], v[136:139], 0\n"
    "ds_read_b128 v[176:179], v201 offset:32768\n"
    "buffer_load_dwordx4 v203, s[32:35], s78 offen offset:1024 lds\n"
    "v_mfma_f32_16x16x32_bf16 a[24:27], v[188:191], v[152:155], a[24:27]\n"
    "s_waitcnt lgkmcnt(3)\n"
    "ds_read_b128 v[180:183], v201 offset:33792\n"
    "v_mfma_f32_16x16x32_bf16 a[40:43], v[160:163], v[136:139], 0\n"
    "ds_read_b128 v[184:187], v201 offset:34816\n"
    "v_mfma_f32_16x16x32_bf16 a[40:43], v[164:167], v[152:155], a[40:43]\n"
    "ds_read_b128 v[188:191], v201 offset:35840\n"
    "v_mfma_f32_16x16x32_bf16 v[120:123], v[192:195], v[136:139], 0\n"
    "v_exp_f32 v41, v41\n"
    "v_mfma_f32_16x16x32_bf16 v[120:123], v[192:195], v[152:155], v[120:123]\n"
    "s_waitcnt lgkmcnt(4)\n"
    "buffer_load_dwordx4 v204, s[32:35], s78 offen offset:2048 lds\n"
    "v_mfma_f32_16x16x32_bf16 a[56:59], v[168:171], v[136:139], 0\n"
    "ds_read_b128 v[160:163], v201 offset:36864\n"
    "v_mfma_f32_16x16x32_bf16 a[56:59], v[172:175], v[152:155], a[56:59]\n"
    "s_waitcnt lgkmcnt(3)\n"
    "ds_read_b128 v[164:167], v201 offset:37888\n"
    "v_mfma_f32_16x16x32_bf16 a[72:75], v[176:179], v[136:139], 0\n"
    "ds_read_b128 v[168:171], v201 offset:38912\n"
    "v_mfma_f32_16x16x32_bf16 a[72:75], v[180:183], v[152:155], a[72:75]\n"
    "s_waitcnt lgkmcnt(3)\n"
    "ds_read_b128 v[172:175], v201 offset:39936\n"
    "v_mfma_f32_16x16x32_bf16 a[88:91], v[184:187], v[136:139], 0\n"
    "ds_read_b128 v[176:179], v201 offset:40960\n"
    "v_mfma_f32_16x16x32_bf16 a[88:91], v[188:191], v[152:155], a[88:91]\n"
    "s_waitcnt lgkmcnt(3)\n"
    "ds_read_b128 v[180:183], v201 offset:41984\n"
    "v_mfma_f32_16x16x32_bf16 a[104:107], v[160:163], v[136:139], 0\n"
    "ds_read_b128 v[184:187], v201 offset:43008\n"
    "v_mfma_f32_16x16x32_bf16 a[104:107], v[164:167], v[152:155], a[104:107]\n"
    "s_waitcnt lgkmcnt(3)\n"
    "ds_read_b128 v[188:191], v201 offset:44032\n"
    "v_mfma_f32_16x16x32_bf16 a[120:123], v[168:171], v[136:139], 0\n"
    "ds_read_b128 v[160:163], v201 offset:45056\n"
    "v_mfma_f32_16x16x32_bf16 a[120:123], v[172:175], v[152:155], a[120:123]\n"
    "s_waitcnt lgkmcnt(3)\n"
    "ds_read_b128 v[164:167], v201 offset:46080\n"
    "v_mfma_f32_16x16x32_bf16 a[136:139], v[176:179], v[136:139], 0\n"
    "ds_read_b128 v[168:171], v201 offset:47104\n"
    "v_mfma_f32_16x16x32_bf16 a[136:139], v[180:183], v[152:155], a[136:139]\n"
    "s_waitcnt lgkmcnt(3)\n"
    "ds_read_b128 v[172:175], v201 offset:48128\n"
    "v_mfma_f32_16x16x32_bf16 a[152:155], v[184:187], v[136:139], 0\n"
    "v_exp_f32 v42, v42\n"
    "v_mfma_f32_16x16x32_bf16 a[152:155], v[188:191], v[152:155], a[152:155]\n"
    "s_waitcnt lgkmcnt(2)\n"
    "v_exp_f32 v43, v43\n"
    "v_mfma_f32_16x16x32_bf16 a[168:171], v[160:163], v[136:139], 0\n"
    "v_exp_f32 v56, v56\n"
    "v_mfma_f32_16x16x32_bf16 a[168:171], v[164:167], v[152:155], a[168:171]\n"
    "s_waitcnt lgkmcnt(0)\n"
    "v_exp_f32 v57, v57\n"
    "v_mfma_f32_16x16x32_bf16 a[184:187], v[168:171], v[136:139], 0\n"
    "v_exp_f32 v58, v58\n"
    "v_exp_f32 v59, v59\n"
    "v_mfma_f32_16x16x32_bf16 a[184:187], v[172:175], v[152:155], a[184:187]\n"
    "v_add_f32 v121, v121, v40\n"
    "v_add_f32 v122, v122, v41\n"
    "v_add_f32 v121, v121, v42\n"
    "v_add_f32 v122, v122, v43\n"
    "v_add_f32 v121, v121, v56\n"
    "v_add_f32 v122, v122, v57\n"
    "s_waitcnt vmcnt(12)\n"
    "ds_read_b128 v[8:11], v158 offset:24576\n"
    "ds_read_b128 v[12:15], v159 offset:24576\n"
    "ds_read_b128 v[16:19], v158 offset:24704\n"
    "ds_read_b128 v[20:23], v159 offset:24704\n"
    "ds_read_b128 v[24:27], v158 offset:24832\n"
    "ds_read_b128 v[28:31], v159 offset:24832\n"
    "s_waitcnt lgkmcnt(0)\n"
    "ds_read_b128 v[160:163], v255\n"
    "ds_read_b128 v[164:167], v255 offset:1024\n"
    "ds_read_b128 v[168:171], v255 offset:2048\n"
    "ds_read_b128 v[172:175], v255 offset:3072\n"
    "ds_read_b128 v[176:179], v255 offset:4096\n"
    "ds_read_b128 v[180:183], v255 offset:5120\n"
    "s_waitcnt lgkmcnt(4)\n"
    "v_mfma_f32_16x16x32_bf16 v[64:67], v[160:163], v[8:11], 0\n"
    "ds_read_b128 v[184:187], v255 offset:6144\n"
    "v_mfma_f32_16x16x32_bf16 v[64:67], v[164:167], v[12:15], v[64:67]\n"
    "s_waitcnt lgkmcnt(3)\n"
    "s_add_u32 m0, s71, 0x6000\n"
    "ds_read_b128 v[188:191], v255 offset:7168\n"
    "v_mfma_f32_16x16x32_bf16 v[64:67], v[168:171], v[16:19], v[64:67]\n"
    "ds_read_b128 v[160:163], v255 offset:8192\n"
    "buffer_load_dwordx4 v200, s[36:39], s60 offen sc1 lds\n"
    "v_mfma_f32_16x16x32_bf16 v[64:67], v[172:175], v[20:23], v[64:67]\n"
    "s_waitcnt lgkmcnt(3)\n"
    "ds_read_b128 v[164:167], v255 offset:9216\n"
    "v_mfma_f32_16x16x32_bf16 v[64:67], v[176:179], v[24:27], v[64:67]\n"
    "ds_read_b128 v[168:171], v255 offset:10240\n"
    "v_mfma_f32_16x16x32_bf16 v[64:67], v[180:183], v[28:31], v[64:67]\n"
    "s_waitcnt lgkmcnt(3)\n"
    "ds_read_b128 v[172:175], v255 offset:11264\n"
    "buffer_load_dwordx4 v200, s[36:39], s60 offen offset:1024 sc1 lds\n"
    "v_mfma_f32_16x16x32_bf16 v[80:83], v[184:187], v[8:11], 0\n"
    "ds_read_b128 v[176:179], v200\n"
    "v_mfma_f32_16x16x32_bf16 v[80:83], v[188:191], v[12:15], v[80:83]\n"
    "s_waitcnt lgkmcnt(3)\n"
    "ds_read_b128 v[180:183], v200 offset:1024\n"
    "v_mfma_f32_16x16x32_bf16 v[80:83], v[160:163], v[16:19], v[80:83]\n"
    "ds_read_b128 v[184:187], v200 offset:2048\n"
    "buffer_load_dwordx4 v200, s[36:39], s60 offen offset:2048 sc1 lds\n"
    "v_mfma_f32_16x16x32_bf16 v[80:83], v[164:167], v[20:23], v[80:83]\n"
    "s_waitcnt lgkmcnt(3)\n"
    "ds_read_b128 v[188:191], v200 offset:3072\n"
    "v_mfma_f32_16x16x32_bf16 v[80:83], v[168:171], v[24:27], v[80:83]\n"
    "ds_read_b128 v[160:163], v200 offset:4096\n"
    "v_mfma_f32_16x16x32_bf16 v[80:83], v[172:175], v[28:31], v[80:83]\n"
    "s_waitcnt lgkmcnt(3)\n"
    "ds_read_b128 v[164:167], v200 offset:5120\n"
    "buffer_load_dwordx4 v200, s[36:39], s60 offen offset:3072 sc1 lds\n"
    "v_mfma_f32_16x16x32_bf16 v[76:79], v[176:179], v[8:11], 0\n"
    "s_add_u32 s65, s60, 0x1000\n"
    "ds_read_b128 v[168:171], v200 offset:6144\n"
    "v_mfma_f32_16x16x32_bf16 v[76:79], v[180:183], v[12:15], v[76:79]\n"
    "s_waitcnt lgkmcnt(3)\n"
    "s_add_u32 m0, s71, 0x7000\n"
    "ds_read_b128 v[172:175], v200 offset:7168\n"
    "v_mfma_f32_16x16x32_bf16 v[76:79], v[184:187], v[16:19], v[76:79]\n"
    "ds_read_b128 v[176:179], v200 offset:8192\n"
    "buffer_load_dwordx4 v200, s[36:39], s65 offen sc1 lds\n"
    "v_mfma_f32_16x16x32_bf16 v[76:79], v[188:191], v[20:23], v[76:79]\n"
    "s_waitcnt lgkmcnt(3)\n"
    "ds_read_b128 v[180:183], v200 offset:9216\n"
    "v_mfma_f32_16x16x32_bf16 v[76:79], v[160:163], v[24:27], v[76:79]\n"
    "ds_read_b128 v[184:187], v200 offset:10240\n"
    "v_mfma_f32_16x16x32_bf16 v[76:79], v[164:167], v[28:31], v[76:79]\n"
    "s_waitcnt lgkmcnt(3)\n"
    "ds_read_b128 v[188:191], v200 offset:11264\n"
    "buffer_load_dwordx4 v200, s[36:39], s65 offen offset:1024 sc1 lds\n"
    "v_max3_f32 v196, v64, v65, v66\n"
    "v_mfma_f32_16x16x32_bf16 v[92:95], v[168:171], v[8:11], 0\n"
    "ds_read_b128 v[160:163], v200 offset:12288\n"
    "v_max3_f32 v196, v196, v67, v80\n"
    "v_max3_f32 v196, v196, v81, v82\n"
    "v_max_f32 v196, v196, v83\n"
    "v_mfma_f32_16x16x32_bf16 v[92:95], v[172:175], v[12:15], v[92:95]\n"
    "s_waitcnt lgkmcnt(3)\n"
    "ds_read_b128 v[164:167], v200 offset:13312\n"
    "v_mov_b32 v197, v196\n"
    "s_nop 1\n"
    "v_permlane32_swap_b32 v196, v197\n"
    "v_max_f32 v196, v196, v197\n"
    "v_mfma_f32_16x16x32_bf16 v[92:95], v[176:179], v[16:19], v[92:95]\n"
    "ds_read_b128 v[168:171], v200 offset:14336\n"
    "v_mov_b32 v197, v196\n"
    "s_nop 1\n"
    "v_permlane16_swap_b32 v196, v197\n"
    "v_max_f32 v196, v196, v197\n"
    "v_mfma_f32_16x16x32_bf16 v[92:95], v[180:183], v[20:23], v[92:95]\n"
    "s_waitcnt lgkmcnt(3)\n"
    "ds_read_b128 v[172:175], v200 offset:15360\n"
    "v_add_f32 v196, 0x42640000, v196\n"
    "v_sub_f32 v108, 0, v196\n"
    "v_sub_f32 v109, 0, v196\n"
    "v_mfma_f32_16x16x32_bf16 v[92:95], v[184:187], v[24:27], v[92:95]\n"
    "ds_read_b128 v[176:179], v200 offset:16384\n"
    "v_sub_f32 v110, 0, v196\n"
    "v_sub_f32 v111, 0, v196\n"
    "v_add_f32 v64, v64, v108\n"
    "v_mfma_f32_16x16x32_bf16 v[92:95], v[188:191], v[28:31], v[92:95]\n"
    "s_waitcnt lgkmcnt(3)\n"
    "ds_read_b128 v[180:183], v200 offset:17408\n"
    "v_add_f32 v65, v65, v108\n"
    "v_add_f32 v66, v66, v108\n"
    "v_add_f32 v67, v67, v108\n"
    "v_mfma_f32_16x16x32_bf16 v[44:47], v[160:163], v[8:11], v[108:111]\n"
    "ds_read_b128 v[184:187], v200 offset:18432\n"
    "v_add_f32 v80, v80, v108\n"
    "v_add_f32 v81, v81, v108\n"
    "v_add_f32 v82, v82, v108\n"
    "v_mfma_f32_16x16x32_bf16 v[44:47], v[164:167], v[12:15], v[44:47]\n"
    "s_waitcnt lgkmcnt(3)\n"
    "ds_read_b128 v[188:191], v200 offset:19456\n"
    "v_add_f32 v83, v83, v108\n"
    "v_exp_f32 v64, v64\n"
    "v_mfma_f32_16x16x32_bf16 v[44:47], v[168:171], v[16:19], v[44:47]\n"
    "ds_read_b128 v[160:163], v200 offset:20480\n"
    "buffer_load_dwordx2 v[218:219], v213, s[44:47], 0 offen sc1\n"
    "v_mfma_f32_16x16x32_bf16 v[44:47], v[172:175], v[20:23], v[44:47]\n"
    "s_waitcnt lgkmcnt(3)\n"
    "ds_read_b128 v[164:167], v200 offset:21504\n"
    "v_exp_f32 v65, v65\n"
    "v_mfma_f32_16x16x32_bf16 v[44:47], v[176:179], v[24:27], v[44:47]\n"
    "ds_read_b128 v[168:171], v200 offset:22528\n"
    "v_exp_f32 v66, v66\n"
    "v_mfma_f32_16x16x32_bf16 v[44:47], v[180:183], v[28:31], v[44:47]\n"
    "s_waitcnt lgkmcnt(3)\n"
    "ds_read_b128 v[172:175], v200 offset:23552\n"
    "v_exp_f32 v67, v67\n"
    "v_mfma_f32_16x16x32_bf16 v[60:63], v[184:187], v[8:11], v[108:111]\n"
    "ds_read_b128 v[176:179], v201 offset:24576\n"
    "v_exp_f32 v80, v80\n"
    "v_mfma_f32_16x16x32_bf16 v[60:63], v[188:191], v[12:15], v[60:63]\n"
    "s_waitcnt lgkmcnt(3)\n"
    "ds_read_b128 v[180:183], v201 offset:25600\n"
    "v_exp_f32 v81, v81\n"
    "v_mfma_f32_16x16x32_bf16 v[60:63], v[160:163], v[16:19], v[60:63]\n"
    "ds_read_b128 v[184:187], v201 offset:26624\n"
    "v_exp_f32 v82, v82\n"
    "v_mfma_f32_16x16x32_bf16 v[60:63], v[164:167], v[20:23], v[60:63]\n"
    "s_waitcnt lgkmcnt(3)\n"
    "ds_read_b128 v[188:191], v201 offset:27648\n"
    "v_exp_f32 v83, v83\n"
    "v_cvt_pk_bf16_f32 v140, v64, v65\n"
    "v_mfma_f32_16x16x32_bf16 v[60:63], v[168:171], v[24:27], v[60:63]\n"
    "ds_read_b128 v[160:163], v201 offset:28672\n"
    "v_cvt_pk_bf16_f32 v141, v66, v67\n"
    "v_cvt_pk_bf16_f32 v142, v80, v81\n"
    "v_cvt_pk_bf16_f32 v143, v82, v83\n"
    "v_add_f32 v76, v76, v108\n"
    "v_add_f32 v77, v77, v108\n"
    "v_add_f32 v78, v78, v108\n"
    "v_add_f32 v79, v79, v108\n"
    "v_add_f32 v92, v92, v108\n"
    "v_add_f32 v93, v93, v108\n"
    "v_add_f32 v94, v94, v108\n"
    "v_add_f32 v95, v95, v108\n"
    "v_exp_f32 v76, v76\n"
    "v_exp_f32 v77, v77\n"
    "v_exp_f32 v78, v78\n"
    "v_exp_f32 v79, v79\n"
    "v_exp_f32 v92, v92\n"
    "v_exp_f32 v93, v93\n"
    "v_exp_f32 v94, v94\n"
    "v_exp_f32 v95, v95\n"
    "v_cvt_pk_bf16_f32 v156, v76, v77\n"
    "v_cvt_pk_bf16_f32 v157, v78, v79\n"
    "v_cvt_pk_bf16_f32 v158, v92, v93\n"
    "v_cvt_pk_bf16_f32 v159, v94, v95\n"
    "v_mfma_f32_16x16x32_bf16 v[60:63], v[172:175], v[28:31], v[60:63]\n"
    "s_waitcnt lgkmcnt(3)\n"
    "ds_read_b128 v[164:167], v201 offset:29696\n"
    "v_exp_f32 v44, v44\n"
    "v_mfma_f32_16x16x32_bf16 a[12:15], v[176:179], v[140:143], 0\n"
    "ds_read_b128 v[168:171], v201 offset:30720\n"
    "v_mfma_f32_16x16x32_bf16 a[12:15], v[180:183], v[156:159], a[12:15]\n"
    "s_waitcnt lgkmcnt(3)\n"
    "ds_read_b128 v[172:175], v201 offset:31744\n"
    "v_mfma_f32_16x16x32_bf16 a[28:31], v[184:187], v[140:143], 0\n"
    "ds_read_b128 v[176:179], v201 offset:32768\n"
    "v_mfma_f32_16x16x32_bf16 a[28:31], v[188:191], v[156:159], a[28:31]\n"
    "s_waitcnt lgkmcnt(3)\n"
    "ds_read_b128 v[180:183], v201 offset:33792\n"
    "v_mfma_f32_16x16x32_bf16 a[44:47], v[160:163], v[140:143], 0\n"
    "ds_read_b128 v[184:187], v201 offset:34816\n"
    "v_mfma_f32_16x16x32_bf16 a[44:47], v[164:167], v[156:159], a[44:47]\n"
    "ds_read_b128 v[188:191], v201 offset:35840\n"
    "v_mfma_f32_16x16x32_bf16 v[124:127], v[192:195], v[140:143], 0\n"
    "v_exp_f32 v45, v45\n"
    "v_mfma_f32_16x16x32_bf16 v[124:127], v[192:195], v[156:159], v[124:127]\n"
    "s_waitcnt lgkmcnt(4)\n"
    "v_exp_f32 v46, v46\n"
    "v_mfma_f32_16x16x32_bf16 a[60:63], v[168:171], v[140:143], 0\n"
    "ds_read_b128 v[160:163], v201 offset:36864\n"
    "v_mfma_f32_16x16x32_bf16 a[60:63], v[172:175], v[156:159], a[60:63]\n"
    "s_waitcnt lgkmcnt(3)\n"
    "ds_read_b128 v[164:167], v201 offset:37888\n"
    "v_mfma_f32_16x16x32_bf16 a[76:79], v[176:179], v[140:143], 0\n"
    "ds_read_b128 v[168:171], v201 offset:38912\n"
    "v_mfma_f32_16x16x32_bf16 a[76:79], v[180:183], v[156:159], a[76:79]\n"
    "s_waitcnt lgkmcnt(3)\n"
    "ds_read_b128 v[172:175], v201 offset:39936\n"
    "v_mfma_f32_16x16x32_bf16 a[92:95], v[184:187], v[140:143], 0\n"
    "ds_read_b128 v[176:179], v201 offset:40960\n"
    "v_mfma_f32_16x16x32_bf16 a[92:95], v[188:191], v[156:159], a[92:95]\n"
    "s_waitcnt lgkmcnt(3)\n"
    "ds_read_b128 v[180:183], v201 offset:41984\n"
    "v_mfma_f32_16x16x32_bf16 a[108:111], v[160:163], v[140:143], 0\n"
    "ds_read_b128 v[184:187], v201 offset:43008\n"
    "v_mfma_f32_16x16x32_bf16 a[108:111], v[164:167], v[156:159], a[108:111]\n"
    "s_waitcnt lgkmcnt(3)\n"
    "ds_read_b128 v[188:191], v201 offset:44032\n"
    "v_mfma_f32_16x16x32_bf16 a[124:127], v[168:171], v[140:143], 0\n"
    "ds_read_b128 v[160:163], v201 offset:45056\n"
    "v_mfma_f32_16x16x32_bf16 a[124:127], v[172:175], v[156:159], a[124:127]\n"
    "s_waitcnt lgkmcnt(3)\n"
    "ds_read_b128 v[164:167], v201 offset:46080\n"
    "v_mfma_f32_16x16x32_bf16 a[140:143], v[176:179], v[140:143], 0\n"
    "ds_read_b128 v[168:171], v201 offset:47104\n"
    "v_mfma_f32_16x16x32_bf16 a[140:143], v[180:183], v[156:159], a[140:143]\n"
    "s_waitcnt lgkmcnt(3)\n"
    "ds_read_b128 v[172:175], v201 offset:48128\n"
    "v_mfma_f32_16x16x32_bf16 a[156:159], v[184:187], v[140:143], 0\n"
    "v_exp_f32 v47, v47\n"
    "v_mfma_f32_16x16x32_bf16 a[156:159], v[188:191], v[156:159], a[156:159]\n"
    "s_waitcnt lgkmcnt(2)\n"
    "v_exp_f32 v60, v60\n"
    "v_mfma_f32_16x16x32_bf16 a[172:175], v[160:163], v[140:143], 0\n"
    "v_exp_f32 v61, v61\n"
    "v_mfma_f32_16x16x32_bf16 a[172:175], v[164:167], v[156:159], a[172:175]\n"
    "s_waitcnt lgkmcnt(0)\n"
    "v_exp_f32 v62, v62\n"
    "v_mfma_f32_16x16x32_bf16 a[188:191], v[168:171], v[140:143], 0\n"
    "v_exp_f32 v63, v63\n"
    "v_mfma_f32_16x16x32_bf16 a[188:191], v[172:175], v[156:159], a[188:191]\n"
    "v_add_f32 v125, v125, v44\n"
    "v_add_f32 v126, v126, v45\n"
    "v_add_f32 v125, v125, v46\n"
    "v_add_f32 v126, v126, v47\n"
    "s_add_u32 s64, s64, 0x3c000\n"
    "s_add_u32 s62, s62, 0x6000\n"
    "s_add_u32 s63, s63, 0x6000\n"
    "s_add_u32 s60, s60, 0x6000\n"
    "s_add_u32 s61, s61, 0x6000\n"
    "v_add_u32 v209, 0x300, v209\n"
    "v_add_u32 v210, 0xc0, v210\n"
    "v_mov_b32 v220, 5\n"
    "s_waitcnt vmcnt(0)\n"
    "v_cmp_ne_u32 s[80:81], s67, v219\n"
    "v_cmp_gt_u32 vcc, s66, v218\n"
    "s_or_b64 vcc, vcc, s[80:81]\n"
    "s_cbranch_vccnz LSPRE2\n"
    "LSPB2:\n"
    "s_barrier\n"
    "s_cmp_eq_u32 s58, 0\n"
    "s_cbranch_scc1 LENT0\n"
    "s_cmp_eq_u32 s58, 1\n"
    "s_cbranch_scc1 LENT1\n"
    "s_cmp_eq_u32 s58, 2\n"
    "s_cbranch_scc1 LENT1\n"
    "LENT2:\n"
    "s_mov_b32 s59, 1\n"
    "s_add_u32 m0, s72, 0x21000\n"
    "s_nop 0\n"
    "buffer_load_dwordx4 v250, s[48:51], 0 offen offset:2048 lds\n"
    "s_add_u32 m0, s72, 0x18000\n"
    "s_nop 0\n"
    "buffer_load_dwordx4 v202, s[32:35], s64 offen lds\n"
    "buffer_load_dwordx4 v203, s[32:35], s64 offen offset:1024 lds\n"
    "buffer_load_dwordx4 v204, s[32:35], s64 offen offset:2048 lds\n"
    "s_add_u32 s64, s64, 0xc000\n"
    "s_mov_b32 s94, 0x3000\n"
    "s_mov_b32 s95, 0x6000\n"
    "s_add_u32 s51, s72, 0x18000\n"
    "v_add_u32 v205, 0x3000, v205\n"
    "v_add_u32 v206, 0x3000, v206\n"
    "v_add_u32 v207, 0x3000, v207\n"
    "v_add_f32 v121, v121, v58\n"
    "v_add_f32 v122, v122, v59\n"
    "ds_read_b128 v[160:163], v200 offset:24576\n"
    "v_add_f32 v125, v125, v60\n"
    "v_add_f32 v126, v126, v61\n"
    "v_add_f32 v125, v125, v62\n"
    "ds_read_b128 v[164:167], v200 offset:25600\n"
    "v_add_f32 v126, v126, v63\n"
    "ds_read_b64 v[228:229], v210 offset:64\n"
    "ds_read2st64_b32 v[240:241], v207 offset0:96 offset1:99\n"
    "ds_read2st64_b32 v[242:243], v207 offset0:102 offset1:105\n"
    "ds_read2st64_b32 v[244:245], v207 offset0:108 offset1:111\n"
    "ds_read2st64_b32 v[246:247], v207 offset0:114 offset1:117\n"
    "ds_read_b128 v[168:171], v200 offset:26624\n"
    "v_cvt_pk_bf16_f32 v140, v44, v45\n"
    "v_cvt_pk_bf16_f32 v141, v46, v47\n"
    "v_cvt_pk_bf16_f32 v136, v40, v41\n"
    "v_cvt_pk_bf16_f32 v137, v42, v43\n"
    "ds_read_b128 v[172:175], v200 offset:27648\n"
    "v_cvt_pk_bf16_f32 v132, v36, v37\n"
    "v_cvt_pk_bf16_f32 v133, v38, v39\n"
    "s_waitcnt lgkmcnt(5)\n"
    "v_cvt_f32_i32 v240, v240\n"
    "v_cvt_f32_i32 v241, v241\n"
    "s_nop 7\n"
    ".p2align 6\n"
    "LLOOP2:\n"
    "v_mfma_f32_16x16x32_bf16 v[64:67], v[160:163], a[192:195], v[96:99]\n"
    "ds_read_b128 v[176:179], v200 offset:28672\n"
    "s_waitcnt lgkmcnt(5)\n"
    "v_cvt_f32_i32 v242, v242\n"
    "v_mfma_f32_16x16x32_bf16 v[64:67], v[164:167], a[196:199], v[64:67]\n"
    "s_waitcnt lgkmcnt(1)\n"
    "v_cvt_f32_i32 v243, v243\n"
    "v_cvt_f32_i32 v244, v244\n"
    "v_mfma_f32_16x16x32_bf16 v[64:67], v[168:171], a[200:203], v[64:67]\n"
    "v_cvt_f32_i32 v245, v245\n"
    "v_cvt_f32_i32 v246, v246\n"
    "v_mfma_f32_16x16x32_bf16 v[68:71], v[168:171], a[224:227], v[100:103]\n"
    "v_cvt_f32_i32 v247, v247\n"
    "v_mul_f32_dpp v240, v228, v240 row_newbcast:0 row_mask:0xf bank_mask:0xf\n"
    "v_mfma_f32_16x16x32_bf16 v[68:71], v[164:167], a[220:223], v[68:71]\n"
    "ds_read_b128 v[180:183], v200 offset:29696\n"
    "v_mul_f32_dpp v241, v228, v241 row_newbcast:1 row_mask:0xf bank_mask:0xf\n"
    "v_mfma_f32_16x16x32_bf16 v[68:71], v[160:163], a[216:219], v[68:71]\n"
    "v_mul_f32_dpp v242, v228, v242 row_newbcast:2 row_mask:0xf bank_mask:0xf\n"
    "v_mul_f32_dpp v243, v228, v243 row_newbcast:3 row_mask:0xf bank_mask:0xf\n"
    "v_mfma_f32_16x16x32_bf16 v[72:75], v[160:163], a[240:243], v[104:107]\n"
    "v_mul_f32_dpp v244, v228, v244 row_newbcast:4 row_mask:0xf bank_mask:0xf\n"
    "v_mul_f32_dpp v245, v228, v245 row_newbcast:5 row_mask:0xf bank_mask:0xf\n"
    "v_mfma_f32_16x16x32_bf16 v[72:75], v[164:167], a[244:247], v[72:75]\n"
    "v_mul_f32_dpp v246, v228, v246 row_newbcast:6 row_mask:0xf bank_mask:0xf\n"
    "v_mul_f32_dpp v247, v228, v247 row_newbcast:7 row_mask:0xf bank_mask:0xf\n"
    "v_mfma_f32_16x16x32_bf16 v[72:75], v[168:171], a[248:251], v[72:75]\n"
    "v_add_f32_dpp v240, v229, v240 row_newbcast:0 row_mask:0xf bank_mask:0xf\n"
    "v_add_f32_dpp v241, v229, v241 row_newbcast:1 row_mask:0xf bank_mask:0xf\n"
    "v_mfma_f32_16x16x32_bf16 v[76:79], v[168:171], v[16:19], v[108:111]\n"
    "v_add_f32_dpp v242, v229, v242 row_newbcast:2 row_mask:0xf bank_mask:0xf\n"
    "v_add_f32_dpp v243, v229, v243 row_newbcast:3 row_mask:0xf bank_mask:0xf\n"
    "v_mfma_f32_16x16x32_bf16 v[76:79], v[164:167], v[12:15], v[76:79]\n"
    "v_add_f32_dpp v244, v229, v244 row_newbcast:4 row_mask:0xf bank_mask:0xf\n"
    "v_add_f32_dpp v245, v229, v245 row_newbcast:5 row_mask:0xf bank_mask:0xf\n"
    "v_mfma_f32_16x16x32_bf16 v[76:79], v[160:163], v[8:11], v[76:79]\n"
    "s_waitcnt lgkmcnt(1)\n"
    "v_add_f32_dpp v246, v229, v246 row_newbcast:6 row_mask:0xf bank_mask:0xf\n"
    "v_add_f32_dpp v247, v229, v247 row_newbcast:7 row_mask:0xf bank_mask:0xf\n"
    "v_mfma_f32_16x16x32_bf16 v[76:79], v[172:175], v[20:23], v[76:79]\n"
    "ds_read_b128 v[160:163], v200 offset:33792\n"
    "v_cvt_pk_bf16_f32 v240, v240, v241\n"
    "v_mfma_f32_16x16x32_bf16 v[76:79], v[176:179], v[24:27], v[76:79]\n"
    "s_waitcnt lgkmcnt(1)\n"
    "v_cvt_pk_bf16_f32 v241, v242, v243\n"
    "v_cvt_pk_bf16_f32 v242, v244, v245\n"
    "v_mfma_f32_16x16x32_bf16 v[76:79], v[180:183], v[28:31], v[76:79]\n"
    "v_cvt_pk_bf16_f32 v243, v246, v247\n"
    "v_cvt_pk_bf16_f32 v128, v32, v33\n"
    "v_mfma_f32_16x16x32_bf16 v[72:75], v[180:183], v[4:7], v[72:75]\n"
    "ds_read_b128 v[164:167], v200 offset:34816\n"
    "v_cvt_pk_bf16_f32 v129, v34, v35\n"
    "v_mfma_f32_16x16x32_bf16 v[72:75], v[176:179], v[0:3], v[72:75]\n"
    "buffer_store_dwordx4 v[240:243], v212, s[40:43], s63 offen sc1\n"
    "v_cvt_pk_bf16_f32 v142, v60, v61\n"
    "v_cvt_pk_bf16_f32 v143, v62, v63\n"
    "v_cvt_pk_bf16_f32 v138, v56, v57\n"
    "v_mfma_f32_16x16x32_bf16 v[72:75], v[172:175], a[252:255], v[72:75]\n"
    "v_cvt_pk_bf16_f32 v139, v58, v59\n"
    "v_cvt_pk_bf16_f32 v134, v52, v53\n"
    "v_mfma_f32_16x16x32_bf16 v[68:71], v[172:175], a[228:231], v[68:71]\n"
    "ds_read_b128 v[168:171], v200 offset:35840\n"
    "v_cvt_pk_bf16_f32 v135, v54, v55\n"
    "v_mfma_f32_16x16x32_bf16 v[68:71], v[176:179], a[232:235], v[68:71]\n"
    "v_cvt_pk_bf16_f32 v130, v48, v49\n"
    "v_cvt_pk_bf16_f32 v131, v50, v51\n"
    "v_mfma_f32_16x16x32_bf16 v[68:71], v[180:183], a[236:239], v[68:71]\n"
    "v_mfma_f32_16x16x32_bf16 v[64:67], v[180:183], a[212:215], v[64:67]\n"
    "v_mfma_f32_16x16x32_bf16 v[64:67], v[176:179], a[208:211], v[64:67]\n"
    "v_mfma_f32_16x16x32_bf16 v[64:67], v[172:175], a[204:207], v[64:67]\n"
    "s_waitcnt lgkmcnt(1)\n"
    "v_mfma_f32_16x16x32_bf16 v[80:83], v[160:163], a[204:207], v[96:99]\n"
    "ds_read_b128 v[172:175], v200 offset:30720\n"
    "v_mfma_f32_16x16x32_bf16 v[80:83], v[164:167], a[208:211], v[80:83]\n"
    "s_waitcnt lgkmcnt(1)\n"
    "v_mfma_f32_16x16x32_bf16 v[80:83], v[168:171], a[212:215], v[80:83]\n"
    "v_mfma_f32_16x16x32_bf16 v[84:87], v[168:171], a[236:239], v[100:103]\n"
    "ds_read_b128 v[176:179], v200 offset:31744\n"
    "v_mfma_f32_16x16x32_bf16 v[84:87], v[164:167], a[232:235], v[84:87]\n"
    "v_exp_f32 v64, v64\n"
    "v_mfma_f32_16x16x32_bf16 v[84:87], v[160:163], a[228:231], v[84:87]\n"
    "v_exp_f32 v65, v65\n"
    "v_mfma_f32_16x16x32_bf16 v[88:91], v[160:163], a[252:255], v[104:107]\n"
    "ds_read_b128 v[180:183], v200 offset:32768\n"
    "v_cvt_pk_bf16_f32 v144, v64, v65\n"
    "v_mfma_f32_16x16x32_bf16 v[88:91], v[164:167], v[0:3], v[88:91]\n"
    "s_add_u32 s65, s60, 0xfffff800\n"
    "v_exp_f32 v66, v66\n"
    "v_mfma_f32_16x16x32_bf16 v[88:91], v[168:171], v[4:7], v[88:91]\n"
    "s_mov_b32 m0, 0x4000\n"
    "v_exp_f32 v67, v67\n"
    "v_mfma_f32_16x16x32_bf16 v[92:95], v[168:171], v[28:31], v[108:111]\n"
    "v_exp_f32 v68, v68\n"
    "v_mfma_f32_16x16x32_bf16 v[92:95], v[164:167], v[24:27], v[92:95]\n"
    "buffer_load_dwordx4 v200, s[36:39], s65 offen sc1 lds\n"
    "v_mfma_f32_16x16x32_bf16 v[92:95], v[160:163], v[20:23], v[92:95]\n"
    "s_waitcnt lgkmcnt(1)\n"
    "v_exp_f32 v69, v69\n"
    "v_mfma_f32_16x16x32_bf16 v[92:95], v[172:175], v[8:11], v[92:95]\n"
    "ds_read_b128 v[160:163], v200 offset:36864\n"
    "v_cvt_pk_bf16_f32 v145, v66, v67\n"
    "v_mfma_f32_16x16x32_bf16 v[92:95], v[176:179], v[12:15], v[92:95]\n"
    "s_waitcnt lgkmcnt(1)\n"
    "v_exp_f32 v70, v70\n"
    "v_mfma_f32_16x16x32_bf16 v[92:95], v[180:183], v[16:19], v[92:95]\n"
    "buffer_load_dwordx4 v200, s[36:39], s65 offen offset:1024 sc1 lds\n"
    "v_mfma_f32_16x16x32_bf16 v[88:91], v[180:183], a[248:251], v[88:91]\n"
    "ds_read_b128 v[164:167], v200 offset:37888\n"
    "v_cvt_pk_bf16_f32 v148, v68, v69\n"
    "v_mfma_f32_16x16x32_bf16 v[88:91], v[176:179], a[244:247], v[88:91]\n"
    "v_exp_f32 v71, v71\n"
    "v_mfma_f32_16x16x32_bf16 v[88:91], v[172:175], a[240:243], v[88:91]\n"
    "v_exp_f32 v72, v72\n"
    "v_mfma_f32_16x16x32_bf16 v[84:87], v[172:175], a[216:219], v[84:87]\n"
    "ds_read_b128 v[168:171], v200 offset:38912\n"
    "v_cvt_pk_bf16_f32 v149, v70, v71\n"
    "v_mfma_f32_16x16x32_bf16 v[84:87], v[176:179], a[220:223], v[84:87]\n"
    "buffer_load_dwordx4 v200, s[36:39], s65 offen offset:2048 sc1 lds\n"
    "v_mfma_f32_16x16x32_bf16 v[84:87], v[180:183], a[224:227], v[84:87]\n"
    "v_exp_f32 v73, v73\n"
    "v_mfma_f32_16x16x32_bf16 v[80:83], v[180:183], a[200:203], v[80:83]\n"
    "v_exp_f32 v74, v74\n"
    "v_mfma_f32_16x16x32_bf16 v[80:83], v[176:179], a[196:199], v[80:83]\n"
    "v_exp_f32 v75, v75\n"
    "v_mfma_f32_16x16x32_bf16 v[80:83], v[172:175], a[192:195], v[80:83]\n"
    "s_waitcnt lgkmcnt(1)\n"
    "buffer_load_dwordx4 v200, s[36:39], s65 offen offset:3072 sc1 lds\n"
    "v_mfma_f32_16x16x32_bf16 v[32:35], v[160:163], a[192:195], v[96:99]\n"
    "ds_read_b128 v[172:175], v200 offset:39936\n"
    "v_cvt_pk_bf16_f32 v152, v72, v73\n"
    "v_mfma_f32_16x16x32_bf16 v[32:35], v[164:167], a[196:199], v[32:35]\n"
    "s_waitcnt lgkmcnt(1)\n"
    "s_add_u32 s65, s60, 0x800\n"
    "v_cvt_pk_bf16_f32 v153, v74, v75\n"
    "v_mfma_f32_16x16x32_bf16 v[32:35], v[168:171], a[200:203], v[32:35]\n"
    "s_mov_b32 m0, 0x5000\n"
    "v_exp_f32 v76, v76\n"
    "v_mfma_f32_16x16x32_bf16 v[36:39], v[168:171], a[224:227], v[100:103]\n"
    "ds_read_b128 v[176:179], v200 offset:40960\n"
    "v_add_f32 v113, v113, v64\n"
    "v_mfma_f32_16x16x32_bf16 v[36:39], v[164:167], a[220:223], v[36:39]\n"
    "buffer_load_dwordx4 v200, s[36:39], s65 offen sc1 lds\n"
    "v_mfma_f32_16x16x32_bf16 v[36:39], v[160:163], a[216:219], v[36:39]\n"
    "v_exp_f32 v77, v77\n"
    "v_mfma_f32_16x16x32_bf16 v[40:43], v[160:163], a[240:243], v[104:107]\n"
    "ds_read_b128 v[180:183], v200 offset:41984\n"
    "v_cvt_pk_bf16_f32 v156, v76, v77\n"
    "v_mfma_f32_16x16x32_bf16 v[40:43], v[164:167], a[244:247], v[40:43]\n"
    "v_exp_f32 v78, v78\n"
    "v_mfma_f32_16x16x32_bf16 v[40:43], v[168:171], a[248:251], v[40:43]\n"
    "buffer_load_dwordx4 v200, s[36:39], s65 offen offset:1024 sc1 lds\n"
    "v_mfma_f32_16x16x32_bf16 v[44:47], v[168:171], v[16:19], v[108:111]\n"
    "v_exp_f32 v79, v79\n"
    "v_mfma_f32_16x16x32_bf16 v[44:47], v[164:167], v[12:15], v[44:47]\n"
    "v_exp_f32 v80, v80\n"
    "v_mfma_f32_16x16x32_bf16 v[44:47], v[160:163], v[8:11], v[44:47]\n"
    "s_waitcnt lgkmcnt(1)\n"
    "v_exp_f32 v81, v81\n"
    "v_mfma_f32_16x16x32_bf16 v[44:47], v[172:175], v[20:23], v[44:47]\n"
    "ds_read_b128 v[160:163], v200 offset:46080\n"
    "v_cvt_pk_bf16_f32 v157, v78, v79\n"
    "v_mfma_f32_16x16x32_bf16 v[44:47], v[176:179], v[24:27], v[44:47]\n"
    "s_waitcnt lgkmcnt(1)\n"
    "buffer_load_dwordx4 v200, s[36:39], s65 offen offset:2048 sc1 lds\n"
    "v_mfma_f32_16x16x32_bf16 v[44:47], v[180:183], v[28:31], v[44:47]\n"
    "v_exp_f32 v82, v82\n"
    "v_mfma_f32_16x16x32_bf16 v[40:43], v[180:183], v[4:7], v[40:43]\n"
    "ds_read_b128 v[164:167], v200 offset:47104\n"
    "v_cvt_pk_bf16_f32 v146, v80, v81\n"
    "v_mfma_f32_16x16x32_bf16 v[40:43], v[176:179], v[0:3], v[40:43]\n"
    "v_exp_f32 v83, v83\n"
    "v_mfma_f32_16x16x32_bf16 v[40:43], v[172:175], a[252:255], v[40:43]\n"
    "buffer_load_dwordx4 v200, s[36:39], s65 offen offset:3072 sc1 lds\n"
    "v_mfma_f32_16x16x32_bf16 v[36:39], v[172:175], a[228:231], v[36:39]\n"
    "s_add_u32 s65, s61, 0xfffff800\n"
    "ds_read_b128 v[168:171], v200 offset:48128\n"
    "v_cvt_pk_bf16_f32 v147, v82, v83\n"
    "v_mfma_f32_16x16x32_bf16 v[36:39], v[176:179], a[232:235], v[36:39]\n"
    "s_mov_b32 m0, 0x16000\n"
    "v_exp_f32 v84, v84\n"
    "v_mfma_f32_16x16x32_bf16 v[36:39], v[180:183], a[236:239], v[36:39]\n"
    "v_exp_f32 v85, v85\n"
    "v_mfma_f32_16x16x32_bf16 v[32:35], v[180:183], a[212:215], v[32:35]\n"
    "buffer_load_dwordx4 v200, s[40:43], s65 offen sc1 lds\n"
    "v_mfma_f32_16x16x32_bf16 v[32:35], v[176:179], a[208:211], v[32:35]\n"
    "v_exp_f32 v86, v86\n"
    "v_mfma_f32_16x16x32_bf16 v[32:35], v[172:175], a[204:207], v[32:35]\n"
    "s_waitcnt lgkmcnt(1)\n"
    "v_exp_f32 v87, v87\n"
    "v_mfma_f32_16x16x32_bf16 v[48:51], v[160:163], a[204:207], v[96:99]\n"
    "ds_read_b128 v[172:175], v200 offset:43008\n"
    "v_cvt_pk_bf16_f32 v150, v84, v85\n"
    "v_mfma_f32_16x16x32_bf16 v[48:51], v[164:167], a[208:211], v[48:51]\n"
    "s_waitcnt lgkmcnt(1)\n"
    "buffer_load_dwordx4 v200, s[40:43], s65 offen offset:1024 sc1 lds\n"
    "v_mfma_f32_16x16x32_bf16 v[48:51], v[168:171], a[212:215], v[48:51]\n"
    "v_exp_f32 v88, v88\n"
    "v_mfma_f32_16x16x32_bf16 v[52:55], v[168:171], a[236:239], v[100:103]\n"
    "ds_read_b128 v[176:179], v200 offset:44032\n"
    "v_cvt_pk_bf16_f32 v151, v86, v87\n"
    "v_mfma_f32_16x16x32_bf16 v[52:55], v[164:167], a[232:235], v[52:55]\n"
    "v_exp_f32 v89, v89\n"
    "v_mfma_f32_16x16x32_bf16 v[52:55], v[160:163], a[228:231], v[52:55]\n"
    "buffer_load_dwordx4 v200, s[40:43], s65 offen offset:2048 sc1 lds\n"
    "v_mfma_f32_16x16x32_bf16 v[56:59], v[160:163], a[252:255], v[104:107]\n"
    "ds_read_b128 v[180:183], v200 offset:45056\n"
    "v_cvt_pk_bf16_f32 v154, v88, v89\n"
    "v_mfma_f32_16x16x32_bf16 v[56:59], v[164:167], v[0:3], v[56:59]\n"
    "v_exp_f32 v90, v90\n"
    "v_mfma_f32_16x16x32_bf16 v[56:59], v[168:171], v[4:7], v[56:59]\n"
    "v_exp_f32 v91, v91\n"
    "v_mfma_f32_16x16x32_bf16 v[60:63], v[168:171], v[28:31], v[108:111]\n"
    "buffer_load_dwordx4 v200, s[40:43], s65 offen offset:3072 sc1 lds\n"
    "v_mfma_f32_16x16x32_bf16 v[60:63], v[164:167], v[24:27], v[60:63]\n"
    "s_add_u32 s65, s61, 0x800\n"
    "v_exp_f32 v92, v92\n"
    "v_mfma_f32_16x16x32_bf16 v[60:63], v[160:163], v[20:23], v[60:63]\n"
    "s_waitcnt lgkmcnt(1)\n"
    "s_mov_b32 m0, 0x17000\n"
    "v_cvt_pk_bf16_f32 v155, v90, v91\n"
    "v_mfma_f32_16x16x32_bf16 v[60:63], v[172:175], v[8:11], v[60:63]\n"
    "ds_read_b128 v[160:163], v201\n"
    "v_add_f32 v114, v114, v65\n"
    "v_mfma_f32_16x16x32_bf16 v[60:63], v[176:179], v[12:15], v[60:63]\n"
    "s_waitcnt lgkmcnt(1)\n"
    "buffer_load_dwordx4 v200, s[40:43], s65 offen sc1 lds\n"
    "v_mfma_f32_16x16x32_bf16 v[60:63], v[180:183], v[16:19], v[60:63]\n"
    "v_exp_f32 v93, v93\n"
    "v_mfma_f32_16x16x32_bf16 v[56:59], v[180:183], a[248:251], v[56:59]\n"
    "ds_read_b128 v[164:167], v201 offset:1024\n"
    "v_cvt_pk_bf16_f32 v158, v92, v93\n"
    "v_mfma_f32_16x16x32_bf16 v[56:59], v[176:179], a[244:247], v[56:59]\n"
    "v_exp_f32 v94, v94\n"
    "v_mfma_f32_16x16x32_bf16 v[56:59], v[172:175], a[240:243], v[56:59]\n"
    "buffer_load_dwordx4 v200, s[40:43], s65 offen offset:1024 sc1 lds\n"
    "v_mfma_f32_16x16x32_bf16 v[52:55], v[172:175], a[216:219], v[52:55]\n"
    "ds_read_b128 v[168:171], v201 offset:2048\n"
    "v_add_f32 v113, v113, v66\n"
    "v_mfma_f32_16x16x32_bf16 v[52:55], v[176:179], a[220:223], v[52:55]\n"
    "v_exp_f32 v95, v95\n"
    "v_mfma_f32_16x16x32_bf16 v[52:55], v[180:183], a[224:227], v[52:55]\n"
    "v_cvt_pk_bf16_f32 v159, v94, v95\n"
    "v_add_f32 v114, v114, v67\n"
    "v_mfma_f32_16x16x32_bf16 v[48:51], v[180:183], a[200:203], v[48:51]\n"
    "buffer_load_dwordx4 v200, s[40:43], s65 offen offset:2048 sc1 lds\n"
    "v_mfma_f32_16x16x32_bf16 v[48:51], v[176:179], a[196:199], v[48:51]\n"
    "v_exp_f32 v32, v32\n"
    "v_mfma_f32_16x16x32_bf16 v[48:51], v[172:175], a[192:195], v[48:51]\n"
    "s_waitcnt lgkmcnt(1)\n"
    "v_exp_f32 v33, v33\n"
    "v_mfma_f32_16x16x32_bf16 a[0:3], v[160:163], v[128:131], a[0:3]\n"
    "ds_read_b128 v[172:175], v201 offset:3072\n"
    "ds_read_b128 v[176:179], v201 offset:4096\n"
    "v_mfma_f32_16x16x32_bf16 a[0:3], v[164:167], v[144:147], a[0:3]\n"
    "buffer_load_dwordx4 v200, s[40:43], s65 offen offset:3072 sc1 lds\n"
    "v_mfma_f32_16x16x32_bf16 a[4:7], v[160:163], v[132:135], a[4:7]\n"
    "v_exp_f32 v34, v34\n"
    "v_mfma_f32_16x16x32_bf16 a[4:7], v[164:167], v[148:151], a[4:7]\n"
    "v_exp_f32 v35, v35\n"
    "v_mfma_f32_16x16x32_bf16 a[8:11], v[160:163], v[136:139], a[8:11]\n"
    "ds_read_b128 v[180:183], v201 offset:5120\n"
    "v_add_f32 v117, v117, v68\n"
    "v_mfma_f32_16x16x32_bf16 a[8:11], v[164:167], v[152:155], a[8:11]\n"
    "v_exp_f32 v36, v36\n"
    "v_mfma_f32_16x16x32_bf16 a[12:15], v[160:163], v[140:143], a[12:15]\n"
    "v_exp_f32 v37, v37\n"
    "v_mfma_f32_16x16x32_bf16 a[12:15], v[164:167], v[156:159], a[12:15]\n"
    "s_waitcnt lgkmcnt(2)\n"
    "v_exp_f32 v38, v38\n"
    "v_mfma_f32_16x16x32_bf16 a[28:31], v[168:171], v[140:143], a[28:31]\n"
    "ds_read_b128 v[160:163], v201 offset:6144\n"
    "v_add_f32 v118, v118, v69\n"
    "v_mfma_f32_16x16x32_bf16 a[28:31], v[172:175], v[156:159], a[28:31]\n"
    "v_exp_f32 v39, v39\n"
    "v_mfma_f32_16x16x32_bf16 a[24:27], v[172:175], v[152:155], a[24:27]\n"
    "v_exp_f32 v40, v40\n"
    "v_mfma_f32_16x16x32_bf16 a[24:27], v[168:171], v[136:139], a[24:27]\n"
    "v_exp_f32 v41, v41\n"
    "v_mfma_f32_16x16x32_bf16 a[20:23], v[168:171], v[132:135], a[20:23]\n"
    "ds_read_b128 v[164:167], v201 offset:7168\n"
    "v_add_f32 v117, v117, v70\n"
    "v_mfma_f32_16x16x32_bf16 a[20:23], v[172:175], v[148:151], a[20:23]\n"
    "v_exp_f32 v42, v42\n"
    "v_mfma_f32_16x16x32_bf16 a[16:19], v[172:175], v[144:147], a[16:19]\n"
    "v_exp_f32 v43, v43\n"
    "v_mfma_f32_16x16x32_bf16 a[16:19], v[168:171], v[128:131], a[16:19]\n"
    "s_waitcnt lgkmcnt(2)\n"
    "v_exp_f32 v44, v44\n"
    "v_mfma_f32_16x16x32_bf16 a[32:35], v[176:179], v[128:131], a[32:35]\n"
    "ds_read_b128 v[168:171], v201 offset:8192\n"
    "v_add_f32 v113, v113, v32\n"
    "v_mfma_f32_16x16x32_bf16 a[32:35], v[180:183], v[144:147], a[32:35]\n"
    "v_exp_f32 v45, v45\n"
    "v_mfma_f32_16x16x32_bf16 a[36:39], v[180:183], v[148:151], a[36:39]\n"
    "v_exp_f32 v46, v46\n"
    "v_mfma_f32_16x16x32_bf16 a[36:39], v[176:179], v[132:135], a[36:39]\n"
    "v_exp_f32 v47, v47\n"
    "v_mfma_f32_16x16x32_bf16 a[40:43], v[176:179], v[136:139], a[40:43]\n"
    "ds_read_b128 v[172:175], v201 offset:9216\n"
    "v_add_f32 v118, v118, v71\n"
    "v_mfma_f32_16x16x32_bf16 a[40:43], v[180:183], v[152:155], a[40:43]\n"
    "v_exp_f32 v48, v48\n"
    "v_mfma_f32_16x16x32_bf16 a[44:47], v[180:183], v[156:159], a[44:47]\n"
    "v_exp_f32 v49, v49\n"
    "v_mfma_f32_16x16x32_bf16 a[44:47], v[176:179], v[140:143], a[44:47]\n"
    "s_waitcnt lgkmcnt(2)\n"
    "v_exp_f32 v50, v50\n"
    "v_mfma_f32_16x16x32_bf16 a[60:63], v[160:163], v[140:143], a[60:63]\n"
    "ds_read_b128 v[176:179], v201 offset:10240\n"
    "v_add_f32 v114, v114, v33\n"
    "v_mfma_f32_16x16x32_bf16 a[60:63], v[164:167], v[156:159], a[60:63]\n"
    "v_exp_f32 v51, v51\n"
    "v_mfma_f32_16x16x32_bf16 a[56:59], v[164:167], v[152:155], a[56:59]\n"
    "v_exp_f32 v52, v52\n"
    "v_mfma_f32_16x16x32_bf16 a[56:59], v[160:163], v[136:139], a[56:59]\n"
    "v_add_f32 v121, v121, v72\n"
    "v_add_f32 v113, v113, v34\n"
    "v_mfma_f32_16x16x32_bf16 a[52:55], v[160:163], v[132:135], a[52:55]\n"
    "ds_read_b128 v[180:183], v201 offset:11264\n"
    "v_add_f32 v122, v122, v73\n"
    "v_mfma_f32_16x16x32_bf16 a[52:55], v[164:167], v[148:151], a[52:55]\n"
    "v_exp_f32 v53, v53\n"
    "v_mfma_f32_16x16x32_bf16 a[48:51], v[164:167], v[144:147], a[48:51]\n"
    "v_add_f32 v114, v114, v35\n"
    "v_add_f32 v121, v121, v74\n"
    "v_mfma_f32_16x16x32_bf16 a[48:51], v[160:163], v[128:131], a[48:51]\n"
    "s_waitcnt lgkmcnt(2)\n"
    "v_add_f32 v117, v117, v36\n"
    "v_add_f32 v122, v122, v75\n"
    "v_mfma_f32_16x16x32_bf16 a[64:67], v[168:171], v[128:131], a[64:67]\n"
    "ds_read_b128 v[160:163], v201 offset:12288\n"
    "v_add_f32 v118, v118, v37\n"
    "v_mfma_f32_16x16x32_bf16 a[64:67], v[172:175], v[144:147], a[64:67]\n"
    "v_exp_f32 v54, v54\n"
    "v_mfma_f32_16x16x32_bf16 a[68:71], v[172:175], v[148:151], a[68:71]\n"
    "v_add_f32 v125, v125, v76\n"
    "v_add_f32 v117, v117, v38\n"
    "v_mfma_f32_16x16x32_bf16 a[68:71], v[168:171], v[132:135], a[68:71]\n"
    "s_add_u32 m0, s51, s95\n"
    "v_exp_f32 v55, v55\n"
    "v_mfma_f32_16x16x32_bf16 a[72:75], v[168:171], v[136:139], a[72:75]\n"
    "ds_read_b128 v[164:167], v201 offset:13312\n"
    "v_add_f32 v126, v126, v77\n"
    "v_mfma_f32_16x16x32_bf16 a[72:75], v[172:175], v[152:155], a[72:75]\n"
    "buffer_load_dwordx4 v202, s[32:35], s64 offen lds\n"
    "v_mfma_f32_16x16x32_bf16 a[76:79], v[172:175], v[156:159], a[76:79]\n"
    "v_add_f32 v118, v118, v39\n"
    "v_add_f32 v125, v125, v78\n"
    "v_mfma_f32_16x16x32_bf16 a[76:79], v[168:171], v[140:143], a[76:79]\n"
    "s_waitcnt lgkmcnt(2)\n"
    "v_add_f32 v121, v121, v40\n"
    "v_add_f32 v126, v126, v79\n"
    "v_mfma_f32_16x16x32_bf16 a[92:95], v[176:179], v[140:143], a[92:95]\n"
    "ds_read_b128 v[168:171], v201 offset:14336\n"
    "v_add_f32 v122, v122, v41\n"
    "v_mfma_f32_16x16x32_bf16 a[92:95], v[180:183], v[156:159], a[92:95]\n"
    "buffer_load_dwordx4 v203, s[32:35], s64 offen offset:1024 lds\n"
    "v_mfma_f32_16x16x32_bf16 a[88:91], v[180:183], v[152:155], a[88:91]\n"
    "v_exp_f32 v56, v56\n"
    "v_mfma_f32_16x16x32_bf16 a[88:91], v[176:179], v[136:139], a[88:91]\n"
    "v_add_f32 v113, v113, v80\n"
    "v_add_f32 v121, v121, v42\n"
    "v_mfma_f32_16x16x32_bf16 a[84:87], v[176:179], v[132:135], a[84:87]\n"
    "ds_read_b128 v[172:175], v201 offset:15360\n"
    "v_add_f32 v114, v114, v81\n"
    "v_mfma_f32_16x16x32_bf16 a[84:87], v[180:183], v[148:151], a[84:87]\n"
    "buffer_load_dwordx4 v204, s[32:35], s64 offen offset:2048 lds\n"
    "v_mfma_f32_16x16x32_bf16 a[80:83], v[180:183], v[144:147], a[80:83]\n"
    "v_exp_f32 v57, v57\n"
    "v_mfma_f32_16x16x32_bf16 a[80:83], v[176:179], v[128:131], a[80:83]\n"
    "s_waitcnt lgkmcnt(2)\n"
    "v_add_f32 v122, v122, v43\n"
    "v_add_f32 v113, v113, v82\n"
    "v_mfma_f32_16x16x32_bf16 a[96:99], v[160:163], v[128:131], a[96:99]\n"
    "s_mov_b32 s95, s94\n"
    "ds_read_b128 v[176:179], v201 offset:16384\n"
    "v_add_f32 v125, v125, v44\n"
    "v_mfma_f32_16x16x32_bf16 a[96:99], v[164:167], v[144:147], a[96:99]\n"
    "s_sub_u32 s94, s94, 0x3000\n"
    "v_exp_f32 v58, v58\n"
    "v_mfma_f32_16x16x32_bf16 a[100:103], v[164:167], v[148:151], a[100:103]\n"
    "s_min_u32 s94, s94, 0x6000\n"
    "v_add_f32 v114, v114, v83\n"
    "v_add_f32 v126, v126, v45\n"
    "v_mfma_f32_16x16x32_bf16 a[100:103], v[160:163], v[132:135], a[100:103]\n"
    "s_sub_u32 s93, s94, s95\n"
    "v_add_f32 v117, v117, v84\n"
    "v_add_f32 v125, v125, v46\n"
    "v_mfma_f32_16x16x32_bf16 a[104:107], v[160:163], v[136:139], a[104:107]\n"
    "ds_read_b128 v[180:183], v201 offset:17408\n"
    "v_add_f32 v118, v118, v85\n"
    "v_mfma_f32_16x16x32_bf16 a[104:107], v[164:167], v[152:155], a[104:107]\n"
    "v_exp_f32 v59, v59\n"
    "v_mfma_f32_16x16x32_bf16 a[108:111], v[164:167], v[156:159], a[108:111]\n"
    "v_add_f32 v126, v126, v47\n"
    "v_add_f32 v117, v117, v86\n"
    "v_mfma_f32_16x16x32_bf16 a[108:111], v[160:163], v[140:143], a[108:111]\n"
    "s_waitcnt lgkmcnt(2)\n"
    "v_add_f32 v113, v113, v48\n"
    "v_add_f32 v118, v118, v87\n"
    "v_mfma_f32_16x16x32_bf16 a[124:127], v[168:171], v[140:143], a[124:127]\n"
    "ds_read_b128 v[160:163], v201 offset:18432\n"
    "v_add_f32 v114, v114, v49\n"
    "v_mfma_f32_16x16x32_bf16 a[124:127], v[172:175], v[156:159], a[124:127]\n"
    "v_exp_f32 v60, v60\n"
    "v_mfma_f32_16x16x32_bf16 a[120:123], v[172:175], v[152:155], a[120:123]\n"
    "v_add_f32 v121, v121, v88\n"
    "v_add_f32 v113, v113, v50\n"
    "v_mfma_f32_16x16x32_bf16 a[120:123], v[168:171], v[136:139], a[120:123]\n"
    "v_exp_f32 v61, v61\n"
    "v_mfma_f32_16x16x32_bf16 a[116:119], v[168:171], v[132:135], a[116:119]\n"
    "ds_read_b128 v[164:167], v201 offset:19456\n"
    "v_add_u32 v210, 0x80, v210\n"
    "v_mfma_f32_16x16x32_bf16 a[116:119], v[172:175], v[148:151], a[116:119]\n"
    "v_add_u32 v207, s93, v207\n"
    "v_add_f32 v122, v122, v89\n"
    "v_mfma_f32_16x16x32_bf16 a[112:115], v[172:175], v[144:147], a[112:115]\n"
    "v_add_f32 v114, v114, v51\n"
    "v_add_f32 v121, v121, v90\n"
    "v_mfma_f32_16x16x32_bf16 a[112:115], v[168:171], v[128:131], a[112:115]\n"
    "s_waitcnt lgkmcnt(2)\n"
    "v_add_f32 v117, v117, v52\n"
    "v_add_f32 v122, v122, v91\n"
    "v_mfma_f32_16x16x32_bf16 a[128:131], v[176:179], v[128:131], a[128:131]\n"
    "ds_read_b128 v[168:171], v201 offset:20480\n"
    "v_add_f32 v118, v118, v53\n"
    "v_mfma_f32_16x16x32_bf16 a[128:131], v[180:183], v[144:147], a[128:131]\n"
    "v_exp_f32 v62, v62\n"
    "v_mfma_f32_16x16x32_bf16 a[132:135], v[180:183], v[148:151], a[132:135]\n"
    "v_add_f32 v125, v125, v92\n"
    "v_add_f32 v117, v117, v54\n"
    "v_mfma_f32_16x16x32_bf16 a[132:135], v[176:179], v[132:135], a[132:135]\n"
    "s_add_u32 s64, s64, 0xc000\n"
    "v_exp_f32 v63, v63\n"
    "v_mfma_f32_16x16x32_bf16 a[136:139], v[176:179], v[136:139], a[136:139]\n"
    "s_add_u32 s62, s62, 0x6000\n"
    "ds_read_b128 v[172:175], v201 offset:21504\n"
    "v_add_f32 v126, v126, v93\n"
    "v_mfma_f32_16x16x32_bf16 a[136:139], v[180:183], v[152:155], a[136:139]\n"
    "s_add_u32 s63, s63, 0x6000\n"
    "v_add_f32 v118, v118, v55\n"
    "v_add_f32 v125, v125, v94\n"
    "v_mfma_f32_16x16x32_bf16 a[140:143], v[180:183], v[156:159], a[140:143]\n"
    "s_add_u32 s60, s60, 0x6000\n"
    "v_add_f32 v121, v121, v56\n"
    "v_add_f32 v126, v126, v95\n"
    "v_mfma_f32_16x16x32_bf16 a[140:143], v[176:179], v[140:143], a[140:143]\n"
    "s_waitcnt lgkmcnt(2)\n"
    "v_add_f32 v122, v122, v57\n"
    "v_mfma_f32_16x16x32_bf16 a[156:159], v[160:163], v[140:143], a[156:159]\n"
    "s_add_u32 s61, s61, 0x6000\n"
    "ds_read_b128 v[176:179], v201 offset:22528\n"
    "v_mfma_f32_16x16x32_bf16 a[156:159], v[164:167], v[156:159], a[156:159]\n"
    "ds_read_b128 v[180:183], v201 offset:23552\n"
    "v_mfma_f32_16x16x32_bf16 a[152:155], v[164:167], v[152:155], a[152:155]\n"
    "s_waitcnt vmcnt(3)\n"
    "v_mfma_f32_16x16x32_bf16 a[152:155], v[160:163], v[136:139], a[152:155]\n"
    "v_mfma_f32_16x16x32_bf16 a[148:151], v[160:163], v[132:135], a[148:151]\n"
    "v_mfma_f32_16x16x32_bf16 a[148:151], v[164:167], v[148:151], a[148:151]\n"
    "v_mfma_f32_16x16x32_bf16 a[144:147], v[164:167], v[144:147], a[144:147]\n"
    "v_mfma_f32_16x16x32_bf16 a[144:147], v[160:163], v[128:131], a[144:147]\n"
    "s_waitcnt lgkmcnt(0)\n"
    "v_mfma_f32_16x16x32_bf16 a[160:163], v[168:171], v[128:131], a[160:163]\n"
    "s_barrier\n"
    "v_mfma_f32_16x16x32_bf16 a[160:163], v[172:175], v[144:147], a[160:163]\n"
    "v_add_f32 v121, v121, v58\n"
    "v_add_f32 v122, v122, v59\n"
    "v_mfma_f32_16x16x32_bf16 a[164:167], v[172:175], v[148:151], a[164:167]\n"
    "ds_read_b128 v[160:163], v200\n"
    "v_add_f32 v125, v125, v60\n"
    "v_mfma_f32_16x16x32_bf16 a[164:167], v[168:171], v[132:135], a[164:167]\n"
    "v_add_f32 v126, v126, v61\n"
    "v_add_f32 v125, v125, v62\n"
    "v_mfma_f32_16x16x32_bf16 a[168:171], v[168:171], v[136:139], a[168:171]\n"
    "ds_read_b128 v[164:167], v200 offset:1024\n"
    "v_add_f32 v126, v126, v63\n"
    "v_mfma_f32_16x16x32_bf16 a[168:171], v[172:175], v[152:155], a[168:171]\n"
    "ds_read_b64 v[228:229], v210\n"
    "ds_read2st64_b32 v[240:241], v207 offset0:96 offset1:99\n"
    "v_mfma_f32_16x16x32_bf16 a[172:175], v[172:175], v[156:159], a[172:175]\n"
    "ds_read2st64_b32 v[242:243], v207 offset0:102 offset1:105\n"
    "ds_read2st64_b32 v[244:245], v207 offset0:108 offset1:111\n"
    "v_mfma_f32_16x16x32_bf16 a[172:175], v[168:171], v[140:143], a[172:175]\n"
    "ds_read2st64_b32 v[246:247], v207 offset0:114 offset1:117\n"
    "v_mfma_f32_16x16x32_bf16 a[188:191], v[176:179], v[140:143], a[188:191]\n"
    "ds_read_b128 v[168:171], v200 offset:2048\n"
    "v_cvt_pk_bf16_f32 v140, v44, v45\n"
    "v_mfma_f32_16x16x32_bf16 a[188:191], v[180:183], v[156:159], a[188:191]\n"
    "v_cvt_pk_bf16_f32 v141, v46, v47\n"
    "v_mfma_f32_16x16x32_bf16 a[184:187], v[176:179], v[136:139], a[184:187]\n"
    "v_cvt_pk_bf16_f32 v136, v40, v41\n"
    "v_cvt_pk_bf16_f32 v137, v42, v43\n"
    "v_mfma_f32_16x16x32_bf16 a[184:187], v[180:183], v[152:155], a[184:187]\n"
    "v_mfma_f32_16x16x32_bf16 a[180:183], v[176:179], v[132:135], a[180:183]\n"
    "ds_read_b128 v[172:175], v200 offset:3072\n"
    "v_cvt_pk_bf16_f32 v132, v36, v37\n"
    "v_mfma_f32_16x16x32_bf16 a[180:183], v[180:183], v[148:151], a[180:183]\n"
    "v_cvt_pk_bf16_f32 v133, v38, v39\n"
    "v_mfma_f32_16x16x32_bf16 a[176:179], v[176:179], v[128:131], a[176:179]\n"
    "s_waitcnt lgkmcnt(5)\n"
    "v_cvt_f32_i32 v240, v240\n"
    "v_cvt_f32_i32 v241, v241\n"
    "v_mfma_f32_16x16x32_bf16 a[176:179], v[180:183], v[144:147], a[176:179]\n"
    "s_waitcnt lgkmcnt(4)\n"
    "v_cvt_f32_i32 v242, v242\n"
    "v_cvt_f32_i32 v243, v243\n"
    "LM02:\n"
    "v_mfma_f32_16x16x32_bf16 v[64:67], v[160:163], a[192:195], v[96:99]\n"
    "ds_read_b128 v[176:179], v200 offset:4096\n"
    "s_waitcnt lgkmcnt(4)\n"
    "v_cvt_f32_i32 v244, v244\n"
    "v_mfma_f32_16x16x32_bf16 v[64:67], v[164:167], a[196:199], v[64:67]\n"
    "s_waitcnt lgkmcnt(1)\n"
    "v_cvt_f32_i32 v245, v245\n"
    "v_cvt_f32_i32 v246, v246\n"
    "v_mfma_f32_16x16x32_bf16 v[64:67], v[168:171], a[200:203], v[64:67]\n"
    "v_cvt_f32_i32 v247, v247\n"
    "v_mul_f32_dpp v240, v228, v240 row_newbcast:0 row_mask:0xf bank_mask:0xf\n"
    "v_mfma_f32_16x16x32_bf16 v[68:71], v[168:171], a[224:227], v[100:103]\n"
    "v_mul_f32_dpp v241, v228, v241 row_newbcast:1 row_mask:0xf bank_mask:0xf\n"
    "v_mul_f32_dpp v242, v228, v242 row_newbcast:2 row_mask:0xf bank_mask:0xf\n"
    "v_mfma_f32_16x16x32_bf16 v[68:71], v[164:167], a[220:223], v[68:71]\n"
    "ds_read_b128 v[180:183], v200 offset:5120\n"
    "v_mul_f32_dpp v243, v228, v243 row_newbcast:3 row_mask:0xf bank_mask:0xf\n"
    "v_mfma_f32_16x16x32_bf16 v[68:71], v[160:163], a[216:219], v[68:71]\n"
    "v_mul_f32_dpp v244, v228, v244 row_newbcast:4 row_mask:0xf bank_mask:0xf\n"
    "v_mul_f32_dpp v245, v228, v245 row_newbcast:5 row_mask:0xf bank_mask:0xf\n"
    "v_mfma_f32_16x16x32_bf16 v[72:75], v[160:163], a[240:243], v[104:107]\n"
    "v_mul_f32_dpp v246, v228, v246 row_newbcast:6 row_mask:0xf bank_mask:0xf\n"
    "v_mul_f32_dpp v247, v228, v247 row_newbcast:7 row_mask:0xf bank_mask:0xf\n"
    "v_mfma_f32_16x16x32_bf16 v[72:75], v[164:167], a[244:247], v[72:75]\n"
    "v_add_f32_dpp v240, v229, v240 row_newbcast:0 row_mask:0xf bank_mask:0xf\n"
    "v_add_f32_dpp v241, v229, v241 row_newbcast:1 row_mask:0xf bank_mask:0xf\n"
    "v_mfma_f32_16x16x32_bf16 v[72:75], v[168:171], a[248:251], v[72:75]\n"
    "v_add_f32_dpp v242, v229, v242 row_newbcast:2 row_mask:0xf bank_mask:0xf\n"
    "v_add_f32_dpp v243, v229, v243 row_newbcast:3 row_mask:0xf bank_mask:0xf\n"
    "v_mfma_f32_16x16x32_bf16 v[76:79], v[168:171], v[16:19], v[108:111]\n"
    "v_add_f32_dpp v244, v229, v244 row_newbcast:4 row_mask:0xf bank_mask:0xf\n"
    "v_add_f32_dpp v245, v229, v245 row_newbcast:5 row_mask:0xf bank_mask:0xf\n"
    "v_mfma_f32_16x16x32_bf16 v[76:79], v[164:167], v[12:15], v[76:79]\n"
    "v_add_f32_dpp v246, v229, v246 row_newbcast:6 row_mask:0xf bank_mask:0xf\n"
    "v_add_f32_dpp v247, v229, v247 row_newbcast:7 row_mask:0xf bank_mask:0xf\n"
    "v_mfma_f32_16x16x32_bf16 v[76:79], v[160:163], v[8:11], v[76:79]\n"
    "s_waitcnt lgkmcnt(1)\n"
    "v_cvt_pk_bf16_f32 v240, v240, v241\n"
    "v_cvt_pk_bf16_f32 v241, v242, v243\n"
    "v_mfma_f32_16x16x32_bf16 v[76:79], v[172:175], v[20:23], v[76:79]\n"
    "ds_read_b128 v[160:163], v200 offset:9216\n"
    "v_cvt_pk_bf16_f32 v242, v244, v245\n"
    "v_mfma_f32_16x16x32_bf16 v[76:79], v[176:179], v[24:27], v[76:79]\n"
    "s_waitcnt lgkmcnt(1)\n"
    "v_cvt_pk_bf16_f32 v243, v246, v247\n"
    "v_cvt_pk_bf16_f32 v128, v32, v33\n"
    "v_mfma_f32_16x16x32_bf16 v[76:79], v[180:183], v[28:31], v[76:79]\n"
    "buffer_store_dwordx4 v[240:243], v212, s[40:43], s63 offen sc1\n"
    "v_cvt_pk_bf16_f32 v129, v34, v35\n"
    "v_cvt_pk_bf16_f32 v142, v60, v61\n"
    "v_cvt_pk_bf16_f32 v143, v62, v63\n"
    "v_mfma_f32_16x16x32_bf16 v[72:75], v[180:183], v[4:7], v[72:75]\n"
    "ds_read_b128 v[164:167], v200 offset:10240\n"
    "v_cvt_pk_bf16_f32 v138, v56, v57\n"
    "v_mfma_f32_16x16x32_bf16 v[72:75], v[176:179], v[0:3], v[72:75]\n"
    "v_cvt_pk_bf16_f32 v139, v58, v59\n"
    "v_cvt_pk_bf16_f32 v134, v52, v53\n"
    "v_mfma_f32_16x16x32_bf16 v[72:75], v[172:175], a[252:255], v[72:75]\n"
    "v_cvt_pk_bf16_f32 v135, v54, v55\n"
    "v_cvt_pk_bf16_f32 v130, v48, v49\n"
    "v_mfma_f32_16x16x32_bf16 v[68:71], v[172:175], a[228:231], v[68:71]\n"
    "ds_read_b128 v[168:171], v200 offset:11264\n"
    "v_cvt_pk_bf16_f32 v131, v50, v51\n"
    "v_mfma_f32_16x16x32_bf16 v[68:71], v[176:179], a[232:235], v[68:71]\n"
    "v_mfma_f32_16x16x32_bf16 v[68:71], v[180:183], a[236:239], v[68:71]\n"
    "v_mfma_f32_16x16x32_bf16 v[64:67], v[180:183], a[212:215], v[64:67]\n"
    "v_mfma_f32_16x16x32_bf16 v[64:67], v[176:179], a[208:211], v[64:67]\n"
    "v_mfma_f32_16x16x32_bf16 v[64:67], v[172:175], a[204:207], v[64:67]\n"
    "s_waitcnt lgkmcnt(1)\n"
    "v_mfma_f32_16x16x32_bf16 v[80:83], v[160:163], a[204:207], v[96:99]\n"
    "ds_read_b128 v[172:175], v200 offset:6144\n"
    "v_mfma_f32_16x16x32_bf16 v[80:83], v[164:167], a[208:211], v[80:83]\n"
    "s_waitcnt lgkmcnt(1)\n"
    "v_mfma_f32_16x16x32_bf16 v[80:83], v[168:171], a[212:215], v[80:83]\n"
    "v_mfma_f32_16x16x32_bf16 v[84:87], v[168:171], a[236:239], v[100:103]\n"
    "ds_read_b128 v[176:179], v200 offset:7168\n"
    "v_mfma_f32_16x16x32_bf16 v[84:87], v[164:167], a[232:235], v[84:87]\n"
    "v_exp_f32 v64, v64\n"
    "v_mfma_f32_16x16x32_bf16 v[84:87], v[160:163], a[228:231], v[84:87]\n"
    "v_exp_f32 v65, v65\n"
    "v_mfma_f32_16x16x32_bf16 v[88:91], v[160:163], a[252:255], v[104:107]\n"
    "ds_read_b128 v[180:183], v200 offset:8192\n"
    "v_cvt_pk_bf16_f32 v144, v64, v65\n"
    "v_mfma_f32_16x16x32_bf16 v[88:91], v[164:167], v[0:3], v[88:91]\n"
    "s_add_u32 s65, s60, 0xfffff800\n"
    "v_exp_f32 v66, v66\n"
    "v_mfma_f32_16x16x32_bf16 v[88:91], v[168:171], v[4:7], v[88:91]\n"
    "s_mov_b32 m0, 0xa000\n"
    "v_exp_f32 v67, v67\n"
    "v_mfma_f32_16x16x32_bf16 v[92:95], v[168:171], v[28:31], v[108:111]\n"
    "v_exp_f32 v68, v68\n"
    "v_mfma_f32_16x16x32_bf16 v[92:95], v[164:167], v[24:27], v[92:95]\n"
    "buffer_load_dwordx4 v200, s[36:39], s65 offen sc1 lds\n"
    "v_mfma_f32_16x16x32_bf16 v[92:95], v[160:163], v[20:23], v[92:95]\n"
    "s_waitcnt lgkmcnt(1)\n"
    "v_exp_f32 v69, v69\n"
    "v_mfma_f32_16x16x32_bf16 v[92:95], v[172:175], v[8:11], v[92:95]\n"
    "ds_read_b128 v[160:163], v200 offset:12288\n"
    "v_cvt_pk_bf16_f32 v145, v66, v67\n"
    "v_mfma_f32_16x16x32_bf16 v[92:95], v[176:179], v[12:15], v[92:95]\n"
    "s_waitcnt lgkmcnt(1)\n"
    "v_exp_f32 v70, v70\n"
    "v_mfma_f32_16x16x32_bf16 v[92:95], v[180:183], v[16:19], v[92:95]\n"
    "buffer_load_dwordx4 v200, s[36:39], s65 offen offset:1024 sc1 lds\n"
    "v_mfma_f32_16x16x32_bf16 v[88:91], v[180:183], a[248:251], v[88:91]\n"
    "ds_read_b128 v[164:167], v200 offset:13312\n"
    "v_cvt_pk_bf16_f32 v148, v68, v69\n"
    "v_mfma_f32_16x16x32_bf16 v[88:91], v[176:179], a[244:247], v[88:91]\n"
    "v_exp_f32 v71, v71\n"
    "v_mfma_f32_16x16x32_bf16 v[88:91], v[172:175], a[240:243], v[88:91]\n"
    "v_exp_f32 v72, v72\n"
    "v_mfma_f32_16x16x32_bf16 v[84:87], v[172:175], a[216:219], v[84:87]\n"
    "ds_read_b128 v[168:171], v200 offset:14336\n"
    "v_cvt_pk_bf16_f32 v149, v70, v71\n"
    "v_mfma_f32_16x16x32_bf16 v[84:87], v[176:179], a[220:223], v[84:87]\n"
    "buffer_load_dwordx4 v200, s[36:39], s65 offen offset:2048 sc1 lds\n"
    "v_mfma_f32_16x16x32_bf16 v[84:87], v[180:183], a[224:227], v[84:87]\n"
    "v_exp_f32 v73, v73\n"
    "v_mfma_f32_16x16x32_bf16 v[80:83], v[180:183], a[200:203], v[80:83]\n"
    "v_exp_f32 v74, v74\n"
    "v_mfma_f32_16x16x32_bf16 v[80:83], v[176:179], a[196:199], v[80:83]\n"
    "v_exp_f32 v75, v75\n"
    "v_mfma_f32_16x16x32_bf16 v[80:83], v[172:175], a[192:195], v[80:83]\n"
    "s_waitcnt lgkmcnt(1)\n"
    "buffer_load_dwordx4 v200, s[36:39], s65 offen offset:3072 sc1 lds\n"
    "v_mfma_f32_16x16x32_bf16 v[32:35], v[160:163], a[192:195], v[96:99]\n"
    "ds_read_b128 v[172:175], v200 offset:15360\n"
    "v_cvt_pk_bf16_f32 v152, v72, v73\n"
    "v_mfma_f32_16x16x32_bf16 v[32:35], v[164:167], a[196:199], v[32:35]\n"
    "s_waitcnt lgkmcnt(1)\n"
    "s_add_u32 s65, s60, 0x800\n"
    "v_cvt_pk_bf16_f32 v153, v74, v75\n"
    "v_mfma_f32_16x16x32_bf16 v[32:35], v[168:171], a[200:203], v[32:35]\n"
    "s_mov_b32 m0, 0xb000\n"
    "v_exp_f32 v76, v76\n"
    "v_mfma_f32_16x16x32_bf16 v[36:39], v[168:171], a[224:227], v[100:103]\n"
    "ds_read_b128 v[176:179], v200 offset:16384\n"
    "v_add_f32 v113, v113, v64\n"
    "v_mfma_f32_16x16x32_bf16 v[36:39], v[164:167], a[220:223], v[36:39]\n"
    "buffer_load_dwordx4 v200, s[36:39], s65 offen sc1 lds\n"
    "v_mfma_f32_16x16x32_bf16 v[36:39], v[160:163], a[216:219], v[36:39]\n"
    "v_exp_f32 v77, v77\n"
    "v_mfma_f32_16x16x32_bf16 v[40:43], v[160:163], a[240:243], v[104:107]\n"
    "ds_read_b128 v[180:183], v200 offset:17408\n"
    "v_cvt_pk_bf16_f32 v156, v76, v77\n"
    "v_mfma_f32_16x16x32_bf16 v[40:43], v[164:167], a[244:247], v[40:43]\n"
    "v_exp_f32 v78, v78\n"
    "v_mfma_f32_16x16x32_bf16 v[40:43], v[168:171], a[248:251], v[40:43]\n"
    "buffer_load_dwordx4 v200, s[36:39], s65 offen offset:1024 sc1 lds\n"
    "v_mfma_f32_16x16x32_bf16 v[44:47], v[168:171], v[16:19], v[108:111]\n"
    "v_exp_f32 v79, v79\n"
    "v_mfma_f32_16x16x32_bf16 v[44:47], v[164:167], v[12:15], v[44:47]\n"
    "v_exp_f32 v80, v80\n"
    "v_mfma_f32_16x16x32_bf16 v[44:47], v[160:163], v[8:11], v[44:47]\n"
    "s_waitcnt lgkmcnt(1)\n"
    "v_exp_f32 v81, v81\n"
    "v_mfma_f32_16x16x32_bf16 v[44:47], v[172:175], v[20:23], v[44:47]\n"
    "ds_read_b128 v[160:163], v200 offset:21504\n"
    "v_cvt_pk_bf16_f32 v157, v78, v79\n"
    "v_mfma_f32_16x16x32_bf16 v[44:47], v[176:179], v[24:27], v[44:47]\n"
    "s_waitcnt lgkmcnt(1)\n"
    "buffer_load_dwordx4 v200, s[36:39], s65 offen offset:2048 sc1 lds\n"
    "v_mfma_f32_16x16x32_bf16 v[44:47], v[180:183], v[28:31], v[44:47]\n"
    "v_exp_f32 v82, v82\n"
    "v_mfma_f32_16x16x32_bf16 v[40:43], v[180:183], v[4:7], v[40:43]\n"
    "ds_read_b128 v[164:167], v200 offset:22528\n"
    "v_cvt_pk_bf16_f32 v146, v80, v81\n"
    "v_mfma_f32_16x16x32_bf16 v[40:43], v[176:179], v[0:3], v[40:43]\n"
    "v_exp_f32 v83, v83\n"
    "v_mfma_f32_16x16x32_bf16 v[40:43], v[172:175], a[252:255], v[40:43]\n"
    "buffer_load_dwordx4 v200, s[36:39], s65 offen offset:3072 sc1 lds\n"
    "v_mfma_f32_16x16x32_bf16 v[36:39], v[172:175], a[228:231], v[36:39]\n"
    "s_add_u32 s65, s61, 0xfffff800\n"
    "ds_read_b128 v[168:171], v200 offset:23552\n"
    "v_cvt_pk_bf16_f32 v147, v82, v83\n"
    "v_mfma_f32_16x16x32_bf16 v[36:39], v[176:179], a[232:235], v[36:39]\n"
    "s_mov_b32 m0, 0x10000\n"
    "v_exp_f32 v84, v84\n"
    "v_mfma_f32_16x16x32_bf16 v[36:39], v[180:183], a[236:239], v[36:39]\n"
    "v_exp_f32 v85, v85\n"
    "v_mfma_f32_16x16x32_bf16 v[32:35], v[180:183], a[212:215], v[32:35]\n"
    "buffer_load_dwordx4 v200, s[40:43], s65 offen sc1 lds\n"
    "v_mfma_f32_16x16x32_bf16 v[32:35], v[176:179], a[208:211], v[32:35]\n"
    "v_exp_f32 v86, v86\n"
    "v_mfma_f32_16x16x32_bf16 v[32:35], v[172:175], a[204:207], v[32:35]\n"
    "s_waitcnt lgkmcnt(1)\n"
    "v_exp_f32 v87, v87\n"
    "v_mfma_f32_16x16x32_bf16 v[48:51], v[160:163], a[204:207], v[96:99]\n"
    "ds_read_b128 v[172:175], v200 offset:18432\n"
    "v_cvt_pk_bf16_f32 v150, v84, v85\n"
    "v_mfma_f32_16x16x32_bf16 v[48:51], v[164:167], a[208:211], v[48:51]\n"
    "s_waitcnt lgkmcnt(1)\n"
    "buffer_load_dwordx4 v200, s[40:43], s65 offen offset:1024 sc1 lds\n"
    "v_mfma_f32_16x16x32_bf16 v[48:51], v[168:171], a[212:215], v[48:51]\n"
    "v_exp_f32 v88, v88\n"
    "v_mfma_f32_16x16x32_bf16 v[52:55], v[168:171], a[236:239], v[100:103]\n"
    "ds_read_b128 v[176:179], v200 offset:19456\n"
    "v_cvt_pk_bf16_f32 v151, v86, v87\n"
    "v_mfma_f32_16x16x32_bf16 v[52:55], v[164:167], a[232:235], v[52:55]\n"
    "v_exp_f32 v89, v89\n"
    "v_mfma_f32_16x16x32_bf16 v[52:55], v[160:163], a[228:231], v[52:55]\n"
    "buffer_load_dwordx4 v200, s[40:43], s65 offen offset:2048 sc1 lds\n"
    "v_mfma_f32_16x16x32_bf16 v[56:59], v[160:163], a[252:255], v[104:107]\n"
    "ds_read_b128 v[180:183], v200 offset:20480\n"
    "v_cvt_pk_bf16_f32 v154, v88, v89\n"
    "v_mfma_f32_16x16x32_bf16 v[56:59], v[164:167], v[0:3], v[56:59]\n"
    "v_exp_f32 v90, v90\n"
    "v_mfma_f32_16x16x32_bf16 v[56:59], v[168:171], v[4:7], v[56:59]\n"
    "v_exp_f32 v91, v91\n"
    "v_mfma_f32_16x16x32_bf16 v[60:63], v[168:171], v[28:31], v[108:111]\n"
    "buffer_load_dwordx4 v200, s[40:43], s65 offen offset:3072 sc1 lds\n"
    "v_mfma_f32_16x16x32_bf16 v[60:63], v[164:167], v[24:27], v[60:63]\n"
    "s_add_u32 s65, s61, 0x800\n"
    "v_exp_f32 v92, v92\n"
    "v_mfma_f32_16x16x32_bf16 v[60:63], v[160:163], v[20:23], v[60:63]\n"
    "s_waitcnt lgkmcnt(1)\n"
    "s_mov_b32 m0, 0x11000\n"
    "v_cvt_pk_bf16_f32 v155, v90, v91\n"
    "v_mfma_f32_16x16x32_bf16 v[60:63], v[172:175], v[8:11], v[60:63]\n"
    "ds_read_b128 v[160:163], v201 offset:24576\n"
    "v_add_f32 v114, v114, v65\n"
    "v_mfma_f32_16x16x32_bf16 v[60:63], v[176:179], v[12:15], v[60:63]\n"
    "s_waitcnt lgkmcnt(1)\n"
    "buffer_load_dwordx4 v200, s[40:43], s65 offen sc1 lds\n"
    "v_mfma_f32_16x16x32_bf16 v[60:63], v[180:183], v[16:19], v[60:63]\n"
    "v_exp_f32 v93, v93\n"
    "v_mfma_f32_16x16x32_bf16 v[56:59], v[180:183], a[248:251], v[56:59]\n"
    "ds_read_b128 v[164:167], v201 offset:25600\n"
    "v_cvt_pk_bf16_f32 v158, v92, v93\n"
    "v_mfma_f32_16x16x32_bf16 v[56:59], v[176:179], a[244:247], v[56:59]\n"
    "v_exp_f32 v94, v94\n"
    "v_mfma_f32_16x16x32_bf16 v[56:59], v[172:175], a[240:243], v[56:59]\n"
    "buffer_load_dwordx4 v200, s[40:43], s65 offen offset:1024 sc1 lds\n"
    "v_mfma_f32_16x16x32_bf16 v[52:55], v[172:175], a[216:219], v[52:55]\n"
    "ds_read_b128 v[168:171], v201 offset:26624\n"
    "v_add_f32 v113, v113, v66\n"
    "v_mfma_f32_16x16x32_bf16 v[52:55], v[176:179], a[220:223], v[52:55]\n"
    "v_exp_f32 v95, v95\n"
    "v_mfma_f32_16x16x32_bf16 v[52:55], v[180:183], a[224:227], v[52:55]\n"
    "v_cvt_pk_bf16_f32 v159, v94, v95\n"
    "v_add_f32 v114, v114, v67\n"
    "v_mfma_f32_16x16x32_bf16 v[48:51], v[180:183], a[200:203], v[48:51]\n"
    "buffer_load_dwordx4 v200, s[40:43], s65 offen offset:2048 sc1 lds\n"
    "v_mfma_f32_16x16x32_bf16 v[48:51], v[176:179], a[196:199], v[48:51]\n"
    "v_exp_f32 v32, v32\n"
    "v_mfma_f32_16x16x32_bf16 v[48:51], v[172:175], a[192:195], v[48:51]\n"
    "s_waitcnt lgkmcnt(1)\n"
    "v_exp_f32 v33, v33\n"
    "v_mfma_f32_16x16x32_bf16 a[0:3], v[160:163], v[128:131], a[0:3]\n"
    "ds_read_b128 v[172:175], v201 offset:27648\n"
    "ds_read_b128 v[176:179], v201 offset:28672\n"
    "v_mfma_f32_16x16x32_bf16 a[0:3], v[164:167], v[144:147], a[0:3]\n"
    "buffer_load_dwordx4 v200, s[40:43], s65 offen offset:3072 sc1 lds\n"
    "v_mfma_f32_16x16x32_bf16 a[4:7], v[160:163], v[132:135], a[4:7]\n"
    "v_exp_f32 v34, v34\n"
    "v_mfma_f32_16x16x32_bf16 a[4:7], v[164:167], v[148:151], a[4:7]\n"
    "v_exp_f32 v35, v35\n"
    "v_mfma_f32_16x16x32_bf16 a[8:11], v[160:163], v[136:139], a[8:11]\n"
    "ds_read_b128 v[180:183], v201 offset:29696\n"
    "v_add_f32 v117, v117, v68\n"
    "v_mfma_f32_16x16x32_bf16 a[8:11], v[164:167], v[152:155], a[8:11]\n"
    "v_exp_f32 v36, v36\n"
    "v_mfma_f32_16x16x32_bf16 a[12:15], v[160:163], v[140:143], a[12:15]\n"
    "v_exp_f32 v37, v37\n"
    "v_mfma_f32_16x16x32_bf16 a[12:15], v[164:167], v[156:159], a[12:15]\n"
    "s_waitcnt lgkmcnt(2)\n"
    "v_exp_f32 v38, v38\n"
    "v_mfma_f32_16x16x32_bf16 a[28:31], v[168:171], v[140:143], a[28:31]\n"
    "ds_read_b128 v[160:163], v201 offset:30720\n"
    "v_add_f32 v118, v118, v69\n"
    "v_mfma_f32_16x16x32_bf16 a[28:31], v[172:175], v[156:159], a[28:31]\n"
    "v_exp_f32 v39, v39\n"
    "v_mfma_f32_16x16x32_bf16 a[24:27], v[172:175], v[152:155], a[24:27]\n"
    "v_exp_f32 v40, v40\n"
    "v_mfma_f32_16x16x32_bf16 a[24:27], v[168:171], v[136:139], a[24:27]\n"
    "v_exp_f32 v41, v41\n"
    "v_mfma_f32_16x16x32_bf16 a[20:23], v[168:171], v[132:135], a[20:23]\n"
    "ds_read_b128 v[164:167], v201 offset:31744\n"
    "v_add_f32 v117, v117, v70\n"
    "v_mfma_f32_16x16x32_bf16 a[20:23], v[172:175], v[148:151], a[20:23]\n"
    "v_exp_f32 v42, v42\n"
    "v_mfma_f32_16x16x32_bf16 a[16:19], v[172:175], v[144:147], a[16:19]\n"
    "v_exp_f32 v43, v43\n"
    "v_mfma_f32_16x16x32_bf16 a[16:19], v[168:171], v[128:131], a[16:19]\n"
    "s_waitcnt lgkmcnt(2)\n"
    "v_exp_f32 v44, v44\n"
    "v_mfma_f32_16x16x32_bf16 a[32:35], v[176:179], v[128:131], a[32:35]\n"
    "ds_read_b128 v[168:171], v201 offset:32768\n"
    "v_add_f32 v113, v113, v32\n"
    "v_mfma_f32_16x16x32_bf16 a[32:35], v[180:183], v[144:147], a[32:35]\n"
    "v_exp_f32 v45, v45\n"
    "v_mfma_f32_16x16x32_bf16 a[36:39], v[180:183], v[148:151], a[36:39]\n"
    "v_exp_f32 v46, v46\n"
    "v_mfma_f32_16x16x32_bf16 a[36:39], v[176:179], v[132:135], a[36:39]\n"
    "v_exp_f32 v47, v47\n"
    "v_mfma_f32_16x16x32_bf16 a[40:43], v[176:179], v[136:139], a[40:43]\n"
    "ds_read_b128 v[172:175], v201 offset:33792\n"
    "v_add_f32 v118, v118, v71\n"
    "v_mfma_f32_16x16x32_bf16 a[40:43], v[180:183], v[152:155], a[40:43]\n"
    "v_exp_f32 v48, v48\n"
    "v_mfma_f32_16x16x32_bf16 a[44:47], v[180:183], v[156:159], a[44:47]\n"
    "v_exp_f32 v49, v49\n"
    "v_mfma_f32_16x16x32_bf16 a[44:47], v[176:179], v[140:143], a[44:47]\n"
    "s_waitcnt lgkmcnt(2)\n"
    "v_exp_f32 v50, v50\n"
    "v_mfma_f32_16x16x32_bf16 a[60:63], v[160:163], v[140:143], a[60:63]\n"
    "ds_read_b128 v[176:179], v201 offset:34816\n"
    "v_add_f32 v114, v114, v33\n"
    "v_mfma_f32_16x16x32_bf16 a[60:63], v[164:167], v[156:159], a[60:63]\n"
    "v_exp_f32 v51, v51\n"
    "v_mfma_f32_16x16x32_bf16 a[56:59], v[164:167], v[152:155], a[56:59]\n"
    "v_exp_f32 v52, v52\n"
    "v_mfma_f32_16x16x32_bf16 a[56:59], v[160:163], v[136:139], a[56:59]\n"
    "v_add_f32 v121, v121, v72\n"
    "v_add_f32 v113, v113, v34\n"
    "v_mfma_f32_16x16x32_bf16 a[52:55], v[160:163], v[132:135], a[52:55]\n"
    "ds_read_b128 v[180:183], v201 offset:35840\n"
    "v_add_f32 v122, v122, v73\n"
    "v_mfma_f32_16x16x32_bf16 a[52:55], v[164:167], v[148:151], a[52:55]\n"
    "v_exp_f32 v53, v53\n"
    "v_mfma_f32_16x16x32_bf16 a[48:51], v[164:167], v[144:147], a[48:51]\n"
    "v_add_f32 v114, v114, v35\n"
    "v_add_f32 v121, v121, v74\n"
    "v_mfma_f32_16x16x32_bf16 a[48:51], v[160:163], v[128:131], a[48:51]\n"
    "s_waitcnt lgkmcnt(2)\n"
    "v_add_f32 v117, v117, v36\n"
    "v_add_f32 v122, v122, v75\n"
    "v_mfma_f32_16x16x32_bf16 a[64:67], v[168:171], v[128:131], a[64:67]\n"
    "ds_read_b128 v[160:163], v201 offset:36864\n"
    "v_add_f32 v118, v118, v37\n"
    "v_mfma_f32_16x16x32_bf16 a[64:67], v[172:175], v[144:147], a[64:67]\n"
    "v_exp_f32 v54, v54\n"
    "v_mfma_f32_16x16x32_bf16 a[68:71], v[172:175], v[148:151], a[68:71]\n"
    "v_add_f32 v125, v125, v76\n"
    "v_add_f32 v117, v117, v38\n"
    "v_mfma_f32_16x16x32_bf16 a[68:71], v[168:171], v[132:135], a[68:71]\n"
    "s_add_u32 m0, s51, s95\n"
    "v_exp_f32 v55, v55\n"
    "v_mfma_f32_16x16x32_bf16 a[72:75], v[168:171], v[136:139], a[72:75]\n"
    "ds_read_b128 v[164:167], v201 offset:37888\n"
    "v_add_f32 v126, v126, v77\n"
    "v_mfma_f32_16x16x32_bf16 a[72:75], v[172:175], v[152:155], a[72:75]\n"
    "buffer_load_dwordx4 v202, s[32:35], s64 offen lds\n"
    "v_mfma_f32_16x16x32_bf16 a[76:79], v[172:175], v[156:159], a[76:79]\n"
    "v_add_f32 v118, v118, v39\n"
    "v_add_f32 v125, v125, v78\n"
    "v_mfma_f32_16x16x32_bf16 a[76:79], v[168:171], v[140:143], a[76:79]\n"
    "s_waitcnt lgkmcnt(2)\n"
    "v_add_f32 v121, v121, v40\n"
    "v_add_f32 v126, v126, v79\n"
    "v_mfma_f32_16x16x32_bf16 a[92:95], v[176:179], v[140:143], a[92:95]\n"
    "ds_read_b128 v[168:171], v201 offset:38912\n"
    "v_add_f32 v122, v122, v41\n"
    "v_mfma_f32_16x16x32_bf16 a[92:95], v[180:183], v[156:159], a[92:95]\n"
    "buffer_load_dwordx4 v203, s[32:35], s64 offen offset:1024 lds\n"
    "v_mfma_f32_16x16x32_bf16 a[88:91], v[180:183], v[152:155], a[88:91]\n"
    "v_exp_f32 v56, v56\n"
    "v_mfma_f32_16x16x32_bf16 a[88:91], v[176:179], v[136:139], a[88:91]\n"
    "v_add_f32 v113, v113, v80\n"
    "v_add_f32 v121, v121, v42\n"
    "v_mfma_f32_16x16x32_bf16 a[84:87], v[176:179], v[132:135], a[84:87]\n"
    "ds_read_b128 v[172:175], v201 offset:39936\n"
    "v_add_f32 v114, v114, v81\n"
    "v_mfma_f32_16x16x32_bf16 a[84:87], v[180:183], v[148:151], a[84:87]\n"
    "buffer_load_dwordx4 v204, s[32:35], s64 offen offset:2048 lds\n"
    "v_mfma_f32_16x16x32_bf16 a[80:83], v[180:183], v[144:147], a[80:83]\n"
    "v_exp_f32 v57, v57\n"
    "v_mfma_f32_16x16x32_bf16 a[80:83], v[176:179], v[128:131], a[80:83]\n"
    "s_waitcnt lgkmcnt(2)\n"
    "v_add_f32 v122, v122, v43\n"
    "v_add_f32 v113, v113, v82\n"
    "v_mfma_f32_16x16x32_bf16 a[96:99], v[160:163], v[128:131], a[96:99]\n"
    "s_mov_b32 s95, s94\n"
    "ds_read_b128 v[176:179], v201 offset:40960\n"
    "v_add_f32 v125, v125, v44\n"
    "v_mfma_f32_16x16x32_bf16 a[96:99], v[164:167], v[144:147], a[96:99]\n"
    "s_sub_u32 s94, s94, 0x3000\n"
    "v_exp_f32 v58, v58\n"
    "v_mfma_f32_16x16x32_bf16 a[100:103], v[164:167], v[148:151], a[100:103]\n"
    "s_min_u32 s94, s94, 0x6000\n"
    "v_add_f32 v114, v114, v83\n"
    "v_add_f32 v126, v126, v45\n"
    "v_mfma_f32_16x16x32_bf16 a[100:103], v[160:163], v[132:135], a[100:103]\n"
    "s_sub_u32 s93, s94, s95\n"
    "v_add_f32 v117, v117, v84\n"
    "v_add_f32 v125, v125, v46\n"
    "v_mfma_f32_16x16x32_bf16 a[104:107], v[160:163], v[136:139], a[104:107]\n"
    "ds_read_b128 v[180:183], v201 offset:41984\n"
    "v_add_f32 v118, v118, v85\n"
    "v_mfma_f32_16x16x32_bf16 a[104:107], v[164:167], v[152:155], a[104:107]\n"
    "v_exp_f32 v59, v59\n"
    "v_mfma_f32_16x16x32_bf16 a[108:111], v[164:167], v[156:159], a[108:111]\n"
    "v_add_f32 v126, v126, v47\n"
    "v_add_f32 v117, v117, v86\n"
    "v_mfma_f32_16x16x32_bf16 a[108:111], v[160:163], v[140:143], a[108:111]\n"
    "s_waitcnt lgkmcnt(2)\n"
    "v_add_f32 v113, v113, v48\n"
    "v_add_f32 v118, v118, v87\n"
    "v_mfma_f32_16x16x32_bf16 a[124:127], v[168:171], v[140:143], a[124:127]\n"
    "ds_read_b128 v[160:163], v201 offset:43008\n"
    "v_add_f32 v114, v114, v49\n"
    "v_mfma_f32_16x16x32_bf16 a[124:127], v[172:175], v[156:159], a[124:127]\n"
    "v_exp_f32 v60, v60\n"
    "v_mfma_f32_16x16x32_bf16 a[120:123], v[172:175], v[152:155], a[120:123]\n"
    "v_add_f32 v121, v121, v88\n"
    "v_add_f32 v113, v113, v50\n"
    "v_mfma_f32_16x16x32_bf16 a[120:123], v[168:171], v[136:139], a[120:123]\n"
    "v_exp_f32 v61, v61\n"
    "v_mfma_f32_16x16x32_bf16 a[116:119], v[168:171], v[132:135], a[116:119]\n"
    "ds_read_b128 v[164:167], v201 offset:44032\n"
    "v_add_u32 v207, s93, v207\n"
    "v_mfma_f32_16x16x32_bf16 a[116:119], v[172:175], v[148:151], a[116:119]\n"
    "v_add_f32 v122, v122, v89\n"
    "v_add_f32 v114, v114, v51\n"
    "v_mfma_f32_16x16x32_bf16 a[112:115], v[172:175], v[144:147], a[112:115]\n"
    "v_add_f32 v121, v121, v90\n"
    "v_add_f32 v117, v117, v52\n"
    "v_mfma_f32_16x16x32_bf16 a[112:115], v[168:171], v[128:131], a[112:115]\n"
    "s_waitcnt lgkmcnt(2)\n"
    "v_exp_f32 v62, v62\n"
    "v_mfma_f32_16x16x32_bf16 a[128:131], v[176:179], v[128:131], a[128:131]\n"
    "ds_read_b128 v[168:171], v201 offset:45056\n"
    "v_add_f32 v122, v122, v91\n"
    "v_mfma_f32_16x16x32_bf16 a[128:131], v[180:183], v[144:147], a[128:131]\n"
    "v_add_f32 v118, v118, v53\n"
    "v_add_f32 v125, v125, v92\n"
    "v_mfma_f32_16x16x32_bf16 a[132:135], v[180:183], v[148:151], a[132:135]\n"
    "v_add_f32 v117, v117, v54\n"
    "v_add_f32 v126, v126, v93\n"
    "v_mfma_f32_16x16x32_bf16 a[132:135], v[176:179], v[132:135], a[132:135]\n"
    "s_add_u32 s64, s64, 0xc000\n"
    "v_exp_f32 v63, v63\n"
    "v_mfma_f32_16x16x32_bf16 a[136:139], v[176:179], v[136:139], a[136:139]\n"
    "s_add_u32 s62, s62, 0x6000\n"
    "ds_read_b128 v[172:175], v201 offset:46080\n"
    "v_add_f32 v118, v118, v55\n"
    "v_mfma_f32_16x16x32_bf16 a[136:139], v[180:183], v[152:155], a[136:139]\n"
    "s_add_u32 s63, s63, 0x6000\n"
    "v_add_f32 v125, v125, v94\n"
    "v_add_f32 v121, v121, v56\n"
    "v_mfma_f32_16x16x32_bf16 a[140:143], v[180:183], v[156:159], a[140:143]\n"
    "s_add_u32 s60, s60, 0x6000\n"
    "v_add_f32 v126, v126, v95\n"
    "v_add_f32 v122, v122, v57\n"
    "v_mfma_f32_16x16x32_bf16 a[140:143], v[176:179], v[140:143], a[140:143]\n"
    "s_waitcnt lgkmcnt(2)\n"
    "v_mfma_f32_16x16x32_bf16 a[156:159], v[160:163], v[140:143], a[156:159]\n"
    "s_add_u32 s61, s61, 0x6000\n"
    "ds_read_b128 v[176:179], v201 offset:47104\n"
    "v_mfma_f32_16x16x32_bf16 a[156:159], v[164:167], v[156:159], a[156:159]\n"
    "ds_read_b128 v[180:183], v201 offset:48128\n"
    "v_mfma_f32_16x16x32_bf16 a[152:155], v[164:167], v[152:155], a[152:155]\n"
    "s_waitcnt vmcnt(3)\n"
    "v_mfma_f32_16x16x32_bf16 a[152:155], v[160:163], v[136:139], a[152:155]\n"
    "v_mfma_f32_16x16x32_bf16 a[148:151], v[160:163], v[132:135], a[148:151]\n"
    "v_mfma_f32_16x16x32_bf16 a[148:151], v[164:167], v[148:151], a[148:151]\n"
    "v_mfma_f32_16x16x32_bf16 a[144:147], v[164:167], v[144:147], a[144:147]\n"
    "v_mfma_f32_16x16x32_bf16 a[144:147], v[160:163], v[128:131], a[144:147]\n"
    "s_waitcnt lgkmcnt(0)\n"
    "v_mfma_f32_16x16x32_bf16 a[160:163], v[168:171], v[128:131], a[160:163]\n"
    "s_barrier\n"
    "LMID2:\n"
    "v_mfma_f32_16x16x32_bf16 a[160:163], v[172:175], v[144:147], a[160:163]\n"
    "v_add_f32 v121, v121, v58\n"
    "v_add_f32 v122, v122, v59\n"
    "v_mfma_f32_16x16x32_bf16 a[164:167], v[172:175], v[148:151], a[164:167]\n"
    "ds_read_b128 v[160:163], v200 offset:24576\n"
    "v_add_f32 v125, v125, v60\n"
    "v_mfma_f32_16x16x32_bf16 a[164:167], v[168:171], v[132:135], a[164:167]\n"
    "v_add_f32 v126, v126, v61\n"
    "v_add_f32 v125, v125, v62\n"
    "v_mfma_f32_16x16x32_bf16 a[168:171], v[168:171], v[136:139], a[168:171]\n"
    "ds_read_b128 v[164:167], v200 offset:25600\n"
    "v_add_f32 v126, v126, v63\n"
    "v_mfma_f32_16x16x32_bf16 a[168:171], v[172:175], v[152:155], a[168:171]\n"
    "ds_read_b64 v[228:229], v210 offset:64\n"
    "ds_read2st64_b32 v[240:241], v207 offset0:96 offset1:99\n"
    "v_mfma_f32_16x16x32_bf16 a[172:175], v[172:175], v[156:159], a[172:175]\n"
    "ds_read2st64_b32 v[242:243], v207 offset0:102 offset1:105\n"
    "ds_read2st64_b32 v[244:245], v207 offset0:108 offset1:111\n"
    "v_mfma_f32_16x16x32_bf16 a[172:175], v[168:171], v[140:143], a[172:175]\n"
    "ds_read2st64_b32 v[246:247], v207 offset0:114 offset1:117\n"
    "v_mfma_f32_16x16x32_bf16 a[188:191], v[176:179], v[140:143], a[188:191]\n"
    "ds_read_b128 v[168:171], v200 offset:26624\n"
    "v_cvt_pk_bf16_f32 v140, v44, v45\n"
    "v_mfma_f32_16x16x32_bf16 a[188:191], v[180:183], v[156:159], a[188:191]\n"
    "v_cvt_pk_bf16_f32 v141, v46, v47\n"
    "v_mfma_f32_16x16x32_bf16 a[184:187], v[176:179], v[136:139], a[184:187]\n"
    "v_cvt_pk_bf16_f32 v136, v40, v41\n"
    "v_cvt_pk_bf16_f32 v137, v42, v43\n"
    "v_mfma_f32_16x16x32_bf16 a[184:187], v[180:183], v[152:155], a[184:187]\n"
    "v_mfma_f32_16x16x32_bf16 a[180:183], v[176:179], v[132:135], a[180:183]\n"
    "s_add_u32 s59, s59, 1\n"
    "ds_read_b128 v[172:175], v200 offset:27648\n"
    "v_cvt_pk_bf16_f32 v132, v36, v37\n"
    "v_mfma_f32_16x16x32_bf16 a[180:183], v[180:183], v[148:151], a[180:183]\n"
    "v_cvt_pk_bf16_f32 v133, v38, v39\n"
    "v_mfma_f32_16x16x32_bf16 a[176:179], v[176:179], v[128:131], a[176:179]\n"
    "s_cmp_lt_u32 s59, 15\n"
    "s_waitcnt lgkmcnt(5)\n"
    "v_cvt_f32_i32 v240, v240\n"
    "v_cvt_f32_i32 v241, v241\n"
    "v_mfma_f32_16x16x32_bf16 a[176:179], v[180:183], v[144:147], a[176:179]\n"
    "s_cbranch_scc1 LLOOP2\n"
    "s_branch LTAIL\n"
    "LENT1:\n"
    "s_mov_b32 s59, 1\n"
    "s_add_u32 m0, s72, 0x21000\n"
    "s_nop 0\n"
    "buffer_load_dwordx4 v250, s[48:51], 0 offen offset:2048 lds\n"
    "s_add_u32 m0, s72, 0x18000\n"
    "s_nop 0\n"
    "buffer_load_dwordx4 v202, s[32:35], s64 offen lds\n"
    "buffer_load_dwordx4 v203, s[32:35], s64 offen offset:1024 lds\n"
    "buffer_load_dwordx4 v204, s[32:35], s64 offen offset:2048 lds\n"
    "s_add_u32 s64, s64, 0xc000\n"
    "s_mov_b32 s94, 0x3000\n"
    "s_mov_b32 s95, 0x6000\n"
    "s_add_u32 s51, s72, 0x18000\n"
    "v_add_u32 v205, 0x3000, v205\n"
    "v_add_u32 v206, 0x3000, v206\n"
    "v_add_u32 v207, 0x3000, v207\n"
    "ds_read_u16_d16_hi v223, v209 offset:6336\n"
    "ds_read_u16_d16_hi v222, v209 offset:192\n"
    "ds_read_b128 v[160:163], v200 offset:24576\n"
    "v_add_f32 v121, v121, v58\n"
    "ds_read_b128 v[232:235], v205 offset:24576\n"
    "v_add_f32 v122, v122, v59\n"
    "ds_read_b128 v[164:167], v200 offset:25600\n"
    "ds_read_b128 v[236:239], v206 offset:24576\n"
    "v_add_f32 v125, v125, v60\n"
    "v_add_f32 v126, v126, v61\n"
    "v_add_f32 v125, v125, v62\n"
    "v_add_f32 v126, v126, v63\n"
    "ds_read_b64 v[228:229], v210 offset:64\n"
    "ds_read2st64_b32 v[240:241], v207 offset0:96 offset1:99\n"
    "ds_read_b128 v[168:171], v200 offset:26624\n"
    "ds_read2st64_b32 v[242:243], v207 offset0:102 offset1:105\n"
    "ds_read2st64_b32 v[244:245], v207 offset0:108 offset1:111\n"
    "ds_read2st64_b32 v[246:247], v207 offset0:114 offset1:117\n"
    "v_cvt_pk_bf16_f32 v140, v44, v45\n"
    "v_cvt_pk_bf16_f32 v141, v46, v47\n"
    "s_waitcnt lgkmcnt(6)\n"
    "v_mul_f32 v226, s75, v223\n"
    "ds_read_b128 v[172:175], v200 offset:27648\n"
    "v_fma_f32 v227, -v222, v226, v217\n"
    "v_cvt_f32_i32 v232, v232\n"
    "v_cvt_f32_i32 v233, v233\n"
    "v_cvt_f32_i32 v234, v234\n"
    "v_cvt_f32_i32 v235, v235\n"
    "s_nop 7\n"
    ".p2align 6\n"
    "LLOOP1:\n"
    "v_mfma_f32_16x16x32_bf16 v[64:67], v[160:163], a[192:195], v[96:99]\n"
    "ds_read_b128 v[176:179], v200 offset:28672\n"
    "v_cvt_f32_i32 v236, v236\n"
    "v_mfma_f32_16x16x32_bf16 v[64:67], v[164:167], a[196:199], v[64:67]\n"
    "s_waitcnt lgkmcnt(1)\n"
    "v_cvt_f32_i32 v237, v237\n"
    "v_cvt_f32_i32 v238, v238\n"
    "v_mfma_f32_16x16x32_bf16 v[64:67], v[168:171], a[200:203], v[64:67]\n"
    "v_cvt_f32_i32 v239, v239\n"
    "v_mul_f32_dpp v232, v226, v232 row_newbcast:0 row_mask:0xf bank_mask:0xf\n"
    "v_mfma_f32_16x16x32_bf16 v[68:71], v[168:171], a[224:227], v[100:103]\n"
    "v_mul_f32_dpp v233, v226, v233 row_newbcast:1 row_mask:0xf bank_mask:0xf\n"
    "v_mul_f32_dpp v234, v226, v234 row_newbcast:2 row_mask:0xf bank_mask:0xf\n"
    "v_mfma_f32_16x16x32_bf16 v[68:71], v[164:167], a[220:223], v[68:71]\n"
    "ds_read_b128 v[180:183], v200 offset:29696\n"
    "v_mul_f32_dpp v235, v226, v235 row_newbcast:3 row_mask:0xf bank_mask:0xf\n"
    "v_mfma_f32_16x16x32_bf16 v[68:71], v[160:163], a[216:219], v[68:71]\n"
    "v_mul_f32_dpp v236, v226, v236 row_newbcast:4 row_mask:0xf bank_mask:0xf\n"
    "v_cvt_f32_i32 v240, v240\n"
    "v_mfma_f32_16x16x32_bf16 v[72:75], v[160:163], a[240:243], v[104:107]\n"
    "v_mul_f32_dpp v237, v226, v237 row_newbcast:5 row_mask:0xf bank_mask:0xf\n"
    "v_cvt_f32_i32 v241, v241\n"
    "v_mfma_f32_16x16x32_bf16 v[72:75], v[164:167], a[244:247], v[72:75]\n"
    "v_mul_f32_dpp v238, v226, v238 row_newbcast:6 row_mask:0xf bank_mask:0xf\n"
    "v_cvt_f32_i32 v242, v242\n"
    "v_mfma_f32_16x16x32_bf16 v[72:75], v[168:171], a[248:251], v[72:75]\n"
    "v_mul_f32_dpp v239, v226, v239 row_newbcast:7 row_mask:0xf bank_mask:0xf\n"
    "v_cvt_f32_i32 v243, v243\n"
    "v_mfma_f32_16x16x32_bf16 v[76:79], v[168:171], v[16:19], v[108:111]\n"
    "v_add_f32_dpp v232, v227, v232 row_newbcast:0 row_mask:0xf bank_mask:0xf\n"
    "v_cvt_f32_i32 v244, v244\n"
    "v_mfma_f32_16x16x32_bf16 v[76:79], v[164:167], v[12:15], v[76:79]\n"
    "v_add_f32_dpp v233, v227, v233 row_newbcast:1 row_mask:0xf bank_mask:0xf\n"
    "v_cvt_f32_i32 v245, v245\n"
    "v_mfma_f32_16x16x32_bf16 v[76:79], v[160:163], v[8:11], v[76:79]\n"
    "s_waitcnt lgkmcnt(1)\n"
    "v_add_f32_dpp v234, v227, v234 row_newbcast:2 row_mask:0xf bank_mask:0xf\n"
    "v_cvt_f32_i32 v246, v246\n"
    "v_mfma_f32_16x16x32_bf16 v[76:79], v[172:175], v[20:23], v[76:79]\n"
    "ds_read_b128 v[160:163], v200 offset:33792\n"
    "v_add_f32_dpp v235, v227, v235 row_newbcast:3 row_mask:0xf bank_mask:0xf\n"
    "v_mfma_f32_16x16x32_bf16 v[76:79], v[176:179], v[24:27], v[76:79]\n"
    "s_waitcnt lgkmcnt(1)\n"
    "v_cvt_f32_i32 v247, v247\n"
    "v_add_f32_dpp v236, v227, v236 row_newbcast:4 row_mask:0xf bank_mask:0xf\n"
    "v_mfma_f32_16x16x32_bf16 v[76:79], v[180:183], v[28:31], v[76:79]\n"
    "v_mul_f32_dpp v240, v228, v240 row_newbcast:0 row_mask:0xf bank_mask:0xf\n"
    "v_add_f32_dpp v237, v227, v237 row_newbcast:5 row_mask:0xf bank_mask:0xf\n"
    "v_mfma_f32_16x16x32_bf16 v[72:75], v[180:183], v[4:7], v[72:75]\n"
    "ds_read_b128 v[164:167], v200 offset:34816\n"
    "v_mul_f32_dpp v241, v228, v241 row_newbcast:1 row_mask:0xf bank_mask:0xf\n"
    "v_mfma_f32_16x16x32_bf16 v[72:75], v[176:179], v[0:3], v[72:75]\n"
    "v_add_f32_dpp v238, v227, v238 row_newbcast:6 row_mask:0xf bank_mask:0xf\n"
    "v_mul_f32_dpp v242, v228, v242 row_newbcast:2 row_mask:0xf bank_mask:0xf\n"
    "v_mfma_f32_16x16x32_bf16 v[72:75], v[172:175], a[252:255], v[72:75]\n"
    "v_add_f32_dpp v239, v227, v239 row_newbcast:7 row_mask:0xf bank_mask:0xf\n"
    "v_mul_f32_dpp v243, v228, v243 row_newbcast:3 row_mask:0xf bank_mask:0xf\n"
    "v_mfma_f32_16x16x32_bf16 v[68:71], v[172:175], a[228:231], v[68:71]\n"
    "ds_read_b128 v[168:171], v200 offset:35840\n"
    "v_cvt_pk_bf16_f32 v232, v232, v233\n"
    "v_mfma_f32_16x16x32_bf16 v[68:71], v[176:179], a[232:235], v[68:71]\n"
    "v_mul_f32_dpp v244, v228, v244 row_newbcast:4 row_mask:0xf bank_mask:0xf\n"
    "v_cvt_pk_bf16_f32 v233, v234, v235\n"
    "v_mfma_f32_16x16x32_bf16 v[68:71], v[180:183], a[236:239], v[68:71]\n"
    "v_mul_f32_dpp v245, v228, v245 row_newbcast:5 row_mask:0xf bank_mask:0xf\n"
    "v_cvt_pk_bf16_f32 v234, v236, v237\n"
    "v_mfma_f32_16x16x32_bf16 v[64:67], v[180:183], a[212:215], v[64:67]\n"
    "v_mul_f32_dpp v246, v228, v246 row_newbcast:6 row_mask:0xf bank_mask:0xf\n"
    "v_cvt_pk_bf16_f32 v235, v238, v239\n"
    "v_mfma_f32_16x16x32_bf16 v[64:67], v[176:179], a[208:211], v[64:67]\n"
    "v_mul_f32_dpp v247, v228, v247 row_newbcast:7 row_mask:0xf bank_mask:0xf\n"
    "v_add_f32_dpp v240, v229, v240 row_newbcast:0 row_mask:0xf bank_mask:0xf\n"
    "v_mfma_f32_16x16x32_bf16 v[64:67], v[172:175], a[204:207], v[64:67]\n"
    "s_waitcnt lgkmcnt(1)\n"
    "v_add_f32_dpp v241, v229, v241 row_newbcast:1 row_mask:0xf bank_mask:0xf\n"
    "v_add_f32_dpp v242, v229, v242 row_newbcast:2 row_mask:0xf bank_mask:0xf\n"
    "v_mfma_f32_16x16x32_bf16 v[80:83], v[160:163], a[204:207], v[96:99]\n"
    "ds_read_b128 v[172:175], v200 offset:30720\n"
    "v_add_f32_dpp v243, v229, v243 row_newbcast:3 row_mask:0xf bank_mask:0xf\n"
    "v_mfma_f32_16x16x32_bf16 v[80:83], v[164:167], a[208:211], v[80:83]\n"
    "s_waitcnt lgkmcnt(1)\n"
    "buffer_store_dwordx4 v[232:235], v211, s[36:39], s62 offen sc1\n"
    "v_add_f32_dpp v244, v229, v244 row_newbcast:4 row_mask:0xf bank_mask:0xf\n"
    "v_add_f32_dpp v245, v229, v245 row_newbcast:5 row_mask:0xf bank_mask:0xf\n"
    "v_add_f32_dpp v246, v229, v246 row_newbcast:6 row_mask:0xf bank_mask:0xf\n"
    "v_mfma_f32_16x16x32_bf16 v[80:83], v[168:171], a[212:215], v[80:83]\n"
    "v_add_f32_dpp v247, v229, v247 row_newbcast:7 row_mask:0xf bank_mask:0xf\n"
    "v_cvt_pk_bf16_f32 v240, v240, v241\n"
    "v_mfma_f32_16x16x32_bf16 v[84:87], v[168:171], a[236:239], v[100:103]\n"
    "ds_read_b128 v[176:179], v200 offset:31744\n"
    "v_cvt_pk_bf16_f32 v241, v242, v243\n"
    "v_mfma_f32_16x16x32_bf16 v[84:87], v[164:167], a[232:235], v[84:87]\n"
    "v_cvt_pk_bf16_f32 v242, v244, v245\n"
    "v_cvt_pk_bf16_f32 v243, v246, v247\n"
    "v_mfma_f32_16x16x32_bf16 v[84:87], v[160:163], a[228:231], v[84:87]\n"
    "buffer_store_dwordx4 v[240:243], v212, s[40:43], s63 offen sc1\n"
    "v_cvt_pk_bf16_f32 v136, v40, v41\n"
    "v_cvt_pk_bf16_f32 v137, v42, v43\n"
    "v_cvt_pk_bf16_f32 v132, v36, v37\n"
    "v_mfma_f32_16x16x32_bf16 v[88:91], v[160:163], a[252:255], v[104:107]\n"
    "ds_read_b128 v[180:183], v200 offset:32768\n"
    "v_cvt_pk_bf16_f32 v133, v38, v39\n"
    "v_mfma_f32_16x16x32_bf16 v[88:91], v[164:167], v[0:3], v[88:91]\n"
    "v_cvt_pk_bf16_f32 v128, v32, v33\n"
    "v_cvt_pk_bf16_f32 v129, v34, v35\n"
    "v_mfma_f32_16x16x32_bf16 v[88:91], v[168:171], v[4:7], v[88:91]\n"
    "v_exp_f32 v64, v64\n"
    "v_mfma_f32_16x16x32_bf16 v[92:95], v[168:171], v[28:31], v[108:111]\n"
    "v_exp_f32 v65, v65\n"
    "v_mfma_f32_16x16x32_bf16 v[92:95], v[164:167], v[24:27], v[92:95]\n"
    "v_exp_f32 v66, v66\n"
    "v_mfma_f32_16x16x32_bf16 v[92:95], v[160:163], v[20:23], v[92:95]\n"
    "s_waitcnt lgkmcnt(1)\n"
    "v_exp_f32 v67, v67\n"
    "v_mfma_f32_16x16x32_bf16 v[92:95], v[172:175], v[8:11], v[92:95]\n"
    "ds_read_b128 v[160:163], v200 offset:36864\n"
    "v_cvt_pk_bf16_f32 v142, v60, v61\n"
    "v_mfma_f32_16x16x32_bf16 v[92:95], v[176:179], v[12:15], v[92:95]\n"
    "s_waitcnt lgkmcnt(1)\n"
    "v_exp_f32 v68, v68\n"
    "v_mfma_f32_16x16x32_bf16 v[92:95], v[180:183], v[16:19], v[92:95]\n"
    "v_exp_f32 v69, v69\n"
    "v_mfma_f32_16x16x32_bf16 v[88:91], v[180:183], a[248:251], v[88:91]\n"
    "ds_read_b128 v[164:167], v200 offset:37888\n"
    "v_cvt_pk_bf16_f32 v143, v62, v63\n"
    "v_mfma_f32_16x16x32_bf16 v[88:91], v[176:179], a[244:247], v[88:91]\n"
    "v_exp_f32 v70, v70\n"
    "v_mfma_f32_16x16x32_bf16 v[88:91], v[172:175], a[240:243], v[88:91]\n"
    "v_exp_f32 v71, v71\n"
    "v_mfma_f32_16x16x32_bf16 v[84:87], v[172:175], a[216:219], v[84:87]\n"
    "ds_read_b128 v[168:171], v200 offset:38912\n"
    "v_cvt_pk_bf16_f32 v138, v56, v57\n"
    "v_mfma_f32_16x16x32_bf16 v[84:87], v[176:179], a[220:223], v[84:87]\n"
    "v_cvt_pk_bf16_f32 v139, v58, v59\n"
    "v_cvt_pk_bf16_f32 v134, v52, v53\n"
    "v_mfma_f32_16x16x32_bf16 v[84:87], v[180:183], a[224:227], v[84:87]\n"
    "v_exp_f32 v72, v72\n"
    "v_mfma_f32_16x16x32_bf16 v[80:83], v[180:183], a[200:203], v[80:83]\n"
    "v_cvt_pk_bf16_f32 v135, v54, v55\n"
    "v_cvt_pk_bf16_f32 v130, v48, v49\n"
    "v_mfma_f32_16x16x32_bf16 v[80:83], v[176:179], a[196:199], v[80:83]\n"
    "v_exp_f32 v73, v73\n"
    "v_mfma_f32_16x16x32_bf16 v[80:83], v[172:175], a[192:195], v[80:83]\n"
    "s_waitcnt lgkmcnt(1)\n"
    "v_cvt_pk_bf16_f32 v131, v50, v51\n"
    "v_cvt_pk_bf16_f32 v144, v64, v65\n"
    "v_mfma_f32_16x16x32_bf16 v[32:35], v[160:163], a[192:195], v[96:99]\n"
    "ds_read_b128 v[172:175], v200 offset:39936\n"
    "v_cvt_pk_bf16_f32 v145, v66, v67\n"
    "v_mfma_f32_16x16x32_bf16 v[32:35], v[164:167], a[196:199], v[32:35]\n"
    "s_waitcnt lgkmcnt(1)\n"
    "s_add_u32 s65, s60, s92\n"
    "v_cvt_pk_bf16_f32 v148, v68, v69\n"
    "v_mfma_f32_16x16x32_bf16 v[32:35], v[168:171], a[200:203], v[32:35]\n"
    "s_add_u32 m0, s90, 0\n"
    "v_exp_f32 v74, v74\n"
    "v_mfma_f32_16x16x32_bf16 v[36:39], v[168:171], a[224:227], v[100:103]\n"
    "ds_read_b128 v[176:179], v200 offset:40960\n"
    "v_cvt_pk_bf16_f32 v149, v70, v71\n"
    "v_mfma_f32_16x16x32_bf16 v[36:39], v[164:167], a[220:223], v[36:39]\n"
    "buffer_load_dwordx4 v200, s[36:39], s65 offen sc1 lds\n"
    "v_mfma_f32_16x16x32_bf16 v[36:39], v[160:163], a[216:219], v[36:39]\n"
    "v_exp_f32 v75, v75\n"
    "v_mfma_f32_16x16x32_bf16 v[40:43], v[160:163], a[240:243], v[104:107]\n"
    "ds_read_b128 v[180:183], v200 offset:41984\n"
    "v_cvt_pk_bf16_f32 v152, v72, v73\n"
    "v_mfma_f32_16x16x32_bf16 v[40:43], v[164:167], a[244:247], v[40:43]\n"
    "v_exp_f32 v76, v76\n"
    "v_mfma_f32_16x16x32_bf16 v[40:43], v[168:171], a[248:251], v[40:43]\n"
    "buffer_load_dwordx4 v200, s[36:39], s65 offen offset:1024 sc1 lds\n"
    "v_mfma_f32_16x16x32_bf16 v[44:47], v[168:171], v[16:19], v[108:111]\n"
    "v_exp_f32 v77, v77\n"
    "v_mfma_f32_16x16x32_bf16 v[44:47], v[164:167], v[12:15], v[44:47]\n"
    "v_exp_f32 v78, v78\n"
    "v_mfma_f32_16x16x32_bf16 v[44:47], v[160:163], v[8:11], v[44:47]\n"
    "s_waitcnt lgkmcnt(1)\n"
    "v_exp_f32 v79, v79\n"
    "v_mfma_f32_16x16x32_bf16 v[44:47], v[172:175], v[20:23], v[44:47]\n"
    "ds_read_b128 v[160:163], v200 offset:46080\n"
    "v_cvt_pk_bf16_f32 v153, v74, v75\n"
    "v_mfma_f32_16x16x32_bf16 v[44:47], v[176:179], v[24:27], v[44:47]\n"
    "s_waitcnt lgkmcnt(1)\n"
    "buffer_load_dwordx4 v200, s[36:39], s65 offen offset:2048 sc1 lds\n"
    "v_mfma_f32_16x16x32_bf16 v[44:47], v[180:183], v[28:31], v[44:47]\n"
    "v_exp_f32 v80, v80\n"
    "v_mfma_f32_16x16x32_bf16 v[40:43], v[180:183], v[4:7], v[40:43]\n"
    "ds_read_b128 v[164:167], v200 offset:47104\n"
    "v_cvt_pk_bf16_f32 v156, v76, v77\n"
    "v_mfma_f32_16x16x32_bf16 v[40:43], v[176:179], v[0:3], v[40:43]\n"
    "v_exp_f32 v81, v81\n"
    "v_mfma_f32_16x16x32_bf16 v[40:43], v[172:175], a[252:255], v[40:43]\n"
    "buffer_load_dwordx4 v200, s[36:39], s65 offen offset:3072 sc1 lds\n"
    "v_mfma_f32_16x16x32_bf16 v[36:39], v[172:175], a[228:231], v[36:39]\n"
    "s_add_u32 s65, s61, s92\n"
    "ds_read_b128 v[168:171], v200 offset:48128\n"
    "v_cvt_pk_bf16_f32 v157, v78, v79\n"
    "v_mfma_f32_16x16x32_bf16 v[36:39], v[176:179], a[232:235], v[36:39]\n"
    "s_add_u32 m0, s90, 0x12000\n"
    "v_exp_f32 v82, v82\n"
    "v_mfma_f32_16x16x32_bf16 v[36:39], v[180:183], a[236:239], v[36:39]\n"
    "v_exp_f32 v83, v83\n"
    "v_mfma_f32_16x16x32_bf16 v[32:35], v[180:183], a[212:215], v[32:35]\n"
    "buffer_load_dwordx4 v200, s[40:43], s65 offen sc1 lds\n"
    "v_mfma_f32_16x16x32_bf16 v[32:35], v[176:179], a[208:211], v[32:35]\n"
    "v_exp_f32 v84, v84\n"
    "v_mfma_f32_16x16x32_bf16 v[32:35], v[172:175], a[204:207], v[32:35]\n"
    "s_waitcnt lgkmcnt(1)\n"
    "v_exp_f32 v85, v85\n"
    "v_mfma_f32_16x16x32_bf16 v[48:51], v[160:163], a[204:207], v[96:99]\n"
    "ds_read_b128 v[172:175], v200 offset:43008\n"
    "v_cvt_pk_bf16_f32 v146, v80, v81\n"
    "v_mfma_f32_16x16x32_bf16 v[48:51], v[164:167], a[208:211], v[48:51]\n"
    "s_waitcnt lgkmcnt(1)\n"
    "buffer_load_dwordx4 v200, s[40:43], s65 offen offset:1024 sc1 lds\n"
    "v_mfma_f32_16x16x32_bf16 v[48:51], v[168:171], a[212:215], v[48:51]\n"
    "v_exp_f32 v86, v86\n"
    "v_mfma_f32_16x16x32_bf16 v[52:55], v[168:171], a[236:239], v[100:103]\n"
    "ds_read_b128 v[176:179], v200 offset:44032\n"
    "v_cvt_pk_bf16_f32 v147, v82, v83\n"
    "v_mfma_f32_16x16x32_bf16 v[52:55], v[164:167], a[232:235], v[52:55]\n"
    "v_exp_f32 v87, v87\n"
    "v_mfma_f32_16x16x32_bf16 v[52:55], v[160:163], a[228:231], v[52:55]\n"
    "buffer_load_dwordx4 v200, s[40:43], s65 offen offset:2048 sc1 lds\n"
    "v_mfma_f32_16x16x32_bf16 v[56:59], v[160:163], a[252:255], v[104:107]\n"
    "ds_read_b128 v[180:183], v200 offset:45056\n"
    "v_cvt_pk_bf16_f32 v150, v84, v85\n"
    "v_mfma_f32_16x16x32_bf16 v[56:59], v[164:167], v[0:3], v[56:59]\n"
    "v_exp_f32 v88, v88\n"
    "v_mfma_f32_16x16x32_bf16 v[56:59], v[168:171], v[4:7], v[56:59]\n"
    "v_exp_f32 v89, v89\n"
    "v_mfma_f32_16x16x32_bf16 v[60:63], v[168:171], v[28:31], v[108:111]\n"
    "buffer_load_dwordx4 v200, s[40:43], s65 offen offset:3072 sc1 lds\n"
    "v_mfma_f32_16x16x32_bf16 v[60:63], v[164:167], v[24:27], v[60:63]\n"
    "v_exp_f32 v90, v90\n"
    "v_mfma_f32_16x16x32_bf16 v[60:63], v[160:163], v[20:23], v[60:63]\n"
    "s_waitcnt lgkmcnt(1)\n"
    "v_exp_f32 v91, v91\n"
    "v_mfma_f32_16x16x32_bf16 v[60:63], v[172:175], v[8:11], v[60:63]\n"
    "ds_read_b128 v[160:163], v201\n"
    "v_cvt_pk_bf16_f32 v151, v86, v87\n"
    "v_mfma_f32_16x16x32_bf16 v[60:63], v[176:179], v[12:15], v[60:63]\n"
    "s_waitcnt lgkmcnt(1)\n"
    "v_exp_f32 v92, v92\n"
    "v_mfma_f32_16x16x32_bf16 v[60:63], v[180:183], v[16:19], v[60:63]\n"
    "v_exp_f32 v93, v93\n"
    "v_mfma_f32_16x16x32_bf16 v[56:59], v[180:183], a[248:251], v[56:59]\n"
    "ds_read_b128 v[164:167], v201 offset:1024\n"
    "v_cvt_pk_bf16_f32 v154, v88, v89\n"
    "v_mfma_f32_16x16x32_bf16 v[56:59], v[176:179], a[244:247], v[56:59]\n"
    "v_exp_f32 v94, v94\n"
    "v_mfma_f32_16x16x32_bf16 v[56:59], v[172:175], a[240:243], v[56:59]\n"
    "v_cvt_pk_bf16_f32 v155, v90, v91\n"
    "v_cvt_pk_bf16_f32 v158, v92, v93\n"
    "v_mfma_f32_16x16x32_bf16 v[52:55], v[172:175], a[216:219], v[52:55]\n"
    "ds_read_b128 v[168:171], v201 offset:2048\n"
    "v_add_f32 v113, v113, v64\n"
    "v_mfma_f32_16x16x32_bf16 v[52:55], v[176:179], a[220:223], v[52:55]\n"
    "v_exp_f32 v95, v95\n"
    "v_mfma_f32_16x16x32_bf16 v[52:55], v[180:183], a[224:227], v[52:55]\n"
    "v_cvt_pk_bf16_f32 v159, v94, v95\n"
    "v_add_f32 v114, v114, v65\n"
    "v_mfma_f32_16x16x32_bf16 v[48:51], v[180:183], a[200:203], v[48:51]\n"
    "v_exp_f32 v32, v32\n"
    "v_mfma_f32_16x16x32_bf16 v[48:51], v[176:179], a[196:199], v[48:51]\n"
    "v_exp_f32 v33, v33\n"
    "v_mfma_f32_16x16x32_bf16 v[48:51], v[172:175], a[192:195], v[48:51]\n"
    "s_waitcnt lgkmcnt(1)\n"
    "v_exp_f32 v34, v34\n"
    "v_mfma_f32_16x16x32_bf16 a[0:3], v[160:163], v[128:131], a[0:3]\n"
    "ds_read_b128 v[172:175], v201 offset:3072\n"
    "ds_read_b128 v[176:179], v201 offset:4096\n"
    "v_mfma_f32_16x16x32_bf16 a[0:3], v[164:167], v[144:147], a[0:3]\n"
    "v_exp_f32 v35, v35\n"
    "v_mfma_f32_16x16x32_bf16 a[4:7], v[160:163], v[132:135], a[4:7]\n"
    "v_exp_f32 v36, v36\n"
    "v_mfma_f32_16x16x32_bf16 a[4:7], v[164:167], v[148:151], a[4:7]\n"
    "v_exp_f32 v37, v37\n"
    "v_mfma_f32_16x16x32_bf16 a[8:11], v[160:163], v[136:139], a[8:11]\n"
    "ds_read_b128 v[180:183], v201 offset:5120\n"
    "v_add_f32 v113, v113, v66\n"
    "v_mfma_f32_16x16x32_bf16 a[8:11], v[164:167], v[152:155], a[8:11]\n"
    "v_exp_f32 v38, v38\n"
    "v_mfma_f32_16x16x32_bf16 a[12:15], v[160:163], v[140:143], a[12:15]\n"
    "v_exp_f32 v39, v39\n"
    "v_mfma_f32_16x16x32_bf16 a[12:15], v[164:167], v[156:159], a[12:15]\n"
    "s_waitcnt lgkmcnt(2)\n"
    "v_exp_f32 v40, v40\n"
    "v_mfma_f32_16x16x32_bf16 a[28:31], v[168:171], v[140:143], a[28:31]\n"
    "ds_read_b128 v[160:163], v201 offset:6144\n"
    "v_add_f32 v114, v114, v67\n"
    "v_mfma_f32_16x16x32_bf16 a[28:31], v[172:175], v[156:159], a[28:31]\n"
    "v_exp_f32 v41, v41\n"
    "v_mfma_f32_16x16x32_bf16 a[24:27], v[172:175], v[152:155], a[24:27]\n"
    "v_exp_f32 v42, v42\n"
    "v_mfma_f32_16x16x32_bf16 a[24:27], v[168:171], v[136:139], a[24:27]\n"
    "v_exp_f32 v43, v43\n"
    "v_mfma_f32_16x16x32_bf16 a[20:23], v[168:171], v[132:135], a[20:23]\n"
    "ds_read_b128 v[164:167], v201 offset:7168\n"
    "v_add_f32 v117, v117, v68\n"
    "v_mfma_f32_16x16x32_bf16 a[20:23], v[172:175], v[148:151], a[20:23]\n"
    "v_exp_f32 v44, v44\n"
    "v_mfma_f32_16x16x32_bf16 a[16:19], v[172:175], v[144:147], a[16:19]\n"
    "v_exp_f32 v45, v45\n"
    "v_mfma_f32_16x16x32_bf16 a[16:19], v[168:171], v[128:131], a[16:19]\n"
    "s_waitcnt lgkmcnt(2)\n"
    "v_exp_f32 v46, v46\n"
    "v_mfma_f32_16x16x32_bf16 a[32:35], v[176:179], v[128:131], a[32:35]\n"
    "ds_read_b128 v[168:171], v201 offset:8192\n"
    "v_add_f32 v118, v118, v69\n"
    "v_mfma_f32_16x16x32_bf16 a[32:35], v[180:183], v[144:147], a[32:35]\n"
    "v_exp_f32 v47, v47\n"
    "v_mfma_f32_16x16x32_bf16 a[36:39], v[180:183], v[148:151], a[36:39]\n"
    "v_exp_f32 v48, v48\n"
    "v_mfma_f32_16x16x32_bf16 a[36:39], v[176:179], v[132:135], a[36:39]\n"
    "v_exp_f32 v49, v49\n"
    "v_mfma_f32_16x16x32_bf16 a[40:43], v[176:179], v[136:139], a[40:43]\n"
    "ds_read_b128 v[172:175], v201 offset:9216\n"
    "v_add_f32 v117, v117, v70\n"
    "v_mfma_f32_16x16x32_bf16 a[40:43], v[180:183], v[152:155], a[40:43]\n"
    "v_exp_f32 v50, v50\n"
    "v_mfma_f32_16x16x32_bf16 a[44:47], v[180:183], v[156:159], a[44:47]\n"
    "v_exp_f32 v51, v51\n"
    "v_mfma_f32_16x16x32_bf16 a[44:47], v[176:179], v[140:143], a[44:47]\n"
    "s_waitcnt lgkmcnt(2)\n"
    "v_add_f32 v113, v113, v32\n"
    "v_add_f32 v118, v118, v71\n"
    "v_mfma_f32_16x16x32_bf16 a[60:63], v[160:163], v[140:143], a[60:63]\n"
    "ds_read_b128 v[176:179], v201 offset:10240\n"
    "v_add_f32 v114, v114, v33\n"
    "v_mfma_f32_16x16x32_bf16 a[60:63], v[164:167], v[156:159], a[60:63]\n"
    "v_exp_f32 v52, v52\n"
    "v_mfma_f32_16x16x32_bf16 a[56:59], v[164:167], v[152:155], a[56:59]\n"
    "v_add_f32 v121, v121, v72\n"
    "v_add_f32 v113, v113, v34\n"
    "v_mfma_f32_16x16x32_bf16 a[56:59], v[160:163], v[136:139], a[56:59]\n"
    "v_exp_f32 v53, v53\n"
    "v_mfma_f32_16x16x32_bf16 a[52:55], v[160:163], v[132:135], a[52:55]\n"
    "ds_read_b128 v[180:183], v201 offset:11264\n"
    "v_add_f32 v122, v122, v73\n"
    "v_mfma_f32_16x16x32_bf16 a[52:55], v[164:167], v[148:151], a[52:55]\n"
    "v_add_f32 v114, v114, v35\n"
    "v_add_f32 v121, v121, v74\n"
    "v_mfma_f32_16x16x32_bf16 a[48:51], v[164:167], v[144:147], a[48:51]\n"
    "v_add_f32 v117, v117, v36\n"
    "v_add_f32 v122, v122, v75\n"
    "v_mfma_f32_16x16x32_bf16 a[48:51], v[160:163], v[128:131], a[48:51]\n"
    "s_waitcnt lgkmcnt(2)\n"
    "v_exp_f32 v54, v54\n"
    "v_mfma_f32_16x16x32_bf16 a[64:67], v[168:171], v[128:131], a[64:67]\n"
    "ds_read_b128 v[160:163], v201 offset:12288\n"
    "v_add_f32 v118, v118, v37\n"
    "v_mfma_f32_16x16x32_bf16 a[64:67], v[172:175], v[144:147], a[64:67]\n"
    "v_add_f32 v125, v125, v76\n"
    "v_add_f32 v117, v117, v38\n"
    "v_mfma_f32_16x16x32_bf16 a[68:71], v[172:175], v[148:151], a[68:71]\n"
    "v_exp_f32 v55, v55\n"
    "v_mfma_f32_16x16x32_bf16 a[68:71], v[168:171], v[132:135], a[68:71]\n"
    "s_add_u32 m0, s51, s95\n"
    "v_add_f32 v126, v126, v77\n"
    "v_add_f32 v118, v118, v39\n"
    "v_mfma_f32_16x16x32_bf16 a[72:75], v[168:171], v[136:139], a[72:75]\n"
    "ds_read_b128 v[164:167], v201 offset:13312\n"
    "v_add_f32 v125, v125, v78\n"
    "v_mfma_f32_16x16x32_bf16 a[72:75], v[172:175], v[152:155], a[72:75]\n"
    "buffer_load_dwordx4 v202, s[32:35], s64 offen lds\n"
    "v_mfma_f32_16x16x32_bf16 a[76:79], v[172:175], v[156:159], a[76:79]\n"
    "v_add_f32 v121, v121, v40\n"
    "v_add_f32 v126, v126, v79\n"
    "v_mfma_f32_16x16x32_bf16 a[76:79], v[168:171], v[140:143], a[76:79]\n"
    "s_waitcnt lgkmcnt(2)\n"
    "v_exp_f32 v56, v56\n"
    "v_mfma_f32_16x16x32_bf16 a[92:95], v[176:179], v[140:143], a[92:95]\n"
    "ds_read_b128 v[168:171], v201 offset:14336\n"
    "v_add_f32 v122, v122, v41\n"
    "v_mfma_f32_16x16x32_bf16 a[92:95], v[180:183], v[156:159], a[92:95]\n"
    "buffer_load_dwordx4 v203, s[32:35], s64 offen offset:1024 lds\n"
    "v_mfma_f32_16x16x32_bf16 a[88:91], v[180:183], v[152:155], a[88:91]\n"
    "v_add_f32 v113, v113, v80\n"
    "v_add_f32 v121, v121, v42\n"
    "v_mfma_f32_16x16x32_bf16 a[88:91], v[176:179], v[136:139], a[88:91]\n"
    "v_exp_f32 v57, v57\n"
    "v_mfma_f32_16x16x32_bf16 a[84:87], v[176:179], v[132:135], a[84:87]\n"
    "ds_read_b128 v[172:175], v201 offset:15360\n"
    "v_add_f32 v114, v114, v81\n"
    "v_mfma_f32_16x16x32_bf16 a[84:87], v[180:183], v[148:151], a[84:87]\n"
    "buffer_load_dwordx4 v204, s[32:35], s64 offen offset:2048 lds\n"
    "v_mfma_f32_16x16x32_bf16 a[80:83], v[180:183], v[144:147], a[80:83]\n"
    "v_add_f32 v122, v122, v43\n"
    "v_add_f32 v113, v113, v82\n"
    "v_mfma_f32_16x16x32_bf16 a[80:83], v[176:179], v[128:131], a[80:83]\n"
    "s_waitcnt lgkmcnt(2)\n"
    "v_add_f32 v125, v125, v44\n"
    "v_add_f32 v114, v114, v83\n"
    "v_mfma_f32_16x16x32_bf16 a[96:99], v[160:163], v[128:131], a[96:99]\n"
    "s_mov_b32 s95, s94\n"
    "ds_read_b128 v[176:179], v201 offset:16384\n"
    "v_add_f32 v126, v126, v45\n"
    "v_mfma_f32_16x16x32_bf16 a[96:99], v[164:167], v[144:147], a[96:99]\n"
    "s_sub_u32 s94, s94, 0x3000\n"
    "v_exp_f32 v58, v58\n"
    "v_mfma_f32_16x16x32_bf16 a[100:103], v[164:167], v[148:151], a[100:103]\n"
    "s_min_u32 s94, s94, 0x6000\n"
    "v_add_f32 v117, v117, v84\n"
    "v_add_f32 v125, v125, v46\n"
    "v_mfma_f32_16x16x32_bf16 a[100:103], v[160:163], v[132:135], a[100:103]\n"
    "s_sub_u32 s93, s94, s95\n"
    "v_exp_f32 v59, v59\n"
    "v_mfma_f32_16x16x32_bf16 a[104:107], v[160:163], v[136:139], a[104:107]\n"
    "ds_read_b128 v[180:183], v201 offset:17408\n"
    "v_add_f32 v118, v118, v85\n"
    "v_mfma_f32_16x16x32_bf16 a[104:107], v[164:167], v[152:155], a[104:107]\n"
    "v_add_f32 v126, v126, v47\n"
    "v_add_f32 v117, v117, v86\n"
    "v_mfma_f32_16x16x32_bf16 a[108:111], v[164:167], v[156:159], a[108:111]\n"
    "v_add_f32 v113, v113, v48\n"
    "v_add_u32 v205, s93, v205\n"
    "v_mfma_f32_16x16x32_bf16 a[108:111], v[160:163], v[140:143], a[108:111]\n"
    "s_waitcnt lgkmcnt(2)\n"
    "v_exp_f32 v60, v60\n"
    "v_mfma_f32_16x16x32_bf16 a[124:127], v[168:171], v[140:143], a[124:127]\n"
    "ds_read_b128 v[160:163], v201 offset:18432\n"
    "v_add_f32 v118, v118, v87\n"
    "v_mfma_f32_16x16x32_bf16 a[124:127], v[172:175], v[156:159], a[124:127]\n"
    "v_add_f32 v114, v114, v49\n"
    "v_add_u32 v209, 0x180, v209\n"
    "v_mfma_f32_16x16x32_bf16 a[120:123], v[172:175], v[152:155], a[120:123]\n"
    "v_add_u32 v206, s93, v206\n"
    "v_add_f32 v121, v121, v88\n"
    "v_mfma_f32_16x16x32_bf16 a[120:123], v[168:171], v[136:139], a[120:123]\n"
    "v_add_f32 v113, v113, v50\n"
    "v_add_u32 v210, 0x80, v210\n"
    "v_mfma_f32_16x16x32_bf16 a[116:119], v[168:171], v[132:135], a[116:119]\n"
    "ds_read_b128 v[164:167], v201 offset:19456\n"
    "v_add_u32 v207, s93, v207\n"
    "v_mfma_f32_16x16x32_bf16 a[116:119], v[172:175], v[148:151], a[116:119]\n"
    "v_exp_f32 v61, v61\n"
    "v_mfma_f32_16x16x32_bf16 a[112:115], v[172:175], v[144:147], a[112:115]\n"
    "v_add_f32 v122, v122, v89\n"
    "v_add_f32 v114, v114, v51\n"
    "v_mfma_f32_16x16x32_bf16 a[112:115], v[168:171], v[128:131], a[112:115]\n"
    "s_waitcnt lgkmcnt(2)\n"
    "v_add_f32 v121, v121, v90\n"
    "v_add_f32 v117, v117, v52\n"
    "v_mfma_f32_16x16x32_bf16 a[128:131], v[176:179], v[128:131], a[128:131]\n"
    "ds_read_b128 v[168:171], v201 offset:20480\n"
    "v_add_f32 v122, v122, v91\n"
    "v_mfma_f32_16x16x32_bf16 a[128:131], v[180:183], v[144:147], a[128:131]\n"
    "v_exp_f32 v62, v62\n"
    "v_mfma_f32_16x16x32_bf16 a[132:135], v[180:183], v[148:151], a[132:135]\n"
    "v_add_f32 v118, v118, v53\n"
    "v_add_f32 v125, v125, v92\n"
    "v_mfma_f32_16x16x32_bf16 a[132:135], v[176:179], v[132:135], a[132:135]\n"
    "s_add_u32 s64, s64, 0xc000\n"
    "v_add_f32 v117, v117, v54\n"
    "v_add_f32 v126, v126, v93\n"
    "v_mfma_f32_16x16x32_bf16 a[136:139], v[176:179], v[136:139], a[136:139]\n"
    "s_add_u32 s62, s62, 0x6000\n"
    "ds_read_b128 v[172:175], v201 offset:21504\n"
    "v_add_f32 v118, v118, v55\n"
    "v_mfma_f32_16x16x32_bf16 a[136:139], v[180:183], v[152:155], a[136:139]\n"
    "s_add_u32 s63, s63, 0x6000\n"
    "v_exp_f32 v63, v63\n"
    "v_mfma_f32_16x16x32_bf16 a[140:143], v[180:183], v[156:159], a[140:143]\n"
    "s_add_u32 s60, s60, 0x6000\n"
    "v_add_f32 v125, v125, v94\n"
    "v_add_f32 v121, v121, v56\n"
    "v_mfma_f32_16x16x32_bf16 a[140:143], v[176:179], v[140:143], a[140:143]\n"
    "s_waitcnt lgkmcnt(2)\n"
    "v_add_f32 v126, v126, v95\n"
    "v_add_f32 v122, v122, v57\n"
    "v_mfma_f32_16x16x32_bf16 a[156:159], v[160:163], v[140:143], a[156:159]\n"
    "s_add_u32 s61, s61, 0x6000\n"
    "ds_read_b128 v[176:179], v201 offset:22528\n"
    "v_mfma_f32_16x16x32_bf16 a[156:159], v[164:167], v[156:159], a[156:159]\n"
    "ds_read_b128 v[180:183], v201 offset:23552\n"
    "v_mfma_f32_16x16x32_bf16 a[152:155], v[164:167], v[152:155], a[152:155]\n"
    "s_waitcnt vmcnt(3)\n"
    "v_mfma_f32_16x16x32_bf16 a[152:155], v[160:163], v[136:139], a[152:155]\n"
    "v_mfma_f32_16x16x32_bf16 a[148:151], v[160:163], v[132:135], a[148:151]\n"
    "v_mfma_f32_16x16x32_bf16 a[148:151], v[164:167], v[148:151], a[148:151]\n"
    "v_mfma_f32_16x16x32_bf16 a[144:147], v[164:167], v[144:147], a[144:147]\n"
    "v_mfma_f32_16x16x32_bf16 a[144:147], v[160:163], v[128:131], a[144:147]\n"
    "s_waitcnt lgkmcnt(0)\n"
    "v_mfma_f32_16x16x32_bf16 a[160:163], v[168:171], v[128:131], a[160:163]\n"
    "s_barrier\n"
    "v_mfma_f32_16x16x32_bf16 a[160:163], v[172:175], v[144:147], a[160:163]\n"
    "ds_read_u16_d16_hi v223, v209 offset:6144\n"
    "ds_read_u16_d16_hi v222, v209\n"
    "v_mfma_f32_16x16x32_bf16 a[164:167], v[172:175], v[148:151], a[164:167]\n"
    "ds_read_b128 v[160:163], v200\n"
    "v_add_f32 v121, v121, v58\n"
    "v_mfma_f32_16x16x32_bf16 a[164:167], v[168:171], v[132:135], a[164:167]\n"
    "ds_read_b128 v[232:235], v205 offset:24576\n"
    "v_add_f32 v122, v122, v59\n"
    "v_mfma_f32_16x16x32_bf16 a[168:171], v[168:171], v[136:139], a[168:171]\n"
    "ds_read_b128 v[164:167], v200 offset:1024\n"
    "ds_read_b128 v[236:239], v206 offset:24576\n"
    "v_mfma_f32_16x16x32_bf16 a[168:171], v[172:175], v[152:155], a[168:171]\n"
    "v_add_f32 v125, v125, v60\n"
    "v_add_f32 v126, v126, v61\n"
    "v_mfma_f32_16x16x32_bf16 a[172:175], v[172:175], v[156:159], a[172:175]\n"
    "v_add_f32 v125, v125, v62\n"
    "v_add_f32 v126, v126, v63\n"
    "v_mfma_f32_16x16x32_bf16 a[172:175], v[168:171], v[140:143], a[172:175]\n"
    "ds_read_b64 v[228:229], v210\n"
    "ds_read2st64_b32 v[240:241], v207 offset0:96 offset1:99\n"
    "v_mfma_f32_16x16x32_bf16 a[188:191], v[176:179], v[140:143], a[188:191]\n"
    "ds_read_b128 v[168:171], v200 offset:2048\n"
    "ds_read2st64_b32 v[242:243], v207 offset0:102 offset1:105\n"
    "v_mfma_f32_16x16x32_bf16 a[188:191], v[180:183], v[156:159], a[188:191]\n"
    "ds_read2st64_b32 v[244:245], v207 offset0:108 offset1:111\n"
    "ds_read2st64_b32 v[246:247], v207 offset0:114 offset1:117\n"
    "v_mfma_f32_16x16x32_bf16 a[184:187], v[176:179], v[136:139], a[184:187]\n"
    "v_cvt_pk_bf16_f32 v140, v44, v45\n"
    "v_cvt_pk_bf16_f32 v141, v46, v47\n"
    "v_mfma_f32_16x16x32_bf16 a[184:187], v[180:183], v[152:155], a[184:187]\n"
    "s_waitcnt lgkmcnt(6)\n"
    "v_mul_f32 v226, s75, v223\n"
    "v_mfma_f32_16x16x32_bf16 a[180:183], v[176:179], v[132:135], a[180:183]\n"
    "ds_read_b128 v[172:175], v200 offset:3072\n"
    "v_fma_f32 v227, -v222, v226, v217\n"
    "v_mfma_f32_16x16x32_bf16 a[180:183], v[180:183], v[148:151], a[180:183]\n"
    "v_cvt_f32_i32 v232, v232\n"
    "v_cvt_f32_i32 v233, v233\n"
    "v_mfma_f32_16x16x32_bf16 a[176:179], v[176:179], v[128:131], a[176:179]\n"
    "v_cvt_f32_i32 v234, v234\n"
    "v_cvt_f32_i32 v235, v235\n"
    "v_mfma_f32_16x16x32_bf16 a[176:179], v[180:183], v[144:147], a[176:179]\n"
    "v_cvt_f32_i32 v236, v236\n"
    "v_cvt_f32_i32 v237, v237\n"
    "LM01:\n"
    "v_mfma_f32_16x16x32_bf16 v[64:67], v[160:163], a[192:195], v[96:99]\n"
    "ds_read_b128 v[176:179], v200 offset:4096\n"
    "v_cvt_f32_i32 v238, v238\n"
    "v_mfma_f32_16x16x32_bf16 v[64:67], v[164:167], a[196:199], v[64:67]\n"
    "s_waitcnt lgkmcnt(1)\n"
    "v_cvt_f32_i32 v239, v239\n"
    "v_mul_f32_dpp v232, v226, v232 row_newbcast:0 row_mask:0xf bank_mask:0xf\n"
    "v_mfma_f32_16x16x32_bf16 v[64:67], v[168:171], a[200:203], v[64:67]\n"
    "v_mul_f32_dpp v233, v226, v233 row_newbcast:1 row_mask:0xf bank_mask:0xf\n"
    "v_mul_f32_dpp v234, v226, v234 row_newbcast:2 row_mask:0xf bank_mask:0xf\n"
    "v_mfma_f32_16x16x32_bf16 v[68:71], v[168:171], a[224:227], v[100:103]\n"
    "v_mul_f32_dpp v235, v226, v235 row_newbcast:3 row_mask:0xf bank_mask:0xf\n"
    "v_mul_f32_dpp v236, v226, v236 row_newbcast:4 row_mask:0xf bank_mask:0xf\n"
    "v_mfma_f32_16x16x32_bf16 v[68:71], v[164:167], a[220:223], v[68:71]\n"
    "ds_read_b128 v[180:183], v200 offset:5120\n"
    "v_cvt_f32_i32 v240, v240\n"
    "v_mfma_f32_16x16x32_bf16 v[68:71], v[160:163], a[216:219], v[68:71]\n"
    "v_mul_f32_dpp v237, v226, v237 row_newbcast:5 row_mask:0xf bank_mask:0xf\n"
    "v_cvt_f32_i32 v241, v241\n"
    "v_mfma_f32_16x16x32_bf16 v[72:75], v[160:163], a[240:243], v[104:107]\n"
    "v_mul_f32_dpp v238, v226, v238 row_newbcast:6 row_mask:0xf bank_mask:0xf\n"
    "v_cvt_f32_i32 v242, v242\n"
    "v_mfma_f32_16x16x32_bf16 v[72:75], v[164:167], a[244:247], v[72:75]\n"
    "v_mul_f32_dpp v239, v226, v239 row_newbcast:7 row_mask:0xf bank_mask:0xf\n"
    "v_cvt_f32_i32 v243, v243\n"
    "v_mfma_f32_16x16x32_bf16 v[72:75], v[168:171], a[248:251], v[72:75]\n"
    "v_add_f32_dpp v232, v227, v232 row_newbcast:0 row_mask:0xf bank_mask:0xf\n"
    "v_cvt_f32_i32 v244, v244\n"
    "v_mfma_f32_16x16x32_bf16 v[76:79], v[168:171], v[16:19], v[108:111]\n"
    "v_add_f32_dpp v233, v227, v233 row_newbcast:1 row_mask:0xf bank_mask:0xf\n"
    "v_cvt_f32_i32 v245, v245\n"
    "v_mfma_f32_16x16x32_bf16 v[76:79], v[164:167], v[12:15], v[76:79]\n"
    "v_add_f32_dpp v234, v227, v234 row_newbcast:2 row_mask:0xf bank_mask:0xf\n"
    "v_cvt_f32_i32 v246, v246\n"
    "v_mfma_f32_16x16x32_bf16 v[76:79], v[160:163], v[8:11], v[76:79]\n"
    "s_waitcnt lgkmcnt(1)\n"
    "v_add_f32_dpp v235, v227, v235 row_newbcast:3 row_mask:0xf bank_mask:0xf\n"
    "v_cvt_f32_i32 v247, v247\n"
    "v_mfma_f32_16x16x32_bf16 v[76:79], v[172:175], v[20:23], v[76:79]\n"
    "ds_read_b128 v[160:163], v200 offset:9216\n"
    "v_add_f32_dpp v236, v227, v236 row_newbcast:4 row_mask:0xf bank_mask:0xf\n"
    "v_mfma_f32_16x16x32_bf16 v[76:79], v[176:179], v[24:27], v[76:79]\n"
    "s_waitcnt lgkmcnt(1)\n"
    "v_mul_f32_dpp v240, v228, v240 row_newbcast:0 row_mask:0xf bank_mask:0xf\n"
    "v_add_f32_dpp v237, v227, v237 row_newbcast:5 row_mask:0xf bank_mask:0xf\n"
    "v_mfma_f32_16x16x32_bf16 v[76:79], v[180:183], v[28:31], v[76:79]\n"
    "v_mul_f32_dpp v241, v228, v241 row_newbcast:1 row_mask:0xf bank_mask:0xf\n"
    "v_add_f32_dpp v238, v227, v238 row_newbcast:6 row_mask:0xf bank_mask:0xf\n"
    "v_mfma_f32_16x16x32_bf16 v[72:75], v[180:183], v[4:7], v[72:75]\n"
    "ds_read_b128 v[164:167], v200 offset:10240\n"
    "v_mul_f32_dpp v242, v228, v242 row_newbcast:2 row_mask:0xf bank_mask:0xf\n"
    "v_mfma_f32_16x16x32_bf16 v[72:75], v[176:179], v[0:3], v[72:75]\n"
    "v_add_f32_dpp v239, v227, v239 row_newbcast:7 row_mask:0xf bank_mask:0xf\n"
    "v_mul_f32_dpp v243, v228, v243 row_newbcast:3 row_mask:0xf bank_mask:0xf\n"
    "v_mfma_f32_16x16x32_bf16 v[72:75], v[172:175], a[252:255], v[72:75]\n"
    "v_cvt_pk_bf16_f32 v232, v232, v233\n"
    "v_mul_f32_dpp v244, v228, v244 row_newbcast:4 row_mask:0xf bank_mask:0xf\n"
    "v_mfma_f32_16x16x32_bf16 v[68:71], v[172:175], a[228:231], v[68:71]\n"
    "ds_read_b128 v[168:171], v200 offset:11264\n"
    "v_cvt_pk_bf16_f32 v233, v234, v235\n"
    "v_mfma_f32_16x16x32_bf16 v[68:71], v[176:179], a[232:235], v[68:71]\n"
    "v_mul_f32_dpp v245, v228, v245 row_newbcast:5 row_mask:0xf bank_mask:0xf\n"
    "v_cvt_pk_bf16_f32 v234, v236, v237\n"
    "v_mfma_f32_16x16x32_bf16 v[68:71], v[180:183], a[236:239], v[68:71]\n"
    "v_mul_f32_dpp v246, v228, v246 row_newbcast:6 row_mask:0xf bank_mask:0xf\n"
    "v_cvt_pk_bf16_f32 v235, v238, v239\n"
    "v_mfma_f32_16x16x32_bf16 v[64:67], v[180:183], a[212:215], v[64:67]\n"
    "v_mul_f32_dpp v247, v228, v247 row_newbcast:7 row_mask:0xf bank_mask:0xf\n"
    "v_add_f32_dpp v240, v229, v240 row_newbcast:0 row_mask:0xf bank_mask:0xf\n"
    "v_mfma_f32_16x16x32_bf16 v[64:67], v[176:179], a[208:211], v[64:67]\n"
    "v_add_f32_dpp v241, v229, v241 row_newbcast:1 row_mask:0xf bank_mask:0xf\n"
    "v_add_f32_dpp v242, v229, v242 row_newbcast:2 row_mask:0xf bank_mask:0xf\n"
    "v_mfma_f32_16x16x32_bf16 v[64:67], v[172:175], a[204:207], v[64:67]\n"
    "s_waitcnt lgkmcnt(1)\n"
    "buffer_store_dwordx4 v[232:235], v211, s[36:39], s62 offen sc1\n"
    "v_add_f32_dpp v243, v229, v243 row_newbcast:3 row_mask:0xf bank_mask:0xf\n"
    "v_add_f32_dpp v244, v229, v244 row_newbcast:4 row_mask:0xf bank_mask:0xf\n"
    "v_add_f32_dpp v245, v229, v245 row_newbcast:5 row_mask:0xf bank_mask:0xf\n"
    "v_mfma_f32_16x16x32_bf16 v[80:83], v[160:163], a[204:207], v[96:99]\n"
    "ds_read_b128 v[172:175], v200 offset:6144\n"
    "v_add_f32_dpp v246, v229, v246 row_newbcast:6 row_mask:0xf bank_mask:0xf\n"
    "v_mfma_f32_16x16x32_bf16 v[80:83], v[164:167], a[208:211], v[80:83]\n"
    "s_waitcnt lgkmcnt(1)\n"
    "v_add_f32_dpp v247, v229, v247 row_newbcast:7 row_mask:0xf bank_mask:0xf\n"
    "v_cvt_pk_bf16_f32 v240, v240, v241\n"
    "v_mfma_f32_16x16x32_bf16 v[80:83], v[168:171], a[212:215], v[80:83]\n"
    "v_cvt_pk_bf16_f32 v241, v242, v243\n"
    "v_cvt_pk_bf16_f32 v242, v244, v245\n"
    "v_mfma_f32_16x16x32_bf16 v[84:87], v[168:171], a[236:239], v[100:103]\n"
    "ds_read_b128 v[176:179], v200 offset:7168\n"
    "v_cvt_pk_bf16_f32 v243, v246, v247\n"
    "v_mfma_f32_16x16x32_bf16 v[84:87], v[164:167], a[232:235], v[84:87]\n"
    "buffer_store_dwordx4 v[240:243], v212, s[40:43], s63 offen sc1\n"
    "v_cvt_pk_bf16_f32 v136, v40, v41\n"
    "v_cvt_pk_bf16_f32 v137, v42, v43\n"
    "v_cvt_pk_bf16_f32 v132, v36, v37\n"
    "v_mfma_f32_16x16x32_bf16 v[84:87], v[160:163], a[228:231], v[84:87]\n"
    "v_cvt_pk_bf16_f32 v133, v38, v39\n"
    "v_cvt_pk_bf16_f32 v128, v32, v33\n"
    "v_mfma_f32_16x16x32_bf16 v[88:91], v[160:163], a[252:255], v[104:107]\n"
    "ds_read_b128 v[180:183], v200 offset:8192\n"
    "v_cvt_pk_bf16_f32 v129, v34, v35\n"
    "v_mfma_f32_16x16x32_bf16 v[88:91], v[164:167], v[0:3], v[88:91]\n"
    "v_exp_f32 v64, v64\n"
    "v_mfma_f32_16x16x32_bf16 v[88:91], v[168:171], v[4:7], v[88:91]\n"
    "v_exp_f32 v65, v65\n"
    "v_mfma_f32_16x16x32_bf16 v[92:95], v[168:171], v[28:31], v[108:111]\n"
    "v_exp_f32 v66, v66\n"
    "v_mfma_f32_16x16x32_bf16 v[92:95], v[164:167], v[24:27], v[92:95]\n"
    "v_exp_f32 v67, v67\n"
    "v_mfma_f32_16x16x32_bf16 v[92:95], v[160:163], v[20:23], v[92:95]\n"
    "s_waitcnt lgkmcnt(1)\n"
    "v_exp_f32 v68, v68\n"
    "v_mfma_f32_16x16x32_bf16 v[92:95], v[172:175], v[8:11], v[92:95]\n"
    "ds_read_b128 v[160:163], v200 offset:12288\n"
    "v_cvt_pk_bf16_f32 v142, v60, v61\n"
    "v_mfma_f32_16x16x32_bf16 v[92:95], v[176:179], v[12:15], v[92:95]\n"
    "s_waitcnt lgkmcnt(1)\n"
    "v_exp_f32 v69, v69\n"
    "v_mfma_f32_16x16x32_bf16 v[92:95], v[180:183], v[16:19], v[92:95]\n"
    "v_exp_f32 v70, v70\n"
    "v_mfma_f32_16x16x32_bf16 v[88:91], v[180:183], a[248:251], v[88:91]\n"
    "ds_read_b128 v[164:167], v200 offset:13312\n"
    "v_cvt_pk_bf16_f32 v143, v62, v63\n"
    "v_mfma_f32_16x16x32_bf16 v[88:91], v[176:179], a[244:247], v[88:91]\n"
    "v_exp_f32 v71, v71\n"
    "v_mfma_f32_16x16x32_bf16 v[88:91], v[172:175], a[240:243], v[88:91]\n"
    "v_cvt_pk_bf16_f32 v138, v56, v57\n"
    "v_cvt_pk_bf16_f32 v139, v58, v59\n"
    "v_mfma_f32_16x16x32_bf16 v[84:87], v[172:175], a[216:219], v[84:87]\n"
    "ds_read_b128 v[168:171], v200 offset:14336\n"
    "v_cvt_pk_bf16_f32 v134, v52, v53\n"
    "v_mfma_f32_16x16x32_bf16 v[84:87], v[176:179], a[220:223], v[84:87]\n"
    "v_exp_f32 v72, v72\n"
    "v_mfma_f32_16x16x32_bf16 v[84:87], v[180:183], a[224:227], v[84:87]\n"
    "v_cvt_pk_bf16_f32 v135, v54, v55\n"
    "v_cvt_pk_bf16_f32 v130, v48, v49\n"
    "v_mfma_f32_16x16x32_bf16 v[80:83], v[180:183], a[200:203], v[80:83]\n"
    "v_exp_f32 v73, v73\n"
    "v_mfma_f32_16x16x32_bf16 v[80:83], v[176:179], a[196:199], v[80:83]\n"
    "v_cvt_pk_bf16_f32 v131, v50, v51\n"
    "v_cvt_pk_bf16_f32 v144, v64, v65\n"
    "v_mfma_f32_16x16x32_bf16 v[80:83], v[172:175], a[192:195], v[80:83]\n"
    "s_waitcnt lgkmcnt(1)\n"
    "v_exp_f32 v74, v74\n"
    "v_mfma_f32_16x16x32_bf16 v[32:35], v[160:163], a[192:195], v[96:99]\n"
    "ds_read_b128 v[172:175], v200 offset:15360\n"
    "v_cvt_pk_bf16_f32 v145, v66, v67\n"
    "v_mfma_f32_16x16x32_bf16 v[32:35], v[164:167], a[196:199], v[32:35]\n"
    "s_waitcnt lgkmcnt(1)\n"
    "s_add_u32 s65, s60, s92\n"
    "v_cvt_pk_bf16_f32 v148, v68, v69\n"
    "v_mfma_f32_16x16x32_bf16 v[32:35], v[168:171], a[200:203], v[32:35]\n"
    "s_add_u32 m0, s90, 0x6000\n"
    "v_exp_f32 v75, v75\n"
    "v_mfma_f32_16x16x32_bf16 v[36:39], v[168:171], a[224:227], v[100:103]\n"
    "ds_read_b128 v[176:179], v200 offset:16384\n"
    "v_cvt_pk_bf16_f32 v149, v70, v71\n"
    "v_mfma_f32_16x16x32_bf16 v[36:39], v[164:167], a[220:223], v[36:39]\n"
    "buffer_load_dwordx4 v200, s[36:39], s65 offen sc1 lds\n"
    "v_mfma_f32_16x16x32_bf16 v[36:39], v[160:163], a[216:219], v[36:39]\n"
    "v_exp_f32 v76, v76\n"
    "v_mfma_f32_16x16x32_bf16 v[40:43], v[160:163], a[240:243], v[104:107]\n"
    "ds_read_b128 v[180:183], v200 offset:17408\n"
    "v_cvt_pk_bf16_f32 v152, v72, v73\n"
    "v_mfma_f32_16x16x32_bf16 v[40:43], v[164:167], a[244:247], v[40:43]\n"
    "v_exp_f32 v77, v77\n"
    "v_mfma_f32_16x16x32_bf16 v[40:43], v[168:171], a[248:251], v[40:43]\n"
    "buffer_load_dwordx4 v200, s[36:39], s65 offen offset:1024 sc1 lds\n"
    "v_mfma_f32_16x16x32_bf16 v[44:47], v[168:171], v[16:19], v[108:111]\n"
    "v_exp_f32 v78, v78\n"
    "v_mfma_f32_16x16x32_bf16 v[44:47], v[164:167], v[12:15], v[44:47]\n"
    "v_exp_f32 v79, v79\n"
    "v_mfma_f32_16x16x32_bf16 v[44:47], v[160:163], v[8:11], v[44:47]\n"
    "s_waitcnt lgkmcnt(1)\n"
    "v_exp_f32 v80, v80\n"
    "v_mfma_f32_16x16x32_bf16 v[44:47], v[172:175], v[20:23], v[44:47]\n"
    "ds_read_b128 v[160:163], v200 offset:21504\n"
    "v_cvt_pk_bf16_f32 v153, v74, v75\n"
    "v_mfma_f32_16x16x32_bf16 v[44:47], v[176:179], v[24:27], v[44:47]\n"
    "s_waitcnt lgkmcnt(1)\n"
    "buffer_load_dwordx4 v200, s[36:39], s65 offen offset:2048 sc1 lds\n"
    "v_mfma_f32_16x16x32_bf16 v[44:47], v[180:183], v[28:31], v[44:47]\n"
    "v_exp_f32 v81, v81\n"
    "v_mfma_f32_16x16x32_bf16 v[40:43], v[180:183], v[4:7], v[40:43]\n"
    "ds_read_b128 v[164:167], v200 offset:22528\n"
    "v_cvt_pk_bf16_f32 v156, v76, v77\n"
    "v_mfma_f32_16x16x32_bf16 v[40:43], v[176:179], v[0:3], v[40:43]\n"
    "v_exp_f32 v82, v82\n"
    "v_mfma_f32_16x16x32_bf16 v[40:43], v[172:175], a[252:255], v[40:43]\n"
    "buffer_load_dwordx4 v200, s[36:39], s65 offen offset:3072 sc1 lds\n"
    "v_mfma_f32_16x16x32_bf16 v[36:39], v[172:175], a[228:231], v[36:39]\n"
    "s_add_u32 s65, s61, s92\n"
    "ds_read_b128 v[168:171], v200 offset:23552\n"
    "v_cvt_pk_bf16_f32 v157, v78, v79\n"
    "v_mfma_f32_16x16x32_bf16 v[36:39], v[176:179], a[232:235], v[36:39]\n"
    "s_add_u32 m0, s90, 0xc000\n"
    "v_exp_f32 v83, v83\n"
    "v_mfma_f32_16x16x32_bf16 v[36:39], v[180:183], a[236:239], v[36:39]\n"
    "v_exp_f32 v84, v84\n"
    "v_mfma_f32_16x16x32_bf16 v[32:35], v[180:183], a[212:215], v[32:35]\n"
    "buffer_load_dwordx4 v200, s[40:43], s65 offen sc1 lds\n"
    "v_mfma_f32_16x16x32_bf16 v[32:35], v[176:179], a[208:211], v[32:35]\n"
    "v_exp_f32 v85, v85\n"
    "v_mfma_f32_16x16x32_bf16 v[32:35], v[172:175], a[204:207], v[32:35]\n"
    "s_waitcnt lgkmcnt(1)\n"
    "v_exp_f32 v86, v86\n"
    "v_mfma_f32_16x16x32_bf16 v[48:51], v[160:163], a[204:207], v[96:99]\n"
    "ds_read_b128 v[172:175], v200 offset:18432\n"
    "v_cvt_pk_bf16_f32 v146, v80, v81\n"
    "v_mfma_f32_16x16x32_bf16 v[48:51], v[164:167], a[208:211], v[48:51]\n"
    "s_waitcnt lgkmcnt(1)\n"
    "buffer_load_dwordx4 v200, s[40:43], s65 offen offset:1024 sc1 lds\n"
    "v_mfma_f32_16x16x32_bf16 v[48:51], v[168:171], a[212:215], v[48:51]\n"
    "v_exp_f32 v87, v87\n"
    "v_mfma_f32_16x16x32_bf16 v[52:55], v[168:171], a[236:239], v[100:103]\n"
    "ds_read_b128 v[176:179], v200 offset:19456\n"
    "v_cvt_pk_bf16_f32 v147, v82, v83\n"
    "v_mfma_f32_16x16x32_bf16 v[52:55], v[164:167], a[232:235], v[52:55]\n"
    "v_exp_f32 v88, v88\n"
    "v_mfma_f32_16x16x32_bf16 v[52:55], v[160:163], a[228:231], v[52:55]\n"
    "buffer_load_dwordx4 v200, s[40:43], s65 offen offset:2048 sc1 lds\n"
    "v_mfma_f32_16x16x32_bf16 v[56:59], v[160:163], a[252:255], v[104:107]\n"
    "ds_read_b128 v[180:183], v200 offset:20480\n"
    "v_cvt_pk_bf16_f32 v150, v84, v85\n"
    "v_mfma_f32_16x16x32_bf16 v[56:59], v[164:167], v[0:3], v[56:59]\n"
    "v_exp_f32 v89, v89\n"
    "v_mfma_f32_16x16x32_bf16 v[56:59], v[168:171], v[4:7], v[56:59]\n"
    "v_exp_f32 v90, v90\n"
    "v_mfma_f32_16x16x32_bf16 v[60:63], v[168:171], v[28:31], v[108:111]\n"
    "buffer_load_dwordx4 v200, s[40:43], s65 offen offset:3072 sc1 lds\n"
    "v_mfma_f32_16x16x32_bf16 v[60:63], v[164:167], v[24:27], v[60:63]\n"
    "v_exp_f32 v91, v91\n"
    "v_mfma_f32_16x16x32_bf16 v[60:63], v[160:163], v[20:23], v[60:63]\n"
    "s_waitcnt lgkmcnt(1)\n"
    "v_exp_f32 v92, v92\n"
    "v_mfma_f32_16x16x32_bf16 v[60:63], v[172:175], v[8:11], v[60:63]\n"
    "ds_read_b128 v[160:163], v201 offset:24576\n"
    "v_cvt_pk_bf16_f32 v151, v86, v87\n"
    "v_mfma_f32_16x16x32_bf16 v[60:63], v[176:179], v[12:15], v[60:63]\n"
    "s_waitcnt lgkmcnt(1)\n"
    "v_exp_f32 v93, v93\n"
    "v_mfma_f32_16x16x32_bf16 v[60:63], v[180:183], v[16:19], v[60:63]\n"
    "v_exp_f32 v94, v94\n"
    "v_mfma_f32_16x16x32_bf16 v[56:59], v[180:183], a[248:251], v[56:59]\n"
    "ds_read_b128 v[164:167], v201 offset:25600\n"
    "v_cvt_pk_bf16_f32 v154, v88, v89\n"
    "v_mfma_f32_16x16x32_bf16 v[56:59], v[176:179], a[244:247], v[56:59]\n"
    "v_cvt_pk_bf16_f32 v155, v90, v91\n"
    "v_cvt_pk_bf16_f32 v158, v92, v93\n"
    "v_mfma_f32_16x16x32_bf16 v[56:59], v[172:175], a[240:243], v[56:59]\n"
    "v_exp_f32 v95, v95\n"
    "v_mfma_f32_16x16x32_bf16 v[52:55], v[172:175], a[216:219], v[52:55]\n"
    "ds_read_b128 v[168:171], v201 offset:26624\n"
    "v_cvt_pk_bf16_f32 v159, v94, v95\n"
    "v_mfma_f32_16x16x32_bf16 v[52:55], v[176:179], a[220:223], v[52:55]\n"
    "v_exp_f32 v32, v32\n"
    "v_mfma_f32_16x16x32_bf16 v[52:55], v[180:183], a[224:227], v[52:55]\n"
    "v_exp_f32 v33, v33\n"
    "v_mfma_f32_16x16x32_bf16 v[48:51], v[180:183], a[200:203], v[48:51]\n"
    "v_exp_f32 v34, v34\n"
    "v_mfma_f32_16x16x32_bf16 v[48:51], v[176:179], a[196:199], v[48:51]\n"
    "v_exp_f32 v35, v35\n"
    "v_mfma_f32_16x16x32_bf16 v[48:51], v[172:175], a[192:195], v[48:51]\n"
    "s_waitcnt lgkmcnt(1)\n"
    "v_exp_f32 v36, v36\n"
    "v_mfma_f32_16x16x32_bf16 a[0:3], v[160:163], v[128:131], a[0:3]\n"
    "ds_read_b128 v[172:175], v201 offset:27648\n"
    "ds_read_b128 v[176:179], v201 offset:28672\n"
    "v_mfma_f32_16x16x32_bf16 a[0:3], v[164:167], v[144:147], a[0:3]\n"
    "v_exp_f32 v37, v37\n"
    "v_mfma_f32_16x16x32_bf16 a[4:7], v[160:163], v[132:135], a[4:7]\n"
    "v_exp_f32 v38, v38\n"
    "v_mfma_f32_16x16x32_bf16 a[4:7], v[164:167], v[148:151], a[4:7]\n"
    "v_exp_f32 v39, v39\n"
    "v_mfma_f32_16x16x32_bf16 a[8:11], v[160:163], v[136:139], a[8:11]\n"
    "ds_read_b128 v[180:183], v201 offset:29696\n"
    "v_add_f32 v113, v113, v64\n"
    "v_mfma_f32_16x16x32_bf16 a[8:11], v[164:167], v[152:155], a[8:11]\n"
    "v_exp_f32 v40, v40\n"
    "v_mfma_f32_16x16x32_bf16 a[12:15], v[160:163], v[140:143], a[12:15]\n"
    "v_exp_f32 v41, v41\n"
    "v_mfma_f32_16x16x32_bf16 a[12:15], v[164:167], v[156:159], a[12:15]\n"
    "s_waitcnt lgkmcnt(2)\n"
    "v_exp_f32 v42, v42\n"
    "v_mfma_f32_16x16x32_bf16 a[28:31], v[168:171], v[140:143], a[28:31]\n"
    "ds_read_b128 v[160:163], v201 offset:30720\n"
    "v_add_f32 v114, v114, v65\n"
    "v_mfma_f32_16x16x32_bf16 a[28:31], v[172:175], v[156:159], a[28:31]\n"
    "v_exp_f32 v43, v43\n"
    "v_mfma_f32_16x16x32_bf16 a[24:27], v[172:175], v[152:155], a[24:27]\n"
    "v_exp_f32 v44, v44\n"
    "v_mfma_f32_16x16x32_bf16 a[24:27], v[168:171], v[136:139], a[24:27]\n"
    "v_exp_f32 v45, v45\n"
    "v_mfma_f32_16x16x32_bf16 a[20:23], v[168:171], v[132:135], a[20:23]\n"
    "ds_read_b128 v[164:167], v201 offset:31744\n"
    "v_add_f32 v113, v113, v66\n"
    "v_mfma_f32_16x16x32_bf16 a[20:23], v[172:175], v[148:151], a[20:23]\n"
    "v_exp_f32 v46, v46\n"
    "v_mfma_f32_16x16x32_bf16 a[16:19], v[172:175], v[144:147], a[16:19]\n"
    "v_exp_f32 v47, v47\n"
    "v_mfma_f32_16x16x32_bf16 a[16:19], v[168:171], v[128:131], a[16:19]\n"
    "s_waitcnt lgkmcnt(2)\n"
    "v_exp_f32 v48, v48\n"
    "v_mfma_f32_16x16x32_bf16 a[32:35], v[176:179], v[128:131], a[32:35]\n"
    "ds_read_b128 v[168:171], v201 offset:32768\n"
    "v_add_f32 v114, v114, v67\n"
    "v_mfma_f32_16x16x32_bf16 a[32:35], v[180:183], v[144:147], a[32:35]\n"
    "v_exp_f32 v49, v49\n"
    "v_mfma_f32_16x16x32_bf16 a[36:39], v[180:183], v[148:151], a[36:39]\n"
    "v_exp_f32 v50, v50\n"
    "v_mfma_f32_16x16x32_bf16 a[36:39], v[176:179], v[132:135], a[36:39]\n"
    "v_add_f32 v117, v117, v68\n"
    "v_add_f32 v118, v118, v69\n"
    "v_mfma_f32_16x16x32_bf16 a[40:43], v[176:179], v[136:139], a[40:43]\n"
    "ds_read_b128 v[172:175], v201 offset:33792\n"
    "v_add_f32 v117, v117, v70\n"
    "v_mfma_f32_16x16x32_bf16 a[40:43], v[180:183], v[152:155], a[40:43]\n"
    "v_exp_f32 v51, v51\n"
    "v_mfma_f32_16x16x32_bf16 a[44:47], v[180:183], v[156:159], a[44:47]\n"
    "v_add_f32 v113, v113, v32\n"
    "v_add_f32 v118, v118, v71\n"
    "v_mfma_f32_16x16x32_bf16 a[44:47], v[176:179], v[140:143], a[44:47]\n"
    "s_waitcnt lgkmcnt(2)\n"
    "v_exp_f32 v52, v52\n"
    "v_mfma_f32_16x16x32_bf16 a[60:63], v[160:163], v[140:143], a[60:63]\n"
    "ds_read_b128 v[176:179], v201 offset:34816\n"
    "v_add_f32 v114, v114, v33\n"
    "v_mfma_f32_16x16x32_bf16 a[60:63], v[164:167], v[156:159], a[60:63]\n"
    "v_add_f32 v121, v121, v72\n"
    "v_add_f32 v113, v113, v34\n"
    "v_mfma_f32_16x16x32_bf16 a[56:59], v[164:167], v[152:155], a[56:59]\n"
    "v_exp_f32 v53, v53\n"
    "v_mfma_f32_16x16x32_bf16 a[56:59], v[160:163], v[136:139], a[56:59]\n"
    "v_add_f32 v122, v122, v73\n"
    "v_add_f32 v114, v114, v35\n"
    "v_mfma_f32_16x16x32_bf16 a[52:55], v[160:163], v[132:135], a[52:55]\n"
    "ds_read_b128 v[180:183], v201 offset:35840\n"
    "v_add_f32 v121, v121, v74\n"
    "v_mfma_f32_16x16x32_bf16 a[52:55], v[164:167], v[148:151], a[52:55]\n"
    "v_add_f32 v117, v117, v36\n"
    "v_add_f32 v122, v122, v75\n"
    "v_mfma_f32_16x16x32_bf16 a[48:51], v[164:167], v[144:147], a[48:51]\n"
    "v_exp_f32 v54, v54\n"
    "v_mfma_f32_16x16x32_bf16 a[48:51], v[160:163], v[128:131], a[48:51]\n"
    "s_waitcnt lgkmcnt(2)\n"
    "v_add_f32 v118, v118, v37\n"
    "v_add_f32 v125, v125, v76\n"
    "v_mfma_f32_16x16x32_bf16 a[64:67], v[168:171], v[128:131], a[64:67]\n"
    "ds_read_b128 v[160:163], v201 offset:36864\n"
    "v_add_f32 v117, v117, v38\n"
    "v_mfma_f32_16x16x32_bf16 a[64:67], v[172:175], v[144:147], a[64:67]\n"
    "v_exp_f32 v55, v55\n"
    "v_mfma_f32_16x16x32_bf16 a[68:71], v[172:175], v[148:151], a[68:71]\n"
    "v_add_f32 v126, v126, v77\n"
    "v_add_f32 v118, v118, v39\n"
    "v_mfma_f32_16x16x32_bf16 a[68:71], v[168:171], v[132:135], a[68:71]\n"
    "s_add_u32 m0, s51, s95\n"
    "v_add_f32 v125, v125, v78\n"
    "v_add_f32 v121, v121, v40\n"
    "v_mfma_f32_16x16x32_bf16 a[72:75], v[168:171], v[136:139], a[72:75]\n"
    "ds_read_b128 v[164:167], v201 offset:37888\n"
    "v_add_f32 v126, v126, v79\n"
    "v_mfma_f32_16x16x32_bf16 a[72:75], v[172:175], v[152:155], a[72:75]\n"
    "buffer_load_dwordx4 v202, s[32:35], s64 offen lds\n"
    "v_mfma_f32_16x16x32_bf16 a[76:79], v[172:175], v[156:159], a[76:79]\n"
    "v_exp_f32 v56, v56\n"
    "v_mfma_f32_16x16x32_bf16 a[76:79], v[168:171], v[140:143], a[76:79]\n"
    "s_waitcnt lgkmcnt(2)\n"
    "v_add_f32 v122, v122, v41\n"
    "v_add_f32 v113, v113, v80\n"
    "v_mfma_f32_16x16x32_bf16 a[92:95], v[176:179], v[140:143], a[92:95]\n"
    "ds_read_b128 v[168:171], v201 offset:38912\n"
    "v_add_f32 v121, v121, v42\n"
    "v_mfma_f32_16x16x32_bf16 a[92:95], v[180:183], v[156:159], a[92:95]\n"
    "buffer_load_dwordx4 v203, s[32:35], s64 offen offset:1024 lds\n"
    "v_mfma_f32_16x16x32_bf16 a[88:91], v[180:183], v[152:155], a[88:91]\n"
    "v_exp_f32 v57, v57\n"
    "v_mfma_f32_16x16x32_bf16 a[88:91], v[176:179], v[136:139], a[88:91]\n"
    "v_add_f32 v114, v114, v81\n"
    "v_add_f32 v122, v122, v43\n"
    "v_mfma_f32_16x16x32_bf16 a[84:87], v[176:179], v[132:135], a[84:87]\n"
    "ds_read_b128 v[172:175], v201 offset:39936\n"
    "v_add_f32 v113, v113, v82\n"
    "v_mfma_f32_16x16x32_bf16 a[84:87], v[180:183], v[148:151], a[84:87]\n"
    "buffer_load_dwordx4 v204, s[32:35], s64 offen offset:2048 lds\n"
    "v_mfma_f32_16x16x32_bf16 a[80:83], v[180:183], v[144:147], a[80:83]\n"
    "v_add_f32 v125, v125, v44\n"
    "v_add_f32 v114, v114, v83\n"
    "v_mfma_f32_16x16x32_bf16 a[80:83], v[176:179], v[128:131], a[80:83]\n"
    "s_waitcnt lgkmcnt(2)\n"
    "v_exp_f32 v58, v58\n"
    "v_mfma_f32_16x16x32_bf16 a[96:99], v[160:163], v[128:131], a[96:99]\n"
    "s_mov_b32 s95, s94\n"
    "ds_read_b128 v[176:179], v201 offset:40960\n"
    "v_add_f32 v126, v126, v45\n"
    "v_mfma_f32_16x16x32_bf16 a[96:99], v[164:167], v[144:147], a[96:99]\n"
    "s_sub_u32 s94, s94, 0x3000\n"
    "v_add_f32 v117, v117, v84\n"
    "v_add_f32 v125, v125, v46\n"
    "v_mfma_f32_16x16x32_bf16 a[100:103], v[164:167], v[148:151], a[100:103]\n"
    "s_min_u32 s94, s94, 0x6000\n"
    "v_exp_f32 v59, v59\n"
    "v_mfma_f32_16x16x32_bf16 a[100:103], v[160:163], v[132:135], a[100:103]\n"
    "s_sub_u32 s93, s94, s95\n"
    "v_add_f32 v118, v118, v85\n"
    "v_add_f32 v126, v126, v47\n"
    "v_mfma_f32_16x16x32_bf16 a[104:107], v[160:163], v[136:139], a[104:107]\n"
    "ds_read_b128 v[180:183], v201 offset:41984\n"
    "v_add_f32 v117, v117, v86\n"
    "v_mfma_f32_16x16x32_bf16 a[104:107], v[164:167], v[152:155], a[104:107]\n"
    "v_add_f32 v113, v113, v48\n"
    "v_add_u32 v205, s93, v205\n"
    "v_mfma_f32_16x16x32_bf16 a[108:111], v[164:167], v[156:159], a[108:111]\n"
    "v_exp_f32 v60, v60\n"
    "v_mfma_f32_16x16x32_bf16 a[108:111], v[160:163], v[140:143], a[108:111]\n"
    "s_waitcnt lgkmcnt(2)\n"
    "v_add_f32 v118, v118, v87\n"
    "v_add_f32 v114, v114, v49\n"
    "v_mfma_f32_16x16x32_bf16 a[124:127], v[168:171], v[140:143], a[124:127]\n"
    "ds_read_b128 v[160:163], v201 offset:43008\n"
    "v_add_u32 v206, s93, v206\n"
    "v_mfma_f32_16x16x32_bf16 a[124:127], v[172:175], v[156:159], a[124:127]\n"
    "v_add_f32 v121, v121, v88\n"
    "v_add_f32 v113, v113, v50\n"
    "v_mfma_f32_16x16x32_bf16 a[120:123], v[172:175], v[152:155], a[120:123]\n"
    "v_exp_f32 v61, v61\n"
    "v_mfma_f32_16x16x32_bf16 a[120:123], v[168:171], v[136:139], a[120:123]\n"
    "v_add_u32 v207, s93, v207\n"
    "v_add_f32 v122, v122, v89\n"
    "v_mfma_f32_16x16x32_bf16 a[116:119], v[168:171], v[132:135], a[116:119]\n"
    "ds_read_b128 v[164:167], v201 offset:44032\n"
    "v_add_f32 v114, v114, v51\n"
    "v_mfma_f32_16x16x32_bf16 a[116:119], v[172:175], v[148:151], a[116:119]\n"
    "v_add_f32 v121, v121, v90\n"
    "v_add_f32 v117, v117, v52\n"
    "v_mfma_f32_16x16x32_bf16 a[112:115], v[172:175], v[144:147], a[112:115]\n"
    "v_exp_f32 v62, v62\n"
    "v_mfma_f32_16x16x32_bf16 a[112:115], v[168:171], v[128:131], a[112:115]\n"
    "s_waitcnt lgkmcnt(2)\n"
    "v_add_f32 v122, v122, v91\n"
    "v_add_f32 v118, v118, v53\n"
    "v_mfma_f32_16x16x32_bf16 a[128:131], v[176:179], v[128:131], a[128:131]\n"
    "ds_read_b128 v[168:171], v201 offset:45056\n"
    "v_add_f32 v125, v125, v92\n"
    "v_mfma_f32_16x16x32_bf16 a[128:131], v[180:183], v[144:147], a[128:131]\n"
    "v_add_f32 v117, v117, v54\n"
    "v_add_f32 v126, v126, v93\n"
    "v_mfma_f32_16x16x32_bf16 a[132:135], v[180:183], v[148:151], a[132:135]\n"
    "v_exp_f32 v63, v63\n"
    "v_mfma_f32_16x16x32_bf16 a[132:135], v[176:179], v[132:135], a[132:135]\n"
    "s_add_u32 s64, s64, 0xc000\n"
    "v_add_f32 v118, v118, v55\n"
    "v_add_f32 v125, v125, v94\n"
    "v_mfma_f32_16x16x32_bf16 a[136:139], v[176:179], v[136:139], a[136:139]\n"
    "s_add_u32 s62, s62, 0x6000\n"
    "ds_read_b128 v[172:175], v201 offset:46080\n"
    "v_add_f32 v121, v121, v56\n"
    "v_mfma_f32_16x16x32_bf16 a[136:139], v[180:183], v[152:155], a[136:139]\n"
    "s_add_u32 s63, s63, 0x6000\n"
    "v_add_f32 v126, v126, v95\n"
    "v_add_f32 v122, v122, v57\n"
    "v_mfma_f32_16x16x32_bf16 a[140:143], v[180:183], v[156:159], a[140:143]\n"
    "s_add_u32 s60, s60, 0x6000\n"
    "v_mfma_f32_16x16x32_bf16 a[140:143], v[176:179], v[140:143], a[140:143]\n"
    "s_waitcnt lgkmcnt(2)\n"
    "v_mfma_f32_16x16x32_bf16 a[156:159], v[160:163], v[140:143], a[156:159]\n"
    "s_add_u32 s61, s61, 0x6000\n"
    "ds_read_b128 v[176:179], v201 offset:47104\n"
    "v_mfma_f32_16x16x32_bf16 a[156:159], v[164:167], v[156:159], a[156:159]\n"
    "ds_read_b128 v[180:183], v201 offset:48128\n"
    "v_mfma_f32_16x16x32_bf16 a[152:155], v[164:167], v[152:155], a[152:155]\n"
    "s_waitcnt vmcnt(3)\n"
    "v_mfma_f32_16x16x32_bf16 a[152:155], v[160:163], v[136:139], a[152:155]\n"
    "v_mfma_f32_16x16x32_bf16 a[148:151], v[160:163], v[132:135], a[148:151]\n"
    "v_mfma_f32_16x16x32_bf16 a[148:151], v[164:167], v[148:151], a[148:151]\n"
    "v_mfma_f32_16x16x32_bf16 a[144:147], v[164:167], v[144:147], a[144:147]\n"
    "v_mfma_f32_16x16x32_bf16 a[144:147], v[160:163], v[128:131], a[144:147]\n"
    "s_waitcnt lgkmcnt(0)\n"
    "v_mfma_f32_16x16x32_bf16 a[160:163], v[168:171], v[128:131], a[160:163]\n"
    "s_barrier\n"
    "LMID1:\n"
    "v_mfma_f32_16x16x32_bf16 a[160:163], v[172:175], v[144:147], a[160:163]\n"
    "ds_read_u16_d16_hi v223, v209 offset:6336\n"
    "ds_read_u16_d16_hi v222, v209 offset:192\n"
    "v_mfma_f32_16x16x32_bf16 a[164:167], v[172:175], v[148:151], a[164:167]\n"
    "ds_read_b128 v[160:163], v200 offset:24576\n"
    "v_add_f32 v121, v121, v58\n"
    "v_mfma_f32_16x16x32_bf16 a[164:167], v[168:171], v[132:135], a[164:167]\n"
    "ds_read_b128 v[232:235], v205 offset:24576\n"
    "v_add_f32 v122, v122, v59\n"
    "v_mfma_f32_16x16x32_bf16 a[168:171], v[168:171], v[136:139], a[168:171]\n"
    "ds_read_b128 v[164:167], v200 offset:25600\n"
    "ds_read_b128 v[236:239], v206 offset:24576\n"
    "v_mfma_f32_16x16x32_bf16 a[168:171], v[172:175], v[152:155], a[168:171]\n"
    "v_add_f32 v125, v125, v60\n"
    "v_add_f32 v126, v126, v61\n"
    "v_mfma_f32_16x16x32_bf16 a[172:175], v[172:175], v[156:159], a[172:175]\n"
    "v_add_f32 v125, v125, v62\n"
    "v_add_f32 v126, v126, v63\n"
    "v_mfma_f32_16x16x32_bf16 a[172:175], v[168:171], v[140:143], a[172:175]\n"
    "ds_read_b64 v[228:229], v210 offset:64\n"
    "ds_read2st64_b32 v[240:241], v207 offset0:96 offset1:99\n"
    "v_mfma_f32_16x16x32_bf16 a[188:191], v[176:179], v[140:143], a[188:191]\n"
    "ds_read_b128 v[168:171], v200 offset:26624\n"
    "ds_read2st64_b32 v[242:243], v207 offset0:102 offset1:105\n"
    "v_mfma_f32_16x16x32_bf16 a[188:191], v[180:183], v[156:159], a[188:191]\n"
    "ds_read2st64_b32 v[244:245], v207 offset0:108 offset1:111\n"
    "ds_read2st64_b32 v[246:247], v207 offset0:114 offset1:117\n"
    "v_mfma_f32_16x16x32_bf16 a[184:187], v[176:179], v[136:139], a[184:187]\n"
    "v_cvt_pk_bf16_f32 v140, v44, v45\n"
    "v_cvt_pk_bf16_f32 v141, v46, v47\n"
    "v_mfma_f32_16x16x32_bf16 a[184:187], v[180:183], v[152:155], a[184:187]\n"
    "s_waitcnt lgkmcnt(6)\n"
    "v_mul_f32 v226, s75, v223\n"
    "v_mfma_f32_16x16x32_bf16 a[180:183], v[176:179], v[132:135], a[180:183]\n"
    "s_add_u32 s59, s59, 1\n"
    "ds_read_b128 v[172:175], v200 offset:27648\n"
    "v_fma_f32 v227, -v222, v226, v217\n"
    "v_mfma_f32_16x16x32_bf16 a[180:183], v[180:183], v[148:151], a[180:183]\n"
    "v_cvt_f32_i32 v232, v232\n"
    "v_cvt_f32_i32 v233, v233\n"
    "v_mfma_f32_16x16x32_bf16 a[176:179], v[176:179], v[128:131], a[176:179]\n"
    "s_cmp_lt_u32 s59, 15\n"
    "v_cvt_f32_i32 v234, v234\n"
    "v_cvt_f32_i32 v235, v235\n"
    "v_mfma_f32_16x16x32_bf16 a[176:179], v[180:183], v[144:147], a[176:179]\n"
    "s_cbranch_scc1 LLOOP1\n"
    "s_branch LTAIL\n"
    "LENT0:\n"
    "s_mov_b32 s59, 1\n"
    "s_add_u32 m0, s72, 0x21000\n"
    "s_nop 0\n"
    "buffer_load_dwordx4 v250, s[48:51], 0 offen offset:2048 lds\n"
    "s_add_u32 m0, s72, 0x18000\n"
    "s_nop 0\n"
    "buffer_load_dwordx4 v202, s[32:35], s64 offen lds\n"
    "buffer_load_dwordx4 v203, s[32:35], s64 offen offset:1024 lds\n"
    "buffer_load_dwordx4 v204, s[32:35], s64 offen offset:2048 lds\n"
    "s_add_u32 s64, s64, 0xc000\n"
    "s_mov_b32 s94, 0x3000\n"
    "s_mov_b32 s95, 0x6000\n"
    "s_add_u32 s51, s72, 0x18000\n"
    "v_add_u32 v205, 0x3000, v205\n"
    "v_add_u32 v206, 0x3000, v206\n"
    "v_add_u32 v207, 0x3000, v207\n"
    "buffer_store_dwordx2 v[220:221], v214, s[44:47], 0 offen sc1\n"
    "ds_read_b128 v[160:163], v200 offset:24576\n"
    "ds_read_u16_d16_hi v223, v209 offset:6336\n"
    "ds_read_u16_d16_hi v222, v209 offset:192\n"
    "v_add_f32 v121, v121, v58\n"
    "ds_read_b128 v[164:167], v200 offset:25600\n"
    "ds_read_b128 v[232:235], v205 offset:24576\n"
    "v_add_f32 v122, v122, v59\n"
    "ds_read_b128 v[236:239], v206 offset:24576\n"
    "v_add_f32 v125, v125, v60\n"
    "v_add_f32 v126, v126, v61\n"
    "v_add_f32 v125, v125, v62\n"
    "v_add_f32 v126, v126, v63\n"
    "ds_read_b128 v[168:171], v200 offset:26624\n"
    "v_cvt_pk_bf16_f32 v140, v44, v45\n"
    "v_cvt_pk_bf16_f32 v141, v46, v47\n"
    "v_cvt_pk_bf16_f32 v136, v40, v41\n"
    "v_cvt_pk_bf16_f32 v137, v42, v43\n"
    "ds_read_b128 v[172:175], v200 offset:27648\n"
    "s_waitcnt lgkmcnt(2)\n"
    "v_mul_f32 v226, s75, v223\n"
    "v_fma_f32 v227, -v222, v226, v217\n"
    "v_cvt_f32_i32 v232, v232\n"
    "v_cvt_f32_i32 v233, v233\n"
    "s_nop 7\n"
    ".p2align 6\n"
    "LLOOP:\n"
    "v_mfma_f32_16x16x32_bf16 v[64:67], v[160:163], a[192:195], v[96:99]\n"
    "ds_read_b128 v[176:179], v200 offset:28672\n"
    "v_cvt_f32_i32 v234, v234\n"
    "v_mfma_f32_16x16x32_bf16 v[64:67], v[164:167], a[196:199], v[64:67]\n"
    "s_waitcnt lgkmcnt(1)\n"
    "v_cvt_f32_i32 v235, v235\n"
    "v_cvt_f32_i32 v236, v236\n"
    "v_mfma_f32_16x16x32_bf16 v[64:67], v[168:171], a[200:203], v[64:67]\n"
    "v_cvt_f32_i32 v237, v237\n"
    "v_cvt_f32_i32 v238, v238\n"
    "v_mfma_f32_16x16x32_bf16 v[68:71], v[168:171], a[224:227], v[100:103]\n"
    "v_cvt_f32_i32 v239, v239\n"
    "v_mul_f32_dpp v232, v226, v232 row_newbcast:0 row_mask:0xf bank_mask:0xf\n"
    "v_mfma_f32_16x16x32_bf16 v[68:71], v[164:167], a[220:223], v[68:71]\n"
    "ds_read_b128 v[180:183], v200 offset:29696\n"
    "v_mul_f32_dpp v233, v226, v233 row_newbcast:1 row_mask:0xf bank_mask:0xf\n"
    "v_mfma_f32_16x16x32_bf16 v[68:71], v[160:163], a[216:219], v[68:71]\n"
    "v_mul_f32_dpp v234, v226, v234 row_newbcast:2 row_mask:0xf bank_mask:0xf\n"
    "v_mul_f32_dpp v235, v226, v235 row_newbcast:3 row_mask:0xf bank_mask:0xf\n"
    "v_mfma_f32_16x16x32_bf16 v[72:75], v[160:163], a[240:243], v[104:107]\n"
    "v_mul_f32_dpp v236, v226, v236 row_newbcast:4 row_mask:0xf bank_mask:0xf\n"
    "v_mul_f32_dpp v237, v226, v237 row_newbcast:5 row_mask:0xf bank_mask:0xf\n"
    "v_mfma_f32_16x16x32_bf16 v[72:75], v[164:167], a[244:247], v[72:75]\n"
    "v_mul_f32_dpp v238, v226, v238 row_newbcast:6 row_mask:0xf bank_mask:0xf\n"
    "v_mul_f32_dpp v239, v226, v239 row_newbcast:7 row_mask:0xf bank_mask:0xf\n"
    "v_mfma_f32_16x16x32_bf16 v[72:75], v[168:171], a[248:251], v[72:75]\n"
    "v_add_f32_dpp v232, v227, v232 row_newbcast:0 row_mask:0xf bank_mask:0xf\n"
    "v_add_f32_dpp v233, v227, v233 row_newbcast:1 row_mask:0xf bank_mask:0xf\n"
    "v_mfma_f32_16x16x32_bf16 v[76:79], v[168:171], v[16:19], v[108:111]\n"
    "v_add_f32_dpp v234, v227, v234 row_newbcast:2 row_mask:0xf bank_mask:0xf\n"
    "v_add_f32_dpp v235, v227, v235 row_newbcast:3 row_mask:0xf bank_mask:0xf\n"
    "v_mfma_f32_16x16x32_bf16 v[76:79], v[164:167], v[12:15], v[76:79]\n"
    "v_add_f32_dpp v236, v227, v236 row_newbcast:4 row_mask:0xf bank_mask:0xf\n"
    "v_add_f32_dpp v237, v227, v237 row_newbcast:5 row_mask:0xf bank_mask:0xf\n"
    "v_mfma_f32_16x16x32_bf16 v[76:79], v[160:163], v[8:11], v[76:79]\n"
    "s_waitcnt lgkmcnt(1)\n"
    "v_add_f32_dpp v238, v227, v238 row_newbcast:6 row_mask:0xf bank_mask:0xf\n"
    "v_add_f32_dpp v239, v227, v239 row_newbcast:7 row_mask:0xf bank_mask:0xf\n"
    "v_mfma_f32_16x16x32_bf16 v[76:79], v[172:175], v[20:23], v[76:79]\n"
    "ds_read_b128 v[160:163], v200 offset:33792\n"
    "v_cvt_pk_bf16_f32 v232, v232, v233\n"
    "v_mfma_f32_16x16x32_bf16 v[76:79], v[176:179], v[24:27], v[76:79]\n"
    "s_waitcnt lgkmcnt(1)\n"
    "v_cvt_pk_bf16_f32 v233, v234, v235\n"
    "v_cvt_pk_bf16_f32 v234, v236, v237\n"
    "v_mfma_f32_16x16x32_bf16 v[76:79], v[180:183], v[28:31], v[76:79]\n"
    "v_cvt_pk_bf16_f32 v235, v238, v239\n"
    "v_cvt_pk_bf16_f32 v132, v36, v37\n"
    "v_mfma_f32_16x16x32_bf16 v[72:75], v[180:183], v[4:7], v[72:75]\n"
    "ds_read_b128 v[164:167], v200 offset:34816\n"
    "v_cvt_pk_bf16_f32 v133, v38, v39\n"
    "v_mfma_f32_16x16x32_bf16 v[72:75], v[176:179], v[0:3], v[72:75]\n"
    "v_cvt_pk_bf16_f32 v128, v32, v33\n"
    "v_cvt_pk_bf16_f32 v129, v34, v35\n"
    "v_mfma_f32_16x16x32_bf16 v[72:75], v[172:175], a[252:255], v[72:75]\n"
    "v_cvt_pk_bf16_f32 v142, v60, v61\n"
    "v_cvt_pk_bf16_f32 v143, v62, v63\n"
    "v_mfma_f32_16x16x32_bf16 v[68:71], v[172:175], a[228:231], v[68:71]\n"
    "ds_read_b128 v[168:171], v200 offset:35840\n"
    "v_cvt_pk_bf16_f32 v138, v56, v57\n"
    "v_mfma_f32_16x16x32_bf16 v[68:71], v[176:179], a[232:235], v[68:71]\n"
    "v_cvt_pk_bf16_f32 v139, v58, v59\n"
    "v_cvt_pk_bf16_f32 v134, v52, v53\n"
    "v_mfma_f32_16x16x32_bf16 v[68:71], v[180:183], a[236:239], v[68:71]\n"
    "v_cvt_pk_bf16_f32 v135, v54, v55\n"
    "v_cvt_pk_bf16_f32 v130, v48, v49\n"
    "v_mfma_f32_16x16x32_bf16 v[64:67], v[180:183], a[212:215], v[64:67]\n"
    "v_cvt_pk_bf16_f32 v131, v50, v51\n"
    "v_mfma_f32_16x16x32_bf16 v[64:67], v[176:179], a[208:211], v[64:67]\n"
    "buffer_store_dwordx4 v[232:235], v211, s[36:39], s62 offen sc1\n"
    "v_mfma_f32_16x16x32_bf16 v[64:67], v[172:175], a[204:207], v[64:67]\n"
    "s_waitcnt lgkmcnt(1)\n"
    "v_mfma_f32_16x16x32_bf16 v[80:83], v[160:163], a[204:207], v[96:99]\n"
    "ds_read_b128 v[172:175], v200 offset:30720\n"
    "v_mfma_f32_16x16x32_bf16 v[80:83], v[164:167], a[208:211], v[80:83]\n"
    "s_waitcnt lgkmcnt(1)\n"
    "v_mfma_f32_16x16x32_bf16 v[80:83], v[168:171], a[212:215], v[80:83]\n"
    "v_mfma_f32_16x16x32_bf16 v[84:87], v[168:171], a[236:239], v[100:103]\n"
    "ds_read_b128 v[176:179], v200 offset:31744\n"
    "v_mfma_f32_16x16x32_bf16 v[84:87], v[164:167], a[232:235], v[84:87]\n"
    "v_exp_f32 v64, v64\n"
    "v_mfma_f32_16x16x32_bf16 v[84:87], v[160:163], a[228:231], v[84:87]\n"
    "v_exp_f32 v65, v65\n"
    "v_mfma_f32_16x16x32_bf16 v[88:91], v[160:163], a[252:255], v[104:107]\n"
    "ds_read_b128 v[180:183], v200 offset:32768\n"
    "v_cvt_pk_bf16_f32 v144, v64, v65\n"
    "v_mfma_f32_16x16x32_bf16 v[88:91], v[164:167], v[0:3], v[88:91]\n"
    "s_add_u32 s65, s60, 0\n"
    "v_exp_f32 v66, v66\n"
    "v_mfma_f32_16x16x32_bf16 v[88:91], v[168:171], v[4:7], v[88:91]\n"
    "s_mov_b32 m0, 0\n"
    "v_exp_f32 v67, v67\n"
    "v_mfma_f32_16x16x32_bf16 v[92:95], v[168:171], v[28:31], v[108:111]\n"
    "v_exp_f32 v68, v68\n"
    "v_mfma_f32_16x16x32_bf16 v[92:95], v[164:167], v[24:27], v[92:95]\n"
    "buffer_load_dwordx4 v200, s[36:39], s65 offen sc1 lds\n"
    "v_mfma_f32_16x16x32_bf16 v[92:95], v[160:163], v[20:23], v[92:95]\n"
    "s_waitcnt lgkmcnt(1)\n"
    "v_exp_f32 v69, v69\n"
    "v_mfma_f32_16x16x32_bf16 v[92:95], v[172:175], v[8:11], v[92:95]\n"
    "ds_read_b128 v[160:163], v200 offset:36864\n"
    "v_cvt_pk_bf16_f32 v145, v66, v67\n"
    "v_mfma_f32_16x16x32_bf16 v[92:95], v[176:179], v[12:15], v[92:95]\n"
    "s_waitcnt lgkmcnt(1)\n"
    "v_exp_f32 v70, v70\n"
    "v_mfma_f32_16x16x32_bf16 v[92:95], v[180:183], v[16:19], v[92:95]\n"
    "buffer_load_dwordx4 v200, s[36:39], s65 offen offset:1024 sc1 lds\n"
    "v_mfma_f32_16x16x32_bf16 v[88:91], v[180:183], a[248:251], v[88:91]\n"
    "ds_read_b128 v[164:167], v200 offset:37888\n"
    "v_cvt_pk_bf16_f32 v148, v68, v69\n"
    "v_mfma_f32_16x16x32_bf16 v[88:91], v[176:179], a[244:247], v[88:91]\n"
    "v_exp_f32 v71, v71\n"
    "v_mfma_f32_16x16x32_bf16 v[88:91], v[172:175], a[240:243], v[88:91]\n"
    "v_exp_f32 v72, v72\n"
    "v_mfma_f32_16x16x32_bf16 v[84:87], v[172:175], a[216:219], v[84:87]\n"
    "ds_read_b128 v[168:171], v200 offset:38912\n"
    "v_cvt_pk_bf16_f32 v149, v70, v71\n"
    "v_mfma_f32_16x16x32_bf16 v[84:87], v[176:179], a[220:223], v[84:87]\n"
    "buffer_load_dwordx4 v200, s[36:39], s65 offen offset:2048 sc1 lds\n"
    "v_mfma_f32_16x16x32_bf16 v[84:87], v[180:183], a[224:227], v[84:87]\n"
    "v_exp_f32 v73, v73\n"
    "v_mfma_f32_16x16x32_bf16 v[80:83], v[180:183], a[200:203], v[80:83]\n"
    "v_exp_f32 v74, v74\n"
    "v_mfma_f32_16x16x32_bf16 v[80:83], v[176:179], a[196:199], v[80:83]\n"
    "v_exp_f32 v75, v75\n"
    "v_mfma_f32_16x16x32_bf16 v[80:83], v[172:175], a[192:195], v[80:83]\n"
    "s_waitcnt lgkmcnt(1)\n"
    "buffer_load_dwordx4 v200, s[36:39], s65 offen offset:3072 sc1 lds\n"
    "v_mfma_f32_16x16x32_bf16 v[32:35], v[160:163], a[192:195], v[96:99]\n"
    "ds_read_b128 v[172:175], v200 offset:39936\n"
    "v_cvt_pk_bf16_f32 v152, v72, v73\n"
    "v_mfma_f32_16x16x32_bf16 v[32:35], v[164:167], a[196:199], v[32:35]\n"
    "s_waitcnt lgkmcnt(1)\n"
    "s_add_u32 s65, s60, 0x1000\n"
    "v_cvt_pk_bf16_f32 v153, v74, v75\n"
    "v_mfma_f32_16x16x32_bf16 v[32:35], v[168:171], a[200:203], v[32:35]\n"
    "s_mov_b32 m0, 0x1000\n"
    "v_exp_f32 v76, v76\n"
    "v_mfma_f32_16x16x32_bf16 v[36:39], v[168:171], a[224:227], v[100:103]\n"
    "ds_read_b128 v[176:179], v200 offset:40960\n"
    "v_add_f32 v113, v113, v64\n"
    "v_mfma_f32_16x16x32_bf16 v[36:39], v[164:167], a[220:223], v[36:39]\n"
    "buffer_load_dwordx4 v200, s[36:39], s65 offen sc1 lds\n"
    "v_mfma_f32_16x16x32_bf16 v[36:39], v[160:163], a[216:219], v[36:39]\n"
    "v_exp_f32 v77, v77\n"
    "v_mfma_f32_16x16x32_bf16 v[40:43], v[160:163], a[240:243], v[104:107]\n"
    "ds_read_b128 v[180:183], v200 offset:41984\n"
    "v_cvt_pk_bf16_f32 v156, v76, v77\n"
    "v_mfma_f32_16x16x32_bf16 v[40:43], v[164:167], a[244:247], v[40:43]\n"
    "v_exp_f32 v78, v78\n"
    "v_mfma_f32_16x16x32_bf16 v[40:43], v[168:171], a[248:251], v[40:43]\n"
    "buffer_load_dwordx4 v200, s[36:39], s65 offen offset:1024 sc1 lds\n"
    "v_mfma_f32_16x16x32_bf16 v[44:47], v[168:171], v[16:19], v[108:111]\n"
    "v_exp_f32 v79, v79\n"
    "v_mfma_f32_16x16x32_bf16 v[44:47], v[164:167], v[12:15], v[44:47]\n"
    "v_exp_f32 v80, v80\n"
    "v_mfma_f32_16x16x32_bf16 v[44:47], v[160:163], v[8:11], v[44:47]\n"
    "s_waitcnt lgkmcnt(1)\n"
    "v_exp_f32 v81, v81\n"
    "v_mfma_f32_16x16x32_bf16 v[44:47], v[172:175], v[20:23], v[44:47]\n"
    "ds_read_b128 v[160:163], v200 offset:46080\n"
    "v_cvt_pk_bf16_f32 v157, v78, v79\n"
    "v_mfma_f32_16x16x32_bf16 v[44:47], v[176:179], v[24:27], v[44:47]\n"
    "s_waitcnt lgkmcnt(1)\n"
    "buffer_load_dwordx4 v200, s[36:39], s65 offen offset:2048 sc1 lds\n"
    "v_mfma_f32_16x16x32_bf16 v[44:47], v[180:183], v[28:31], v[44:47]\n"
    "v_exp_f32 v82, v82\n"
    "v_mfma_f32_16x16x32_bf16 v[40:43], v[180:183], v[4:7], v[40:43]\n"
    "ds_read_b128 v[164:167], v200 offset:47104\n"
    "v_cvt_pk_bf16_f32 v146, v80, v81\n"
    "v_mfma_f32_16x16x32_bf16 v[40:43], v[176:179], v[0:3], v[40:43]\n"
    "v_exp_f32 v83, v83\n"
    "v_mfma_f32_16x16x32_bf16 v[40:43], v[172:175], a[252:255], v[40:43]\n"
    "buffer_load_dwordx4 v200, s[36:39], s65 offen offset:3072 sc1 lds\n"
    "v_mfma_f32_16x16x32_bf16 v[36:39], v[172:175], a[228:231], v[36:39]\n"
    "s_add_u32 s65, s61, 0\n"
    "ds_read_b128 v[168:171], v200 offset:48128\n"
    "v_cvt_pk_bf16_f32 v147, v82, v83\n"
    "v_mfma_f32_16x16x32_bf16 v[36:39], v[176:179], a[232:235], v[36:39]\n"
    "s_mov_b32 m0, 0x12000\n"
    "v_exp_f32 v84, v84\n"
    "v_mfma_f32_16x16x32_bf16 v[36:39], v[180:183], a[236:239], v[36:39]\n"
    "v_exp_f32 v85, v85\n"
    "v_mfma_f32_16x16x32_bf16 v[32:35], v[180:183], a[212:215], v[32:35]\n"
    "buffer_load_dwordx4 v200, s[40:43], s65 offen sc1 lds\n"
    "v_mfma_f32_16x16x32_bf16 v[32:35], v[176:179], a[208:211], v[32:35]\n"
    "v_exp_f32 v86, v86\n"
    "v_mfma_f32_16x16x32_bf16 v[32:35], v[172:175], a[204:207], v[32:35]\n"
    "s_waitcnt lgkmcnt(1)\n"
    "v_exp_f32 v87, v87\n"
    "v_mfma_f32_16x16x32_bf16 v[48:51], v[160:163], a[204:207], v[96:99]\n"
    "ds_read_b128 v[172:175], v200 offset:43008\n"
    "v_cvt_pk_bf16_f32 v150, v84, v85\n"
    "v_mfma_f32_16x16x32_bf16 v[48:51], v[164:167], a[208:211], v[48:51]\n"
    "s_waitcnt lgkmcnt(1)\n"
    "buffer_load_dwordx4 v200, s[40:43], s65 offen offset:1024 sc1 lds\n"
    "v_mfma_f32_16x16x32_bf16 v[48:51], v[168:171], a[212:215], v[48:51]\n"
    "v_exp_f32 v88, v88\n"
    "v_mfma_f32_16x16x32_bf16 v[52:55], v[168:171], a[236:239], v[100:103]\n"
    "ds_read_b128 v[176:179], v200 offset:44032\n"
    "v_cvt_pk_bf16_f32 v151, v86, v87\n"
    "v_mfma_f32_16x16x32_bf16 v[52:55], v[164:167], a[232:235], v[52:55]\n"
    "v_exp_f32 v89, v89\n"
    "v_mfma_f32_16x16x32_bf16 v[52:55], v[160:163], a[228:231], v[52:55]\n"
    "buffer_load_dwordx4 v200, s[40:43], s65 offen offset:2048 sc1 lds\n"
    "v_mfma_f32_16x16x32_bf16 v[56:59], v[160:163], a[252:255], v[104:107]\n"
    "ds_read_b128 v[180:183], v200 offset:45056\n"
    "v_cvt_pk_bf16_f32 v154, v88, v89\n"
    "v_mfma_f32_16x16x32_bf16 v[56:59], v[164:167], v[0:3], v[56:59]\n"
    "v_exp_f32 v90, v90\n"
    "v_mfma_f32_16x16x32_bf16 v[56:59], v[168:171], v[4:7], v[56:59]\n"
    "v_exp_f32 v91, v91\n"
    "v_mfma_f32_16x16x32_bf16 v[60:63], v[168:171], v[28:31], v[108:111]\n"
    "buffer_load_dwordx4 v200, s[40:43], s65 offen offset:3072 sc1 lds\n"
    "v_mfma_f32_16x16x32_bf16 v[60:63], v[164:167], v[24:27], v[60:63]\n"
    "s_add_u32 s65, s61, 0x1000\n"
    "v_exp_f32 v92, v92\n"
    "v_mfma_f32_16x16x32_bf16 v[60:63], v[160:163], v[20:23], v[60:63]\n"
    "s_waitcnt lgkmcnt(1)\n"
    "s_mov_b32 m0, 0x13000\n"
    "v_cvt_pk_bf16_f32 v155, v90, v91\n"
    "v_mfma_f32_16x16x32_bf16 v[60:63], v[172:175], v[8:11], v[60:63]\n"
    "ds_read_b128 v[160:163], v201\n"
    "v_add_f32 v114, v114, v65\n"
    "v_mfma_f32_16x16x32_bf16 v[60:63], v[176:179], v[12:15], v[60:63]\n"
    "s_waitcnt lgkmcnt(1)\n"
    "buffer_load_dwordx4 v200, s[40:43], s65 offen sc1 lds\n"
    "v_mfma_f32_16x16x32_bf16 v[60:63], v[180:183], v[16:19], v[60:63]\n"
    "v_exp_f32 v93, v93\n"
    "v_mfma_f32_16x16x32_bf16 v[56:59], v[180:183], a[248:251], v[56:59]\n"
    "ds_read_b128 v[164:167], v201 offset:1024\n"
    "v_cvt_pk_bf16_f32 v158, v92, v93\n"
    "v_mfma_f32_16x16x32_bf16 v[56:59], v[176:179], a[244:247], v[56:59]\n"
    "v_exp_f32 v94, v94\n"
    "v_mfma_f32_16x16x32_bf16 v[56:59], v[172:175], a[240:243], v[56:59]\n"
    "buffer_load_dwordx4 v200, s[40:43], s65 offen offset:1024 sc1 lds\n"
    "v_mfma_f32_16x16x32_bf16 v[52:55], v[172:175], a[216:219], v[52:55]\n"
    "ds_read_b128 v[168:171], v201 offset:2048\n"
    "v_add_f32 v113, v113, v66\n"
    "v_mfma_f32_16x16x32_bf16 v[52:55], v[176:179], a[220:223], v[52:55]\n"
    "v_exp_f32 v95, v95\n"
    "v_mfma_f32_16x16x32_bf16 v[52:55], v[180:183], a[224:227], v[52:55]\n"
    "v_cvt_pk_bf16_f32 v159, v94, v95\n"
    "v_add_f32 v114, v114, v67\n"
    "v_mfma_f32_16x16x32_bf16 v[48:51], v[180:183], a[200:203], v[48:51]\n"
    "buffer_load_dwordx4 v200, s[40:43], s65 offen offset:2048 sc1 lds\n"
    "v_mfma_f32_16x16x32_bf16 v[48:51], v[176:179], a[196:199], v[48:51]\n"
    "v_exp_f32 v32, v32\n"
    "v_mfma_f32_16x16x32_bf16 v[48:51], v[172:175], a[192:195], v[48:51]\n"
    "s_waitcnt lgkmcnt(1)\n"
    "v_exp_f32 v33, v33\n"
    "v_mfma_f32_16x16x32_bf16 a[0:3], v[160:163], v[128:131], a[0:3]\n"
    "ds_read_b128 v[172:175], v201 offset:3072\n"
    "ds_read_b128 v[176:179], v201 offset:4096\n"
    "v_mfma_f32_16x16x32_bf16 a[0:3], v[164:167], v[144:147], a[0:3]\n"
    "buffer_load_dwordx4 v200, s[40:43], s65 offen offset:3072 sc1 lds\n"
    "v_mfma_f32_16x16x32_bf16 a[4:7], v[160:163], v[132:135], a[4:7]\n"
    "v_exp_f32 v34, v34\n"
    "v_mfma_f32_16x16x32_bf16 a[4:7], v[164:167], v[148:151], a[4:7]\n"
    "v_exp_f32 v35, v35\n"
    "v_mfma_f32_16x16x32_bf16 a[8:11], v[160:163], v[136:139], a[8:11]\n"
    "ds_read_b128 v[180:183], v201 offset:5120\n"
    "v_add_f32 v117, v117, v68\n"
    "v_mfma_f32_16x16x32_bf16 a[8:11], v[164:167], v[152:155], a[8:11]\n"
    "v_exp_f32 v36, v36\n"
    "v_mfma_f32_16x16x32_bf16 a[12:15], v[160:163], v[140:143], a[12:15]\n"
    "v_exp_f32 v37, v37\n"
    "v_mfma_f32_16x16x32_bf16 a[12:15], v[164:167], v[156:159], a[12:15]\n"
    "s_waitcnt lgkmcnt(2)\n"
    "v_exp_f32 v38, v38\n"
    "v_mfma_f32_16x16x32_bf16 a[28:31], v[168:171], v[140:143], a[28:31]\n"
    "ds_read_b128 v[160:163], v201 offset:6144\n"
    "v_add_f32 v118, v118, v69\n"
    "v_mfma_f32_16x16x32_bf16 a[28:31], v[172:175], v[156:159], a[28:31]\n"
    "v_exp_f32 v39, v39\n"
    "v_mfma_f32_16x16x32_bf16 a[24:27], v[172:175], v[152:155], a[24:27]\n"
    "v_exp_f32 v40, v40\n"
    "v_mfma_f32_16x16x32_bf16 a[24:27], v[168:171], v[136:139], a[24:27]\n"
    "v_exp_f32 v41, v41\n"
    "v_mfma_f32_16x16x32_bf16 a[20:23], v[168:171], v[132:135], a[20:23]\n"
    "ds_read_b128 v[164:167], v201 offset:7168\n"
    "v_add_f32 v117, v117, v70\n"
    "v_mfma_f32_16x16x32_bf16 a[20:23], v[172:175], v[148:151], a[20:23]\n"
    "v_exp_f32 v42, v42\n"
    "v_mfma_f32_16x16x32_bf16 a[16:19], v[172:175], v[144:147], a[16:19]\n"
    "v_exp_f32 v43, v43\n"
    "v_mfma_f32_16x16x32_bf16 a[16:19], v[168:171], v[128:131], a[16:19]\n"
    "s_waitcnt lgkmcnt(2)\n"
    "v_exp_f32 v44, v44\n"
    "v_mfma_f32_16x16x32_bf16 a[32:35], v[176:179], v[128:131], a[32:35]\n"
    "ds_read_b128 v[168:171], v201 offset:8192\n"
    "v_add_f32 v113, v113, v32\n"
    "v_mfma_f32_16x16x32_bf16 a[32:35], v[180:183], v[144:147], a[32:35]\n"
    "buffer_load_dwordx2 v[218:219], v213, s[44:47], 0 offen sc1\n"
    "v_mfma_f32_16x16x32_bf16 a[36:39], v[180:183], v[148:151], a[36:39]\n"
    "v_exp_f32 v45, v45\n"
    "v_mfma_f32_16x16x32_bf16 a[36:39], v[176:179], v[132:135], a[36:39]\n"
    "v_exp_f32 v46, v46\n"
    "v_mfma_f32_16x16x32_bf16 a[40:43], v[176:179], v[136:139], a[40:43]\n"
    "ds_read_b128 v[172:175], v201 offset:9216\n"
    "v_add_f32 v118, v118, v71\n"
    "v_mfma_f32_16x16x32_bf16 a[40:43], v[180:183], v[152:155], a[40:43]\n"
    "v_exp_f32 v47, v47\n"
    "v_mfma_f32_16x16x32_bf16 a[44:47], v[180:183], v[156:159], a[44:47]\n"
    "v_exp_f32 v48, v48\n"
    "v_mfma_f32_16x16x32_bf16 a[44:47], v[176:179], v[140:143], a[44:47]\n"
    "s_waitcnt lgkmcnt(2)\n"
    "v_exp_f32 v49, v49\n"
    "v_mfma_f32_16x16x32_bf16 a[60:63], v[160:163], v[140:143], a[60:63]\n"
    "ds_read_b128 v[176:179], v201 offset:10240\n"
    "v_add_f32 v114, v114, v33\n"
    "v_mfma_f32_16x16x32_bf16 a[60:63], v[164:167], v[156:159], a[60:63]\n"
    "v_exp_f32 v50, v50\n"
    "v_mfma_f32_16x16x32_bf16 a[56:59], v[164:167], v[152:155], a[56:59]\n"
    "v_exp_f32 v51, v51\n"
    "v_mfma_f32_16x16x32_bf16 a[56:59], v[160:163], v[136:139], a[56:59]\n"
    "v_exp_f32 v52, v52\n"
    "v_mfma_f32_16x16x32_bf16 a[52:55], v[160:163], v[132:135], a[52:55]\n"
    "ds_read_b128 v[180:183], v201 offset:11264\n"
    "v_add_f32 v121, v121, v72\n"
    "v_mfma_f32_16x16x32_bf16 a[52:55], v[164:167], v[148:151], a[52:55]\n"
    "v_add_f32 v113, v113, v34\n"
    "v_add_f32 v122, v122, v73\n"
    "v_mfma_f32_16x16x32_bf16 a[48:51], v[164:167], v[144:147], a[48:51]\n"
    "v_exp_f32 v53, v53\n"
    "v_mfma_f32_16x16x32_bf16 a[48:51], v[160:163], v[128:131], a[48:51]\n"
    "s_waitcnt lgkmcnt(2)\n"
    "v_add_f32 v114, v114, v35\n"
    "v_add_f32 v121, v121, v74\n"
    "v_mfma_f32_16x16x32_bf16 a[64:67], v[168:171], v[128:131], a[64:67]\n"
    "ds_read_b128 v[160:163], v201 offset:12288\n"
    "v_add_f32 v117, v117, v36\n"
    "v_mfma_f32_16x16x32_bf16 a[64:67], v[172:175], v[144:147], a[64:67]\n"
    "v_exp_f32 v54, v54\n"
    "v_mfma_f32_16x16x32_bf16 a[68:71], v[172:175], v[148:151], a[68:71]\n"
    "v_add_f32 v122, v122, v75\n"
    "v_add_f32 v118, v118, v37\n"
    "v_mfma_f32_16x16x32_bf16 a[68:71], v[168:171], v[132:135], a[68:71]\n"
    "s_add_u32 m0, s51, s95\n"
    "v_add_f32 v125, v125, v76\n"
    "v_add_f32 v117, v117, v38\n"
    "v_mfma_f32_16x16x32_bf16 a[72:75], v[168:171], v[136:139], a[72:75]\n"
    "ds_read_b128 v[164:167], v201 offset:13312\n"
    "v_add_f32 v126, v126, v77\n"
    "v_mfma_f32_16x16x32_bf16 a[72:75], v[172:175], v[152:155], a[72:75]\n"
    "buffer_load_dwordx4 v202, s[32:35], s64 offen lds\n"
    "v_mfma_f32_16x16x32_bf16 a[76:79], v[172:175], v[156:159], a[76:79]\n"
    "v_exp_f32 v55, v55\n"
    "v_mfma_f32_16x16x32_bf16 a[76:79], v[168:171], v[140:143], a[76:79]\n"
    "s_waitcnt lgkmcnt(2)\n"
    "v_add_f32 v118, v118, v39\n"
    "v_add_f32 v125, v125, v78\n"
    "v_mfma_f32_16x16x32_bf16 a[92:95], v[176:179], v[140:143], a[92:95]\n"
    "ds_read_b128 v[168:171], v201 offset:14336\n"
    "v_add_f32 v121, v121, v40\n"
    "v_mfma_f32_16x16x32_bf16 a[92:95], v[180:183], v[156:159], a[92:95]\n"
    "buffer_load_dwordx4 v203, s[32:35], s64 offen offset:1024 lds\n"
    "v_mfma_f32_16x16x32_bf16 a[88:91], v[180:183], v[152:155], a[88:91]\n"
    "v_exp_f32 v56, v56\n"
    "v_mfma_f32_16x16x32_bf16 a[88:91], v[176:179], v[136:139], a[88:91]\n"
    "v_add_f32 v126, v126, v79\n"
    "v_add_f32 v122, v122, v41\n"
    "v_mfma_f32_16x16x32_bf16 a[84:87], v[176:179], v[132:135], a[84:87]\n"
    "ds_read_b128 v[172:175], v201 offset:15360\n"
    "v_add_f32 v113, v113, v80\n"
    "v_mfma_f32_16x16x32_bf16 a[84:87], v[180:183], v[148:151], a[84:87]\n"
    "buffer_load_dwordx4 v204, s[32:35], s64 offen offset:2048 lds\n"
    "v_mfma_f32_16x16x32_bf16 a[80:83], v[180:183], v[144:147], a[80:83]\n"
    "v_add_f32 v121, v121, v42\n"
    "v_add_f32 v114, v114, v81\n"
    "v_mfma_f32_16x16x32_bf16 a[80:83], v[176:179], v[128:131], a[80:83]\n"
    "s_waitcnt lgkmcnt(2)\n"
    "v_exp_f32 v57, v57\n"
    "v_mfma_f32_16x16x32_bf16 a[96:99], v[160:163], v[128:131], a[96:99]\n"
    "s_mov_b32 s95, s94\n"
    "ds_read_b128 v[176:179], v201 offset:16384\n"
    "v_add_f32 v122, v122, v43\n"
    "v_mfma_f32_16x16x32_bf16 a[96:99], v[164:167], v[144:147], a[96:99]\n"
    "s_sub_u32 s94, s94, 0x3000\n"
    "v_add_f32 v113, v113, v82\n"
    "v_add_f32 v125, v125, v44\n"
    "v_mfma_f32_16x16x32_bf16 a[100:103], v[164:167], v[148:151], a[100:103]\n"
    "s_min_u32 s94, s94, 0x6000\n"
    "v_exp_f32 v58, v58\n"
    "v_mfma_f32_16x16x32_bf16 a[100:103], v[160:163], v[132:135], a[100:103]\n"
    "s_sub_u32 s93, s94, s95\n"
    "v_add_f32 v114, v114, v83\n"
    "v_add_f32 v126, v126, v45\n"
    "v_mfma_f32_16x16x32_bf16 a[104:107], v[160:163], v[136:139], a[104:107]\n"
    "ds_read_b128 v[180:183], v201 offset:17408\n"
    "v_add_f32 v117, v117, v84\n"
    "v_mfma_f32_16x16x32_bf16 a[104:107], v[164:167], v[152:155], a[104:107]\n"
    "v_add_f32 v125, v125, v46\n"
    "v_add_f32 v118, v118, v85\n"
    "v_mfma_f32_16x16x32_bf16 a[108:111], v[164:167], v[156:159], a[108:111]\n"
    "v_exp_f32 v59, v59\n"
    "v_mfma_f32_16x16x32_bf16 a[108:111], v[160:163], v[140:143], a[108:111]\n"
    "s_waitcnt lgkmcnt(2)\n"
    "v_add_f32 v126, v126, v47\n"
    "v_add_f32 v117, v117, v86\n"
    "v_mfma_f32_16x16x32_bf16 a[124:127], v[168:171], v[140:143], a[124:127]\n"
    "ds_read_b128 v[160:163], v201 offset:18432\n"
    "v_add_f32 v113, v113, v48\n"
    "v_mfma_f32_16x16x32_bf16 a[124:127], v[172:175], v[156:159], a[124:127]\n"
    "v_exp_f32 v60, v60\n"
    "v_mfma_f32_16x16x32_bf16 a[120:123], v[172:175], v[152:155], a[120:123]\n"
    "v_add_f32 v118, v118, v87\n"
    "v_add_f32 v114, v114, v49\n"
    "v_mfma_f32_16x16x32_bf16 a[120:123], v[168:171], v[136:139], a[120:123]\n"
    "v_add_u32 v220, 1, v220\n"
    "v_add_u32 v205, s93, v205\n"
    "v_mfma_f32_16x16x32_bf16 a[116:119], v[168:171], v[132:135], a[116:119]\n"
    "ds_read_b128 v[164:167], v201 offset:19456\n"
    "v_add_f32 v121, v121, v88\n"
    "v_mfma_f32_16x16x32_bf16 a[116:119], v[172:175], v[148:151], a[116:119]\n"
    "v_add_f32 v113, v113, v50\n"
    "v_add_u32 v209, 0x180, v209\n"
    "v_mfma_f32_16x16x32_bf16 a[112:115], v[172:175], v[144:147], a[112:115]\n"
    "v_exp_f32 v61, v61\n"
    "v_mfma_f32_16x16x32_bf16 a[112:115], v[168:171], v[128:131], a[112:115]\n"
    "s_waitcnt lgkmcnt(2)\n"
    "v_add_u32 v206, s93, v206\n"
    "v_add_f32 v122, v122, v89\n"
    "v_mfma_f32_16x16x32_bf16 a[128:131], v[176:179], v[128:131], a[128:131]\n"
    "ds_read_b128 v[168:171], v201 offset:20480\n"
    "v_add_f32 v114, v114, v51\n"
    "v_mfma_f32_16x16x32_bf16 a[128:131], v[180:183], v[144:147], a[128:131]\n"
    "s_add_u32 s64, s64, 0xc000\n"
    "v_add_f32 v121, v121, v90\n"
    "v_add_f32 v117, v117, v52\n"
    "v_mfma_f32_16x16x32_bf16 a[132:135], v[180:183], v[148:151], a[132:135]\n"
    "s_add_u32 s62, s62, 0x6000\n"
    "v_exp_f32 v62, v62\n"
    "v_mfma_f32_16x16x32_bf16 a[132:135], v[176:179], v[132:135], a[132:135]\n"
    "s_add_u32 s63, s63, 0x6000\n"
    "v_add_f32 v122, v122, v91\n"
    "v_add_f32 v118, v118, v53\n"
    "v_mfma_f32_16x16x32_bf16 a[136:139], v[176:179], v[136:139], a[136:139]\n"
    "s_add_u32 s60, s60, 0x6000\n"
    "ds_read_b128 v[172:175], v201 offset:21504\n"
    "v_add_f32 v125, v125, v92\n"
    "v_mfma_f32_16x16x32_bf16 a[136:139], v[180:183], v[152:155], a[136:139]\n"
    "s_add_u32 s61, s61, 0x6000\n"
    "v_add_f32 v117, v117, v54\n"
    "v_add_f32 v126, v126, v93\n"
    "v_mfma_f32_16x16x32_bf16 a[140:143], v[180:183], v[156:159], a[140:143]\n"
    "s_add_u32 s66, s66, s74\n"
    "v_exp_f32 v63, v63\n"
    "v_mfma_f32_16x16x32_bf16 a[140:143], v[176:179], v[140:143], a[140:143]\n"
    "s_waitcnt lgkmcnt(2)\n"
    "v_add_f32 v118, v118, v55\n"
    "v_add_f32 v125, v125, v94\n"
    "v_mfma_f32_16x16x32_bf16 a[156:159], v[160:163], v[140:143], a[156:159]\n"
    "s_min_u32 s66, s66, 33\n"
    "ds_read_b128 v[176:179], v201 offset:22528\n"
    "v_add_f32 v121, v121, v56\n"
    "v_mfma_f32_16x16x32_bf16 a[156:159], v[164:167], v[156:159], a[156:159]\n"
    "ds_read_b128 v[180:183], v201 offset:23552\n"
    "v_add_f32 v126, v126, v95\n"
    "v_mfma_f32_16x16x32_bf16 a[152:155], v[164:167], v[152:155], a[152:155]\n"
    "s_waitcnt vmcnt(3)\n"
    "v_add_f32 v122, v122, v57\n"
    "v_mfma_f32_16x16x32_bf16 a[152:155], v[160:163], v[136:139], a[152:155]\n"
    "v_cmp_ne_u32 s[80:81], s67, v219\n"
    "v_mfma_f32_16x16x32_bf16 a[148:151], v[160:163], v[132:135], a[148:151]\n"
    "v_cmp_gt_u32 vcc, s66, v218\n"
    "v_mfma_f32_16x16x32_bf16 a[148:151], v[164:167], v[148:151], a[148:151]\n"
    "s_or_b64 vcc, vcc, s[80:81]\n"
    "v_mfma_f32_16x16x32_bf16 a[144:147], v[164:167], v[144:147], a[144:147]\n"
    "s_cbranch_vccnz LSPRE4\n"
    "LSPB4:\n"
    "v_mfma_f32_16x16x32_bf16 a[144:147], v[160:163], v[128:131], a[144:147]\n"
    "s_waitcnt lgkmcnt(0)\n"
    "v_mfma_f32_16x16x32_bf16 a[160:163], v[168:171], v[128:131], a[160:163]\n"
    "s_barrier\n"
    "v_mfma_f32_16x16x32_bf16 a[160:163], v[172:175], v[144:147], a[160:163]\n"
    "buffer_store_dwordx2 v[220:221], v214, s[44:47], 0 offen sc1\n"
    "v_mfma_f32_16x16x32_bf16 a[164:167], v[172:175], v[148:151], a[164:167]\n"
    "ds_read_b128 v[160:163], v200\n"
    "ds_read_u16_d16_hi v223, v209 offset:6144\n"
    "v_mfma_f32_16x16x32_bf16 a[164:167], v[168:171], v[132:135], a[164:167]\n"
    "ds_read_u16_d16_hi v222, v209\n"
    "v_add_f32 v121, v121, v58\n"
    "v_mfma_f32_16x16x32_bf16 a[168:171], v[168:171], v[136:139], a[168:171]\n"
    "ds_read_b128 v[164:167], v200 offset:1024\n"
    "ds_read_b128 v[232:235], v205 offset:24576\n"
    "v_mfma_f32_16x16x32_bf16 a[168:171], v[172:175], v[152:155], a[168:171]\n"
    "v_add_f32 v122, v122, v59\n"
    "ds_read_b128 v[236:239], v206 offset:24576\n"
    "v_mfma_f32_16x16x32_bf16 a[172:175], v[172:175], v[156:159], a[172:175]\n"
    "v_add_f32 v125, v125, v60\n"
    "v_add_f32 v126, v126, v61\n"
    "v_mfma_f32_16x16x32_bf16 a[172:175], v[168:171], v[140:143], a[172:175]\n"
    "v_add_f32 v125, v125, v62\n"
    "v_add_f32 v126, v126, v63\n"
    "v_mfma_f32_16x16x32_bf16 a[188:191], v[176:179], v[140:143], a[188:191]\n"
    "ds_read_b128 v[168:171], v200 offset:2048\n"
    "v_cvt_pk_bf16_f32 v140, v44, v45\n"
    "v_mfma_f32_16x16x32_bf16 a[188:191], v[180:183], v[156:159], a[188:191]\n"
    "v_cvt_pk_bf16_f32 v141, v46, v47\n"
    "v_mfma_f32_16x16x32_bf16 a[184:187], v[176:179], v[136:139], a[184:187]\n"
    "v_cvt_pk_bf16_f32 v136, v40, v41\n"
    "v_cvt_pk_bf16_f32 v137, v42, v43\n"
    "v_mfma_f32_16x16x32_bf16 a[184:187], v[180:183], v[152:155], a[184:187]\n"
    "v_mfma_f32_16x16x32_bf16 a[180:183], v[176:179], v[132:135], a[180:183]\n"
    "ds_read_b128 v[172:175], v200 offset:3072\n"
    "s_waitcnt lgkmcnt(2)\n"
    "v_mfma_f32_16x16x32_bf16 a[180:183], v[180:183], v[148:151], a[180:183]\n"
    "v_mul_f32 v226, s75, v223\n"
    "v_fma_f32 v227, -v222, v226, v217\n"
    "v_mfma_f32_16x16x32_bf16 a[176:179], v[176:179], v[128:131], a[176:179]\n"
    "v_cvt_f32_i32 v232, v232\n"
    "v_cvt_f32_i32 v233, v233\n"
    "v_mfma_f32_16x16x32_bf16 a[176:179], v[180:183], v[144:147], a[176:179]\n"
    "v_cvt_f32_i32 v234, v234\n"
    "v_cvt_f32_i32 v235, v235\n"
    "LM0:\n"
    "v_mfma_f32_16x16x32_bf16 v[64:67], v[160:163], a[192:195], v[96:99]\n"
    "ds_read_b128 v[176:179], v200 offset:4096\n"
    "v_cvt_f32_i32 v236, v236\n"
    "v_mfma_f32_16x16x32_bf16 v[64:67], v[164:167], a[196:199], v[64:67]\n"
    "s_waitcnt lgkmcnt(1)\n"
    "v_cvt_f32_i32 v237, v237\n"
    "v_cvt_f32_i32 v238, v238\n"
    "v_mfma_f32_16x16x32_bf16 v[64:67], v[168:171], a[200:203], v[64:67]\n"
    "v_cvt_f32_i32 v239, v239\n"
    "v_mul_f32_dpp v232, v226, v232 row_newbcast:0 row_mask:0xf bank_mask:0xf\n"
    "v_mfma_f32_16x16x32_bf16 v[68:71], v[168:171], a[224:227], v[100:103]\n"
    "v_mul_f32_dpp v233, v226, v233 row_newbcast:1 row_mask:0xf bank_mask:0xf\n"
    "v_mul_f32_dpp v234, v226, v234 row_newbcast:2 row_mask:0xf bank_mask:0xf\n"
    "v_mfma_f32_16x16x32_bf16 v[68:71], v[164:167], a[220:223], v[68:71]\n"
    "ds_read_b128 v[180:183], v200 offset:5120\n"
    "v_mul_f32_dpp v235, v226, v235 row_newbcast:3 row_mask:0xf bank_mask:0xf\n"
    "v_mfma_f32_16x16x32_bf16 v[68:71], v[160:163], a[216:219], v[68:71]\n"
    "v_mul_f32_dpp v236, v226, v236 row_newbcast:4 row_mask:0xf bank_mask:0xf\n"
    "v_mul_f32_dpp v237, v226, v237 row_newbcast:5 row_mask:0xf bank_mask:0xf\n"
    "v_mfma_f32_16x16x32_bf16 v[72:75], v[160:163], a[240:243], v[104:107]\n"
    "v_mul_f32_dpp v238, v226, v238 row_newbcast:6 row_mask:0xf bank_mask:0xf\n"
    "v_mul_f32_dpp v239, v226, v239 row_newbcast:7 row_mask:0xf bank_mask:0xf\n"
    "v_mfma_f32_16x16x32_bf16 v[72:75], v[164:167], a[244:247], v[72:75]\n"
    "v_add_f32_dpp v232, v227, v232 row_newbcast:0 row_mask:0xf bank_mask:0xf\n"
    "v_add_f32_dpp v233, v227, v233 row_newbcast:1 row_mask:0xf bank_mask:0xf\n"
    "v_mfma_f32_16x16x32_bf16 v[72:75], v[168:171], a[248:251], v[72:75]\n"
    "v_add_f32_dpp v234, v227, v234 row_newbcast:2 row_mask:0xf bank_mask:0xf\n"
    "v_add_f32_dpp v235, v227, v235 row_newbcast:3 row_mask:0xf bank_mask:0xf\n"
    "v_mfma_f32_16x16x32_bf16 v[76:79], v[168:171], v[16:19], v[108:111]\n"
    "v_add_f32_dpp v236, v227, v236 row_newbcast:4 row_mask:0xf bank_mask:0xf\n"
    "v_add_f32_dpp v237, v227, v237 row_newbcast:5 row_mask:0xf bank_mask:0xf\n"
    "v_mfma_f32_16x16x32_bf16 v[76:79], v[164:167], v[12:15], v[76:79]\n"
    "v_add_f32_dpp v238, v227, v238 row_newbcast:6 row_mask:0xf bank_mask:0xf\n"
    "v_add_f32_dpp v239, v227, v239 row_newbcast:7 row_mask:0xf bank_mask:0xf\n"
    "v_mfma_f32_16x16x32_bf16 v[76:79], v[160:163], v[8:11], v[76:79]\n"
    "s_waitcnt lgkmcnt(1)\n"
    "v_cvt_pk_bf16_f32 v232, v232, v233\n"
    "v_cvt_pk_bf16_f32 v233, v234, v235\n"
    "v_mfma_f32_16x16x32_bf16 v[76:79], v[172:175], v[20:23], v[76:79]\n"
    "ds_read_b128 v[160:163], v200 offset:9216\n"
    "v_cvt_pk_bf16_f32 v234, v236, v237\n"
    "v_mfma_f32_16x16x32_bf16 v[76:79], v[176:179], v[24:27], v[76:79]\n"
    "s_waitcnt lgkmcnt(1)\n"
    "v_cvt_pk_bf16_f32 v235, v238, v239\n"
    "v_cvt_pk_bf16_f32 v132, v36, v37\n"
    "v_mfma_f32_16x16x32_bf16 v[76:79], v[180:183], v[28:31], v[76:79]\n"
    "buffer_store_dwordx4 v[232:235], v211, s[36:39], s62 offen sc1\n"
    "v_cvt_pk_bf16_f32 v133, v38, v39\n"
    "v_cvt_pk_bf16_f32 v128, v32, v33\n"
    "v_cvt_pk_bf16_f32 v129, v34, v35\n"
    "v_mfma_f32_16x16x32_bf16 v[72:75], v[180:183], v[4:7], v[72:75]\n"
    "ds_read_b128 v[164:167], v200 offset:10240\n"
    "v_cvt_pk_bf16_f32 v142, v60, v61\n"
    "v_mfma_f32_16x16x32_bf16 v[72:75], v[176:179], v[0:3], v[72:75]\n"
    "v_cvt_pk_bf16_f32 v143, v62, v63\n"
    "v_cvt_pk_bf16_f32 v138, v56, v57\n"
    "v_mfma_f32_16x16x32_bf16 v[72:75], v[172:175], a[252:255], v[72:75]\n"
    "v_cvt_pk_bf16_f32 v139, v58, v59\n"
    "v_cvt_pk_bf16_f32 v134, v52, v53\n"
    "v_mfma_f32_16x16x32_bf16 v[68:71], v[172:175], a[228:231], v[68:71]\n"
    "ds_read_b128 v[168:171], v200 offset:11264\n"
    "v_cvt_pk_bf16_f32 v135, v54, v55\n"
    "v_mfma_f32_16x16x32_bf16 v[68:71], v[176:179], a[232:235], v[68:71]\n"
    "v_cvt_pk_bf16_f32 v130, v48, v49\n"
    "v_cvt_pk_bf16_f32 v131, v50, v51\n"
    "v_mfma_f32_16x16x32_bf16 v[68:71], v[180:183], a[236:239], v[68:71]\n"
    "v_mfma_f32_16x16x32_bf16 v[64:67], v[180:183], a[212:215], v[64:67]\n"
    "v_mfma_f32_16x16x32_bf16 v[64:67], v[176:179], a[208:211], v[64:67]\n"
    "v_mfma_f32_16x16x32_bf16 v[64:67], v[172:175], a[204:207], v[64:67]\n"
    "s_waitcnt lgkmcnt(1)\n"
    "v_mfma_f32_16x16x32_bf16 v[80:83], v[160:163], a[204:207], v[96:99]\n"
    "ds_read_b128 v[172:175], v200 offset:6144\n"
    "v_mfma_f32_16x16x32_bf16 v[80:83], v[164:167], a[208:211], v[80:83]\n"
    "s_waitcnt lgkmcnt(1)\n"
    "v_mfma_f32_16x16x32_bf16 v[80:83], v[168:171], a[212:215], v[80:83]\n"
    "v_mfma_f32_16x16x32_bf16 v[84:87], v[168:171], a[236:239], v[100:103]\n"
    "ds_read_b128 v[176:179], v200 offset:7168\n"
    "v_mfma_f32_16x16x32_bf16 v[84:87], v[164:167], a[232:235], v[84:87]\n"
    "v_exp_f32 v64, v64\n"
    "v_mfma_f32_16x16x32_bf16 v[84:87], v[160:163], a[228:231], v[84:87]\n"
    "v_exp_f32 v65, v65\n"
    "v_mfma_f32_16x16x32_bf16 v[88:91], v[160:163], a[252:255], v[104:107]\n"
    "ds_read_b128 v[180:183], v200 offset:8192\n"
    "v_cvt_pk_bf16_f32 v144, v64, v65\n"
    "v_mfma_f32_16x16x32_bf16 v[88:91], v[164:167], v[0:3], v[88:91]\n"
    "s_add_u32 s65, s60, 0\n"
    "v_exp_f32 v66, v66\n"
    "v_mfma_f32_16x16x32_bf16 v[88:91], v[168:171], v[4:7], v[88:91]\n"
    "s_mov_b32 m0, 0x6000\n"
    "v_exp_f32 v67, v67\n"
    "v_mfma_f32_16x16x32_bf16 v[92:95], v[168:171], v[28:31], v[108:111]\n"
    "v_exp_f32 v68, v68\n"
    "v_mfma_f32_16x16x32_bf16 v[92:95], v[164:167], v[24:27], v[92:95]\n"
    "buffer_load_dwordx4 v200, s[36:39], s65 offen sc1 lds\n"
    "v_mfma_f32_16x16x32_bf16 v[92:95], v[160:163], v[20:23], v[92:95]\n"
    "s_waitcnt lgkmcnt(1)\n"
    "v_exp_f32 v69, v69\n"
    "v_mfma_f32_16x16x32_bf16 v[92:95], v[172:175], v[8:11], v[92:95]\n"
    "ds_read_b128 v[160:163], v200 offset:12288\n"
    "v_cvt_pk_bf16_f32 v145, v66, v67\n"
    "v_mfma_f32_16x16x32_bf16 v[92:95], v[176:179], v[12:15], v[92:95]\n"
    "s_waitcnt lgkmcnt(1)\n"
    "v_exp_f32 v70, v70\n"
    "v_mfma_f32_16x16x32_bf16 v[92:95], v[180:183], v[16:19], v[92:95]\n"
    "buffer_load_dwordx4 v200, s[36:39], s65 offen offset:1024 sc1 lds\n"
    "v_mfma_f32_16x16x32_bf16 v[88:91], v[180:183], a[248:251], v[88:91]\n"
    "ds_read_b128 v[164:167], v200 offset:13312\n"
    "v_cvt_pk_bf16_f32 v148, v68, v69\n"
    "v_mfma_f32_16x16x32_bf16 v[88:91], v[176:179], a[244:247], v[88:91]\n"
    "v_exp_f32 v71, v71\n"
    "v_mfma_f32_16x16x32_bf16 v[88:91], v[172:175], a[240:243], v[88:91]\n"
    "v_exp_f32 v72, v72\n"
    "v_mfma_f32_16x16x32_bf16 v[84:87], v[172:175], a[216:219], v[84:87]\n"
    "ds_read_b128 v[168:171], v200 offset:14336\n"
    "v_cvt_pk_bf16_f32 v149, v70, v71\n"
    "v_mfma_f32_16x16x32_bf16 v[84:87], v[176:179], a[220:223], v[84:87]\n"
    "buffer_load_dwordx4 v200, s[36:39], s65 offen offset:2048 sc1 lds\n"
    "v_mfma_f32_16x16x32_bf16 v[84:87], v[180:183], a[224:227], v[84:87]\n"
    "v_exp_f32 v73, v73\n"
    "v_mfma_f32_16x16x32_bf16 v[80:83], v[180:183], a[200:203], v[80:83]\n"
    "v_exp_f32 v74, v74\n"
    "v_mfma_f32_16x16x32_bf16 v[80:83], v[176:179], a[196:199], v[80:83]\n"
    "v_exp_f32 v75, v75\n"
    "v_mfma_f32_16x16x32_bf16 v[80:83], v[172:175], a[192:195], v[80:83]\n"
    "s_waitcnt lgkmcnt(1)\n"
    "buffer_load_dwordx4 v200, s[36:39], s65 offen offset:3072 sc1 lds\n"
    "v_mfma_f32_16x16x32_bf16 v[32:35], v[160:163], a[192:195], v[96:99]\n"
    "ds_read_b128 v[172:175], v200 offset:15360\n"
    "v_cvt_pk_bf16_f32 v152, v72, v73\n"
    "v_mfma_f32_16x16x32_bf16 v[32:35], v[164:167], a[196:199], v[32:35]\n"
    "s_waitcnt lgkmcnt(1)\n"
    "s_add_u32 s65, s60, 0x1000\n"
    "v_cvt_pk_bf16_f32 v153, v74, v75\n"
    "v_mfma_f32_16x16x32_bf16 v[32:35], v[168:171], a[200:203], v[32:35]\n"
    "s_mov_b32 m0, 0x7000\n"
    "v_exp_f32 v76, v76\n"
    "v_mfma_f32_16x16x32_bf16 v[36:39], v[168:171], a[224:227], v[100:103]\n"
    "ds_read_b128 v[176:179], v200 offset:16384\n"
    "v_add_f32 v113, v113, v64\n"
    "v_mfma_f32_16x16x32_bf16 v[36:39], v[164:167], a[220:223], v[36:39]\n"
    "buffer_load_dwordx4 v200, s[36:39], s65 offen sc1 lds\n"
    "v_mfma_f32_16x16x32_bf16 v[36:39], v[160:163], a[216:219], v[36:39]\n"
    "v_exp_f32 v77, v77\n"
    "v_mfma_f32_16x16x32_bf16 v[40:43], v[160:163], a[240:243], v[104:107]\n"
    "ds_read_b128 v[180:183], v200 offset:17408\n"
    "v_cvt_pk_bf16_f32 v156, v76, v77\n"
    "v_mfma_f32_16x16x32_bf16 v[40:43], v[164:167], a[244:247], v[40:43]\n"
    "v_exp_f32 v78, v78\n"
    "v_mfma_f32_16x16x32_bf16 v[40:43], v[168:171], a[248:251], v[40:43]\n"
    "buffer_load_dwordx4 v200, s[36:39], s65 offen offset:1024 sc1 lds\n"
    "v_mfma_f32_16x16x32_bf16 v[44:47], v[168:171], v[16:19], v[108:111]\n"
    "v_exp_f32 v79, v79\n"
    "v_mfma_f32_16x16x32_bf16 v[44:47], v[164:167], v[12:15], v[44:47]\n"
    "v_exp_f32 v80, v80\n"
    "v_mfma_f32_16x16x32_bf16 v[44:47], v[160:163], v[8:11], v[44:47]\n"
    "s_waitcnt lgkmcnt(1)\n"
    "v_exp_f32 v81, v81\n"
    "v_mfma_f32_16x16x32_bf16 v[44:47], v[172:175], v[20:23], v[44:47]\n"
    "ds_read_b128 v[160:163], v200 offset:21504\n"
    "v_cvt_pk_bf16_f32 v157, v78, v79\n"
    "v_mfma_f32_16x16x32_bf16 v[44:47], v[176:179], v[24:27], v[44:47]\n"
    "s_waitcnt lgkmcnt(1)\n"
    "buffer_load_dwordx4 v200, s[36:39], s65 offen offset:2048 sc1 lds\n"
    "v_mfma_f32_16x16x32_bf16 v[44:47], v[180:183], v[28:31], v[44:47]\n"
    "v_exp_f32 v82, v82\n"
    "v_mfma_f32_16x16x32_bf16 v[40:43], v[180:183], v[4:7], v[40:43]\n"
    "ds_read_b128 v[164:167], v200 offset:22528\n"
    "v_cvt_pk_bf16_f32 v146, v80, v81\n"
    "v_mfma_f32_16x16x32_bf16 v[40:43], v[176:179], v[0:3], v[40:43]\n"
    "v_exp_f32 v83, v83\n"
    "v_mfma_f32_16x16x32_bf16 v[40:43], v[172:175], a[252:255], v[40:43]\n"
    "buffer_load_dwordx4 v200, s[36:39], s65 offen offset:3072 sc1 lds\n"
    "v_mfma_f32_16x16x32_bf16 v[36:39], v[172:175], a[228:231], v[36:39]\n"
    "s_add_u32 s65, s61, 0\n"
    "ds_read_b128 v[168:171], v200 offset:23552\n"
    "v_cvt_pk_bf16_f32 v147, v82, v83\n"
    "v_mfma_f32_16x16x32_bf16 v[36:39], v[176:179], a[232:235], v[36:39]\n"
    "s_mov_b32 m0, 0xc000\n"
    "v_exp_f32 v84, v84\n"
    "v_mfma_f32_16x16x32_bf16 v[36:39], v[180:183], a[236:239], v[36:39]\n"
    "v_exp_f32 v85, v85\n"
    "v_mfma_f32_16x16x32_bf16 v[32:35], v[180:183], a[212:215], v[32:35]\n"
    "buffer_load_dwordx4 v200, s[40:43], s65 offen sc1 lds\n"
    "v_mfma_f32_16x16x32_bf16 v[32:35], v[176:179], a[208:211], v[32:35]\n"
    "v_exp_f32 v86, v86\n"
    "v_mfma_f32_16x16x32_bf16 v[32:35], v[172:175], a[204:207], v[32:35]\n"
    "s_waitcnt lgkmcnt(1)\n"
    "v_exp_f32 v87, v87\n"
    "v_mfma_f32_16x16x32_bf16 v[48:51], v[160:163], a[204:207], v[96:99]\n"
    "ds_read_b128 v[172:175], v200 offset:18432\n"
    "v_cvt_pk_bf16_f32 v150, v84, v85\n"
    "v_mfma_f32_16x16x32_bf16 v[48:51], v[164:167], a[208:211], v[48:51]\n"
    "s_waitcnt lgkmcnt(1)\n"
    "buffer_load_dwordx4 v200, s[40:43], s65 offen offset:1024 sc1 lds\n"
    "v_mfma_f32_16x16x32_bf16 v[48:51], v[168:171], a[212:215], v[48:51]\n"
    "v_exp_f32 v88, v88\n"
    "v_mfma_f32_16x16x32_bf16 v[52:55], v[168:171], a[236:239], v[100:103]\n"
    "ds_read_b128 v[176:179], v200 offset:19456\n"
    "v_cvt_pk_bf16_f32 v151, v86, v87\n"
    "v_mfma_f32_16x16x32_bf16 v[52:55], v[164:167], a[232:235], v[52:55]\n"
    "v_exp_f32 v89, v89\n"
    "v_mfma_f32_16x16x32_bf16 v[52:55], v[160:163], a[228:231], v[52:55]\n"
    "buffer_load_dwordx4 v200, s[40:43], s65 offen offset:2048 sc1 lds\n"
    "v_mfma_f32_16x16x32_bf16 v[56:59], v[160:163], a[252:255], v[104:107]\n"
    "ds_read_b128 v[180:183], v200 offset:20480\n"
    "v_cvt_pk_bf16_f32 v154, v88, v89\n"
    "v_mfma_f32_16x16x32_bf16 v[56:59], v[164:167], v[0:3], v[56:59]\n"
    "v_exp_f32 v90, v90\n"
    "v_mfma_f32_16x16x32_bf16 v[56:59], v[168:171], v[4:7], v[56:59]\n"
    "v_exp_f32 v91, v91\n"
    "v_mfma_f32_16x16x32_bf16 v[60:63], v[168:171], v[28:31], v[108:111]\n"
    "buffer_load_dwordx4 v200, s[40:43], s65 offen offset:3072 sc1 lds\n"
    "v_mfma_f32_16x16x32_bf16 v[60:63], v[164:167], v[24:27], v[60:63]\n"
    "s_add_u32 s65, s61, 0x1000\n"
    "v_exp_f32 v92, v92\n"
    "v_mfma_f32_16x16x32_bf16 v[60:63], v[160:163], v[20:23], v[60:63]\n"
    "s_waitcnt lgkmcnt(1)\n"
    "s_mov_b32 m0, 0xd000\n"
    "v_cvt_pk_bf16_f32 v155, v90, v91\n"
    "v_mfma_f32_16x16x32_bf16 v[60:63], v[172:175], v[8:11], v[60:63]\n"
    "ds_read_b128 v[160:163], v201 offset:24576\n"
    "v_add_f32 v114, v114, v65\n"
    "v_mfma_f32_16x16x32_bf16 v[60:63], v[176:179], v[12:15], v[60:63]\n"
    "s_waitcnt lgkmcnt(1)\n"
    "buffer_load_dwordx4 v200, s[40:43], s65 offen sc1 lds\n"
    "v_mfma_f32_16x16x32_bf16 v[60:63], v[180:183], v[16:19], v[60:63]\n"
    "v_exp_f32 v93, v93\n"
    "v_mfma_f32_16x16x32_bf16 v[56:59], v[180:183], a[248:251], v[56:59]\n"
    "ds_read_b128 v[164:167], v201 offset:25600\n"
    "v_cvt_pk_bf16_f32 v158, v92, v93\n"
    "v_mfma_f32_16x16x32_bf16 v[56:59], v[176:179], a[244:247], v[56:59]\n"
    "v_exp_f32 v94, v94\n"
    "v_mfma_f32_16x16x32_bf16 v[56:59], v[172:175], a[240:243], v[56:59]\n"
    "buffer_load_dwordx4 v200, s[40:43], s65 offen offset:1024 sc1 lds\n"
    "v_mfma_f32_16x16x32_bf16 v[52:55], v[172:175], a[216:219], v[52:55]\n"
    "ds_read_b128 v[168:171], v201 offset:26624\n"
    "v_add_f32 v113, v113, v66\n"
    "v_mfma_f32_16x16x32_bf16 v[52:55], v[176:179], a[220:223], v[52:55]\n"
    "v_exp_f32 v95, v95\n"
    "v_mfma_f32_16x16x32_bf16 v[52:55], v[180:183], a[224:227], v[52:55]\n"
    "v_cvt_pk_bf16_f32 v159, v94, v95\n"
    "v_add_f32 v114, v114, v67\n"
    "v_mfma_f32_16x16x32_bf16 v[48:51], v[180:183], a[200:203], v[48:51]\n"
    "buffer_load_dwordx4 v200, s[40:43], s65 offen offset:2048 sc1 lds\n"
    "v_mfma_f32_16x16x32_bf16 v[48:51], v[176:179], a[196:199], v[48:51]\n"
    "v_exp_f32 v32, v32\n"
    "v_mfma_f32_16x16x32_bf16 v[48:51], v[172:175], a[192:195], v[48:51]\n"
    "s_waitcnt lgkmcnt(1)\n"
    "v_exp_f32 v33, v33\n"
    "v_mfma_f32_16x16x32_bf16 a[0:3], v[160:163], v[128:131], a[0:3]\n"
    "ds_read_b128 v[172:175], v201 offset:27648\n"
    "ds_read_b128 v[176:179], v201 offset:28672\n"
    "v_mfma_f32_16x16x32_bf16 a[0:3], v[164:167], v[144:147], a[0:3]\n"
    "buffer_load_dwordx4 v200, s[40:43], s65 offen offset:3072 sc1 lds\n"
    "v_mfma_f32_16x16x32_bf16 a[4:7], v[160:163], v[132:135], a[4:7]\n"
    "v_exp_f32 v34, v34\n"
    "v_mfma_f32_16x16x32_bf16 a[4:7], v[164:167], v[148:151], a[4:7]\n"
    "v_exp_f32 v35, v35\n"
    "v_mfma_f32_16x16x32_bf16 a[8:11], v[160:163], v[136:139], a[8:11]\n"
    "ds_read_b128 v[180:183], v201 offset:29696\n"
    "v_add_f32 v117, v117, v68\n"
    "v_mfma_f32_16x16x32_bf16 a[8:11], v[164:167], v[152:155], a[8:11]\n"
    "v_exp_f32 v36, v36\n"
    "v_mfma_f32_16x16x32_bf16 a[12:15], v[160:163], v[140:143], a[12:15]\n"
    "v_exp_f32 v37, v37\n"
    "v_mfma_f32_16x16x32_bf16 a[12:15], v[164:167], v[156:159], a[12:15]\n"
    "s_waitcnt lgkmcnt(2)\n"
    "v_exp_f32 v38, v38\n"
    "v_mfma_f32_16x16x32_bf16 a[28:31], v[168:171], v[140:143], a[28:31]\n"
    "ds_read_b128 v[160:163], v201 offset:30720\n"
    "v_add_f32 v118, v118, v69\n"
    "v_mfma_f32_16x16x32_bf16 a[28:31], v[172:175], v[156:159], a[28:31]\n"
    "v_exp_f32 v39, v39\n"
    "v_mfma_f32_16x16x32_bf16 a[24:27], v[172:175], v[152:155], a[24:27]\n"
    "v_exp_f32 v40, v40\n"
    "v_mfma_f32_16x16x32_bf16 a[24:27], v[168:171], v[136:139], a[24:27]\n"
    "v_exp_f32 v41, v41\n"
    "v_mfma_f32_16x16x32_bf16 a[20:23], v[168:171], v[132:135], a[20:23]\n"
    "ds_read_b128 v[164:167], v201 offset:31744\n"
    "v_add_f32 v117, v117, v70\n"
    "v_mfma_f32_16x16x32_bf16 a[20:23], v[172:175], v[148:151], a[20:23]\n"
    "v_exp_f32 v42, v42\n"
    "v_mfma_f32_16x16x32_bf16 a[16:19], v[172:175], v[144:147], a[16:19]\n"
    "v_exp_f32 v43, v43\n"
    "v_mfma_f32_16x16x32_bf16 a[16:19], v[168:171], v[128:131], a[16:19]\n"
    "s_waitcnt lgkmcnt(2)\n"
    "v_exp_f32 v44, v44\n"
    "v_mfma_f32_16x16x32_bf16 a[32:35], v[176:179], v[128:131], a[32:35]\n"
    "ds_read_b128 v[168:171], v201 offset:32768\n"
    "v_add_f32 v113, v113, v32\n"
    "v_mfma_f32_16x16x32_bf16 a[32:35], v[180:183], v[144:147], a[32:35]\n"
    "buffer_load_dwordx2 v[218:219], v213, s[44:47], 0 offen sc1\n"
    "v_mfma_f32_16x16x32_bf16 a[36:39], v[180:183], v[148:151], a[36:39]\n"
    "v_exp_f32 v45, v45\n"
    "v_mfma_f32_16x16x32_bf16 a[36:39], v[176:179], v[132:135], a[36:39]\n"
    "v_exp_f32 v46, v46\n"
    "v_mfma_f32_16x16x32_bf16 a[40:43], v[176:179], v[136:139], a[40:43]\n"
    "ds_read_b128 v[172:175], v201 offset:33792\n"
    "v_add_f32 v118, v118, v71\n"
    "v_mfma_f32_16x16x32_bf16 a[40:43], v[180:183], v[152:155], a[40:43]\n"
    "v_exp_f32 v47, v47\n"
    "v_mfma_f32_16x16x32_bf16 a[44:47], v[180:183], v[156:159], a[44:47]\n"
    "v_exp_f32 v48, v48\n"
    "v_mfma_f32_16x16x32_bf16 a[44:47], v[176:179], v[140:143], a[44:47]\n"
    "s_waitcnt lgkmcnt(2)\n"
    "v_exp_f32 v49, v49\n"
    "v_mfma_f32_16x16x32_bf16 a[60:63], v[160:163], v[140:143], a[60:63]\n"
    "ds_read_b128 v[176:179], v201 offset:34816\n"
    "v_add_f32 v114, v114, v33\n"
    "v_mfma_f32_16x16x32_bf16 a[60:63], v[164:167], v[156:159], a[60:63]\n"
    "v_exp_f32 v50, v50\n"
    "v_mfma_f32_16x16x32_bf16 a[56:59], v[164:167], v[152:155], a[56:59]\n"
    "v_exp_f32 v51, v51\n"
    "v_mfma_f32_16x16x32_bf16 a[56:59], v[160:163], v[136:139], a[56:59]\n"
    "v_exp_f32 v52, v52\n"
    "v_mfma_f32_16x16x32_bf16 a[52:55], v[160:163], v[132:135], a[52:55]\n"
    "ds_read_b128 v[180:183], v201 offset:35840\n"
    "v_add_f32 v121, v121, v72\n"
    "v_mfma_f32_16x16x32_bf16 a[52:55], v[164:167], v[148:151], a[52:55]\n"
    "v_add_f32 v113, v113, v34\n"
    "v_add_f32 v122, v122, v73\n"
    "v_mfma_f32_16x16x32_bf16 a[48:51], v[164:167], v[144:147], a[48:51]\n"
    "v_exp_f32 v53, v53\n"
    "v_mfma_f32_16x16x32_bf16 a[48:51], v[160:163], v[128:131], a[48:51]\n"
    "s_waitcnt lgkmcnt(2)\n"
    "v_add_f32 v114, v114, v35\n"
    "v_add_f32 v121, v121, v74\n"
    "v_mfma_f32_16x16x32_bf16 a[64:67], v[168:171], v[128:131], a[64:67]\n"
    "ds_read_b128 v[160:163], v201 offset:36864\n"
    "v_add_f32 v117, v117, v36\n"
    "v_mfma_f32_16x16x32_bf16 a[64:67], v[172:175], v[144:147], a[64:67]\n"
    "v_exp_f32 v54, v54\n"
    "v_mfma_f32_16x16x32_bf16 a[68:71], v[172:175], v[148:151], a[68:71]\n"
    "v_add_f32 v122, v122, v75\n"
    "v_add_f32 v118, v118, v37\n"
    "v_mfma_f32_16x16x32_bf16 a[68:71], v[168:171], v[132:135], a[68:71]\n"
    "s_add_u32 m0, s51, s95\n"
    "v_add_f32 v125, v125, v76\n"
    "v_add_f32 v117, v117, v38\n"
    "v_mfma_f32_16x16x32_bf16 a[72:75], v[168:171], v[136:139], a[72:75]\n"
    "ds_read_b128 v[164:167], v201 offset:37888\n"
    "v_add_f32 v126, v126, v77\n"
    "v_mfma_f32_16x16x32_bf16 a[72:75], v[172:175], v[152:155], a[72:75]\n"
    "buffer_load_dwordx4 v202, s[32:35], s64 offen lds\n"
    "v_mfma_f32_16x16x32_bf16 a[76:79], v[172:175], v[156:159], a[76:79]\n"
    "v_exp_f32 v55, v55\n"
    "v_mfma_f32_16x16x32_bf16 a[76:79], v[168:171], v[140:143], a[76:79]\n"
    "s_waitcnt lgkmcnt(2)\n"
    "v_add_f32 v118, v118, v39\n"
    "v_add_f32 v125, v125, v78\n"
    "v_mfma_f32_16x16x32_bf16 a[92:95], v[176:179], v[140:143], a[92:95]\n"
    "ds_read_b128 v[168:171], v201 offset:38912\n"
    "v_add_f32 v121, v121, v40\n"
    "v_mfma_f32_16x16x32_bf16 a[92:95], v[180:183], v[156:159], a[92:95]\n"
    "buffer_load_dwordx4 v203, s[32:35], s64 offen offset:1024 lds\n"
    "v_mfma_f32_16x16x32_bf16 a[88:91], v[180:183], v[152:155], a[88:91]\n"
    "v_exp_f32 v56, v56\n"
    "v_mfma_f32_16x16x32_bf16 a[88:91], v[176:179], v[136:139], a[88:91]\n"
    "v_add_f32 v126, v126, v79\n"
    "v_add_f32 v122, v122, v41\n"
    "v_mfma_f32_16x16x32_bf16 a[84:87], v[176:179], v[132:135], a[84:87]\n"
    "ds_read_b128 v[172:175], v201 offset:39936\n"
    "v_add_f32 v113, v113, v80\n"
    "v_mfma_f32_16x16x32_bf16 a[84:87], v[180:183], v[148:151], a[84:87]\n"
    "buffer_load_dwordx4 v204, s[32:35], s64 offen offset:2048 lds\n"
    "v_mfma_f32_16x16x32_bf16 a[80:83], v[180:183], v[144:147], a[80:83]\n"
    "v_add_f32 v121, v121, v42\n"
    "v_add_f32 v114, v114, v81\n"
    "v_mfma_f32_16x16x32_bf16 a[80:83], v[176:179], v[128:131], a[80:83]\n"
    "s_waitcnt lgkmcnt(2)\n"
    "v_exp_f32 v57, v57\n"
    "v_mfma_f32_16x16x32_bf16 a[96:99], v[160:163], v[128:131], a[96:99]\n"
    "s_mov_b32 s95, s94\n"
    "ds_read_b128 v[176:179], v201 offset:40960\n"
    "v_add_f32 v122, v122, v43\n"
    "v_mfma_f32_16x16x32_bf16 a[96:99], v[164:167], v[144:147], a[96:99]\n"
    "s_sub_u32 s94, s94, 0x3000\n"
    "v_add_f32 v113, v113, v82\n"
    "v_add_f32 v125, v125, v44\n"
    "v_mfma_f32_16x16x32_bf16 a[100:103], v[164:167], v[148:151], a[100:103]\n"
    "s_min_u32 s94, s94, 0x6000\n"
    "v_exp_f32 v58, v58\n"
    "v_mfma_f32_16x16x32_bf16 a[100:103], v[160:163], v[132:135], a[100:103]\n"
    "s_sub_u32 s93, s94, s95\n"
    "v_add_f32 v114, v114, v83\n"
    "v_add_f32 v126, v126, v45\n"
    "v_mfma_f32_16x16x32_bf16 a[104:107], v[160:163], v[136:139], a[104:107]\n"
    "ds_read_b128 v[180:183], v201 offset:41984\n"
    "v_add_f32 v117, v117, v84\n"
    "v_mfma_f32_16x16x32_bf16 a[104:107], v[164:167], v[152:155], a[104:107]\n"
    "v_add_f32 v125, v125, v46\n"
    "v_add_f32 v118, v118, v85\n"
    "v_mfma_f32_16x16x32_bf16 a[108:111], v[164:167], v[156:159], a[108:111]\n"
    "v_exp_f32 v59, v59\n"
    "v_mfma_f32_16x16x32_bf16 a[108:111], v[160:163], v[140:143], a[108:111]\n"
    "s_waitcnt lgkmcnt(2)\n"
    "v_add_f32 v126, v126, v47\n"
    "v_add_f32 v117, v117, v86\n"
    "v_mfma_f32_16x16x32_bf16 a[124:127], v[168:171], v[140:143], a[124:127]\n"
    "ds_read_b128 v[160:163], v201 offset:43008\n"
    "v_add_f32 v113, v113, v48\n"
    "v_mfma_f32_16x16x32_bf16 a[124:127], v[172:175], v[156:159], a[124:127]\n"
    "v_exp_f32 v60, v60\n"
    "v_mfma_f32_16x16x32_bf16 a[120:123], v[172:175], v[152:155], a[120:123]\n"
    "v_add_f32 v118, v118, v87\n"
    "v_add_f32 v114, v114, v49\n"
    "v_mfma_f32_16x16x32_bf16 a[120:123], v[168:171], v[136:139], a[120:123]\n"
    "v_add_u32 v205, s93, v205\n"
    "v_add_f32 v121, v121, v88\n"
    "v_mfma_f32_16x16x32_bf16 a[116:119], v[168:171], v[132:135], a[116:119]\n"
    "ds_read_b128 v[164:167], v201 offset:44032\n"
    "v_add_f32 v113, v113, v50\n"
    "v_mfma_f32_16x16x32_bf16 a[116:119], v[172:175], v[148:151], a[116:119]\n"
    "v_exp_f32 v61, v61\n"
    "v_mfma_f32_16x16x32_bf16 a[112:115], v[172:175], v[144:147], a[112:115]\n"
    "v_add_u32 v220, 1, v220\n"
    "v_add_u32 v206, s93, v206\n"
    "v_mfma_f32_16x16x32_bf16 a[112:115], v[168:171], v[128:131], a[112:115]\n"
    "s_waitcnt lgkmcnt(2)\n"
    "v_add_f32 v122, v122, v89\n"
    "v_add_f32 v114, v114, v51\n"
    "v_mfma_f32_16x16x32_bf16 a[128:131], v[176:179], v[128:131], a[128:131]\n"
    "ds_read_b128 v[168:171], v201 offset:45056\n"
    "v_add_f32 v121, v121, v90\n"
    "v_mfma_f32_16x16x32_bf16 a[128:131], v[180:183], v[144:147], a[128:131]\n"
    "s_add_u32 s64, s64, 0xc000\n"
    "v_add_f32 v117, v117, v52\n"
    "v_add_f32 v122, v122, v91\n"
    "v_mfma_f32_16x16x32_bf16 a[132:135], v[180:183], v[148:151], a[132:135]\n"
    "s_add_u32 s62, s62, 0x6000\n"
    "v_exp_f32 v62, v62\n"
    "v_mfma_f32_16x16x32_bf16 a[132:135], v[176:179], v[132:135], a[132:135]\n"
    "s_add_u32 s63, s63, 0x6000\n"
    "v_add_f32 v118, v118, v53\n"
    "v_add_f32 v125, v125, v92\n"
    "v_mfma_f32_16x16x32_bf16 a[136:139], v[176:179], v[136:139], a[136:139]\n"
    "s_add_u32 s60, s60, 0x6000\n"
    "ds_read_b128 v[172:175], v201 offset:46080\n"
    "v_add_f32 v117, v117, v54\n"
    "v_mfma_f32_16x16x32_bf16 a[136:139], v[180:183], v[152:155], a[136:139]\n"
    "s_add_u32 s61, s61, 0x6000\n"
    "v_exp_f32 v63, v63\n"
    "v_mfma_f32_16x16x32_bf16 a[140:143], v[180:183], v[156:159], a[140:143]\n"
    "s_add_u32 s66, s66, s74\n"
    "v_add_f32 v126, v126, v93\n"
    "v_add_f32 v118, v118, v55\n"
    "v_mfma_f32_16x16x32_bf16 a[140:143], v[176:179], v[140:143], a[140:143]\n"
    "s_waitcnt lgkmcnt(2)\n"
    "v_add_f32 v125, v125, v94\n"
    "v_add_f32 v121, v121, v56\n"
    "v_mfma_f32_16x16x32_bf16 a[156:159], v[160:163], v[140:143], a[156:159]\n"
    "s_min_u32 s66, s66, 33\n"
    "ds_read_b128 v[176:179], v201 offset:47104\n"
    "v_add_f32 v126, v126, v95\n"
    "v_mfma_f32_16x16x32_bf16 a[156:159], v[164:167], v[156:159], a[156:159]\n"
    "ds_read_b128 v[180:183], v201 offset:48128\n"
    "v_add_f32 v122, v122, v57\n"
    "v_mfma_f32_16x16x32_bf16 a[152:155], v[164:167], v[152:155], a[152:155]\n"
    "s_waitcnt vmcnt(3)\n"
    "v_mfma_f32_16x16x32_bf16 a[152:155], v[160:163], v[136:139], a[152:155]\n"
    "v_cmp_ne_u32 s[80:81], s67, v219\n"
    "v_mfma_f32_16x16x32_bf16 a[148:151], v[160:163], v[132:135], a[148:151]\n"
    "v_cmp_gt_u32 vcc, s66, v218\n"
    "v_mfma_f32_16x16x32_bf16 a[148:151], v[164:167], v[148:151], a[148:151]\n"
    "s_or_b64 vcc, vcc, s[80:81]\n"
    "v_mfma_f32_16x16x32_bf16 a[144:147], v[164:167], v[144:147], a[144:147]\n"
    "s_cbranch_vccnz LSPRE3\n"
    "LSPB3:\n"
    "v_mfma_f32_16x16x32_bf16 a[144:147], v[160:163], v[128:131], a[144:147]\n"
    "s_waitcnt lgkmcnt(0)\n"
    "v_mfma_f32_16x16x32_bf16 a[160:163], v[168:171], v[128:131], a[160:163]\n"
    "s_barrier\n"
    "LMID:\n"
    "v_mfma_f32_16x16x32_bf16 a[160:163], v[172:175], v[144:147], a[160:163]\n"
    "buffer_store_dwordx2 v[220:221], v214, s[44:47], 0 offen sc1\n"
    "v_mfma_f32_16x16x32_bf16 a[164:167], v[172:175], v[148:151], a[164:167]\n"
    "ds_read_b128 v[160:163], v200 offset:24576\n"
    "ds_read_u16_d16_hi v223, v209 offset:6336\n"
    "v_mfma_f32_16x16x32_bf16 a[164:167], v[168:171], v[132:135], a[164:167]\n"
    "ds_read_u16_d16_hi v222, v209 offset:192\n"
    "v_add_f32 v121, v121, v58\n"
    "v_mfma_f32_16x16x32_bf16 a[168:171], v[168:171], v[136:139], a[168:171]\n"
    "ds_read_b128 v[164:167], v200 offset:25600\n"
    "ds_read_b128 v[232:235], v205 offset:24576\n"
    "v_mfma_f32_16x16x32_bf16 a[168:171], v[172:175], v[152:155], a[168:171]\n"
    "v_add_f32 v122, v122, v59\n"
    "ds_read_b128 v[236:239], v206 offset:24576\n"
    "v_mfma_f32_16x16x32_bf16 a[172:175], v[172:175], v[156:159], a[172:175]\n"
    "v_add_f32 v125, v125, v60\n"
    "v_add_f32 v126, v126, v61\n"
    "v_mfma_f32_16x16x32_bf16 a[172:175], v[168:171], v[140:143], a[172:175]\n"
    "v_add_f32 v125, v125, v62\n"
    "v_add_f32 v126, v126, v63\n"
    "v_mfma_f32_16x16x32_bf16 a[188:191], v[176:179], v[140:143], a[188:191]\n"
    "ds_read_b128 v[168:171], v200 offset:26624\n"
    "v_cvt_pk_bf16_f32 v140, v44, v45\n"
    "v_mfma_f32_16x16x32_bf16 a[188:191], v[180:183], v[156:159], a[188:191]\n"
    "v_cvt_pk_bf16_f32 v141, v46, v47\n"
    "v_mfma_f32_16x16x32_bf16 a[184:187], v[176:179], v[136:139], a[184:187]\n"
    "v_cvt_pk_bf16_f32 v136, v40, v41\n"
    "v_cvt_pk_bf16_f32 v137, v42, v43\n"
    "v_mfma_f32_16x16x32_bf16 a[184:187], v[180:183], v[152:155], a[184:187]\n"
    "v_mfma_f32_16x16x32_bf16 a[180:183], v[176:179], v[132:135], a[180:183]\n"
    "s_add_u32 s59, s59, 1\n"
    "ds_read_b128 v[172:175], v200 offset:27648\n"
    "s_waitcnt lgkmcnt(2)\n"
    "v_mfma_f32_16x16x32_bf16 a[180:183], v[180:183], v[148:151], a[180:183]\n"
    "v_mul_f32 v226, s75, v223\n"
    "v_fma_f32 v227, -v222, v226, v217\n"
    "v_mfma_f32_16x16x32_bf16 a[176:179], v[176:179], v[128:131], a[176:179]\n"
    "s_cmp_lt_u32 s59, 15\n"
    "v_cvt_f32_i32 v232, v232\n"
    "v_cvt_f32_i32 v233, v233\n"
    "v_mfma_f32_16x16x32_bf16 a[176:179], v[180:183], v[144:147], a[176:179]\n"
    "s_cbranch_scc1 LLOOP\n"
    "LTAIL:\n"
    "s_waitcnt lgkmcnt(0)\n"
    "s_nop 1\n"
    "v_lshlrev_b32 v196, 3, v231\n"
    "buffer_load_dword v197, v196, s[52:55], s91 offen\n"
    "LDF1:\n"
    "v_mfma_f32_16x16x32_bf16 v[64:67], v[160:163], a[192:195], v[96:99]\n"
    "ds_read_b128 v[176:179], v200 offset:28672\n"
    "v_cvt_pk_bf16_f32 v128, v32, v33\n"
    "v_mfma_f32_16x16x32_bf16 v[64:67], v[164:167], a[196:199], v[64:67]\n"
    "s_add_u32 m0, s71, 0\n"
    "v_cvt_pk_bf16_f32 v129, v34, v35\n"
    "v_cvt_pk_bf16_f32 v132, v36, v37\n"
    "v_mfma_f32_16x16x32_bf16 v[68:71], v[164:167], a[220:223], v[100:103]\n"
    "buffer_load_dwordx4 v200, s[36:39], s60 offen sc1 lds\n"
    "v_mfma_f32_16x16x32_bf16 v[68:71], v[160:163], a[216:219], v[68:71]\n"
    "v_cvt_pk_bf16_f32 v133, v38, v39\n"
    "v_cvt_pk_bf16_f32 v136, v40, v41\n"
    "v_mfma_f32_16x16x32_bf16 v[72:75], v[160:163], a[240:243], v[104:107]\n"
    "ds_read_b128 v[180:183], v200 offset:29696\n"
    "v_cvt_pk_bf16_f32 v137, v42, v43\n"
    "v_mfma_f32_16x16x32_bf16 v[72:75], v[164:167], a[244:247], v[72:75]\n"
    "v_cvt_pk_bf16_f32 v130, v48, v49\n"
    "v_cvt_pk_bf16_f32 v131, v50, v51\n"
    "v_mfma_f32_16x16x32_bf16 v[76:79], v[164:167], v[12:15], v[108:111]\n"
    "buffer_load_dwordx4 v200, s[36:39], s60 offen offset:1024 sc1 lds\n"
    "v_mfma_f32_16x16x32_bf16 v[76:79], v[160:163], v[8:11], v[76:79]\n"
    "v_cvt_pk_bf16_f32 v134, v52, v53\n"
    "v_cvt_pk_bf16_f32 v135, v54, v55\n"
    "v_mfma_f32_16x16x32_bf16 v[76:79], v[168:171], v[16:19], v[76:79]\n"
    "ds_read_b128 v[160:163], v200 offset:34816\n"
    "v_cvt_pk_bf16_f32 v138, v56, v57\n"
    "v_mfma_f32_16x16x32_bf16 v[76:79], v[172:175], v[20:23], v[76:79]\n"
    "v_cvt_pk_bf16_f32 v139, v58, v59\n"
    "v_cvt_pk_bf16_f32 v142, v60, v61\n"
    "v_mfma_f32_16x16x32_bf16 v[72:75], v[172:175], a[252:255], v[72:75]\n"
    "buffer_load_dwordx4 v200, s[36:39], s60 offen offset:2048 sc1 lds\n"
    "v_mfma_f32_16x16x32_bf16 v[72:75], v[168:171], a[248:251], v[72:75]\n"
    "v_cvt_pk_bf16_f32 v143, v62, v63\n"
    "v_add_f32 v113, v113, v114\n"
    "v_mfma_f32_16x16x32_bf16 v[68:71], v[168:171], a[224:227], v[68:71]\n"
    "ds_read_b128 v[164:167], v200 offset:35840\n"
    "v_add_f32 v117, v117, v118\n"
    "v_mfma_f32_16x16x32_bf16 v[68:71], v[172:175], a[228:231], v[68:71]\n"
    "v_add_f32 v121, v121, v122\n"
    "v_add_f32 v125, v125, v126\n"
    "v_mfma_f32_16x16x32_bf16 v[64:67], v[172:175], a[204:207], v[64:67]\n"
    "buffer_load_dwordx4 v200, s[36:39], s60 offen offset:3072 sc1 lds\n"
    "v_mfma_f32_16x16x32_bf16 v[64:67], v[168:171], a[200:203], v[64:67]\n"
    "s_waitcnt lgkmcnt(2)\n"
    "v_mov_b32 v115, v113\n"
    "v_mov_b32 v119, v117\n"
    "v_mfma_f32_16x16x32_bf16 v[64:67], v[176:179], a[208:211], v[64:67]\n"
    "s_add_u32 s65, s60, 0x1000\n"
    "ds_read_b128 v[168:171], v200 offset:32768\n"
    "v_mov_b32 v123, v121\n"
    "v_mfma_f32_16x16x32_bf16 v[64:67], v[180:183], a[212:215], v[64:67]\n"
    "s_add_u32 m0, s71, 0x1000\n"
    "v_mov_b32 v127, v125\n"
    "v_permlane32_swap_b32 v113, v115\n"
    "v_mfma_f32_16x16x32_bf16 v[68:71], v[180:183], a[236:239], v[68:71]\n"
    "buffer_load_dwordx4 v200, s[36:39], s65 offen sc1 lds\n"
    "v_mfma_f32_16x16x32_bf16 v[68:71], v[176:179], a[232:235], v[68:71]\n"
    "v_permlane32_swap_b32 v117, v119\n"
    "v_permlane32_swap_b32 v121, v123\n"
    "v_mfma_f32_16x16x32_bf16 v[72:75], v[176:179], v[0:3], v[72:75]\n"
    "ds_read_b128 v[172:175], v200 offset:33792\n"
    "v_permlane32_swap_b32 v125, v127\n"
    "v_mfma_f32_16x16x32_bf16 v[72:75], v[180:183], v[4:7], v[72:75]\n"
    "v_add_f32 v113, v113, v115\n"
    "v_add_f32 v117, v117, v119\n"
    "v_mfma_f32_16x16x32_bf16 v[76:79], v[180:183], v[28:31], v[76:79]\n"
    "buffer_load_dwordx4 v200, s[36:39], s65 offen offset:1024 sc1 lds\n"
    "v_mfma_f32_16x16x32_bf16 v[76:79], v[176:179], v[24:27], v[76:79]\n"
    "s_waitcnt lgkmcnt(2)\n"
    "v_add_f32 v121, v121, v123\n"
    "v_add_f32 v125, v125, v127\n"
    "v_mfma_f32_16x16x32_bf16 v[92:95], v[160:163], v[24:27], v[108:111]\n"
    "ds_read_b128 v[176:179], v200 offset:30720\n"
    "v_mov_b32 v115, v113\n"
    "v_mfma_f32_16x16x32_bf16 v[92:95], v[164:167], v[28:31], v[92:95]\n"
    "s_add_u32 m0, s71, 0x12000\n"
    "v_exp_f32 v64, v64\n"
    "v_mfma_f32_16x16x32_bf16 v[88:91], v[164:167], v[4:7], v[104:107]\n"
    "buffer_load_dwordx4 v200, s[40:43], s61 offen sc1 lds\n"
    "v_mfma_f32_16x16x32_bf16 v[88:91], v[160:163], v[0:3], v[88:91]\n"
    "v_exp_f32 v65, v65\n"
    "v_mfma_f32_16x16x32_bf16 v[84:87], v[160:163], a[232:235], v[100:103]\n"
    "ds_read_b128 v[180:183], v200 offset:31744\n"
    "v_cvt_pk_bf16_f32 v144, v64, v65\n"
    "v_mfma_f32_16x16x32_bf16 v[84:87], v[164:167], a[236:239], v[84:87]\n"
    "v_exp_f32 v66, v66\n"
    "v_mfma_f32_16x16x32_bf16 v[80:83], v[164:167], a[212:215], v[96:99]\n"
    "buffer_load_dwordx4 v200, s[40:43], s61 offen offset:1024 sc1 lds\n"
    "v_mfma_f32_16x16x32_bf16 v[80:83], v[160:163], a[208:211], v[80:83]\n"
    "s_waitcnt lgkmcnt(2)\n"
    "v_exp_f32 v67, v67\n"
    "v_mfma_f32_16x16x32_bf16 v[80:83], v[168:171], a[200:203], v[80:83]\n"
    "ds_read_b128 v[160:163], v200 offset:36864\n"
    "v_cvt_pk_bf16_f32 v145, v66, v67\n"
    "v_mfma_f32_16x16x32_bf16 v[80:83], v[172:175], a[204:207], v[80:83]\n"
    "v_exp_f32 v68, v68\n"
    "v_mfma_f32_16x16x32_bf16 v[84:87], v[172:175], a[228:231], v[84:87]\n"
    "buffer_load_dwordx4 v200, s[40:43], s61 offen offset:2048 sc1 lds\n"
    "v_mfma_f32_16x16x32_bf16 v[84:87], v[168:171], a[224:227], v[84:87]\n"
    "v_exp_f32 v69, v69\n"
    "v_mfma_f32_16x16x32_bf16 v[88:91], v[168:171], a[248:251], v[88:91]\n"
    "ds_read_b128 v[164:167], v200 offset:37888\n"
    "v_cvt_pk_bf16_f32 v148, v68, v69\n"
    "v_mfma_f32_16x16x32_bf16 v[88:91], v[172:175], a[252:255], v[88:91]\n"
    "v_exp_f32 v70, v70\n"
    "v_mfma_f32_16x16x32_bf16 v[92:95], v[172:175], v[20:23], v[92:95]\n"
    "buffer_load_dwordx4 v200, s[40:43], s61 offen offset:3072 sc1 lds\n"
    "v_mfma_f32_16x16x32_bf16 v[92:95], v[168:171], v[16:19], v[92:95]\n"
    "s_waitcnt lgkmcnt(2)\n"
    "v_exp_f32 v71, v71\n"
    "v_mfma_f32_16x16x32_bf16 v[92:95], v[176:179], v[8:11], v[92:95]\n"
    "s_add_u32 s65, s61, 0x1000\n"
    "ds_read_b128 v[168:171], v200 offset:38912\n"
    "v_cvt_pk_bf16_f32 v149, v70, v71\n"
    "v_mfma_f32_16x16x32_bf16 v[92:95], v[180:183], v[12:15], v[92:95]\n"
    "s_add_u32 m0, s71, 0x13000\n"
    "v_exp_f32 v72, v72\n"
    "v_mfma_f32_16x16x32_bf16 v[88:91], v[180:183], a[244:247], v[88:91]\n"
    "buffer_load_dwordx4 v200, s[40:43], s65 offen sc1 lds\n"
    "v_mfma_f32_16x16x32_bf16 v[88:91], v[176:179], a[240:243], v[88:91]\n"
    "v_exp_f32 v73, v73\n"
    "v_mfma_f32_16x16x32_bf16 v[84:87], v[176:179], a[216:219], v[84:87]\n"
    "ds_read_b128 v[172:175], v200 offset:39936\n"
    "v_cvt_pk_bf16_f32 v152, v72, v73\n"
    "v_mfma_f32_16x16x32_bf16 v[84:87], v[180:183], a[220:223], v[84:87]\n"
    "v_exp_f32 v74, v74\n"
    "v_mfma_f32_16x16x32_bf16 v[80:83], v[180:183], a[196:199], v[80:83]\n"
    "buffer_load_dwordx4 v200, s[40:43], s65 offen offset:1024 sc1 lds\n"
    "v_mfma_f32_16x16x32_bf16 v[80:83], v[176:179], a[192:195], v[80:83]\n"
    "s_waitcnt lgkmcnt(2)\n"
    "v_exp_f32 v75, v75\n"
    "v_mfma_f32_16x16x32_bf16 v[32:35], v[160:163], a[192:195], v[96:99]\n"
    "ds_read_b128 v[176:179], v200 offset:40960\n"
    "v_cvt_pk_bf16_f32 v153, v74, v75\n"
    "v_mfma_f32_16x16x32_bf16 v[32:35], v[164:167], a[196:199], v[32:35]\n"
    "v_exp_f32 v76, v76\n"
    "v_mfma_f32_16x16x32_bf16 v[36:39], v[164:167], a[220:223], v[100:103]\n"
    "v_exp_f32 v77, v77\n"
    "v_mfma_f32_16x16x32_bf16 v[36:39], v[160:163], a[216:219], v[36:39]\n"
    "v_exp_f32 v78, v78\n"
    "v_mfma_f32_16x16x32_bf16 v[40:43], v[160:163], a[240:243], v[104:107]\n"
    "ds_read_b128 v[180:183], v200 offset:41984\n"
    "v_cvt_pk_bf16_f32 v156, v76, v77\n"
    "v_mfma_f32_16x16x32_bf16 v[40:43], v[164:167], a[244:247], v[40:43]\n"
    "v_exp_f32 v79, v79\n"
    "v_mfma_f32_16x16x32_bf16 v[44:47], v[164:167], v[12:15], v[108:111]\n"
    "v_exp_f32 v80, v80\n"
    "v_mfma_f32_16x16x32_bf16 v[44:47], v[160:163], v[8:11], v[44:47]\n"
    "s_waitcnt lgkmcnt(2)\n"
    "v_exp_f32 v81, v81\n"
    "v_mfma_f32_16x16x32_bf16 v[44:47], v[168:171], v[16:19], v[44:47]\n"
    "ds_read_b128 v[160:163], v200 offset:47104\n"
    "v_cvt_pk_bf16_f32 v157, v78, v79\n"
    "v_mfma_f32_16x16x32_bf16 v[44:47], v[172:175], v[20:23], v[44:47]\n"
    "v_exp_f32 v82, v82\n"
    "v_mfma_f32_16x16x32_bf16 v[40:43], v[172:175], a[252:255], v[40:43]\n"
    "v_exp_f32 v83, v83\n"
    "v_mfma_f32_16x16x32_bf16 v[40:43], v[168:171], a[248:251], v[40:43]\n"
    "v_exp_f32 v84, v84\n"
    "v_mfma_f32_16x16x32_bf16 v[36:39], v[168:171], a[224:227], v[36:39]\n"
    "ds_read_b128 v[164:167], v200 offset:48128\n"
    "v_cvt_pk_bf16_f32 v146, v80, v81\n"
    "v_mfma_f32_16x16x32_bf16 v[36:39], v[172:175], a[228:231], v[36:39]\n"
    "v_exp_f32 v85, v85\n"
    "v_mfma_f32_16x16x32_bf16 v[32:35], v[172:175], a[204:207], v[32:35]\n"
    "v_exp_f32 v86, v86\n"
    "v_mfma_f32_16x16x32_bf16 v[32:35], v[168:171], a[200:203], v[32:35]\n"
    "s_waitcnt lgkmcnt(2)\n"
    "v_exp_f32 v87, v87\n"
    "v_mfma_f32_16x16x32_bf16 v[32:35], v[176:179], a[208:211], v[32:35]\n"
    "ds_read_b128 v[168:171], v200 offset:45056\n"
    "v_cvt_pk_bf16_f32 v147, v82, v83\n"
    "v_mfma_f32_16x16x32_bf16 v[32:35], v[180:183], a[212:215], v[32:35]\n"
    "v_exp_f32 v88, v88\n"
    "v_mfma_f32_16x16x32_bf16 v[36:39], v[180:183], a[236:239], v[36:39]\n"
    "v_exp_f32 v89, v89\n"
    "v_mfma_f32_16x16x32_bf16 v[36:39], v[176:179], a[232:235], v[36:39]\n"
    "v_exp_f32 v90, v90\n"
    "v_mfma_f32_16x16x32_bf16 v[40:43], v[176:179], v[0:3], v[40:43]\n"
    "ds_read_b128 v[172:175], v200 offset:46080\n"
    "v_cvt_pk_bf16_f32 v150, v84, v85\n"
    "v_mfma_f32_16x16x32_bf16 v[40:43], v[180:183], v[4:7], v[40:43]\n"
    "v_exp_f32 v91, v91\n"
    "v_mfma_f32_16x16x32_bf16 v[44:47], v[180:183], v[28:31], v[44:47]\n"
    "v_exp_f32 v92, v92\n"
    "v_mfma_f32_16x16x32_bf16 v[44:47], v[176:179], v[24:27], v[44:47]\n"
    "s_waitcnt lgkmcnt(2)\n"
    "v_exp_f32 v93, v93\n"
    "v_mfma_f32_16x16x32_bf16 v[60:63], v[160:163], v[24:27], v[108:111]\n"
    "ds_read_b128 v[176:179], v200 offset:43008\n"
    "v_cvt_pk_bf16_f32 v151, v86, v87\n"
    "v_mfma_f32_16x16x32_bf16 v[60:63], v[164:167], v[28:31], v[60:63]\n"
    "v_exp_f32 v94, v94\n"
    "v_mfma_f32_16x16x32_bf16 v[56:59], v[164:167], v[4:7], v[104:107]\n"
    "v_exp_f32 v95, v95\n"
    "v_mfma_f32_16x16x32_bf16 v[56:59], v[160:163], v[0:3], v[56:59]\n"
    "s_cbranch_execz LDRYR\n"
    "v_exp_f32 v32, v32\n"
    "v_mfma_f32_16x16x32_bf16 v[52:55], v[160:163], a[232:235], v[100:103]\n"
    "ds_read_b128 v[180:183], v200 offset:44032\n"
    "v_cvt_pk_bf16_f32 v154, v88, v89\n"
    "v_mfma_f32_16x16x32_bf16 v[52:55], v[164:167], a[236:239], v[52:55]\n"
    "v_exp_f32 v33, v33\n"
    "v_mfma_f32_16x16x32_bf16 v[48:51], v[164:167], a[212:215], v[96:99]\n"
    "s_waitcnt vmcnt(6)\n"
    "v_exp_f32 v34, v34\n"
    "v_mfma_f32_16x16x32_bf16 v[48:51], v[160:163], a[208:211], v[48:51]\n"
    "s_waitcnt lgkmcnt(2)\n"
    "v_exp_f32 v35, v35\n"
    "v_mfma_f32_16x16x32_bf16 v[48:51], v[168:171], a[200:203], v[48:51]\n"
    "s_barrier\n"
    "v_exp_f32 v36, v36\n"
    "LDF4:\n"
    "v_mfma_f32_16x16x32_bf16 v[48:51], v[172:175], a[204:207], v[48:51]\n"
    "ds_read_b128 v[160:163], v200\n"
    "v_cvt_pk_bf16_f32 v155, v90, v91\n"
    "v_mfma_f32_16x16x32_bf16 v[52:55], v[172:175], a[228:231], v[52:55]\n"
    "v_exp_f32 v37, v37\n"
    "v_mfma_f32_16x16x32_bf16 v[52:55], v[168:171], a[224:227], v[52:55]\n"
    "v_exp_f32 v38, v38\n"
    "v_mfma_f32_16x16x32_bf16 v[56:59], v[168:171], a[248:251], v[56:59]\n"
    "ds_read_b128 v[164:167], v200 offset:1024\n"
    "v_cvt_pk_bf16_f32 v158, v92, v93\n"
    "v_mfma_f32_16x16x32_bf16 v[56:59], v[172:175], a[252:255], v[56:59]\n"
    "buffer_load_dwordx2 v[218:219], v213, s[44:47], 0 offen sc1\n"
    "v_mfma_f32_16x16x32_bf16 v[60:63], v[172:175], v[20:23], v[60:63]\n"
    "v_exp_f32 v39, v39\n"
    "v_mfma_f32_16x16x32_bf16 v[60:63], v[168:171], v[16:19], v[60:63]\n"
    "s_waitcnt lgkmcnt(2)\n"
    "v_exp_f32 v40, v40\n"
    "v_mfma_f32_16x16x32_bf16 v[60:63], v[176:179], v[8:11], v[60:63]\n"
    "ds_read_b128 v[168:171], v200 offset:2048\n"
    "v_cvt_pk_bf16_f32 v159, v94, v95\n"
    "v_mfma_f32_16x16x32_bf16 v[60:63], v[180:183], v[12:15], v[60:63]\n"
    "v_exp_f32 v41, v41\n"
    "v_mfma_f32_16x16x32_bf16 v[56:59], v[180:183], a[244:247], v[56:59]\n"
    "v_exp_f32 v42, v42\n"
    "v_mfma_f32_16x16x32_bf16 v[56:59], v[176:179], a[240:243], v[56:59]\n"
    "v_exp_f32 v43, v43\n"
    "v_mfma_f32_16x16x32_bf16 v[52:55], v[176:179], a[216:219], v[52:55]\n"
    "ds_read_b128 v[172:175], v200 offset:3072\n"
    "v_cvt_pk_bf16_f32 v32, v32, v33\n"
    "v_mfma_f32_16x16x32_bf16 v[52:55], v[180:183], a[220:223], v[52:55]\n"
    "v_exp_f32 v44, v44\n"
    "v_mfma_f32_16x16x32_bf16 v[48:51], v[180:183], a[196:199], v[48:51]\n"
    "v_exp_f32 v45, v45\n"
    "v_mfma_f32_16x16x32_bf16 v[48:51], v[176:179], a[192:195], v[48:51]\n"
    "s_waitcnt lgkmcnt(2)\n"
    "v_exp_f32 v46, v46\n"
    "v_mfma_f32_16x16x32_bf16 v[64:67], v[160:163], a[192:195], v[96:99]\n"
    "ds_read_b128 v[176:179], v200 offset:4096\n"
    "v_cvt_pk_bf16_f32 v33, v34, v35\n"
    "v_mfma_f32_16x16x32_bf16 v[64:67], v[164:167], a[196:199], v[64:67]\n"
    "v_exp_f32 v47, v47\n"
    "v_mfma_f32_16x16x32_bf16 v[68:71], v[164:167], a[220:223], v[100:103]\n"
    "v_cvt_pk_bf16_f32 v36, v36, v37\n"
    "v_cvt_pk_bf16_f32 v37, v38, v39\n"
    "v_mfma_f32_16x16x32_bf16 v[68:71], v[160:163], a[216:219], v[68:71]\n"
    "v_cvt_pk_bf16_f32 v40, v40, v41\n"
    "v_cvt_pk_bf16_f32 v41, v42, v43\n"
    "v_mfma_f32_16x16x32_bf16 v[72:75], v[160:163], a[240:243], v[104:107]\n"
    "ds_read_b128 v[180:183], v200 offset:5120\n"
    "v_cvt_pk_bf16_f32 v44, v44, v45\n"
    "v_mfma_f32_16x16x32_bf16 v[72:75], v[164:167], a[244:247], v[72:75]\n"
    "v_cvt_pk_bf16_f32 v45, v46, v47\n"
    "v_mov_b32 v119, v117\n"
    "v_mfma_f32_16x16x32_bf16 v[76:79], v[164:167], v[12:15], v[108:111]\n"
    "v_exp_f32 v48, v48\n"
    "v_mfma_f32_16x16x32_bf16 v[76:79], v[160:163], v[8:11], v[76:79]\n"
    "s_waitcnt lgkmcnt(2)\n"
    "v_exp_f32 v49, v49\n"
    "v_mfma_f32_16x16x32_bf16 v[76:79], v[168:171], v[16:19], v[76:79]\n"
    "ds_read_b128 v[160:163], v200 offset:10240\n"
    "v_cvt_pk_bf16_f32 v34, v48, v49\n"
    "v_mfma_f32_16x16x32_bf16 v[76:79], v[172:175], v[20:23], v[76:79]\n"
    "v_exp_f32 v50, v50\n"
    "v_mfma_f32_16x16x32_bf16 v[72:75], v[172:175], a[252:255], v[72:75]\n"
    "v_exp_f32 v51, v51\n"
    "v_mfma_f32_16x16x32_bf16 v[72:75], v[168:171], a[248:251], v[72:75]\n"
    "v_exp_f32 v52, v52\n"
    "v_mfma_f32_16x16x32_bf16 v[68:71], v[168:171], a[224:227], v[68:71]\n"
    "ds_read_b128 v[164:167], v200 offset:11264\n"
    "v_cvt_pk_bf16_f32 v35, v50, v51\n"
    "v_mfma_f32_16x16x32_bf16 v[68:71], v[172:175], a[228:231], v[68:71]\n"
    "v_exp_f32 v53, v53\n"
    "v_mfma_f32_16x16x32_bf16 v[64:67], v[172:175], a[204:207], v[64:67]\n"
    "v_exp_f32 v54, v54\n"
    "v_mfma_f32_16x16x32_bf16 v[64:67], v[168:171], a[200:203], v[64:67]\n"
    "s_waitcnt lgkmcnt(2)\n"
    "v_exp_f32 v55, v55\n"
    "v_mfma_f32_16x16x32_bf16 v[64:67], v[176:179], a[208:211], v[64:67]\n"
    "s_add_u32 s66, s66, s74\n"
    "ds_read_b128 v[168:171], v200 offset:8192\n"
    "v_cvt_pk_bf16_f32 v38, v52, v53\n"
    "v_mfma_f32_16x16x32_bf16 v[64:67], v[180:183], a[212:215], v[64:67]\n"
    "s_add_u32 s60, s72, 0xbd000\n"
    "v_exp_f32 v56, v56\n"
    "v_mfma_f32_16x16x32_bf16 v[68:71], v[180:183], a[236:239], v[68:71]\n"
    "s_add_u32 s61, s71, 0xba000\n"
    "v_exp_f32 v57, v57\n"
    "v_mfma_f32_16x16x32_bf16 v[68:71], v[176:179], a[232:235], v[68:71]\n"
    "v_exp_f32 v58, v58\n"
    "v_mfma_f32_16x16x32_bf16 v[72:75], v[176:179], v[0:3], v[72:75]\n"
    "ds_read_b128 v[172:175], v200 offset:9216\n"
    "v_cvt_pk_bf16_f32 v39, v54, v55\n"
    "v_mfma_f32_16x16x32_bf16 v[72:75], v[180:183], v[4:7], v[72:75]\n"
    "s_cbranch_execz LDRYR\n"
    "v_exp_f32 v59, v59\n"
    "v_mfma_f32_16x16x32_bf16 v[76:79], v[180:183], v[28:31], v[76:79]\n"
    "s_waitcnt vmcnt(0)\n"
    "v_cmp_ne_u32 s[80:81], s67, v219\n"
    "v_cvt_pk_bf16_f32 v42, v56, v57\n"
    "v_mfma_f32_16x16x32_bf16 v[76:79], v[176:179], v[24:27], v[76:79]\n"
    "s_waitcnt lgkmcnt(2)\n"
    "v_cmp_gt_u32 vcc, s66, v218\n"
    "v_cvt_pk_bf16_f32 v43, v58, v59\n"
    "v_mfma_f32_16x16x32_bf16 v[92:95], v[160:163], v[24:27], v[108:111]\n"
    "ds_read_b128 v[176:179], v200 offset:6144\n"
    "v_mov_b32 v123, v121\n"
    "v_mfma_f32_16x16x32_bf16 v[92:95], v[164:167], v[28:31], v[92:95]\n"
    "s_or_b64 vcc, vcc, s[80:81]\n"
    "v_exp_f32 v64, v64\n"
    "v_mfma_f32_16x16x32_bf16 v[88:91], v[164:167], v[4:7], v[104:107]\n"
    "s_cbranch_vccnz LSPRE5\n"
    "LSPB5:\n"
    "v_exp_f32 v65, v65\n"
    "v_mfma_f32_16x16x32_bf16 v[88:91], v[160:163], v[0:3], v[88:91]\n"
    "v_exp_f32 v66, v66\n"
    "v_mfma_f32_16x16x32_bf16 v[84:87], v[160:163], a[232:235], v[100:103]\n"
    "s_barrier\n"
    "ds_read_b128 v[180:183], v200 offset:7168\n"
    "v_cvt_pk_bf16_f32 v64, v64, v65\n"
    "LDF7:\n"
    "v_mfma_f32_16x16x32_bf16 v[84:87], v[164:167], a[236:239], v[84:87]\n"
    "s_add_u32 m0, s72, 0x6000\n"
    "v_exp_f32 v67, v67\n"
    "v_mfma_f32_16x16x32_bf16 v[80:83], v[164:167], a[212:215], v[96:99]\n"
    "buffer_load_dwordx4 v200, s[36:39], s60 offen sc1 lds\n"
    "v_mfma_f32_16x16x32_bf16 v[80:83], v[160:163], a[208:211], v[80:83]\n"
    "s_waitcnt lgkmcnt(2)\n"
    "v_exp_f32 v68, v68\n"
    "v_mfma_f32_16x16x32_bf16 v[80:83], v[168:171], a[200:203], v[80:83]\n"
    "ds_read_b128 v[160:163], v200 offset:12288\n"
    "v_cvt_pk_bf16_f32 v65, v66, v67\n"
    "v_mfma_f32_16x16x32_bf16 v[80:83], v[172:175], a[204:207], v[80:83]\n"
    "buffer_load_dwordx4 v200, s[36:39], s60 offen offset:1024 sc1 lds\n"
    "v_mfma_f32_16x16x32_bf16 v[84:87], v[172:175], a[228:231], v[84:87]\n"
    "v_exp_f32 v69, v69\n"
    "v_mfma_f32_16x16x32_bf16 v[84:87], v[168:171], a[224:227], v[84:87]\n"
    "buffer_load_dwordx4 v200, s[36:39], s60 offen offset:2048 sc1 lds\n"
    "v_mfma_f32_16x16x32_bf16 v[88:91], v[168:171], a[248:251], v[88:91]\n"
    "ds_read_b128 v[164:167], v200 offset:13312\n"
    "v_cvt_pk_bf16_f32 v68, v68, v69\n"
    "v_mfma_f32_16x16x32_bf16 v[88:91], v[172:175], a[252:255], v[88:91]\n"
    "v_exp_f32 v60, v60\n"
    "v_mfma_f32_16x16x32_bf16 v[92:95], v[172:175], v[20:23], v[92:95]\n"
    "s_add_u32 m0, s71, 0x18000\n"
    "v_exp_f32 v70, v70\n"
    "v_mfma_f32_16x16x32_bf16 v[92:95], v[168:171], v[16:19], v[92:95]\n"
    "s_waitcnt lgkmcnt(2)\n"
    "buffer_load_dwordx4 v200, s[40:43], s61 offen sc1 lds\n"
    "v_mfma_f32_16x16x32_bf16 v[92:95], v[176:179], v[8:11], v[92:95]\n"
    "ds_read_b128 v[168:171], v200 offset:14336\n"
    "v_mov_b32 v127, v125\n"
    "v_mfma_f32_16x16x32_bf16 v[92:95], v[180:183], v[12:15], v[92:95]\n"
    "v_exp_f32 v61, v61\n"
    "v_mfma_f32_16x16x32_bf16 v[88:91], v[180:183], a[244:247], v[88:91]\n"
    "v_exp_f32 v71, v71\n"
    "v_mfma_f32_16x16x32_bf16 v[88:91], v[176:179], a[240:243], v[88:91]\n"
    "v_exp_f32 v62, v62\n"
    "v_mfma_f32_16x16x32_bf16 v[84:87], v[176:179], a[216:219], v[84:87]\n"
    "ds_read_b128 v[172:175], v200 offset:15360\n"
    "v_cvt_pk_bf16_f32 v46, v60, v61\n"
    "v_mfma_f32_16x16x32_bf16 v[84:87], v[180:183], a[220:223], v[84:87]\n"
    "v_exp_f32 v72, v72\n"
    "v_mfma_f32_16x16x32_bf16 v[80:83], v[180:183], a[196:199], v[80:83]\n"
    "v_exp_f32 v63, v63\n"
    "v_mfma_f32_16x16x32_bf16 v[80:83], v[176:179], a[192:195], v[80:83]\n"
    "s_waitcnt lgkmcnt(2)\n"
    "buffer_load_dwordx4 v200, s[40:43], s61 offen offset:1024 sc1 lds\n"
    "v_mfma_f32_16x16x32_bf16 v[232:235], v[160:163], a[192:195], v[96:99]\n"
    "ds_read_b128 v[176:179], v200 offset:16384\n"
    "v_cvt_pk_bf16_f32 v47, v62, v63\n"
    "v_mfma_f32_16x16x32_bf16 v[232:235], v[164:167], a[196:199], v[232:235]\n"
    "v_exp_f32 v73, v73\n"
    "v_mfma_f32_16x16x32_bf16 v[236:239], v[164:167], a[220:223], v[100:103]\n"
    "v_exp_f32 v74, v74\n"
    "v_mfma_f32_16x16x32_bf16 v[236:239], v[160:163], a[216:219], v[236:239]\n"
    "v_exp_f32 v75, v75\n"
    "v_mfma_f32_16x16x32_bf16 v[240:243], v[160:163], a[240:243], v[104:107]\n"
    "ds_read_b128 v[180:183], v200 offset:17408\n"
    "v_cvt_pk_bf16_f32 v69, v70, v71\n"
    "v_mfma_f32_16x16x32_bf16 v[240:243], v[164:167], a[244:247], v[240:243]\n"
    "v_exp_f32 v76, v76\n"
    "v_mfma_f32_16x16x32_bf16 v[244:247], v[164:167], v[12:15], v[108:111]\n"
    "v_exp_f32 v77, v77\n"
    "v_mfma_f32_16x16x32_bf16 v[244:247], v[160:163], v[8:11], v[244:247]\n"
    "s_waitcnt lgkmcnt(2)\n"
    "buffer_load_dwordx4 v200, s[40:43], s61 offen offset:2048 sc1 lds\n"
    "v_mfma_f32_16x16x32_bf16 v[244:247], v[168:171], v[16:19], v[244:247]\n"
    "ds_read_b128 v[160:163], v200 offset:22528\n"
    "v_cvt_pk_bf16_f32 v72, v72, v73\n"
    "v_mfma_f32_16x16x32_bf16 v[244:247], v[172:175], v[20:23], v[244:247]\n"
    "v_exp_f32 v78, v78\n"
    "v_mfma_f32_16x16x32_bf16 v[240:243], v[172:175], a[252:255], v[240:243]\n"
    "v_exp_f32 v79, v79\n"
    "v_mfma_f32_16x16x32_bf16 v[240:243], v[168:171], a[248:251], v[240:243]\n"
    "v_exp_f32 v80, v80\n"
    "v_mfma_f32_16x16x32_bf16 v[236:239], v[168:171], a[224:227], v[236:239]\n"
    "ds_read_b128 v[164:167], v200 offset:23552\n"
    "v_cvt_pk_bf16_f32 v73, v74, v75\n"
    "v_mfma_f32_16x16x32_bf16 v[236:239], v[172:175], a[228:231], v[236:239]\n"
    "v_exp_f32 v81, v81\n"
    "v_mfma_f32_16x16x32_bf16 v[232:235], v[172:175], a[204:207], v[232:235]\n"
    "v_exp_f32 v82, v82\n"
    "v_mfma_f32_16x16x32_bf16 v[232:235], v[168:171], a[200:203], v[232:235]\n"
    "s_waitcnt lgkmcnt(2)\n"
    "buffer_load_dwordx4 v200, s[40:43], s61 offen offset:3072 sc1 lds\n"
    "v_mfma_f32_16x16x32_bf16 v[232:235], v[176:179], a[208:211], v[232:235]\n"
    "ds_read_b128 v[168:171], v200 offset:20480\n"
    "v_cvt_pk_bf16_f32 v76, v76, v77\n"
    "v_mfma_f32_16x16x32_bf16 v[232:235], v[180:183], a[212:215], v[232:235]\n"
    "v_exp_f32 v83, v83\n"
    "v_mfma_f32_16x16x32_bf16 v[236:239], v[180:183], a[236:239], v[236:239]\n"
    "v_exp_f32 v84, v84\n"
    "v_mfma_f32_16x16x32_bf16 v[236:239], v[176:179], a[232:235], v[236:239]\n"
    "v_exp_f32 v85, v85\n"
    "v_mfma_f32_16x16x32_bf16 v[240:243], v[176:179], v[0:3], v[240:243]\n"
    "ds_read_b128 v[172:175], v200 offset:21504\n"
    "v_cvt_pk_bf16_f32 v77, v78, v79\n"
    "v_mfma_f32_16x16x32_bf16 v[240:243], v[180:183], v[4:7], v[240:243]\n"
    "s_add_u32 s65, s61, 0x1000\n"
    "v_exp_f32 v86, v86\n"
    "v_mfma_f32_16x16x32_bf16 v[244:247], v[180:183], v[28:31], v[244:247]\n"
    "s_add_u32 m0, s71, 0x19000\n"
    "v_exp_f32 v87, v87\n"
    "v_mfma_f32_16x16x32_bf16 v[244:247], v[176:179], v[24:27], v[244:247]\n"
    "s_waitcnt lgkmcnt(2)\n"
    "buffer_load_dwordx4 v200, s[40:43], s65 offen sc1 lds\n"
    "v_mfma_f32_16x16x32_bf16 v[60:63], v[160:163], v[24:27], v[108:111]\n"
    "ds_read_b128 v[176:179], v200 offset:18432\n"
    "v_cvt_pk_bf16_f32 v66, v80, v81\n"
    "v_mfma_f32_16x16x32_bf16 v[60:63], v[164:167], v[28:31], v[60:63]\n"
    "v_exp_f32 v88, v88\n"
    "v_mfma_f32_16x16x32_bf16 v[56:59], v[164:167], v[4:7], v[104:107]\n"
    "v_exp_f32 v89, v89\n"
    "v_mfma_f32_16x16x32_bf16 v[56:59], v[160:163], v[0:3], v[56:59]\n"
    "v_exp_f32 v90, v90\n"
    "v_mfma_f32_16x16x32_bf16 v[52:55], v[160:163], a[232:235], v[100:103]\n"
    "ds_read_b128 v[180:183], v200 offset:19456\n"
    "v_cvt_pk_bf16_f32 v67, v82, v83\n"
    "v_mfma_f32_16x16x32_bf16 v[52:55], v[164:167], a[236:239], v[52:55]\n"
    "s_cbranch_execz LDRYR\n"
    "v_exp_f32 v91, v91\n"
    "v_mfma_f32_16x16x32_bf16 v[48:51], v[164:167], a[212:215], v[96:99]\n"
    "s_waitcnt vmcnt(5)\n"
    "v_exp_f32 v92, v92\n"
    "v_mfma_f32_16x16x32_bf16 v[48:51], v[160:163], a[208:211], v[48:51]\n"
    "s_waitcnt lgkmcnt(2)\n"
    "buffer_load_dwordx4 v200, s[40:43], s65 offen offset:1024 sc1 lds\n"
    "v_mfma_f32_16x16x32_bf16 v[48:51], v[168:171], a[200:203], v[48:51]\n"
    "s_barrier\n"
    "v_exp_f32 v93, v93\n"
    "LDF9:\n"
    "v_mfma_f32_16x16x32_bf16 v[48:51], v[172:175], a[204:207], v[48:51]\n"
    "ds_read_b128 v[160:163], v200 offset:24576\n"
    "v_cvt_pk_bf16_f32 v70, v84, v85\n"
    "v_mfma_f32_16x16x32_bf16 v[52:55], v[172:175], a[228:231], v[52:55]\n"
    "v_exp_f32 v94, v94\n"
    "v_mfma_f32_16x16x32_bf16 v[52:55], v[168:171], a[224:227], v[52:55]\n"
    "v_exp_f32 v95, v95\n"
    "v_mfma_f32_16x16x32_bf16 v[56:59], v[168:171], a[248:251], v[56:59]\n"
    "ds_read_b128 v[164:167], v200 offset:25600\n"
    "v_cvt_pk_bf16_f32 v71, v86, v87\n"
    "v_mfma_f32_16x16x32_bf16 v[56:59], v[172:175], a[252:255], v[56:59]\n"
    "v_exp_f32 v232, v232\n"
    "v_mfma_f32_16x16x32_bf16 v[60:63], v[172:175], v[20:23], v[60:63]\n"
    "v_exp_f32 v233, v233\n"
    "v_mfma_f32_16x16x32_bf16 v[60:63], v[168:171], v[16:19], v[60:63]\n"
    "s_waitcnt lgkmcnt(2)\n"
    "v_exp_f32 v234, v234\n"
    "v_mfma_f32_16x16x32_bf16 v[60:63], v[176:179], v[8:11], v[60:63]\n"
    "s_lshl_b32 s76, s56, 3\n"
    "ds_read_b128 v[168:171], v200 offset:26624\n"
    "v_cvt_pk_bf16_f32 v74, v88, v89\n"
    "v_mfma_f32_16x16x32_bf16 v[60:63], v[180:183], v[12:15], v[60:63]\n"
    "s_add_u32 s76, s76, s57\n"
    "v_exp_f32 v235, v235\n"
    "v_mfma_f32_16x16x32_bf16 v[56:59], v[180:183], a[244:247], v[56:59]\n"
    "s_mul_i32 s76, s76, 0x18000\n"
    "v_exp_f32 v236, v236\n"
    "v_mfma_f32_16x16x32_bf16 v[56:59], v[176:179], a[240:243], v[56:59]\n"
    "s_add_u32 s76, s76, s88\n"
    "v_exp_f32 v237, v237\n"
    "v_mfma_f32_16x16x32_bf16 v[52:55], v[176:179], a[216:219], v[52:55]\n"
    "s_add_u32 s28, s24, s76\n"
    "ds_read_b128 v[172:175], v200 offset:27648\n"
    "v_cvt_pk_bf16_f32 v75, v90, v91\n"
    "v_mfma_f32_16x16x32_bf16 v[52:55], v[180:183], a[220:223], v[52:55]\n"
    "s_addc_u32 s29, s25, 0\n"
    "v_exp_f32 v238, v238\n"
    "v_mfma_f32_16x16x32_bf16 v[48:51], v[180:183], a[196:199], v[48:51]\n"
    "s_and_b32 s29, s29, 0xffff\n"
    "v_exp_f32 v239, v239\n"
    "v_mfma_f32_16x16x32_bf16 v[48:51], v[176:179], a[192:195], v[48:51]\n"
    "s_waitcnt lgkmcnt(2)\n"
    "v_exp_f32 v240, v240\n"
    "v_mfma_f32_16x16x32_bf16 v[80:83], v[160:163], a[192:195], v[96:99]\n"
    "s_mov_b32 s30, 0x6000\n"
    "ds_read_b128 v[176:179], v200 offset:28672\n"
    "v_cvt_pk_bf16_f32 v78, v92, v93\n"
    "v_mfma_f32_16x16x32_bf16 v[80:83], v[164:167], a[196:199], v[80:83]\n"
    "s_mov_b32 s31, 0x20000\n"
    "v_cvt_pk_bf16_f32 v79, v94, v95\n"
    "v_permlane16_swap_b32 v113, v115\n"
    "v_mfma_f32_16x16x32_bf16 v[84:87], v[164:167], a[220:223], v[100:103]\n"
    "s_xor_b32 s94, s70, 0x80000000\n"
    "v_exp_f32 v241, v241\n"
    "v_mfma_f32_16x16x32_bf16 v[84:87], v[160:163], a[216:219], v[84:87]\n"
    "s_xor_b32 s95, s70, 0x80000000\n"
    "v_exp_f32 v242, v242\n"
    "v_mfma_f32_16x16x32_bf16 v[88:91], v[160:163], a[240:243], v[104:107]\n"
    "s_mov_b32 s62, 0x1800\n"
    "ds_read_b128 v[180:183], v200 offset:29696\n"
    "v_permlane16_swap_b32 v117, v119\n"
    "v_mfma_f32_16x16x32_bf16 v[88:91], v[164:167], a[244:247], v[88:91]\n"
    "s_mov_b32 s63, 0x3000\n"
    "v_exp_f32 v243, v243\n"
    "v_mfma_f32_16x16x32_bf16 v[92:95], v[164:167], v[12:15], v[108:111]\n"
    "s_mov_b32 s64, 0x4800\n"
    "v_exp_f32 v244, v244\n"
    "v_mfma_f32_16x16x32_bf16 v[92:95], v[160:163], v[8:11], v[92:95]\n"
    "s_waitcnt lgkmcnt(2)\n"
    "v_exp_f32 v245, v245\n"
    "v_mfma_f32_16x16x32_bf16 v[92:95], v[168:171], v[16:19], v[92:95]\n"
    "ds_read_b128 v[160:163], v200 offset:34816\n"
    "v_permlane16_swap_b32 v121, v123\n"
    "v_mfma_f32_16x16x32_bf16 v[92:95], v[172:175], v[20:23], v[92:95]\n"
    "v_exp_f32 v246, v246\n"
    "v_mfma_f32_16x16x32_bf16 v[88:91], v[172:175], a[252:255], v[88:91]\n"
    "v_exp_f32 v247, v247\n"
    "v_mfma_f32_16x16x32_bf16 v[88:91], v[168:171], a[248:251], v[88:91]\n"
    "v_exp_f32 v48, v48\n"
    "v_mfma_f32_16x16x32_bf16 v[84:87], v[168:171], a[224:227], v[84:87]\n"
    "ds_read_b128 v[164:167], v200 offset:35840\n"
    "v_permlane16_swap_b32 v125, v127\n"
    "v_mfma_f32_16x16x32_bf16 v[84:87], v[172:175], a[228:231], v[84:87]\n"
    "v_exp_f32 v49, v49\n"
    "v_mfma_f32_16x16x32_bf16 v[80:83], v[172:175], a[204:207], v[80:83]\n"
    "v_exp_f32 v50, v50\n"
    "v_mfma_f32_16x16x32_bf16 v[80:83], v[168:171], a[200:203], v[80:83]\n"
    "s_waitcnt lgkmcnt(2)\n"
    "v_exp_f32 v51, v51\n"
    "v_mfma_f32_16x16x32_bf16 v[80:83], v[176:179], a[208:211], v[80:83]\n"
    "ds_read_b128 v[168:171], v200 offset:32768\n"
    "v_add_f32 v113, v113, v115\n"
    "v_mfma_f32_16x16x32_bf16 v[80:83], v[180:183], a[212:215], v[80:83]\n"
    "v_exp_f32 v52, v52\n"
    "v_mfma_f32_16x16x32_bf16 v[84:87], v[180:183], a[236:239], v[84:87]\n"
    "v_exp_f32 v53, v53\n"
    "v_mfma_f32_16x16x32_bf16 v[84:87], v[176:179], a[232:235], v[84:87]\n"
    "v_exp_f32 v54, v54\n"
    "v_mfma_f32_16x16x32_bf16 v[88:91], v[176:179], v[0:3], v[88:91]\n"
    "ds_read_b128 v[172:175], v200 offset:33792\n"
    "v_add_f32 v117, v117, v119\n"
    "v_mfma_f32_16x16x32_bf16 v[88:91], v[180:183], v[4:7], v[88:91]\n"
    "v_add_f32 v121, v121, v123\n"
    "v_add_f32 v125, v125, v127\n"
    "v_mfma_f32_16x16x32_bf16 v[92:95], v[180:183], v[28:31], v[92:95]\n"
    "v_exp_f32 v55, v55\n"
    "v_mfma_f32_16x16x32_bf16 v[92:95], v[176:179], v[24:27], v[92:95]\n"
    "s_waitcnt lgkmcnt(2)\n"
    "v_add_f32 v112, v112, v113\n"
    "v_add_f32 v116, v116, v117\n"
    "v_mfma_f32_16x16x32_bf16 v[252:255], v[160:163], v[24:27], v[108:111]\n"
    "ds_read_b128 v[176:179], v200 offset:30720\n"
    "v_add_f32 v120, v120, v121\n"
    "v_mfma_f32_16x16x32_bf16 v[252:255], v[164:167], v[28:31], v[252:255]\n"
    "v_add_f32 v124, v124, v125\n"
    "v_and_b32 v198, 15, v231\n"
    "v_mfma_f32_16x16x32_bf16 v[248:251], v[164:167], v[4:7], v[104:107]\n"
    "v_mul_u32_u24 v205, 0x180, v198\n"
    "v_lshrrev_b32 v199, 4, v231\n"
    "v_mfma_f32_16x16x32_bf16 v[248:251], v[160:163], v[0:3], v[248:251]\n"
    "v_lshl_add_u32 v205, v199, 4, v205\n"
    "v_add_u32 v207, 0x18000, v200\n"
    "v_mfma_f32_16x16x32_bf16 v[188:191], v[160:163], a[232:235], v[100:103]\n"
    "ds_read_b128 v[180:183], v200 offset:31744\n"
    "v_cvt_pk_bf16_f32 v232, v232, v233\n"
    "v_mfma_f32_16x16x32_bf16 v[188:191], v[164:167], a[236:239], v[188:191]\n"
    "v_exp_f32 v56, v56\n"
    "v_mfma_f32_16x16x32_bf16 v[184:187], v[164:167], a[212:215], v[96:99]\n"
    "v_exp_f32 v57, v57\n"
    "v_mfma_f32_16x16x32_bf16 v[184:187], v[160:163], a[208:211], v[184:187]\n"
    "s_waitcnt lgkmcnt(2)\n"
    "v_exp_f32 v58, v58\n"
    "v_mfma_f32_16x16x32_bf16 v[184:187], v[168:171], a[200:203], v[184:187]\n"
    "ds_read_b128 v[160:163], v201\n"
    "v_cvt_pk_bf16_f32 v233, v234, v235\n"
    "v_mfma_f32_16x16x32_bf16 v[184:187], v[172:175], a[204:207], v[184:187]\n"
    "v_exp_f32 v59, v59\n"
    "v_mfma_f32_16x16x32_bf16 v[188:191], v[172:175], a[228:231], v[188:191]\n"
    "v_exp_f32 v60, v60\n"
    "v_mfma_f32_16x16x32_bf16 v[188:191], v[168:171], a[224:227], v[188:191]\n"
    "v_exp_f32 v61, v61\n"
    "v_mfma_f32_16x16x32_bf16 v[248:251], v[168:171], a[248:251], v[248:251]\n"
    "ds_read_b128 v[164:167], v201 offset:1024\n"
    "v_cvt_pk_bf16_f32 v236, v236, v237\n"
    "v_mfma_f32_16x16x32_bf16 v[248:251], v[172:175], a[252:255], v[248:251]\n"
    "v_exp_f32 v62, v62\n"
    "v_mfma_f32_16x16x32_bf16 v[252:255], v[172:175], v[20:23], v[252:255]\n"
    "v_exp_f32 v63, v63\n"
    "v_mfma_f32_16x16x32_bf16 v[252:255], v[168:171], v[16:19], v[252:255]\n"
    "s_waitcnt lgkmcnt(2)\n"
    "v_exp_f32 v80, v80\n"
    "v_mfma_f32_16x16x32_bf16 v[252:255], v[176:179], v[8:11], v[252:255]\n"
    "ds_read_b128 v[168:171], v201 offset:24576\n"
    "v_cvt_pk_bf16_f32 v237, v238, v239\n"
    "v_mfma_f32_16x16x32_bf16 v[252:255], v[180:183], v[12:15], v[252:255]\n"
    "v_exp_f32 v81, v81\n"
    "v_mfma_f32_16x16x32_bf16 v[248:251], v[180:183], a[244:247], v[248:251]\n"
    "v_exp_f32 v82, v82\n"
    "v_mfma_f32_16x16x32_bf16 v[248:251], v[176:179], a[240:243], v[248:251]\n"
    "v_exp_f32 v83, v83\n"
    "v_mfma_f32_16x16x32_bf16 v[188:191], v[176:179], a[216:219], v[188:191]\n"
    "ds_read_b128 v[172:175], v201 offset:25600\n"
    "v_cvt_pk_bf16_f32 v240, v240, v241\n"
    "v_mfma_f32_16x16x32_bf16 v[188:191], v[180:183], a[220:223], v[188:191]\n"
    "v_exp_f32 v84, v84\n"
    "v_mfma_f32_16x16x32_bf16 v[184:187], v[180:183], a[196:199], v[184:187]\n"
    "v_exp_f32 v85, v85\n"
    "v_mfma_f32_16x16x32_bf16 v[184:187], v[176:179], a[192:195], v[184:187]\n"
    "s_waitcnt lgkmcnt(2)\n"
    "v_exp_f32 v86, v86\n"
    "v_mfma_f32_16x16x32_bf16 a[0:3], v[160:163], v[128:131], a[0:3]\n"
    "ds_read_b128 v[176:179], v201 offset:2048\n"
    "v_cvt_pk_bf16_f32 v241, v242, v243\n"
    "v_mfma_f32_16x16x32_bf16 a[0:3], v[164:167], v[144:147], a[0:3]\n"
    "v_exp_f32 v87, v87\n"
    "v_mfma_f32_16x16x32_bf16 a[4:7], v[160:163], v[132:135], a[4:7]\n"
    "v_exp_f32 v88, v88\n"
    "v_mfma_f32_16x16x32_bf16 a[4:7], v[164:167], v[148:151], a[4:7]\n"
    "v_exp_f32 v89, v89\n"
    "v_mfma_f32_16x16x32_bf16 a[8:11], v[160:163], v[136:139], a[8:11]\n"
    "ds_read_b128 v[180:183], v201 offset:3072\n"
    "v_cvt_pk_bf16_f32 v244, v244, v245\n"
    "v_mfma_f32_16x16x32_bf16 a[8:11], v[164:167], v[152:155], a[8:11]\n"
    "v_cvt_pk_bf16_f32 v245, v246, v247\n"
    "v_cvt_pk_bf16_f32 v234, v48, v49\n"
    "v_mfma_f32_16x16x32_bf16 a[12:15], v[160:163], v[140:143], a[12:15]\n"
    "v_cvt_pk_bf16_f32 v235, v50, v51\n"
    "v_cvt_pk_bf16_f32 v238, v52, v53\n"
    "v_mfma_f32_16x16x32_bf16 a[12:15], v[164:167], v[156:159], a[12:15]\n"
    "v_cvt_pk_bf16_f32 v239, v54, v55\n"
    "v_cvt_pk_bf16_f32 v242, v56, v57\n"
    "v_mfma_f32_16x16x32_bf16 v[112:115], v[192:195], v[144:147], v[112:115]\n"
    "v_exp_f32 v90, v90\n"
    "v_mfma_f32_16x16x32_bf16 v[112:115], v[192:195], v[32:35], v[112:115]\n"
    "s_cbranch_execz LDRYR\n"
    "v_cvt_pk_bf16_f32 v243, v58, v59\n"
    "v_cvt_pk_bf16_f32 v246, v60, v61\n"
    "v_mfma_f32_16x16x32_bf16 v[112:115], v[192:195], v[64:67], v[112:115]\n"
    "v_cvt_pk_bf16_f32 v247, v62, v63\n"
    "v_cvt_pk_bf16_f32 v80, v80, v81\n"
    "v_mfma_f32_16x16x32_bf16 v[116:119], v[192:195], v[148:151], v[116:119]\n"
    "v_exp_f32 v91, v91\n"
    "v_mfma_f32_16x16x32_bf16 v[116:119], v[192:195], v[36:39], v[116:119]\n"
    "v_exp_f32 v92, v92\n"
    "v_mfma_f32_16x16x32_bf16 v[116:119], v[192:195], v[68:71], v[116:119]\n"
    "s_waitcnt lgkmcnt(2)\n"
    "v_exp_f32 v93, v93\n"
    "v_mfma_f32_16x16x32_bf16 a[0:3], v[168:171], v[32:35], a[0:3]\n"
    "ds_read_b128 v[160:163], v201 offset:26624\n"
    "v_cvt_pk_bf16_f32 v81, v82, v83\n"
    "v_mfma_f32_16x16x32_bf16 a[0:3], v[172:175], v[64:67], a[0:3]\n"
    "v_exp_f32 v94, v94\n"
    "v_mfma_f32_16x16x32_bf16 a[4:7], v[168:171], v[36:39], a[4:7]\n"
    "v_exp_f32 v95, v95\n"
    "v_mfma_f32_16x16x32_bf16 a[4:7], v[172:175], v[68:71], a[4:7]\n"
    "v_exp_f32 v184, v184\n"
    "v_mfma_f32_16x16x32_bf16 a[8:11], v[168:171], v[40:43], a[8:11]\n"
    "ds_read_b128 v[164:167], v201 offset:27648\n"
    "v_cvt_pk_bf16_f32 v84, v84, v85\n"
    "v_mfma_f32_16x16x32_bf16 a[8:11], v[172:175], v[72:75], a[8:11]\n"
    "v_exp_f32 v185, v185\n"
    "v_mfma_f32_16x16x32_bf16 a[12:15], v[168:171], v[44:47], a[12:15]\n"
    "v_exp_f32 v186, v186\n"
    "v_mfma_f32_16x16x32_bf16 a[12:15], v[172:175], v[76:79], a[12:15]\n"
    "v_exp_f32 v187, v187\n"
    "v_mfma_f32_16x16x32_bf16 v[120:123], v[192:195], v[152:155], v[120:123]\n"
    "v_exp_f32 v188, v188\n"
    "v_mfma_f32_16x16x32_bf16 v[120:123], v[192:195], v[40:43], v[120:123]\n"
    "v_exp_f32 v189, v189\n"
    "v_mfma_f32_16x16x32_bf16 v[120:123], v[192:195], v[72:75], v[120:123]\n"
    "v_exp_f32 v190, v190\n"
    "v_mfma_f32_16x16x32_bf16 v[124:127], v[192:195], v[156:159], v[124:127]\n"
    "v_exp_f32 v191, v191\n"
    "v_mfma_f32_16x16x32_bf16 v[124:127], v[192:195], v[44:47], v[124:127]\n"
    "s_waitcnt vmcnt(0)\n"
    "v_exp_f32 v248, v248\n"
    "v_mfma_f32_16x16x32_bf16 v[124:127], v[192:195], v[76:79], v[124:127]\n"
    "s_waitcnt lgkmcnt(2)\n"
    "v_exp_f32 v249, v249\n"
    "v_mfma_f32_16x16x32_bf16 a[16:19], v[176:179], v[128:131], a[16:19]\n"
    "s_barrier\n"
    "v_exp_f32 v250, v250\n"
    "LDF12:\n"
    "v_mfma_f32_16x16x32_bf16 a[16:19], v[180:183], v[144:147], a[16:19]\n"
    "ds_read_b128 v[168:171], v207\n"
    "v_cvt_pk_bf16_f32 v85, v86, v87\n"
    "v_mfma_f32_16x16x32_bf16 a[20:23], v[176:179], v[132:135], a[20:23]\n"
    "v_exp_f32 v251, v251\n"
    "v_mfma_f32_16x16x32_bf16 a[20:23], v[180:183], v[148:151], a[20:23]\n"
    "v_exp_f32 v252, v252\n"
    "v_mfma_f32_16x16x32_bf16 a[24:27], v[176:179], v[136:139], a[24:27]\n"
    "ds_read_b128 v[172:175], v207 offset:1024\n"
    "v_cvt_pk_bf16_f32 v88, v88, v89\n"
    "v_mfma_f32_16x16x32_bf16 a[24:27], v[180:183], v[152:155], a[24:27]\n"
    "v_exp_f32 v253, v253\n"
    "v_mfma_f32_16x16x32_bf16 a[28:31], v[176:179], v[140:143], a[28:31]\n"
    "v_exp_f32 v254, v254\n"
    "v_mfma_f32_16x16x32_bf16 a[28:31], v[180:183], v[156:159], a[28:31]\n"
    "s_waitcnt lgkmcnt(2)\n"
    "v_cvt_pk_bf16_f32 v89, v90, v91\n"
    "v_cvt_pk_bf16_f32 v92, v92, v93\n"
    "v_mfma_f32_16x16x32_bf16 a[16:19], v[160:163], v[32:35], a[16:19]\n"
    "ds_read_b128 v[176:179], v207 offset:2048\n"
    "v_cvt_pk_bf16_f32 v93, v94, v95\n"
    "v_mfma_f32_16x16x32_bf16 a[16:19], v[164:167], v[64:67], a[16:19]\n"
    "ds_read_b128 v[180:183], v207 offset:3072\n"
    "v_cvt_pk_bf16_f32 v82, v184, v185\n"
    "v_mfma_f32_16x16x32_bf16 a[20:23], v[160:163], v[36:39], a[20:23]\n"
    "v_cvt_pk_bf16_f32 v83, v186, v187\n"
    "v_cvt_pk_bf16_f32 v86, v188, v189\n"
    "v_mfma_f32_16x16x32_bf16 a[20:23], v[164:167], v[68:71], a[20:23]\n"
    "v_exp_f32 v255, v255\n"
    "v_mfma_f32_16x16x32_bf16 a[24:27], v[160:163], v[40:43], a[24:27]\n"
    "v_cvt_pk_bf16_f32 v87, v190, v191\n"
    "v_cvt_pk_bf16_f32 v90, v248, v249\n"
    "v_mfma_f32_16x16x32_bf16 a[24:27], v[164:167], v[72:75], a[24:27]\n"
    "v_cvt_pk_bf16_f32 v91, v250, v251\n"
    "v_cvt_pk_bf16_f32 v94, v252, v253\n"
    "v_mfma_f32_16x16x32_bf16 a[28:31], v[160:163], v[44:47], a[28:31]\n"
    "v_cvt_pk_bf16_f32 v95, v254, v255\n"
    "v_mfma_f32_16x16x32_bf16 a[28:31], v[164:167], v[76:79], a[28:31]\n"
    "v_mfma_f32_16x16x32_bf16 v[112:115], v[192:195], v[232:235], v[112:115]\n"
    "ds_read_b128 v[160:163], v201 offset:4096\n"
    "v_mfma_f32_16x16x32_bf16 v[112:115], v[192:195], v[80:83], v[112:115]\n"
    "s_waitcnt lgkmcnt(3)\n"
    "v_mfma_f32_16x16x32_bf16 a[0:3], v[168:171], v[232:235], a[0:3]\n"
    "v_mfma_f32_16x16x32_bf16 a[0:3], v[172:175], v[80:83], a[0:3]\n"
    "s_waitcnt lgkmcnt(1)\n"
    "v_mfma_f32_16x16x32_bf16 a[16:19], v[176:179], v[232:235], a[16:19]\n"
    "ds_read_b128 v[164:167], v201 offset:5120\n"
    "v_mfma_f32_16x16x32_bf16 a[16:19], v[180:183], v[80:83], a[16:19]\n"
    "v_mfma_f32_16x16x32_bf16 v[116:119], v[192:195], v[236:239], v[116:119]\n"
    "v_mul_f32 v222, s68, v112\n"
    "v_rcp_f32 v222, v222\n"
    "v_mfma_f32_16x16x32_bf16 v[116:119], v[192:195], v[84:87], v[116:119]\n"
    "v_accvgpr_read_b32 v96, a0\n"
    "v_accvgpr_read_b32 v97, a1\n"
    "v_accvgpr_read_b32 v98, a2\n"
    "v_mfma_f32_16x16x32_bf16 a[4:7], v[168:171], v[236:239], a[4:7]\n"
    "v_accvgpr_read_b32 v99, a3\n"
    "v_fma_f32 v96, v96, v222, s94\n"
    "v_fma_f32 v97, v97, v222, s94\n"
    "v_mfma_f32_16x16x32_bf16 a[4:7], v[172:175], v[84:87], a[4:7]\n"
    "v_fma_f32 v98, v98, v222, s94\n"
    "v_fma_f32 v99, v99, v222, s94\n"
    "v_cvt_pk_bf16_f32 v48, v96, v97\n"
    "v_mfma_f32_16x16x32_bf16 a[20:23], v[176:179], v[236:239], a[20:23]\n"
    "v_cvt_pk_bf16_f32 v49, v98, v99\n"
    "v_accvgpr_read_b32 v100, a16\n"
    "v_accvgpr_read_b32 v101, a17\n"
    "v_mfma_f32_16x16x32_bf16 a[20:23], v[180:183], v[84:87], a[20:23]\n"
    "v_accvgpr_read_b32 v102, a18\n"
    "v_accvgpr_read_b32 v103, a19\n"
    "v_fma_f32 v100, v100, v222, s94\n"
    "v_mfma_f32_16x16x32_bf16 v[120:123], v[192:195], v[240:243], v[120:123]\n"
    "v_fma_f32 v101, v101, v222, s94\n"
    "v_fma_f32 v102, v102, v222, s94\n"
    "v_fma_f32 v103, v103, v222, s94\n"
    "v_mfma_f32_16x16x32_bf16 v[120:123], v[192:195], v[88:91], v[120:123]\n"
    "v_cvt_pk_bf16_f32 v50, v100, v101\n"
    "v_cvt_pk_bf16_f32 v51, v102, v103\n"
    "v_mfma_f32_16x16x32_bf16 a[8:11], v[168:171], v[240:243], a[8:11]\n"
    "buffer_store_dwordx4 v[48:51], v205, s[28:31], 0 offen sc0 sc1\n"
    "v_mul_f32 v224, s68, v116\n"
    "v_rcp_f32 v224, v224\n"
    "v_accvgpr_read_b32 v104, a4\n"
    "v_mfma_f32_16x16x32_bf16 a[8:11], v[172:175], v[88:91], a[8:11]\n"
    "v_accvgpr_read_b32 v105, a5\n"
    "v_accvgpr_read_b32 v106, a6\n"
    "v_accvgpr_read_b32 v107, a7\n"
    "v_mfma_f32_16x16x32_bf16 a[24:27], v[176:179], v[240:243], a[24:27]\n"
    "v_fma_f32 v104, v104, v224, s94\n"
    "v_fma_f32 v105, v105, v224, s94\n"
    "v_fma_f32 v106, v106, v224, s94\n"
    "v_mfma_f32_16x16x32_bf16 a[24:27], v[180:183], v[88:91], a[24:27]\n"
    "v_fma_f32 v107, v107, v224, s94\n"
    "v_cvt_pk_bf16_f32 v52, v104, v105\n"
    "v_cvt_pk_bf16_f32 v53, v106, v107\n"
    "v_mfma_f32_16x16x32_bf16 v[124:127], v[192:195], v[244:247], v[124:127]\n"
    "v_accvgpr_read_b32 v108, a20\n"
    "v_accvgpr_read_b32 v109, a21\n"
    "v_accvgpr_read_b32 v110, a22\n"
    "v_mfma_f32_16x16x32_bf16 v[124:127], v[192:195], v[92:95], v[124:127]\n"
    "v_accvgpr_read_b32 v111, a23\n"
    "v_fma_f32 v108, v108, v224, s94\n"
    "v_fma_f32 v109, v109, v224, s94\n"
    "v_mfma_f32_16x16x32_bf16 a[12:15], v[168:171], v[244:247], a[12:15]\n"
    "v_fma_f32 v110, v110, v224, s94\n"
    "v_fma_f32 v111, v111, v224, s94\n"
    "v_mfma_f32_16x16x32_bf16 a[12:15], v[172:175], v[92:95], a[12:15]\n"
    "v_cvt_pk_bf16_f32 v54, v108, v109\n"
    "v_cvt_pk_bf16_f32 v55, v110, v111\n"
    "v_mfma_f32_16x16x32_bf16 a[28:31], v[176:179], v[244:247], a[28:31]\n"
    "ds_read_b128 v[168:171], v201 offset:28672\n"
    "v_mfma_f32_16x16x32_bf16 a[28:31], v[180:183], v[92:95], a[28:31]\n"
    "s_waitcnt lgkmcnt(1)\n"
    "ds_read_b128 v[172:175], v201 offset:29696\n"
    "v_mfma_f32_16x16x32_bf16 a[32:35], v[160:163], v[128:131], a[32:35]\n"
    "ds_read_b128 v[176:179], v207 offset:4096\n"
    "v_mfma_f32_16x16x32_bf16 a[32:35], v[164:167], v[144:147], a[32:35]\n"
    "buffer_store_dwordx4 v[52:55], v205, s[28:31], s62 offen sc0 sc1\n"
    "v_mul_f32 v226, s68, v120\n"
    "v_rcp_f32 v226, v226\n"
    "v_mfma_f32_16x16x32_bf16 a[36:39], v[160:163], v[132:135], a[36:39]\n"
    "v_accvgpr_read_b32 v96, a8\n"
    "v_accvgpr_read_b32 v97, a9\n"
    "v_mfma_f32_16x16x32_bf16 a[36:39], v[164:167], v[148:151], a[36:39]\n"
    "v_accvgpr_read_b32 v98, a10\n"
    "v_accvgpr_read_b32 v99, a11\n"
    "v_mfma_f32_16x16x32_bf16 a[40:43], v[160:163], v[136:139], a[40:43]\n"
    "ds_read_b128 v[180:183], v207 offset:5120\n"
    "v_fma_f32 v96, v96, v226, s94\n"
    "v_mfma_f32_16x16x32_bf16 a[40:43], v[164:167], v[152:155], a[40:43]\n"
    "v_fma_f32 v97, v97, v226, s94\n"
    "v_fma_f32 v98, v98, v226, s94\n"
    "v_mfma_f32_16x16x32_bf16 a[44:47], v[160:163], v[140:143], a[44:47]\n"
    "v_fma_f32 v99, v99, v226, s94\n"
    "v_cvt_pk_bf16_f32 v56, v96, v97\n"
    "v_mfma_f32_16x16x32_bf16 a[44:47], v[164:167], v[156:159], a[44:47]\n"
    "s_waitcnt lgkmcnt(2)\n"
    "v_cvt_pk_bf16_f32 v57, v98, v99\n"
    "v_accvgpr_read_b32 v100, a24\n"
    "v_mfma_f32_16x16x32_bf16 a[44:47], v[168:171], v[44:47], a[44:47]\n"
    "ds_read_b128 v[160:163], v201 offset:6144\n"
    "v_accvgpr_read_b32 v101, a25\n"
    "v_mfma_f32_16x16x32_bf16 a[44:47], v[172:175], v[76:79], a[44:47]\n"
    "v_accvgpr_read_b32 v102, a26\n"
    "v_accvgpr_read_b32 v103, a27\n"
    "v_mfma_f32_16x16x32_bf16 a[40:43], v[168:171], v[40:43], a[40:43]\n"
    "v_fma_f32 v100, v100, v226, s94\n"
    "v_fma_f32 v101, v101, v226, s94\n"
    "v_mfma_f32_16x16x32_bf16 a[40:43], v[172:175], v[72:75], a[40:43]\n"
    "v_fma_f32 v102, v102, v226, s94\n"
    "v_fma_f32 v103, v103, v226, s94\n"
    "v_mfma_f32_16x16x32_bf16 a[36:39], v[168:171], v[36:39], a[36:39]\n"
    "ds_read_b128 v[164:167], v201 offset:7168\n"
    "v_cvt_pk_bf16_f32 v58, v100, v101\n"
    "v_mfma_f32_16x16x32_bf16 a[36:39], v[172:175], v[68:71], a[36:39]\n"
    "v_cvt_pk_bf16_f32 v59, v102, v103\n"
    "v_mfma_f32_16x16x32_bf16 a[32:35], v[168:171], v[32:35], a[32:35]\n"
    "buffer_store_dwordx4 v[56:59], v205, s[28:31], s63 offen sc0 sc1\n"
    "v_mul_f32 v228, s68, v124\n"
    "v_rcp_f32 v228, v228\n"
    "v_mfma_f32_16x16x32_bf16 a[32:35], v[172:175], v[64:67], a[32:35]\n"
    "s_waitcnt lgkmcnt(2)\n"
    "v_accvgpr_read_b32 v104, a12\n"
    "v_accvgpr_read_b32 v105, a13\n"
    "v_mfma_f32_16x16x32_bf16 a[32:35], v[176:179], v[232:235], a[32:35]\n"
    "ds_read_b128 v[168:171], v201 offset:30720\n"
    "v_accvgpr_read_b32 v106, a14\n"
    "v_mfma_f32_16x16x32_bf16 a[32:35], v[180:183], v[80:83], a[32:35]\n"
    "v_accvgpr_read_b32 v107, a15\n"
    "v_fma_f32 v104, v104, v228, s94\n"
    "v_mfma_f32_16x16x32_bf16 a[36:39], v[176:179], v[236:239], a[36:39]\n"
    "v_fma_f32 v105, v105, v228, s94\n"
    "v_fma_f32 v106, v106, v228, s94\n"
    "v_mfma_f32_16x16x32_bf16 a[36:39], v[180:183], v[84:87], a[36:39]\n"
    "v_fma_f32 v107, v107, v228, s94\n"
    "v_cvt_pk_bf16_f32 v60, v104, v105\n"
    "v_mfma_f32_16x16x32_bf16 a[40:43], v[176:179], v[240:243], a[40:43]\n"
    "ds_read_b128 v[172:175], v201 offset:31744\n"
    "v_cvt_pk_bf16_f32 v61, v106, v107\n"
    "v_mfma_f32_16x16x32_bf16 a[40:43], v[180:183], v[88:91], a[40:43]\n"
    "v_accvgpr_read_b32 v108, a28\n"
    "v_accvgpr_read_b32 v109, a29\n"
    "v_mfma_f32_16x16x32_bf16 a[44:47], v[176:179], v[244:247], a[44:47]\n"
    "v_accvgpr_read_b32 v110, a30\n"
    "v_accvgpr_read_b32 v111, a31\n"
    "v_mfma_f32_16x16x32_bf16 a[44:47], v[180:183], v[92:95], a[44:47]\n"
    "s_waitcnt lgkmcnt(2)\n"
    "v_fma_f32 v108, v108, v228, s94\n"
    "v_fma_f32 v109, v109, v228, s94\n"
    "v_mfma_f32_16x16x32_bf16 a[48:51], v[160:163], v[128:131], a[48:51]\n"
    "ds_read_b128 v[176:179], v207 offset:6144\n"
    "v_fma_f32 v110, v110, v228, s94\n"
    "v_mfma_f32_16x16x32_bf16 a[48:51], v[164:167], v[144:147], a[48:51]\n"
    "v_fma_f32 v111, v111, v228, s94\n"
    "v_cvt_pk_bf16_f32 v62, v108, v109\n"
    "v_mfma_f32_16x16x32_bf16 a[52:55], v[160:163], v[132:135], a[52:55]\n"
    "s_cbranch_execz LDRYR\n"
    "LDF13:\n"
    "v_cvt_pk_bf16_f32 v63, v110, v111\n"
    "v_mfma_f32_16x16x32_bf16 a[52:55], v[164:167], v[148:151], a[52:55]\n"
    "buffer_store_dwordx4 v[60:63], v205, s[28:31], s64 offen sc0 sc1\n"
    "v_accvgpr_read_b32 v96, a32\n"
    "v_accvgpr_read_b32 v97, a33\n"
    "v_accvgpr_read_b32 v98, a34\n"
    "v_mfma_f32_16x16x32_bf16 a[56:59], v[160:163], v[136:139], a[56:59]\n"
    "ds_read_b128 v[180:183], v207 offset:7168\n"
    "v_accvgpr_read_b32 v99, a35\n"
    "v_mfma_f32_16x16x32_bf16 a[56:59], v[164:167], v[152:155], a[56:59]\n"
    "v_fma_f32 v96, v96, v222, s94\n"
    "v_fma_f32 v97, v97, v222, s94\n"
    "v_mfma_f32_16x16x32_bf16 a[60:63], v[160:163], v[140:143], a[60:63]\n"
    "v_fma_f32 v98, v98, v222, s94\n"
    "v_fma_f32 v99, v99, v222, s94\n"
    "v_mfma_f32_16x16x32_bf16 a[60:63], v[164:167], v[156:159], a[60:63]\n"
    "s_waitcnt lgkmcnt(2)\n"
    "v_cvt_pk_bf16_f32 v184, v96, v97\n"
    "v_cvt_pk_bf16_f32 v185, v98, v99\n"
    "v_mfma_f32_16x16x32_bf16 a[60:63], v[168:171], v[44:47], a[60:63]\n"
    "ds_read_b128 v[160:163], v201 offset:8192\n"
    "v_accvgpr_read_b32 v100, a36\n"
    "v_mfma_f32_16x16x32_bf16 a[60:63], v[172:175], v[76:79], a[60:63]\n"
    "v_accvgpr_read_b32 v101, a37\n"
    "v_accvgpr_read_b32 v102, a38\n"
    "v_mfma_f32_16x16x32_bf16 a[56:59], v[168:171], v[40:43], a[56:59]\n"
    "v_accvgpr_read_b32 v103, a39\n"
    "v_fma_f32 v100, v100, v224, s94\n"
    "v_mfma_f32_16x16x32_bf16 a[56:59], v[172:175], v[72:75], a[56:59]\n"
    "v_fma_f32 v101, v101, v224, s94\n"
    "v_fma_f32 v102, v102, v224, s94\n"
    "v_mfma_f32_16x16x32_bf16 a[52:55], v[168:171], v[36:39], a[52:55]\n"
    "ds_read_b128 v[164:167], v201 offset:9216\n"
    "v_fma_f32 v103, v103, v224, s94\n"
    "v_mfma_f32_16x16x32_bf16 a[52:55], v[172:175], v[68:71], a[52:55]\n"
    "v_cvt_pk_bf16_f32 v188, v100, v101\n"
    "v_cvt_pk_bf16_f32 v189, v102, v103\n"
    "v_mfma_f32_16x16x32_bf16 a[48:51], v[168:171], v[32:35], a[48:51]\n"
    "v_accvgpr_read_b32 v104, a40\n"
    "v_accvgpr_read_b32 v105, a41\n"
    "v_mfma_f32_16x16x32_bf16 a[48:51], v[172:175], v[64:67], a[48:51]\n"
    "s_waitcnt lgkmcnt(2)\n"
    "v_accvgpr_read_b32 v106, a42\n"
    "v_accvgpr_read_b32 v107, a43\n"
    "v_mfma_f32_16x16x32_bf16 a[48:51], v[176:179], v[232:235], a[48:51]\n"
    "ds_read_b128 v[168:171], v201 offset:32768\n"
    "v_fma_f32 v104, v104, v226, s94\n"
    "v_mfma_f32_16x16x32_bf16 a[48:51], v[180:183], v[80:83], a[48:51]\n"
    "v_fma_f32 v105, v105, v226, s94\n"
    "v_fma_f32 v106, v106, v226, s94\n"
    "v_mfma_f32_16x16x32_bf16 a[52:55], v[176:179], v[236:239], a[52:55]\n"
    "v_fma_f32 v107, v107, v226, s94\n"
    "v_cvt_pk_bf16_f32 v248, v104, v105\n"
    "v_mfma_f32_16x16x32_bf16 a[52:55], v[180:183], v[84:87], a[52:55]\n"
    "v_cvt_pk_bf16_f32 v249, v106, v107\n"
    "v_accvgpr_read_b32 v108, a44\n"
    "v_mfma_f32_16x16x32_bf16 a[56:59], v[176:179], v[240:243], a[56:59]\n"
    "ds_read_b128 v[172:175], v201 offset:33792\n"
    "v_accvgpr_read_b32 v109, a45\n"
    "v_mfma_f32_16x16x32_bf16 a[56:59], v[180:183], v[88:91], a[56:59]\n"
    "v_accvgpr_read_b32 v110, a46\n"
    "v_accvgpr_read_b32 v111, a47\n"
    "v_mfma_f32_16x16x32_bf16 a[60:63], v[176:179], v[244:247], a[60:63]\n"
    "v_fma_f32 v108, v108, v228, s94\n"
    "v_fma_f32 v109, v109, v228, s94\n"
    "v_mfma_f32_16x16x32_bf16 a[60:63], v[180:183], v[92:95], a[60:63]\n"
    "s_waitcnt lgkmcnt(2)\n"
    "v_fma_f32 v110, v110, v228, s94\n"
    "v_fma_f32 v111, v111, v228, s94\n"
    "v_mfma_f32_16x16x32_bf16 a[64:67], v[160:163], v[128:131], a[64:67]\n"
    "ds_read_b128 v[176:179], v207 offset:8192\n"
    "v_cvt_pk_bf16_f32 v252, v108, v109\n"
    "v_mfma_f32_16x16x32_bf16 a[64:67], v[164:167], v[144:147], a[64:67]\n"
    "v_cvt_pk_bf16_f32 v253, v110, v111\n"
    "v_accvgpr_read_b32 v96, a48\n"
    "v_mfma_f32_16x16x32_bf16 a[68:71], v[160:163], v[132:135], a[68:71]\n"
    "v_accvgpr_read_b32 v97, a49\n"
    "v_accvgpr_read_b32 v98, a50\n"
    "v_mfma_f32_16x16x32_bf16 a[68:71], v[164:167], v[148:151], a[68:71]\n"
    "v_accvgpr_read_b32 v99, a51\n"
    "v_fma_f32 v96, v96, v222, s94\n"
    "v_mfma_f32_16x16x32_bf16 a[72:75], v[160:163], v[136:139], a[72:75]\n"
    "ds_read_b128 v[180:183], v207 offset:9216\n"
    "v_fma_f32 v97, v97, v222, s94\n"
    "v_mfma_f32_16x16x32_bf16 a[72:75], v[164:167], v[152:155], a[72:75]\n"
    "v_fma_f32 v98, v98, v222, s94\n"
    "v_fma_f32 v99, v99, v222, s94\n"
    "v_mfma_f32_16x16x32_bf16 a[76:79], v[160:163], v[140:143], a[76:79]\n"
    "v_cvt_pk_bf16_f32 v186, v96, v97\n"
    "v_cvt_pk_bf16_f32 v187, v98, v99\n"
    "v_mfma_f32_16x16x32_bf16 a[76:79], v[164:167], v[156:159], a[76:79]\n"
    "s_waitcnt lgkmcnt(2)\n"
    "buffer_store_dwordx4 v[184:187], v205, s[28:31], 0 offen offset:64 sc0 sc1\n"
    "v_accvgpr_read_b32 v100, a52\n"
    "v_accvgpr_read_b32 v101, a53\n"
    "v_accvgpr_read_b32 v102, a54\n"
    "v_mfma_f32_16x16x32_bf16 a[76:79], v[168:171], v[44:47], a[76:79]\n"
    "ds_read_b128 v[160:163], v201 offset:10240\n"
    "v_accvgpr_read_b32 v103, a55\n"
    "v_mfma_f32_16x16x32_bf16 a[76:79], v[172:175], v[76:79], a[76:79]\n"
    "v_fma_f32 v100, v100, v224, s94\n"
    "v_fma_f32 v101, v101, v224, s94\n"
    "v_mfma_f32_16x16x32_bf16 a[72:75], v[168:171], v[40:43], a[72:75]\n"
    "v_fma_f32 v102, v102, v224, s94\n"
    "v_fma_f32 v103, v103, v224, s94\n"
    "v_mfma_f32_16x16x32_bf16 a[72:75], v[172:175], v[72:75], a[72:75]\n"
    "v_cvt_pk_bf16_f32 v190, v100, v101\n"
    "v_cvt_pk_bf16_f32 v191, v102, v103\n"
    "v_mfma_f32_16x16x32_bf16 a[68:71], v[168:171], v[36:39], a[68:71]\n"
    "ds_read_b128 v[164:167], v201 offset:11264\n"
    "v_mfma_f32_16x16x32_bf16 a[68:71], v[172:175], v[68:71], a[68:71]\n"
    "buffer_store_dwordx4 v[188:191], v205, s[28:31], s62 offen offset:64 sc0 sc1\n"
    "v_accvgpr_read_b32 v104, a56\n"
    "v_accvgpr_read_b32 v105, a57\n"
    "v_accvgpr_read_b32 v106, a58\n"
    "v_mfma_f32_16x16x32_bf16 a[64:67], v[168:171], v[32:35], a[64:67]\n"
    "v_accvgpr_read_b32 v107, a59\n"
    "v_fma_f32 v104, v104, v226, s94\n"
    "v_mfma_f32_16x16x32_bf16 a[64:67], v[172:175], v[64:67], a[64:67]\n"
    "s_waitcnt lgkmcnt(2)\n"
    "v_fma_f32 v105, v105, v226, s94\n"
    "v_fma_f32 v106, v106, v226, s94\n"
    "v_mfma_f32_16x16x32_bf16 a[64:67], v[176:179], v[232:235], a[64:67]\n"
    "ds_read_b128 v[168:171], v201 offset:34816\n"
    "v_fma_f32 v107, v107, v226, s94\n"
    "v_mfma_f32_16x16x32_bf16 a[64:67], v[180:183], v[80:83], a[64:67]\n"
    "v_cvt_pk_bf16_f32 v250, v104, v105\n"
    "v_cvt_pk_bf16_f32 v251, v106, v107\n"
    "v_mfma_f32_16x16x32_bf16 a[68:71], v[176:179], v[236:239], a[68:71]\n"
    "buffer_store_dwordx4 v[248:251], v205, s[28:31], s63 offen offset:64 sc0 sc1\n"
    "v_accvgpr_read_b32 v108, a60\n"
    "v_accvgpr_read_b32 v109, a61\n"
    "v_accvgpr_read_b32 v110, a62\n"
    "v_mfma_f32_16x16x32_bf16 a[68:71], v[180:183], v[84:87], a[68:71]\n"
    "v_accvgpr_read_b32 v111, a63\n"
    "v_fma_f32 v108, v108, v228, s94\n"
    "v_mfma_f32_16x16x32_bf16 a[72:75], v[176:179], v[240:243], a[72:75]\n"
    "ds_read_b128 v[172:175], v201 offset:35840\n"
    "v_fma_f32 v109, v109, v228, s94\n"
    "v_mfma_f32_16x16x32_bf16 a[72:75], v[180:183], v[88:91], a[72:75]\n"
    "v_fma_f32 v110, v110, v228, s94\n"
    "v_fma_f32 v111, v111, v228, s94\n"
    "v_mfma_f32_16x16x32_bf16 a[76:79], v[176:179], v[244:247], a[76:79]\n"
    "v_cvt_pk_bf16_f32 v254, v108, v109\n"
    "v_cvt_pk_bf16_f32 v255, v110, v111\n"
    "v_mfma_f32_16x16x32_bf16 a[76:79], v[180:183], v[92:95], a[76:79]\n"
    "s_waitcnt lgkmcnt(2)\n"
    "buffer_store_dwordx4 v[252:255], v205, s[28:31], s64 offen offset:64 sc0 sc1\n"
    "v_accvgpr_read_b32 v96, a64\n"
    "v_accvgpr_read_b32 v97, a65\n"
    "v_accvgpr_read_b32 v98, a66\n"
    "v_mfma_f32_16x16x32_bf16 a[80:83], v[160:163], v[128:131], a[80:83]\n"
    "ds_read_b128 v[176:179], v207 offset:10240\n"
    "v_accvgpr_read_b32 v99, a67\n"
    "v_mfma_f32_16x16x32_bf16 a[80:83], v[164:167], v[144:147], a[80:83]\n"
    "v_fma_f32 v96, v96, v222, s94\n"
    "v_fma_f32 v97, v97, v222, s94\n"
    "v_mfma_f32_16x16x32_bf16 a[84:87], v[160:163], v[132:135], a[84:87]\n"
    "v_fma_f32 v98, v98, v222, s94\n"
    "v_fma_f32 v99, v99, v222, s94\n"
    "v_mfma_f32_16x16x32_bf16 a[84:87], v[164:167], v[148:151], a[84:87]\n"
    "v_cvt_pk_bf16_f32 v48, v96, v97\n"
    "v_cvt_pk_bf16_f32 v49, v98, v99\n"
    "v_mfma_f32_16x16x32_bf16 a[88:91], v[160:163], v[136:139], a[88:91]\n"
    "ds_read_b128 v[180:183], v207 offset:11264\n"
    "v_accvgpr_read_b32 v100, a68\n"
    "v_mfma_f32_16x16x32_bf16 a[88:91], v[164:167], v[152:155], a[88:91]\n"
    "v_accvgpr_read_b32 v101, a69\n"
    "v_accvgpr_read_b32 v102, a70\n"
    "v_mfma_f32_16x16x32_bf16 a[92:95], v[160:163], v[140:143], a[92:95]\n"
    "v_accvgpr_read_b32 v103, a71\n"
    "v_fma_f32 v100, v100, v224, s94\n"
    "v_mfma_f32_16x16x32_bf16 a[92:95], v[164:167], v[156:159], a[92:95]\n"
    "s_waitcnt lgkmcnt(2)\n"
    "v_fma_f32 v101, v101, v224, s94\n"
    "v_fma_f32 v102, v102, v224, s94\n"
    "v_mfma_f32_16x16x32_bf16 a[92:95], v[168:171], v[44:47], a[92:95]\n"
    "ds_read_b128 v[160:163], v201 offset:12288\n"
    "v_fma_f32 v103, v103, v224, s94\n"
    "v_mfma_f32_16x16x32_bf16 a[92:95], v[172:175], v[76:79], a[92:95]\n"
    "v_cvt_pk_bf16_f32 v52, v100, v101\n"
    "v_cvt_pk_bf16_f32 v53, v102, v103\n"
    "v_mfma_f32_16x16x32_bf16 a[88:91], v[168:171], v[40:43], a[88:91]\n"
    "v_accvgpr_read_b32 v104, a72\n"
    "v_accvgpr_read_b32 v105, a73\n"
    "v_mfma_f32_16x16x32_bf16 a[88:91], v[172:175], v[72:75], a[88:91]\n"
    "v_accvgpr_read_b32 v106, a74\n"
    "v_accvgpr_read_b32 v107, a75\n"
    "v_mfma_f32_16x16x32_bf16 a[84:87], v[168:171], v[36:39], a[84:87]\n"
    "ds_read_b128 v[164:167], v201 offset:13312\n"
    "v_fma_f32 v104, v104, v226, s94\n"
    "v_mfma_f32_16x16x32_bf16 a[84:87], v[172:175], v[68:71], a[84:87]\n"
    "v_fma_f32 v105, v105, v226, s94\n"
    "v_fma_f32 v106, v106, v226, s94\n"
    "v_mfma_f32_16x16x32_bf16 a[80:83], v[168:171], v[32:35], a[80:83]\n"
    "s_cbranch_execz LDRYR\n"
    "LDF14:\n"
    "v_fma_f32 v107, v107, v226, s94\n"
    "v_cvt_pk_bf16_f32 v56, v104, v105\n"
    "v_mfma_f32_16x16x32_bf16 a[80:83], v[172:175], v[64:67], a[80:83]\n"
    "s_waitcnt lgkmcnt(2)\n"
    "v_cvt_pk_bf16_f32 v57, v106, v107\n"
    "v_accvgpr_read_b32 v108, a76\n"
    "v_mfma_f32_16x16x32_bf16 a[80:83], v[176:179], v[232:235], a[80:83]\n"
    "ds_read_b128 v[168:171], v201 offset:36864\n"
    "v_accvgpr_read_b32 v109, a77\n"
    "v_mfma_f32_16x16x32_bf16 a[80:83], v[180:183], v[80:83], a[80:83]\n"
    "v_accvgpr_read_b32 v110, a78\n"
    "v_accvgpr_read_b32 v111, a79\n"
    "v_mfma_f32_16x16x32_bf16 a[84:87], v[176:179], v[236:239], a[84:87]\n"
    "v_fma_f32 v108, v108, v228, s94\n"
    "v_fma_f32 v109, v109, v228, s94\n"
    "v_mfma_f32_16x16x32_bf16 a[84:87], v[180:183], v[84:87], a[84:87]\n"
    "v_fma_f32 v110, v110, v228, s94\n"
    "v_fma_f32 v111, v111, v228, s94\n"
    "v_mfma_f32_16x16x32_bf16 a[88:91], v[176:179], v[240:243], a[88:91]\n"
    "ds_read_b128 v[172:175], v201 offset:37888\n"
    "v_cvt_pk_bf16_f32 v60, v108, v109\n"
    "v_mfma_f32_16x16x32_bf16 a[88:91], v[180:183], v[88:91], a[88:91]\n"
    "v_cvt_pk_bf16_f32 v61, v110, v111\n"
    "v_mfma_f32_16x16x32_bf16 a[92:95], v[176:179], v[244:247], a[92:95]\n"
    "v_mfma_f32_16x16x32_bf16 a[92:95], v[180:183], v[92:95], a[92:95]\n"
    "s_waitcnt lgkmcnt(2)\n"
    "v_accvgpr_read_b32 v96, a80\n"
    "v_accvgpr_read_b32 v97, a81\n"
    "v_mfma_f32_16x16x32_bf16 a[96:99], v[160:163], v[128:131], a[96:99]\n"
    "ds_read_b128 v[176:179], v207 offset:12288\n"
    "v_accvgpr_read_b32 v98, a82\n"
    "v_mfma_f32_16x16x32_bf16 a[96:99], v[164:167], v[144:147], a[96:99]\n"
    "v_accvgpr_read_b32 v99, a83\n"
    "v_fma_f32 v96, v96, v222, s94\n"
    "v_mfma_f32_16x16x32_bf16 a[100:103], v[160:163], v[132:135], a[100:103]\n"
    "v_fma_f32 v97, v97, v222, s94\n"
    "v_fma_f32 v98, v98, v222, s94\n"
    "v_mfma_f32_16x16x32_bf16 a[100:103], v[164:167], v[148:151], a[100:103]\n"
    "v_fma_f32 v99, v99, v222, s94\n"
    "v_cvt_pk_bf16_f32 v50, v96, v97\n"
    "v_mfma_f32_16x16x32_bf16 a[104:107], v[160:163], v[136:139], a[104:107]\n"
    "ds_read_b128 v[180:183], v207 offset:13312\n"
    "v_cvt_pk_bf16_f32 v51, v98, v99\n"
    "v_mfma_f32_16x16x32_bf16 a[104:107], v[164:167], v[152:155], a[104:107]\n"
    "buffer_store_dwordx4 v[48:51], v205, s[28:31], 0 offen offset:128 sc0 sc1\n"
    "v_accvgpr_read_b32 v100, a84\n"
    "v_accvgpr_read_b32 v101, a85\n"
    "v_accvgpr_read_b32 v102, a86\n"
    "v_mfma_f32_16x16x32_bf16 a[108:111], v[160:163], v[140:143], a[108:111]\n"
    "v_accvgpr_read_b32 v103, a87\n"
    "v_fma_f32 v100, v100, v224, s94\n"
    "v_mfma_f32_16x16x32_bf16 a[108:111], v[164:167], v[156:159], a[108:111]\n"
    "s_waitcnt lgkmcnt(2)\n"
    "s_cmp_eq_u32 s57, 0\n"
    "s_cselect_b32 s77, s74, 0\n"
    "v_mov_b32 v196, 0x40000000\n"
    "v_mov_b32 v197, 0x800\n"
    "v_cmp_gt_u32 vcc, s77, v231\n"
    "v_mov_b32 v198, 0\n"
    "v_mov_b32 v198, 0\n"
    "v_cndmask_b32 v197, v196, v197, vcc\n"
    "buffer_store_dword v221, v197, s[44:47], 0 offen sc1\n"
    "v_mfma_f32_16x16x32_bf16 a[108:111], v[168:171], v[44:47], a[108:111]\n"
    "ds_read_b128 v[160:163], v201 offset:14336\n"
    "v_fma_f32 v101, v101, v224, s94\n"
    "v_mfma_f32_16x16x32_bf16 a[108:111], v[172:175], v[76:79], a[108:111]\n"
    "v_fma_f32 v102, v102, v224, s94\n"
    "v_fma_f32 v103, v103, v224, s94\n"
    "v_mfma_f32_16x16x32_bf16 a[104:107], v[168:171], v[40:43], a[104:107]\n"
    "v_cvt_pk_bf16_f32 v54, v100, v101\n"
    "v_cvt_pk_bf16_f32 v55, v102, v103\n"
    "v_mfma_f32_16x16x32_bf16 a[104:107], v[172:175], v[72:75], a[104:107]\n"
    "buffer_store_dwordx4 v[52:55], v205, s[28:31], s62 offen offset:128 sc0 sc1\n"
    "v_accvgpr_read_b32 v104, a88\n"
    "v_accvgpr_read_b32 v105, a89\n"
    "v_accvgpr_read_b32 v106, a90\n"
    "v_mfma_f32_16x16x32_bf16 a[100:103], v[168:171], v[36:39], a[100:103]\n"
    "ds_read_b128 v[164:167], v201 offset:15360\n"
    "v_accvgpr_read_b32 v107, a91\n"
    "v_mfma_f32_16x16x32_bf16 a[100:103], v[172:175], v[68:71], a[100:103]\n"
    "v_fma_f32 v104, v104, v226, s94\n"
    "v_fma_f32 v105, v105, v226, s94\n"
    "v_mfma_f32_16x16x32_bf16 a[96:99], v[168:171], v[32:35], a[96:99]\n"
    "v_fma_f32 v106, v106, v226, s94\n"
    "v_fma_f32 v107, v107, v226, s94\n"
    "v_mfma_f32_16x16x32_bf16 a[96:99], v[172:175], v[64:67], a[96:99]\n"
    "s_waitcnt lgkmcnt(2)\n"
    "v_cvt_pk_bf16_f32 v58, v104, v105\n"
    "v_cvt_pk_bf16_f32 v59, v106, v107\n"
    "v_mfma_f32_16x16x32_bf16 a[96:99], v[176:179], v[232:235], a[96:99]\n"
    "ds_read_b128 v[168:171], v201 offset:38912\n"
    "v_mfma_f32_16x16x32_bf16 a[96:99], v[180:183], v[80:83], a[96:99]\n"
    "buffer_store_dwordx4 v[56:59], v205, s[28:31], s63 offen offset:128 sc0 sc1\n"
    "v_accvgpr_read_b32 v108, a92\n"
    "v_accvgpr_read_b32 v109, a93\n"
    "v_accvgpr_read_b32 v110, a94\n"
    "v_mfma_f32_16x16x32_bf16 a[100:103], v[176:179], v[236:239], a[100:103]\n"
    "v_accvgpr_read_b32 v111, a95\n"
    "v_fma_f32 v108, v108, v228, s94\n"
    "v_mfma_f32_16x16x32_bf16 a[100:103], v[180:183], v[84:87], a[100:103]\n"
    "v_fma_f32 v109, v109, v228, s94\n"
    "v_fma_f32 v110, v110, v228, s94\n"
    "v_mfma_f32_16x16x32_bf16 a[104:107], v[176:179], v[240:243], a[104:107]\n"
    "ds_read_b128 v[172:175], v201 offset:39936\n"
    "v_fma_f32 v111, v111, v228, s94\n"
    "v_mfma_f32_16x16x32_bf16 a[104:107], v[180:183], v[88:91], a[104:107]\n"
    "v_cvt_pk_bf16_f32 v62, v108, v109\n"
    "v_cvt_pk_bf16_f32 v63, v110, v111\n"
    "v_mfma_f32_16x16x32_bf16 a[108:111], v[176:179], v[244:247], a[108:111]\n"
    "buffer_store_dwordx4 v[60:63], v205, s[28:31], s64 offen offset:128 sc0 sc1\n"
    "v_mfma_f32_16x16x32_bf16 a[108:111], v[180:183], v[92:95], a[108:111]\n"
    "s_waitcnt lgkmcnt(2)\n"
    "v_accvgpr_read_b32 v96, a96\n"
    "v_accvgpr_read_b32 v97, a97\n"
    "v_mfma_f32_16x16x32_bf16 a[112:115], v[160:163], v[128:131], a[112:115]\n"
    "ds_read_b128 v[176:179], v207 offset:14336\n"
    "v_accvgpr_read_b32 v98, a98\n"
    "v_mfma_f32_16x16x32_bf16 a[112:115], v[164:167], v[144:147], a[112:115]\n"
    "v_accvgpr_read_b32 v99, a99\n"
    "v_fma_f32 v96, v96, v222, s94\n"
    "v_mfma_f32_16x16x32_bf16 a[116:119], v[160:163], v[132:135], a[116:119]\n"
    "v_fma_f32 v97, v97, v222, s94\n"
    "v_fma_f32 v98, v98, v222, s94\n"
    "v_mfma_f32_16x16x32_bf16 a[116:119], v[164:167], v[148:151], a[116:119]\n"
    "v_fma_f32 v99, v99, v222, s94\n"
    "v_cvt_pk_bf16_f32 v184, v96, v97\n"
    "v_mfma_f32_16x16x32_bf16 a[120:123], v[160:163], v[136:139], a[120:123]\n"
    "ds_read_b128 v[180:183], v207 offset:15360\n"
    "v_cvt_pk_bf16_f32 v185, v98, v99\n"
    "v_mfma_f32_16x16x32_bf16 a[120:123], v[164:167], v[152:155], a[120:123]\n"
    "v_accvgpr_read_b32 v100, a100\n"
    "v_accvgpr_read_b32 v101, a101\n"
    "v_mfma_f32_16x16x32_bf16 a[124:127], v[160:163], v[140:143], a[124:127]\n"
    "v_accvgpr_read_b32 v102, a102\n"
    "v_accvgpr_read_b32 v103, a103\n"
    "v_mfma_f32_16x16x32_bf16 a[124:127], v[164:167], v[156:159], a[124:127]\n"
    "s_waitcnt lgkmcnt(2)\n"
    "v_fma_f32 v100, v100, v224, s94\n"
    "v_fma_f32 v101, v101, v224, s94\n"
    "v_mfma_f32_16x16x32_bf16 a[124:127], v[168:171], v[44:47], a[124:127]\n"
    "ds_read_b128 v[160:163], v201 offset:16384\n"
    "v_fma_f32 v102, v102, v224, s94\n"
    "v_mfma_f32_16x16x32_bf16 a[124:127], v[172:175], v[76:79], a[124:127]\n"
    "v_fma_f32 v103, v103, v224, s94\n"
    "v_cvt_pk_bf16_f32 v188, v100, v101\n"
    "v_mfma_f32_16x16x32_bf16 a[120:123], v[168:171], v[40:43], a[120:123]\n"
    "v_cvt_pk_bf16_f32 v189, v102, v103\n"
    "v_accvgpr_read_b32 v104, a104\n"
    "v_mfma_f32_16x16x32_bf16 a[120:123], v[172:175], v[72:75], a[120:123]\n"
    "v_accvgpr_read_b32 v105, a105\n"
    "v_accvgpr_read_b32 v106, a106\n"
    "v_mfma_f32_16x16x32_bf16 a[116:119], v[168:171], v[36:39], a[116:119]\n"
    "ds_read_b128 v[164:167], v201 offset:17408\n"
    "v_accvgpr_read_b32 v107, a107\n"
    "v_mfma_f32_16x16x32_bf16 a[116:119], v[172:175], v[68:71], a[116:119]\n"
    "v_fma_f32 v104, v104, v226, s94\n"
    "v_fma_f32 v105, v105, v226, s94\n"
    "v_mfma_f32_16x16x32_bf16 a[112:115], v[168:171], v[32:35], a[112:115]\n"
    "v_fma_f32 v106, v106, v226, s94\n"
    "v_fma_f32 v107, v107, v226, s94\n"
    "v_mfma_f32_16x16x32_bf16 a[112:115], v[172:175], v[64:67], a[112:115]\n"
    "s_waitcnt lgkmcnt(2)\n"
    "v_cvt_pk_bf16_f32 v248, v104, v105\n"
    "v_cvt_pk_bf16_f32 v249, v106, v107\n"
    "v_mfma_f32_16x16x32_bf16 a[112:115], v[176:179], v[232:235], a[112:115]\n"
    "ds_read_b128 v[168:171], v201 offset:40960\n"
    "v_accvgpr_read_b32 v108, a108\n"
    "v_mfma_f32_16x16x32_bf16 a[112:115], v[180:183], v[80:83], a[112:115]\n"
    "v_accvgpr_read_b32 v109, a109\n"
    "v_accvgpr_read_b32 v110, a110\n"
    "v_mfma_f32_16x16x32_bf16 a[116:119], v[176:179], v[236:239], a[116:119]\n"
    "v_accvgpr_read_b32 v111, a111\n"
    "v_fma_f32 v108, v108, v228, s94\n"
    "v_mfma_f32_16x16x32_bf16 a[116:119], v[180:183], v[84:87], a[116:119]\n"
    "v_fma_f32 v109, v109, v228, s94\n"
    "v_fma_f32 v110, v110, v228, s94\n"
    "v_mfma_f32_16x16x32_bf16 a[120:123], v[176:179], v[240:243], a[120:123]\n"
    "ds_read_b128 v[172:175], v201 offset:41984\n"
    "v_fma_f32 v111, v111, v228, s94\n"
    "v_mfma_f32_16x16x32_bf16 a[120:123], v[180:183], v[88:91], a[120:123]\n"
    "v_cvt_pk_bf16_f32 v252, v108, v109\n"
    "v_cvt_pk_bf16_f32 v253, v110, v111\n"
    "v_mfma_f32_16x16x32_bf16 a[124:127], v[176:179], v[244:247], a[124:127]\n"
    "v_mfma_f32_16x16x32_bf16 a[124:127], v[180:183], v[92:95], a[124:127]\n"
    "s_waitcnt lgkmcnt(2)\n"
    "v_accvgpr_read_b32 v96, a112\n"
    "v_accvgpr_read_b32 v97, a113\n"
    "v_mfma_f32_16x16x32_bf16 a[128:131], v[160:163], v[128:131], a[128:131]\n"
    "ds_read_b128 v[176:179], v207 offset:16384\n"
    "v_accvgpr_read_b32 v98, a114\n"
    "v_mfma_f32_16x16x32_bf16 a[128:131], v[164:167], v[144:147], a[128:131]\n"
    "v_accvgpr_read_b32 v99, a115\n"
    "v_fma_f32 v96, v96, v222, s94\n"
    "v_mfma_f32_16x16x32_bf16 a[132:135], v[160:163], v[132:135], a[132:135]\n"
    "s_cbranch_execz LDRYR\n"
    "LDF15:\n"
    "v_fma_f32 v97, v97, v222, s94\n"
    "v_fma_f32 v98, v98, v222, s94\n"
    "v_mfma_f32_16x16x32_bf16 a[132:135], v[164:167], v[148:151], a[132:135]\n"
    "v_fma_f32 v99, v99, v222, s94\n"
    "v_cvt_pk_bf16_f32 v186, v96, v97\n"
    "v_mfma_f32_16x16x32_bf16 a[136:139], v[160:163], v[136:139], a[136:139]\n"
    "ds_read_b128 v[180:183], v207 offset:17408\n"
    "v_cvt_pk_bf16_f32 v187, v98, v99\n"
    "v_mfma_f32_16x16x32_bf16 a[136:139], v[164:167], v[152:155], a[136:139]\n"
    "buffer_store_dwordx4 v[184:187], v205, s[28:31], 0 offen offset:192 sc0 sc1\n"
    "v_accvgpr_read_b32 v100, a116\n"
    "v_accvgpr_read_b32 v101, a117\n"
    "v_accvgpr_read_b32 v102, a118\n"
    "v_mfma_f32_16x16x32_bf16 a[140:143], v[160:163], v[140:143], a[140:143]\n"
    "v_accvgpr_read_b32 v103, a119\n"
    "v_fma_f32 v100, v100, v224, s94\n"
    "v_mfma_f32_16x16x32_bf16 a[140:143], v[164:167], v[156:159], a[140:143]\n"
    "s_waitcnt lgkmcnt(2)\n"
    "v_fma_f32 v101, v101, v224, s94\n"
    "v_fma_f32 v102, v102, v224, s94\n"
    "v_mfma_f32_16x16x32_bf16 a[140:143], v[168:171], v[44:47], a[140:143]\n"
    "ds_read_b128 v[160:163], v201 offset:18432\n"
    "v_fma_f32 v103, v103, v224, s94\n"
    "v_mfma_f32_16x16x32_bf16 a[140:143], v[172:175], v[76:79], a[140:143]\n"
    "v_cvt_pk_bf16_f32 v190, v100, v101\n"
    "v_cvt_pk_bf16_f32 v191, v102, v103\n"
    "v_mfma_f32_16x16x32_bf16 a[136:139], v[168:171], v[40:43], a[136:139]\n"
    "buffer_store_dwordx4 v[188:191], v205, s[28:31], s62 offen offset:192 sc0 sc1\n"
    "v_accvgpr_read_b32 v104, a120\n"
    "v_accvgpr_read_b32 v105, a121\n"
    "v_accvgpr_read_b32 v106, a122\n"
    "v_mfma_f32_16x16x32_bf16 a[136:139], v[172:175], v[72:75], a[136:139]\n"
    "v_accvgpr_read_b32 v107, a123\n"
    "v_fma_f32 v104, v104, v226, s94\n"
    "v_mfma_f32_16x16x32_bf16 a[132:135], v[168:171], v[36:39], a[132:135]\n"
    "ds_read_b128 v[164:167], v201 offset:19456\n"
    "v_fma_f32 v105, v105, v226, s94\n"
    "v_mfma_f32_16x16x32_bf16 a[132:135], v[172:175], v[68:71], a[132:135]\n"
    "v_fma_f32 v106, v106, v226, s94\n"
    "v_fma_f32 v107, v107, v226, s94\n"
    "v_mfma_f32_16x16x32_bf16 a[128:131], v[168:171], v[32:35], a[128:131]\n"
    "v_cvt_pk_bf16_f32 v250, v104, v105\n"
    "v_cvt_pk_bf16_f32 v251, v106, v107\n"
    "v_mfma_f32_16x16x32_bf16 a[128:131], v[172:175], v[64:67], a[128:131]\n"
    "s_waitcnt lgkmcnt(2)\n"
    "buffer_store_dwordx4 v[248:251], v205, s[28:31], s63 offen offset:192 sc0 sc1\n"
    "v_accvgpr_read_b32 v108, a124\n"
    "v_accvgpr_read_b32 v109, a125\n"
    "v_accvgpr_read_b32 v110, a126\n"
    "v_mfma_f32_16x16x32_bf16 a[128:131], v[176:179], v[232:235], a[128:131]\n"
    "ds_read_b128 v[168:171], v201 offset:43008\n"
    "v_accvgpr_read_b32 v111, a127\n"
    "v_mfma_f32_16x16x32_bf16 a[128:131], v[180:183], v[80:83], a[128:131]\n"
    "v_fma_f32 v108, v108, v228, s94\n"
    "v_fma_f32 v109, v109, v228, s94\n"
    "v_mfma_f32_16x16x32_bf16 a[132:135], v[176:179], v[236:239], a[132:135]\n"
    "v_fma_f32 v110, v110, v228, s94\n"
    "v_fma_f32 v111, v111, v228, s94\n"
    "v_mfma_f32_16x16x32_bf16 a[132:135], v[180:183], v[84:87], a[132:135]\n"
    "v_cvt_pk_bf16_f32 v254, v108, v109\n"
    "v_cvt_pk_bf16_f32 v255, v110, v111\n"
    "v_mfma_f32_16x16x32_bf16 a[136:139], v[176:179], v[240:243], a[136:139]\n"
    "ds_read_b128 v[172:175], v201 offset:44032\n"
    "v_mfma_f32_16x16x32_bf16 a[136:139], v[180:183], v[88:91], a[136:139]\n"
    "buffer_store_dwordx4 v[252:255], v205, s[28:31], s64 offen offset:192 sc0 sc1\n"
    "v_mfma_f32_16x16x32_bf16 a[140:143], v[176:179], v[244:247], a[140:143]\n"
    "v_mfma_f32_16x16x32_bf16 a[140:143], v[180:183], v[92:95], a[140:143]\n"
    "s_waitcnt lgkmcnt(2)\n"
    "v_accvgpr_read_b32 v96, a128\n"
    "v_accvgpr_read_b32 v97, a129\n"
    "v_mfma_f32_16x16x32_bf16 a[144:147], v[160:163], v[128:131], a[144:147]\n"
    "ds_read_b128 v[176:179], v207 offset:18432\n"
    "v_accvgpr_read_b32 v98, a130\n"
    "v_mfma_f32_16x16x32_bf16 a[144:147], v[164:167], v[144:147], a[144:147]\n"
    "v_accvgpr_read_b32 v99, a131\n"
    "v_fma_f32 v96, v96, v222, s94\n"
    "v_mfma_f32_16x16x32_bf16 a[148:151], v[160:163], v[132:135], a[148:151]\n"
    "v_fma_f32 v97, v97, v222, s94\n"
    "v_fma_f32 v98, v98, v222, s94\n"
    "v_mfma_f32_16x16x32_bf16 a[148:151], v[164:167], v[148:151], a[148:151]\n"
    "v_fma_f32 v99, v99, v222, s94\n"
    "v_cvt_pk_bf16_f32 v48, v96, v97\n"
    "v_mfma_f32_16x16x32_bf16 a[152:155], v[160:163], v[136:139], a[152:155]\n"
    "ds_read_b128 v[180:183], v207 offset:19456\n"
    "v_cvt_pk_bf16_f32 v49, v98, v99\n"
    "v_mfma_f32_16x16x32_bf16 a[152:155], v[164:167], v[152:155], a[152:155]\n"
    "v_accvgpr_read_b32 v100, a132\n"
    "v_accvgpr_read_b32 v101, a133\n"
    "v_mfma_f32_16x16x32_bf16 a[156:159], v[160:163], v[140:143], a[156:159]\n"
    "v_accvgpr_read_b32 v102, a134\n"
    "v_accvgpr_read_b32 v103, a135\n"
    "v_mfma_f32_16x16x32_bf16 a[156:159], v[164:167], v[156:159], a[156:159]\n"
    "s_waitcnt lgkmcnt(2)\n"
    "v_fma_f32 v100, v100, v224, s94\n"
    "v_fma_f32 v101, v101, v224, s94\n"
    "v_mfma_f32_16x16x32_bf16 a[156:159], v[168:171], v[44:47], a[156:159]\n"
    "ds_read_b128 v[160:163], v201 offset:20480\n"
    "v_fma_f32 v102, v102, v224, s94\n"
    "v_mfma_f32_16x16x32_bf16 a[156:159], v[172:175], v[76:79], a[156:159]\n"
    "v_fma_f32 v103, v103, v224, s94\n"
    "v_cvt_pk_bf16_f32 v52, v100, v101\n"
    "v_mfma_f32_16x16x32_bf16 a[152:155], v[168:171], v[40:43], a[152:155]\n"
    "v_cvt_pk_bf16_f32 v53, v102, v103\n"
    "v_accvgpr_read_b32 v104, a136\n"
    "v_mfma_f32_16x16x32_bf16 a[152:155], v[172:175], v[72:75], a[152:155]\n"
    "v_accvgpr_read_b32 v105, a137\n"
    "v_accvgpr_read_b32 v106, a138\n"
    "v_mfma_f32_16x16x32_bf16 a[148:151], v[168:171], v[36:39], a[148:151]\n"
    "ds_read_b128 v[164:167], v201 offset:21504\n"
    "v_accvgpr_read_b32 v107, a139\n"
    "v_mfma_f32_16x16x32_bf16 a[148:151], v[172:175], v[68:71], a[148:151]\n"
    "v_fma_f32 v104, v104, v226, s94\n"
    "v_fma_f32 v105, v105, v226, s94\n"
    "v_mfma_f32_16x16x32_bf16 a[144:147], v[168:171], v[32:35], a[144:147]\n"
    "v_fma_f32 v106, v106, v226, s94\n"
    "v_fma_f32 v107, v107, v226, s94\n"
    "v_mfma_f32_16x16x32_bf16 a[144:147], v[172:175], v[64:67], a[144:147]\n"
    "s_waitcnt lgkmcnt(2)\n"
    "v_cvt_pk_bf16_f32 v56, v104, v105\n"
    "v_cvt_pk_bf16_f32 v57, v106, v107\n"
    "v_mfma_f32_16x16x32_bf16 a[144:147], v[176:179], v[232:235], a[144:147]\n"
    "ds_read_b128 v[168:171], v201 offset:45056\n"
    "v_accvgpr_read_b32 v108, a140\n"
    "v_mfma_f32_16x16x32_bf16 a[144:147], v[180:183], v[80:83], a[144:147]\n"
    "v_accvgpr_read_b32 v109, a141\n"
    "v_accvgpr_read_b32 v110, a142\n"
    "v_mfma_f32_16x16x32_bf16 a[148:151], v[176:179], v[236:239], a[148:151]\n"
    "v_accvgpr_read_b32 v111, a143\n"
    "v_fma_f32 v108, v108, v228, s94\n"
    "v_mfma_f32_16x16x32_bf16 a[148:151], v[180:183], v[84:87], a[148:151]\n"
    "v_fma_f32 v109, v109, v228, s94\n"
    "v_fma_f32 v110, v110, v228, s94\n"
    "v_mfma_f32_16x16x32_bf16 a[152:155], v[176:179], v[240:243], a[152:155]\n"
    "ds_read_b128 v[172:175], v201 offset:46080\n"
    "v_fma_f32 v111, v111, v228, s94\n"
    "v_mfma_f32_16x16x32_bf16 a[152:155], v[180:183], v[88:91], a[152:155]\n"
    "v_cvt_pk_bf16_f32 v60, v108, v109\n"
    "v_cvt_pk_bf16_f32 v61, v110, v111\n"
    "v_mfma_f32_16x16x32_bf16 a[156:159], v[176:179], v[244:247], a[156:159]\n"
    "v_mfma_f32_16x16x32_bf16 a[156:159], v[180:183], v[92:95], a[156:159]\n"
    "s_waitcnt lgkmcnt(2)\n"
    "v_accvgpr_read_b32 v96, a144\n"
    "v_accvgpr_read_b32 v97, a145\n"
    "v_mfma_f32_16x16x32_bf16 a[160:163], v[160:163], v[128:131], a[160:163]\n"
    "ds_read_b128 v[176:179], v207 offset:20480\n"
    "v_accvgpr_read_b32 v98, a146\n"
    "v_mfma_f32_16x16x32_bf16 a[160:163], v[164:167], v[144:147], a[160:163]\n"
    "v_accvgpr_read_b32 v99, a147\n"
    "v_fma_f32 v96, v96, v222, s94\n"
    "v_mfma_f32_16x16x32_bf16 a[164:167], v[160:163], v[132:135], a[164:167]\n"
    "v_fma_f32 v97, v97, v222, s94\n"
    "v_fma_f32 v98, v98, v222, s94\n"
    "v_mfma_f32_16x16x32_bf16 a[164:167], v[164:167], v[148:151], a[164:167]\n"
    "v_fma_f32 v99, v99, v222, s94\n"
    "v_cvt_pk_bf16_f32 v50, v96, v97\n"
    "v_mfma_f32_16x16x32_bf16 a[168:171], v[160:163], v[136:139], a[168:171]\n"
    "ds_read_b128 v[180:183], v207 offset:21504\n"
    "v_cvt_pk_bf16_f32 v51, v98, v99\n"
    "v_mfma_f32_16x16x32_bf16 a[168:171], v[164:167], v[152:155], a[168:171]\n"
    "buffer_store_dwordx4 v[48:51], v205, s[28:31], 0 offen offset:256 sc0 sc1\n"
    "v_accvgpr_read_b32 v100, a148\n"
    "v_accvgpr_read_b32 v101, a149\n"
    "v_accvgpr_read_b32 v102, a150\n"
    "v_mfma_f32_16x16x32_bf16 a[172:175], v[160:163], v[140:143], a[172:175]\n"
    "v_accvgpr_read_b32 v103, a151\n"
    "v_fma_f32 v100, v100, v224, s94\n"
    "v_mfma_f32_16x16x32_bf16 a[172:175], v[164:167], v[156:159], a[172:175]\n"
    "s_waitcnt lgkmcnt(2)\n"
    "v_fma_f32 v101, v101, v224, s94\n"
    "v_fma_f32 v102, v102, v224, s94\n"
    "v_mfma_f32_16x16x32_bf16 a[172:175], v[168:171], v[44:47], a[172:175]\n"
    "ds_read_b128 v[160:163], v201 offset:22528\n"
    "v_fma_f32 v103, v103, v224, s94\n"
    "v_mfma_f32_16x16x32_bf16 a[172:175], v[172:175], v[76:79], a[172:175]\n"
    "v_cvt_pk_bf16_f32 v54, v100, v101\n"
    "v_cvt_pk_bf16_f32 v55, v102, v103\n"
    "v_mfma_f32_16x16x32_bf16 a[168:171], v[168:171], v[40:43], a[168:171]\n"
    "buffer_store_dwordx4 v[52:55], v205, s[28:31], s62 offen offset:256 sc0 sc1\n"
    "v_accvgpr_read_b32 v104, a152\n"
    "v_accvgpr_read_b32 v105, a153\n"
    "v_accvgpr_read_b32 v106, a154\n"
    "v_mfma_f32_16x16x32_bf16 a[168:171], v[172:175], v[72:75], a[168:171]\n"
    "v_accvgpr_read_b32 v107, a155\n"
    "v_fma_f32 v104, v104, v226, s94\n"
    "v_mfma_f32_16x16x32_bf16 a[164:167], v[168:171], v[36:39], a[164:167]\n"
    "ds_read_b128 v[164:167], v201 offset:23552\n"
    "v_fma_f32 v105, v105, v226, s94\n"
    "v_mfma_f32_16x16x32_bf16 a[164:167], v[172:175], v[68:71], a[164:167]\n"
    "v_fma_f32 v106, v106, v226, s94\n"
    "v_fma_f32 v107, v107, v226, s94\n"
    "v_mfma_f32_16x16x32_bf16 a[160:163], v[168:171], v[32:35], a[160:163]\n"
    "s_cbranch_execz LDRYR\n"
    "LDF16:\n"
    "v_cvt_pk_bf16_f32 v58, v104, v105\n"
    "v_cvt_pk_bf16_f32 v59, v106, v107\n"
    "v_mfma_f32_16x16x32_bf16 a[160:163], v[172:175], v[64:67], a[160:163]\n"
    "s_waitcnt lgkmcnt(2)\n"
    "buffer_store_dwordx4 v[56:59], v205, s[28:31], s63 offen offset:256 sc0 sc1\n"
    "v_accvgpr_read_b32 v108, a156\n"
    "v_accvgpr_read_b32 v109, a157\n"
    "v_accvgpr_read_b32 v110, a158\n"
    "v_mfma_f32_16x16x32_bf16 a[160:163], v[176:179], v[232:235], a[160:163]\n"
    "ds_read_b128 v[168:171], v201 offset:47104\n"
    "v_accvgpr_read_b32 v111, a159\n"
    "v_mfma_f32_16x16x32_bf16 a[160:163], v[180:183], v[80:83], a[160:163]\n"
    "v_fma_f32 v108, v108, v228, s94\n"
    "v_fma_f32 v109, v109, v228, s94\n"
    "v_mfma_f32_16x16x32_bf16 a[164:167], v[176:179], v[236:239], a[164:167]\n"
    "v_fma_f32 v110, v110, v228, s94\n"
    "v_fma_f32 v111, v111, v228, s94\n"
    "v_mfma_f32_16x16x32_bf16 a[164:167], v[180:183], v[84:87], a[164:167]\n"
    "v_cvt_pk_bf16_f32 v62, v108, v109\n"
    "v_cvt_pk_bf16_f32 v63, v110, v111\n"
    "v_mfma_f32_16x16x32_bf16 a[168:171], v[176:179], v[240:243], a[168:171]\n"
    "ds_read_b128 v[172:175], v201 offset:48128\n"
    "v_mfma_f32_16x16x32_bf16 a[168:171], v[180:183], v[88:91], a[168:171]\n"
    "buffer_store_dwordx4 v[60:63], v205, s[28:31], s64 offen offset:256 sc0 sc1\n"
    "v_mfma_f32_16x16x32_bf16 a[172:175], v[176:179], v[244:247], a[172:175]\n"
    "v_mfma_f32_16x16x32_bf16 a[172:175], v[180:183], v[92:95], a[172:175]\n"
    "s_waitcnt lgkmcnt(2)\n"
    "v_accvgpr_read_b32 v96, a160\n"
    "v_accvgpr_read_b32 v97, a161\n"
    "v_mfma_f32_16x16x32_bf16 a[176:179], v[160:163], v[128:131], a[176:179]\n"
    "ds_read_b128 v[176:179], v207 offset:22528\n"
    "v_accvgpr_read_b32 v98, a162\n"
    "v_mfma_f32_16x16x32_bf16 a[176:179], v[164:167], v[144:147], a[176:179]\n"
    "v_accvgpr_read_b32 v99, a163\n"
    "v_fma_f32 v96, v96, v222, s94\n"
    "v_mfma_f32_16x16x32_bf16 a[180:183], v[160:163], v[132:135], a[180:183]\n"
    "v_fma_f32 v97, v97, v222, s94\n"
    "v_fma_f32 v98, v98, v222, s94\n"
    "v_mfma_f32_16x16x32_bf16 a[180:183], v[164:167], v[148:151], a[180:183]\n"
    "v_fma_f32 v99, v99, v222, s94\n"
    "v_cvt_pk_bf16_f32 v184, v96, v97\n"
    "v_mfma_f32_16x16x32_bf16 a[184:187], v[160:163], v[136:139], a[184:187]\n"
    "ds_read_b128 v[180:183], v207 offset:23552\n"
    "v_cvt_pk_bf16_f32 v185, v98, v99\n"
    "v_mfma_f32_16x16x32_bf16 a[184:187], v[164:167], v[152:155], a[184:187]\n"
    "v_accvgpr_read_b32 v100, a164\n"
    "v_accvgpr_read_b32 v101, a165\n"
    "v_mfma_f32_16x16x32_bf16 a[188:191], v[160:163], v[140:143], a[188:191]\n"
    "v_accvgpr_read_b32 v102, a166\n"
    "v_accvgpr_read_b32 v103, a167\n"
    "v_mfma_f32_16x16x32_bf16 a[188:191], v[164:167], v[156:159], a[188:191]\n"
    "s_waitcnt lgkmcnt(2)\n"
    "v_fma_f32 v100, v100, v224, s94\n"
    "v_fma_f32 v101, v101, v224, s94\n"
    "v_mfma_f32_16x16x32_bf16 a[188:191], v[168:171], v[44:47], a[188:191]\n"
    "v_fma_f32 v102, v102, v224, s94\n"
    "v_fma_f32 v103, v103, v224, s94\n"
    "v_mfma_f32_16x16x32_bf16 a[188:191], v[172:175], v[76:79], a[188:191]\n"
    "v_cvt_pk_bf16_f32 v188, v100, v101\n"
    "v_cvt_pk_bf16_f32 v189, v102, v103\n"
    "v_mfma_f32_16x16x32_bf16 a[184:187], v[168:171], v[40:43], a[184:187]\n"
    "v_accvgpr_read_b32 v104, a168\n"
    "v_accvgpr_read_b32 v105, a169\n"
    "v_mfma_f32_16x16x32_bf16 a[184:187], v[172:175], v[72:75], a[184:187]\n"
    "v_accvgpr_read_b32 v106, a170\n"
    "v_accvgpr_read_b32 v107, a171\n"
    "v_mfma_f32_16x16x32_bf16 a[180:183], v[168:171], v[36:39], a[180:183]\n"
    "v_fma_f32 v104, v104, v226, s94\n"
    "v_fma_f32 v105, v105, v226, s94\n"
    "v_mfma_f32_16x16x32_bf16 a[180:183], v[172:175], v[68:71], a[180:183]\n"
    "v_fma_f32 v106, v106, v226, s94\n"
    "v_fma_f32 v107, v107, v226, s94\n"
    "v_mfma_f32_16x16x32_bf16 a[176:179], v[168:171], v[32:35], a[176:179]\n"
    "v_cvt_pk_bf16_f32 v248, v104, v105\n"
    "v_cvt_pk_bf16_f32 v249, v106, v107\n"
    "v_mfma_f32_16x16x32_bf16 a[176:179], v[172:175], v[64:67], a[176:179]\n"
    "s_waitcnt lgkmcnt(0)\n"
    "v_accvgpr_read_b32 v108, a172\n"
    "v_accvgpr_read_b32 v109, a173\n"
    "v_mfma_f32_16x16x32_bf16 a[176:179], v[176:179], v[232:235], a[176:179]\n"
    "v_accvgpr_read_b32 v110, a174\n"
    "v_accvgpr_read_b32 v111, a175\n"
    "v_mfma_f32_16x16x32_bf16 a[176:179], v[180:183], v[80:83], a[176:179]\n"
    "v_fma_f32 v108, v108, v228, s94\n"
    "v_fma_f32 v109, v109, v228, s94\n"
    "v_mfma_f32_16x16x32_bf16 a[180:183], v[176:179], v[236:239], a[180:183]\n"
    "v_fma_f32 v110, v110, v228, s94\n"
    "v_fma_f32 v111, v111, v228, s94\n"
    "v_mfma_f32_16x16x32_bf16 a[180:183], v[180:183], v[84:87], a[180:183]\n"
    "v_cvt_pk_bf16_f32 v252, v108, v109\n"
    "v_cvt_pk_bf16_f32 v253, v110, v111\n"
    "v_mfma_f32_16x16x32_bf16 a[184:187], v[176:179], v[240:243], a[184:187]\n"
    "v_mfma_f32_16x16x32_bf16 a[184:187], v[180:183], v[88:91], a[184:187]\n"
    "v_mfma_f32_16x16x32_bf16 a[188:191], v[176:179], v[244:247], a[188:191]\n"
    "s_cbranch_execz LDRYR\n"
    "v_mfma_f32_16x16x32_bf16 a[188:191], v[180:183], v[92:95], a[188:191]\n"
    "v_accvgpr_read_b32 v96, a176\n"
    "v_accvgpr_read_b32 v97, a177\n"
    "v_accvgpr_read_b32 v98, a178\n"
    "v_accvgpr_read_b32 v99, a179\n"
    "v_fma_f32 v96, v96, v222, s94\n"
    "v_fma_f32 v97, v97, v222, s94\n"
    "v_fma_f32 v98, v98, v222, s94\n"
    "v_fma_f32 v99, v99, v222, s94\n"
    "v_cvt_pk_bf16_f32 v186, v96, v97\n"
    "v_cvt_pk_bf16_f32 v187, v98, v99\n"
    "buffer_store_dwordx4 v[184:187], v205, s[28:31], 0 offen offset:320 sc0 sc1\n"
    "v_accvgpr_read_b32 v100, a180\n"
    "v_accvgpr_read_b32 v101, a181\n"
    "v_accvgpr_read_b32 v102, a182\n"
    "v_accvgpr_read_b32 v103, a183\n"
    "v_fma_f32 v100, v100, v224, s94\n"
    "v_fma_f32 v101, v101, v224, s94\n"
    "v_fma_f32 v102, v102, v224, s94\n"
    "v_fma_f32 v103, v103, v224, s94\n"
    "v_cvt_pk_bf16_f32 v190, v100, v101\n"
    "v_cvt_pk_bf16_f32 v191, v102, v103\n"
    "buffer_store_dwordx4 v[188:191], v205, s[28:31], s62 offen offset:320 sc0 sc1\n"
    "v_accvgpr_read_b32 v104, a184\n"
    "v_accvgpr_read_b32 v105, a185\n"
    "v_accvgpr_read_b32 v106, a186\n"
    "v_accvgpr_read_b32 v107, a187\n"
    "v_fma_f32 v104, v104, v226, s94\n"
    "v_fma_f32 v105, v105, v226, s94\n"
    "v_fma_f32 v106, v106, v226, s94\n"
    "v_fma_f32 v107, v107, v226, s94\n"
    "v_cvt_pk_bf16_f32 v250, v104, v105\n"
    "v_cvt_pk_bf16_f32 v251, v106, v107\n"
    "buffer_store_dwordx4 v[248:251], v205, s[28:31], s63 offen offset:320 sc0 sc1\n"
    "v_accvgpr_read_b32 v108, a188\n"
    "v_accvgpr_read_b32 v109, a189\n"
    "v_accvgpr_read_b32 v110, a190\n"
    "v_accvgpr_read_b32 v111, a191\n"
    "v_fma_f32 v108, v108, v228, s94\n"
    "v_fma_f32 v109, v109, v228, s94\n"
    "v_fma_f32 v110, v110, v228, s94\n"
    "v_fma_f32 v111, v111, v228, s94\n"
    "v_cvt_pk_bf16_f32 v254, v108, v109\n"
    "v_cvt_pk_bf16_f32 v255, v110, v111\n"
    "buffer_store_dwordx4 v[252:255], v205, s[28:31], s64 offen offset:320 sc0 sc1\n"
    "v_add_f32 v196, v112, v116\n"
    "v_add_f32 v197, v120, v124\n"
    "v_add_f32 v196, v196, v197\n"
    "v_cmp_ngt_f32 vcc, 0x45800000, v196\n"
    "s_barrier\n"
    "s_cbranch_vccnz LSLOW\n"
    "LFIN:\n"
    "s_branch LEND\n"
    "LSPRE0:\n"
    "LSPIN0:\n"
    "s_sleep 1\n"
    "buffer_load_dwordx2 v[218:219], v213, s[44:47], 0 offen sc1\n"
    "s_waitcnt vmcnt(0)\n"
    "s_add_u32 s73, s73, 1\n"
    "s_cmp_gt_u32 s73, 200000\n"
    "s_cbranch_scc1 LSPB0\n"
    "v_cmp_ne_u32 s[80:81], s67, v219\n"
    "v_cmp_gt_u32 vcc, s66, v218\n"
    "s_or_b64 vcc, vcc, s[80:81]\n"
    "s_cbranch_vccnz LSPIN0\n"
    "s_branch LSPB0\n"
    "LSPRE1:\n"
    "LSPIN1:\n"
    "s_sleep 1\n"
    "buffer_load_dwordx2 v[218:219], v213, s[44:47], 0 offen sc1\n"
    "s_waitcnt vmcnt(0)\n"
    "s_add_u32 s73, s73, 1\n"
    "s_cmp_gt_u32 s73, 200000\n"
    "s_cbranch_scc1 LSPB1\n"
    "v_cmp_ne_u32 s[80:81], s67, v219\n"
    "v_cmp_gt_u32 vcc, s66, v218\n"
    "s_or_b64 vcc, vcc, s[80:81]\n"
    "s_cbranch_vccnz LSPIN1\n"
    "s_branch LSPB1\n"
    "LSPRE2:\n"
    "LSPIN2:\n"
    "s_sleep 1\n"
    "buffer_load_dwordx2 v[218:219], v213, s[44:47], 0 offen sc1\n"
    "s_waitcnt vmcnt(0)\n"
    "s_add_u32 s73, s73, 1\n"
    "s_cmp_gt_u32 s73, 200000\n"
    "s_cbranch_scc1 LSPB2\n"
    "v_cmp_ne_u32 s[80:81], s67, v219\n"
    "v_cmp_gt_u32 vcc, s66, v218\n"
    "s_or_b64 vcc, vcc, s[80:81]\n"
    "s_cbranch_vccnz LSPIN2\n"
    "s_branch LSPB2\n"
    "LSPRE3:\n"
    "LSPIN3:\n"
    "s_sleep 1\n"
    "buffer_load_dwordx2 v[218:219], v213, s[44:47], 0 offen sc1\n"
    "s_waitcnt vmcnt(0)\n"
    "s_add_u32 s73, s73, 1\n"
    "s_cmp_gt_u32 s73, 200000\n"
    "s_cbranch_scc1 LSPB3\n"
    "v_cmp_ne_u32 s[80:81], s67, v219\n"
    "v_cmp_gt_u32 vcc, s66, v218\n"
    "s_or_b64 vcc, vcc, s[80:81]\n"
    "s_cbranch_vccnz LSPIN3\n"
    "s_branch LSPB3\n"
    "LSPRE4:\n"
    "LSPIN4:\n"
    "s_sleep 1\n"
    "buffer_load_dwordx2 v[218:219], v213, s[44:47], 0 offen sc1\n"
    "s_waitcnt vmcnt(0)\n"
    "s_add_u32 s73, s73, 1\n"
    "s_cmp_gt_u32 s73, 200000\n"
    "s_cbranch_scc1 LSPB4\n"
    "v_cmp_ne_u32 s[80:81], s67, v219\n"
    "v_cmp_gt_u32 vcc, s66, v218\n"
    "s_or_b64 vcc, vcc, s[80:81]\n"
    "s_cbranch_vccnz LSPIN4\n"
    "s_branch LSPB4\n"
    "LSPRE5:\n"
    "LSPIN5:\n"
    "s_sleep 1\n"
    "buffer_load_dwordx2 v[218:219], v213, s[44:47], 0 offen sc1\n"
    "s_waitcnt vmcnt(0)\n"
    "s_add_u32 s73, s73, 1\n"
    "s_cmp_gt_u32 s73, 200000\n"
    "s_cbranch_scc1 LSPB5\n"
    "v_cmp_ne_u32 s[80:81], s67, v219\n"
    "v_cmp_gt_u32 vcc, s66, v218\n"
    "s_or_b64 vcc, vcc, s[80:81]\n"
    "s_cbranch_vccnz LSPIN5\n"
    "s_branch LSPB5\n"
    "LDRYR:\n"
    "s_setpc_b64 s[100:101]\n"
    "LSLOW:\n"
    "s_waitcnt vmcnt(0)\n"
    "s_nop 15\n"
    "s_mul_i32 s62, s58, 0x9000\n"
    "v_add_u32 v205, s62, v200\n"
    "s_mov_b32 s63, 0\n"
    "v_mov_b32 v128, 0\n"
    "v_mov_b32 v129, 0\n"
    "v_mov_b32 v130, 0\n"
    "v_mov_b32 v131, 0\n"
    "v_mov_b32 v96, 0xff61b1e6\n"
    "v_mov_b32 v100, 0xff61b1e6\n"
    "v_mov_b32 v104, 0xff61b1e6\n"
    "v_mov_b32 v108, 0xff61b1e6\n"
    "v_mfma_f32_16x16x32_bf16 a[0:3], v[128:131], v[128:131], 0\n"
    "v_mfma_f32_16x16x32_bf16 a[4:7], v[128:131], v[128:131], 0\n"
    "v_mfma_f32_16x16x32_bf16 a[8:11], v[128:131], v[128:131], 0\n"
    "v_mfma_f32_16x16x32_bf16 a[12:15], v[128:131], v[128:131], 0\n"
    "v_mfma_f32_16x16x32_bf16 a[16:19], v[128:131], v[128:131], 0\n"
    "v_mfma_f32_16x16x32_bf16 a[20:23], v[128:131], v[128:131], 0\n"
    "v_mfma_f32_16x16x32_bf16 a[24:27], v[128:131], v[128:131], 0\n"
    "v_mfma_f32_16x16x32_bf16 a[28:31], v[128:131], v[128:131], 0\n"
    "v_mfma_f32_16x16x32_bf16 a[32:35], v[128:131], v[128:131], 0\n"
    "v_mfma_f32_16x16x32_bf16 a[36:39], v[128:131], v[128:131], 0\n"
    "v_mfma_f32_16x16x32_bf16 a[40:43], v[128:131], v[128:131], 0\n"
    "v_mfma_f32_16x16x32_bf16 a[44:47], v[128:131], v[128:131], 0\n"
    "v_mfma_f32_16x16x32_bf16 a[48:51], v[128:131], v[128:131], 0\n"
    "v_mfma_f32_16x16x32_bf16 a[52:55], v[128:131], v[128:131], 0\n"
    "v_mfma_f32_16x16x32_bf16 a[56:59], v[128:131], v[128:131], 0\n"
    "v_mfma_f32_16x16x32_bf16 a[60:63], v[128:131], v[128:131], 0\n"
    "v_mfma_f32_16x16x32_bf16 a[64:67], v[128:131], v[128:131], 0\n"
    "v_mfma_f32_16x16x32_bf16 a[68:71], v[128:131], v[128:131], 0\n"
    "v_mfma_f32_16x16x32_bf16 a[72:75], v[128:131], v[128:131], 0\n"
    "v_mfma_f32_16x16x32_bf16 a[76:79], v[128:131], v[128:131], 0\n"
    "v_mfma_f32_16x16x32_bf16 a[80:83], v[128:131], v[128:131], 0\n"
    "v_mfma_f32_16x16x32_bf16 a[84:87], v[128:131], v[128:131], 0\n"
    "v_mfma_f32_16x16x32_bf16 a[88:91], v[128:131], v[128:131], 0\n"
    "v_mfma_f32_16x16x32_bf16 a[92:95], v[128:131], v[128:131], 0\n"
    "v_mfma_f32_16x16x32_bf16 a[96:99], v[128:131], v[128:131], 0\n"
    "v_mfma_f32_16x16x32_bf16 a[100:103], v[128:131], v[128:131], 0\n"
    "v_mfma_f32_16x16x32_bf16 a[104:107], v[128:131], v[128:131], 0\n"
    "v_mfma_f32_16x16x32_bf16 a[108:111], v[128:131], v[128:131], 0\n"
    "v_mfma_f32_16x16x32_bf16 a[112:115], v[128:131], v[128:131], 0\n"
    "v_mfma_f32_16x16x32_bf16 a[116:119], v[128:131], v[128:131], 0\n"
    "v_mfma_f32_16x16x32_bf16 a[120:123], v[128:131], v[128:131], 0\n"
    "v_mfma_f32_16x16x32_bf16 a[124:127], v[128:131], v[128:131], 0\n"
    "v_mfma_f32_16x16x32_bf16 a[128:131], v[128:131], v[128:131], 0\n"
    "v_mfma_f32_16x16x32_bf16 a[132:135], v[128:131], v[128:131], 0\n"
    "v_mfma_f32_16x16x32_bf16 a[136:139], v[128:131], v[128:131], 0\n"
    "v_mfma_f32_16x16x32_bf16 a[140:143], v[128:131], v[128:131], 0\n"
    "v_mfma_f32_16x16x32_bf16 a[144:147], v[128:131], v[128:131], 0\n"
    "v_mfma_f32_16x16x32_bf16 a[148:151], v[128:131], v[128:131], 0\n"
    "v_mfma_f32_16x16x32_bf16 a[152:155], v[128:131], v[128:131], 0\n"
    "v_mfma_f32_16x16x32_bf16 a[156:159], v[128:131], v[128:131], 0\n"
    "v_mfma_f32_16x16x32_bf16 a[160:163], v[128:131], v[128:131], 0\n"
    "v_mfma_f32_16x16x32_bf16 a[164:167], v[128:131], v[128:131], 0\n"
    "v_mfma_f32_16x16x32_bf16 a[168:171], v[128:131], v[128:131], 0\n"
    "v_mfma_f32_16x16x32_bf16 a[172:175], v[128:131], v[128:131], 0\n"
    "v_mfma_f32_16x16x32_bf16 a[176:179], v[128:131], v[128:131], 0\n"
    "v_mfma_f32_16x16x32_bf16 a[180:183], v[128:131], v[128:131], 0\n"
    "v_mfma_f32_16x16x32_bf16 a[184:187], v[128:131], v[128:131], 0\n"
    "v_mfma_f32_16x16x32_bf16 a[188:191], v[128:131], v[128:131], 0\n"
    "v_mfma_f32_16x16x32_bf16 v[112:115], v[128:131], v[128:131], 0\n"
    "v_mfma_f32_16x16x32_bf16 v[116:119], v[128:131], v[128:131], 0\n"
    "v_mfma_f32_16x16x32_bf16 v[120:123], v[128:131], v[128:131], 0\n"
    "v_mfma_f32_16x16x32_bf16 v[124:127], v[128:131], v[128:131], 0\n"
    "s_nop 15\n"
    "LSLOOP:\n"
    "s_add_u32 s78, s63, 0\n"
    "s_add_u32 m0, s62, 0\n"
    "s_nop 0\n"
    "buffer_load_dwordx4 v200, s[36:39], s78 offen sc1 lds\n"
    "buffer_load_dwordx4 v200, s[36:39], s78 offen offset:1024 sc1 lds\n"
    "buffer_load_dwordx4 v200, s[36:39], s78 offen offset:2048 sc1 lds\n"
    "buffer_load_dwordx4 v200, s[36:39], s78 offen offset:3072 sc1 lds\n"
    "s_add_u32 s78, s63, 0x1000\n"
    "s_add_u32 m0, s62, 0x1000\n"
    "s_nop 0\n"
    "buffer_load_dwordx4 v200, s[36:39], s78 offen sc1 lds\n"
    "buffer_load_dwordx4 v200, s[36:39], s78 offen offset:1024 sc1 lds\n"
    "buffer_load_dwordx4 v200, s[36:39], s78 offen offset:2048 sc1 lds\n"
    "buffer_load_dwordx4 v200, s[36:39], s78 offen offset:3072 sc1 lds\n"
    "s_add_u32 s78, s63, 0x2000\n"
    "s_add_u32 m0, s62, 0x2000\n"
    "s_nop 0\n"
    "buffer_load_dwordx4 v200, s[36:39], s78 offen sc1 lds\n"
    "buffer_load_dwordx4 v200, s[36:39], s78 offen offset:1024 sc1 lds\n"
    "buffer_load_dwordx4 v200, s[36:39], s78 offen offset:2048 sc1 lds\n"
    "buffer_load_dwordx4 v200, s[36:39], s78 offen offset:3072 sc1 lds\n"
    "s_add_u32 s78, s63, 0\n"
    "s_add_u32 m0, s62, 0x3000\n"
    "s_nop 0\n"
    "buffer_load_dwordx4 v200, s[40:43], s78 offen sc1 lds\n"
    "buffer_load_dwordx4 v200, s[40:43], s78 offen offset:1024 sc1 lds\n"
    "buffer_load_dwordx4 v200, s[40:43], s78 offen offset:2048 sc1 lds\n"
    "buffer_load_dwordx4 v200, s[40:43], s78 offen offset:3072 sc1 lds\n"
    "s_add_u32 s78, s63, 0x1000\n"
    "s_add_u32 m0, s62, 0x4000\n"
    "s_nop 0\n"
    "buffer_load_dwordx4 v200, s[40:43], s78 offen sc1 lds\n"
    "buffer_load_dwordx4 v200, s[40:43], s78 offen offset:1024 sc1 lds\n"
    "buffer_load_dwordx4 v200, s[40:43], s78 offen offset:2048 sc1 lds\n"
    "buffer_load_dwordx4 v200, s[40:43], s78 offen offset:3072 sc1 lds\n"
    "s_add_u32 s78, s63, 0x2000\n"
    "s_add_u32 m0, s62, 0x5000\n"
    "s_nop 0\n"
    "buffer_load_dwordx4 v200, s[40:43], s78 offen sc1 lds\n"
    "buffer_load_dwordx4 v200, s[40:43], s78 offen offset:1024 sc1 lds\n"
    "buffer_load_dwordx4 v200, s[40:43], s78 offen offset:2048 sc1 lds\n"
    "buffer_load_dwordx4 v200, s[40:43], s78 offen offset:3072 sc1 lds\n"
    "s_add_u32 s78, s63, 0x3000\n"
    "s_add_u32 m0, s62, 0x6000\n"
    "s_nop 0\n"
    "buffer_load_dwordx4 v200, s[40:43], s78 offen sc1 lds\n"
    "buffer_load_dwordx4 v200, s[40:43], s78 offen offset:1024 sc1 lds\n"
    "buffer_load_dwordx4 v200, s[40:43], s78 offen offset:2048 sc1 lds\n"
    "buffer_load_dwordx4 v200, s[40:43], s78 offen offset:3072 sc1 lds\n"
    "s_add_u32 s78, s63, 0x4000\n"
    "s_add_u32 m0, s62, 0x7000\n"
    "s_nop 0\n"
    "buffer_load_dwordx4 v200, s[40:43], s78 offen sc1 lds\n"
    "buffer_load_dwordx4 v200, s[40:43], s78 offen offset:1024 sc1 lds\n"
    "buffer_load_dwordx4 v200, s[40:43], s78 offen offset:2048 sc1 lds\n"
    "buffer_load_dwordx4 v200, s[40:43], s78 offen offset:3072 sc1 lds\n"
    "s_add_u32 s78, s63, 0x5000\n"
    "s_add_u32 m0, s62, 0x8000\n"
    "s_nop 0\n"
    "buffer_load_dwordx4 v200, s[40:43], s78 offen sc1 lds\n"
    "buffer_load_dwordx4 v200, s[40:43], s78 offen offset:1024 sc1 lds\n"
    "buffer_load_dwordx4 v200, s[40:43], s78 offen offset:2048 sc1 lds\n"
    "buffer_load_dwordx4 v200, s[40:43], s78 offen offset:3072 sc1 lds\n"
    "s_waitcnt vmcnt(0)\n"
    "ds_read_b128 v[160:163], v205\n"
    "ds_read_b128 v[164:167], v205 offset:1024\n"
    "s_waitcnt lgkmcnt(1)\n"
    "v_mfma_f32_16x16x32_bf16 v[32:35], v[160:163], a[192:195], 0\n"
    "s_waitcnt lgkmcnt(0)\n"
    "v_mfma_f32_16x16x32_bf16 v[32:35], v[164:167], a[196:199], v[32:35]\n"
    "v_mfma_f32_16x16x32_bf16 v[36:39], v[164:167], a[220:223], 0\n"
    "v_mfma_f32_16x16x32_bf16 v[36:39], v[160:163], a[216:219], v[36:39]\n"
    "v_mfma_f32_16x16x32_bf16 v[40:43], v[160:163], a[240:243], 0\n"
    "v_mfma_f32_16x16x32_bf16 v[40:43], v[164:167], a[244:247], v[40:43]\n"
    "v_mfma_f32_16x16x32_bf16 v[44:47], v[164:167], v[12:15], 0\n"
    "v_mfma_f32_16x16x32_bf16 v[44:47], v[160:163], v[8:11], v[44:47]\n"
    "ds_read_b128 v[168:171], v205 offset:2048\n"
    "ds_read_b128 v[172:175], v205 offset:3072\n"
    "s_waitcnt lgkmcnt(1)\n"
    "v_mfma_f32_16x16x32_bf16 v[44:47], v[168:171], v[16:19], v[44:47]\n"
    "s_waitcnt lgkmcnt(0)\n"
    "v_mfma_f32_16x16x32_bf16 v[44:47], v[172:175], v[20:23], v[44:47]\n"
    "v_mfma_f32_16x16x32_bf16 v[40:43], v[172:175], a[252:255], v[40:43]\n"
    "v_mfma_f32_16x16x32_bf16 v[40:43], v[168:171], a[248:251], v[40:43]\n"
    "v_mfma_f32_16x16x32_bf16 v[36:39], v[168:171], a[224:227], v[36:39]\n"
    "v_mfma_f32_16x16x32_bf16 v[36:39], v[172:175], a[228:231], v[36:39]\n"
    "v_mfma_f32_16x16x32_bf16 v[32:35], v[172:175], a[204:207], v[32:35]\n"
    "v_mfma_f32_16x16x32_bf16 v[32:35], v[168:171], a[200:203], v[32:35]\n"
    "ds_read_b128 v[176:179], v205 offset:4096\n"
    "ds_read_b128 v[180:183], v205 offset:5120\n"
    "s_waitcnt lgkmcnt(1)\n"
    "v_mfma_f32_16x16x32_bf16 v[32:35], v[176:179], a[208:211], v[32:35]\n"
    "s_waitcnt lgkmcnt(0)\n"
    "v_mfma_f32_16x16x32_bf16 v[32:35], v[180:183], a[212:215], v[32:35]\n"
    "v_mfma_f32_16x16x32_bf16 v[36:39], v[180:183], a[236:239], v[36:39]\n"
    "v_mfma_f32_16x16x32_bf16 v[36:39], v[176:179], a[232:235], v[36:39]\n"
    "v_mfma_f32_16x16x32_bf16 v[40:43], v[176:179], v[0:3], v[40:43]\n"
    "v_mfma_f32_16x16x32_bf16 v[40:43], v[180:183], v[4:7], v[40:43]\n"
    "v_mfma_f32_16x16x32_bf16 v[44:47], v[180:183], v[28:31], v[44:47]\n"
    "v_mfma_f32_16x16x32_bf16 v[44:47], v[176:179], v[24:27], v[44:47]\n"
    "ds_read_b128 v[160:163], v205 offset:10240\n"
    "ds_read_b128 v[164:167], v205 offset:11264\n"
    "s_waitcnt lgkmcnt(1)\n"
    "v_mfma_f32_16x16x32_bf16 v[60:63], v[160:163], v[24:27], 0\n"
    "s_waitcnt lgkmcnt(0)\n"
    "v_mfma_f32_16x16x32_bf16 v[60:63], v[164:167], v[28:31], v[60:63]\n"
    "v_mfma_f32_16x16x32_bf16 v[56:59], v[164:167], v[4:7], 0\n"
    "v_mfma_f32_16x16x32_bf16 v[56:59], v[160:163], v[0:3], v[56:59]\n"
    "v_mfma_f32_16x16x32_bf16 v[52:55], v[160:163], a[232:235], 0\n"
    "v_mfma_f32_16x16x32_bf16 v[52:55], v[164:167], a[236:239], v[52:55]\n"
    "v_mfma_f32_16x16x32_bf16 v[48:51], v[164:167], a[212:215], 0\n"
    "v_mfma_f32_16x16x32_bf16 v[48:51], v[160:163], a[208:211], v[48:51]\n"
    "ds_read_b128 v[168:171], v205 offset:8192\n"
    "ds_read_b128 v[172:175], v205 offset:9216\n"
    "s_waitcnt lgkmcnt(1)\n"
    "v_mfma_f32_16x16x32_bf16 v[48:51], v[168:171], a[200:203], v[48:51]\n"
    "s_waitcnt lgkmcnt(0)\n"
    "v_mfma_f32_16x16x32_bf16 v[48:51], v[172:175], a[204:207], v[48:51]\n"
    "v_mfma_f32_16x16x32_bf16 v[52:55], v[172:175], a[228:231], v[52:55]\n"
    "v_mfma_f32_16x16x32_bf16 v[52:55], v[168:171], a[224:227], v[52:55]\n"
    "v_mfma_f32_16x16x32_bf16 v[56:59], v[168:171], a[248:251], v[56:59]\n"
    "v_mfma_f32_16x16x32_bf16 v[56:59], v[172:175], a[252:255], v[56:59]\n"
    "v_mfma_f32_16x16x32_bf16 v[60:63], v[172:175], v[20:23], v[60:63]\n"
    "v_mfma_f32_16x16x32_bf16 v[60:63], v[168:171], v[16:19], v[60:63]\n"
    "ds_read_b128 v[176:179], v205 offset:6144\n"
    "ds_read_b128 v[180:183], v205 offset:7168\n"
    "s_waitcnt lgkmcnt(1)\n"
    "v_mfma_f32_16x16x32_bf16 v[60:63], v[176:179], v[8:11], v[60:63]\n"
    "s_waitcnt lgkmcnt(0)\n"
    "v_mfma_f32_16x16x32_bf16 v[60:63], v[180:183], v[12:15], v[60:63]\n"
    "v_mfma_f32_16x16x32_bf16 v[56:59], v[180:183], a[244:247], v[56:59]\n"
    "v_mfma_f32_16x16x32_bf16 v[56:59], v[176:179], a[240:243], v[56:59]\n"
    "v_mfma_f32_16x16x32_bf16 v[52:55], v[176:179], a[216:219], v[52:55]\n"
    "v_mfma_f32_16x16x32_bf16 v[52:55], v[180:183], a[220:223], v[52:55]\n"
    "v_mfma_f32_16x16x32_bf16 v[48:51], v[180:183], a[196:199], v[48:51]\n"
    "v_mfma_f32_16x16x32_bf16 v[48:51], v[176:179], a[192:195], v[48:51]\n"
    "s_add_u32 s78, s63, 0x3000\n"
    "s_add_u32 m0, s62, 0\n"
    "s_nop 0\n"
    "buffer_load_dwordx4 v200, s[36:39], s78 offen sc1 lds\n"
    "buffer_load_dwordx4 v200, s[36:39], s78 offen offset:1024 sc1 lds\n"
    "buffer_load_dwordx4 v200, s[36:39], s78 offen offset:2048 sc1 lds\n"
    "buffer_load_dwordx4 v200, s[36:39], s78 offen offset:3072 sc1 lds\n"
    "s_add_u32 s78, s63, 0x4000\n"
    "s_add_u32 m0, s62, 0x1000\n"
    "s_nop 0\n"
    "buffer_load_dwordx4 v200, s[36:39], s78 offen sc1 lds\n"
    "buffer_load_dwordx4 v200, s[36:39], s78 offen offset:1024 sc1 lds\n"
    "buffer_load_dwordx4 v200, s[36:39], s78 offen offset:2048 sc1 lds\n"
    "buffer_load_dwordx4 v200, s[36:39], s78 offen offset:3072 sc1 lds\n"
    "s_add_u32 s78, s63, 0x5000\n"
    "s_add_u32 m0, s62, 0x2000\n"
    "s_nop 0\n"
    "buffer_load_dwordx4 v200, s[36:39], s78 offen sc1 lds\n"
    "buffer_load_dwordx4 v200, s[36:39], s78 offen offset:1024 sc1 lds\n"
    "buffer_load_dwordx4 v200, s[36:39], s78 offen offset:2048 sc1 lds\n"
    "buffer_load_dwordx4 v200, s[36:39], s78 offen offset:3072 sc1 lds\n"
    "s_waitcnt vmcnt(0)\n"
    "ds_read_b128 v[160:163], v205\n"
    "ds_read_b128 v[164:167], v205 offset:1024\n"
    "s_waitcnt lgkmcnt(1)\n"
    "v_mfma_f32_16x16x32_bf16 v[64:67], v[160:163], a[192:195], 0\n"
    "s_waitcnt lgkmcnt(0)\n"
    "v_mfma_f32_16x16x32_bf16 v[64:67], v[164:167], a[196:199], v[64:67]\n"
    "v_mfma_f32_16x16x32_bf16 v[68:71], v[164:167], a[220:223], 0\n"
    "v_mfma_f32_16x16x32_bf16 v[68:71], v[160:163], a[216:219], v[68:71]\n"
    "v_mfma_f32_16x16x32_bf16 v[72:75], v[160:163], a[240:243], 0\n"
    "v_mfma_f32_16x16x32_bf16 v[72:75], v[164:167], a[244:247], v[72:75]\n"
    "v_mfma_f32_16x16x32_bf16 v[76:79], v[164:167], v[12:15], 0\n"
    "v_mfma_f32_16x16x32_bf16 v[76:79], v[160:163], v[8:11], v[76:79]\n"
    "ds_read_b128 v[168:171], v205 offset:2048\n"
    "ds_read_b128 v[172:175], v205 offset:3072\n"
    "s_waitcnt lgkmcnt(1)\n"
    "v_mfma_f32_16x16x32_bf16 v[76:79], v[168:171], v[16:19], v[76:79]\n"
    "s_waitcnt lgkmcnt(0)\n"
    "v_mfma_f32_16x16x32_bf16 v[76:79], v[172:175], v[20:23], v[76:79]\n"
    "v_mfma_f32_16x16x32_bf16 v[72:75], v[172:175], a[252:255], v[72:75]\n"
    "v_mfma_f32_16x16x32_bf16 v[72:75], v[168:171], a[248:251], v[72:75]\n"
    "v_mfma_f32_16x16x32_bf16 v[68:71], v[168:171], a[224:227], v[68:71]\n"
    "v_mfma_f32_16x16x32_bf16 v[68:71], v[172:175], a[228:231], v[68:71]\n"
    "v_mfma_f32_16x16x32_bf16 v[64:67], v[172:175], a[204:207], v[64:67]\n"
    "v_mfma_f32_16x16x32_bf16 v[64:67], v[168:171], a[200:203], v[64:67]\n"
    "ds_read_b128 v[176:179], v205 offset:4096\n"
    "ds_read_b128 v[180:183], v205 offset:5120\n"
    "s_waitcnt lgkmcnt(1)\n"
    "v_mfma_f32_16x16x32_bf16 v[64:67], v[176:179], a[208:211], v[64:67]\n"
    "s_waitcnt lgkmcnt(0)\n"
    "v_mfma_f32_16x16x32_bf16 v[64:67], v[180:183], a[212:215], v[64:67]\n"
    "v_mfma_f32_16x16x32_bf16 v[68:71], v[180:183], a[236:239], v[68:71]\n"
    "v_mfma_f32_16x16x32_bf16 v[68:71], v[176:179], a[232:235], v[68:71]\n"
    "v_mfma_f32_16x16x32_bf16 v[72:75], v[176:179], v[0:3], v[72:75]\n"
    "v_mfma_f32_16x16x32_bf16 v[72:75], v[180:183], v[4:7], v[72:75]\n"
    "v_mfma_f32_16x16x32_bf16 v[76:79], v[180:183], v[28:31], v[76:79]\n"
    "v_mfma_f32_16x16x32_bf16 v[76:79], v[176:179], v[24:27], v[76:79]\n"
    "ds_read_b128 v[160:163], v205 offset:10240\n"
    "ds_read_b128 v[164:167], v205 offset:11264\n"
    "s_waitcnt lgkmcnt(1)\n"
    "v_mfma_f32_16x16x32_bf16 v[92:95], v[160:163], v[24:27], 0\n"
    "s_waitcnt lgkmcnt(0)\n"
    "v_mfma_f32_16x16x32_bf16 v[92:95], v[164:167], v[28:31], v[92:95]\n"
    "v_mfma_f32_16x16x32_bf16 v[88:91], v[164:167], v[4:7], 0\n"
    "v_mfma_f32_16x16x32_bf16 v[88:91], v[160:163], v[0:3], v[88:91]\n"
    "v_mfma_f32_16x16x32_bf16 v[84:87], v[160:163], a[232:235], 0\n"
    "v_mfma_f32_16x16x32_bf16 v[84:87], v[164:167], a[236:239], v[84:87]\n"
    "v_mfma_f32_16x16x32_bf16 v[80:83], v[164:167], a[212:215], 0\n"
    "v_mfma_f32_16x16x32_bf16 v[80:83], v[160:163], a[208:211], v[80:83]\n"
    "ds_read_b128 v[168:171], v205 offset:8192\n"
    "ds_read_b128 v[172:175], v205 offset:9216\n"
    "s_waitcnt lgkmcnt(1)\n"
    "v_mfma_f32_16x16x32_bf16 v[80:83], v[168:171], a[200:203], v[80:83]\n"
    "s_waitcnt lgkmcnt(0)\n"
    "v_mfma_f32_16x16x32_bf16 v[80:83], v[172:175], a[204:207], v[80:83]\n"
    "v_mfma_f32_16x16x32_bf16 v[84:87], v[172:175], a[228:231], v[84:87]\n"
    "v_mfma_f32_16x16x32_bf16 v[84:87], v[168:171], a[224:227], v[84:87]\n"
    "v_mfma_f32_16x16x32_bf16 v[88:91], v[168:171], a[248:251], v[88:91]\n"
    "v_mfma_f32_16x16x32_bf16 v[88:91], v[172:175], a[252:255], v[88:91]\n"
    "v_mfma_f32_16x16x32_bf16 v[92:95], v[172:175], v[20:23], v[92:95]\n"
    "v_mfma_f32_16x16x32_bf16 v[92:95], v[168:171], v[16:19], v[92:95]\n"
    "ds_read_b128 v[176:179], v205 offset:6144\n"
    "ds_read_b128 v[180:183], v205 offset:7168\n"
    "s_waitcnt lgkmcnt(1)\n"
    "v_mfma_f32_16x16x32_bf16 v[92:95], v[176:179], v[8:11], v[92:95]\n"
    "s_waitcnt lgkmcnt(0)\n"
    "v_mfma_f32_16x16x32_bf16 v[92:95], v[180:183], v[12:15], v[92:95]\n"
    "v_mfma_f32_16x16x32_bf16 v[88:91], v[180:183], a[244:247], v[88:91]\n"
    "v_mfma_f32_16x16x32_bf16 v[88:91], v[176:179], a[240:243], v[88:91]\n"
    "v_mfma_f32_16x16x32_bf16 v[84:87], v[176:179], a[216:219], v[84:87]\n"
    "v_mfma_f32_16x16x32_bf16 v[84:87], v[180:183], a[220:223], v[84:87]\n"
    "v_mfma_f32_16x16x32_bf16 v[80:83], v[180:183], a[196:199], v[80:83]\n"
    "v_mfma_f32_16x16x32_bf16 v[80:83], v[176:179], a[192:195], v[80:83]\n"
    "v_max3_f32 v196, v32, v33, v34\n"
    "v_max3_f32 v196, v196, v35, v48\n"
    "v_max3_f32 v196, v196, v49, v50\n"
    "v_max3_f32 v196, v196, v51, v64\n"
    "v_max3_f32 v196, v196, v65, v66\n"
    "s_nop 2\n"
    "v_max3_f32 v196, v196, v67, v80\n"
    "v_max3_f32 v196, v196, v81, v82\n"
    "v_max_f32 v196, v196, v83\n"
    "v_mov_b32 v197, v196\n"
    "s_nop 1\n"
    "v_permlane32_swap_b32 v196, v197\n"
    "v_max_f32 v196, v196, v197\n"
    "v_mov_b32 v197, v196\n"
    "s_nop 1\n"
    "v_permlane16_swap_b32 v196, v197\n"
    "v_max_f32 v196, v196, v197\n"
    "v_max_f32 v196, v196, v96\n"
    "v_sub_f32 v197, v96, v196\n"
    "v_mov_b32 v96, v196\n"
    "v_exp_f32 v197, v197\n"
    "v_sub_f32 v32, v32, v196\n"
    "v_sub_f32 v33, v33, v196\n"
    "v_sub_f32 v34, v34, v196\n"
    "v_sub_f32 v35, v35, v196\n"
    "v_sub_f32 v48, v48, v196\n"
    "v_sub_f32 v49, v49, v196\n"
    "v_sub_f32 v50, v50, v196\n"
    "v_sub_f32 v51, v51, v196\n"
    "v_sub_f32 v64, v64, v196\n"
    "v_sub_f32 v65, v65, v196\n"
    "v_sub_f32 v66, v66, v196\n"
    "v_sub_f32 v67, v67, v196\n"
    "v_sub_f32 v80, v80, v196\n"
    "v_sub_f32 v81, v81, v196\n"
    "v_sub_f32 v82, v82, v196\n"
    "v_sub_f32 v83, v83, v196\n"
    "v_mul_f32 v112, v112, v197\n"
    "v_mul_f32 v113, v113, v197\n"
    "v_mul_f32 v114, v114, v197\n"
    "v_mul_f32 v115, v115, v197\n"
    "v_accvgpr_read_b32 v198, a0\n"
    "v_accvgpr_read_b32 v199, a1\n"
    "v_mul_f32 v198, v198, v197\n"
    "v_mul_f32 v199, v199, v197\n"
    "v_accvgpr_write_b32 a0, v198\n"
    "v_accvgpr_write_b32 a1, v199\n"
    "v_accvgpr_read_b32 v198, a2\n"
    "v_accvgpr_read_b32 v199, a3\n"
    "v_mul_f32 v198, v198, v197\n"
    "v_mul_f32 v199, v199, v197\n"
    "v_accvgpr_write_b32 a2, v198\n"
    "v_accvgpr_write_b32 a3, v199\n"
    "v_accvgpr_read_b32 v198, a16\n"
    "v_accvgpr_read_b32 v199, a17\n"
    "v_mul_f32 v198, v198, v197\n"
    "v_mul_f32 v199, v199, v197\n"
    "v_accvgpr_write_b32 a16, v198\n"
    "v_accvgpr_write_b32 a17, v199\n"
    "v_accvgpr_read_b32 v198, a18\n"
    "v_accvgpr_read_b32 v199, a19\n"
    "v_mul_f32 v198, v198, v197\n"
    "v_mul_f32 v199, v199, v197\n"
    "v_accvgpr_write_b32 a18, v198\n"
    "v_accvgpr_write_b32 a19, v199\n"
    "v_accvgpr_read_b32 v198, a32\n"
    "v_accvgpr_read_b32 v199, a33\n"
    "v_mul_f32 v198, v198, v197\n"
    "v_mul_f32 v199, v199, v197\n"
    "v_accvgpr_write_b32 a32, v198\n"
    "v_accvgpr_write_b32 a33, v199\n"
    "v_accvgpr_read_b32 v198, a34\n"
    "v_accvgpr_read_b32 v199, a35\n"
    "v_mul_f32 v198, v198, v197\n"
    "v_mul_f32 v199, v199, v197\n"
    "v_accvgpr_write_b32 a34, v198\n"
    "v_accvgpr_write_b32 a35, v199\n"
    "v_accvgpr_read_b32 v198, a48\n"
    "v_accvgpr_read_b32 v199, a49\n"
    "v_mul_f32 v198, v198, v197\n"
    "v_mul_f32 v199, v199, v197\n"
    "v_accvgpr_write_b32 a48, v198\n"
    "v_accvgpr_write_b32 a49, v199\n"
    "v_accvgpr_read_b32 v198, a50\n"
    "v_accvgpr_read_b32 v199, a51\n"
    "v_mul_f32 v198, v198, v197\n"
    "v_mul_f32 v199, v199, v197\n"
    "v_accvgpr_write_b32 a50, v198\n"
    "v_accvgpr_write_b32 a51, v199\n"
    "v_accvgpr_read_b32 v198, a64\n"
    "v_accvgpr_read_b32 v199, a65\n"
    "v_mul_f32 v198, v198, v197\n"
    "v_mul_f32 v199, v199, v197\n"
    "v_accvgpr_write_b32 a64, v198\n"
    "v_accvgpr_write_b32 a65, v199\n"
    "v_accvgpr_read_b32 v198, a66\n"
    "v_accvgpr_read_b32 v199, a67\n"
    "v_mul_f32 v198, v198, v197\n"
    "v_mul_f32 v199, v199, v197\n"
    "v_accvgpr_write_b32 a66, v198\n"
    "v_accvgpr_write_b32 a67, v199\n"
    "v_accvgpr_read_b32 v198, a80\n"
    "v_accvgpr_read_b32 v199, a81\n"
    "v_mul_f32 v198, v198, v197\n"
    "v_mul_f32 v199, v199, v197\n"
    "v_accvgpr_write_b32 a80, v198\n"
    "v_accvgpr_write_b32 a81, v199\n"
    "v_accvgpr_read_b32 v198, a82\n"
    "v_accvgpr_read_b32 v199, a83\n"
    "v_mul_f32 v198, v198, v197\n"
    "v_mul_f32 v199, v199, v197\n"
    "v_accvgpr_write_b32 a82, v198\n"
    "v_accvgpr_write_b32 a83, v199\n"
    "v_accvgpr_read_b32 v198, a96\n"
    "v_accvgpr_read_b32 v199, a97\n"
    "v_mul_f32 v198, v198, v197\n"
    "v_mul_f32 v199, v199, v197\n"
    "v_accvgpr_write_b32 a96, v198\n"
    "v_accvgpr_write_b32 a97, v199\n"
    "v_accvgpr_read_b32 v198, a98\n"
    "v_accvgpr_read_b32 v199, a99\n"
    "v_mul_f32 v198, v198, v197\n"
    "v_mul_f32 v199, v199, v197\n"
    "v_accvgpr_write_b32 a98, v198\n"
    "v_accvgpr_write_b32 a99, v199\n"
    "v_accvgpr_read_b32 v198, a112\n"
    "v_accvgpr_read_b32 v199, a113\n"
    "v_mul_f32 v198, v198, v197\n"
    "v_mul_f32 v199, v199, v197\n"
    "v_accvgpr_write_b32 a112, v198\n"
    "v_accvgpr_write_b32 a113, v199\n"
    "v_accvgpr_read_b32 v198, a114\n"
    "v_accvgpr_read_b32 v199, a115\n"
    "v_mul_f32 v198, v198, v197\n"
    "v_mul_f32 v199, v199, v197\n"
    "v_accvgpr_write_b32 a114, v198\n"
    "v_accvgpr_write_b32 a115, v199\n"
    "v_accvgpr_read_b32 v198, a128\n"
    "v_accvgpr_read_b32 v199, a129\n"
    "v_mul_f32 v198, v198, v197\n"
    "v_mul_f32 v199, v199, v197\n"
    "v_accvgpr_write_b32 a128, v198\n"
    "v_accvgpr_write_b32 a129, v199\n"
    "v_accvgpr_read_b32 v198, a130\n"
    "v_accvgpr_read_b32 v199, a131\n"
    "v_mul_f32 v198, v198, v197\n"
    "v_mul_f32 v199, v199, v197\n"
    "v_accvgpr_write_b32 a130, v198\n"
    "v_accvgpr_write_b32 a131, v199\n"
    "v_accvgpr_read_b32 v198, a144\n"
    "v_accvgpr_read_b32 v199, a145\n"
    "v_mul_f32 v198, v198, v197\n"
    "v_mul_f32 v199, v199, v197\n"
    "v_accvgpr_write_b32 a144, v198\n"
    "v_accvgpr_write_b32 a145, v199\n"
    "v_accvgpr_read_b32 v198, a146\n"
    "v_accvgpr_read_b32 v199, a147\n"
    "v_mul_f32 v198, v198, v197\n"
    "v_mul_f32 v199, v199, v197\n"
    "v_accvgpr_write_b32 a146, v198\n"
    "v_accvgpr_write_b32 a147, v199\n"
    "v_accvgpr_read_b32 v198, a160\n"
    "v_accvgpr_read_b32 v199, a161\n"
    "v_mul_f32 v198, v198, v197\n"
    "v_mul_f32 v199, v199, v197\n"
    "v_accvgpr_write_b32 a160, v198\n"
    "v_accvgpr_write_b32 a161, v199\n"
    "v_accvgpr_read_b32 v198, a162\n"
    "v_accvgpr_read_b32 v199, a163\n"
    "v_mul_f32 v198, v198, v197\n"
    "v_mul_f32 v199, v199, v197\n"
    "v_accvgpr_write_b32 a162, v198\n"
    "v_accvgpr_write_b32 a163, v199\n"
    "v_accvgpr_read_b32 v198, a176\n"
    "v_accvgpr_read_b32 v199, a177\n"
    "v_mul_f32 v198, v198, v197\n"
    "v_mul_f32 v199, v199, v197\n"
    "v_accvgpr_write_b32 a176, v198\n"
    "v_accvgpr_write_b32 a177, v199\n"
    "v_accvgpr_read_b32 v198, a178\n"
    "v_accvgpr_read_b32 v199, a179\n"
    "v_mul_f32 v198, v198, v197\n"
    "v_mul_f32 v199, v199, v197\n"
    "v_accvgpr_write_b32 a178, v198\n"
    "v_accvgpr_write_b32 a179, v199\n"
    "v_max3_f32 v196, v36, v37, v38\n"
    "v_max3_f32 v196, v196, v39, v52\n"
    "v_max3_f32 v196, v196, v53, v54\n"
    "v_max3_f32 v196, v196, v55, v68\n"
    "v_max3_f32 v196, v196, v69, v70\n"
    "v_max3_f32 v196, v196, v71, v84\n"
    "v_max3_f32 v196, v196, v85, v86\n"
    "v_max_f32 v196, v196, v87\n"
    "v_mov_b32 v197, v196\n"
    "s_nop 1\n"
    "v_permlane32_swap_b32 v196, v197\n"
    "v_max_f32 v196, v196, v197\n"
    "v_mov_b32 v197, v196\n"
    "s_nop 1\n"
    "v_permlane16_swap_b32 v196, v197\n"
    "v_max_f32 v196, v196, v197\n"
    "v_max_f32 v196, v196, v100\n"
    "v_sub_f32 v197, v100, v196\n"
    "v_mov_b32 v100, v196\n"
    "v_exp_f32 v197, v197\n"
    "v_sub_f32 v36, v36, v196\n"
    "v_sub_f32 v37, v37, v196\n"
    "v_sub_f32 v38, v38, v196\n"
    "v_sub_f32 v39, v39, v196\n"
    "v_sub_f32 v52, v52, v196\n"
    "v_sub_f32 v53, v53, v196\n"
    "v_sub_f32 v54, v54, v196\n"
    "v_sub_f32 v55, v55, v196\n"
    "v_sub_f32 v68, v68, v196\n"
    "v_sub_f32 v69, v69, v196\n"
    "v_sub_f32 v70, v70, v196\n"
    "v_sub_f32 v71, v71, v196\n"
    "v_sub_f32 v84, v84, v196\n"
    "v_sub_f32 v85, v85, v196\n"
    "v_sub_f32 v86, v86, v196\n"
    "v_sub_f32 v87, v87, v196\n"
    "v_mul_f32 v116, v116, v197\n"
    "v_mul_f32 v117, v117, v197\n"
    "v_mul_f32 v118, v118, v197\n"
    "v_mul_f32 v119, v119, v197\n"
    "v_accvgpr_read_b32 v198, a4\n"
    "v_accvgpr_read_b32 v199, a5\n"
    "v_mul_f32 v198, v198, v197\n"
    "v_mul_f32 v199, v199, v197\n"
    "v_accvgpr_write_b32 a4, v198\n"
    "v_accvgpr_write_b32 a5, v199\n"
    "v_accvgpr_read_b32 v198, a6\n"
    "v_accvgpr_read_b32 v199, a7\n"
    "v_mul_f32 v198, v198, v197\n"
    "v_mul_f32 v199, v199, v197\n"
    "v_accvgpr_write_b32 a6, v198\n"
    "v_accvgpr_write_b32 a7, v199\n"
    "v_accvgpr_read_b32 v198, a20\n"
    "v_accvgpr_read_b32 v199, a21\n"
    "v_mul_f32 v198, v198, v197\n"
    "v_mul_f32 v199, v199, v197\n"
    "v_accvgpr_write_b32 a20, v198\n"
    "v_accvgpr_write_b32 a21, v199\n"
    "v_accvgpr_read_b32 v198, a22\n"
    "v_accvgpr_read_b32 v199, a23\n"
    "v_mul_f32 v198, v198, v197\n"
    "v_mul_f32 v199, v199, v197\n"
    "v_accvgpr_write_b32 a22, v198\n"
    "v_accvgpr_write_b32 a23, v199\n"
    "v_accvgpr_read_b32 v198, a36\n"
    "v_accvgpr_read_b32 v199, a37\n"
    "v_mul_f32 v198, v198, v197\n"
    "v_mul_f32 v199, v199, v197\n"
    "v_accvgpr_write_b32 a36, v198\n"
    "v_accvgpr_write_b32 a37, v199\n"
    "v_accvgpr_read_b32 v198, a38\n"
    "v_accvgpr_read_b32 v199, a39\n"
    "v_mul_f32 v198, v198, v197\n"
    "v_mul_f32 v199, v199, v197\n"
    "v_accvgpr_write_b32 a38, v198\n"
    "v_accvgpr_write_b32 a39, v199\n"
    "v_accvgpr_read_b32 v198, a52\n"
    "v_accvgpr_read_b32 v199, a53\n"
    "v_mul_f32 v198, v198, v197\n"
    "v_mul_f32 v199, v199, v197\n"
    "v_accvgpr_write_b32 a52, v198\n"
    "v_accvgpr_write_b32 a53, v199\n"
    "v_accvgpr_read_b32 v198, a54\n"
    "v_accvgpr_read_b32 v199, a55\n"
    "v_mul_f32 v198, v198, v197\n"
    "v_mul_f32 v199, v199, v197\n"
    "v_accvgpr_write_b32 a54, v198\n"
    "v_accvgpr_write_b32 a55, v199\n"
    "v_accvgpr_read_b32 v198, a68\n"
    "v_accvgpr_read_b32 v199, a69\n"
    "v_mul_f32 v198, v198, v197\n"
    "v_mul_f32 v199, v199, v197\n"
    "v_accvgpr_write_b32 a68, v198\n"
    "v_accvgpr_write_b32 a69, v199\n"
    "v_accvgpr_read_b32 v198, a70\n"
    "v_accvgpr_read_b32 v199, a71\n"
    "v_mul_f32 v198, v198, v197\n"
    "v_mul_f32 v199, v199, v197\n"
    "v_accvgpr_write_b32 a70, v198\n"
    "v_accvgpr_write_b32 a71, v199\n"
    "v_accvgpr_read_b32 v198, a84\n"
    "v_accvgpr_read_b32 v199, a85\n"
    "v_mul_f32 v198, v198, v197\n"
    "v_mul_f32 v199, v199, v197\n"
    "v_accvgpr_write_b32 a84, v198\n"
    "v_accvgpr_write_b32 a85, v199\n"
    "v_accvgpr_read_b32 v198, a86\n"
    "v_accvgpr_read_b32 v199, a87\n"
    "v_mul_f32 v198, v198, v197\n"
    "v_mul_f32 v199, v199, v197\n"
    "v_accvgpr_write_b32 a86, v198\n"
    "v_accvgpr_write_b32 a87, v199\n"
    "v_accvgpr_read_b32 v198, a100\n"
    "v_accvgpr_read_b32 v199, a101\n"
    "v_mul_f32 v198, v198, v197\n"
    "v_mul_f32 v199, v199, v197\n"
    "v_accvgpr_write_b32 a100, v198\n"
    "v_accvgpr_write_b32 a101, v199\n"
    "v_accvgpr_read_b32 v198, a102\n"
    "v_accvgpr_read_b32 v199, a103\n"
    "v_mul_f32 v198, v198, v197\n"
    "v_mul_f32 v199, v199, v197\n"
    "v_accvgpr_write_b32 a102, v198\n"
    "v_accvgpr_write_b32 a103, v199\n"
    "v_accvgpr_read_b32 v198, a116\n"
    "v_accvgpr_read_b32 v199, a117\n"
    "v_mul_f32 v198, v198, v197\n"
    "v_mul_f32 v199, v199, v197\n"
    "v_accvgpr_write_b32 a116, v198\n"
    "v_accvgpr_write_b32 a117, v199\n"
    "v_accvgpr_read_b32 v198, a118\n"
    "v_accvgpr_read_b32 v199, a119\n"
    "v_mul_f32 v198, v198, v197\n"
    "v_mul_f32 v199, v199, v197\n"
    "v_accvgpr_write_b32 a118, v198\n"
    "v_accvgpr_write_b32 a119, v199\n"
    "v_accvgpr_read_b32 v198, a132\n"
    "v_accvgpr_read_b32 v199, a133\n"
    "v_mul_f32 v198, v198, v197\n"
    "v_mul_f32 v199, v199, v197\n"
    "v_accvgpr_write_b32 a132, v198\n"
    "v_accvgpr_write_b32 a133, v199\n"
    "v_accvgpr_read_b32 v198, a134\n"
    "v_accvgpr_read_b32 v199, a135\n"
    "v_mul_f32 v198, v198, v197\n"
    "v_mul_f32 v199, v199, v197\n"
    "v_accvgpr_write_b32 a134, v198\n"
    "v_accvgpr_write_b32 a135, v199\n"
    "v_accvgpr_read_b32 v198, a148\n"
    "v_accvgpr_read_b32 v199, a149\n"
    "v_mul_f32 v198, v198, v197\n"
    "v_mul_f32 v199, v199, v197\n"
    "v_accvgpr_write_b32 a148, v198\n"
    "v_accvgpr_write_b32 a149, v199\n"
    "v_accvgpr_read_b32 v198, a150\n"
    "v_accvgpr_read_b32 v199, a151\n"
    "v_mul_f32 v198, v198, v197\n"
    "v_mul_f32 v199, v199, v197\n"
    "v_accvgpr_write_b32 a150, v198\n"
    "v_accvgpr_write_b32 a151, v199\n"
    "v_accvgpr_read_b32 v198, a164\n"
    "v_accvgpr_read_b32 v199, a165\n"
    "v_mul_f32 v198, v198, v197\n"
    "v_mul_f32 v199, v199, v197\n"
    "v_accvgpr_write_b32 a164, v198\n"
    "v_accvgpr_write_b32 a165, v199\n"
    "v_accvgpr_read_b32 v198, a166\n"
    "v_accvgpr_read_b32 v199, a167\n"
    "v_mul_f32 v198, v198, v197\n"
    "v_mul_f32 v199, v199, v197\n"
    "v_accvgpr_write_b32 a166, v198\n"
    "v_accvgpr_write_b32 a167, v199\n"
    "v_accvgpr_read_b32 v198, a180\n"
    "v_accvgpr_read_b32 v199, a181\n"
    "v_mul_f32 v198, v198, v197\n"
    "v_mul_f32 v199, v199, v197\n"
    "v_accvgpr_write_b32 a180, v198\n"
    "v_accvgpr_write_b32 a181, v199\n"
    "v_accvgpr_read_b32 v198, a182\n"
    "v_accvgpr_read_b32 v199, a183\n"
    "v_mul_f32 v198, v198, v197\n"
    "v_mul_f32 v199, v199, v197\n"
    "v_accvgpr_write_b32 a182, v198\n"
    "v_accvgpr_write_b32 a183, v199\n"
    "v_max3_f32 v196, v40, v41, v42\n"
    "v_max3_f32 v196, v196, v43, v56\n"
    "v_max3_f32 v196, v196, v57, v58\n"
    "v_max3_f32 v196, v196, v59, v72\n"
    "v_max3_f32 v196, v196, v73, v74\n"
    "v_max3_f32 v196, v196, v75, v88\n"
    "v_max3_f32 v196, v196, v89, v90\n"
    "v_max_f32 v196, v196, v91\n"
    "v_mov_b32 v197, v196\n"
    "s_nop 1\n"
    "v_permlane32_swap_b32 v196, v197\n"
    "v_max_f32 v196, v196, v197\n"
    "v_mov_b32 v197, v196\n"
    "s_nop 1\n"
    "v_permlane16_swap_b32 v196, v197\n"
    "v_max_f32 v196, v196, v197\n"
    "v_max_f32 v196, v196, v104\n"
    "v_sub_f32 v197, v104, v196\n"
    "v_mov_b32 v104, v196\n"
    "v_exp_f32 v197, v197\n"
    "v_sub_f32 v40, v40, v196\n"
    "v_sub_f32 v41, v41, v196\n"
    "v_sub_f32 v42, v42, v196\n"
    "v_sub_f32 v43, v43, v196\n"
    "v_sub_f32 v56, v56, v196\n"
    "v_sub_f32 v57, v57, v196\n"
    "v_sub_f32 v58, v58, v196\n"
    "v_sub_f32 v59, v59, v196\n"
    "v_sub_f32 v72, v72, v196\n"
    "v_sub_f32 v73, v73, v196\n"
    "v_sub_f32 v74, v74, v196\n"
    "v_sub_f32 v75, v75, v196\n"
    "v_sub_f32 v88, v88, v196\n"
    "v_sub_f32 v89, v89, v196\n"
    "v_sub_f32 v90, v90, v196\n"
    "v_sub_f32 v91, v91, v196\n"
    "v_mul_f32 v120, v120, v197\n"
    "v_mul_f32 v121, v121, v197\n"
    "v_mul_f32 v122, v122, v197\n"
    "v_mul_f32 v123, v123, v197\n"
    "v_accvgpr_read_b32 v198, a8\n"
    "v_accvgpr_read_b32 v199, a9\n"
    "v_mul_f32 v198, v198, v197\n"
    "v_mul_f32 v199, v199, v197\n"
    "v_accvgpr_write_b32 a8, v198\n"
    "v_accvgpr_write_b32 a9, v199\n"
    "v_accvgpr_read_b32 v198, a10\n"
    "v_accvgpr_read_b32 v199, a11\n"
    "v_mul_f32 v198, v198, v197\n"
    "v_mul_f32 v199, v199, v197\n"
    "v_accvgpr_write_b32 a10, v198\n"
    "v_accvgpr_write_b32 a11, v199\n"
    "v_accvgpr_read_b32 v198, a24\n"
    "v_accvgpr_read_b32 v199, a25\n"
    "v_mul_f32 v198, v198, v197\n"
    "v_mul_f32 v199, v199, v197\n"
    "v_accvgpr_write_b32 a24, v198\n"
    "v_accvgpr_write_b32 a25, v199\n"
    "v_accvgpr_read_b32 v198, a26\n"
    "v_accvgpr_read_b32 v199, a27\n"
    "v_mul_f32 v198, v198, v197\n"
    "v_mul_f32 v199, v199, v197\n"
    "v_accvgpr_write_b32 a26, v198\n"
    "v_accvgpr_write_b32 a27, v199\n"
    "v_accvgpr_read_b32 v198, a40\n"
    "v_accvgpr_read_b32 v199, a41\n"
    "v_mul_f32 v198, v198, v197\n"
    "v_mul_f32 v199, v199, v197\n"
    "v_accvgpr_write_b32 a40, v198\n"
    "v_accvgpr_write_b32 a41, v199\n"
    "v_accvgpr_read_b32 v198, a42\n"
    "v_accvgpr_read_b32 v199, a43\n"
    "v_mul_f32 v198, v198, v197\n"
    "v_mul_f32 v199, v199, v197\n"
    "v_accvgpr_write_b32 a42, v198\n"
    "v_accvgpr_write_b32 a43, v199\n"
    "v_accvgpr_read_b32 v198, a56\n"
    "v_accvgpr_read_b32 v199, a57\n"
    "v_mul_f32 v198, v198, v197\n"
    "v_mul_f32 v199, v199, v197\n"
    "v_accvgpr_write_b32 a56, v198\n"
    "v_accvgpr_write_b32 a57, v199\n"
    "v_accvgpr_read_b32 v198, a58\n"
    "v_accvgpr_read_b32 v199, a59\n"
    "v_mul_f32 v198, v198, v197\n"
    "v_mul_f32 v199, v199, v197\n"
    "v_accvgpr_write_b32 a58, v198\n"
    "v_accvgpr_write_b32 a59, v199\n"
    "v_accvgpr_read_b32 v198, a72\n"
    "v_accvgpr_read_b32 v199, a73\n"
    "v_mul_f32 v198, v198, v197\n"
    "v_mul_f32 v199, v199, v197\n"
    "v_accvgpr_write_b32 a72, v198\n"
    "v_accvgpr_write_b32 a73, v199\n"
    "v_accvgpr_read_b32 v198, a74\n"
    "v_accvgpr_read_b32 v199, a75\n"
    "v_mul_f32 v198, v198, v197\n"
    "v_mul_f32 v199, v199, v197\n"
    "v_accvgpr_write_b32 a74, v198\n"
    "v_accvgpr_write_b32 a75, v199\n"
    "v_accvgpr_read_b32 v198, a88\n"
    "v_accvgpr_read_b32 v199, a89\n"
    "v_mul_f32 v198, v198, v197\n"
    "v_mul_f32 v199, v199, v197\n"
    "v_accvgpr_write_b32 a88, v198\n"
    "v_accvgpr_write_b32 a89, v199\n"
    "v_accvgpr_read_b32 v198, a90\n"
    "v_accvgpr_read_b32 v199, a91\n"
    "v_mul_f32 v198, v198, v197\n"
    "v_mul_f32 v199, v199, v197\n"
    "v_accvgpr_write_b32 a90, v198\n"
    "v_accvgpr_write_b32 a91, v199\n"
    "v_accvgpr_read_b32 v198, a104\n"
    "v_accvgpr_read_b32 v199, a105\n"
    "v_mul_f32 v198, v198, v197\n"
    "v_mul_f32 v199, v199, v197\n"
    "v_accvgpr_write_b32 a104, v198\n"
    "v_accvgpr_write_b32 a105, v199\n"
    "v_accvgpr_read_b32 v198, a106\n"
    "v_accvgpr_read_b32 v199, a107\n"
    "v_mul_f32 v198, v198, v197\n"
    "v_mul_f32 v199, v199, v197\n"
    "v_accvgpr_write_b32 a106, v198\n"
    "v_accvgpr_write_b32 a107, v199\n"
    "v_accvgpr_read_b32 v198, a120\n"
    "v_accvgpr_read_b32 v199, a121\n"
    "v_mul_f32 v198, v198, v197\n"
    "v_mul_f32 v199, v199, v197\n"
    "v_accvgpr_write_b32 a120, v198\n"
    "v_accvgpr_write_b32 a121, v199\n"
    "v_accvgpr_read_b32 v198, a122\n"
    "v_accvgpr_read_b32 v199, a123\n"
    "v_mul_f32 v198, v198, v197\n"
    "v_mul_f32 v199, v199, v197\n"
    "v_accvgpr_write_b32 a122, v198\n"
    "v_accvgpr_write_b32 a123, v199\n"
    "v_accvgpr_read_b32 v198, a136\n"
    "v_accvgpr_read_b32 v199, a137\n"
    "v_mul_f32 v198, v198, v197\n"
    "v_mul_f32 v199, v199, v197\n"
    "v_accvgpr_write_b32 a136, v198\n"
    "v_accvgpr_write_b32 a137, v199\n"
    "v_accvgpr_read_b32 v198, a138\n"
    "v_accvgpr_read_b32 v199, a139\n"
    "v_mul_f32 v198, v198, v197\n"
    "v_mul_f32 v199, v199, v197\n"
    "v_accvgpr_write_b32 a138, v198\n"
    "v_accvgpr_write_b32 a139, v199\n"
    "v_accvgpr_read_b32 v198, a152\n"
    "v_accvgpr_read_b32 v199, a153\n"
    "v_mul_f32 v198, v198, v197\n"
    "v_mul_f32 v199, v199, v197\n"
    "v_accvgpr_write_b32 a152, v198\n"
    "v_accvgpr_write_b32 a153, v199\n"
    "v_accvgpr_read_b32 v198, a154\n"
    "v_accvgpr_read_b32 v199, a155\n"
    "v_mul_f32 v198, v198, v197\n"
    "v_mul_f32 v199, v199, v197\n"
    "v_accvgpr_write_b32 a154, v198\n"
    "v_accvgpr_write_b32 a155, v199\n"
    "v_accvgpr_read_b32 v198, a168\n"
    "v_accvgpr_read_b32 v199, a169\n"
    "v_mul_f32 v198, v198, v197\n"
    "v_mul_f32 v199, v199, v197\n"
    "v_accvgpr_write_b32 a168, v198\n"
    "v_accvgpr_write_b32 a169, v199\n"
    "v_accvgpr_read_b32 v198, a170\n"
    "v_accvgpr_read_b32 v199, a171\n"
    "v_mul_f32 v198, v198, v197\n"
    "v_mul_f32 v199, v199, v197\n"
    "v_accvgpr_write_b32 a170, v198\n"
    "v_accvgpr_write_b32 a171, v199\n"
    "v_accvgpr_read_b32 v198, a184\n"
    "v_accvgpr_read_b32 v199, a185\n"
    "v_mul_f32 v198, v198, v197\n"
    "v_mul_f32 v199, v199, v197\n"
    "v_accvgpr_write_b32 a184, v198\n"
    "v_accvgpr_write_b32 a185, v199\n"
    "v_accvgpr_read_b32 v198, a186\n"
    "v_accvgpr_read_b32 v199, a187\n"
    "v_mul_f32 v198, v198, v197\n"
    "v_mul_f32 v199, v199, v197\n"
    "v_accvgpr_write_b32 a186, v198\n"
    "v_accvgpr_write_b32 a187, v199\n"
    "v_max3_f32 v196, v44, v45, v46\n"
    "v_max3_f32 v196, v196, v47, v60\n"
    "v_max3_f32 v196, v196, v61, v62\n"
    "v_max3_f32 v196, v196, v63, v76\n"
    "v_max3_f32 v196, v196, v77, v78\n"
    "v_max3_f32 v196, v196, v79, v92\n"
    "v_max3_f32 v196, v196, v93, v94\n"
    "v_max_f32 v196, v196, v95\n"
    "v_mov_b32 v197, v196\n"
    "s_nop 1\n"
    "v_permlane32_swap_b32 v196, v197\n"
    "v_max_f32 v196, v196, v197\n"
    "v_mov_b32 v197, v196\n"
    "s_nop 1\n"
    "v_permlane16_swap_b32 v196, v197\n"
    "v_max_f32 v196, v196, v197\n"
    "v_max_f32 v196, v196, v108\n"
    "v_sub_f32 v197, v108, v196\n"
    "v_mov_b32 v108, v196\n"
    "v_exp_f32 v197, v197\n"
    "v_sub_f32 v44, v44, v196\n"
    "v_sub_f32 v45, v45, v196\n"
    "v_sub_f32 v46, v46, v196\n"
    "v_sub_f32 v47, v47, v196\n"
    "v_sub_f32 v60, v60, v196\n"
    "v_sub_f32 v61, v61, v196\n"
    "v_sub_f32 v62, v62, v196\n"
    "v_sub_f32 v63, v63, v196\n"
    "v_sub_f32 v76, v76, v196\n"
    "v_sub_f32 v77, v77, v196\n"
    "v_sub_f32 v78, v78, v196\n"
    "v_sub_f32 v79, v79, v196\n"
    "v_sub_f32 v92, v92, v196\n"
    "v_sub_f32 v93, v93, v196\n"
    "v_sub_f32 v94, v94, v196\n"
    "v_sub_f32 v95, v95, v196\n"
    "v_mul_f32 v124, v124, v197\n"
    "v_mul_f32 v125, v125, v197\n"
    "v_mul_f32 v126, v126, v197\n"
    "v_mul_f32 v127, v127, v197\n"
    "v_accvgpr_read_b32 v198, a12\n"
    "v_accvgpr_read_b32 v199, a13\n"
    "v_mul_f32 v198, v198, v197\n"
    "v_mul_f32 v199, v199, v197\n"
    "v_accvgpr_write_b32 a12, v198\n"
    "v_accvgpr_write_b32 a13, v199\n"
    "v_accvgpr_read_b32 v198, a14\n"
    "v_accvgpr_read_b32 v199, a15\n"
    "v_mul_f32 v198, v198, v197\n"
    "v_mul_f32 v199, v199, v197\n"
    "v_accvgpr_write_b32 a14, v198\n"
    "v_accvgpr_write_b32 a15, v199\n"
    "v_accvgpr_read_b32 v198, a28\n"
    "v_accvgpr_read_b32 v199, a29\n"
    "v_mul_f32 v198, v198, v197\n"
    "v_mul_f32 v199, v199, v197\n"
    "v_accvgpr_write_b32 a28, v198\n"
    "v_accvgpr_write_b32 a29, v199\n"
    "v_accvgpr_read_b32 v198, a30\n"
    "v_accvgpr_read_b32 v199, a31\n"
    "v_mul_f32 v198, v198, v197\n"
    "v_mul_f32 v199, v199, v197\n"
    "v_accvgpr_write_b32 a30, v198\n"
    "v_accvgpr_write_b32 a31, v199\n"
    "v_accvgpr_read_b32 v198, a44\n"
    "v_accvgpr_read_b32 v199, a45\n"
    "v_mul_f32 v198, v198, v197\n"
    "v_mul_f32 v199, v199, v197\n"
    "v_accvgpr_write_b32 a44, v198\n"
    "v_accvgpr_write_b32 a45, v199\n"
    "v_accvgpr_read_b32 v198, a46\n"
    "v_accvgpr_read_b32 v199, a47\n"
    "v_mul_f32 v198, v198, v197\n"
    "v_mul_f32 v199, v199, v197\n"
    "v_accvgpr_write_b32 a46, v198\n"
    "v_accvgpr_write_b32 a47, v199\n"
    "v_accvgpr_read_b32 v198, a60\n"
    "v_accvgpr_read_b32 v199, a61\n"
    "v_mul_f32 v198, v198, v197\n"
    "v_mul_f32 v199, v199, v197\n"
    "v_accvgpr_write_b32 a60, v198\n"
    "v_accvgpr_write_b32 a61, v199\n"
    "v_accvgpr_read_b32 v198, a62\n"
    "v_accvgpr_read_b32 v199, a63\n"
    "v_mul_f32 v198, v198, v197\n"
    "v_mul_f32 v199, v199, v197\n"
    "v_accvgpr_write_b32 a62, v198\n"
    "v_accvgpr_write_b32 a63, v199\n"
    "v_accvgpr_read_b32 v198, a76\n"
    "v_accvgpr_read_b32 v199, a77\n"
    "v_mul_f32 v198, v198, v197\n"
    "v_mul_f32 v199, v199, v197\n"
    "v_accvgpr_write_b32 a76, v198\n"
    "v_accvgpr_write_b32 a77, v199\n"
    "v_accvgpr_read_b32 v198, a78\n"
    "v_accvgpr_read_b32 v199, a79\n"
    "v_mul_f32 v198, v198, v197\n"
    "v_mul_f32 v199, v199, v197\n"
    "v_accvgpr_write_b32 a78, v198\n"
    "v_accvgpr_write_b32 a79, v199\n"
    "v_accvgpr_read_b32 v198, a92\n"
    "v_accvgpr_read_b32 v199, a93\n"
    "v_mul_f32 v198, v198, v197\n"
    "v_mul_f32 v199, v199, v197\n"
    "v_accvgpr_write_b32 a92, v198\n"
    "v_accvgpr_write_b32 a93, v199\n"
    "v_accvgpr_read_b32 v198, a94\n"
    "v_accvgpr_read_b32 v199, a95\n"
    "v_mul_f32 v198, v198, v197\n"
    "v_mul_f32 v199, v199, v197\n"
    "v_accvgpr_write_b32 a94, v198\n"
    "v_accvgpr_write_b32 a95, v199\n"
    "v_accvgpr_read_b32 v198, a108\n"
    "v_accvgpr_read_b32 v199, a109\n"
    "v_mul_f32 v198, v198, v197\n"
    "v_mul_f32 v199, v199, v197\n"
    "v_accvgpr_write_b32 a108, v198\n"
    "v_accvgpr_write_b32 a109, v199\n"
    "v_accvgpr_read_b32 v198, a110\n"
    "v_accvgpr_read_b32 v199, a111\n"
    "v_mul_f32 v198, v198, v197\n"
    "v_mul_f32 v199, v199, v197\n"
    "v_accvgpr_write_b32 a110, v198\n"
    "v_accvgpr_write_b32 a111, v199\n"
    "v_accvgpr_read_b32 v198, a124\n"
    "v_accvgpr_read_b32 v199, a125\n"
    "v_mul_f32 v198, v198, v197\n"
    "v_mul_f32 v199, v199, v197\n"
    "v_accvgpr_write_b32 a124, v198\n"
    "v_accvgpr_write_b32 a125, v199\n"
    "v_accvgpr_read_b32 v198, a126\n"
    "v_accvgpr_read_b32 v199, a127\n"
    "v_mul_f32 v198, v198, v197\n"
    "v_mul_f32 v199, v199, v197\n"
    "v_accvgpr_write_b32 a126, v198\n"
    "v_accvgpr_write_b32 a127, v199\n"
    "v_accvgpr_read_b32 v198, a140\n"
    "v_accvgpr_read_b32 v199, a141\n"
    "v_mul_f32 v198, v198, v197\n"
    "v_mul_f32 v199, v199, v197\n"
    "v_accvgpr_write_b32 a140, v198\n"
    "v_accvgpr_write_b32 a141, v199\n"
    "v_accvgpr_read_b32 v198, a142\n"
    "v_accvgpr_read_b32 v199, a143\n"
    "v_mul_f32 v198, v198, v197\n"
    "v_mul_f32 v199, v199, v197\n"
    "v_accvgpr_write_b32 a142, v198\n"
    "v_accvgpr_write_b32 a143, v199\n"
    "v_accvgpr_read_b32 v198, a156\n"
    "v_accvgpr_read_b32 v199, a157\n"
    "v_mul_f32 v198, v198, v197\n"
    "v_mul_f32 v199, v199, v197\n"
    "v_accvgpr_write_b32 a156, v198\n"
    "v_accvgpr_write_b32 a157, v199\n"
    "v_accvgpr_read_b32 v198, a158\n"
    "v_accvgpr_read_b32 v199, a159\n"
    "v_mul_f32 v198, v198, v197\n"
    "v_mul_f32 v199, v199, v197\n"
    "v_accvgpr_write_b32 a158, v198\n"
    "v_accvgpr_write_b32 a159, v199\n"
    "v_accvgpr_read_b32 v198, a172\n"
    "v_accvgpr_read_b32 v199, a173\n"
    "v_mul_f32 v198, v198, v197\n"
    "v_mul_f32 v199, v199, v197\n"
    "v_accvgpr_write_b32 a172, v198\n"
    "v_accvgpr_write_b32 a173, v199\n"
    "v_accvgpr_read_b32 v198, a174\n"
    "v_accvgpr_read_b32 v199, a175\n"
    "v_mul_f32 v198, v198, v197\n"
    "v_mul_f32 v199, v199, v197\n"
    "v_accvgpr_write_b32 a174, v198\n"
    "v_accvgpr_write_b32 a175, v199\n"
    "v_accvgpr_read_b32 v198, a188\n"
    "v_accvgpr_read_b32 v199, a189\n"
    "v_mul_f32 v198, v198, v197\n"
    "v_mul_f32 v199, v199, v197\n"
    "v_accvgpr_write_b32 a188, v198\n"
    "v_accvgpr_write_b32 a189, v199\n"
    "v_accvgpr_read_b32 v198, a190\n"
    "v_accvgpr_read_b32 v199, a191\n"
    "v_mul_f32 v198, v198, v197\n"
    "v_mul_f32 v199, v199, v197\n"
    "v_accvgpr_write_b32 a190, v198\n"
    "v_accvgpr_write_b32 a191, v199\n"
    "v_exp_f32 v32, v32\n"
    "v_exp_f32 v33, v33\n"
    "v_exp_f32 v34, v34\n"
    "v_cvt_pk_bf16_f32 v128, v32, v33\n"
    "v_exp_f32 v35, v35\n"
    "v_exp_f32 v36, v36\n"
    "v_cvt_pk_bf16_f32 v129, v34, v35\n"
    "v_exp_f32 v37, v37\n"
    "v_exp_f32 v38, v38\n"
    "v_cvt_pk_bf16_f32 v132, v36, v37\n"
    "v_exp_f32 v39, v39\n"
    "v_exp_f32 v40, v40\n"
    "v_cvt_pk_bf16_f32 v133, v38, v39\n"
    "v_exp_f32 v41, v41\n"
    "v_exp_f32 v42, v42\n"
    "v_cvt_pk_bf16_f32 v136, v40, v41\n"
    "v_exp_f32 v43, v43\n"
    "v_exp_f32 v44, v44\n"
    "v_cvt_pk_bf16_f32 v137, v42, v43\n"
    "v_exp_f32 v45, v45\n"
    "v_exp_f32 v46, v46\n"
    "v_cvt_pk_bf16_f32 v140, v44, v45\n"
    "v_exp_f32 v47, v47\n"
    "v_exp_f32 v48, v48\n"
    "v_cvt_pk_bf16_f32 v141, v46, v47\n"
    "v_exp_f32 v49, v49\n"
    "v_exp_f32 v50, v50\n"
    "v_cvt_pk_bf16_f32 v130, v48, v49\n"
    "v_exp_f32 v51, v51\n"
    "v_exp_f32 v52, v52\n"
    "v_cvt_pk_bf16_f32 v131, v50, v51\n"
    "v_exp_f32 v53, v53\n"
    "v_exp_f32 v54, v54\n"
    "v_cvt_pk_bf16_f32 v134, v52, v53\n"
    "v_exp_f32 v55, v55\n"
    "v_exp_f32 v56, v56\n"
    "v_cvt_pk_bf16_f32 v135, v54, v55\n"
    "v_exp_f32 v57, v57\n"
    "v_exp_f32 v58, v58\n"
    "v_cvt_pk_bf16_f32 v138, v56, v57\n"
    "v_exp_f32 v59, v59\n"
    "v_exp_f32 v60, v60\n"
    "v_cvt_pk_bf16_f32 v139, v58, v59\n"
    "v_exp_f32 v61, v61\n"
    "v_exp_f32 v62, v62\n"
    "v_cvt_pk_bf16_f32 v142, v60, v61\n"
    "v_exp_f32 v63, v63\n"
    "s_nop 0\n"
    "v_cvt_pk_bf16_f32 v143, v62, v63\n"
    "v_exp_f32 v64, v64\n"
    "v_exp_f32 v65, v65\n"
    "v_exp_f32 v66, v66\n"
    "v_cvt_pk_bf16_f32 v144, v64, v65\n"
    "v_exp_f32 v67, v67\n"
    "v_exp_f32 v68, v68\n"
    "v_cvt_pk_bf16_f32 v145, v66, v67\n"
    "v_exp_f32 v69, v69\n"
    "v_exp_f32 v70, v70\n"
    "v_cvt_pk_bf16_f32 v148, v68, v69\n"
    "v_exp_f32 v71, v71\n"
    "v_exp_f32 v72, v72\n"
    "v_cvt_pk_bf16_f32 v149, v70, v71\n"
    "v_exp_f32 v73, v73\n"
    "v_exp_f32 v74, v74\n"
    "v_cvt_pk_bf16_f32 v152, v72, v73\n"
    "v_exp_f32 v75, v75\n"
    "v_exp_f32 v76, v76\n"
    "v_cvt_pk_bf16_f32 v153, v74, v75\n"
    "v_exp_f32 v77, v77\n"
    "v_exp_f32 v78, v78\n"
    "v_cvt_pk_bf16_f32 v156, v76, v77\n"
    "v_exp_f32 v79, v79\n"
    "v_exp_f32 v80, v80\n"
    "v_cvt_pk_bf16_f32 v157, v78, v79\n"
    "v_exp_f32 v81, v81\n"
    "v_exp_f32 v82, v82\n"
    "v_cvt_pk_bf16_f32 v146, v80, v81\n"
    "v_exp_f32 v83, v83\n"
    "v_exp_f32 v84, v84\n"
    "v_cvt_pk_bf16_f32 v147, v82, v83\n"
    "v_exp_f32 v85, v85\n"
    "v_exp_f32 v86, v86\n"
    "v_cvt_pk_bf16_f32 v150, v84, v85\n"
    "v_exp_f32 v87, v87\n"
    "v_exp_f32 v88, v88\n"
    "v_cvt_pk_bf16_f32 v151, v86, v87\n"
    "v_exp_f32 v89, v89\n"
    "v_exp_f32 v90, v90\n"
    "v_cvt_pk_bf16_f32 v154, v88, v89\n"
    "v_exp_f32 v91, v91\n"
    "v_exp_f32 v92, v92\n"
    "v_cvt_pk_bf16_f32 v155, v90, v91\n"
    "v_exp_f32 v93, v93\n"
    "v_exp_f32 v94, v94\n"
    "v_cvt_pk_bf16_f32 v158, v92, v93\n"
    "v_exp_f32 v95, v95\n"
    "s_nop 0\n"
    "v_cvt_pk_bf16_f32 v159, v94, v95\n"
    "ds_read_b128 v[160:163], v205 offset:12288\n"
    "ds_read_b128 v[164:167], v205 offset:13312\n"
    "s_waitcnt lgkmcnt(1)\n"
    "v_mfma_f32_16x16x32_bf16 a[0:3], v[160:163], v[128:131], a[0:3]\n"
    "s_waitcnt lgkmcnt(0)\n"
    "v_mfma_f32_16x16x32_bf16 a[0:3], v[164:167], v[144:147], a[0:3]\n"
    "v_mfma_f32_16x16x32_bf16 a[4:7], v[160:163], v[132:135], a[4:7]\n"
    "v_mfma_f32_16x16x32_bf16 a[4:7], v[164:167], v[148:151], a[4:7]\n"
    "v_mfma_f32_16x16x32_bf16 a[8:11], v[160:163], v[136:139], a[8:11]\n"
    "v_mfma_f32_16x16x32_bf16 a[8:11], v[164:167], v[152:155], a[8:11]\n"
    "v_mfma_f32_16x16x32_bf16 a[12:15], v[160:163], v[140:143], a[12:15]\n"
    "v_mfma_f32_16x16x32_bf16 a[12:15], v[164:167], v[156:159], a[12:15]\n"
    "ds_read_b128 v[168:171], v205 offset:14336\n"
    "ds_read_b128 v[172:175], v205 offset:15360\n"
    "s_waitcnt lgkmcnt(1)\n"
    "v_mfma_f32_16x16x32_bf16 a[28:31], v[168:171], v[140:143], a[28:31]\n"
    "s_waitcnt lgkmcnt(0)\n"
    "v_mfma_f32_16x16x32_bf16 a[28:31], v[172:175], v[156:159], a[28:31]\n"
    "v_mfma_f32_16x16x32_bf16 a[24:27], v[172:175], v[152:155], a[24:27]\n"
    "v_mfma_f32_16x16x32_bf16 a[24:27], v[168:171], v[136:139], a[24:27]\n"
    "v_mfma_f32_16x16x32_bf16 a[20:23], v[168:171], v[132:135], a[20:23]\n"
    "v_mfma_f32_16x16x32_bf16 a[20:23], v[172:175], v[148:151], a[20:23]\n"
    "v_mfma_f32_16x16x32_bf16 a[16:19], v[172:175], v[144:147], a[16:19]\n"
    "v_mfma_f32_16x16x32_bf16 a[16:19], v[168:171], v[128:131], a[16:19]\n"
    "ds_read_b128 v[176:179], v205 offset:16384\n"
    "ds_read_b128 v[180:183], v205 offset:17408\n"
    "s_waitcnt lgkmcnt(1)\n"
    "v_mfma_f32_16x16x32_bf16 a[32:35], v[176:179], v[128:131], a[32:35]\n"
    "s_waitcnt lgkmcnt(0)\n"
    "v_mfma_f32_16x16x32_bf16 a[32:35], v[180:183], v[144:147], a[32:35]\n"
    "v_mfma_f32_16x16x32_bf16 a[36:39], v[180:183], v[148:151], a[36:39]\n"
    "v_mfma_f32_16x16x32_bf16 a[36:39], v[176:179], v[132:135], a[36:39]\n"
    "v_mfma_f32_16x16x32_bf16 a[40:43], v[176:179], v[136:139], a[40:43]\n"
    "v_mfma_f32_16x16x32_bf16 a[40:43], v[180:183], v[152:155], a[40:43]\n"
    "v_mfma_f32_16x16x32_bf16 a[44:47], v[180:183], v[156:159], a[44:47]\n"
    "v_mfma_f32_16x16x32_bf16 a[44:47], v[176:179], v[140:143], a[44:47]\n"
    "ds_read_b128 v[160:163], v205 offset:18432\n"
    "ds_read_b128 v[164:167], v205 offset:19456\n"
    "s_waitcnt lgkmcnt(1)\n"
    "v_mfma_f32_16x16x32_bf16 a[60:63], v[160:163], v[140:143], a[60:63]\n"
    "s_waitcnt lgkmcnt(0)\n"
    "v_mfma_f32_16x16x32_bf16 a[60:63], v[164:167], v[156:159], a[60:63]\n"
    "v_mfma_f32_16x16x32_bf16 a[56:59], v[164:167], v[152:155], a[56:59]\n"
    "v_mfma_f32_16x16x32_bf16 a[56:59], v[160:163], v[136:139], a[56:59]\n"
    "v_mfma_f32_16x16x32_bf16 a[52:55], v[160:163], v[132:135], a[52:55]\n"
    "v_mfma_f32_16x16x32_bf16 a[52:55], v[164:167], v[148:151], a[52:55]\n"
    "v_mfma_f32_16x16x32_bf16 a[48:51], v[164:167], v[144:147], a[48:51]\n"
    "v_mfma_f32_16x16x32_bf16 a[48:51], v[160:163], v[128:131], a[48:51]\n"
    "ds_read_b128 v[168:171], v205 offset:20480\n"
    "ds_read_b128 v[172:175], v205 offset:21504\n"
    "s_waitcnt lgkmcnt(1)\n"
    "v_mfma_f32_16x16x32_bf16 a[64:67], v[168:171], v[128:131], a[64:67]\n"
    "s_waitcnt lgkmcnt(0)\n"
    "v_mfma_f32_16x16x32_bf16 a[64:67], v[172:175], v[144:147], a[64:67]\n"
    "v_mfma_f32_16x16x32_bf16 a[68:71], v[172:175], v[148:151], a[68:71]\n"
    "v_mfma_f32_16x16x32_bf16 a[68:71], v[168:171], v[132:135], a[68:71]\n"
    "v_mfma_f32_16x16x32_bf16 a[72:75], v[168:171], v[136:139], a[72:75]\n"
    "v_mfma_f32_16x16x32_bf16 a[72:75], v[172:175], v[152:155], a[72:75]\n"
    "v_mfma_f32_16x16x32_bf16 a[76:79], v[172:175], v[156:159], a[76:79]\n"
    "v_mfma_f32_16x16x32_bf16 a[76:79], v[168:171], v[140:143], a[76:79]\n"
    "ds_read_b128 v[176:179], v205 offset:22528\n"
    "ds_read_b128 v[180:183], v205 offset:23552\n"
    "s_waitcnt lgkmcnt(1)\n"
    "v_mfma_f32_16x16x32_bf16 a[92:95], v[176:179], v[140:143], a[92:95]\n"
    "s_waitcnt lgkmcnt(0)\n"
    "v_mfma_f32_16x16x32_bf16 a[92:95], v[180:183], v[156:159], a[92:95]\n"
    "v_mfma_f32_16x16x32_bf16 a[88:91], v[180:183], v[152:155], a[88:91]\n"
    "v_mfma_f32_16x16x32_bf16 a[88:91], v[176:179], v[136:139], a[88:91]\n"
    "v_mfma_f32_16x16x32_bf16 a[84:87], v[176:179], v[132:135], a[84:87]\n"
    "v_mfma_f32_16x16x32_bf16 a[84:87], v[180:183], v[148:151], a[84:87]\n"
    "v_mfma_f32_16x16x32_bf16 a[80:83], v[180:183], v[144:147], a[80:83]\n"
    "v_mfma_f32_16x16x32_bf16 a[80:83], v[176:179], v[128:131], a[80:83]\n"
    "ds_read_b128 v[160:163], v205 offset:24576\n"
    "ds_read_b128 v[164:167], v205 offset:25600\n"
    "s_waitcnt lgkmcnt(1)\n"
    "v_mfma_f32_16x16x32_bf16 a[96:99], v[160:163], v[128:131], a[96:99]\n"
    "s_waitcnt lgkmcnt(0)\n"
    "v_mfma_f32_16x16x32_bf16 a[96:99], v[164:167], v[144:147], a[96:99]\n"
    "v_mfma_f32_16x16x32_bf16 a[100:103], v[164:167], v[148:151], a[100:103]\n"
    "v_mfma_f32_16x16x32_bf16 a[100:103], v[160:163], v[132:135], a[100:103]\n"
    "v_mfma_f32_16x16x32_bf16 a[104:107], v[160:163], v[136:139], a[104:107]\n"
    "v_mfma_f32_16x16x32_bf16 a[104:107], v[164:167], v[152:155], a[104:107]\n"
    "v_mfma_f32_16x16x32_bf16 a[108:111], v[164:167], v[156:159], a[108:111]\n"
    "v_mfma_f32_16x16x32_bf16 a[108:111], v[160:163], v[140:143], a[108:111]\n"
    "ds_read_b128 v[168:171], v205 offset:26624\n"
    "ds_read_b128 v[172:175], v205 offset:27648\n"
    "s_waitcnt lgkmcnt(1)\n"
    "v_mfma_f32_16x16x32_bf16 a[124:127], v[168:171], v[140:143], a[124:127]\n"
    "s_waitcnt lgkmcnt(0)\n"
    "v_mfma_f32_16x16x32_bf16 a[124:127], v[172:175], v[156:159], a[124:127]\n"
    "v_mfma_f32_16x16x32_bf16 a[120:123], v[172:175], v[152:155], a[120:123]\n"
    "v_mfma_f32_16x16x32_bf16 a[120:123], v[168:171], v[136:139], a[120:123]\n"
    "v_mfma_f32_16x16x32_bf16 a[116:119], v[168:171], v[132:135], a[116:119]\n"
    "v_mfma_f32_16x16x32_bf16 a[116:119], v[172:175], v[148:151], a[116:119]\n"
    "v_mfma_f32_16x16x32_bf16 a[112:115], v[172:175], v[144:147], a[112:115]\n"
    "v_mfma_f32_16x16x32_bf16 a[112:115], v[168:171], v[128:131], a[112:115]\n"
    "ds_read_b128 v[176:179], v205 offset:28672\n"
    "ds_read_b128 v[180:183], v205 offset:29696\n"
    "s_waitcnt lgkmcnt(1)\n"
    "v_mfma_f32_16x16x32_bf16 a[128:131], v[176:179], v[128:131], a[128:131]\n"
    "s_waitcnt lgkmcnt(0)\n"
    "v_mfma_f32_16x16x32_bf16 a[128:131], v[180:183], v[144:147], a[128:131]\n"
    "v_mfma_f32_16x16x32_bf16 a[132:135], v[180:183], v[148:151], a[132:135]\n"
    "v_mfma_f32_16x16x32_bf16 a[132:135], v[176:179], v[132:135], a[132:135]\n"
    "v_mfma_f32_16x16x32_bf16 a[136:139], v[176:179], v[136:139], a[136:139]\n"
    "v_mfma_f32_16x16x32_bf16 a[136:139], v[180:183], v[152:155], a[136:139]\n"
    "v_mfma_f32_16x16x32_bf16 a[140:143], v[180:183], v[156:159], a[140:143]\n"
    "v_mfma_f32_16x16x32_bf16 a[140:143], v[176:179], v[140:143], a[140:143]\n"
    "ds_read_b128 v[160:163], v205 offset:30720\n"
    "ds_read_b128 v[164:167], v205 offset:31744\n"
    "s_waitcnt lgkmcnt(1)\n"
    "v_mfma_f32_16x16x32_bf16 a[156:159], v[160:163], v[140:143], a[156:159]\n"
    "s_waitcnt lgkmcnt(0)\n"
    "v_mfma_f32_16x16x32_bf16 a[156:159], v[164:167], v[156:159], a[156:159]\n"
    "v_mfma_f32_16x16x32_bf16 a[152:155], v[164:167], v[152:155], a[152:155]\n"
    "v_mfma_f32_16x16x32_bf16 a[152:155], v[160:163], v[136:139], a[152:155]\n"
    "v_mfma_f32_16x16x32_bf16 a[148:151], v[160:163], v[132:135], a[148:151]\n"
    "v_mfma_f32_16x16x32_bf16 a[148:151], v[164:167], v[148:151], a[148:151]\n"
    "v_mfma_f32_16x16x32_bf16 a[144:147], v[164:167], v[144:147], a[144:147]\n"
    "v_mfma_f32_16x16x32_bf16 a[144:147], v[160:163], v[128:131], a[144:147]\n"
    "ds_read_b128 v[168:171], v205 offset:32768\n"
    "ds_read_b128 v[172:175], v205 offset:33792\n"
    "s_waitcnt lgkmcnt(1)\n"
    "v_mfma_f32_16x16x32_bf16 a[160:163], v[168:171], v[128:131], a[160:163]\n"
    "s_waitcnt lgkmcnt(0)\n"
    "v_mfma_f32_16x16x32_bf16 a[160:163], v[172:175], v[144:147], a[160:163]\n"
    "v_mfma_f32_16x16x32_bf16 a[164:167], v[172:175], v[148:151], a[164:167]\n"
    "v_mfma_f32_16x16x32_bf16 a[164:167], v[168:171], v[132:135], a[164:167]\n"
    "v_mfma_f32_16x16x32_bf16 a[168:171], v[168:171], v[136:139], a[168:171]\n"
    "v_mfma_f32_16x16x32_bf16 a[168:171], v[172:175], v[152:155], a[168:171]\n"
    "v_mfma_f32_16x16x32_bf16 a[172:175], v[172:175], v[156:159], a[172:175]\n"
    "v_mfma_f32_16x16x32_bf16 a[172:175], v[168:171], v[140:143], a[172:175]\n"
    "ds_read_b128 v[176:179], v205 offset:34816\n"
    "ds_read_b128 v[180:183], v205 offset:35840\n"
    "s_waitcnt lgkmcnt(1)\n"
    "v_mfma_f32_16x16x32_bf16 a[188:191], v[176:179], v[140:143], a[188:191]\n"
    "s_waitcnt lgkmcnt(0)\n"
    "v_mfma_f32_16x16x32_bf16 a[188:191], v[180:183], v[156:159], a[188:191]\n"
    "v_mfma_f32_16x16x32_bf16 a[184:187], v[176:179], v[136:139], a[184:187]\n"
    "v_mfma_f32_16x16x32_bf16 a[184:187], v[180:183], v[152:155], a[184:187]\n"
    "v_mfma_f32_16x16x32_bf16 a[180:183], v[176:179], v[132:135], a[180:183]\n"
    "v_mfma_f32_16x16x32_bf16 a[180:183], v[180:183], v[148:151], a[180:183]\n"
    "v_mfma_f32_16x16x32_bf16 a[176:179], v[176:179], v[128:131], a[176:179]\n"
    "v_mfma_f32_16x16x32_bf16 a[176:179], v[180:183], v[144:147], a[176:179]\n"
    "v_mfma_f32_16x16x32_bf16 v[112:115], v[192:195], v[128:131], v[112:115]\n"
    "v_mfma_f32_16x16x32_bf16 v[112:115], v[192:195], v[144:147], v[112:115]\n"
    "v_mfma_f32_16x16x32_bf16 v[116:119], v[192:195], v[132:135], v[116:119]\n"
    "v_mfma_f32_16x16x32_bf16 v[116:119], v[192:195], v[148:151], v[116:119]\n"
    "v_mfma_f32_16x16x32_bf16 v[120:123], v[192:195], v[136:139], v[120:123]\n"
    "v_mfma_f32_16x16x32_bf16 v[120:123], v[192:195], v[152:155], v[120:123]\n"
    "v_mfma_f32_16x16x32_bf16 v[124:127], v[192:195], v[140:143], v[124:127]\n"
    "v_mfma_f32_16x16x32_bf16 v[124:127], v[192:195], v[156:159], v[124:127]\n"
    "s_nop 15\n"
    "s_add_u32 s63, s63, 0x6000\n"
    "s_cmp_lt_u32 s63, 0xc0000\n"
    "s_cbranch_scc1 LSLOOP\n"
    "s_branch LEPI\n"
    "LEPI:\n"
    "s_lshl_b32 s76, s56, 3\n"
    "s_add_u32 s76, s76, s57\n"
    "s_mul_i32 s76, s76, 0x18000\n"
    "s_add_u32 s76, s76, s88\n"
    "s_add_u32 s28, s24, s76\n"
    "s_addc_u32 s29, s25, 0\n"
    "s_and_b32 s29, s29, 0xffff\n"
    "s_mov_b32 s30, 0x6000\n"
    "s_mov_b32 s31, 0x20000\n"
    "s_xor_b32 s94, s70, 0x80000000\n"
    "s_xor_b32 s95, s70, 0x80000000\n"
    "s_mov_b32 s62, 0x1800\n"
    "s_mov_b32 s63, 0x3000\n"
    "s_mov_b32 s64, 0x4800\n"
    "v_and_b32 v198, 15, v231\n"
    "v_mul_u32_u24 v205, 0x180, v198\n"
    "v_lshrrev_b32 v199, 4, v231\n"
    "v_lshl_add_u32 v205, v199, 4, v205\n"
    "v_mul_f32 v222, s68, v112\n"
    "v_rcp_f32 v222, v222\n"
    "v_mul_f32 v224, s68, v116\n"
    "v_rcp_f32 v224, v224\n"
    "v_mul_f32 v226, s68, v120\n"
    "v_rcp_f32 v226, v226\n"
    "v_mul_f32 v228, s68, v124\n"
    "v_rcp_f32 v228, v228\n"
    "v_mov_b32 v223, v222\n"
    "v_mov_b32 v225, v224\n"
    "v_mov_b32 v227, v226\n"
    "v_mov_b32 v229, v228\n"
    "v_accvgpr_read_b32 v96, a0\n"
    "v_accvgpr_read_b32 v97, a1\n"
    "v_accvgpr_read_b32 v98, a2\n"
    "v_accvgpr_read_b32 v99, a3\n"
    "v_pk_fma_f32 v[96:97], v[96:97], v[222:223], s[94:95]\n"
    "v_pk_fma_f32 v[98:99], v[98:99], v[222:223], s[94:95]\n"
    "v_cvt_pk_bf16_f32 v32, v96, v97\n"
    "v_cvt_pk_bf16_f32 v33, v98, v99\n"
    "v_accvgpr_read_b32 v100, a16\n"
    "v_accvgpr_read_b32 v101, a17\n"
    "v_accvgpr_read_b32 v102, a18\n"
    "v_accvgpr_read_b32 v103, a19\n"
    "v_pk_fma_f32 v[100:101], v[100:101], v[222:223], s[94:95]\n"
    "v_pk_fma_f32 v[102:103], v[102:103], v[222:223], s[94:95]\n"
    "v_cvt_pk_bf16_f32 v34, v100, v101\n"
    "v_cvt_pk_bf16_f32 v35, v102, v103\n"
    "buffer_store_dwordx4 v[32:35], v205, s[28:31], 0 offen sc0 sc1\n"
    "v_accvgpr_read_b32 v104, a32\n"
    "v_accvgpr_read_b32 v105, a33\n"
    "v_accvgpr_read_b32 v106, a34\n"
    "v_accvgpr_read_b32 v107, a35\n"
    "v_pk_fma_f32 v[104:105], v[104:105], v[222:223], s[94:95]\n"
    "v_pk_fma_f32 v[106:107], v[106:107], v[222:223], s[94:95]\n"
    "v_cvt_pk_bf16_f32 v36, v104, v105\n"
    "v_cvt_pk_bf16_f32 v37, v106, v107\n"
    "v_accvgpr_read_b32 v108, a48\n"
    "v_accvgpr_read_b32 v109, a49\n"
    "v_accvgpr_read_b32 v110, a50\n"
    "v_accvgpr_read_b32 v111, a51\n"
    "v_pk_fma_f32 v[108:109], v[108:109], v[222:223], s[94:95]\n"
    "v_pk_fma_f32 v[110:111], v[110:111], v[222:223], s[94:95]\n"
    "v_cvt_pk_bf16_f32 v38, v108, v109\n"
    "v_cvt_pk_bf16_f32 v39, v110, v111\n"
    "buffer_store_dwordx4 v[36:39], v205, s[28:31], 0 offen offset:64 sc0 sc1\n"
    "v_accvgpr_read_b32 v96, a64\n"
    "v_accvgpr_read_b32 v97, a65\n"
    "v_accvgpr_read_b32 v98, a66\n"
    "v_accvgpr_read_b32 v99, a67\n"
    "v_pk_fma_f32 v[96:97], v[96:97], v[222:223], s[94:95]\n"
    "v_pk_fma_f32 v[98:99], v[98:99], v[222:223], s[94:95]\n"
    "v_cvt_pk_bf16_f32 v40, v96, v97\n"
    "v_cvt_pk_bf16_f32 v41, v98, v99\n"
    "v_accvgpr_read_b32 v100, a80\n"
    "v_accvgpr_read_b32 v101, a81\n"
    "v_accvgpr_read_b32 v102, a82\n"
    "v_accvgpr_read_b32 v103, a83\n"
    "v_pk_fma_f32 v[100:101], v[100:101], v[222:223], s[94:95]\n"
    "v_pk_fma_f32 v[102:103], v[102:103], v[222:223], s[94:95]\n"
    "v_cvt_pk_bf16_f32 v42, v100, v101\n"
    "v_cvt_pk_bf16_f32 v43, v102, v103\n"
    "buffer_store_dwordx4 v[40:43], v205, s[28:31], 0 offen offset:128 sc0 sc1\n"
    "v_accvgpr_read_b32 v104, a96\n"
    "v_accvgpr_read_b32 v105, a97\n"
    "v_accvgpr_read_b32 v106, a98\n"
    "v_accvgpr_read_b32 v107, a99\n"
    "v_pk_fma_f32 v[104:105], v[104:105], v[222:223], s[94:95]\n"
    "v_pk_fma_f32 v[106:107], v[106:107], v[222:223], s[94:95]\n"
    "v_cvt_pk_bf16_f32 v44, v104, v105\n"
    "v_cvt_pk_bf16_f32 v45, v106, v107\n"
    "v_accvgpr_read_b32 v108, a112\n"
    "v_accvgpr_read_b32 v109, a113\n"
    "v_accvgpr_read_b32 v110, a114\n"
    "v_accvgpr_read_b32 v111, a115\n"
    "v_pk_fma_f32 v[108:109], v[108:109], v[222:223], s[94:95]\n"
    "v_pk_fma_f32 v[110:111], v[110:111], v[222:223], s[94:95]\n"
    "v_cvt_pk_bf16_f32 v46, v108, v109\n"
    "v_cvt_pk_bf16_f32 v47, v110, v111\n"
    "buffer_store_dwordx4 v[44:47], v205, s[28:31], 0 offen offset:192 sc0 sc1\n"
    "v_accvgpr_read_b32 v96, a128\n"
    "v_accvgpr_read_b32 v97, a129\n"
    "v_accvgpr_read_b32 v98, a130\n"
    "v_accvgpr_read_b32 v99, a131\n"
    "v_pk_fma_f32 v[96:97], v[96:97], v[222:223], s[94:95]\n"
    "v_pk_fma_f32 v[98:99], v[98:99], v[222:223], s[94:95]\n"
    "v_cvt_pk_bf16_f32 v48, v96, v97\n"
    "v_cvt_pk_bf16_f32 v49, v98, v99\n"
    "v_accvgpr_read_b32 v100, a144\n"
    "v_accvgpr_read_b32 v101, a145\n"
    "v_accvgpr_read_b32 v102, a146\n"
    "v_accvgpr_read_b32 v103, a147\n"
    "v_pk_fma_f32 v[100:101], v[100:101], v[222:223], s[94:95]\n"
    "v_pk_fma_f32 v[102:103], v[102:103], v[222:223], s[94:95]\n"
    "v_cvt_pk_bf16_f32 v50, v100, v101\n"
    "v_cvt_pk_bf16_f32 v51, v102, v103\n"
    "buffer_store_dwordx4 v[48:51], v205, s[28:31], 0 offen offset:256 sc0 sc1\n"
    "v_accvgpr_read_b32 v104, a160\n"
    "v_accvgpr_read_b32 v105, a161\n"
    "v_accvgpr_read_b32 v106, a162\n"
    "v_accvgpr_read_b32 v107, a163\n"
    "v_pk_fma_f32 v[104:105], v[104:105], v[222:223], s[94:95]\n"
    "v_pk_fma_f32 v[106:107], v[106:107], v[222:223], s[94:95]\n"
    "v_cvt_pk_bf16_f32 v52, v104, v105\n"
    "v_cvt_pk_bf16_f32 v53, v106, v107\n"
    "v_accvgpr_read_b32 v108, a176\n"
    "v_accvgpr_read_b32 v109, a177\n"
    "v_accvgpr_read_b32 v110, a178\n"
    "v_accvgpr_read_b32 v111, a179\n"
    "v_pk_fma_f32 v[108:109], v[108:109], v[222:223], s[94:95]\n"
    "v_pk_fma_f32 v[110:111], v[110:111], v[222:223], s[94:95]\n"
    "v_cvt_pk_bf16_f32 v54, v108, v109\n"
    "v_cvt_pk_bf16_f32 v55, v110, v111\n"
    "buffer_store_dwordx4 v[52:55], v205, s[28:31], 0 offen offset:320 sc0 sc1\n"
    "v_accvgpr_read_b32 v96, a4\n"
    "v_accvgpr_read_b32 v97, a5\n"
    "v_accvgpr_read_b32 v98, a6\n"
    "v_accvgpr_read_b32 v99, a7\n"
    "v_pk_fma_f32 v[96:97], v[96:97], v[224:225], s[94:95]\n"
    "v_pk_fma_f32 v[98:99], v[98:99], v[224:225], s[94:95]\n"
    "v_cvt_pk_bf16_f32 v56, v96, v97\n"
    "v_cvt_pk_bf16_f32 v57, v98, v99\n"
    "v_accvgpr_read_b32 v100, a20\n"
    "v_accvgpr_read_b32 v101, a21\n"
    "v_accvgpr_read_b32 v102, a22\n"
    "v_accvgpr_read_b32 v103, a23\n"
    "v_pk_fma_f32 v[100:101], v[100:101], v[224:225], s[94:95]\n"
    "v_pk_fma_f32 v[102:103], v[102:103], v[224:225], s[94:95]\n"
    "v_cvt_pk_bf16_f32 v58, v100, v101\n"
    "v_cvt_pk_bf16_f32 v59, v102, v103\n"
    "buffer_store_dwordx4 v[56:59], v205, s[28:31], s62 offen sc0 sc1\n"
    "v_accvgpr_read_b32 v104, a36\n"
    "v_accvgpr_read_b32 v105, a37\n"
    "v_accvgpr_read_b32 v106, a38\n"
    "v_accvgpr_read_b32 v107, a39\n"
    "v_pk_fma_f32 v[104:105], v[104:105], v[224:225], s[94:95]\n"
    "v_pk_fma_f32 v[106:107], v[106:107], v[224:225], s[94:95]\n"
    "v_cvt_pk_bf16_f32 v60, v104, v105\n"
    "v_cvt_pk_bf16_f32 v61, v106, v107\n"
    "v_accvgpr_read_b32 v108, a52\n"
    "v_accvgpr_read_b32 v109, a53\n"
    "v_accvgpr_read_b32 v110, a54\n"
    "v_accvgpr_read_b32 v111, a55\n"
    "v_pk_fma_f32 v[108:109], v[108:109], v[224:225], s[94:95]\n"
    "v_pk_fma_f32 v[110:111], v[110:111], v[224:225], s[94:95]\n"
    "v_cvt_pk_bf16_f32 v62, v108, v109\n"
    "v_cvt_pk_bf16_f32 v63, v110, v111\n"
    "buffer_store_dwordx4 v[60:63], v205, s[28:31], s62 offen offset:64 sc0 sc1\n"
    "v_accvgpr_read_b32 v96, a68\n"
    "v_accvgpr_read_b32 v97, a69\n"
    "v_accvgpr_read_b32 v98, a70\n"
    "v_accvgpr_read_b32 v99, a71\n"
    "v_pk_fma_f32 v[96:97], v[96:97], v[224:225], s[94:95]\n"
    "v_pk_fma_f32 v[98:99], v[98:99], v[224:225], s[94:95]\n"
    "v_cvt_pk_bf16_f32 v32, v96, v97\n"
    "v_cvt_pk_bf16_f32 v33, v98, v99\n"
    "v_accvgpr_read_b32 v100, a84\n"
    "v_accvgpr_read_b32 v101, a85\n"
    "v_accvgpr_read_b32 v102, a86\n"
    "v_accvgpr_read_b32 v103, a87\n"
    "v_pk_fma_f32 v[100:101], v[100:101], v[224:225], s[94:95]\n"
    "v_pk_fma_f32 v[102:103], v[102:103], v[224:225], s[94:95]\n"
    "v_cvt_pk_bf16_f32 v34, v100, v101\n"
    "v_cvt_pk_bf16_f32 v35, v102, v103\n"
    "buffer_store_dwordx4 v[32:35], v205, s[28:31], s62 offen offset:128 sc0 sc1\n"
    "v_accvgpr_read_b32 v104, a100\n"
    "v_accvgpr_read_b32 v105, a101\n"
    "v_accvgpr_read_b32 v106, a102\n"
    "v_accvgpr_read_b32 v107, a103\n"
    "v_pk_fma_f32 v[104:105], v[104:105], v[224:225], s[94:95]\n"
    "v_pk_fma_f32 v[106:107], v[106:107], v[224:225], s[94:95]\n"
    "v_cvt_pk_bf16_f32 v36, v104, v105\n"
    "v_cvt_pk_bf16_f32 v37, v106, v107\n"
    "v_accvgpr_read_b32 v108, a116\n"
    "v_accvgpr_read_b32 v109, a117\n"
    "v_accvgpr_read_b32 v110, a118\n"
    "v_accvgpr_read_b32 v111, a119\n"
    "v_pk_fma_f32 v[108:109], v[108:109], v[224:225], s[94:95]\n"
    "v_pk_fma_f32 v[110:111], v[110:111], v[224:225], s[94:95]\n"
    "v_cvt_pk_bf16_f32 v38, v108, v109\n"
    "v_cvt_pk_bf16_f32 v39, v110, v111\n"
    "buffer_store_dwordx4 v[36:39], v205, s[28:31], s62 offen offset:192 sc0 sc1\n"
    "v_accvgpr_read_b32 v96, a132\n"
    "v_accvgpr_read_b32 v97, a133\n"
    "v_accvgpr_read_b32 v98, a134\n"
    "v_accvgpr_read_b32 v99, a135\n"
    "v_pk_fma_f32 v[96:97], v[96:97], v[224:225], s[94:95]\n"
    "v_pk_fma_f32 v[98:99], v[98:99], v[224:225], s[94:95]\n"
    "v_cvt_pk_bf16_f32 v40, v96, v97\n"
    "v_cvt_pk_bf16_f32 v41, v98, v99\n"
    "v_accvgpr_read_b32 v100, a148\n"
    "v_accvgpr_read_b32 v101, a149\n"
    "v_accvgpr_read_b32 v102, a150\n"
    "v_accvgpr_read_b32 v103, a151\n"
    "v_pk_fma_f32 v[100:101], v[100:101], v[224:225], s[94:95]\n"
    "v_pk_fma_f32 v[102:103], v[102:103], v[224:225], s[94:95]\n"
    "v_cvt_pk_bf16_f32 v42, v100, v101\n"
    "v_cvt_pk_bf16_f32 v43, v102, v103\n"
    "buffer_store_dwordx4 v[40:43], v205, s[28:31], s62 offen offset:256 sc0 sc1\n"
    "v_accvgpr_read_b32 v104, a164\n"
    "v_accvgpr_read_b32 v105, a165\n"
    "v_accvgpr_read_b32 v106, a166\n"
    "v_accvgpr_read_b32 v107, a167\n"
    "v_pk_fma_f32 v[104:105], v[104:105], v[224:225], s[94:95]\n"
    "v_pk_fma_f32 v[106:107], v[106:107], v[224:225], s[94:95]\n"
    "v_cvt_pk_bf16_f32 v44, v104, v105\n"
    "v_cvt_pk_bf16_f32 v45, v106, v107\n"
    "v_accvgpr_read_b32 v108, a180\n"
    "v_accvgpr_read_b32 v109, a181\n"
    "v_accvgpr_read_b32 v110, a182\n"
    "v_accvgpr_read_b32 v111, a183\n"
    "v_pk_fma_f32 v[108:109], v[108:109], v[224:225], s[94:95]\n"
    "v_pk_fma_f32 v[110:111], v[110:111], v[224:225], s[94:95]\n"
    "v_cvt_pk_bf16_f32 v46, v108, v109\n"
    "v_cvt_pk_bf16_f32 v47, v110, v111\n"
    "buffer_store_dwordx4 v[44:47], v205, s[28:31], s62 offen offset:320 sc0 sc1\n"
    "v_accvgpr_read_b32 v96, a8\n"
    "v_accvgpr_read_b32 v97, a9\n"
    "v_accvgpr_read_b32 v98, a10\n"
    "v_accvgpr_read_b32 v99, a11\n"
    "v_pk_fma_f32 v[96:97], v[96:97], v[226:227], s[94:95]\n"
    "v_pk_fma_f32 v[98:99], v[98:99], v[226:227], s[94:95]\n"
    "v_cvt_pk_bf16_f32 v48, v96, v97\n"
    "v_cvt_pk_bf16_f32 v49, v98, v99\n"
    "v_accvgpr_read_b32 v100, a24\n"
    "v_accvgpr_read_b32 v101, a25\n"
    "v_accvgpr_read_b32 v102, a26\n"
    "v_accvgpr_read_b32 v103, a27\n"
    "v_pk_fma_f32 v[100:101], v[100:101], v[226:227], s[94:95]\n"
    "v_pk_fma_f32 v[102:103], v[102:103], v[226:227], s[94:95]\n"
    "v_cvt_pk_bf16_f32 v50, v100, v101\n"
    "v_cvt_pk_bf16_f32 v51, v102, v103\n"
    "buffer_store_dwordx4 v[48:51], v205, s[28:31], s63 offen sc0 sc1\n"
    "v_accvgpr_read_b32 v104, a40\n"
    "v_accvgpr_read_b32 v105, a41\n"
    "v_accvgpr_read_b32 v106, a42\n"
    "v_accvgpr_read_b32 v107, a43\n"
    "v_pk_fma_f32 v[104:105], v[104:105], v[226:227], s[94:95]\n"
    "v_pk_fma_f32 v[106:107], v[106:107], v[226:227], s[94:95]\n"
    "v_cvt_pk_bf16_f32 v52, v104, v105\n"
    "v_cvt_pk_bf16_f32 v53, v106, v107\n"
    "v_accvgpr_read_b32 v108, a56\n"
    "v_accvgpr_read_b32 v109, a57\n"
    "v_accvgpr_read_b32 v110, a58\n"
    "v_accvgpr_read_b32 v111, a59\n"
    "v_pk_fma_f32 v[108:109], v[108:109], v[226:227], s[94:95]\n"
    "v_pk_fma_f32 v[110:111], v[110:111], v[226:227], s[94:95]\n"
    "v_cvt_pk_bf16_f32 v54, v108, v109\n"
    "v_cvt_pk_bf16_f32 v55, v110, v111\n"
    "buffer_store_dwordx4 v[52:55], v205, s[28:31], s63 offen offset:64 sc0 sc1\n"
    "v_accvgpr_read_b32 v96, a72\n"
    "v_accvgpr_read_b32 v97, a73\n"
    "v_accvgpr_read_b32 v98, a74\n"
    "v_accvgpr_read_b32 v99, a75\n"
    "v_pk_fma_f32 v[96:97], v[96:97], v[226:227], s[94:95]\n"
    "v_pk_fma_f32 v[98:99], v[98:99], v[226:227], s[94:95]\n"
    "v_cvt_pk_bf16_f32 v56, v96, v97\n"
    "v_cvt_pk_bf16_f32 v57, v98, v99\n"
    "v_accvgpr_read_b32 v100, a88\n"
    "v_accvgpr_read_b32 v101, a89\n"
    "v_accvgpr_read_b32 v102, a90\n"
    "v_accvgpr_read_b32 v103, a91\n"
    "v_pk_fma_f32 v[100:101], v[100:101], v[226:227], s[94:95]\n"
    "v_pk_fma_f32 v[102:103], v[102:103], v[226:227], s[94:95]\n"
    "v_cvt_pk_bf16_f32 v58, v100, v101\n"
    "v_cvt_pk_bf16_f32 v59, v102, v103\n"
    "buffer_store_dwordx4 v[56:59], v205, s[28:31], s63 offen offset:128 sc0 sc1\n"
    "v_accvgpr_read_b32 v104, a104\n"
    "v_accvgpr_read_b32 v105, a105\n"
    "v_accvgpr_read_b32 v106, a106\n"
    "v_accvgpr_read_b32 v107, a107\n"
    "v_pk_fma_f32 v[104:105], v[104:105], v[226:227], s[94:95]\n"
    "v_pk_fma_f32 v[106:107], v[106:107], v[226:227], s[94:95]\n"
    "v_cvt_pk_bf16_f32 v60, v104, v105\n"
    "v_cvt_pk_bf16_f32 v61, v106, v107\n"
    "v_accvgpr_read_b32 v108, a120\n"
    "v_accvgpr_read_b32 v109, a121\n"
    "v_accvgpr_read_b32 v110, a122\n"
    "v_accvgpr_read_b32 v111, a123\n"
    "v_pk_fma_f32 v[108:109], v[108:109], v[226:227], s[94:95]\n"
    "v_pk_fma_f32 v[110:111], v[110:111], v[226:227], s[94:95]\n"
    "v_cvt_pk_bf16_f32 v62, v108, v109\n"
    "v_cvt_pk_bf16_f32 v63, v110, v111\n"
    "buffer_store_dwordx4 v[60:63], v205, s[28:31], s63 offen offset:192 sc0 sc1\n"
    "v_accvgpr_read_b32 v96, a136\n"
    "v_accvgpr_read_b32 v97, a137\n"
    "v_accvgpr_read_b32 v98, a138\n"
    "v_accvgpr_read_b32 v99, a139\n"
    "v_pk_fma_f32 v[96:97], v[96:97], v[226:227], s[94:95]\n"
    "v_pk_fma_f32 v[98:99], v[98:99], v[226:227], s[94:95]\n"
    "v_cvt_pk_bf16_f32 v32, v96, v97\n"
    "v_cvt_pk_bf16_f32 v33, v98, v99\n"
    "v_accvgpr_read_b32 v100, a152\n"
    "v_accvgpr_read_b32 v101, a153\n"
    "v_accvgpr_read_b32 v102, a154\n"
    "v_accvgpr_read_b32 v103, a155\n"
    "v_pk_fma_f32 v[100:101], v[100:101], v[226:227], s[94:95]\n"
    "v_pk_fma_f32 v[102:103], v[102:103], v[226:227], s[94:95]\n"
    "v_cvt_pk_bf16_f32 v34, v100, v101\n"
    "v_cvt_pk_bf16_f32 v35, v102, v103\n"
    "buffer_store_dwordx4 v[32:35], v205, s[28:31], s63 offen offset:256 sc0 sc1\n"
    "v_accvgpr_read_b32 v104, a168\n"
    "v_accvgpr_read_b32 v105, a169\n"
    "v_accvgpr_read_b32 v106, a170\n"
    "v_accvgpr_read_b32 v107, a171\n"
    "v_pk_fma_f32 v[104:105], v[104:105], v[226:227], s[94:95]\n"
    "v_pk_fma_f32 v[106:107], v[106:107], v[226:227], s[94:95]\n"
    "v_cvt_pk_bf16_f32 v36, v104, v105\n"
    "v_cvt_pk_bf16_f32 v37, v106, v107\n"
    "v_accvgpr_read_b32 v108, a184\n"
    "v_accvgpr_read_b32 v109, a185\n"
    "v_accvgpr_read_b32 v110, a186\n"
    "v_accvgpr_read_b32 v111, a187\n"
    "v_pk_fma_f32 v[108:109], v[108:109], v[226:227], s[94:95]\n"
    "v_pk_fma_f32 v[110:111], v[110:111], v[226:227], s[94:95]\n"
    "v_cvt_pk_bf16_f32 v38, v108, v109\n"
    "v_cvt_pk_bf16_f32 v39, v110, v111\n"
    "buffer_store_dwordx4 v[36:39], v205, s[28:31], s63 offen offset:320 sc0 sc1\n"
    "v_accvgpr_read_b32 v96, a12\n"
    "v_accvgpr_read_b32 v97, a13\n"
    "v_accvgpr_read_b32 v98, a14\n"
    "v_accvgpr_read_b32 v99, a15\n"
    "v_pk_fma_f32 v[96:97], v[96:97], v[228:229], s[94:95]\n"
    "v_pk_fma_f32 v[98:99], v[98:99], v[228:229], s[94:95]\n"
    "v_cvt_pk_bf16_f32 v40, v96, v97\n"
    "v_cvt_pk_bf16_f32 v41, v98, v99\n"
    "v_accvgpr_read_b32 v100, a28\n"
    "v_accvgpr_read_b32 v101, a29\n"
    "v_accvgpr_read_b32 v102, a30\n"
    "v_accvgpr_read_b32 v103, a31\n"
    "v_pk_fma_f32 v[100:101], v[100:101], v[228:229], s[94:95]\n"
    "v_pk_fma_f32 v[102:103], v[102:103], v[228:229], s[94:95]\n"
    "v_cvt_pk_bf16_f32 v42, v100, v101\n"
    "v_cvt_pk_bf16_f32 v43, v102, v103\n"
    "buffer_store_dwordx4 v[40:43], v205, s[28:31], s64 offen sc0 sc1\n"
    "v_accvgpr_read_b32 v104, a44\n"
    "v_accvgpr_read_b32 v105, a45\n"
    "v_accvgpr_read_b32 v106, a46\n"
    "v_accvgpr_read_b32 v107, a47\n"
    "v_pk_fma_f32 v[104:105], v[104:105], v[228:229], s[94:95]\n"
    "v_pk_fma_f32 v[106:107], v[106:107], v[228:229], s[94:95]\n"
    "v_cvt_pk_bf16_f32 v44, v104, v105\n"
    "v_cvt_pk_bf16_f32 v45, v106, v107\n"
    "v_accvgpr_read_b32 v108, a60\n"
    "v_accvgpr_read_b32 v109, a61\n"
    "v_accvgpr_read_b32 v110, a62\n"
    "v_accvgpr_read_b32 v111, a63\n"
    "v_pk_fma_f32 v[108:109], v[108:109], v[228:229], s[94:95]\n"
    "v_pk_fma_f32 v[110:111], v[110:111], v[228:229], s[94:95]\n"
    "v_cvt_pk_bf16_f32 v46, v108, v109\n"
    "v_cvt_pk_bf16_f32 v47, v110, v111\n"
    "buffer_store_dwordx4 v[44:47], v205, s[28:31], s64 offen offset:64 sc0 sc1\n"
    "v_accvgpr_read_b32 v96, a76\n"
    "v_accvgpr_read_b32 v97, a77\n"
    "v_accvgpr_read_b32 v98, a78\n"
    "v_accvgpr_read_b32 v99, a79\n"
    "v_pk_fma_f32 v[96:97], v[96:97], v[228:229], s[94:95]\n"
    "v_pk_fma_f32 v[98:99], v[98:99], v[228:229], s[94:95]\n"
    "v_cvt_pk_bf16_f32 v48, v96, v97\n"
    "v_cvt_pk_bf16_f32 v49, v98, v99\n"
    "v_accvgpr_read_b32 v100, a92\n"
    "v_accvgpr_read_b32 v101, a93\n"
    "v_accvgpr_read_b32 v102, a94\n"
    "v_accvgpr_read_b32 v103, a95\n"
    "v_pk_fma_f32 v[100:101], v[100:101], v[228:229], s[94:95]\n"
    "v_pk_fma_f32 v[102:103], v[102:103], v[228:229], s[94:95]\n"
    "v_cvt_pk_bf16_f32 v50, v100, v101\n"
    "v_cvt_pk_bf16_f32 v51, v102, v103\n"
    "buffer_store_dwordx4 v[48:51], v205, s[28:31], s64 offen offset:128 sc0 sc1\n"
    "v_accvgpr_read_b32 v104, a108\n"
    "v_accvgpr_read_b32 v105, a109\n"
    "v_accvgpr_read_b32 v106, a110\n"
    "v_accvgpr_read_b32 v107, a111\n"
    "v_pk_fma_f32 v[104:105], v[104:105], v[228:229], s[94:95]\n"
    "v_pk_fma_f32 v[106:107], v[106:107], v[228:229], s[94:95]\n"
    "v_cvt_pk_bf16_f32 v52, v104, v105\n"
    "v_cvt_pk_bf16_f32 v53, v106, v107\n"
    "v_accvgpr_read_b32 v108, a124\n"
    "v_accvgpr_read_b32 v109, a125\n"
    "v_accvgpr_read_b32 v110, a126\n"
    "v_accvgpr_read_b32 v111, a127\n"
    "v_pk_fma_f32 v[108:109], v[108:109], v[228:229], s[94:95]\n"
    "v_pk_fma_f32 v[110:111], v[110:111], v[228:229], s[94:95]\n"
    "v_cvt_pk_bf16_f32 v54, v108, v109\n"
    "v_cvt_pk_bf16_f32 v55, v110, v111\n"
    "buffer_store_dwordx4 v[52:55], v205, s[28:31], s64 offen offset:192 sc0 sc1\n"
    "v_accvgpr_read_b32 v96, a140\n"
    "v_accvgpr_read_b32 v97, a141\n"
    "v_accvgpr_read_b32 v98, a142\n"
    "v_accvgpr_read_b32 v99, a143\n"
    "v_pk_fma_f32 v[96:97], v[96:97], v[228:229], s[94:95]\n"
    "v_pk_fma_f32 v[98:99], v[98:99], v[228:229], s[94:95]\n"
    "v_cvt_pk_bf16_f32 v56, v96, v97\n"
    "v_cvt_pk_bf16_f32 v57, v98, v99\n"
    "v_accvgpr_read_b32 v100, a156\n"
    "v_accvgpr_read_b32 v101, a157\n"
    "v_accvgpr_read_b32 v102, a158\n"
    "v_accvgpr_read_b32 v103, a159\n"
    "v_pk_fma_f32 v[100:101], v[100:101], v[228:229], s[94:95]\n"
    "v_pk_fma_f32 v[102:103], v[102:103], v[228:229], s[94:95]\n"
    "v_cvt_pk_bf16_f32 v58, v100, v101\n"
    "v_cvt_pk_bf16_f32 v59, v102, v103\n"
    "buffer_store_dwordx4 v[56:59], v205, s[28:31], s64 offen offset:256 sc0 sc1\n"
    "v_accvgpr_read_b32 v104, a172\n"
    "v_accvgpr_read_b32 v105, a173\n"
    "v_accvgpr_read_b32 v106, a174\n"
    "v_accvgpr_read_b32 v107, a175\n"
    "v_pk_fma_f32 v[104:105], v[104:105], v[228:229], s[94:95]\n"
    "v_pk_fma_f32 v[106:107], v[106:107], v[228:229], s[94:95]\n"
    "v_cvt_pk_bf16_f32 v60, v104, v105\n"
    "v_cvt_pk_bf16_f32 v61, v106, v107\n"
    "v_accvgpr_read_b32 v108, a188\n"
    "v_accvgpr_read_b32 v109, a189\n"
    "v_accvgpr_read_b32 v110, a190\n"
    "v_accvgpr_read_b32 v111, a191\n"
    "v_pk_fma_f32 v[108:109], v[108:109], v[228:229], s[94:95]\n"
    "v_pk_fma_f32 v[110:111], v[110:111], v[228:229], s[94:95]\n"
    "v_cvt_pk_bf16_f32 v62, v108, v109\n"
    "v_cvt_pk_bf16_f32 v63, v110, v111\n"
    "buffer_store_dwordx4 v[60:63], v205, s[28:31], s64 offen offset:320 sc0 sc1\n"
    "s_branch LFIN\n"
    "LEND:\n"
    :
    : "s"(__builtin_amdgcn_kernarg_segment_ptr()), "s"(blockIdx.x), "s"(wid)
    : "v0", "v1", "v2", "v3", "v4", "v5", "v6", "v7", "v8", "v9", "v10", "v11", "v12", "v13", "v14", "v15", "v16", "v17", "v18", "v19", "v20", "v21", "v22", "v23", "v24", "v25", "v26", "v27", "v28", "v29", "v30", "v31", "v32", "v33", "v34", "v35", "v36", "v37", "v38", "v39", "v40", "v41", "v42", "v43", "v44", "v45", "v46", "v47", "v48", "v49", "v50", "v51", "v52", "v53", "v54", "v55", "v56", "v57", "v58", "v59", "v60", "v61", "v62", "v63", "v64", "v65", "v66", "v67", "v68", "v69", "v70", "v71", "v72", "v73", "v74", "v75", "v76", "v77", "v78", "v79", "v80", "v81", "v82", "v83", "v84", "v85", "v86", "v87", "v88", "v89", "v90", "v91", "v92", "v93", "v94", "v95", "v96", "v97", "v98", "v99", "v100", "v101", "v102", "v103", "v104", "v105", "v106", "v107", "v108", "v109", "v110", "v111", "v112", "v113", "v114", "v115", "v116", "v117", "v118", "v119", "v120", "v121", "v122", "v123", "v124", "v125", "v126", "v127", "v128", "v129", "v130", "v131", "v132", "v133", "v134", "v135", "v136", "v137", "v138", "v139", "v140", "v141", "v142", "v143", "v144", "v145", "v146", "v147", "v148", "v149", "v150", "v151", "v152", "v153", "v154", "v155", "v156", "v157", "v158", "v159", "v160", "v161", "v162", "v163", "v164", "v165", "v166", "v167", "v168", "v169", "v170", "v171", "v172", "v173", "v174", "v175", "v176", "v177", "v178", "v179", "v180", "v181", "v182", "v183", "v184", "v185", "v186", "v187", "v188", "v189", "v190", "v191", "v192", "v193", "v194", "v195", "v196", "v197", "v198", "v199", "v200", "v201", "v202", "v203", "v204", "v205", "v206", "v207", "v208", "v209", "v210", "v211", "v212", "v213", "v214", "v215", "v216", "v217", "v218", "v219", "v220", "v221", "v222", "v223", "v224", "v225", "v226", "v227", "v228", "v229", "v230", "v231", "v232", "v233", "v234", "v235", "v236", "v237", "v238", "v239", "v240", "v241", "v242", "v243", "v244", "v245", "v246", "v247", "v248", "v249", "v250", "v251", "v252", "v253", "v254", "v255", "a0", "a1", "a2", "a3", "a4", "a5", "a6", "a7", "a8", "a9", "a10", "a11", "a12", "a13", "a14", "a15", "a16", "a17", "a18", "a19", "a20", "a21", "a22", "a23", "a24", "a25", "a26", "a27", "a28", "a29", "a30", "a31", "a32", "a33", "a34", "a35", "a36", "a37", "a38", "a39", "a40", "a41", "a42", "a43", "a44", "a45", "a46", "a47", "a48", "a49", "a50", "a51", "a52", "a53", "a54", "a55", "a56", "a57", "a58", "a59", "a60", "a61", "a62", "a63", "a64", "a65", "a66", "a67", "a68", "a69", "a70", "a71", "a72", "a73", "a74", "a75", "a76", "a77", "a78", "a79", "a80", "a81", "a82", "a83", "a84", "a85", "a86", "a87", "a88", "a89", "a90", "a91", "a92", "a93", "a94", "a95", "a96", "a97", "a98", "a99", "a100", "a101", "a102", "a103", "a104", "a105", "a106", "a107", "a108", "a109", "a110", "a111", "a112", "a113", "a114", "a115", "a116", "a117", "a118", "a119", "a120", "a121", "a122", "a123", "a124", "a125", "a126", "a127", "a128", "a129", "a130", "a131", "a132", "a133", "a134", "a135", "a136", "a137", "a138", "a139", "a140", "a141", "a142", "a143", "a144", "a145", "a146", "a147", "a148", "a149", "a150", "a151", "a152", "a153", "a154", "a155", "a156", "a157", "a158", "a159", "a160", "a161", "a162", "a163", "a164", "a165", "a166", "a167", "a168", "a169", "a170", "a171", "a172", "a173", "a174", "a175", "a176", "a177", "a178", "a179", "a180", "a181", "a182", "a183", "a184", "a185", "a186", "a187", "a188", "a189", "a190", "a191", "a192", "a193", "a194", "a195", "a196", "a197", "a198", "a199", "a200", "a201", "a202", "a203", "a204", "a205", "a206", "a207", "a208", "a209", "a210", "a211", "a212", "a213", "a214", "a215", "a216", "a217", "a218", "a219", "a220", "a221", "a222", "a223", "a224", "a225", "a226", "a227", "a228", "a229", "a230", "a231", "a232", "a233", "a234", "a235", "a236", "a237", "a238", "a239", "a240", "a241", "a242", "a243", "a244", "a245", "a246", "a247", "a248", "a249", "a250", "a251", "a252", "a253", "a254", "a255", "s8", "s9", "s10", "s11", "s12", "s13", "s14", "s15", "s16", "s17", "s18", "s19", "s20", "s21", "s22", "s23", "s24", "s25", "s26", "s27", "s28", "s29", "s30", "s31", "s32", "s33", "s34", "s35", "s36", "s37", "s38", "s39", "s40", "s41", "s42", "s43", "s44", "s45", "s46", "s47", "s48", "s49", "s50", "s51", "s52", "s53", "s54", "s55", "s56", "s57", "s58", "s59", "s60", "s61", "s62", "s63", "s64", "s65", "s66", "s67", "s68", "s69", "s70", "s71", "s72", "s73", "s74", "s75", "s76", "s77", "s78", "s79", "s80", "s81", "s82", "s83", "s84", "s85", "s86", "s87", "s88", "s89", "s90", "s91", "s92", "s93", "s94", "s95", "s96", "s97", "s98", "s99", "s100", "s101", "vcc", "scc", "memory");
}

extern "C" void kernel_launch(void* const* d_in, const int* in_sizes, int n_in, void* d_out, int out_size, void* d_ws, size_t ws_size,
                              hipStream_t stream) {
  (void)hipFuncSetAttribute((const void*)attn_fused, hipFuncAttributeMaxDynamicSharedMemorySize, 153088);
  attn_fused<<<256, 256, 153088, stream>>>(d_in[0], d_in[1], d_in[2], d_in[3], d_in[4], d_in[5], d_in[6], d_ws, d_out);
}
```
